```python
import torch

def _softmax(x, axis=-1):
    return torch.softmax(x, dim=axis)
def _gelu(x):
    return torch.nn.functional.gelu(x, approximate="tanh")
def _silu(x):
    return torch.nn.functional.silu(x)
def _sigmoid(x):
    return torch.sigmoid(x)
def _layernorm(x, axis=-1, eps=1e-5):
    m = x.mean(dim=axis, keepdim=True)
    v = x.var(dim=axis, keepdim=True, unbiased=False)
    return (x - m) / torch.sqrt(v + eps)
def _scatter_add(tgt, idx, src, axis=0):
    return tgt.clone().index_add_(axis, idx, src)
def _segment_sum(src, seg, n):
    out = torch.zeros((n,) + src.shape[1:], dtype=src.dtype)
    return out.index_add_(0, seg, src)
def _pad(x, pw):
    flat = tuple(v for pair in reversed(pw) for v in pair)
    return torch.nn.functional.pad(x, flat)

kernel_name = 'concat_heads__gqa_block_cached__mha_block_cached__near_ridge'

def setup_inputs():

    _w = (lambda a: (lambda t: t.to(torch.bfloat16) if t.is_floating_point() else t)(torch.as_tensor(a)))
    def _A_setup():
        B, Sq, D = 16, 160, 640
        Hq, Hkv, Sk, Dh = 32, 32, 512, 192
        assert Hq % Hkv == 0, (Hq, Hkv)
        return {
            "x": _w((lambda *s: torch.randn(*s, dtype=torch.bfloat16) * 0.8)(B, Sq, D)),
            "k_cache": _w((lambda *s: torch.randn(*s, dtype=torch.bfloat16) * 0.8)(B, Hkv, Sk, Dh)),
            "v_cache": _w((lambda *s: torch.randn(*s, dtype=torch.bfloat16) * 0.05)(B, Hkv, Sk, Dh)),
            "wq": _w((lambda *s: torch.randn(*s, dtype=torch.bfloat16) * 0.8)(D, Hq * Dh) * (D ** -0.5)),
            "wo": _w((lambda *s: torch.randn(*s, dtype=torch.bfloat16) * 0.05)(Hq * Dh, D) * ((Hq * Dh) ** -0.5)),
        }
    _A_in = _A_setup()

    def _B_setup():
        B, Sq, D = 16, 160, 640
        H, Sk, Dh = 32, 512, 192
        return {
            "x": _w((lambda *s: torch.randn(*s, dtype=torch.bfloat16) * 0.8)(B, Sq, D)),
            "k_cache": _w((lambda *s: torch.randn(*s, dtype=torch.bfloat16) * 0.8)(B, H, Sk, Dh)),
            "v_cache": _w((lambda *s: torch.randn(*s, dtype=torch.bfloat16) * 0.05)(B, H, Sk, Dh)),
            "wq": _w((lambda *s: torch.randn(*s, dtype=torch.bfloat16) * 0.8)(D, H * Dh) * (D ** -0.5)),
            "wo": _w((lambda *s: torch.randn(*s, dtype=torch.bfloat16) * 0.05)(H * Dh, D) * ((H * Dh) ** -0.5)),
        }
    _B_in = _B_setup()

    return {
        "A_x": _A_in["x"],
        "A_k_cache": _A_in["k_cache"],
        "A_v_cache": _A_in["v_cache"],
        "A_wq": _A_in["wq"],
        "A_wo": _A_in["wo"],
        "B_x": _B_in["x"],
        "B_k_cache": _B_in["k_cache"],
        "B_v_cache": _B_in["v_cache"],
        "B_wq": _B_in["wq"],
        "B_wo": _B_in["wo"],
    }

def reference(A_x, A_k_cache, A_v_cache, A_wq, A_wo, B_x, B_k_cache, B_v_cache, B_wq, B_wo):
    def _A_ref(x, k_cache, v_cache, wq, wo):
        B, Sq, D = x.shape
        Hq, Dh = 32, 192
        Hkv = k_cache.shape[1]
        rep = Hq // Hkv
        q = (lambda x, *d: x.permute(*d))((x @ wq).reshape(B, Sq, Hq, Dh), 0, 2, 1, 3)
        k = (lambda x, n, axis: torch.repeat_interleave(x, n, dim=axis))(k_cache, rep, axis=1)
        v = (lambda x, n, axis: torch.repeat_interleave(x, n, dim=axis))(v_cache, rep, axis=1)
        scores = torch.einsum("bhqd,bhkd->bhqk", q, k) * (Dh ** -0.5)
        p = _softmax(scores, axis=-1)
        o = torch.einsum("bhqk,bhkd->bhqd", p, v)
        return (lambda x, *d: x.permute(*d))(o, 0, 2, 1, 3).reshape(B, Sq, Hq * Dh) @ wo

    def _B_ref(x, k_cache, v_cache, wq, wo):
        B, Sq, D = x.shape
        H, Dh = 32, 192
        q = (lambda x, *d: x.permute(*d))((x @ wq).reshape(B, Sq, H, Dh), 0, 2, 1, 3)
        scores = torch.einsum("bhqd,bhkd->bhqk", q, k_cache) * (Dh ** -0.5)
        p = _softmax(scores, axis=-1)
        o = torch.einsum("bhqk,bhkd->bhqd", p, v_cache)
        return (lambda x, *d: x.permute(*d))(o, 0, 2, 1, 3).reshape(B, Sq, H * Dh) @ wo

    a = _A_ref(A_x, A_k_cache, A_v_cache, A_wq, A_wo)
    b = _B_ref(B_x, B_k_cache, B_v_cache, B_wq, B_wo)
    return torch.concatenate([a, b], axis=-1)
```

```cpp
#include <hip/hip_runtime.h>
#include <hip/hip_bf16.h>

constexpr unsigned LDS1 = 160 * 1024;

struct Args { const void* p[2][8]; };

__global__ __launch_bounds__(256, 1) void fused_kernel(Args P) {
  unsigned tid = threadIdx.x;
  asm volatile(
R"A(.macro S_ a,b,c
s_add_u32 \a, \b, \c
.endm
.macro W_ a
s_waitcnt lgkmcnt(\a)
.endm
.macro L_ x,r,s,o
buffer_load_dwordx4 \x, s[\r:\r+3], \s offen offset:\o lds
.endm
.macro X_ a,b
v_accvgpr_write_b32 \a, \b
.endm
.macro R_ a,b
v_accvgpr_read_b32 \a, \b
.endm
.macro Ta d,x,o
ds_read_b64_tr_b16 a[\d:\d+1], \x offset:\o
.endm
.macro Da d,x,o
ds_read_b128 a[\d:\d+3], \x offset:\o
.endm
.macro Zvaa d,a,b
v_mfma_f32_16x16x32_bf16 v[\d:\d+3], a[\a:\a+3], a[\b:\b+3], 0
.endm
.macro Mvaa d,a,b
v_mfma_f32_16x16x32_bf16 v[\d:\d+3], a[\a:\a+3], a[\b:\b+3], v[\d:\d+3]
.endm
.macro U_ a,b,c
v_mul_f32 \a, \b, \c
.endm
.macro C_ d,a,b
v_cvt_pk_bf16_f32 v\d, v\a, v\b
.endm
.macro Ln x,r,s,o
buffer_load_dwordx4 \x, s[\r:\r+3], \s offen offset:\o nt lds
.endm
.macro Dv d,x,o
ds_read_b128 v[\d:\d+3], \x offset:\o
.endm
.macro Zvvv d,a,b
v_mfma_f32_16x16x32_bf16 v[\d:\d+3], v[\a:\a+3], v[\b:\b+3], 0
.endm
.macro Mvvv d,a,b
v_mfma_f32_16x16x32_bf16 v[\d:\d+3], v[\a:\a+3], v[\b:\b+3], v[\d:\d+3]
.endm
.macro B_ a,b,c
v_sub_f32 \a, \b, \c
.endm
.macro E_ r
v_exp_f32 v\r, v\r
.endm
.macro Nvvv d,a,b,c
v_mfma_f32_16x16x32_bf16 v[\d:\d+3], v[\a:\a+3], v[\b:\b+3], v[\c:\c+3]
.endm
.macro Tv d,x,o
ds_read_b64_tr_b16 v[\d:\d+1], \x offset:\o
.endm
.macro Zaav d,a,b
v_mfma_f32_16x16x32_bf16 a[\d:\d+3], a[\a:\a+3], v[\b:\b+3], 0
.endm
.macro Zavv d,a,b
v_mfma_f32_16x16x32_bf16 a[\d:\d+3], v[\a:\a+3], v[\b:\b+3], 0
.endm
.macro A_ a,b,c
v_add_f32 \a, \b, \c
.endm
.macro Mavv d,a,b
v_mfma_f32_16x16x32_bf16 a[\d:\d+3], v[\a:\a+3], v[\b:\b+3], a[\d:\d+3]
.endm
.macro Zvav d,a,b
v_mfma_f32_16x16x32_bf16 v[\d:\d+3], a[\a:\a+3], v[\b:\b+3], 0
.endm
.macro Mvav d,a,b
v_mfma_f32_16x16x32_bf16 v[\d:\d+3], a[\a:\a+3], v[\b:\b+3], v[\d:\d+3]
.endm
.macro Y_ r,o
ds_write_addtid_b32 v\r offset:\o
.endm
.macro Zaaa d,a,b
v_mfma_f32_16x16x32_bf16 a[\d:\d+3], a[\a:\a+3], a[\b:\b+3], 0
.endm
.macro Maaa d,a,b
v_mfma_f32_16x16x32_bf16 a[\d:\d+3], a[\a:\a+3], a[\b:\b+3], a[\d:\d+3]
.endm
.macro Maav d,a,b
v_mfma_f32_16x16x32_bf16 a[\d:\d+3], a[\a:\a+3], v[\b:\b+3], a[\d:\d+3]
.endm
.macro Nvav d,a,b,c
v_mfma_f32_16x16x32_bf16 v[\d:\d+3], a[\a:\a+3], v[\b:\b+3], v[\c:\c+3]
.endm
s_and_b32 s3, %2, 4
s_lshl_b32 s3, s3, 4
s_load_dwordx16 s[8:23], %1, s3
s_memrealtime s[100:101]
s_memtime s[90:91]
v_readfirstlane_b32 s4, %0
v_and_b32 v16, 63, %0
v_lshlrev_b32 v10, 4, v16
s_mov_b32 s7, %2
s_lshr_b32 s4, s4, 6
.Lcode0:
s_getpc_b64 s[44:45]
s_mov_b32 s46, .Ldone-.Lcode0
s_mov_b32 s47, 0x00020000
s_lshr_b32 s66, s7, 3
s_lshl_b32 s66, s66, 2
S_ s66,s66,s4
s_lshl_b32 s66, s66, 7
v_lshlrev_b32 v40, 10, v10
buffer_load_dword v247, v40, s[44:47], s66 offen
s_and_b32 s3, s7, 1
s_lshl_b32 s3, s3, 3
s_lshr_b32 s53, s7, 5
S_ s3,s3,s53
s_and_b32 s6, s7, 2
s_lshl_b32 s6, s6, 1
s_bfe_u32 s53, s7, 0x20003
S_ s6,s6,s53
s_and_b32 s5, s7, 4
s_lshl_b32 s5, s5, 2
S_ s5,s5,s3
s_mul_i32 s50, s4, 5120
s_mul_i32 s60, s4, 51200
s_lshr_b32 s77, s4, 1
s_and_b32 s53, s4, 1
s_mul_i32 s59, s53, 393216
s_lshl_b32 s52, s53, 11
s_mul_i32 s61, s53, 6144
s_mul_i32 s51, s77, 61440
S_ s55,s51,40960
S_ s51,s55,s61
s_sub_u32 s52, s51, s52
s_lshl_b32 s86, s53, 2
s_mul_i32 s56, s53, 10240
v_lshrrev_b32 v17, 3, v16
v_and_b32 v18, 7, v16
v_lshrrev_b32 v23, 4, v16
v_and_b32 v24, 1, v23
v_lshlrev_b32 v24, 2, v24
v_xor_b32 v24, v18, v24
v_mul_u32_u24 v6, 12288, v17
v_lshl_add_u32 v6, v24, 4, v6
v_add_u32 v24, s86, v23
v_xor_b32 v24, v18, v24
v_mul_u32_u24 v4, 1280, v17
v_lshl_add_u32 v4, v24, 4, v4
v_xor_b32 v5, 64, v4
s_getreg_b32 s53, hwreg(HW_REG_XCC_ID, 0, 4)
s_bitcmp1_b32 0xaaaa, s53
s_cselect_b32 s58, 2, 0
s_cselect_b32 s89, 1, 0
s_cselect_b32 s53, 768, 0
S_ s59,s59,s53
s_lshl_b32 s54, s6, 2
S_ s54,s54,s77
s_mul_i32 s86, s54, 384
s_mul_i32 s87, s3, 204800
s_lshl_b32 s53, s3, 5
S_ s53,s53,s54
s_mul_i32 s85, s53, 196608
s_mov_b32 s30, 7853184
s_mov_b32 s31, 0x00020000
s_mov_b32 s26, 204800
s_mov_b32 s27, 0x00020000
W_ 0
s_mov_b32 s98, s100
s_mov_b32 s99, 0
s_mov_b32 s88, s90
S_ s28,s10,s86
s_addc_u32 s29, s11, 0
S_ s24,s8,s87
s_addc_u32 s25, s9, 0
s_mov_b32 s11, 0
S_ s65,s59,0
S_ m0,s52,8192
s_mov_b64 s[92:93], s[18:19]
s_mov_b32 s53, 251658240
L_ v6,28,s65,0
S_ s65,s59,98304
S_ m0,s52,9216
S_ s92,s92,s53
s_addc_u32 s93, s93, 0
L_ v6,28,s65,0
S_ s65,s59,196608
S_ m0,s52,10240
s_mov_b32 s94, 131072
s_mov_b32 s95, 0x00020000
L_ v6,28,s65,0
S_ s65,s59,294912
S_ m0,s52,11264
s_lshl_b32 s96, s5, 3
S_ s96,s96,s6
L_ v6,28,s65,0
S_ s65,s60,0
S_ m0,s50,0
s_lshl_b32 s96, s96, 9
v_cmp_gt_u32 vcc, 8, v16
L_ v4,24,s65,0
S_ s65,s60,10240
S_ m0,s50,1024
s_lshl_b32 s54, s5, 12
v_lshl_add_u32 v24, v16, 9, s54
L_ v5,24,s65,0
S_ s65,s60,20480
S_ m0,s50,2048
v_mov_b32 v12, 0x7fff0000
v_cndmask_b32 v12, v12, v24, vcc
L_ v4,24,s65,0
S_ s65,s60,30720
S_ m0,s50,3072
s_lshl_b32 s54, s6, 2
v_add_u32 v13, s54, v12
L_ v5,24,s65,0
S_ s65,s60,40960
S_ m0,s50,4096
v_lshrrev_b32 v19, 2, v10
v_mul_u32_u24 v25, 384, v17
L_ v4,24,s65,0
s_cmp_eq_u32 s4, 0
s_cbranch_scc0 .Lhs0a
buffer_load_dword v20, v19, s[92:95], s96 offen offset:128 sc1
buffer_load_dword v21, v19, s[92:95], s96 offen offset:384 sc1
buffer_load_dword a252, v19, s[92:95], s96 offen offset:256 sc1
buffer_load_dword a253, v13, s[92:95], 0 offen offset:256 sc1
buffer_load_dword v22, off, s[92:95], s96 offset:0 sc1
.Lhs0a:
S_ s65,s59,0
S_ m0,s52,16256
v_xor_b32 v24, v18, v23
v_lshl_add_u32 v7, v24, 4, v25
v_lshlrev_b32 v24, 1, v23
v_xor_b32 v24, v18, v24
v_lshl_add_u32 v9, v24, 4, v25
v_and_b32 v26, 15, v16
v_lshrrev_b32 v27, 2, v26
v_and_b32 v28, 3, v16
v_lshrrev_b32 v24, 1, v26
v_xor_b32 v24, v23, v24
L_ v6,28,s65,128
S_ s65,s59,98304
S_ m0,s52,17280
v_lshlrev_b32 v24, 4, v24
v_lshl_add_u32 v24, v18, 7, v24
v_lshrrev_b32 v25, 3, v26
v_lshl_add_u32 v2, v25, 10, v24
v_add_u32 v2, s56, v2
v_mul_u32_u24 v1, 3072, v25
v_add3_u32 v1, v1, v24, s55
v_lshlrev_b32 v24, 2, v25
v_xor_b32 v24, v28, v24
v_lshlrev_b32 v24, 4, v24
L_ v6,28,s65,128
S_ s65,s59,196608
S_ m0,s52,18304
v_lshl_add_u32 v24, v27, 7, v24
v_lshl_add_u32 %0, v23, 10, v24
v_add_u32 %0, s55, %0
v_and_b32 v29, 1, v23
v_lshl_add_u32 v24, v29, 1, v25
v_lshlrev_b32 v24, 1, v24
v_lshrrev_b32 v30, 1, v28
v_xor_b32 v24, v30, v24
v_lshlrev_b32 v24, 4, v24
v_lshl_add_u32 v29, v29, 2, v27
L_ v6,28,s65,128
S_ s65,s59,294912
S_ m0,s52,19328
v_lshl_add_u32 v24, v29, 7, v24
v_and_b32 v29, 1, v28
v_lshl_add_u32 v24, v29, 3, v24
v_lshrrev_b32 v29, 1, v23
v_mul_u32_u24 v3, 3072, v29
v_add3_u32 v3, v3, v24, s55
X_ a242,%0
X_ a244,v1
X_ a243,v2
X_ a245,v3
L_ v6,28,s65,128
S_ s65,s59,0
S_ m0,s52,24320
X_ a240,v4
X_ a248,v10
X_ a241,v12
X_ a251,v13
v_xor_b32 v1, 64, %0
v_xor_b32 v3, 64, v2
v_xor_b32 v8, 64, v7
S_ s32,s12,s85
s_addc_u32 s33, s13, 0
s_mov_b32 s34, 589824
L_ v6,28,s65,256
S_ s65,s59,98304
S_ m0,s52,25344
s_mov_b32 s35, 0x00020000
S_ s36,s14,s85
s_addc_u32 s37, s15, 0
s_mov_b32 s38, 589824
s_mov_b32 s39, 0x00020000
s_lshl_b32 s87, s87, 1
S_ s20,s20,s87
s_addc_u32 s21, s21, 0
s_mov_b32 s57, 20000
s_mov_b32 s48, 1037384597
L_ v6,28,s65,256
S_ s65,s59,196608
S_ m0,s52,26368
s_mov_b32 s49, 1037384597
s_mul_i32 s53, s5, 1638400
S_ s53,s53,134217728
S_ s22,s18,s53
s_addc_u32 s23, s19, 0
s_lshl_b32 s53, s6, 3
s_lshr_b32 s54, s7, 5
S_ s53,s53,s54
s_lshl_b32 s53, s53, 2
S_ s53,s53,s4
L_ v6,28,s65,256
S_ s65,s59,294912
S_ m0,s52,27392
s_mul_i32 s87, s53, 30720
s_lshl_b32 s53, s6, 2
S_ s53,s53,s77
s_mul_i32 s72, s53, 245760
s_and_b32 s54, s4, 1
s_mul_i32 s53, s54, 61440
S_ s72,s72,s53
s_lshl_b32 s53, s77, 6
s_xor_b32 s83, s53, 64
s_xor_b32 s53, s4, 2
L_ v6,28,s65,256
S_ s65,s60,128
S_ m0,s50,20480
s_mul_i32 s75, s53, 10240
s_cmp_eq_u32 s4, 0
s_cselect_b32 s73, 0, 2147418112
s_mul_i32 s53, s4, 5120
s_mul_i32 s78, s6, 204800
S_ s78,s78,s53
s_and_b32 s84, s6, 7
s_mul_i32 s84, s84, 20480
S_ s84,s84,s53
s_mul_i32 s53, s6, 5120
L_ v4,24,s65,0
S_ s65,s60,10368
S_ m0,s50,21504
S_ s53,s53,163840
s_lshl_b32 s54, s4, 10
S_ s74,s53,s54
S_ s53,s53,4096
s_cmp_eq_u32 s4, 0
s_cselect_b32 s82, s53, 4026531840
s_and_b32 s53, s4, 1
s_mul_i32 s53, s53, 204800
s_lshl_b32 s54, s6, 7
S_ s53,s53,s54
L_ v5,24,s65,0
S_ s65,s60,20608
S_ m0,s50,22528
s_lshl_b32 s54, s77, 6
S_ s8,s53,s54
s_and_b32 s53, s6, 1
s_mul_i32 s53, s53, 204800
s_lshr_b32 s54, s6, 1
s_lshl_b32 s54, s54, 6
S_ s53,s53,s54
S_ s53,s53,1024
s_mul_i32 s54, s4, 40960
S_ s9,s53,s54
L_ v4,24,s65,0
S_ s65,s60,30848
S_ m0,s50,23552
S_ s53,s53,163840
s_cmp_eq_u32 s4, 0
s_cselect_b32 s10, s53, 4026531840
R_ v12,a248
v_lshrrev_b32 v12, 4, v12
v_min_u32 v10, 11, v12
s_mul_i32 s56, s4, 12
v_add_u32 v10, s56, v10
v_mul_u32_u24 v11, 43691, v10
v_lshrrev_b32 v11, 18, v11
L_ v5,24,s65,0
S_ s65,s60,41088
S_ m0,s50,24576
v_lshlrev_b32 v10, 7, v10
v_mul_u32_u24 v11, 11520, v11
v_add_u32 v10, v10, v11
v_min_u32 v12, 9, v12
s_and_b32 s56, s6, 3
s_lshl_b32 s56, s56, 2
S_ s56,s56,s4
s_mul_i32 s56, s56, 10
v_add_u32 v12, s56, v12
v_mul_u32_u24 v12, 1280, v12
L_ v4,24,s65,0
S_ s65,s59,786432
S_ m0,s52,0
s_lshr_b32 s56, s7, 5
s_mul_i32 s56, s56, 98304
s_and_b32 s55, s59, 4095
S_ s56,s56,s55
s_mul_i32 s55, s77, 384
s_sub_u32 s56, s56, s55
s_mov_b32 s63, 0
L_ v6,28,s65,0
S_ s65,s59,884736
S_ m0,s52,1024
s_nop 0
L_ v6,28,s65,0
S_ s65,s59,983040
S_ m0,s52,2048
s_nop 0
L_ v6,28,s65,0
S_ s65,s59,1081344
S_ m0,s52,3072
s_nop 0
L_ v6,28,s65,0
S_ s55,s56,786432
buffer_load_dword a254, v10, s[28:31], s55 offen
S_ s55,s56,1572864
buffer_load_dword a254, v10, s[28:31], s55 offen
buffer_load_dword a254, v12, s[24:27], s63 offen offset:256
S_ s55,s56,2359296
buffer_load_dword a254, v10, s[28:31], s55 offen
buffer_load_dword a254, v12, s[24:27], s63 offen offset:384
s_waitcnt vmcnt(22) lgkmcnt(0)
s_barrier
s_memtime s[90:91]
Ta 20,%0,8192
Ta 22,%0,8704
Ta 24,%0,12288
Ta 26,%0,12800
Da 60,v2,0
Da 100,v3,0
Da 64,v2,2048
Da 104,v3,2048
Da 68,v2,4096
Da 108,v3,4096
Da 72,v2,6144
Da 112,v3,6144
Da 76,v2,8192
Da 116,v3,8192
Ta 28,%0,8200
Ta 30,%0,8712
Ta 32,%0,12296
Ta 34,%0,12808
s_cmp_eq_u32 s4, 0
s_cbranch_scc0 .Lhs0b
v_readfirstlane_b32 s62, v22
.Lhs0l:
s_nop 3
S_ s62,s62,1
v_cmp_eq_u32 vcc, s62, v20
s_nop 1
s_or_b32 s53, vcc_lo, vcc_hi
v_cmp_eq_u32 vcc, s62, v21
s_nop 1
s_or_b32 s54, vcc_lo, vcc_hi
s_or_b32 s53, s53, s54
s_cbranch_scc1 .Lhs0l
v_mov_b32 v22, s62
s_mov_b64 exec, 1
s_nop 1
buffer_store_dword v22, off, s[92:95], s96 sc1
s_mov_b64 exec, -1
s_nop 1
.Lhs0b:
s_cmp_eq_u32 s58, 0
s_cbranch_scc1 .Lq
.Lqy:
W_ 13
Zvaa 16,20,60
s_nop 0
Ta 36,v1,8192
Ta 38,v1,8704
W_ 14
Mvaa 16,24,100
R_ v12,a248
W_ 13
Zvaa 24,20,64
v_lshrrev_b32 v12, 4, v12
W_ 12
Mvaa 24,24,104
s_nop 0
Ta 224,v1,12288
Ta 226,v1,12800
W_ 13
Zvaa 32,20,68
v_min_u32 v10, 11, v12
W_ 12
Mvaa 32,24,108
s_mul_i32 s56, s4, 12
W_ 11
Zvaa 40,20,72
v_add_u32 v10, s56, v10
W_ 10
Mvaa 40,24,112
v_mul_u32_u24 v11, 43691, v10
W_ 9
Zvaa 48,20,76
v_lshrrev_b32 v11, 18, v11
W_ 8
Mvaa 48,24,116
W_ 4
v_lshlrev_b32 v10, 7, v10
Zvaa 52,32,116
s_nop 0
Ta 20,v1,8200
Ta 22,v1,8712
Mvaa 52,28,76
v_mul_u32_u24 v11, 11520, v11
Zvaa 44,32,112
v_add_u32 v10, v10, v11
Mvaa 44,28,72
s_nop 0
Ta 24,v1,12296
Ta 26,v1,12808
Zvaa 36,32,108
v_min_u32 v12, 9, v12
Mvaa 36,28,68
s_and_b32 s56, s6, 3
Zvaa 28,32,104
s_lshl_b32 s56, s56, 2
Mvaa 28,28,64
S_ s56,s56,s4
Zvaa 20,32,100
s_waitcnt vmcnt(18) lgkmcnt(0)
s_barrier
s_mul_i32 s56, s56, 10
Mvaa 20,28,60
S_ s65,s59,786432
v_add_u32 v12, s56, v12
Zvaa 56,36,60
S_ m0,s52,8064
Ta 28,%0,16384
Ta 30,%0,16896
Mvaa 56,224,100
L_ v6,28,s65,128
Zvaa 64,36,64
S_ s65,s59,884736
v_mul_u32_u24 v12, 1280, v12
Mvaa 64,224,104
S_ m0,s52,9088
Ta 32,%0,20480
Ta 34,%0,20992
Zvaa 72,36,68
s_lshr_b32 s56, s7, 5
Mvaa 72,224,108
L_ v6,28,s65,128
Zvaa 80,36,72
S_ s65,s59,983040
s_mul_i32 s56, s56, 98304
Mvaa 80,224,112
S_ m0,s52,10112
s_and_b32 s55, s59, 4095
Zvaa 88,36,76
S_ s56,s56,s55
Mvaa 88,224,116
s_mul_i32 s55, s77, 384
Zvaa 92,24,116
s_sub_u32 s56, s56, s55
Ta 36,%0,16392
Ta 38,%0,16904
Mvaa 92,20,76
L_ v6,28,s65,128
Zvaa 84,24,112
S_ s65,s59,1081344
s_mov_b32 s63, 0
Mvaa 84,20,72
S_ m0,s52,11136
Ta 224,%0,20488
Ta 226,%0,21000
Zvaa 76,24,108
S_ s55,s56,3145728
Mvaa 76,20,68
L_ v6,28,s65,128
Zvaa 68,24,104
S_ s65,s60,256
Mvaa 68,20,64
S_ m0,s50,0
Zvaa 60,24,100
Mvaa 60,20,60
W_ 4
Zvaa 96,28,60
s_nop 0
Ta 20,v1,16384
Ta 22,v1,16896
Mvaa 96,32,100
L_ v4,24,s65,0
Zvaa 104,28,64
S_ s65,s60,10496
Mvaa 104,32,104
S_ m0,s50,1024
Ta 24,v1,20480
Ta 26,v1,20992
Zvaa 112,28,68
Mvaa 112,32,108
L_ v5,24,s65,0
Zvaa 120,28,72
S_ s65,s60,20736
Mvaa 120,32,112
S_ m0,s50,2048
Zvaa 128,28,76
Mvaa 128,32,116
W_ 4
Zvaa 132,224,116
s_nop 0
Ta 28,v1,16392
Ta 30,v1,16904
Mvaa 132,36,76
L_ v4,24,s65,0
Zvaa 124,224,112
S_ s65,s60,30976
Mvaa 124,36,72
S_ m0,s50,3072
Ta 32,v1,20488
Ta 34,v1,21000
Zvaa 116,224,108
Mvaa 116,36,68
L_ v5,24,s65,0
Zvaa 108,224,104
S_ s65,s60,41216
Mvaa 108,36,64
S_ m0,s50,4096
Zvaa 100,224,100
s_waitcnt vmcnt(17) lgkmcnt(0)
s_barrier
Mvaa 100,36,60
Da 140,v2,20480
Zvaa 136,20,60
s_nop 0
Ta 36,%0,24576
Ta 38,%0,25088
Mvaa 136,24,100
L_ v4,24,s65,0
Zvaa 144,20,64
S_ s65,s59,786432
Da 180,v3,20480
Mvaa 144,24,104
S_ m0,s52,16128
Ta 224,%0,28672
Ta 226,%0,29184
Zvaa 152,20,68
Mvaa 152,24,108
Da 144,v2,22528
Zvaa 160,20,72
L_ v6,28,s65,256
Mvaa 160,24,112
S_ s65,s59,884736
Zvaa 168,20,76
S_ m0,s52,17152
Da 184,v3,22528
Mvaa 168,24,116
Zvaa 172,32,116
s_nop 0
Ta 20,%0,24584
Ta 22,%0,25096
Mvaa 172,28,76
Da 148,v2,24576
Zvaa 164,32,112
L_ v6,28,s65,256
Mvaa 164,28,72
S_ s65,s59,983040
Ta 24,%0,28680
Ta 26,%0,29192
Zvaa 156,32,108
S_ m0,s52,18176
Da 188,v3,24576
Mvaa 156,28,68
Zvaa 148,32,104
L_ v6,28,s65,256
Mvaa 148,28,64
S_ s65,s59,1081344
Da 152,v2,26624
Zvaa 140,32,100
S_ m0,s52,19200
Mvaa 140,28,60
W_ 9
Zvaa 176,36,60
s_nop 0
Ta 28,v1,24576
Ta 30,v1,25088
Mvaa 176,224,100
Da 192,v3,26624
Zvaa 184,36,64
L_ v6,28,s65,256
Mvaa 184,224,104
s_nop 0
Ta 32,v1,28672
Ta 34,v1,29184
Zvaa 192,36,68
Da 156,v2,28672
Mvaa 192,224,108
Zvaa 200,36,72
Da 196,v3,28672
Mvaa 200,224,112
buffer_load_dword a254, v10, s[28:31], s55 offen
Zvaa 208,36,76
Mvaa 208,224,116
W_ 9
Zvaa 212,24,116
s_nop 0
Ta 36,v1,24584
Ta 38,v1,25096
Mvaa 212,20,76
buffer_load_dword a254, v12, s[24:27], s63 offen offset:512
Zvaa 204,24,112
Mvaa 204,20,72
s_nop 0
Ta 224,v1,28680
Ta 226,v1,29192
Zvaa 196,24,108
Mvaa 196,20,68
Zvaa 188,24,104
Mvaa 188,20,64
Zvaa 180,24,100
s_waitcnt vmcnt(20) lgkmcnt(0)
s_barrier
Mvaa 180,20,60
S_ s65,s59,1572864
Zvaa 216,28,60
S_ m0,s52,24576
Ta 20,%0,0
Ta 22,%0,512
Mvaa 216,32,100
L_ v6,28,s65,0
Zvaa 224,28,64
S_ s65,s59,1671168
Mvaa 224,32,104
S_ m0,s52,25600
Ta 24,%0,4096
Ta 26,%0,4608
Zvaa 232,28,68
Mvaa 232,32,108
L_ v6,28,s65,0
Zvaa 240,28,72
S_ s65,s59,1769472
Mvaa 240,32,112
S_ m0,s52,26624
Zvaa 248,28,76
Mvaa 248,32,116
Zvaa 252,224,116
s_nop 0
Ta 28,%0,8
Ta 30,%0,520
Mvaa 252,36,76
L_ v6,28,s65,0
Zvaa 244,224,112
S_ s65,s59,1867776
Mvaa 244,36,72
S_ m0,s52,27648
Ta 32,%0,4104
Ta 34,%0,4616
Zvaa 236,224,108
Mvaa 236,36,68
L_ v6,28,s65,0
Zvaa 228,224,104
S_ s65,s60,384
Mvaa 228,36,64
S_ m0,s50,20480
Zvaa 220,224,100
Mvaa 220,36,60
W_ 4
Mvaa 16,20,140
s_nop 0
Ta 36,v1,0
Ta 38,v1,512
Mvaa 16,24,180
L_ v4,24,s65,0
Mvaa 24,20,144
S_ s65,s60,10624
Mvaa 24,24,184
S_ m0,s50,21504
Ta 224,v1,4096
Ta 226,v1,4608
Mvaa 32,20,148
Mvaa 32,24,188
L_ v5,24,s65,0
Mvaa 40,20,152
S_ s65,s60,20864
Mvaa 40,24,192
S_ m0,s50,22528
Mvaa 48,20,156
Mvaa 48,24,196
W_ 4
Mvaa 52,32,196
s_nop 0
Ta 20,v1,8
Ta 22,v1,520
Mvaa 52,28,156
L_ v4,24,s65,0
Mvaa 44,32,192
S_ s65,s60,31104
Mvaa 44,28,152
S_ m0,s50,23552
Ta 24,v1,4104
Ta 26,v1,4616
Mvaa 36,32,188
Mvaa 36,28,148
L_ v5,24,s65,0
Mvaa 28,32,184
S_ s65,s60,41344
Mvaa 28,28,144
S_ m0,s50,24576
Mvaa 20,32,180
s_waitcnt vmcnt(19) lgkmcnt(0)
s_barrier
Mvaa 20,28,140
L_ v4,24,s65,0
Mvaa 56,36,140
S_ s65,s59,1572864
Ta 28,%0,8192
Ta 30,%0,8704
Mvaa 56,224,180
S_ m0,s52,-128
Mvaa 64,36,144
Mvaa 64,224,184
s_nop 0
Ta 32,%0,12288
Ta 34,%0,12800
Mvaa 72,36,148
L_ v6,28,s65,128
Mvaa 72,224,188
S_ s65,s59,1671168
Mvaa 80,36,152
S_ m0,s52,896
Mvaa 80,224,192
Mvaa 88,36,156
L_ v6,28,s65,128
Mvaa 88,224,196
S_ s65,s59,1769472
Mvaa 92,24,196
S_ m0,s52,1920
Ta 36,%0,8200
Ta 38,%0,8712
Mvaa 92,20,156
S_ s55,s56,3932160
Mvaa 84,24,192
L_ v6,28,s65,128
Mvaa 84,20,152
S_ s65,s59,1867776
Ta 224,%0,12296
Ta 226,%0,12808
Mvaa 76,24,188
S_ m0,s52,2944
Mvaa 76,20,148
Mvaa 68,24,184
L_ v6,28,s65,128
Mvaa 68,20,144
Mvaa 60,24,180
Mvaa 60,20,140
W_ 4
Mvaa 96,28,140
s_nop 0
Ta 20,v1,8192
Ta 22,v1,8704
Mvaa 96,32,180
buffer_load_dword a254, v10, s[28:31], s55 offen
Mvaa 104,28,144
Mvaa 104,32,184
s_nop 0
Ta 24,v1,12288
Ta 26,v1,12800
Mvaa 112,28,148
Mvaa 112,32,188
buffer_load_dword a254, v12, s[24:27], s63 offen offset:640
Mvaa 120,28,152
Mvaa 120,32,192
Mvaa 128,28,156
Mvaa 128,32,196
W_ 4
Mvaa 132,224,196
s_nop 0
Ta 28,v1,8200
Ta 30,v1,8712
Mvaa 132,36,156
Mvaa 124,224,192
Mvaa 124,36,152
s_nop 0
Ta 32,v1,12296
Ta 34,v1,12808
Mvaa 116,224,188
Mvaa 116,36,148
Mvaa 108,224,184
Mvaa 108,36,144
Mvaa 100,224,180
s_waitcnt vmcnt(17) lgkmcnt(0)
s_barrier
Mvaa 100,36,140
S_ s65,s59,1572864
Da 60,v2,0
Mvaa 136,20,140
S_ m0,s52,7936
Ta 36,%0,16384
Ta 38,%0,16896
Mvaa 136,24,180
L_ v6,28,s65,256
Mvaa 144,20,144
S_ s65,s59,1671168
Da 100,v3,0
Mvaa 144,24,184
S_ m0,s52,8960
Ta 224,%0,20480
Ta 226,%0,20992
Mvaa 152,20,148
Mvaa 152,24,188
Da 64,v2,2048
Mvaa 160,20,152
L_ v6,28,s65,256
Mvaa 160,24,192
S_ s65,s59,1769472
Mvaa 168,20,156
S_ m0,s52,9984
Da 104,v3,2048
Mvaa 168,24,196
Mvaa 172,32,196
s_nop 0
Ta 20,%0,16392
Ta 22,%0,16904
Mvaa 172,28,156
Da 68,v2,4096
Mvaa 164,32,192
L_ v6,28,s65,256
Mvaa 164,28,152
S_ s65,s59,1867776
Ta 24,%0,20488
Ta 26,%0,21000
Mvaa 156,32,188
S_ m0,s52,11008
Da 108,v3,4096
Mvaa 156,28,148
Mvaa 148,32,184
L_ v6,28,s65,256
Mvaa 148,28,144
Da 72,v2,6144
Mvaa 140,32,180
Mvaa 140,28,140
W_ 9
Mvaa 176,36,140
s_nop 0
Ta 28,v1,16384
Ta 30,v1,16896
Mvaa 176,224,180
Da 112,v3,6144
Mvaa 184,36,144
Mvaa 184,224,184
s_nop 0
Ta 32,v1,20480
Ta 34,v1,20992
Mvaa 192,36,148
Da 76,v2,8192
Mvaa 192,224,188
Mvaa 200,36,152
Da 116,v3,8192
Mvaa 200,224,192
Mvaa 208,36,156
Mvaa 208,224,196
W_ 9
Mvaa 212,24,196
s_nop 0
Ta 36,v1,16392
Ta 38,v1,16904
Mvaa 212,20,156
Mvaa 204,24,192
Mvaa 204,20,152
s_nop 0
Ta 224,v1,20488
Ta 226,v1,21000
Mvaa 196,24,188
Mvaa 196,20,148
Mvaa 188,24,184
Mvaa 188,20,144
Mvaa 180,24,180
s_waitcnt vmcnt(15) lgkmcnt(0)
s_barrier
Mvaa 180,20,140
S_ s65,s59,2359296
Mvaa 216,28,140
S_ m0,s52,16384
Ta 20,%0,24576
Ta 22,%0,25088
Mvaa 216,32,180
L_ v6,28,s65,0
Mvaa 224,28,144
S_ s65,s59,2457600
Mvaa 224,32,184
S_ m0,s52,17408
Ta 24,%0,28672
Ta 26,%0,29184
Mvaa 232,28,148
Mvaa 232,32,188
L_ v6,28,s65,0
Mvaa 240,28,152
S_ s65,s59,2555904
Mvaa 240,32,192
S_ m0,s52,18432
Mvaa 248,28,156
Mvaa 248,32,196
Mvaa 252,224,196
s_nop 0
Ta 28,%0,24584
Ta 30,%0,25096
Mvaa 252,36,156
L_ v6,28,s65,0
Mvaa 244,224,192
S_ s65,s59,2654208
Mvaa 244,36,152
S_ m0,s52,19456
Ta 32,%0,28680
Ta 34,%0,29192
Mvaa 236,224,188
Mvaa 236,36,148
L_ v6,28,s65,0
Mvaa 228,224,184
S_ s65,s60,512
Mvaa 228,36,144
S_ m0,s50,0
Mvaa 220,224,180
Mvaa 220,36,140
W_ 4
Mvaa 16,20,60
s_nop 0
Ta 36,v1,24576
Ta 38,v1,25088
Mvaa 16,24,100
L_ v4,24,s65,0
Mvaa 24,20,64
S_ s65,s60,10752
Mvaa 24,24,104
S_ m0,s50,1024
Ta 224,v1,28672
Ta 226,v1,29184
Mvaa 32,20,68
Mvaa 32,24,108
L_ v5,24,s65,0
Mvaa 40,20,72
S_ s65,s60,20992
Mvaa 40,24,112
S_ m0,s50,2048
Mvaa 48,20,76
Mvaa 48,24,116
W_ 4
Mvaa 52,32,116
s_nop 0
Ta 20,v1,24584
Ta 22,v1,25096
Mvaa 52,28,76
L_ v4,24,s65,0
Mvaa 44,32,112
S_ s65,s60,31232
Mvaa 44,28,72
S_ m0,s50,3072
Ta 24,v1,28680
Ta 26,v1,29192
Mvaa 36,32,108
Mvaa 36,28,68
L_ v5,24,s65,0
Mvaa 28,32,104
S_ s65,s60,41472
Mvaa 28,28,64
S_ m0,s50,4096
Mvaa 20,32,100
s_waitcnt vmcnt(14) lgkmcnt(0)
s_barrier
Mvaa 20,28,60
L_ v4,24,s65,0
Mvaa 56,36,60
S_ s65,s59,2359296
Ta 28,%0,0
Ta 30,%0,512
Mvaa 56,224,100
S_ m0,s52,24448
Mvaa 64,36,64
Mvaa 64,224,104
s_nop 0
Ta 32,%0,4096
Ta 34,%0,4608
Mvaa 72,36,68
L_ v6,28,s65,128
Mvaa 72,224,108
S_ s65,s59,2457600
Mvaa 80,36,72
S_ m0,s52,25472
Mvaa 80,224,112
Mvaa 88,36,76
L_ v6,28,s65,128
Mvaa 88,224,116
S_ s65,s59,2555904
Mvaa 92,24,116
S_ m0,s52,26496
Ta 36,%0,8
Ta 38,%0,520
Mvaa 92,20,76
S_ s55,s56,4718592
Mvaa 84,24,112
L_ v6,28,s65,128
Mvaa 84,20,72
S_ s65,s59,2654208
Ta 224,%0,4104
Ta 226,%0,4616
Mvaa 76,24,108
S_ m0,s52,27520
Mvaa 76,20,68
Mvaa 68,24,104
L_ v6,28,s65,128
Mvaa 68,20,64
Mvaa 60,24,100
Mvaa 60,20,60
W_ 4
Mvaa 96,28,60
s_nop 0
Ta 20,v1,0
Ta 22,v1,512
Mvaa 96,32,100
buffer_load_dword a254, v10, s[28:31], s55 offen
Mvaa 104,28,64
Mvaa 104,32,104
s_nop 0
Ta 24,v1,4096
Ta 26,v1,4608
Mvaa 112,28,68
Mvaa 112,32,108
buffer_load_dword a254, v12, s[24:27], s63 offen offset:768
Mvaa 120,28,72
Mvaa 120,32,112
Mvaa 128,28,76
Mvaa 128,32,116
W_ 4
Mvaa 132,224,116
s_nop 0
Ta 28,v1,8
Ta 30,v1,520
Mvaa 132,36,76
Mvaa 124,224,112
Mvaa 124,36,72
s_nop 0
Ta 32,v1,4104
Ta 34,v1,4616
Mvaa 116,224,108
Mvaa 116,36,68
Mvaa 108,224,104
Mvaa 108,36,64
Mvaa 100,224,100
s_waitcnt vmcnt(15) lgkmcnt(0)
s_barrier
Mvaa 100,36,60
S_ s65,s59,2359296
Da 140,v2,20480
Mvaa 136,20,60
S_ m0,s52,-256
Ta 36,%0,8192
Ta 38,%0,8704
Mvaa 136,24,100
L_ v6,28,s65,256
Mvaa 144,20,64
S_ s65,s59,2457600
Da 180,v3,20480
Mvaa 144,24,104
S_ m0,s52,768
Ta 224,%0,12288
Ta 226,%0,12800
Mvaa 152,20,68
Mvaa 152,24,108
Da 144,v2,22528
Mvaa 160,20,72
L_ v6,28,s65,256
Mvaa 160,24,112
S_ s65,s59,2555904
Mvaa 168,20,76
S_ m0,s52,1792
Da 184,v3,22528
Mvaa 168,24,116
Mvaa 172,32,116
s_nop 0
Ta 20,%0,8200
Ta 22,%0,8712
Mvaa 172,28,76
Da 148,v2,24576
Mvaa 164,32,112
L_ v6,28,s65,256
Mvaa 164,28,72
S_ s65,s59,2654208
Ta 24,%0,12296
Ta 26,%0,12808
Mvaa 156,32,108
S_ m0,s52,2816
Da 188,v3,24576
Mvaa 156,28,68
Mvaa 148,32,104
L_ v6,28,s65,256
Mvaa 148,28,64
Da 152,v2,26624
Mvaa 140,32,100
Mvaa 140,28,60
W_ 9
Mvaa 176,36,60
s_nop 0
Ta 28,v1,8192
Ta 30,v1,8704
Mvaa 176,224,100
Da 192,v3,26624
Mvaa 184,36,64
Mvaa 184,224,104
s_nop 0
Ta 32,v1,12288
Ta 34,v1,12800
Mvaa 192,36,68
Da 156,v2,28672
Mvaa 192,224,108
Mvaa 200,36,72
Da 196,v3,28672
Mvaa 200,224,112
Mvaa 208,36,76
Mvaa 208,224,116
W_ 9
Mvaa 212,24,116
s_nop 0
Ta 36,v1,8200
Ta 38,v1,8712
Mvaa 212,20,76
Mvaa 204,24,112
Mvaa 204,20,72
s_nop 0
Ta 224,v1,12296
Ta 226,v1,12808
Mvaa 196,24,108
Mvaa 196,20,68
Mvaa 188,24,104
Mvaa 188,20,64
Mvaa 180,24,100
s_waitcnt vmcnt(15) lgkmcnt(0)
s_barrier
Mvaa 180,20,60
S_ s65,s59,3145728
Mvaa 216,28,60
S_ m0,s52,8192
Ta 20,%0,16384
Ta 22,%0,16896
Mvaa 216,32,100
L_ v6,28,s65,0
Mvaa 224,28,64
S_ s65,s59,3244032
Mvaa 224,32,104
S_ m0,s52,9216
Ta 24,%0,20480
Ta 26,%0,20992
Mvaa 232,28,68
Mvaa 232,32,108
L_ v6,28,s65,0
Mvaa 240,28,72
S_ s65,s59,3342336
Mvaa 240,32,112
S_ m0,s52,10240
Mvaa 248,28,76
Mvaa 248,32,116
Mvaa 252,224,116
s_nop 0
Ta 28,%0,16392
Ta 30,%0,16904
Mvaa 252,36,76
L_ v6,28,s65,0
Mvaa 244,224,112
S_ s65,s59,3440640
Mvaa 244,36,72
S_ m0,s52,11264
Ta 32,%0,20488
Ta 34,%0,21000
Mvaa 236,224,108
Mvaa 236,36,68
L_ v6,28,s65,0
Mvaa 228,224,104
S_ s65,s60,640
Mvaa 228,36,64
S_ m0,s50,20480
Mvaa 220,224,100
Mvaa 220,36,60
W_ 4
Mvaa 16,20,140
s_nop 0
Ta 36,v1,16384
Ta 38,v1,16896
Mvaa 16,24,180
L_ v4,24,s65,0
Mvaa 24,20,144
S_ s65,s60,10880
Mvaa 24,24,184
S_ m0,s50,21504
Ta 224,v1,20480
Ta 226,v1,20992
Mvaa 32,20,148
Mvaa 32,24,188
L_ v5,24,s65,0
Mvaa 40,20,152
S_ s65,s60,21120
Mvaa 40,24,192
S_ m0,s50,22528
Mvaa 48,20,156
Mvaa 48,24,196
W_ 4
Mvaa 52,32,196
s_nop 0
Ta 20,v1,16392
Ta 22,v1,16904
Mvaa 52,28,156
L_ v4,24,s65,0
Mvaa 44,32,192
S_ s65,s60,31360
Mvaa 44,28,152
S_ m0,s50,23552
Ta 24,v1,20488
Ta 26,v1,21000
Mvaa 36,32,188
Mvaa 36,28,148
L_ v5,24,s65,0
Mvaa 28,32,184
S_ s65,s60,41600
Mvaa 28,28,144
S_ m0,s50,24576
Mvaa 20,32,180
s_waitcnt vmcnt(14) lgkmcnt(0)
s_barrier
Mvaa 20,28,140
L_ v4,24,s65,0
Mvaa 56,36,140
S_ s65,s59,3145728
Ta 28,%0,24576
Ta 30,%0,25088
Mvaa 56,224,180
S_ m0,s52,16256
Mvaa 64,36,144
Mvaa 64,224,184
s_nop 0
Ta 32,%0,28672
Ta 34,%0,29184
Mvaa 72,36,148
L_ v6,28,s65,128
Mvaa 72,224,188
S_ s65,s59,3244032
Mvaa 80,36,152
S_ m0,s52,17280
Mvaa 80,224,192
Mvaa 88,36,156
L_ v6,28,s65,128
Mvaa 88,224,196
S_ s65,s59,3342336
Mvaa 92,24,196
S_ m0,s52,18304
Ta 36,%0,24584
Ta 38,%0,25096
Mvaa 92,20,156
S_ s55,s56,5505024
Mvaa 84,24,192
L_ v6,28,s65,128
Mvaa 84,20,152
S_ s65,s59,3440640
Ta 224,%0,28680
Ta 226,%0,29192
Mvaa 76,24,188
S_ m0,s52,19328
Mvaa 76,20,148
Mvaa 68,24,184
L_ v6,28,s65,128
Mvaa 68,20,144
Mvaa 60,24,180
Mvaa 60,20,140
W_ 4
Mvaa 96,28,140
s_nop 0
Ta 20,v1,24576
Ta 22,v1,25088
Mvaa 96,32,180
buffer_load_dword a254, v10, s[28:31], s55 offen
Mvaa 104,28,144
Mvaa 104,32,184
s_nop 0
Ta 24,v1,28672
Ta 26,v1,29184
Mvaa 112,28,148
Mvaa 112,32,188
buffer_load_dword a254, v12, s[24:27], s63 offen offset:896
Mvaa 120,28,152
Mvaa 120,32,192
Mvaa 128,28,156
Mvaa 128,32,196
W_ 4
Mvaa 132,224,196
s_nop 0
Ta 28,v1,24584
Ta 30,v1,25096
Mvaa 132,36,156
Mvaa 124,224,192
Mvaa 124,36,152
s_nop 0
Ta 32,v1,28680
Ta 34,v1,29192
Mvaa 116,224,188
Mvaa 116,36,148
Mvaa 108,224,184
Mvaa 108,36,144
Mvaa 100,224,180
s_waitcnt vmcnt(15) lgkmcnt(0)
s_barrier
Mvaa 100,36,140
S_ s65,s59,3145728
Da 60,v2,0
Mvaa 136,20,140
S_ m0,s52,24320
Ta 36,%0,0
Ta 38,%0,512
Mvaa 136,24,180
L_ v6,28,s65,256
Mvaa 144,20,144
S_ s65,s59,3244032
Da 100,v3,0
Mvaa 144,24,184
S_ m0,s52,25344
Ta 224,%0,4096
Ta 226,%0,4608
Mvaa 152,20,148
Mvaa 152,24,188
Da 64,v2,2048
Mvaa 160,20,152
L_ v6,28,s65,256
Mvaa 160,24,192
S_ s65,s59,3342336
Mvaa 168,20,156
S_ m0,s52,26368
Da 104,v3,2048
Mvaa 168,24,196
Mvaa 172,32,196
s_nop 0
Ta 20,%0,8
Ta 22,%0,520
Mvaa 172,28,156
Da 68,v2,4096
Mvaa 164,32,192
L_ v6,28,s65,256
Mvaa 164,28,152
S_ s65,s59,3440640
Ta 24,%0,4104
Ta 26,%0,4616
Mvaa 156,32,188
S_ m0,s52,27392
Da 108,v3,4096
Mvaa 156,28,148
Mvaa 148,32,184
L_ v6,28,s65,256
Mvaa 148,28,144
Da 72,v2,6144
Mvaa 140,32,180
Mvaa 140,28,140
W_ 9
Mvaa 176,36,140
s_nop 0
Ta 28,v1,0
Ta 30,v1,512
Mvaa 176,224,180
Da 112,v3,6144
Mvaa 184,36,144
Mvaa 184,224,184
s_nop 0
Ta 32,v1,4096
Ta 34,v1,4608
Mvaa 192,36,148
Da 76,v2,8192
Mvaa 192,224,188
Mvaa 200,36,152
Da 116,v3,8192
Mvaa 200,224,192
Mvaa 208,36,156
Mvaa 208,224,196
W_ 9
Mvaa 212,24,196
s_nop 0
Ta 36,v1,8
Ta 38,v1,520
Mvaa 212,20,156
Mvaa 204,24,192
Mvaa 204,20,152
s_nop 0
Ta 224,v1,4104
Ta 226,v1,4616
Mvaa 196,24,188
Mvaa 196,20,148
Mvaa 188,24,184
Mvaa 188,20,144
Mvaa 180,24,180
s_waitcnt vmcnt(15) lgkmcnt(0)
s_barrier
Mvaa 180,20,140
S_ s65,s59,3932160
Mvaa 216,28,140
S_ m0,s52,0
Ta 20,%0,8192
Ta 22,%0,8704
Mvaa 216,32,180
L_ v6,28,s65,0
Mvaa 224,28,144
S_ s65,s59,4030464
Mvaa 224,32,184
S_ m0,s52,1024
Ta 24,%0,12288
Ta 26,%0,12800
Mvaa 232,28,148
Mvaa 232,32,188
L_ v6,28,s65,0
Mvaa 240,28,152
S_ s65,s59,4128768
Mvaa 240,32,192
S_ m0,s52,2048
Mvaa 248,28,156
Mvaa 248,32,196
Mvaa 252,224,196
s_nop 0
Ta 28,%0,8200
Ta 30,%0,8712
Mvaa 252,36,156
L_ v6,28,s65,0
Mvaa 244,224,192
S_ s65,s59,4227072
Mvaa 244,36,152
S_ m0,s52,3072
Ta 32,%0,12296
Ta 34,%0,12808
Mvaa 236,224,188
Mvaa 236,36,148
L_ v6,28,s65,0
Mvaa 228,224,184
S_ s65,s60,768
Mvaa 228,36,144
S_ m0,s50,0
Mvaa 220,224,180
Mvaa 220,36,140
W_ 4
Mvaa 16,20,60
s_nop 0
Ta 36,v1,8192
Ta 38,v1,8704
Mvaa 16,24,100
L_ v4,24,s65,0
Mvaa 24,20,64
S_ s65,s60,11008
R_ v13,a248
Mvaa 24,24,104
S_ m0,s50,1024
Ta 224,v1,12288
Ta 226,v1,12800
Mvaa 32,20,68
v_lshlrev_b32 v13, 10, v13
Mvaa 32,24,108
L_ v5,24,s65,0
Mvaa 40,20,72
S_ s65,s60,21248
Mvaa 40,24,112
S_ m0,s50,2048
Mvaa 48,20,76
Mvaa 48,24,116
W_ 4
Mvaa 52,32,116
s_nop 0
Ta 20,v1,8200
Ta 22,v1,8712
Mvaa 52,28,76
L_ v4,24,s65,0
Mvaa 44,32,112
S_ s65,s60,31488
Mvaa 44,28,72
S_ m0,s50,3072
Ta 24,v1,12296
Ta 26,v1,12808
Mvaa 36,32,108
Mvaa 36,28,68
L_ v5,24,s65,0
Mvaa 28,32,104
S_ s65,s60,41728
Mvaa 28,28,64
S_ m0,s50,4096
Mvaa 20,32,100
s_waitcnt vmcnt(14) lgkmcnt(0)
s_barrier
Mvaa 20,28,60
L_ v4,24,s65,0
Mvaa 56,36,60
S_ s65,s59,3932160
Ta 28,%0,16384
Ta 30,%0,16896
Mvaa 56,224,100
S_ m0,s52,8064
Mvaa 64,36,64
Mvaa 64,224,104
s_nop 0
Ta 32,%0,20480
Ta 34,%0,20992
Mvaa 72,36,68
L_ v6,28,s65,128
Mvaa 72,224,108
S_ s65,s59,4030464
Mvaa 80,36,72
S_ m0,s52,9088
Mvaa 80,224,112
Mvaa 88,36,76
L_ v6,28,s65,128
Mvaa 88,224,116
S_ s65,s59,4128768
Mvaa 92,24,116
S_ m0,s52,10112
Ta 36,%0,16392
Ta 38,%0,16904
Mvaa 92,20,76
S_ s55,s56,6291456
Mvaa 84,24,112
L_ v6,28,s65,128
Mvaa 84,20,72
S_ s65,s59,4227072
Ta 224,%0,20488
Ta 226,%0,21000
Mvaa 76,24,108
S_ m0,s52,11136
Mvaa 76,20,68
Mvaa 68,24,104
L_ v6,28,s65,128
Mvaa 68,20,64
Mvaa 60,24,100
Mvaa 60,20,60
W_ 4
Mvaa 96,28,60
s_nop 0
Ta 20,v1,16384
Ta 22,v1,16896
Mvaa 96,32,100
buffer_load_dword a220, v13, s[44:47], s66 offen
Mvaa 104,28,64
Mvaa 104,32,104
s_nop 0
Ta 24,v1,20480
Ta 26,v1,20992
Mvaa 112,28,68
Mvaa 112,32,108
buffer_load_dword a254, v10, s[28:31], s55 offen
Mvaa 120,28,72
Mvaa 120,32,112
Mvaa 128,28,76
Mvaa 128,32,116
W_ 4
Mvaa 132,224,116
s_nop 0
Ta 28,v1,16392
Ta 30,v1,16904
Mvaa 132,36,76
buffer_load_dword a254, v12, s[24:27], s63 offen offset:1024
Mvaa 124,224,112
Mvaa 124,36,72
s_nop 0
Ta 32,v1,20488
Ta 34,v1,21000
Mvaa 116,224,108
Mvaa 116,36,68
Mvaa 108,224,104
Mvaa 108,36,64
Mvaa 100,224,100
s_waitcnt vmcnt(16) lgkmcnt(0)
s_barrier
Mvaa 100,36,60
S_ s65,s59,3932160
Da 140,v2,20480
Mvaa 136,20,60
S_ m0,s52,16128
Ta 36,%0,24576
Ta 38,%0,25088
Mvaa 136,24,100
L_ v6,28,s65,256
Mvaa 144,20,64
S_ s65,s59,4030464
Da 180,v3,20480
Mvaa 144,24,104
S_ m0,s52,17152
Ta 224,%0,28672
Ta 226,%0,29184
Mvaa 152,20,68
Mvaa 152,24,108
Da 144,v2,22528
Mvaa 160,20,72
L_ v6,28,s65,256
Mvaa 160,24,112
S_ s65,s59,4128768
Mvaa 168,20,76
S_ m0,s52,18176
Da 184,v3,22528
Mvaa 168,24,116
Mvaa 172,32,116
s_nop 0
Ta 20,%0,24584
Ta 22,%0,25096
Mvaa 172,28,76
Da 148,v2,24576
Mvaa 164,32,112
L_ v6,28,s65,256
Mvaa 164,28,72
S_ s65,s59,4227072
Ta 24,%0,28680
Ta 26,%0,29192
Mvaa 156,32,108
S_ m0,s52,19200
Da 188,v3,24576
Mvaa 156,28,68
Mvaa 148,32,104
L_ v6,28,s65,256
Mvaa 148,28,64
Da 152,v2,26624
Mvaa 140,32,100
Mvaa 140,28,60
W_ 9
Mvaa 176,36,60
s_nop 0
Ta 28,v1,24576
Ta 30,v1,25088
Mvaa 176,224,100
Da 192,v3,26624
Mvaa 184,36,64
Mvaa 184,224,104
s_nop 0
Ta 32,v1,28672
Ta 34,v1,29184
Mvaa 192,36,68
Da 156,v2,28672
Mvaa 192,224,108
Mvaa 200,36,72
Da 196,v3,28672
Mvaa 200,224,112
Mvaa 208,36,76
Mvaa 208,224,116
W_ 9
Mvaa 212,24,116
s_nop 0
Ta 36,v1,24584
Ta 38,v1,25096
Mvaa 212,20,76
Mvaa 204,24,112
Mvaa 204,20,72
s_nop 0
Ta 224,v1,28680
Ta 226,v1,29192
Mvaa 196,24,108
Mvaa 196,20,68
Mvaa 188,24,104
Mvaa 188,20,64
Mvaa 180,24,100
s_waitcnt vmcnt(16) lgkmcnt(0)
s_barrier
Mvaa 180,20,60
S_ s65,s59,4718592
Mvaa 216,28,60
S_ m0,s52,24576
Ta 20,%0,0
Ta 22,%0,512
Mvaa 216,32,100
L_ v6,28,s65,0
Mvaa 224,28,64
S_ s65,s59,4816896
Mvaa 224,32,104
S_ m0,s52,25600
Ta 24,%0,4096
Ta 26,%0,4608
Mvaa 232,28,68
Mvaa 232,32,108
L_ v6,28,s65,0
Mvaa 240,28,72
S_ s65,s59,4915200
Mvaa 240,32,112
S_ m0,s52,26624
Mvaa 248,28,76
Mvaa 248,32,116
Mvaa 252,224,116
s_nop 0
Ta 28,%0,8
Ta 30,%0,520
Mvaa 252,36,76
L_ v6,28,s65,0
Mvaa 244,224,112
S_ s65,s59,5013504
Mvaa 244,36,72
S_ m0,s52,27648
Ta 32,%0,4104
Ta 34,%0,4616
Mvaa 236,224,108
Mvaa 236,36,68
L_ v6,28,s65,0
Mvaa 228,224,104
S_ s65,s60,896
Mvaa 228,36,64
S_ m0,s50,20480
Mvaa 220,224,100
Mvaa 220,36,60
W_ 4
Mvaa 16,20,140
s_nop 0
Ta 36,v1,0
Ta 38,v1,512
Mvaa 16,24,180
L_ v4,24,s65,0
Mvaa 24,20,144
S_ s65,s60,11136
Mvaa 24,24,184
S_ m0,s50,21504
Ta 224,v1,4096
Ta 226,v1,4608
Mvaa 32,20,148
Mvaa 32,24,188
L_ v5,24,s65,0
Mvaa 40,20,152
S_ s65,s60,21376
Mvaa 40,24,192
S_ m0,s50,22528
Mvaa 48,20,156
Mvaa 48,24,196
W_ 4
Mvaa 52,32,196
s_nop 0
Ta 20,v1,8
Ta 22,v1,520
Mvaa 52,28,156
L_ v4,24,s65,0
Mvaa 44,32,192
S_ s65,s60,31616
Mvaa 44,28,152
S_ m0,s50,23552
Ta 24,v1,4104
Ta 26,v1,4616
Mvaa 36,32,188
Mvaa 36,28,148
L_ v5,24,s65,0
Mvaa 28,32,184
S_ s65,s60,41856
Mvaa 28,28,144
S_ m0,s50,24576
Mvaa 20,32,180
s_waitcnt vmcnt(15) lgkmcnt(0)
s_barrier
Mvaa 20,28,140
L_ v4,24,s65,0
Mvaa 56,36,140
S_ s65,s59,4718592
Ta 28,%0,8192
Ta 30,%0,8704
Mvaa 56,224,180
S_ m0,s52,-128
Mvaa 64,36,144
Mvaa 64,224,184
s_nop 0
Ta 32,%0,12288
Ta 34,%0,12800
Mvaa 72,36,148
L_ v6,28,s65,128
Mvaa 72,224,188
S_ s65,s59,4816896
Mvaa 80,36,152
S_ m0,s52,896
Mvaa 80,224,192
Mvaa 88,36,156
L_ v6,28,s65,128
Mvaa 88,224,196
S_ s65,s59,4915200
Mvaa 92,24,196
S_ m0,s52,1920
Ta 36,%0,8200
Ta 38,%0,8712
Mvaa 92,20,156
S_ s55,s56,7077888
Mvaa 84,24,192
L_ v6,28,s65,128
Mvaa 84,20,152
S_ s65,s59,5013504
Ta 224,%0,12296
Ta 226,%0,12808
Mvaa 76,24,188
S_ m0,s52,2944
Mvaa 76,20,148
Mvaa 68,24,184
L_ v6,28,s65,128
Mvaa 68,20,144
Mvaa 60,24,180
Mvaa 60,20,140
W_ 4
Mvaa 96,28,140
s_nop 0
Ta 20,v1,8192
Ta 22,v1,8704
Mvaa 96,32,180
buffer_load_dword a254, v10, s[28:31], s55 offen
Mvaa 104,28,144
Mvaa 104,32,184
s_nop 0
Ta 24,v1,12288
Ta 26,v1,12800
Mvaa 112,28,148
Mvaa 112,32,188
buffer_load_dword a254, v12, s[24:27], s63 offen offset:1152
Mvaa 120,28,152
Mvaa 120,32,192
Mvaa 128,28,156
Mvaa 128,32,196
W_ 4
Mvaa 132,224,196
s_nop 0
Ta 28,v1,8200
Ta 30,v1,8712
Mvaa 132,36,156
Mvaa 124,224,192
Mvaa 124,36,152
s_nop 0
Ta 32,v1,12296
Ta 34,v1,12808
Mvaa 116,224,188
Mvaa 116,36,148
Mvaa 108,224,184
Mvaa 108,36,144
Mvaa 100,224,180
s_waitcnt vmcnt(15) lgkmcnt(0)
s_barrier
Mvaa 100,36,140
S_ s65,s59,4718592
Da 60,v2,0
Mvaa 136,20,140
S_ m0,s52,7936
Ta 36,%0,16384
Ta 38,%0,16896
Mvaa 136,24,180
L_ v6,28,s65,256
Mvaa 144,20,144
S_ s65,s59,4816896
Da 100,v3,0
Mvaa 144,24,184
S_ m0,s52,8960
Ta 224,%0,20480
Ta 226,%0,20992
Mvaa 152,20,148
Mvaa 152,24,188
Da 64,v2,2048
Mvaa 160,20,152
L_ v6,28,s65,256
Mvaa 160,24,192
S_ s65,s59,4915200
Mvaa 168,20,156
S_ m0,s52,9984
Da 104,v3,2048
Mvaa 168,24,196
Mvaa 172,32,196
s_nop 0
Ta 20,%0,16392
Ta 22,%0,16904
Mvaa 172,28,156
Da 68,v2,4096
Mvaa 164,32,192
L_ v6,28,s65,256
Mvaa 164,28,152
S_ s65,s59,5013504
Ta 24,%0,20488
Ta 26,%0,21000
Mvaa 156,32,188
S_ m0,s52,11008
Da 108,v3,4096
Mvaa 156,28,148
Mvaa 148,32,184
L_ v6,28,s65,256
Mvaa 148,28,144
Da 72,v2,6144
Mvaa 140,32,180
Mvaa 140,28,140
W_ 9
Mvaa 176,36,140
s_nop 0
Ta 28,v1,16384
Ta 30,v1,16896
Mvaa 176,224,180
Da 112,v3,6144
Mvaa 184,36,144
Mvaa 184,224,184
s_nop 0
Ta 32,v1,20480
Ta 34,v1,20992
Mvaa 192,36,148
Da 76,v2,8192
Mvaa 192,224,188
Mvaa 200,36,152
Da 116,v3,8192
Mvaa 200,224,192
Mvaa 208,36,156
Mvaa 208,224,196
W_ 9
Mvaa 212,24,196
s_nop 0
Ta 36,v1,16392
Ta 38,v1,16904
Mvaa 212,20,156
Mvaa 204,24,192
Mvaa 204,20,152
s_nop 0
Ta 224,v1,20488
Ta 226,v1,21000
Mvaa 196,24,188
)A"
R"A(Mvaa 196,20,148
Mvaa 188,24,184
Mvaa 188,20,144
Mvaa 180,24,180
s_waitcnt vmcnt(15) lgkmcnt(0)
s_barrier
Mvaa 180,20,140
S_ s65,s59,5505024
Mvaa 216,28,140
S_ m0,s52,16384
Ta 20,%0,24576
Ta 22,%0,25088
Mvaa 216,32,180
L_ v6,28,s65,0
Mvaa 224,28,144
S_ s65,s59,5603328
Mvaa 224,32,184
S_ m0,s52,17408
Ta 24,%0,28672
Ta 26,%0,29184
Mvaa 232,28,148
Mvaa 232,32,188
L_ v6,28,s65,0
Mvaa 240,28,152
S_ s65,s59,5701632
Mvaa 240,32,192
S_ m0,s52,18432
Mvaa 248,28,156
Mvaa 248,32,196
Mvaa 252,224,196
s_nop 0
Ta 28,%0,24584
Ta 30,%0,25096
Mvaa 252,36,156
L_ v6,28,s65,0
Mvaa 244,224,192
S_ s65,s59,5799936
Mvaa 244,36,152
S_ m0,s52,19456
Ta 32,%0,28680
Ta 34,%0,29192
Mvaa 236,224,188
Mvaa 236,36,148
L_ v6,28,s65,0
Mvaa 228,224,184
S_ s65,s60,1024
Mvaa 228,36,144
S_ m0,s50,0
Mvaa 220,224,180
Mvaa 220,36,140
W_ 4
Mvaa 16,20,60
s_nop 0
Ta 36,v1,24576
Ta 38,v1,25088
Mvaa 16,24,100
L_ v4,24,s65,0
Mvaa 24,20,64
S_ s65,s60,11264
Mvaa 24,24,104
S_ m0,s50,1024
Ta 224,v1,28672
Ta 226,v1,29184
Mvaa 32,20,68
Mvaa 32,24,108
L_ v5,24,s65,0
Mvaa 40,20,72
S_ s65,s60,21504
Mvaa 40,24,112
S_ m0,s50,2048
Mvaa 48,20,76
Mvaa 48,24,116
W_ 4
Mvaa 52,32,116
s_nop 0
Ta 20,v1,24584
Ta 22,v1,25096
Mvaa 52,28,76
L_ v4,24,s65,0
Mvaa 44,32,112
S_ s65,s60,31744
Mvaa 44,28,72
S_ m0,s50,3072
Ta 24,v1,28680
Ta 26,v1,29192
Mvaa 36,32,108
Mvaa 36,28,68
L_ v5,24,s65,0
Mvaa 28,32,104
S_ s65,s60,41984
Mvaa 28,28,64
S_ m0,s50,4096
Mvaa 20,32,100
s_waitcnt vmcnt(14) lgkmcnt(0)
s_barrier
Mvaa 20,28,60
L_ v4,24,s65,0
Mvaa 56,36,60
S_ s65,s59,5505024
Ta 28,%0,0
Ta 30,%0,512
Mvaa 56,224,100
S_ m0,s52,24448
Mvaa 64,36,64
Mvaa 64,224,104
s_nop 0
Ta 32,%0,4096
Ta 34,%0,4608
Mvaa 72,36,68
L_ v6,28,s65,128
Mvaa 72,224,108
S_ s65,s59,5603328
Mvaa 80,36,72
S_ m0,s52,25472
Mvaa 80,224,112
Mvaa 88,36,76
L_ v6,28,s65,128
Mvaa 88,224,116
S_ s65,s59,5701632
Mvaa 92,24,116
S_ m0,s52,26496
Ta 36,%0,8
Ta 38,%0,520
Mvaa 92,20,76
Mvaa 84,24,112
L_ v6,28,s65,128
Mvaa 84,20,72
S_ s65,s59,5799936
Ta 224,%0,4104
Ta 226,%0,4616
Mvaa 76,24,108
S_ m0,s52,27520
Mvaa 76,20,68
Mvaa 68,24,104
L_ v6,28,s65,128
Mvaa 68,20,64
Mvaa 60,24,100
Mvaa 60,20,60
W_ 4
Mvaa 96,28,60
s_nop 0
Ta 20,v1,0
Ta 22,v1,512
Mvaa 96,32,100
Mvaa 104,28,64
Mvaa 104,32,104
s_nop 0
Ta 24,v1,4096
Ta 26,v1,4608
Mvaa 112,28,68
Mvaa 112,32,108
Mvaa 120,28,72
Mvaa 120,32,112
Mvaa 128,28,76
Mvaa 128,32,116
W_ 4
Mvaa 132,224,116
s_nop 0
Ta 28,v1,8
Ta 30,v1,520
Mvaa 132,36,76
Mvaa 124,224,112
Mvaa 124,36,72
s_nop 0
Ta 32,v1,4104
Ta 34,v1,4616
Mvaa 116,224,108
Mvaa 116,36,68
Mvaa 108,224,104
Mvaa 108,36,64
Mvaa 100,224,100
s_waitcnt vmcnt(13) lgkmcnt(0)
s_barrier
Mvaa 100,36,60
S_ s65,s59,5505024
Da 140,v2,20480
Mvaa 136,20,60
S_ m0,s52,-256
Ta 36,%0,8192
Ta 38,%0,8704
Mvaa 136,24,100
L_ v6,28,s65,256
Mvaa 144,20,64
S_ s65,s59,5603328
Da 180,v3,20480
Mvaa 144,24,104
S_ m0,s52,768
Ta 224,%0,12288
Ta 226,%0,12800
Mvaa 152,20,68
Mvaa 152,24,108
Da 144,v2,22528
Mvaa 160,20,72
L_ v6,28,s65,256
Mvaa 160,24,112
S_ s65,s59,5701632
Mvaa 168,20,76
S_ m0,s52,1792
Da 184,v3,22528
Mvaa 168,24,116
Mvaa 172,32,116
s_nop 0
Ta 20,%0,8200
Ta 22,%0,8712
Mvaa 172,28,76
Da 148,v2,24576
Mvaa 164,32,112
L_ v6,28,s65,256
Mvaa 164,28,72
S_ s65,s59,5799936
Ta 24,%0,12296
Ta 26,%0,12808
Mvaa 156,32,108
S_ m0,s52,2816
Da 188,v3,24576
Mvaa 156,28,68
Mvaa 148,32,104
L_ v6,28,s65,256
Mvaa 148,28,64
Da 152,v2,26624
Mvaa 140,32,100
Mvaa 140,28,60
W_ 9
Mvaa 176,36,60
s_nop 0
Ta 28,v1,8192
Ta 30,v1,8704
Mvaa 176,224,100
Da 192,v3,26624
Mvaa 184,36,64
Mvaa 184,224,104
s_nop 0
Ta 32,v1,12288
Ta 34,v1,12800
Mvaa 192,36,68
Da 156,v2,28672
Mvaa 192,224,108
Mvaa 200,36,72
Da 196,v3,28672
Mvaa 200,224,112
Mvaa 208,36,76
Mvaa 208,224,116
W_ 9
Mvaa 212,24,116
s_nop 0
Ta 36,v1,8200
Ta 38,v1,8712
Mvaa 212,20,76
Mvaa 204,24,112
Mvaa 204,20,72
s_nop 0
Ta 224,v1,12296
Ta 226,v1,12808
Mvaa 196,24,108
Mvaa 196,20,68
Mvaa 188,24,104
Mvaa 188,20,64
Mvaa 180,24,100
s_waitcnt vmcnt(13) lgkmcnt(0)
s_barrier
Mvaa 180,20,60
S_ s65,s59,6291456
Mvaa 216,28,60
S_ m0,s52,7936
Ta 20,%0,16384
Ta 22,%0,16896
Mvaa 216,32,100
L_ v6,28,s65,256
Mvaa 224,28,64
S_ s65,s59,6389760
Mvaa 224,32,104
S_ m0,s52,8960
Ta 24,%0,20480
Ta 26,%0,20992
Mvaa 232,28,68
Mvaa 232,32,108
L_ v6,28,s65,256
Mvaa 240,28,72
S_ s65,s59,6488064
Mvaa 240,32,112
S_ m0,s52,9984
Mvaa 248,28,76
Mvaa 248,32,116
Mvaa 252,224,116
s_nop 0
Ta 28,%0,16392
Ta 30,%0,16904
Mvaa 252,36,76
L_ v6,28,s65,256
Mvaa 244,224,112
S_ s65,s59,6586368
Mvaa 244,36,72
S_ m0,s52,11008
Ta 32,%0,20488
Ta 34,%0,21000
Mvaa 236,224,108
Mvaa 236,36,68
L_ v6,28,s65,256
Mvaa 228,224,104
S_ s65,s60,1152
Mvaa 228,36,64
S_ m0,s50,20480
Mvaa 220,224,100
Mvaa 220,36,60
W_ 4
Mvaa 16,20,140
s_nop 0
Ta 36,v1,16384
Ta 38,v1,16896
Mvaa 16,24,180
L_ v4,24,s65,0
Mvaa 24,20,144
S_ s65,s60,11392
Mvaa 24,24,184
S_ m0,s50,21504
Ta 224,v1,20480
Ta 226,v1,20992
Mvaa 32,20,148
Mvaa 32,24,188
L_ v5,24,s65,0
Mvaa 40,20,152
S_ s65,s60,21632
Mvaa 40,24,192
S_ m0,s50,22528
Mvaa 48,20,156
Mvaa 48,24,196
W_ 4
Mvaa 52,32,196
s_nop 0
Ta 20,v1,16392
Ta 22,v1,16904
Mvaa 52,28,156
L_ v4,24,s65,0
Mvaa 44,32,192
S_ s65,s60,31872
Mvaa 44,28,152
S_ m0,s50,23552
Ta 24,v1,20488
Ta 26,v1,21000
Mvaa 36,32,188
Mvaa 36,28,148
L_ v5,24,s65,0
Mvaa 28,32,184
S_ s65,s60,42112
Mvaa 28,28,144
S_ m0,s50,24576
Mvaa 20,32,180
s_waitcnt vmcnt(12) lgkmcnt(0)
s_barrier
Mvaa 20,28,140
L_ v4,24,s65,0
Mvaa 56,36,140
S_ s65,s59,7077888
Ta 28,%0,24576
Ta 30,%0,25088
Mvaa 56,224,180
S_ m0,s52,16128
Mvaa 64,36,144
Mvaa 64,224,184
s_nop 0
Ta 32,%0,28672
Ta 34,%0,29184
Mvaa 72,36,148
L_ v6,28,s65,256
Mvaa 72,224,188
S_ s65,s59,7176192
Mvaa 80,36,152
S_ m0,s52,17152
Mvaa 80,224,192
Mvaa 88,36,156
L_ v6,28,s65,256
Mvaa 88,224,196
S_ s65,s59,7274496
Mvaa 92,24,196
S_ m0,s52,18176
Ta 36,%0,24584
Ta 38,%0,25096
Mvaa 92,20,156
Mvaa 84,24,192
L_ v6,28,s65,256
Mvaa 84,20,152
S_ s65,s59,7372800
Ta 224,%0,28680
Ta 226,%0,29192
Mvaa 76,24,188
S_ m0,s52,19200
Mvaa 76,20,148
Mvaa 68,24,184
L_ v6,28,s65,256
Mvaa 68,20,144
Mvaa 60,24,180
Mvaa 60,20,140
W_ 4
Mvaa 96,28,140
s_nop 0
Ta 20,v1,24576
Ta 22,v1,25088
Mvaa 96,32,180
Mvaa 104,28,144
Mvaa 104,32,184
s_nop 0
Ta 24,v1,28672
Ta 26,v1,29184
Mvaa 112,28,148
Mvaa 112,32,188
Mvaa 120,28,152
Mvaa 120,32,192
Mvaa 128,28,156
Mvaa 128,32,196
W_ 4
Mvaa 132,224,196
s_nop 0
Ta 28,v1,24584
Ta 30,v1,25096
Mvaa 132,36,156
Mvaa 124,224,192
Mvaa 124,36,152
s_nop 0
Ta 32,v1,28680
Ta 34,v1,29192
Mvaa 116,224,188
Mvaa 116,36,148
Mvaa 108,224,184
Mvaa 108,36,144
Mvaa 100,224,180
s_waitcnt vmcnt(13) lgkmcnt(0)
s_barrier
Mvaa 100,36,140
S_ s65,s59,6291456
Da 60,v2,0
Mvaa 136,20,140
S_ m0,s52,24448
Ta 36,%0,0
Ta 38,%0,512
Mvaa 136,24,180
L_ v6,28,s65,128
Mvaa 144,20,144
S_ s65,s59,6389760
Da 100,v3,0
Mvaa 144,24,184
S_ m0,s52,25472
Ta 224,%0,4096
Ta 226,%0,4608
Mvaa 152,20,148
Mvaa 152,24,188
Da 64,v2,2048
Mvaa 160,20,152
L_ v6,28,s65,128
Mvaa 160,24,192
S_ s65,s59,6488064
Mvaa 168,20,156
S_ m0,s52,26496
Da 104,v3,2048
Mvaa 168,24,196
Mvaa 172,32,196
s_nop 0
Ta 20,%0,8
Ta 22,%0,520
Mvaa 172,28,156
Da 68,v2,4096
Mvaa 164,32,192
L_ v6,28,s65,128
Mvaa 164,28,152
S_ s65,s59,6586368
Ta 24,%0,4104
Ta 26,%0,4616
Mvaa 156,32,188
S_ m0,s52,27520
Da 108,v3,4096
Mvaa 156,28,148
Mvaa 148,32,184
L_ v6,28,s65,128
Mvaa 148,28,144
Da 72,v2,6144
Mvaa 140,32,180
Mvaa 140,28,140
W_ 9
Mvaa 176,36,140
s_nop 0
Ta 28,v1,0
Ta 30,v1,512
Mvaa 176,224,180
Da 112,v3,6144
Mvaa 184,36,144
Mvaa 184,224,184
s_nop 0
Ta 32,v1,4096
Ta 34,v1,4608
Mvaa 192,36,148
Da 76,v2,8192
Mvaa 192,224,188
Mvaa 200,36,152
Da 116,v3,8192
Mvaa 200,224,192
Mvaa 208,36,156
Mvaa 208,224,196
W_ 9
Mvaa 212,24,196
s_nop 0
Ta 36,v1,8
Ta 38,v1,520
Mvaa 212,20,156
Mvaa 204,24,192
Mvaa 204,20,152
s_nop 0
Ta 224,v1,4104
Ta 226,v1,4616
Mvaa 196,24,188
Mvaa 196,20,148
Mvaa 188,24,184
Mvaa 188,20,144
Mvaa 180,24,180
s_waitcnt vmcnt(8) lgkmcnt(0)
s_barrier
Mvaa 180,20,140
S_ s65,s59,7077888
Mvaa 216,28,140
S_ m0,s52,-128
Ta 20,v1,8200
Ta 22,v1,8712
Mvaa 216,32,180
L_ v6,28,s65,128
Mvaa 224,28,144
S_ s65,s59,7176192
Mvaa 224,32,184
S_ m0,s52,896
Ta 24,v1,12296
Ta 26,v1,12808
Mvaa 232,28,148
Mvaa 232,32,188
L_ v6,28,s65,128
Mvaa 240,28,152
S_ s65,s59,7274496
Mvaa 240,32,192
S_ m0,s52,1920
Mvaa 248,28,156
Mvaa 248,32,196
Mvaa 252,224,196
s_nop 0
Ta 28,v1,8192
Ta 30,v1,8704
Mvaa 252,36,156
L_ v6,28,s65,128
Mvaa 244,224,192
S_ s65,s59,7372800
Mvaa 244,36,152
S_ m0,s52,2944
Ta 32,v1,12288
Ta 34,v1,12800
Mvaa 236,224,188
Mvaa 236,36,148
L_ v6,28,s65,128
Mvaa 228,224,184
S_ s65,s60,0
Mvaa 228,36,144
S_ m0,s50,0
Mvaa 220,224,180
Mvaa 220,36,140
W_ 4
Mvaa 220,20,60
s_nop 0
Ta 36,%0,8200
Ta 38,%0,8712
Mvaa 220,24,100
Da 140,v2,20480
Mvaa 228,20,64
L_ v4,24,s65,0
Mvaa 228,24,104
S_ s65,s60,10240
Ta 224,%0,12296
Ta 226,%0,12808
Mvaa 236,20,68
S_ m0,s50,1024
Da 180,v3,20480
Mvaa 236,24,108
Mvaa 244,20,72
Da 144,v2,22528
Mvaa 244,24,112
L_ v5,24,s65,0
Mvaa 252,20,76
S_ s65,s60,20480
Mvaa 252,24,116
W_ 7
Da 184,v3,22528
S_ m0,s50,2048
Mvaa 248,32,116
s_nop 0
Ta 20,%0,8192
Ta 22,%0,8704
Mvaa 248,28,76
L_ v4,24,s65,0
Mvaa 240,32,112
S_ s65,s60,30720
Da 148,v2,24576
Mvaa 240,28,72
S_ m0,s50,3072
Ta 24,%0,12288
Ta 26,%0,12800
Mvaa 232,32,108
Mvaa 232,28,68
Da 188,v3,24576
Mvaa 224,32,104
L_ v5,24,s65,0
Mvaa 224,28,64
S_ s65,s60,40960
Mvaa 216,32,100
s_waitcnt vmcnt(12) lgkmcnt(0)
s_barrier
Da 152,v2,26624
Mvaa 216,28,60
S_ m0,s50,4096
Mvaa 180,36,60
s_nop 0
Ta 28,v1,16392
Ta 30,v1,16904
Mvaa 180,224,100
Da 192,v3,26624
Mvaa 188,36,64
L_ v4,24,s65,0
Mvaa 188,224,104
S_ s65,s59,6291456
Ta 32,v1,20488
Ta 34,v1,21000
Mvaa 196,36,68
S_ m0,s52,8192
Da 156,v2,28672
Mvaa 196,224,108
Mvaa 204,36,72
L_ v6,28,s65,0
Mvaa 204,224,112
S_ s65,s59,6389760
Da 196,v3,28672
Mvaa 212,36,76
S_ m0,s52,9216
Mvaa 212,224,116
Mvaa 208,24,116
s_nop 0
Ta 36,v1,16384
Ta 38,v1,16896
Mvaa 208,20,76
L_ v6,28,s65,0
Mvaa 200,24,112
S_ s65,s59,6488064
Mvaa 200,20,72
S_ m0,s52,10240
Ta 224,v1,20480
Ta 226,v1,20992
Mvaa 192,24,108
Mvaa 192,20,68
L_ v6,28,s65,0
Mvaa 184,24,104
S_ s65,s59,6586368
Mvaa 184,20,64
S_ m0,s52,11264
Mvaa 176,24,100
Mvaa 176,20,60
W_ 6
Mvaa 220,28,140
s_nop 0
Ta 20,%0,16392
Ta 22,%0,16904
Mvaa 220,32,180
L_ v6,28,s65,0
Mvaa 228,28,144
Mvaa 228,32,184
s_nop 0
Ta 24,%0,20488
Ta 26,%0,21000
Mvaa 236,28,148
Mvaa 236,32,188
Mvaa 244,28,152
Mvaa 244,32,192
W_ 9
Mvaa 252,28,156
W_ 8
Mvaa 252,32,196
W_ 4
Mvaa 248,224,196
s_nop 0
Ta 28,%0,16384
Ta 30,%0,16896
Mvaa 248,36,156
Mvaa 240,224,192
Mvaa 240,36,152
s_nop 0
Ta 32,%0,20480
Ta 34,%0,20992
Mvaa 232,224,188
Mvaa 232,36,148
Mvaa 224,224,184
Mvaa 224,36,144
Mvaa 216,224,180
s_waitcnt vmcnt(13) lgkmcnt(0)
s_barrier
Mvaa 216,36,140
S_ s65,s59,7077888
Mvaa 180,20,140
S_ m0,s52,16384
Ta 36,v1,24584
Ta 38,v1,25096
Mvaa 180,24,180
L_ v6,28,s65,0
Mvaa 188,20,144
S_ s65,s59,7176192
Mvaa 188,24,184
S_ m0,s52,17408
Ta 224,v1,28680
Ta 226,v1,29192
Mvaa 196,20,148
U_ v248,s48,v248
U_ v249,s48,v249
Mvaa 196,24,188
L_ v6,28,s65,0
Mvaa 204,20,152
S_ s65,s59,7274496
U_ v250,s48,v250
U_ v251,s48,v251
Mvaa 204,24,192
S_ m0,s52,18432
U_ v252,s48,v252
U_ v253,s48,v253
Mvaa 212,20,156
U_ v254,s48,v254
U_ v255,s48,v255
Mvaa 212,24,196
C_ 255,254,255
C_ 254,252,253
Mvaa 208,32,196
s_nop 0
Ta 20,v1,24576
Ta 22,v1,25088
Mvaa 208,28,156
L_ v6,28,s65,0
Mvaa 200,32,192
S_ s65,s59,7372800
C_ 253,250,251
C_ 252,248,249
Mvaa 200,28,152
S_ m0,s52,19456
Ta 24,v1,28672
Ta 26,v1,29184
Mvaa 192,32,188
X_ a216,v252
X_ a217,v253
Mvaa 192,28,148
L_ v6,28,s65,0
Mvaa 184,32,184
X_ a218,v254
X_ a219,v255
Mvaa 184,28,144
U_ v240,s48,v240
U_ v241,s48,v241
Mvaa 176,32,180
U_ v242,s48,v242
U_ v243,s48,v243
Mvaa 176,28,140
W_ 4
U_ v244,s48,v244
U_ v245,s48,v245
Mvaa 140,36,60
s_nop 0
Ta 28,%0,24584
Ta 30,%0,25096
Mvaa 140,224,100
U_ v246,s48,v246
U_ v247,s48,v247
Mvaa 148,36,64
C_ 251,246,247
C_ 250,244,245
Mvaa 148,224,104
s_nop 0
Ta 32,%0,28680
Ta 34,%0,29192
Mvaa 156,36,68
C_ 249,242,243
C_ 248,240,241
Mvaa 156,224,108
X_ a212,v248
X_ a213,v249
Mvaa 164,36,72
X_ a214,v250
X_ a215,v251
Mvaa 164,224,112
U_ v232,s48,v232
U_ v233,s48,v233
Mvaa 172,36,76
U_ v234,s48,v234
U_ v235,s48,v235
Mvaa 172,224,116
W_ 4
U_ v236,s48,v236
U_ v237,s48,v237
Mvaa 168,24,116
s_nop 0
Ta 36,%0,24576
Ta 38,%0,25088
Mvaa 168,20,76
U_ v238,s48,v238
U_ v239,s48,v239
Mvaa 160,24,112
C_ 247,238,239
C_ 246,236,237
Mvaa 160,20,72
s_nop 0
Ta 224,%0,28672
Ta 226,%0,29184
Mvaa 152,24,108
C_ 245,234,235
C_ 244,232,233
Mvaa 152,20,68
X_ a208,v244
X_ a209,v245
Mvaa 144,24,104
X_ a210,v246
X_ a211,v247
Mvaa 144,20,64
U_ v224,s48,v224
U_ v225,s48,v225
Mvaa 136,24,100
s_waitcnt vmcnt(13) lgkmcnt(0)
s_barrier
U_ v226,s48,v226
Mvaa 136,20,60
S_ s65,s59,-768
U_ v227,s48,v227
Mvaa 100,28,60
S_ m0,s52,24320
Ta 20,v1,8
Ta 22,v1,520
Mvaa 100,32,100
L_ v6,28,s65,256
Mvaa 108,28,64
S_ s65,s59,97536
U_ v228,s48,v228
U_ v229,s48,v229
Mvaa 108,32,104
S_ m0,s52,25344
Ta 24,v1,4104
Ta 26,v1,4616
Mvaa 116,28,68
U_ v230,s48,v230
U_ v231,s48,v231
Mvaa 116,32,108
L_ v6,28,s65,256
Mvaa 124,28,72
S_ s65,s59,195840
C_ 243,230,231
C_ 242,228,229
Mvaa 124,32,112
S_ m0,s52,26368
C_ 241,226,227
C_ 240,224,225
Mvaa 132,28,76
X_ a204,v240
X_ a205,v241
Mvaa 132,32,116
X_ a206,v242
X_ a207,v243
Mvaa 128,224,116
s_nop 0
Ta 28,v1,0
Ta 30,v1,512
Mvaa 128,36,76
L_ v6,28,s65,256
Mvaa 120,224,112
S_ s65,s59,294144
U_ v216,s48,v216
U_ v217,s48,v217
Mvaa 120,36,72
S_ m0,s52,27392
Ta 32,v1,4096
Ta 34,v1,4608
Mvaa 112,224,108
U_ v218,s48,v218
U_ v219,s48,v219
Mvaa 112,36,68
L_ v6,28,s65,256
Mvaa 104,224,104
S_ s65,s60,128
U_ v220,s48,v220
U_ v221,s48,v221
Mvaa 104,36,64
S_ m0,s50,20480
U_ v222,s48,v222
U_ v223,s48,v223
Mvaa 96,224,100
C_ 239,222,223
C_ 238,220,221
Mvaa 96,36,60
W_ 4
C_ 237,218,219
C_ 236,216,217
Mvaa 140,20,140
s_nop 0
Ta 36,%0,8
Ta 38,%0,520
Mvaa 140,24,180
L_ v4,24,s65,0
Mvaa 148,20,144
S_ s65,s60,10368
X_ a200,v236
X_ a201,v237
Mvaa 148,24,184
S_ m0,s50,21504
Ta 224,%0,4104
Ta 226,%0,4616
Mvaa 156,20,148
X_ a202,v238
X_ a203,v239
Mvaa 156,24,188
L_ v5,24,s65,0
Mvaa 164,20,152
S_ s65,s60,20608
U_ v208,s48,v208
U_ v209,s48,v209
Mvaa 164,24,192
S_ m0,s50,22528
U_ v210,s48,v210
U_ v211,s48,v211
Mvaa 172,20,156
U_ v212,s48,v212
U_ v213,s48,v213
Mvaa 172,24,196
W_ 4
U_ v214,s48,v214
U_ v215,s48,v215
Mvaa 168,32,196
s_nop 0
Ta 20,%0,0
Ta 22,%0,512
Mvaa 168,28,156
L_ v4,24,s65,0
Mvaa 160,32,192
S_ s65,s60,30848
C_ 235,214,215
C_ 234,212,213
Mvaa 160,28,152
S_ m0,s50,23552
Ta 24,%0,4096
Ta 26,%0,4608
Mvaa 152,32,188
C_ 233,210,211
C_ 232,208,209
Mvaa 152,28,148
L_ v5,24,s65,0
Mvaa 144,32,184
S_ s65,s60,41088
X_ a176,v232
X_ a177,v233
Mvaa 144,28,144
S_ m0,s50,24576
X_ a178,v234
X_ a179,v235
Mvaa 136,32,180
s_waitcnt vmcnt(12) lgkmcnt(0)
s_barrier
U_ v200,s48,v200
Mvaa 136,28,140
L_ v4,24,s65,0
Mvaa 100,36,140
S_ s65,s59,785664
Ta 28,v1,8200
Ta 30,v1,8712
Mvaa 100,224,180
S_ m0,s52,0
U_ v201,s48,v201
Mvaa 108,36,144
U_ v202,s48,v202
U_ v203,s48,v203
Mvaa 108,224,184
s_nop 0
Ta 32,v1,12296
Ta 34,v1,12808
Mvaa 116,36,148
L_ v6,28,s65,0
Mvaa 116,224,188
S_ s65,s59,883968
U_ v204,s48,v204
U_ v205,s48,v205
Mvaa 124,36,152
S_ m0,s52,1024
U_ v206,s48,v206
U_ v207,s48,v207
Mvaa 124,224,192
C_ 231,206,207
C_ 230,204,205
Mvaa 132,36,156
L_ v6,28,s65,0
Mvaa 132,224,196
C_ 229,202,203
S_ s65,s59,982272
Mvaa 128,24,196
S_ m0,s52,2048
Ta 36,v1,8192
Ta 38,v1,8704
Mvaa 128,20,156
C_ 228,200,201
Mvaa 120,24,192
L_ v6,28,s65,0
Mvaa 120,20,152
S_ s65,s59,1080576
Ta 224,v1,12288
Ta 226,v1,12800
Mvaa 112,24,188
S_ m0,s52,3072
X_ a172,v228
X_ a173,v229
Mvaa 112,20,148
X_ a174,v230
X_ a175,v231
Mvaa 104,24,184
L_ v6,28,s65,0
Mvaa 104,20,144
U_ v192,s48,v192
U_ v193,s48,v193
Mvaa 96,24,180
U_ v194,s48,v194
U_ v195,s48,v195
Mvaa 96,20,140
W_ 4
U_ v196,s48,v196
U_ v197,s48,v197
Mvaa 60,28,60
s_nop 0
Ta 20,%0,8200
Ta 22,%0,8712
Mvaa 60,32,100
U_ v198,s48,v198
U_ v199,s48,v199
Mvaa 68,28,64
C_ 227,198,199
C_ 226,196,197
Mvaa 68,32,104
s_nop 0
Ta 24,%0,12296
Ta 26,%0,12808
Mvaa 76,28,68
C_ 225,194,195
C_ 224,192,193
Mvaa 76,32,108
X_ a168,v224
X_ a169,v225
Mvaa 84,28,72
X_ a170,v226
X_ a171,v227
Mvaa 84,32,112
U_ v184,s48,v184
U_ v185,s48,v185
Mvaa 92,28,76
U_ v186,s48,v186
U_ v187,s48,v187
Mvaa 92,32,116
W_ 4
U_ v188,s48,v188
U_ v189,s48,v189
Mvaa 88,224,116
s_nop 0
Ta 28,%0,8192
Ta 30,%0,8704
Mvaa 88,36,76
U_ v190,s48,v190
U_ v191,s48,v191
Mvaa 80,224,112
C_ 223,190,191
C_ 222,188,189
Mvaa 80,36,72
s_nop 0
Ta 32,%0,12288
Ta 34,%0,12800
Mvaa 72,224,108
C_ 221,186,187
C_ 220,184,185
Mvaa 72,36,68
X_ a164,v220
X_ a165,v221
Mvaa 64,224,104
X_ a166,v222
X_ a167,v223
Mvaa 64,36,64
U_ v176,s48,v176
U_ v177,s48,v177
Mvaa 56,224,100
s_waitcnt vmcnt(13) lgkmcnt(0)
s_barrier
U_ v178,s48,v178
Mvaa 56,36,60
S_ s65,s59,-768
U_ v179,s48,v179
Mvaa 20,20,60
S_ m0,s52,8192
Ta 36,v1,16392
Ta 38,v1,16904
Mvaa 20,24,100
L_ v6,28,s65,0
Mvaa 28,20,64
S_ s65,s59,97536
U_ v180,s48,v180
U_ v181,s48,v181
Mvaa 28,24,104
S_ m0,s52,9216
Ta 224,v1,20488
Ta 226,v1,21000
Mvaa 36,20,68
U_ v182,s48,v182
U_ v183,s48,v183
Mvaa 36,24,108
L_ v6,28,s65,0
Mvaa 44,20,72
S_ s65,s59,195840
C_ 219,182,183
C_ 218,180,181
Mvaa 44,24,112
S_ m0,s52,10240
C_ 217,178,179
C_ 216,176,177
Mvaa 52,20,76
X_ a160,v216
X_ a161,v217
Mvaa 52,24,116
X_ a162,v218
X_ a163,v219
Mvaa 48,32,116
s_nop 0
Ta 20,v1,16384
Ta 22,v1,16896
Mvaa 48,28,76
L_ v6,28,s65,0
Mvaa 40,32,112
S_ s65,s59,294144
U_ v168,s48,v168
U_ v169,s48,v169
Mvaa 40,28,72
S_ m0,s52,11264
Ta 24,v1,20480
Ta 26,v1,20992
Mvaa 32,32,108
U_ v170,s48,v170
U_ v171,s48,v171
Mvaa 32,28,68
L_ v6,28,s65,0
Mvaa 24,32,104
U_ v172,s48,v172
U_ v173,s48,v173
Mvaa 24,28,64
U_ v174,s48,v174
U_ v175,s48,v175
Mvaa 16,32,100
C_ 215,174,175
C_ 214,172,173
Mvaa 16,28,60
W_ 4
C_ 213,170,171
C_ 212,168,169
Mvaa 60,36,140
s_nop 0
Ta 28,%0,16392
Ta 30,%0,16904
Mvaa 60,224,180
X_ a136,v212
X_ a137,v213
Mvaa 68,36,144
X_ a138,v214
X_ a139,v215
Mvaa 68,224,184
s_nop 0
Ta 32,%0,20488
Ta 34,%0,21000
Mvaa 76,36,148
U_ v160,s48,v160
U_ v161,s48,v161
Mvaa 76,224,188
U_ v162,s48,v162
U_ v163,s48,v163
Mvaa 84,36,152
U_ v164,s48,v164
U_ v165,s48,v165
Mvaa 84,224,192
U_ v166,s48,v166
U_ v167,s48,v167
Mvaa 92,36,156
C_ 211,166,167
C_ 210,164,165
Mvaa 92,224,196
W_ 4
C_ 209,162,163
C_ 208,160,161
Mvaa 88,24,196
s_nop 0
Ta 36,%0,16384
Ta 38,%0,16896
Mvaa 88,20,156
X_ a132,v208
X_ a133,v209
Mvaa 80,24,192
X_ a134,v210
X_ a135,v211
Mvaa 80,20,152
s_nop 0
Ta 224,%0,20480
Ta 226,%0,20992
Mvaa 72,24,188
U_ v152,s48,v152
U_ v153,s48,v153
Mvaa 72,20,148
U_ v154,s48,v154
U_ v155,s48,v155
Mvaa 64,24,184
U_ v156,s48,v156
U_ v157,s48,v157
Mvaa 64,20,144
U_ v158,s48,v158
U_ v159,s48,v159
Mvaa 56,24,180
s_waitcnt vmcnt(63) lgkmcnt(0)
s_barrier
C_ 207,158,159
Mvaa 56,20,140
S_ s65,s59,-768
C_ 206,156,157
Mvaa 20,28,140
S_ m0,s52,16256
C_ 205,154,155
C_ 204,152,153
Mvaa 20,32,180
L_ v6,28,s65,128
Mvaa 28,28,144
S_ s65,s59,97536
X_ a128,v204
X_ a129,v205
Mvaa 28,32,184
S_ m0,s52,17280
X_ a130,v206
X_ a131,v207
Mvaa 36,28,148
U_ v144,s48,v144
U_ v145,s48,v145
Mvaa 36,32,188
L_ v6,28,s65,128
Mvaa 44,28,152
S_ s65,s59,195840
U_ v146,s48,v146
U_ v147,s48,v147
Mvaa 44,32,192
S_ m0,s52,18304
U_ v148,s48,v148
U_ v149,s48,v149
Mvaa 52,28,156
U_ v150,s48,v150
U_ v151,s48,v151
Mvaa 52,32,196
C_ 203,150,151
C_ 202,148,149
Mvaa 48,224,196
L_ v6,28,s65,128
Mvaa 48,36,156
S_ s65,s59,294144
C_ 201,146,147
C_ 200,144,145
Mvaa 40,224,192
S_ m0,s52,19328
X_ a124,v200
X_ a125,v201
Mvaa 40,36,152
X_ a126,v202
X_ a127,v203
Mvaa 32,224,188
L_ v6,28,s65,128
Mvaa 32,36,148
U_ v136,s48,v136
U_ v137,s48,v137
Mvaa 24,224,184
U_ v138,s48,v138
U_ v139,s48,v139
Mvaa 24,36,144
U_ v140,s48,v140
U_ v141,s48,v141
Mvaa 16,224,180
U_ v142,s48,v142
U_ v143,s48,v143
Mvaa 16,36,140
C_ 199,142,143
C_ 198,140,141
C_ 197,138,139
C_ 196,136,137
X_ a120,v196
X_ a121,v197
X_ a122,v198
X_ a123,v199
U_ v128,s48,v128
U_ v129,s48,v129
U_ v130,s48,v130
U_ v131,s48,v131
U_ v132,s48,v132
U_ v133,s48,v133
U_ v134,s48,v134
U_ v135,s48,v135
C_ 195,134,135
C_ 194,132,133
C_ 193,130,131
C_ 192,128,129
X_ a96,v192
X_ a97,v193
X_ a98,v194
X_ a99,v195
U_ v120,s48,v120
U_ v121,s48,v121
U_ v122,s48,v122
U_ v123,s48,v123
U_ v124,s48,v124
U_ v125,s48,v125
U_ v126,s48,v126
U_ v127,s48,v127
C_ 191,126,127
C_ 190,124,125
C_ 189,122,123
C_ 188,120,121
X_ a92,v188
X_ a93,v189
X_ a94,v190
X_ a95,v191
U_ v112,s48,v112
U_ v113,s48,v113
U_ v114,s48,v114
U_ v115,s48,v115
U_ v116,s48,v116
U_ v117,s48,v117
U_ v118,s48,v118
U_ v119,s48,v119
C_ 187,118,119
C_ 186,116,117
C_ 185,114,115
C_ 184,112,113
X_ a88,v184
X_ a89,v185
X_ a90,v186
X_ a91,v187
U_ v104,s48,v104
U_ v105,s48,v105
U_ v106,s48,v106
U_ v107,s48,v107
U_ v108,s48,v108
U_ v109,s48,v109
U_ v110,s48,v110
U_ v111,s48,v111
C_ 183,110,111
C_ 182,108,109
C_ 181,106,107
C_ 180,104,105
X_ a84,v180
X_ a85,v181
X_ a86,v182
X_ a87,v183
U_ v96,s48,v96
U_ v97,s48,v97
U_ v98,s48,v98
U_ v99,s48,v99
U_ v100,s48,v100
U_ v101,s48,v101
U_ v102,s48,v102
U_ v103,s48,v103
C_ 179,102,103
C_ 178,100,101
C_ 177,98,99
C_ 176,96,97
X_ a80,v176
X_ a81,v177
X_ a82,v178
X_ a83,v179
v_pk_mul_f32 v[88:89], v[88:89], s[48:49]
v_pk_mul_f32 v[90:91], v[90:91], s[48:49]
v_pk_mul_f32 v[92:93], v[92:93], s[48:49]
v_pk_mul_f32 v[94:95], v[94:95], s[48:49]
C_ 175,94,95
C_ 174,92,93
C_ 173,90,91
C_ 172,88,89
X_ a56,v172
X_ a57,v173
X_ a58,v174
X_ a59,v175
v_pk_mul_f32 v[80:81], v[80:81], s[48:49]
v_pk_mul_f32 v[82:83], v[82:83], s[48:49]
v_pk_mul_f32 v[84:85], v[84:85], s[48:49]
v_pk_mul_f32 v[86:87], v[86:87], s[48:49]
C_ 171,86,87
C_ 170,84,85
C_ 169,82,83
C_ 168,80,81
X_ a52,v168
X_ a53,v169
X_ a54,v170
X_ a55,v171
v_pk_mul_f32 v[72:73], v[72:73], s[48:49]
v_pk_mul_f32 v[74:75], v[74:75], s[48:49]
v_pk_mul_f32 v[76:77], v[76:77], s[48:49]
v_pk_mul_f32 v[78:79], v[78:79], s[48:49]
C_ 167,78,79
C_ 166,76,77
C_ 165,74,75
C_ 164,72,73
X_ a48,v164
X_ a49,v165
X_ a50,v166
X_ a51,v167
v_pk_mul_f32 v[64:65], v[64:65], s[48:49]
v_pk_mul_f32 v[66:67], v[66:67], s[48:49]
v_pk_mul_f32 v[68:69], v[68:69], s[48:49]
v_pk_mul_f32 v[70:71], v[70:71], s[48:49]
C_ 163,70,71
C_ 162,68,69
C_ 161,66,67
C_ 160,64,65
X_ a44,v160
X_ a45,v161
X_ a46,v162
X_ a47,v163
v_pk_mul_f32 v[56:57], v[56:57], s[48:49]
v_pk_mul_f32 v[58:59], v[58:59], s[48:49]
v_pk_mul_f32 v[60:61], v[60:61], s[48:49]
v_pk_mul_f32 v[62:63], v[62:63], s[48:49]
C_ 159,62,63
C_ 158,60,61
C_ 157,58,59
C_ 156,56,57
X_ a40,v156
X_ a41,v157
X_ a42,v158
X_ a43,v159
v_pk_mul_f32 v[48:49], v[48:49], s[48:49]
v_pk_mul_f32 v[50:51], v[50:51], s[48:49]
v_pk_mul_f32 v[52:53], v[52:53], s[48:49]
v_pk_mul_f32 v[54:55], v[54:55], s[48:49]
C_ 155,54,55
C_ 154,52,53
C_ 153,50,51
C_ 152,48,49
X_ a16,v152
X_ a17,v153
X_ a18,v154
X_ a19,v155
v_pk_mul_f32 v[40:41], v[40:41], s[48:49]
v_pk_mul_f32 v[42:43], v[42:43], s[48:49]
v_pk_mul_f32 v[44:45], v[44:45], s[48:49]
v_pk_mul_f32 v[46:47], v[46:47], s[48:49]
C_ 151,46,47
C_ 150,44,45
C_ 149,42,43
C_ 148,40,41
X_ a12,v148
X_ a13,v149
X_ a14,v150
X_ a15,v151
s_sub_u32 s59, s59, 768
s_waitcnt vmcnt(0) lgkmcnt(0)
s_barrier
Ta 20,%0,8192
Ta 22,%0,8704
Ta 24,%0,12288
Ta 26,%0,12800
Da 60,v2,0
Da 100,v3,0
Da 64,v2,2048
Da 104,v3,2048
Da 68,v2,4096
Da 108,v3,4096
Da 72,v2,6144
Da 112,v3,6144
Da 76,v2,8192
Da 116,v3,8192
Ta 28,%0,8200
Ta 30,%0,8712
Ta 32,%0,12296
Ta 34,%0,12808
v_pk_mul_f32 v[32:33], v[32:33], s[48:49]
v_pk_mul_f32 v[34:35], v[34:35], s[48:49]
v_pk_mul_f32 v[36:37], v[36:37], s[48:49]
v_pk_mul_f32 v[38:39], v[38:39], s[48:49]
C_ 147,38,39
C_ 146,36,37
C_ 145,34,35
C_ 144,32,33
X_ a8,v144
X_ a9,v145
X_ a10,v146
X_ a11,v147
v_pk_mul_f32 v[24:25], v[24:25], s[48:49]
v_pk_mul_f32 v[26:27], v[26:27], s[48:49]
v_pk_mul_f32 v[28:29], v[28:29], s[48:49]
v_pk_mul_f32 v[30:31], v[30:31], s[48:49]
C_ 143,30,31
C_ 142,28,29
C_ 141,26,27
C_ 140,24,25
X_ a4,v140
X_ a5,v141
X_ a6,v142
X_ a7,v143
v_pk_mul_f32 v[16:17], v[16:17], s[48:49]
v_pk_mul_f32 v[18:19], v[18:19], s[48:49]
v_pk_mul_f32 v[20:21], v[20:21], s[48:49]
v_pk_mul_f32 v[22:23], v[22:23], s[48:49]
C_ 139,22,23
C_ 138,20,21
C_ 137,18,19
C_ 136,16,17
X_ a0,v136
X_ a1,v137
X_ a2,v138
X_ a3,v139
s_sub_u32 s90, s90, s88
s_cmp_gt_u32 s90, 4000
s_cselect_b32 s11, 1, 0
s_mov_b32 s12, 1471
s_mov_b32 s13, 113
s_nop 1
.p2align 3
.Lq:
W_ 13
Zvaa 16,20,60
s_nop 0
Ta 36,v1,8192
Ta 38,v1,8704
W_ 14
Mvaa 16,24,100
R_ v12,a248
W_ 13
Zvaa 24,20,64
v_lshrrev_b32 v12, 4, v12
W_ 12
Mvaa 24,24,104
s_nop 0
Ta 224,v1,12288
Ta 226,v1,12800
W_ 13
Zvaa 32,20,68
v_min_u32 v10, 11, v12
W_ 12
Mvaa 32,24,108
s_mul_i32 s56, s4, 12
W_ 11
Zvaa 40,20,72
v_add_u32 v10, s56, v10
W_ 10
Mvaa 40,24,112
v_mul_u32_u24 v11, 43691, v10
W_ 9
Zvaa 48,20,76
v_lshrrev_b32 v11, 18, v11
W_ 8
Mvaa 48,24,116
W_ 4
v_lshlrev_b32 v10, 7, v10
Zvaa 52,32,116
s_nop 0
Ta 20,v1,8200
Ta 22,v1,8712
Mvaa 52,28,76
v_mul_u32_u24 v11, 11520, v11
Zvaa 44,32,112
v_add_u32 v10, v10, v11
Mvaa 44,28,72
s_nop 0
Ta 24,v1,12296
Ta 26,v1,12808
Zvaa 36,32,108
v_min_u32 v12, 9, v12
Mvaa 36,28,68
s_and_b32 s56, s6, 3
Zvaa 28,32,104
s_lshl_b32 s56, s56, 2
Mvaa 28,28,64
S_ s56,s56,s4
Zvaa 20,32,100
s_waitcnt vmcnt(18) lgkmcnt(0)
s_barrier
s_mul_i32 s56, s56, 10
Mvaa 20,28,60
S_ s65,s59,786432
v_add_u32 v12, s56, v12
Zvaa 56,36,60
S_ m0,s52,8064
Ta 28,%0,16384
Ta 30,%0,16896
Mvaa 56,224,100
L_ v6,28,s65,128
Zvaa 64,36,64
S_ s65,s59,884736
v_mul_u32_u24 v12, 1280, v12
Mvaa 64,224,104
S_ m0,s52,9088
Ta 32,%0,20480
Ta 34,%0,20992
Zvaa 72,36,68
s_lshr_b32 s56, s7, 5
Mvaa 72,224,108
L_ v6,28,s65,128
Zvaa 80,36,72
S_ s65,s59,983040
s_mul_i32 s56, s56, 98304
Mvaa 80,224,112
S_ m0,s52,10112
s_and_b32 s55, s59, 4095
Zvaa 88,36,76
S_ s56,s56,s55
Mvaa 88,224,116
s_mul_i32 s55, s77, 384
Zvaa 92,24,116
s_sub_u32 s56, s56, s55
Ta 36,%0,16392
Ta 38,%0,16904
Mvaa 92,20,76
L_ v6,28,s65,128
Zvaa 84,24,112
S_ s65,s59,1081344
s_mov_b32 s63, 0
Mvaa 84,20,72
S_ m0,s52,11136
Ta 224,%0,20488
Ta 226,%0,21000
Zvaa 76,24,108
s_bitcmp1_b32 1, s58
s_cselect_b32 s63, 0, 4026531840
Mvaa 76,20,68
L_ v6,28,s65,128
Zvaa 68,24,104
S_ s65,s60,256
S_ s56,s56,s63
Mvaa 68,20,64
S_ m0,s50,0
S_ s55,s56,3145728
Zvaa 60,24,100
Mvaa 60,20,60
W_ 4
Zvaa 96,28,60
s_nop 0
Ta 20,v1,16384
Ta 22,v1,16896
Mvaa 96,32,100
L_ v4,24,s65,0
Zvaa 104,28,64
S_ s65,s60,10496
Mvaa 104,32,104
S_ m0,s50,1024
Ta 24,v1,20480
Ta 26,v1,20992
Zvaa 112,28,68
Mvaa 112,32,108
L_ v5,24,s65,0
Zvaa 120,28,72
S_ s65,s60,20736
Mvaa 120,32,112
S_ m0,s50,2048
Zvaa 128,28,76
Mvaa 128,32,116
W_ 4
Zvaa 132,224,116
s_nop 0
Ta 28,v1,16392
Ta 30,v1,16904
Mvaa 132,36,76
L_ v4,24,s65,0
Zvaa 124,224,112
S_ s65,s60,30976
Mvaa 124,36,72
S_ m0,s50,3072
Ta 32,v1,20488
Ta 34,v1,21000
Zvaa 116,224,108
Mvaa 116,36,68
L_ v5,24,s65,0
Zvaa 108,224,104
S_ s65,s60,41216
Mvaa 108,36,64
S_ m0,s50,4096
Zvaa 100,224,100
s_waitcnt vmcnt(17) lgkmcnt(0)
s_barrier
s_cmp_eq_u32 s11, 1
s_cbranch_scc1 .Lpace1
.Lpace1_back:
Mvaa 100,36,60
Da 140,v2,20480
Zvaa 136,20,60
s_nop 0
Ta 36,%0,24576
Ta 38,%0,25088
Mvaa 136,24,100
L_ v4,24,s65,0
Zvaa 144,20,64
S_ s65,s59,786432
Da 180,v3,20480
Mvaa 144,24,104
S_ m0,s52,16128
Ta 224,%0,28672
Ta 226,%0,29184
Zvaa 152,20,68
Mvaa 152,24,108
Da 144,v2,22528
Zvaa 160,20,72
L_ v6,28,s65,256
Mvaa 160,24,112
S_ s65,s59,884736
Zvaa 168,20,76
S_ m0,s52,17152
Da 184,v3,22528
Mvaa 168,24,116
Zvaa 172,32,116
s_nop 0
Ta 20,%0,24584
Ta 22,%0,25096
Mvaa 172,28,76
Da 148,v2,24576
Zvaa 164,32,112
L_ v6,28,s65,256
Mvaa 164,28,72
S_ s65,s59,983040
Ta 24,%0,28680
Ta 26,%0,29192
Zvaa 156,32,108
S_ m0,s52,18176
Da 188,v3,24576
Mvaa 156,28,68
Zvaa 148,32,104
L_ v6,28,s65,256
Mvaa 148,28,64
S_ s65,s59,1081344
Da 152,v2,26624
Zvaa 140,32,100
S_ m0,s52,19200
Mvaa 140,28,60
W_ 9
Zvaa 176,36,60
s_nop 0
Ta 28,v1,24576
Ta 30,v1,25088
Mvaa 176,224,100
Da 192,v3,26624
Zvaa 184,36,64
L_ v6,28,s65,256
Mvaa 184,224,104
s_nop 0
Ta 32,v1,28672
Ta 34,v1,29184
Zvaa 192,36,68
Da 156,v2,28672
Mvaa 192,224,108
Zvaa 200,36,72
Da 196,v3,28672
Mvaa 200,224,112
buffer_load_dword a254, v10, s[28:31], s55 offen
Zvaa 208,36,76
Mvaa 208,224,116
W_ 9
Zvaa 212,24,116
s_nop 0
Ta 36,v1,24584
Ta 38,v1,25096
Mvaa 212,20,76
buffer_load_dword a254, v12, s[24:27], s63 offen offset:512
Zvaa 204,24,112
Mvaa 204,20,72
s_nop 0
Ta 224,v1,28680
Ta 226,v1,29192
Zvaa 196,24,108
Mvaa 196,20,68
Zvaa 188,24,104
Mvaa 188,20,64
Zvaa 180,24,100
s_waitcnt vmcnt(20) lgkmcnt(0)
s_barrier
Mvaa 180,20,60
S_ s65,s59,1572864
Zvaa 216,28,60
S_ m0,s52,24576
Ta 20,%0,0
Ta 22,%0,512
Mvaa 216,32,100
L_ v6,28,s65,0
Zvaa 224,28,64
S_ s65,s59,1671168
Mvaa 224,32,104
S_ m0,s52,25600
Ta 24,%0,4096
Ta 26,%0,4608
Zvaa 232,28,68
Mvaa 232,32,108
L_ v6,28,s65,0
Zvaa 240,28,72
S_ s65,s59,1769472
Mvaa 240,32,112
S_ m0,s52,26624
Zvaa 248,28,76
Mvaa 248,32,116
Zvaa 252,224,116
s_nop 0
Ta 28,%0,8
Ta 30,%0,520
Mvaa 252,36,76
L_ v6,28,s65,0
Zvaa 244,224,112
S_ s65,s59,1867776
Mvaa 244,36,72
S_ m0,s52,27648
Ta 32,%0,4104
Ta 34,%0,4616
Zvaa 236,224,108
Mvaa 236,36,68
L_ v6,28,s65,0
Zvaa 228,224,104
S_ s65,s60,384
Mvaa 228,36,64
S_ m0,s50,20480
Zvaa 220,224,100
Mvaa 220,36,60
W_ 4
Mvaa 16,20,140
s_nop 0
Ta 36,v1,0
Ta 38,v1,512
Mvaa 16,24,180
L_ v4,24,s65,0
Mvaa 24,20,144
S_ s65,s60,10624
Mvaa 24,24,184
S_ m0,s50,21504
Ta 224,v1,4096
Ta 226,v1,4608
Mvaa 32,20,148
Mvaa 32,24,188
L_ v5,24,s65,0
Mvaa 40,20,152
S_ s65,s60,20864
Mvaa 40,24,192
S_ m0,s50,22528
Mvaa 48,20,156
Mvaa 48,24,196
W_ 4
Mvaa 52,32,196
s_nop 0
Ta 20,v1,8
Ta 22,v1,520
Mvaa 52,28,156
L_ v4,24,s65,0
Mvaa 44,32,192
S_ s65,s60,31104
Mvaa 44,28,152
S_ m0,s50,23552
Ta 24,v1,4104
Ta 26,v1,4616
Mvaa 36,32,188
Mvaa 36,28,148
L_ v5,24,s65,0
Mvaa 28,32,184
S_ s65,s60,41344
Mvaa 28,28,144
S_ m0,s50,24576
Mvaa 20,32,180
s_waitcnt vmcnt(19) lgkmcnt(0)
s_barrier
s_cmp_eq_u32 s11, 1
s_cbranch_scc1 .Lpace3
.Lpace3_back:
Mvaa 20,28,140
L_ v4,24,s65,0
Mvaa 56,36,140
S_ s65,s59,1572864
Ta 28,%0,8192
Ta 30,%0,8704
Mvaa 56,224,180
S_ m0,s52,-128
Mvaa 64,36,144
Mvaa 64,224,184
s_nop 0
Ta 32,%0,12288
Ta 34,%0,12800
Mvaa 72,36,148
L_ v6,28,s65,128
Mvaa 72,224,188
S_ s65,s59,1671168
Mvaa 80,36,152
S_ m0,s52,896
Mvaa 80,224,192
Mvaa 88,36,156
L_ v6,28,s65,128
Mvaa 88,224,196
S_ s65,s59,1769472
Mvaa 92,24,196
S_ m0,s52,1920
Ta 36,%0,8200
Ta 38,%0,8712
Mvaa 92,20,156
S_ s55,s56,3932160
Mvaa 84,24,192
L_ v6,28,s65,128
Mvaa 84,20,152
S_ s65,s59,1867776
Ta 224,%0,12296
Ta 226,%0,12808
Mvaa 76,24,188
S_ m0,s52,2944
Mvaa 76,20,148
Mvaa 68,24,184
L_ v6,28,s65,128
Mvaa 68,20,144
Mvaa 60,24,180
Mvaa 60,20,140
W_ 4
Mvaa 96,28,140
s_nop 0
Ta 20,v1,8192
Ta 22,v1,8704
Mvaa 96,32,180
buffer_load_dword a254, v10, s[28:31], s55 offen
Mvaa 104,28,144
Mvaa 104,32,184
s_nop 0
Ta 24,v1,12288
Ta 26,v1,12800
Mvaa 112,28,148
Mvaa 112,32,188
buffer_load_dword a254, v12, s[24:27], s63 offen offset:640
Mvaa 120,28,152
Mvaa 120,32,192
Mvaa 128,28,156
Mvaa 128,32,196
W_ 4
Mvaa 132,224,196
s_nop 0
Ta 28,v1,8200
Ta 30,v1,8712
Mvaa 132,36,156
Mvaa 124,224,192
Mvaa 124,36,152
s_nop 0
Ta 32,v1,12296
Ta 34,v1,12808
Mvaa 116,224,188
Mvaa 116,36,148
Mvaa 108,224,184
Mvaa 108,36,144
Mvaa 100,224,180
s_waitcnt vmcnt(17) lgkmcnt(0)
s_barrier
Mvaa 100,36,140
S_ s65,s59,1572864
Da 60,v2,0
Mvaa 136,20,140
S_ m0,s52,7936
Ta 36,%0,16384
Ta 38,%0,16896
Mvaa 136,24,180
L_ v6,28,s65,256
Mvaa 144,20,144
S_ s65,s59,1671168
Da 100,v3,0
Mvaa 144,24,184
S_ m0,s52,8960
Ta 224,%0,20480
Ta 226,%0,20992
Mvaa 152,20,148
Mvaa 152,24,188
Da 64,v2,2048
Mvaa 160,20,152
L_ v6,28,s65,256
Mvaa 160,24,192
S_ s65,s59,1769472
Mvaa 168,20,156
S_ m0,s52,9984
Da 104,v3,2048
Mvaa 168,24,196
Mvaa 172,32,196
s_nop 0
Ta 20,%0,16392
Ta 22,%0,16904
Mvaa 172,28,156
Da 68,v2,4096
Mvaa 164,32,192
L_ v6,28,s65,256
Mvaa 164,28,152
S_ s65,s59,1867776
Ta 24,%0,20488
Ta 26,%0,21000
Mvaa 156,32,188
S_ m0,s52,11008
Da 108,v3,4096
Mvaa 156,28,148
Mvaa 148,32,184
L_ v6,28,s65,256
Mvaa 148,28,144
Da 72,v2,6144
Mvaa 140,32,180
Mvaa 140,28,140
W_ 9
Mvaa 176,36,140
s_nop 0
Ta 28,v1,16384
Ta 30,v1,16896
Mvaa 176,224,180
Da 112,v3,6144
)A"
R"A(Mvaa 184,36,144
Mvaa 184,224,184
s_nop 0
Ta 32,v1,20480
Ta 34,v1,20992
Mvaa 192,36,148
Da 76,v2,8192
Mvaa 192,224,188
Mvaa 200,36,152
Da 116,v3,8192
Mvaa 200,224,192
Mvaa 208,36,156
Mvaa 208,224,196
W_ 9
Mvaa 212,24,196
s_nop 0
Ta 36,v1,16392
Ta 38,v1,16904
Mvaa 212,20,156
Mvaa 204,24,192
Mvaa 204,20,152
s_nop 0
Ta 224,v1,20488
Ta 226,v1,21000
Mvaa 196,24,188
Mvaa 196,20,148
Mvaa 188,24,184
Mvaa 188,20,144
Mvaa 180,24,180
s_waitcnt vmcnt(15) lgkmcnt(0)
s_barrier
s_cmp_eq_u32 s11, 1
s_cbranch_scc1 .Lpace5
.Lpace5_back:
Mvaa 180,20,140
S_ s65,s59,2359296
Mvaa 216,28,140
S_ m0,s52,16384
Ta 20,%0,24576
Ta 22,%0,25088
Mvaa 216,32,180
L_ v6,28,s65,0
Mvaa 224,28,144
S_ s65,s59,2457600
Mvaa 224,32,184
S_ m0,s52,17408
Ta 24,%0,28672
Ta 26,%0,29184
Mvaa 232,28,148
Mvaa 232,32,188
L_ v6,28,s65,0
Mvaa 240,28,152
S_ s65,s59,2555904
Mvaa 240,32,192
S_ m0,s52,18432
Mvaa 248,28,156
Mvaa 248,32,196
Mvaa 252,224,196
s_nop 0
Ta 28,%0,24584
Ta 30,%0,25096
Mvaa 252,36,156
L_ v6,28,s65,0
Mvaa 244,224,192
S_ s65,s59,2654208
Mvaa 244,36,152
S_ m0,s52,19456
Ta 32,%0,28680
Ta 34,%0,29192
Mvaa 236,224,188
Mvaa 236,36,148
L_ v6,28,s65,0
Mvaa 228,224,184
S_ s65,s60,512
Mvaa 228,36,144
S_ m0,s50,0
Mvaa 220,224,180
Mvaa 220,36,140
W_ 4
Mvaa 16,20,60
s_nop 0
Ta 36,v1,24576
Ta 38,v1,25088
Mvaa 16,24,100
L_ v4,24,s65,0
Mvaa 24,20,64
S_ s65,s60,10752
Mvaa 24,24,104
S_ m0,s50,1024
Ta 224,v1,28672
Ta 226,v1,29184
Mvaa 32,20,68
Mvaa 32,24,108
L_ v5,24,s65,0
Mvaa 40,20,72
S_ s65,s60,20992
Mvaa 40,24,112
S_ m0,s50,2048
Mvaa 48,20,76
Mvaa 48,24,116
W_ 4
Mvaa 52,32,116
s_nop 0
Ta 20,v1,24584
Ta 22,v1,25096
Mvaa 52,28,76
L_ v4,24,s65,0
Mvaa 44,32,112
S_ s65,s60,31232
Mvaa 44,28,72
S_ m0,s50,3072
Ta 24,v1,28680
Ta 26,v1,29192
Mvaa 36,32,108
Mvaa 36,28,68
L_ v5,24,s65,0
Mvaa 28,32,104
S_ s65,s60,41472
Mvaa 28,28,64
S_ m0,s50,4096
Mvaa 20,32,100
s_waitcnt vmcnt(14) lgkmcnt(0)
s_barrier
Mvaa 20,28,60
L_ v4,24,s65,0
Mvaa 56,36,60
S_ s65,s59,2359296
Ta 28,%0,0
Ta 30,%0,512
Mvaa 56,224,100
S_ m0,s52,24448
Mvaa 64,36,64
Mvaa 64,224,104
s_nop 0
Ta 32,%0,4096
Ta 34,%0,4608
Mvaa 72,36,68
L_ v6,28,s65,128
Mvaa 72,224,108
S_ s65,s59,2457600
Mvaa 80,36,72
S_ m0,s52,25472
Mvaa 80,224,112
Mvaa 88,36,76
L_ v6,28,s65,128
Mvaa 88,224,116
S_ s65,s59,2555904
Mvaa 92,24,116
S_ m0,s52,26496
Ta 36,%0,8
Ta 38,%0,520
Mvaa 92,20,76
S_ s55,s56,4718592
Mvaa 84,24,112
L_ v6,28,s65,128
Mvaa 84,20,72
S_ s65,s59,2654208
Ta 224,%0,4104
Ta 226,%0,4616
Mvaa 76,24,108
S_ m0,s52,27520
Mvaa 76,20,68
Mvaa 68,24,104
L_ v6,28,s65,128
Mvaa 68,20,64
Mvaa 60,24,100
Mvaa 60,20,60
W_ 4
Mvaa 96,28,60
s_nop 0
Ta 20,v1,0
Ta 22,v1,512
Mvaa 96,32,100
buffer_load_dword a254, v10, s[28:31], s55 offen
Mvaa 104,28,64
Mvaa 104,32,104
s_nop 0
Ta 24,v1,4096
Ta 26,v1,4608
Mvaa 112,28,68
Mvaa 112,32,108
buffer_load_dword a254, v12, s[24:27], s63 offen offset:768
Mvaa 120,28,72
Mvaa 120,32,112
Mvaa 128,28,76
Mvaa 128,32,116
W_ 4
Mvaa 132,224,116
s_nop 0
Ta 28,v1,8
Ta 30,v1,520
Mvaa 132,36,76
Mvaa 124,224,112
Mvaa 124,36,72
s_nop 0
Ta 32,v1,4104
Ta 34,v1,4616
Mvaa 116,224,108
Mvaa 116,36,68
Mvaa 108,224,104
Mvaa 108,36,64
Mvaa 100,224,100
s_waitcnt vmcnt(15) lgkmcnt(0)
s_barrier
s_cmp_eq_u32 s11, 1
s_cbranch_scc1 .Lpace7
.Lpace7_back:
Mvaa 100,36,60
S_ s65,s59,2359296
Da 140,v2,20480
Mvaa 136,20,60
S_ m0,s52,-256
Ta 36,%0,8192
Ta 38,%0,8704
Mvaa 136,24,100
L_ v6,28,s65,256
Mvaa 144,20,64
S_ s65,s59,2457600
Da 180,v3,20480
Mvaa 144,24,104
S_ m0,s52,768
Ta 224,%0,12288
Ta 226,%0,12800
Mvaa 152,20,68
Mvaa 152,24,108
Da 144,v2,22528
Mvaa 160,20,72
L_ v6,28,s65,256
Mvaa 160,24,112
S_ s65,s59,2555904
Mvaa 168,20,76
S_ m0,s52,1792
Da 184,v3,22528
Mvaa 168,24,116
Mvaa 172,32,116
s_nop 0
Ta 20,%0,8200
Ta 22,%0,8712
Mvaa 172,28,76
Da 148,v2,24576
Mvaa 164,32,112
L_ v6,28,s65,256
Mvaa 164,28,72
S_ s65,s59,2654208
Ta 24,%0,12296
Ta 26,%0,12808
Mvaa 156,32,108
S_ m0,s52,2816
Da 188,v3,24576
Mvaa 156,28,68
Mvaa 148,32,104
L_ v6,28,s65,256
Mvaa 148,28,64
Da 152,v2,26624
Mvaa 140,32,100
Mvaa 140,28,60
W_ 9
Mvaa 176,36,60
s_nop 0
Ta 28,v1,8192
Ta 30,v1,8704
Mvaa 176,224,100
Da 192,v3,26624
Mvaa 184,36,64
Mvaa 184,224,104
s_nop 0
Ta 32,v1,12288
Ta 34,v1,12800
Mvaa 192,36,68
Da 156,v2,28672
Mvaa 192,224,108
Mvaa 200,36,72
Da 196,v3,28672
Mvaa 200,224,112
Mvaa 208,36,76
Mvaa 208,224,116
W_ 9
Mvaa 212,24,116
s_nop 0
Ta 36,v1,8200
Ta 38,v1,8712
Mvaa 212,20,76
Mvaa 204,24,112
Mvaa 204,20,72
s_nop 0
Ta 224,v1,12296
Ta 226,v1,12808
Mvaa 196,24,108
Mvaa 196,20,68
Mvaa 188,24,104
Mvaa 188,20,64
Mvaa 180,24,100
s_waitcnt vmcnt(15) lgkmcnt(0)
s_barrier
Mvaa 180,20,60
S_ s65,s59,3145728
Mvaa 216,28,60
S_ m0,s52,8192
Ta 20,%0,16384
Ta 22,%0,16896
Mvaa 216,32,100
L_ v6,28,s65,0
Mvaa 224,28,64
S_ s65,s59,3244032
Mvaa 224,32,104
S_ m0,s52,9216
Ta 24,%0,20480
Ta 26,%0,20992
Mvaa 232,28,68
Mvaa 232,32,108
L_ v6,28,s65,0
Mvaa 240,28,72
S_ s65,s59,3342336
Mvaa 240,32,112
S_ m0,s52,10240
Mvaa 248,28,76
Mvaa 248,32,116
Mvaa 252,224,116
s_nop 0
Ta 28,%0,16392
Ta 30,%0,16904
Mvaa 252,36,76
L_ v6,28,s65,0
Mvaa 244,224,112
S_ s65,s59,3440640
Mvaa 244,36,72
S_ m0,s52,11264
Ta 32,%0,20488
Ta 34,%0,21000
Mvaa 236,224,108
Mvaa 236,36,68
L_ v6,28,s65,0
Mvaa 228,224,104
S_ s65,s60,640
Mvaa 228,36,64
S_ m0,s50,20480
Mvaa 220,224,100
Mvaa 220,36,60
W_ 4
Mvaa 16,20,140
s_nop 0
Ta 36,v1,16384
Ta 38,v1,16896
Mvaa 16,24,180
L_ v4,24,s65,0
Mvaa 24,20,144
S_ s65,s60,10880
Mvaa 24,24,184
S_ m0,s50,21504
Ta 224,v1,20480
Ta 226,v1,20992
Mvaa 32,20,148
Mvaa 32,24,188
L_ v5,24,s65,0
Mvaa 40,20,152
S_ s65,s60,21120
Mvaa 40,24,192
S_ m0,s50,22528
Mvaa 48,20,156
Mvaa 48,24,196
W_ 4
Mvaa 52,32,196
s_nop 0
Ta 20,v1,16392
Ta 22,v1,16904
Mvaa 52,28,156
L_ v4,24,s65,0
Mvaa 44,32,192
S_ s65,s60,31360
Mvaa 44,28,152
S_ m0,s50,23552
Ta 24,v1,20488
Ta 26,v1,21000
Mvaa 36,32,188
Mvaa 36,28,148
L_ v5,24,s65,0
Mvaa 28,32,184
S_ s65,s60,41600
Mvaa 28,28,144
S_ m0,s50,24576
Mvaa 20,32,180
s_waitcnt vmcnt(14) lgkmcnt(0)
s_barrier
s_cmp_eq_u32 s11, 1
s_cbranch_scc1 .Lpace9
.Lpace9_back:
Mvaa 20,28,140
L_ v4,24,s65,0
Mvaa 56,36,140
S_ s65,s59,3145728
Ta 28,%0,24576
Ta 30,%0,25088
Mvaa 56,224,180
S_ m0,s52,16256
Mvaa 64,36,144
Mvaa 64,224,184
s_nop 0
Ta 32,%0,28672
Ta 34,%0,29184
Mvaa 72,36,148
L_ v6,28,s65,128
Mvaa 72,224,188
S_ s65,s59,3244032
Mvaa 80,36,152
S_ m0,s52,17280
Mvaa 80,224,192
Mvaa 88,36,156
L_ v6,28,s65,128
Mvaa 88,224,196
S_ s65,s59,3342336
Mvaa 92,24,196
S_ m0,s52,18304
Ta 36,%0,24584
Ta 38,%0,25096
Mvaa 92,20,156
S_ s55,s56,5505024
Mvaa 84,24,192
L_ v6,28,s65,128
Mvaa 84,20,152
S_ s65,s59,3440640
Ta 224,%0,28680
Ta 226,%0,29192
Mvaa 76,24,188
S_ m0,s52,19328
Mvaa 76,20,148
Mvaa 68,24,184
L_ v6,28,s65,128
Mvaa 68,20,144
Mvaa 60,24,180
Mvaa 60,20,140
W_ 4
Mvaa 96,28,140
s_nop 0
Ta 20,v1,24576
Ta 22,v1,25088
Mvaa 96,32,180
buffer_load_dword a254, v10, s[28:31], s55 offen
Mvaa 104,28,144
Mvaa 104,32,184
s_nop 0
Ta 24,v1,28672
Ta 26,v1,29184
Mvaa 112,28,148
Mvaa 112,32,188
buffer_load_dword a254, v12, s[24:27], s63 offen offset:896
Mvaa 120,28,152
Mvaa 120,32,192
Mvaa 128,28,156
Mvaa 128,32,196
W_ 4
Mvaa 132,224,196
s_nop 0
Ta 28,v1,24584
Ta 30,v1,25096
Mvaa 132,36,156
Mvaa 124,224,192
Mvaa 124,36,152
s_nop 0
Ta 32,v1,28680
Ta 34,v1,29192
Mvaa 116,224,188
Mvaa 116,36,148
Mvaa 108,224,184
Mvaa 108,36,144
Mvaa 100,224,180
s_waitcnt vmcnt(15) lgkmcnt(0)
s_barrier
Mvaa 100,36,140
S_ s65,s59,3145728
Da 60,v2,0
Mvaa 136,20,140
S_ m0,s52,24320
Ta 36,%0,0
Ta 38,%0,512
Mvaa 136,24,180
L_ v6,28,s65,256
Mvaa 144,20,144
S_ s65,s59,3244032
Da 100,v3,0
Mvaa 144,24,184
S_ m0,s52,25344
Ta 224,%0,4096
Ta 226,%0,4608
Mvaa 152,20,148
Mvaa 152,24,188
Da 64,v2,2048
Mvaa 160,20,152
L_ v6,28,s65,256
Mvaa 160,24,192
S_ s65,s59,3342336
Mvaa 168,20,156
S_ m0,s52,26368
Da 104,v3,2048
Mvaa 168,24,196
Mvaa 172,32,196
s_nop 0
Ta 20,%0,8
Ta 22,%0,520
Mvaa 172,28,156
Da 68,v2,4096
Mvaa 164,32,192
L_ v6,28,s65,256
Mvaa 164,28,152
S_ s65,s59,3440640
Ta 24,%0,4104
Ta 26,%0,4616
Mvaa 156,32,188
S_ m0,s52,27392
Da 108,v3,4096
Mvaa 156,28,148
Mvaa 148,32,184
L_ v6,28,s65,256
Mvaa 148,28,144
Da 72,v2,6144
Mvaa 140,32,180
Mvaa 140,28,140
W_ 9
Mvaa 176,36,140
s_nop 0
Ta 28,v1,0
Ta 30,v1,512
Mvaa 176,224,180
Da 112,v3,6144
Mvaa 184,36,144
Mvaa 184,224,184
s_nop 0
Ta 32,v1,4096
Ta 34,v1,4608
Mvaa 192,36,148
Da 76,v2,8192
Mvaa 192,224,188
Mvaa 200,36,152
Da 116,v3,8192
Mvaa 200,224,192
Mvaa 208,36,156
Mvaa 208,224,196
W_ 9
Mvaa 212,24,196
s_nop 0
Ta 36,v1,8
Ta 38,v1,520
Mvaa 212,20,156
Mvaa 204,24,192
Mvaa 204,20,152
s_nop 0
Ta 224,v1,4104
Ta 226,v1,4616
Mvaa 196,24,188
Mvaa 196,20,148
Mvaa 188,24,184
Mvaa 188,20,144
Mvaa 180,24,180
s_waitcnt vmcnt(15) lgkmcnt(0)
s_barrier
s_cmp_eq_u32 s11, 1
s_cbranch_scc1 .Lpace11
.Lpace11_back:
Mvaa 180,20,140
S_ s65,s59,3932160
Mvaa 216,28,140
S_ m0,s52,0
Ta 20,%0,8192
Ta 22,%0,8704
Mvaa 216,32,180
L_ v6,28,s65,0
Mvaa 224,28,144
S_ s65,s59,4030464
Mvaa 224,32,184
S_ m0,s52,1024
Ta 24,%0,12288
Ta 26,%0,12800
Mvaa 232,28,148
Mvaa 232,32,188
L_ v6,28,s65,0
Mvaa 240,28,152
S_ s65,s59,4128768
Mvaa 240,32,192
S_ m0,s52,2048
Mvaa 248,28,156
Mvaa 248,32,196
Mvaa 252,224,196
s_nop 0
Ta 28,%0,8200
Ta 30,%0,8712
Mvaa 252,36,156
L_ v6,28,s65,0
Mvaa 244,224,192
S_ s65,s59,4227072
Mvaa 244,36,152
S_ m0,s52,3072
Ta 32,%0,12296
Ta 34,%0,12808
Mvaa 236,224,188
Mvaa 236,36,148
L_ v6,28,s65,0
Mvaa 228,224,184
S_ s65,s60,768
Mvaa 228,36,144
S_ m0,s50,0
Mvaa 220,224,180
Mvaa 220,36,140
W_ 4
Mvaa 16,20,60
s_nop 0
Ta 36,v1,8192
Ta 38,v1,8704
Mvaa 16,24,100
L_ v4,24,s65,0
Mvaa 24,20,64
S_ s65,s60,11008
R_ v13,a248
Mvaa 24,24,104
S_ m0,s50,1024
Ta 224,v1,12288
Ta 226,v1,12800
Mvaa 32,20,68
v_lshlrev_b32 v13, 10, v13
Mvaa 32,24,108
L_ v5,24,s65,0
Mvaa 40,20,72
S_ s65,s60,21248
Mvaa 40,24,112
S_ m0,s50,2048
Mvaa 48,20,76
Mvaa 48,24,116
W_ 4
Mvaa 52,32,116
s_nop 0
Ta 20,v1,8200
Ta 22,v1,8712
Mvaa 52,28,76
L_ v4,24,s65,0
Mvaa 44,32,112
S_ s65,s60,31488
Mvaa 44,28,72
S_ m0,s50,3072
Ta 24,v1,12296
Ta 26,v1,12808
Mvaa 36,32,108
Mvaa 36,28,68
L_ v5,24,s65,0
Mvaa 28,32,104
S_ s65,s60,41728
Mvaa 28,28,64
S_ m0,s50,4096
Mvaa 20,32,100
s_waitcnt vmcnt(14) lgkmcnt(0)
s_barrier
Mvaa 20,28,60
L_ v4,24,s65,0
Mvaa 56,36,60
S_ s65,s59,3932160
Ta 28,%0,16384
Ta 30,%0,16896
Mvaa 56,224,100
S_ m0,s52,8064
Mvaa 64,36,64
Mvaa 64,224,104
s_nop 0
Ta 32,%0,20480
Ta 34,%0,20992
Mvaa 72,36,68
L_ v6,28,s65,128
Mvaa 72,224,108
S_ s65,s59,4030464
Mvaa 80,36,72
S_ m0,s52,9088
Mvaa 80,224,112
Mvaa 88,36,76
L_ v6,28,s65,128
Mvaa 88,224,116
S_ s65,s59,4128768
Mvaa 92,24,116
S_ m0,s52,10112
Ta 36,%0,16392
Ta 38,%0,16904
Mvaa 92,20,76
S_ s55,s56,6291456
Mvaa 84,24,112
L_ v6,28,s65,128
Mvaa 84,20,72
S_ s65,s59,4227072
Ta 224,%0,20488
Ta 226,%0,21000
Mvaa 76,24,108
S_ m0,s52,11136
Mvaa 76,20,68
Mvaa 68,24,104
L_ v6,28,s65,128
Mvaa 68,20,64
Mvaa 60,24,100
Mvaa 60,20,60
W_ 4
Mvaa 96,28,60
s_nop 0
Ta 20,v1,16384
Ta 22,v1,16896
Mvaa 96,32,100
buffer_load_dword a220, v13, s[44:47], s66 offen
Mvaa 104,28,64
Mvaa 104,32,104
s_nop 0
Ta 24,v1,20480
Ta 26,v1,20992
Mvaa 112,28,68
Mvaa 112,32,108
buffer_load_dword a254, v10, s[28:31], s55 offen
Mvaa 120,28,72
Mvaa 120,32,112
Mvaa 128,28,76
Mvaa 128,32,116
W_ 4
Mvaa 132,224,116
s_nop 0
Ta 28,v1,16392
Ta 30,v1,16904
Mvaa 132,36,76
buffer_load_dword a254, v12, s[24:27], s63 offen offset:1024
Mvaa 124,224,112
Mvaa 124,36,72
s_nop 0
Ta 32,v1,20488
Ta 34,v1,21000
Mvaa 116,224,108
Mvaa 116,36,68
Mvaa 108,224,104
Mvaa 108,36,64
Mvaa 100,224,100
s_waitcnt vmcnt(16) lgkmcnt(0)
s_barrier
s_cmp_eq_u32 s11, 1
s_cbranch_scc1 .Lpace13
.Lpace13_back:
Mvaa 100,36,60
S_ s65,s59,3932160
Da 140,v2,20480
Mvaa 136,20,60
S_ m0,s52,16128
Ta 36,%0,24576
Ta 38,%0,25088
Mvaa 136,24,100
L_ v6,28,s65,256
Mvaa 144,20,64
S_ s65,s59,4030464
Da 180,v3,20480
Mvaa 144,24,104
S_ m0,s52,17152
Ta 224,%0,28672
Ta 226,%0,29184
Mvaa 152,20,68
Mvaa 152,24,108
Da 144,v2,22528
Mvaa 160,20,72
L_ v6,28,s65,256
Mvaa 160,24,112
S_ s65,s59,4128768
Mvaa 168,20,76
S_ m0,s52,18176
Da 184,v3,22528
Mvaa 168,24,116
Mvaa 172,32,116
s_nop 0
Ta 20,%0,24584
Ta 22,%0,25096
Mvaa 172,28,76
Da 148,v2,24576
Mvaa 164,32,112
L_ v6,28,s65,256
Mvaa 164,28,72
S_ s65,s59,4227072
Ta 24,%0,28680
Ta 26,%0,29192
Mvaa 156,32,108
S_ m0,s52,19200
Da 188,v3,24576
Mvaa 156,28,68
Mvaa 148,32,104
L_ v6,28,s65,256
Mvaa 148,28,64
Da 152,v2,26624
Mvaa 140,32,100
Mvaa 140,28,60
W_ 9
Mvaa 176,36,60
s_nop 0
Ta 28,v1,24576
Ta 30,v1,25088
Mvaa 176,224,100
Da 192,v3,26624
Mvaa 184,36,64
Mvaa 184,224,104
s_nop 0
Ta 32,v1,28672
Ta 34,v1,29184
Mvaa 192,36,68
Da 156,v2,28672
Mvaa 192,224,108
Mvaa 200,36,72
Da 196,v3,28672
Mvaa 200,224,112
Mvaa 208,36,76
Mvaa 208,224,116
W_ 9
Mvaa 212,24,116
s_nop 0
Ta 36,v1,24584
Ta 38,v1,25096
Mvaa 212,20,76
Mvaa 204,24,112
Mvaa 204,20,72
s_nop 0
Ta 224,v1,28680
Ta 226,v1,29192
Mvaa 196,24,108
Mvaa 196,20,68
Mvaa 188,24,104
Mvaa 188,20,64
Mvaa 180,24,100
s_waitcnt vmcnt(16) lgkmcnt(0)
s_barrier
Mvaa 180,20,60
S_ s65,s59,4718592
Mvaa 216,28,60
S_ m0,s52,24576
Ta 20,%0,0
Ta 22,%0,512
Mvaa 216,32,100
L_ v6,28,s65,0
Mvaa 224,28,64
S_ s65,s59,4816896
Mvaa 224,32,104
S_ m0,s52,25600
Ta 24,%0,4096
Ta 26,%0,4608
Mvaa 232,28,68
Mvaa 232,32,108
L_ v6,28,s65,0
Mvaa 240,28,72
S_ s65,s59,4915200
Mvaa 240,32,112
S_ m0,s52,26624
Mvaa 248,28,76
Mvaa 248,32,116
Mvaa 252,224,116
s_nop 0
Ta 28,%0,8
Ta 30,%0,520
Mvaa 252,36,76
L_ v6,28,s65,0
Mvaa 244,224,112
S_ s65,s59,5013504
Mvaa 244,36,72
S_ m0,s52,27648
Ta 32,%0,4104
Ta 34,%0,4616
Mvaa 236,224,108
Mvaa 236,36,68
L_ v6,28,s65,0
Mvaa 228,224,104
S_ s65,s60,896
Mvaa 228,36,64
S_ m0,s50,20480
Mvaa 220,224,100
Mvaa 220,36,60
W_ 4
Mvaa 16,20,140
s_nop 0
Ta 36,v1,0
Ta 38,v1,512
Mvaa 16,24,180
L_ v4,24,s65,0
Mvaa 24,20,144
S_ s65,s60,11136
Mvaa 24,24,184
S_ m0,s50,21504
Ta 224,v1,4096
Ta 226,v1,4608
Mvaa 32,20,148
Mvaa 32,24,188
L_ v5,24,s65,0
Mvaa 40,20,152
S_ s65,s60,21376
Mvaa 40,24,192
S_ m0,s50,22528
Mvaa 48,20,156
Mvaa 48,24,196
W_ 4
Mvaa 52,32,196
s_nop 0
Ta 20,v1,8
Ta 22,v1,520
Mvaa 52,28,156
L_ v4,24,s65,0
Mvaa 44,32,192
S_ s65,s60,31616
Mvaa 44,28,152
S_ m0,s50,23552
Ta 24,v1,4104
Ta 26,v1,4616
Mvaa 36,32,188
Mvaa 36,28,148
L_ v5,24,s65,0
Mvaa 28,32,184
S_ s65,s60,41856
Mvaa 28,28,144
S_ m0,s50,24576
Mvaa 20,32,180
s_waitcnt vmcnt(15) lgkmcnt(0)
s_barrier
s_cmp_eq_u32 s11, 1
s_cbranch_scc1 .Lpace15
.Lpace15_back:
Mvaa 20,28,140
L_ v4,24,s65,0
Mvaa 56,36,140
S_ s65,s59,4718592
Ta 28,%0,8192
Ta 30,%0,8704
Mvaa 56,224,180
S_ m0,s52,-128
Mvaa 64,36,144
Mvaa 64,224,184
s_nop 0
Ta 32,%0,12288
Ta 34,%0,12800
Mvaa 72,36,148
L_ v6,28,s65,128
Mvaa 72,224,188
S_ s65,s59,4816896
Mvaa 80,36,152
S_ m0,s52,896
Mvaa 80,224,192
Mvaa 88,36,156
L_ v6,28,s65,128
Mvaa 88,224,196
S_ s65,s59,4915200
Mvaa 92,24,196
S_ m0,s52,1920
Ta 36,%0,8200
Ta 38,%0,8712
Mvaa 92,20,156
S_ s55,s56,7077888
Mvaa 84,24,192
L_ v6,28,s65,128
Mvaa 84,20,152
S_ s65,s59,5013504
Ta 224,%0,12296
Ta 226,%0,12808
Mvaa 76,24,188
S_ m0,s52,2944
Mvaa 76,20,148
Mvaa 68,24,184
L_ v6,28,s65,128
Mvaa 68,20,144
Mvaa 60,24,180
Mvaa 60,20,140
W_ 4
Mvaa 96,28,140
s_nop 0
Ta 20,v1,8192
Ta 22,v1,8704
Mvaa 96,32,180
buffer_load_dword a254, v10, s[28:31], s55 offen
Mvaa 104,28,144
Mvaa 104,32,184
s_nop 0
Ta 24,v1,12288
Ta 26,v1,12800
Mvaa 112,28,148
Mvaa 112,32,188
buffer_load_dword a254, v12, s[24:27], s63 offen offset:1152
Mvaa 120,28,152
Mvaa 120,32,192
Mvaa 128,28,156
Mvaa 128,32,196
W_ 4
Mvaa 132,224,196
s_nop 0
Ta 28,v1,8200
Ta 30,v1,8712
Mvaa 132,36,156
Mvaa 124,224,192
Mvaa 124,36,152
s_nop 0
Ta 32,v1,12296
Ta 34,v1,12808
Mvaa 116,224,188
Mvaa 116,36,148
Mvaa 108,224,184
Mvaa 108,36,144
Mvaa 100,224,180
s_waitcnt vmcnt(15) lgkmcnt(0)
s_barrier
Mvaa 100,36,140
S_ s65,s59,4718592
Da 60,v2,0
Mvaa 136,20,140
S_ m0,s52,7936
Ta 36,%0,16384
Ta 38,%0,16896
Mvaa 136,24,180
L_ v6,28,s65,256
Mvaa 144,20,144
S_ s65,s59,4816896
Da 100,v3,0
Mvaa 144,24,184
S_ m0,s52,8960
Ta 224,%0,20480
Ta 226,%0,20992
Mvaa 152,20,148
Mvaa 152,24,188
Da 64,v2,2048
Mvaa 160,20,152
L_ v6,28,s65,256
Mvaa 160,24,192
S_ s65,s59,4915200
Mvaa 168,20,156
S_ m0,s52,9984
Da 104,v3,2048
Mvaa 168,24,196
Mvaa 172,32,196
s_nop 0
Ta 20,%0,16392
Ta 22,%0,16904
Mvaa 172,28,156
Da 68,v2,4096
Mvaa 164,32,192
L_ v6,28,s65,256
Mvaa 164,28,152
S_ s65,s59,5013504
Ta 24,%0,20488
Ta 26,%0,21000
Mvaa 156,32,188
S_ m0,s52,11008
Da 108,v3,4096
Mvaa 156,28,148
Mvaa 148,32,184
L_ v6,28,s65,256
Mvaa 148,28,144
Da 72,v2,6144
Mvaa 140,32,180
Mvaa 140,28,140
W_ 9
Mvaa 176,36,140
s_nop 0
Ta 28,v1,16384
Ta 30,v1,16896
Mvaa 176,224,180
Da 112,v3,6144
Mvaa 184,36,144
Mvaa 184,224,184
s_nop 0
Ta 32,v1,20480
Ta 34,v1,20992
Mvaa 192,36,148
Da 76,v2,8192
Mvaa 192,224,188
Mvaa 200,36,152
Da 116,v3,8192
Mvaa 200,224,192
Mvaa 208,36,156
Mvaa 208,224,196
W_ 9
Mvaa 212,24,196
s_nop 0
Ta 36,v1,16392
Ta 38,v1,16904
Mvaa 212,20,156
Mvaa 204,24,192
Mvaa 204,20,152
s_nop 0
Ta 224,v1,20488
Ta 226,v1,21000
Mvaa 196,24,188
Mvaa 196,20,148
Mvaa 188,24,184
Mvaa 188,20,144
Mvaa 180,24,180
s_waitcnt vmcnt(15) lgkmcnt(0)
s_barrier
s_cmp_eq_u32 s11, 1
s_cbranch_scc1 .Lpace17
.Lpace17_back:
Mvaa 180,20,140
S_ s65,s59,5505024
Mvaa 216,28,140
S_ m0,s52,16384
Ta 20,%0,24576
Ta 22,%0,25088
Mvaa 216,32,180
L_ v6,28,s65,0
Mvaa 224,28,144
S_ s65,s59,5603328
Mvaa 224,32,184
S_ m0,s52,17408
Ta 24,%0,28672
Ta 26,%0,29184
Mvaa 232,28,148
Mvaa 232,32,188
L_ v6,28,s65,0
Mvaa 240,28,152
S_ s65,s59,5701632
Mvaa 240,32,192
S_ m0,s52,18432
Mvaa 248,28,156
Mvaa 248,32,196
Mvaa 252,224,196
s_nop 0
Ta 28,%0,24584
Ta 30,%0,25096
Mvaa 252,36,156
L_ v6,28,s65,0
Mvaa 244,224,192
S_ s65,s59,5799936
Mvaa 244,36,152
S_ m0,s52,19456
Ta 32,%0,28680
Ta 34,%0,29192
Mvaa 236,224,188
Mvaa 236,36,148
L_ v6,28,s65,0
Mvaa 228,224,184
S_ s65,s60,1024
Mvaa 228,36,144
S_ m0,s50,0
Mvaa 220,224,180
Mvaa 220,36,140
W_ 4
Mvaa 16,20,60
s_nop 0
Ta 36,v1,24576
Ta 38,v1,25088
Mvaa 16,24,100
L_ v4,24,s65,0
Mvaa 24,20,64
S_ s65,s60,11264
Mvaa 24,24,104
S_ m0,s50,1024
Ta 224,v1,28672
Ta 226,v1,29184
Mvaa 32,20,68
Mvaa 32,24,108
L_ v5,24,s65,0
Mvaa 40,20,72
S_ s65,s60,21504
Mvaa 40,24,112
S_ m0,s50,2048
Mvaa 48,20,76
Mvaa 48,24,116
W_ 4
Mvaa 52,32,116
s_nop 0
Ta 20,v1,24584
Ta 22,v1,25096
Mvaa 52,28,76
L_ v4,24,s65,0
Mvaa 44,32,112
S_ s65,s60,31744
Mvaa 44,28,72
S_ m0,s50,3072
Ta 24,v1,28680
Ta 26,v1,29192
Mvaa 36,32,108
Mvaa 36,28,68
L_ v5,24,s65,0
Mvaa 28,32,104
S_ s65,s60,41984
Mvaa 28,28,64
S_ m0,s50,4096
Mvaa 20,32,100
s_waitcnt vmcnt(14) lgkmcnt(0)
s_barrier
Mvaa 20,28,60
L_ v4,24,s65,0
Mvaa 56,36,60
S_ s65,s59,5505024
Ta 28,%0,0
Ta 30,%0,512
Mvaa 56,224,100
S_ m0,s52,24448
Mvaa 64,36,64
Mvaa 64,224,104
s_nop 0
Ta 32,%0,4096
Ta 34,%0,4608
Mvaa 72,36,68
L_ v6,28,s65,128
Mvaa 72,224,108
S_ s65,s59,5603328
Mvaa 80,36,72
S_ m0,s52,25472
Mvaa 80,224,112
Mvaa 88,36,76
L_ v6,28,s65,128
Mvaa 88,224,116
S_ s65,s59,5701632
Mvaa 92,24,116
S_ m0,s52,26496
Ta 36,%0,8
Ta 38,%0,520
Mvaa 92,20,76
Mvaa 84,24,112
L_ v6,28,s65,128
Mvaa 84,20,72
S_ s65,s59,5799936
Ta 224,%0,4104
Ta 226,%0,4616
Mvaa 76,24,108
S_ m0,s52,27520
Mvaa 76,20,68
Mvaa 68,24,104
L_ v6,28,s65,128
Mvaa 68,20,64
S_ s65,s61,0
Mvaa 60,24,100
S_ m0,s51,36864
Mvaa 60,20,60
W_ 4
Mvaa 96,28,60
s_nop 0
Ta 20,v1,0
Ta 22,v1,512
Mvaa 96,32,100
Ln v7,32,s65,0
Mvaa 104,28,64
Mvaa 104,32,104
s_nop 0
Ta 24,v1,4096
Ta 26,v1,4608
Mvaa 112,28,68
Mvaa 112,32,108
Ln v8,32,s65,3072
Mvaa 120,28,72
S_ m0,s51,37760
Mvaa 120,32,112
Mvaa 128,28,76
Mvaa 128,32,116
W_ 4
Mvaa 132,224,116
s_nop 0
Ta 28,v1,8
Ta 30,v1,520
Mvaa 132,36,76
Ln v7,32,s65,128
Mvaa 124,224,112
Mvaa 124,36,72
s_nop 0
Ta 32,v1,4104
Ta 34,v1,4616
Mvaa 116,224,108
Mvaa 116,36,68
Ln v8,32,s65,3200
Mvaa 108,224,104
S_ m0,s51,38656
Mvaa 108,36,64
Mvaa 100,224,100
s_waitcnt vmcnt(17) lgkmcnt(0)
s_barrier
s_cmp_eq_u32 s11, 1
s_cbranch_scc1 .Lpace19
.Lpace19_back:
Mvaa 100,36,60
Da 140,v2,20480
Mvaa 136,20,60
s_nop 0
Ta 36,%0,8192
Ta 38,%0,8704
Mvaa 136,24,100
Ln v7,32,s65,256
Mvaa 144,20,64
Da 180,v3,20480
Mvaa 144,24,104
s_nop 0
Ta 224,%0,12288
Ta 226,%0,12800
Mvaa 152,20,68
Mvaa 152,24,108
Da 144,v2,22528
Mvaa 160,20,72
Ln v8,32,s65,3328
Mvaa 160,24,112
S_ s65,s59,5505024
Mvaa 168,20,76
S_ m0,s52,-256
Da 184,v3,22528
Mvaa 168,24,116
Mvaa 172,32,116
s_nop 0
Ta 20,%0,8200
Ta 22,%0,8712
Mvaa 172,28,76
Da 148,v2,24576
Mvaa 164,32,112
L_ v6,28,s65,256
Mvaa 164,28,72
S_ s65,s59,5603328
Ta 24,%0,12296
Ta 26,%0,12808
Mvaa 156,32,108
S_ m0,s52,768
Da 188,v3,24576
Mvaa 156,28,68
Mvaa 148,32,104
L_ v6,28,s65,256
Mvaa 148,28,64
S_ s65,s59,5701632
Da 152,v2,26624
Mvaa 140,32,100
S_ m0,s52,1792
Mvaa 140,28,60
W_ 9
Mvaa 176,36,60
s_nop 0
Ta 28,v1,8192
Ta 30,v1,8704
Mvaa 176,224,100
Da 192,v3,26624
Mvaa 184,36,64
L_ v6,28,s65,256
Mvaa 184,224,104
S_ s65,s59,5799936
Ta 32,v1,12288
Ta 34,v1,12800
Mvaa 192,36,68
S_ m0,s52,2816
Da 156,v2,28672
Mvaa 192,224,108
Mvaa 200,36,72
Da 196,v3,28672
Mvaa 200,224,112
L_ v6,28,s65,256
Mvaa 208,36,76
Mvaa 208,224,116
W_ 9
Mvaa 212,24,116
s_nop 0
Ta 36,v1,8200
Ta 38,v1,8712
Mvaa 212,20,76
Mvaa 204,24,112
Mvaa 204,20,72
s_nop 0
Ta 224,v1,12296
Ta 226,v1,12808
Mvaa 196,24,108
Mvaa 196,20,68
Mvaa 188,24,104
Mvaa 188,20,64
Mvaa 180,24,100
s_waitcnt vmcnt(19) lgkmcnt(0)
s_barrier
Mvaa 180,20,60
S_ s65,s59,6291456
Mvaa 216,28,60
S_ m0,s52,7936
Ta 20,%0,16384
Ta 22,%0,16896
Mvaa 216,32,100
L_ v6,28,s65,256
Mvaa 224,28,64
S_ s65,s59,6389760
Mvaa 224,32,104
S_ m0,s52,8960
Ta 24,%0,20480
Ta 26,%0,20992
Mvaa 232,28,68
Mvaa 232,32,108
L_ v6,28,s65,256
Mvaa 240,28,72
S_ s65,s59,6488064
Mvaa 240,32,112
S_ m0,s52,9984
Mvaa 248,28,76
Mvaa 248,32,116
Mvaa 252,224,116
s_nop 0
Ta 28,%0,16392
Ta 30,%0,16904
Mvaa 252,36,76
L_ v6,28,s65,256
Mvaa 244,224,112
S_ s65,s59,6586368
Mvaa 244,36,72
S_ m0,s52,11008
Ta 32,%0,20488
Ta 34,%0,21000
Mvaa 236,224,108
Mvaa 236,36,68
L_ v6,28,s65,256
Mvaa 228,224,104
S_ s65,s60,1152
Mvaa 228,36,64
S_ m0,s50,20480
Mvaa 220,224,100
Mvaa 220,36,60
W_ 4
Mvaa 16,20,140
s_nop 0
Ta 36,v1,16384
Ta 38,v1,16896
Mvaa 16,24,180
L_ v4,24,s65,0
Mvaa 24,20,144
S_ s65,s60,11392
Mvaa 24,24,184
S_ m0,s50,21504
Ta 224,v1,20480
Ta 226,v1,20992
Mvaa 32,20,148
Mvaa 32,24,188
L_ v5,24,s65,0
Mvaa 40,20,152
S_ s65,s60,21632
Mvaa 40,24,192
S_ m0,s50,22528
Mvaa 48,20,156
Mvaa 48,24,196
W_ 4
Mvaa 52,32,196
s_nop 0
Ta 20,v1,16392
Ta 22,v1,16904
Mvaa 52,28,156
L_ v4,24,s65,0
Mvaa 44,32,192
S_ s65,s60,31872
Mvaa 44,28,152
S_ m0,s50,23552
Ta 24,v1,20488
Ta 26,v1,21000
Mvaa 36,32,188
Mvaa 36,28,148
L_ v5,24,s65,0
Mvaa 28,32,184
S_ s65,s60,42112
Mvaa 28,28,144
S_ m0,s50,24576
Mvaa 20,32,180
s_waitcnt vmcnt(18) lgkmcnt(0)
s_barrier
s_cmp_eq_u32 s11, 1
s_cbranch_scc1 .Lpace21
.Lpace21_back:
Mvaa 20,28,140
L_ v4,24,s65,0
Mvaa 56,36,140
S_ s65,s59,7077888
Ta 28,%0,24576
Ta 30,%0,25088
Mvaa 56,224,180
S_ m0,s52,16128
Mvaa 64,36,144
Mvaa 64,224,184
s_nop 0
Ta 32,%0,28672
Ta 34,%0,29184
Mvaa 72,36,148
L_ v6,28,s65,256
Mvaa 72,224,188
S_ s65,s59,7176192
Mvaa 80,36,152
S_ m0,s52,17152
Mvaa 80,224,192
Mvaa 88,36,156
L_ v6,28,s65,256
Mvaa 88,224,196
S_ s65,s59,7274496
Mvaa 92,24,196
S_ m0,s52,18176
Ta 36,%0,24584
Ta 38,%0,25096
Mvaa 92,20,156
Mvaa 84,24,192
L_ v6,28,s65,256
Mvaa 84,20,152
S_ s65,s59,7372800
Ta 224,%0,28680
Ta 226,%0,29192
Mvaa 76,24,188
S_ m0,s52,19200
Mvaa 76,20,148
Mvaa 68,24,184
L_ v6,28,s65,256
Mvaa 68,20,144
S_ s65,s61,12288
Mvaa 60,24,180
S_ m0,s51,49152
Mvaa 60,20,140
W_ 4
Mvaa 96,28,140
s_nop 0
Ta 20,v1,24576
Ta 22,v1,25088
Mvaa 96,32,180
Ln v7,32,s65,0
Mvaa 104,28,144
Mvaa 104,32,184
s_nop 0
Ta 24,v1,28672
Ta 26,v1,29184
Mvaa 112,28,148
Mvaa 112,32,188
Ln v8,32,s65,3072
Mvaa 120,28,152
S_ m0,s51,50048
Mvaa 120,32,192
Mvaa 128,28,156
Mvaa 128,32,196
W_ 4
Mvaa 132,224,196
s_nop 0
Ta 28,v1,24584
Ta 30,v1,25096
Mvaa 132,36,156
Ln v7,32,s65,128
Mvaa 124,224,192
Mvaa 124,36,152
s_nop 0
Ta 32,v1,28680
Ta 34,v1,29192
Mvaa 116,224,188
Mvaa 116,36,148
Ln v8,32,s65,3200
Mvaa 108,224,184
S_ m0,s51,50944
Mvaa 108,36,144
Mvaa 100,224,180
s_waitcnt vmcnt(17) lgkmcnt(0)
s_barrier
Mvaa 100,36,140
Da 60,v2,0
Mvaa 136,20,140
s_nop 0
Ta 36,%0,0
Ta 38,%0,512
Mvaa 136,24,180
Ln v7,32,s65,256
Mvaa 144,20,144
Da 100,v3,0
Mvaa 144,24,184
s_nop 0
Ta 224,%0,4096
Ta 226,%0,4608
Mvaa 152,20,148
Mvaa 152,24,188
Da 64,v2,2048
Mvaa 160,20,152
Ln v8,32,s65,3328
Mvaa 160,24,192
S_ s65,s59,6291456
Mvaa 168,20,156
S_ m0,s52,24448
Da 104,v3,2048
Mvaa 168,24,196
Mvaa 172,32,196
s_nop 0
Ta 20,%0,8
Ta 22,%0,520
Mvaa 172,28,156
Da 68,v2,4096
Mvaa 164,32,192
L_ v6,28,s65,128
Mvaa 164,28,152
S_ s65,s59,6389760
Ta 24,%0,4104
Ta 26,%0,4616
Mvaa 156,32,188
S_ m0,s52,25472
Da 108,v3,4096
Mvaa 156,28,148
Mvaa 148,32,184
L_ v6,28,s65,128
Mvaa 148,28,144
S_ s65,s59,6488064
Da 72,v2,6144
Mvaa 140,32,180
S_ m0,s52,26496
Mvaa 140,28,140
W_ 9
Mvaa 176,36,140
s_nop 0
Ta 28,v1,0
Ta 30,v1,512
Mvaa 176,224,180
Da 112,v3,6144
Mvaa 184,36,144
L_ v6,28,s65,128
Mvaa 184,224,184
S_ s65,s59,6586368
Ta 32,v1,4096
Ta 34,v1,4608
Mvaa 192,36,148
S_ m0,s52,27520
Da 76,v2,8192
Mvaa 192,224,188
Mvaa 200,36,152
Da 116,v3,8192
Mvaa 200,224,192
L_ v6,28,s65,128
Mvaa 208,36,156
Mvaa 208,224,196
W_ 9
Mvaa 212,24,196
s_nop 0
Ta 36,v1,8
Ta 38,v1,520
Mvaa 212,20,156
Mvaa 204,24,192
Mvaa 204,20,152
s_nop 0
Ta 224,v1,4104
Ta 226,v1,4616
Mvaa 196,24,188
Mvaa 196,20,148
Mvaa 188,24,184
Mvaa 188,20,144
Mvaa 180,24,180
s_waitcnt vmcnt(14) lgkmcnt(0)
s_barrier
s_cmp_eq_u32 s11, 1
s_cbranch_scc1 .Lpace23
.Lpace23_back:
Mvaa 180,20,140
S_ s65,s59,7077888
Mvaa 216,28,140
S_ m0,s52,-128
Ta 20,v1,8200
Ta 22,v1,8712
Mvaa 216,32,180
L_ v6,28,s65,128
Mvaa 224,28,144
S_ s65,s59,7176192
Mvaa 224,32,184
S_ m0,s52,896
Ta 24,v1,12296
Ta 26,v1,12808
Mvaa 232,28,148
Mvaa 232,32,188
L_ v6,28,s65,128
Mvaa 240,28,152
S_ s65,s59,7274496
Mvaa 240,32,192
S_ m0,s52,1920
Mvaa 248,28,156
Mvaa 248,32,196
Mvaa 252,224,196
s_nop 0
Ta 28,v1,8192
Ta 30,v1,8704
Mvaa 252,36,156
L_ v6,28,s65,128
Mvaa 244,224,192
S_ s65,s59,7372800
Mvaa 244,36,152
S_ m0,s52,2944
Ta 32,v1,12288
Ta 34,v1,12800
Mvaa 236,224,188
Mvaa 236,36,148
L_ v6,28,s65,128
Mvaa 228,224,184
Mvaa 228,36,144
Mvaa 220,224,180
Mvaa 220,36,140
W_ 4
Mvaa 220,20,60
s_nop 0
Ta 36,%0,8200
Ta 38,%0,8712
Mvaa 220,24,100
Da 140,v2,20480
Mvaa 228,20,64
Mvaa 228,24,104
s_nop 0
Ta 224,%0,12296
Ta 226,%0,12808
Mvaa 236,20,68
Da 180,v3,20480
Mvaa 236,24,108
Mvaa 244,20,72
Da 144,v2,22528
Mvaa 244,24,112
Mvaa 252,20,76
Mvaa 252,24,116
W_ 7
Da 184,v3,22528
Mvaa 248,32,116
s_nop 0
Ta 20,%0,8192
Ta 22,%0,8704
Mvaa 248,28,76
Mvaa 240,32,112
Da 148,v2,24576
Mvaa 240,28,72
s_nop 0
Ta 24,%0,12288
Ta 26,%0,12800
Mvaa 232,32,108
Mvaa 232,28,68
Da 188,v3,24576
Mvaa 224,32,104
Mvaa 224,28,64
Mvaa 216,32,100
s_waitcnt vmcnt(14) lgkmcnt(0)
s_barrier
Da 152,v2,26624
Mvaa 216,28,60
S_ s65,s59,6291456
Mvaa 180,36,60
S_ m0,s52,8192
Ta 28,v1,16392
Ta 30,v1,16904
Mvaa 180,224,100
Da 192,v3,26624
Mvaa 188,36,64
L_ v6,28,s65,0
Mvaa 188,224,104
S_ s65,s59,6389760
Ta 32,v1,20488
Ta 34,v1,21000
Mvaa 196,36,68
S_ m0,s52,9216
Da 156,v2,28672
Mvaa 196,224,108
Mvaa 204,36,72
L_ v6,28,s65,0
Mvaa 204,224,112
S_ s65,s59,6488064
Da 196,v3,28672
Mvaa 212,36,76
S_ m0,s52,10240
Mvaa 212,224,116
Mvaa 208,24,116
s_nop 0
Ta 36,v1,16384
Ta 38,v1,16896
Mvaa 208,20,76
L_ v6,28,s65,0
Mvaa 200,24,112
S_ s65,s59,6586368
Mvaa 200,20,72
S_ m0,s52,11264
Ta 224,v1,20480
Ta 226,v1,20992
Mvaa 192,24,108
Mvaa 192,20,68
L_ v6,28,s65,0
Mvaa 184,24,104
Mvaa 184,20,64
Mvaa 176,24,100
Mvaa 176,20,60
W_ 6
Mvaa 220,28,140
s_nop 0
Ta 20,%0,16392
Ta 22,%0,16904
Mvaa 220,32,180
Mvaa 228,28,144
Mvaa 228,32,184
s_nop 0
Ta 24,%0,20488
Ta 26,%0,21000
Mvaa 236,28,148
Mvaa 236,32,188
Mvaa 244,28,152
Mvaa 244,32,192
W_ 9
Mvaa 252,28,156
W_ 8
Mvaa 252,32,196
W_ 4
Mvaa 248,224,196
s_nop 0
Ta 28,%0,16384
Ta 30,%0,16896
Mvaa 248,36,156
Mvaa 240,224,192
Mvaa 240,36,152
s_nop 0
Ta 32,%0,20480
Ta 34,%0,20992
Mvaa 232,224,188
Mvaa 232,36,148
Mvaa 224,224,184
Mvaa 224,36,144
Mvaa 216,224,180
s_waitcnt vmcnt(8) lgkmcnt(0)
s_barrier
s_cmp_eq_u32 s11, 1
s_cbranch_scc1 .Lpace25
.Lpace25_back:
Mvaa 216,36,140
S_ s65,s59,7077888
Mvaa 180,20,140
S_ m0,s52,16384
Ta 36,v1,24584
Ta 38,v1,25096
Mvaa 180,24,180
L_ v6,28,s65,0
Mvaa 188,20,144
S_ s65,s59,7176192
Mvaa 188,24,184
S_ m0,s52,17408
Ta 224,v1,28680
Ta 226,v1,29192
Mvaa 196,20,148
U_ v248,s48,v248
U_ v249,s48,v249
Mvaa 196,24,188
L_ v6,28,s65,0
Mvaa 204,20,152
S_ s65,s59,7274496
U_ v250,s48,v250
U_ v251,s48,v251
Mvaa 204,24,192
S_ m0,s52,18432
U_ v252,s48,v252
U_ v253,s48,v253
Mvaa 212,20,156
U_ v254,s48,v254
U_ v255,s48,v255
Mvaa 212,24,196
C_ 255,254,255
C_ 254,252,253
Mvaa 208,32,196
s_nop 0
Ta 20,v1,24576
Ta 22,v1,25088
Mvaa 208,28,156
L_ v6,28,s65,0
Mvaa 200,32,192
S_ s65,s59,7372800
C_ 253,250,251
C_ 252,248,249
Mvaa 200,28,152
S_ m0,s52,19456
Ta 24,v1,28672
Ta 26,v1,29184
Mvaa 192,32,188
U_ v240,s48,v240
U_ v241,s48,v241
Mvaa 192,28,148
L_ v6,28,s65,0
Mvaa 184,32,184
U_ v242,s48,v242
U_ v243,s48,v243
Mvaa 184,28,144
U_ v244,s48,v244
U_ v245,s48,v245
Mvaa 176,32,180
U_ v246,s48,v246
U_ v247,s48,v247
Mvaa 176,28,140
W_ 4
C_ 251,246,247
C_ 250,244,245
Mvaa 140,36,60
s_nop 0
Ta 28,%0,24584
Ta 30,%0,25096
Mvaa 140,224,100
C_ 249,242,243
C_ 248,240,241
Mvaa 148,36,64
U_ v232,s48,v232
U_ v233,s48,v233
Mvaa 148,224,104
s_nop 0
Ta 32,%0,28680
Ta 34,%0,29192
Mvaa 156,36,68
U_ v234,s48,v234
U_ v235,s48,v235
Mvaa 156,224,108
U_ v236,s48,v236
U_ v237,s48,v237
Mvaa 164,36,72
U_ v238,s48,v238
U_ v239,s48,v239
Mvaa 164,224,112
C_ 247,238,239
C_ 246,236,237
Mvaa 172,36,76
C_ 245,234,235
C_ 244,232,233
Mvaa 172,224,116
W_ 4
U_ v224,s48,v224
U_ v225,s48,v225
Mvaa 168,24,116
s_nop 0
Ta 36,%0,24576
Ta 38,%0,25088
Mvaa 168,20,76
U_ v226,s48,v226
U_ v227,s48,v227
Mvaa 160,24,112
U_ v228,s48,v228
U_ v229,s48,v229
Mvaa 160,20,72
s_nop 0
Ta 224,%0,28672
Ta 226,%0,29184
Mvaa 152,24,108
U_ v230,s48,v230
U_ v231,s48,v231
Mvaa 152,20,68
C_ 243,230,231
C_ 242,228,229
Mvaa 144,24,104
C_ 241,226,227
C_ 240,224,225
Mvaa 144,20,64
U_ v216,s48,v216
U_ v217,s48,v217
Mvaa 136,24,100
s_waitcnt vmcnt(8) lgkmcnt(0)
s_barrier
U_ v218,s48,v218
Mvaa 136,20,60
S_ s65,s61,0
U_ v219,s48,v219
Mvaa 100,28,60
S_ m0,s51,24576
Ta 20,v1,8
Ta 22,v1,520
Mvaa 100,32,100
Ln v9,36,s65,0
Mvaa 108,28,64
U_ v220,s48,v220
U_ v221,s48,v221
Mvaa 108,32,104
s_nop 0
Ta 24,v1,4104
Ta 26,v1,4616
Mvaa 116,28,68
U_ v222,s48,v222
U_ v223,s48,v223
Mvaa 116,32,108
Ln v9,36,s65,3072
Mvaa 124,28,72
S_ m0,s51,25472
C_ 239,222,223
C_ 238,220,221
Mvaa 124,32,112
C_ 237,218,219
C_ 236,216,217
Mvaa 132,28,76
U_ v208,s48,v208
U_ v209,s48,v209
Mvaa 132,32,116
U_ v210,s48,v210
U_ v211,s48,v211
Mvaa 128,224,116
s_nop 0
Ta 28,v1,0
Ta 30,v1,512
Mvaa 128,36,76
Ln v9,36,s65,128
Mvaa 120,224,112
U_ v212,s48,v212
U_ v213,s48,v213
Mvaa 120,36,72
s_nop 0
Ta 32,v1,4096
Ta 34,v1,4608
Mvaa 112,224,108
U_ v214,s48,v214
U_ v215,s48,v215
Mvaa 112,36,68
Ln v9,36,s65,3200
Mvaa 104,224,104
S_ m0,s51,26368
C_ 235,214,215
C_ 234,212,213
Mvaa 104,36,64
C_ 233,210,211
C_ 232,208,209
Mvaa 96,224,100
U_ v200,s48,v200
U_ v201,s48,v201
Mvaa 96,36,60
W_ 4
U_ v202,s48,v202
U_ v203,s48,v203
Mvaa 140,20,140
s_nop 0
Ta 36,%0,8
Ta 38,%0,520
Mvaa 140,24,180
Ln v9,36,s65,256
Mvaa 148,20,144
U_ v204,s48,v204
U_ v205,s48,v205
Mvaa 148,24,184
s_nop 0
Ta 224,%0,4104
Ta 226,%0,4616
Mvaa 156,20,148
U_ v206,s48,v206
)A"
R"A(U_ v207,s48,v207
Mvaa 156,24,188
Ln v9,36,s65,3328
Mvaa 164,20,152
C_ 231,206,207
C_ 230,204,205
Mvaa 164,24,192
C_ 229,202,203
C_ 228,200,201
Mvaa 172,20,156
U_ v192,s48,v192
U_ v193,s48,v193
Mvaa 172,24,196
W_ 4
U_ v194,s48,v194
U_ v195,s48,v195
Mvaa 168,32,196
s_nop 0
Ta 20,%0,0
Ta 22,%0,512
Mvaa 168,28,156
U_ v196,s48,v196
U_ v197,s48,v197
Mvaa 160,32,192
U_ v198,s48,v198
U_ v199,s48,v199
Mvaa 160,28,152
s_nop 0
Ta 24,%0,4096
Ta 26,%0,4608
Mvaa 152,32,188
C_ 227,198,199
C_ 226,196,197
Mvaa 152,28,148
C_ 225,194,195
C_ 224,192,193
Mvaa 144,32,184
U_ v184,s48,v184
U_ v185,s48,v185
Mvaa 144,28,144
U_ v186,s48,v186
U_ v187,s48,v187
Mvaa 136,32,180
s_waitcnt vmcnt(10) lgkmcnt(0)
s_barrier
s_cmp_eq_u32 s11, 1
s_cbranch_scc1 .Lpace27
.Lpace27_back:
U_ v188,s48,v188
Mvaa 136,28,140
U_ v189,s48,v189
Mvaa 100,36,140
s_nop 0
Ta 28,v1,8200
Ta 30,v1,8712
Mvaa 100,224,180
U_ v190,s48,v190
U_ v191,s48,v191
Mvaa 108,36,144
C_ 223,190,191
C_ 222,188,189
Mvaa 108,224,184
s_nop 0
Ta 32,v1,12296
Ta 34,v1,12808
Mvaa 116,36,148
C_ 221,186,187
C_ 220,184,185
Mvaa 116,224,188
U_ v176,s48,v176
U_ v177,s48,v177
Mvaa 124,36,152
U_ v178,s48,v178
U_ v179,s48,v179
Mvaa 124,224,192
U_ v180,s48,v180
U_ v181,s48,v181
Mvaa 132,36,156
U_ v182,s48,v182
U_ v183,s48,v183
Mvaa 132,224,196
C_ 219,182,183
C_ 218,180,181
Mvaa 128,24,196
s_nop 0
Ta 36,v1,8192
Ta 38,v1,8704
Mvaa 128,20,156
C_ 217,178,179
C_ 216,176,177
Mvaa 120,24,192
U_ v168,s48,v168
U_ v169,s48,v169
Mvaa 120,20,152
s_nop 0
Ta 224,v1,12288
Ta 226,v1,12800
Mvaa 112,24,188
U_ v170,s48,v170
U_ v171,s48,v171
Mvaa 112,20,148
U_ v172,s48,v172
U_ v173,s48,v173
Mvaa 104,24,184
U_ v174,s48,v174
U_ v175,s48,v175
Mvaa 104,20,144
C_ 215,174,175
C_ 214,172,173
Mvaa 96,24,180
C_ 213,170,171
C_ 212,168,169
Mvaa 96,20,140
W_ 4
U_ v160,s48,v160
U_ v161,s48,v161
Mvaa 60,28,60
s_nop 0
Ta 20,%0,8200
Ta 22,%0,8712
Mvaa 60,32,100
U_ v162,s48,v162
U_ v163,s48,v163
Mvaa 68,28,64
U_ v164,s48,v164
U_ v165,s48,v165
Mvaa 68,32,104
s_nop 0
Ta 24,%0,12296
Ta 26,%0,12808
Mvaa 76,28,68
U_ v166,s48,v166
U_ v167,s48,v167
Mvaa 76,32,108
C_ 211,166,167
C_ 210,164,165
Mvaa 84,28,72
C_ 209,162,163
C_ 208,160,161
Mvaa 84,32,112
U_ v152,s48,v152
U_ v153,s48,v153
Mvaa 92,28,76
U_ v154,s48,v154
U_ v155,s48,v155
Mvaa 92,32,116
W_ 4
U_ v156,s48,v156
U_ v157,s48,v157
Mvaa 88,224,116
s_nop 0
Ta 28,%0,8192
Ta 30,%0,8704
Mvaa 88,36,76
U_ v158,s48,v158
U_ v159,s48,v159
Mvaa 80,224,112
C_ 207,158,159
C_ 206,156,157
Mvaa 80,36,72
s_nop 0
Ta 32,%0,12288
Ta 34,%0,12800
Mvaa 72,224,108
C_ 205,154,155
C_ 204,152,153
Mvaa 72,36,68
U_ v144,s48,v144
U_ v145,s48,v145
Mvaa 64,224,104
U_ v146,s48,v146
U_ v147,s48,v147
Mvaa 64,36,64
U_ v148,s48,v148
U_ v149,s48,v149
Mvaa 56,224,100
s_waitcnt vmcnt(6) lgkmcnt(0)
s_barrier
U_ v150,s48,v150
Mvaa 56,36,60
S_ s65,s61,24576
U_ v151,s48,v151
Mvaa 20,20,60
S_ m0,s51,0
Ta 36,v1,16392
Ta 38,v1,16904
Mvaa 20,24,100
Ln v7,32,s65,0
Mvaa 28,20,64
C_ 203,150,151
C_ 202,148,149
Mvaa 28,24,104
s_nop 0
Ta 224,v1,20488
Ta 226,v1,21000
Mvaa 36,20,68
C_ 201,146,147
C_ 200,144,145
Mvaa 36,24,108
Ln v8,32,s65,3072
Mvaa 44,20,72
S_ m0,s51,896
U_ v136,s48,v136
U_ v137,s48,v137
Mvaa 44,24,112
U_ v138,s48,v138
U_ v139,s48,v139
Mvaa 52,20,76
U_ v140,s48,v140
U_ v141,s48,v141
Mvaa 52,24,116
U_ v142,s48,v142
U_ v143,s48,v143
Mvaa 48,32,116
s_nop 0
Ta 20,v1,16384
Ta 22,v1,16896
Mvaa 48,28,76
Ln v7,32,s65,128
Mvaa 40,32,112
C_ 199,142,143
C_ 198,140,141
Mvaa 40,28,72
s_nop 0
Ta 24,v1,20480
Ta 26,v1,20992
Mvaa 32,32,108
C_ 197,138,139
C_ 196,136,137
Mvaa 32,28,68
Ln v8,32,s65,3200
Mvaa 24,32,104
S_ m0,s51,1792
U_ v128,s48,v128
U_ v129,s48,v129
Mvaa 24,28,64
U_ v130,s48,v130
U_ v131,s48,v131
Mvaa 16,32,100
U_ v132,s48,v132
U_ v133,s48,v133
Mvaa 16,28,60
W_ 4
U_ v134,s48,v134
U_ v135,s48,v135
Mvaa 60,36,140
s_nop 0
Ta 28,%0,16392
Ta 30,%0,16904
Mvaa 60,224,180
Ln v7,32,s65,256
Mvaa 68,36,144
C_ 195,134,135
C_ 194,132,133
Mvaa 68,224,184
s_nop 0
Ta 32,%0,20488
Ta 34,%0,21000
Mvaa 76,36,148
C_ 193,130,131
C_ 192,128,129
Mvaa 76,224,188
Ln v8,32,s65,3328
Mvaa 84,36,152
U_ v120,s48,v120
U_ v121,s48,v121
Mvaa 84,224,192
U_ v122,s48,v122
U_ v123,s48,v123
Mvaa 92,36,156
U_ v124,s48,v124
U_ v125,s48,v125
Mvaa 92,224,196
W_ 4
U_ v126,s48,v126
U_ v127,s48,v127
Mvaa 88,24,196
s_nop 0
Ta 36,%0,16384
Ta 38,%0,16896
Mvaa 88,20,156
C_ 191,126,127
C_ 190,124,125
Mvaa 80,24,192
C_ 189,122,123
C_ 188,120,121
Mvaa 80,20,152
s_nop 0
Ta 224,%0,20480
Ta 226,%0,20992
Mvaa 72,24,188
U_ v112,s48,v112
U_ v113,s48,v113
Mvaa 72,20,148
U_ v114,s48,v114
U_ v115,s48,v115
Mvaa 64,24,184
U_ v116,s48,v116
U_ v117,s48,v117
Mvaa 64,20,144
U_ v118,s48,v118
U_ v119,s48,v119
Mvaa 56,24,180
s_waitcnt vmcnt(51) lgkmcnt(0)
s_barrier
s_cmp_eq_u32 s11, 1
s_cbranch_scc1 .Lpace29
.Lpace29_back:
C_ 187,118,119
Mvaa 56,20,140
S_ s65,s61,12288
C_ 186,116,117
Mvaa 20,28,140
S_ m0,s51,12288
C_ 185,114,115
C_ 184,112,113
Mvaa 20,32,180
Ln v9,36,s65,0
Mvaa 28,28,144
U_ v104,s48,v104
U_ v105,s48,v105
Mvaa 28,32,184
U_ v106,s48,v106
U_ v107,s48,v107
Mvaa 36,28,148
U_ v108,s48,v108
U_ v109,s48,v109
Mvaa 36,32,188
Ln v9,36,s65,3072
Mvaa 44,28,152
S_ m0,s51,13184
U_ v110,s48,v110
U_ v111,s48,v111
Mvaa 44,32,192
C_ 183,110,111
C_ 182,108,109
Mvaa 52,28,156
C_ 181,106,107
C_ 180,104,105
Mvaa 52,32,196
U_ v96,s48,v96
U_ v97,s48,v97
Mvaa 48,224,196
Ln v9,36,s65,128
Mvaa 48,36,156
U_ v98,s48,v98
U_ v99,s48,v99
Mvaa 40,224,192
U_ v100,s48,v100
U_ v101,s48,v101
Mvaa 40,36,152
U_ v102,s48,v102
U_ v103,s48,v103
Mvaa 32,224,188
Ln v9,36,s65,3200
Mvaa 32,36,148
S_ m0,s51,14080
C_ 179,102,103
C_ 178,100,101
Mvaa 24,224,184
C_ 177,98,99
C_ 176,96,97
Mvaa 24,36,144
v_pk_mul_f32 v[88:89], v[88:89], s[48:49]
v_pk_mul_f32 v[90:91], v[90:91], s[48:49]
Mvaa 16,224,180
Ln v9,36,s65,256
Mvaa 16,36,140
v_pk_mul_f32 v[92:93], v[92:93], s[48:49]
v_pk_mul_f32 v[94:95], v[94:95], s[48:49]
Ln v9,36,s65,3328
C_ 175,94,95
C_ 174,92,93
C_ 173,90,91
C_ 172,88,89
v_pk_mul_f32 v[80:81], v[80:81], s[48:49]
v_pk_mul_f32 v[82:83], v[82:83], s[48:49]
v_pk_mul_f32 v[84:85], v[84:85], s[48:49]
v_pk_mul_f32 v[86:87], v[86:87], s[48:49]
C_ 171,86,87
C_ 170,84,85
C_ 169,82,83
C_ 168,80,81
v_pk_mul_f32 v[72:73], v[72:73], s[48:49]
v_pk_mul_f32 v[74:75], v[74:75], s[48:49]
v_pk_mul_f32 v[76:77], v[76:77], s[48:49]
v_pk_mul_f32 v[78:79], v[78:79], s[48:49]
C_ 167,78,79
C_ 166,76,77
C_ 165,74,75
C_ 164,72,73
v_pk_mul_f32 v[40:41], v[40:41], s[48:49]
v_pk_mul_f32 v[42:43], v[42:43], s[48:49]
v_pk_mul_f32 v[44:45], v[44:45], s[48:49]
v_pk_mul_f32 v[46:47], v[46:47], s[48:49]
C_ 151,46,47
C_ 150,44,45
C_ 149,42,43
C_ 148,40,41
v_pk_mul_f32 v[32:33], v[32:33], s[48:49]
v_pk_mul_f32 v[34:35], v[34:35], s[48:49]
v_pk_mul_f32 v[36:37], v[36:37], s[48:49]
v_pk_mul_f32 v[38:39], v[38:39], s[48:49]
C_ 147,38,39
C_ 146,36,37
C_ 145,34,35
C_ 144,32,33
v_pk_mul_f32 v[24:25], v[24:25], s[48:49]
v_pk_mul_f32 v[26:27], v[26:27], s[48:49]
v_pk_mul_f32 v[28:29], v[28:29], s[48:49]
v_pk_mul_f32 v[30:31], v[30:31], s[48:49]
C_ 143,30,31
C_ 142,28,29
C_ 141,26,27
C_ 140,24,25
R_ %0,a244
v_xor_b32 v1, 64, %0
Dv 30,%0,43008
Dv 34,v1,43008
Dv 38,%0,44032
v_pk_mul_f32 v[64:65], v[64:65], s[48:49]
v_pk_mul_f32 v[66:67], v[66:67], s[48:49]
v_pk_mul_f32 v[68:69], v[68:69], s[48:49]
v_pk_mul_f32 v[70:71], v[70:71], s[48:49]
C_ 163,70,71
C_ 162,68,69
C_ 161,66,67
C_ 160,64,65
v_pk_mul_f32 v[56:57], v[56:57], s[48:49]
v_pk_mul_f32 v[58:59], v[58:59], s[48:49]
v_pk_mul_f32 v[60:61], v[60:61], s[48:49]
v_pk_mul_f32 v[62:63], v[62:63], s[48:49]
C_ 159,62,63
C_ 158,60,61
C_ 157,58,59
C_ 156,56,57
v_pk_mul_f32 v[48:49], v[48:49], s[48:49]
v_pk_mul_f32 v[50:51], v[50:51], s[48:49]
v_pk_mul_f32 v[52:53], v[52:53], s[48:49]
v_pk_mul_f32 v[54:55], v[54:55], s[48:49]
C_ 155,54,55
C_ 154,52,53
C_ 153,50,51
C_ 152,48,49
v_pk_mul_f32 v[16:17], v[16:17], s[48:49]
v_pk_mul_f32 v[18:19], v[18:19], s[48:49]
v_pk_mul_f32 v[20:21], v[20:21], s[48:49]
v_pk_mul_f32 v[22:23], v[22:23], s[48:49]
C_ 139,22,23
C_ 138,20,21
C_ 137,18,19
C_ 136,16,17
s_cmp_eq_u32 s58, 1
s_cbranch_scc1 .Latt1
s_cmp_eq_u32 s58, 2
s_cbranch_scc1 .Latt2
.Latt0:
s_cmp_eq_u32 s4, 0
s_cbranch_scc0 .Lhs_tok0
R_ v11,a241
buffer_load_dword a255, v11, s[92:95], 0 offen offset:0 sc1
.Lhs_tok0:
S_ s59,s59,768
W_ 2
Zvvv 96,30,136
Dv 42,v1,44032
R_ v2,a245
Zvvv 100,30,140
v_xor_b32 v3, 32, v2
v_xor_b32 v4, 64, v2
Zvvv 104,30,144
v_xor_b32 v5, 96, v2
Zvvv 108,30,148
Zvvv 112,30,152
W_ 2
Mvvv 96,34,156
Dv 30,%0,45056
Mvvv 100,34,160
Mvvv 104,34,164
Mvvv 108,34,168
Mvvv 112,34,172
W_ 2
Mvvv 96,38,176
Dv 34,v1,45056
Mvvv 100,38,180
Mvvv 104,38,184
Mvvv 108,38,188
Mvvv 112,38,192
W_ 2
Mvvv 96,42,196
Dv 38,%0,36864
Mvvv 100,42,200
Mvvv 104,42,204
Mvvv 108,42,208
Mvvv 112,42,212
W_ 2
Mvvv 96,30,216
Dv 42,v1,36864
Mvvv 100,30,220
Mvvv 104,30,224
Mvvv 108,30,228
Mvvv 112,30,232
W_ 2
Mvvv 96,34,236
Dv 30,%0,37888
Mvvv 100,34,240
Mvvv 104,34,244
Mvvv 108,34,248
Mvvv 112,34,252
W_ 2
s_nop 1
v_max3_f32 v56, v96, v97, v98
v_max_f32 v56, v56, v99
Zvvv 76,38,136
Dv 34,v1,37888
v_max3_f32 v60, v100, v101, v102
Zvvv 80,38,140
v_max_f32 v60, v60, v103
Zvvv 84,38,144
v_max3_f32 v64, v104, v105, v106
v_max_f32 v64, v64, v107
Zvvv 88,38,148
v_max3_f32 v68, v108, v109, v110
v_max_f32 v68, v68, v111
Zvvv 92,38,152
W_ 2
v_max3_f32 v72, v112, v113, v114
v_max_f32 v72, v72, v115
Mvvv 76,42,156
Dv 38,%0,38912
Mvvv 80,42,160
Mvvv 84,42,164
Mvvv 88,42,168
Mvvv 92,42,172
W_ 2
Mvvv 76,30,176
Dv 42,v1,38912
Mvvv 80,30,180
Mvvv 84,30,184
Mvvv 88,30,188
Mvvv 92,30,192
s_waitcnt vmcnt(34) lgkmcnt(0)
s_barrier
Mvvv 76,34,196
S_ s65,s61,36864
Dv 30,%0,55296
Mvvv 76,38,216
S_ m0,s51,36864
Mvvv 76,42,236
Ln v7,32,s65,0
Mvvv 80,34,200
Mvvv 80,38,220
Ln v8,32,s65,3072
Mvvv 80,42,240
S_ m0,s51,37760
Mvvv 84,34,204
Ln v7,32,s65,128
Mvvv 84,38,224
v_max3_f32 v56, v56, v76, v77
v_max3_f32 v56, v56, v78, v79
Mvvv 84,42,244
Ln v8,32,s65,3200
Mvvv 88,34,208
S_ m0,s51,38656
v_max3_f32 v60, v60, v80, v81
v_max3_f32 v60, v60, v82, v83
Mvvv 88,38,228
Ln v7,32,s65,256
Mvvv 88,42,248
Mvvv 92,34,212
Ln v8,32,s65,3328
Mvvv 92,38,232
v_max3_f32 v64, v64, v84, v85
v_max3_f32 v64, v64, v86, v87
Mvvv 92,42,252
Dv 34,v1,55296
.p2align 3
v_mov_b32 v11, 0x3f803f80
v_permlane32_swap_b32 v56, v60
v_max3_f32 v68, v68, v88, v89
v_max3_f32 v68, v68, v90, v91
v_max_f32 v56, v56, v60
s_nop 1
v_max3_f32 v72, v72, v92, v93
v_max3_f32 v72, v72, v94, v95
v_mov_b32 v73, v72
v_permlane32_swap_b32 v64, v68
v_max_f32 v64, v64, v68
v_permlane32_swap_b32 v72, v73
v_max_f32 v72, v72, v73
v_mov_b32 v73, v72
v_permlane16_swap_b32 v56, v64
v_max_f32 v56, v56, v64
v_permlane16_swap_b32 v72, v73
B_ v56,0xc0e00000,v56
v_max_f32 v72, v72, v73
v_mov_b32 v64, v56
B_ v72,0xc0e00000,v72
v_mov_b32 v73, v72
v_permlane16_swap_b32 v56, v64
v_mov_b32 v60, v56
v_mov_b32 v68, v64
v_mov_b64 v[74:75], v[72:73]
v_permlane32_swap_b32 v56, v60
v_permlane32_swap_b32 v64, v68
v_mov_b32 v57, v56
v_mov_b32 v61, v60
v_mov_b32 v65, v64
v_mov_b32 v69, v68
v_pk_add_f32 v[96:97], v[96:97], v[56:57]
v_pk_add_f32 v[98:99], v[98:99], v[56:57]
v_pk_add_f32 v[76:77], v[76:77], v[56:57]
v_pk_add_f32 v[78:79], v[78:79], v[56:57]
v_mov_b64 v[58:59], v[56:57]
v_pk_add_f32 v[100:101], v[100:101], v[60:61]
v_pk_add_f32 v[102:103], v[102:103], v[60:61]
v_pk_add_f32 v[80:81], v[80:81], v[60:61]
v_pk_add_f32 v[82:83], v[82:83], v[60:61]
v_mov_b64 v[62:63], v[60:61]
v_pk_add_f32 v[104:105], v[104:105], v[64:65]
v_pk_add_f32 v[106:107], v[106:107], v[64:65]
v_pk_add_f32 v[84:85], v[84:85], v[64:65]
v_pk_add_f32 v[86:87], v[86:87], v[64:65]
v_mov_b64 v[66:67], v[64:65]
v_pk_add_f32 v[108:109], v[108:109], v[68:69]
v_pk_add_f32 v[110:111], v[110:111], v[68:69]
v_pk_add_f32 v[88:89], v[88:89], v[68:69]
v_pk_add_f32 v[90:91], v[90:91], v[68:69]
v_mov_b64 v[70:71], v[68:69]
v_pk_add_f32 v[112:113], v[112:113], v[72:73]
v_pk_add_f32 v[114:115], v[114:115], v[72:73]
v_pk_add_f32 v[92:93], v[92:93], v[72:73]
v_pk_add_f32 v[94:95], v[94:95], v[72:73]
E_ 96
E_ 97
E_ 98
E_ 99
E_ 76
E_ 77
E_ 78
E_ 79
C_ 99,98,99
C_ 98,96,97
C_ 96,76,77
C_ 97,78,79
E_ 100
E_ 101
E_ 102
E_ 103
E_ 80
E_ 81
E_ 82
E_ 83
C_ 103,102,103
C_ 102,100,101
C_ 100,80,81
C_ 101,82,83
.Lstep0_1:
W_ 1
Nvvv 116,30,136,56
s_nop 0
Dv 38,%0,56320
Dv 42,v1,56320
W_ 2
Mvvv 116,34,156
E_ 104
Nvvv 120,30,140,60
E_ 105
Mvvv 120,34,160
E_ 106
Nvvv 124,30,144,64
E_ 107
Mvvv 124,34,164
E_ 84
Nvvv 128,30,148,68
E_ 85
Mvvv 128,34,168
E_ 86
Nvvv 132,30,152,72
E_ 87
Mvvv 132,34,172
W_ 0
C_ 107,106,107
C_ 106,104,105
Mvvv 132,38,192
s_nop 0
Dv 30,%0,57344
Dv 34,v1,57344
Mvvv 132,42,212
C_ 104,84,85
C_ 105,86,87
Mvvv 128,38,188
E_ 108
Mvvv 128,42,208
E_ 109
Mvvv 124,38,184
E_ 110
Mvvv 124,42,204
E_ 111
Mvvv 120,38,180
E_ 88
Mvvv 120,42,200
E_ 89
Mvvv 116,38,176
E_ 90
Mvvv 116,42,196
W_ 0
E_ 91
Mvvv 116,30,216
s_nop 0
Dv 38,%0,49152
Dv 42,v1,49152
Mvvv 116,34,236
C_ 111,110,111
C_ 110,108,109
Mvvv 120,30,220
C_ 108,88,89
C_ 109,90,91
Mvvv 120,34,240
E_ 112
Mvvv 124,30,224
E_ 113
Mvvv 124,34,244
E_ 114
Mvvv 128,30,228
E_ 115
Mvvv 128,34,248
E_ 92
Mvvv 132,30,232
E_ 93
Mvvv 132,34,252
W_ 0
E_ 94
Nvvv 76,38,136,56
s_nop 0
Dv 30,%0,50176
Dv 34,v1,50176
Mvvv 76,42,156
E_ 95
Nvvv 80,38,140,60
C_ 115,114,115
C_ 114,112,113
Mvvv 80,42,160
C_ 112,92,93
C_ 113,94,95
Nvvv 84,38,144,64
E_ 116
Mvvv 84,42,164
E_ 117
Nvvv 88,38,148,68
E_ 118
Mvvv 88,42,168
E_ 119
Nvvv 92,38,152,72
C_ 119,118,119
C_ 118,116,117
Mvvv 92,42,172
W_ 0
E_ 120
Mvvv 92,30,192
s_nop 0
Dv 38,%0,51200
Dv 42,v1,51200
Mvvv 92,34,212
X_ a180,v11
X_ a181,v11
Mvvv 88,30,188
X_ a182,v11
X_ a183,v11
Mvvv 88,34,208
E_ 121
Mvvv 84,30,184
E_ 122
Mvvv 84,34,204
E_ 123
Mvvv 80,30,180
C_ 123,122,123
C_ 122,120,121
Mvvv 80,34,200
E_ 124
Mvvv 76,30,176
E_ 125
Mvvv 76,34,196
S_ s65,s61,24576
E_ 126
W_ 1
Mvvv 76,38,216
S_ m0,s51,49152
E_ 127
W_ 0
Mvvv 76,42,236
s_waitcnt vmcnt(18) lgkmcnt(0)
s_barrier
C_ 127,126,127
Mvvv 80,38,220
L_ v9,36,s65,0
Mvvv 80,42,240
C_ 126,124,125
Mvvv 84,38,224
L_ v9,36,s65,3072
S_ m0,s51,50048
Mvvv 84,42,244
s_nop 0
Tv 30,v2,24576
Tv 32,v2,30720
Mvvv 88,38,228
L_ v9,36,s65,128
Mvvv 88,42,248
s_nop 0
Tv 34,v3,24576
Tv 36,v3,30720
Mvvv 92,38,232
L_ v9,36,s65,3200
S_ m0,s51,50944
Mvvv 92,42,252
E_ 128
Zaav 220,180,96
L_ v9,36,s65,256
Zaav 224,180,100
E_ 129
Zaav 228,180,104
L_ v9,36,s65,3328
Zaav 232,180,108
E_ 130
Zaav 236,180,112
W_ 0
E_ 131
Zavv 0,30,96
s_nop 0
Tv 38,v4,24576
Tv 40,v4,30720
Zavv 20,34,96
C_ 131,130,131
C_ 130,128,129
Zavv 24,34,100
s_nop 0
Tv 42,v5,24576
Tv 44,v5,30720
Zavv 4,30,100
E_ 132
Zavv 8,30,104
E_ 133
Zavv 28,34,104
R_ v46,a220
R_ v47,a224
Zavv 32,34,108
R_ v48,a228
R_ v49,a232
Zavv 12,30,108
R_ v50,a236
Zavv 16,30,112
E_ 134
Zavv 36,34,112
W_ 0
E_ 135
Zavv 56,38,112
s_nop 0
Tv 30,v2,25600
Tv 32,v2,31744
Zavv 76,42,112
C_ 135,134,135
C_ 134,132,133
Zavv 72,42,108
s_nop 0
Tv 34,v3,25600
Tv 36,v3,31744
Zavv 52,38,108
E_ 76
Zavv 48,38,104
E_ 77
Zavv 68,42,104
E_ 78
Zavv 64,42,100
E_ 79
Zavv 44,38,100
C_ 116,76,77
C_ 117,78,79
Zavv 40,38,96
E_ 80
Zavv 60,42,96
W_ 0
E_ 81
Zavv 80,30,96
s_nop 0
Tv 38,v4,25600
Tv 40,v4,31744
Zavv 100,34,96
E_ 82
Zavv 104,34,100
s_nop 0
Tv 42,v5,25600
Tv 44,v5,31744
Zavv 84,30,100
E_ 83
Zavv 88,30,104
C_ 120,80,81
C_ 121,82,83
Zavv 108,34,104
E_ 84
Zavv 112,34,108
E_ 85
Zavv 92,30,108
E_ 86
Zavv 96,30,112
E_ 87
Zavv 116,34,112
W_ 0
C_ 124,84,85
C_ 125,86,87
Zavv 136,38,112
s_nop 0
Tv 30,v2,26624
Tv 32,v2,32768
Zavv 156,42,112
E_ 88
Zavv 152,42,108
s_nop 0
Tv 34,v3,26624
Tv 36,v3,32768
Zavv 132,38,108
E_ 89
Zavv 128,38,104
E_ 90
Zavv 148,42,104
E_ 91
Zavv 144,42,100
C_ 128,88,89
C_ 129,90,91
Zavv 124,38,100
E_ 92
Zavv 120,38,96
E_ 93
Zavv 140,42,96
W_ 0
E_ 94
Zavv 160,30,96
s_nop 0
Tv 38,v4,26624
Tv 40,v4,32768
Zavv 180,34,96
E_ 95
Zavv 184,34,100
s_nop 0
Tv 42,v5,26624
Tv 44,v5,32768
Zavv 164,30,100
C_ 132,92,93
C_ 133,94,95
Zavv 168,30,104
v_mov_b32 v92, 0x3f803f80
v_mov_b32 v93, 0x3f803f80
Zavv 188,34,104
S_ s65,s61,49152
v_mov_b32 v94, 0x3f803f80
v_mov_b32 v95, 0x3f803f80
Zavv 192,34,108
S_ m0,s51,24576
Zavv 172,30,108
s_waitcnt vmcnt(18) lgkmcnt(0)
s_barrier
Zavv 176,30,112
Ln v7,32,s65,0
Zavv 196,34,112
Zavv 216,38,112
Dv 30,%0,6144
Zavv 236,42,112
Dv 34,v1,6144
Zavv 232,42,108
Ln v8,32,s65,3072
S_ m0,s51,25472
Zavv 212,38,108
Zavv 208,38,104
Ln v7,32,s65,128
Zavv 228,42,104
Zavv 224,42,100
Ln v8,32,s65,3200
S_ m0,s51,26368
Zavv 204,38,100
Zavv 200,38,96
Ln v7,32,s65,256
Zavv 220,42,96
Ln v8,32,s65,3328
.Lstep0_2:
Zvvv 76,92,116
Dv 38,%0,7168
Zvvv 80,92,120
Dv 42,v1,7168
Zvvv 84,92,124
Zvvv 88,92,128
Zvvv 92,92,132
W_ 2
Nvvv 96,30,136,56
Mvvv 96,34,156
Nvvv 100,30,140,60
s_nop 1
v_max3_f32 v10, v76, v80, v84
s_nop 0
v_max3_f32 v10, v10, v88, v92
Mvvv 100,34,160
v_cmp_lt_f32 vcc, 0x42000000, v10
s_cbranch_vccnz .Ltramp0_1
Nvvv 104,30,144,64
A_ v46,v46,v76
A_ v47,v47,v80
Mvvv 104,34,164
A_ v48,v48,v84
A_ v49,v49,v88
Nvvv 108,30,148,68
A_ v50,v50,v92
Mvvv 108,34,168
Nvvv 112,30,152,72
Mvvv 112,34,172
W_ 0
Mvvv 112,38,192
Dv 30,%0,8192
Mvvv 112,42,212
Dv 34,v1,8192
Mvvv 108,38,188
Mvvv 108,42,208
Mvvv 104,38,184
Mvvv 104,42,204
Mvvv 100,38,180
Mvvv 100,42,200
Mvvv 96,38,176
Mvvv 96,42,196
W_ 0
Mvvv 96,30,216
Dv 38,%0,0
Mvvv 96,34,236
Dv 42,v1,0
Mvvv 100,30,220
Mvvv 100,34,240
Mvvv 104,30,224
Mvvv 104,34,244
Mvvv 108,30,228
Mvvv 108,34,248
s_nop 0
E_ 96
Mvvv 112,30,232
E_ 97
Mvvv 112,34,252
W_ 0
E_ 98
Nvvv 76,38,136,56
Dv 30,%0,1024
Mvvv 76,42,156
Dv 34,v1,1024
Nvvv 80,38,140,60
E_ 99
Mvvv 80,42,160
C_ 99,98,99
C_ 98,96,97
Nvvv 84,38,144,64
E_ 100
Mvvv 84,42,164
E_ 101
Nvvv 88,38,148,68
E_ 102
Mvvv 88,42,168
E_ 103
Nvvv 92,38,152,72
C_ 103,102,103
C_ 102,100,101
Mvvv 92,42,172
W_ 0
E_ 104
Mvvv 92,30,192
Dv 38,%0,2048
Mvvv 92,34,212
Dv 42,v1,2048
Mvvv 88,30,188
E_ 105
Mvvv 88,34,208
E_ 106
Mvvv 84,30,184
S_ s65,s61,36864
E_ 107
Mvvv 84,34,204
S_ m0,s51,0
C_ 107,106,107
C_ 106,104,105
Mvvv 80,30,180
s_waitcnt vmcnt(18) lgkmcnt(0)
s_barrier
Mvvv 80,34,200
L_ v9,36,s65,0
Mvvv 76,30,176
E_ 108
Mvvv 76,34,196
L_ v9,36,s65,3072
S_ m0,s51,896
Mvvv 76,38,216
Tv 30,v2,12288
Tv 32,v2,18432
Mvvv 76,42,236
L_ v9,36,s65,128
Mvvv 80,38,220
s_nop 0
Tv 34,v3,12288
Tv 36,v3,18432
Mvvv 80,42,240
L_ v9,36,s65,3200
S_ m0,s51,1792
Mvvv 84,38,224
E_ 109
Mvvv 84,42,244
L_ v9,36,s65,256
Mvvv 88,38,228
E_ 110
Mvvv 88,42,248
L_ v9,36,s65,3328
Mvvv 92,38,232
E_ 111
Mvvv 92,42,252
W_ 0
C_ 111,110,111
C_ 110,108,109
Mavv 0,30,116
s_nop 0
Tv 38,v4,12288
Tv 40,v4,18432
Mavv 20,34,116
E_ 112
Mavv 24,34,120
s_nop 0
Tv 42,v5,12288
Tv 44,v5,18432
Mavv 4,30,120
E_ 113
Mavv 8,30,124
E_ 114
Mavv 28,34,124
E_ 115
Mavv 32,34,128
C_ 115,114,115
C_ 114,112,113
Mavv 12,30,128
E_ 76
Mavv 16,30,132
E_ 77
Mavv 36,34,132
W_ 0
E_ 78
Mavv 56,38,132
s_nop 0
Tv 30,v2,13312
Tv 32,v2,19456
Mavv 76,42,132
E_ 79
Mavv 72,42,128
s_nop 0
Tv 34,v3,13312
Tv 36,v3,19456
Mavv 52,38,128
C_ 96,76,77
C_ 97,78,79
Mavv 48,38,124
E_ 80
Mavv 68,42,124
E_ 81
Mavv 64,42,120
E_ 82
Mavv 44,38,120
E_ 83
Mavv 40,38,116
C_ 100,80,81
C_ 101,82,83
Mavv 60,42,116
W_ 0
E_ 84
Mavv 80,30,116
s_nop 0
Tv 38,v4,13312
Tv 40,v4,19456
Mavv 100,34,116
E_ 85
Mavv 104,34,120
s_nop 0
Tv 42,v5,13312
Tv 44,v5,19456
Mavv 84,30,120
E_ 86
Mavv 88,30,124
E_ 87
Mavv 108,34,124
C_ 104,84,85
C_ 105,86,87
Mavv 112,34,128
E_ 88
Mavv 92,30,128
E_ 89
Mavv 96,30,132
E_ 90
Mavv 116,34,132
W_ 0
E_ 91
Mavv 136,38,132
s_nop 0
Tv 30,v2,14336
Tv 32,v2,20480
Mavv 156,42,132
C_ 108,88,89
C_ 109,90,91
Mavv 152,42,128
s_nop 0
Tv 34,v3,14336
Tv 36,v3,20480
Mavv 132,38,128
E_ 92
Mavv 128,38,124
E_ 93
Mavv 148,42,124
E_ 94
Mavv 144,42,120
E_ 95
Mavv 124,38,120
C_ 112,92,93
C_ 113,94,95
Mavv 120,38,116
v_mov_b32 v92, 0x3f803f80
v_mov_b32 v93, 0x3f803f80
Mavv 140,42,116
W_ 0
v_mov_b32 v94, 0x3f803f80
v_mov_b32 v95, 0x3f803f80
Mavv 160,30,116
s_nop 0
Tv 38,v4,14336
Tv 40,v4,20480
Mavv 180,34,116
Mavv 184,34,120
s_nop 0
Tv 42,v5,14336
Tv 44,v5,20480
Mavv 164,30,120
Mavv 168,30,124
Mavv 188,34,124
S_ s65,s61,61440
Mavv 192,34,128
S_ m0,s51,12288
Mavv 172,30,128
s_waitcnt vmcnt(18) lgkmcnt(0)
s_barrier
Mavv 176,30,132
Ln v7,32,s65,0
Mavv 196,34,132
Mavv 216,38,132
Dv 30,%0,43008
Mavv 236,42,132
Dv 34,v1,43008
Mavv 232,42,128
Ln v8,32,s65,3072
S_ m0,s51,13184
Mavv 212,38,128
Mavv 208,38,124
Ln v7,32,s65,128
Mavv 228,42,124
Mavv 224,42,120
Ln v8,32,s65,3200
S_ m0,s51,14080
Mavv 204,38,120
Mavv 200,38,116
Ln v7,32,s65,256
Mavv 220,42,116
s_cmp_eq_u32 s4, 0
s_cbranch_scc1 .Lhs_a0
.Lhs_a0_back:
.Lstep0_3:
Zvvv 76,92,96
Dv 38,%0,44032
Zvvv 80,92,100
Dv 42,v1,44032
Zvvv 84,92,104
Zvvv 88,92,108
Zvvv 92,92,112
W_ 2
Nvvv 116,30,136,56
Mvvv 116,34,156
Nvvv 120,30,140,60
s_nop 1
v_max3_f32 v10, v76, v80, v84
s_nop 0
v_max3_f32 v10, v10, v88, v92
Mvvv 120,34,160
v_cmp_lt_f32 vcc, 0x42000000, v10
s_cbranch_vccnz .Ltramp0_2
Nvvv 124,30,144,64
A_ v46,v46,v76
A_ v47,v47,v80
Mvvv 124,34,164
A_ v48,v48,v84
A_ v49,v49,v88
Nvvv 128,30,148,68
A_ v50,v50,v92
Mvvv 128,34,168
Ln v8,32,s65,3328
Nvvv 132,30,152,72
Mvvv 132,34,172
W_ 0
Mvvv 132,38,192
Dv 30,%0,45056
Mvvv 132,42,212
Dv 34,v1,45056
Mvvv 128,38,188
Mvvv 128,42,208
Mvvv 124,38,184
Mvvv 124,42,204
Mvvv 120,38,180
Mvvv 120,42,200
Mvvv 116,38,176
Mvvv 116,42,196
W_ 0
Mvvv 116,30,216
Dv 38,%0,36864
Mvvv 116,34,236
Dv 42,v1,36864
Mvvv 120,30,220
Mvvv 120,34,240
Mvvv 124,30,224
Mvvv 124,34,244
Mvvv 128,30,228
Mvvv 128,34,248
s_nop 0
E_ 116
Mvvv 132,30,232
E_ 117
Mvvv 132,34,252
W_ 0
E_ 118
Nvvv 76,38,136,56
Dv 30,%0,37888
Mvvv 76,42,156
Dv 34,v1,37888
Nvvv 80,38,140,60
E_ 119
Mvvv 80,42,160
C_ 119,118,119
C_ 118,116,117
Nvvv 84,38,144,64
E_ 120
Mvvv 84,42,164
E_ 121
Nvvv 88,38,148,68
E_ 122
Mvvv 88,42,168
E_ 123
Nvvv 92,38,152,72
C_ 123,122,123
C_ 122,120,121
Mvvv 92,42,172
W_ 0
E_ 124
Mvvv 92,30,192
Dv 38,%0,38912
Mvvv 92,34,212
Dv 42,v1,38912
Mvvv 88,30,188
E_ 125
Mvvv 88,34,208
E_ 126
Mvvv 84,30,184
S_ s65,s61,49152
E_ 127
Mvvv 84,34,204
S_ m0,s51,36864
C_ 127,126,127
C_ 126,124,125
Mvvv 80,30,180
s_waitcnt vmcnt(18) lgkmcnt(0)
s_barrier
Mvvv 80,34,200
L_ v9,36,s65,0
Mvvv 76,30,176
E_ 128
Mvvv 76,34,196
L_ v9,36,s65,3072
S_ m0,s51,37760
Mvvv 76,38,216
Tv 30,v2,49152
Tv 32,v2,55296
Mvvv 76,42,236
L_ v9,36,s65,128
Mvvv 80,38,220
s_nop 0
Tv 34,v3,49152
Tv 36,v3,55296
Mvvv 80,42,240
L_ v9,36,s65,3200
S_ m0,s51,38656
Mvvv 84,38,224
E_ 129
Mvvv 84,42,244
L_ v9,36,s65,256
Mvvv 88,38,228
E_ 130
Mvvv 88,42,248
L_ v9,36,s65,3328
Mvvv 92,38,232
E_ 131
Mvvv 92,42,252
W_ 0
C_ 131,130,131
C_ 130,128,129
Mavv 0,30,96
s_nop 0
Tv 38,v4,49152
Tv 40,v4,55296
Mavv 20,34,96
E_ 132
Mavv 24,34,100
s_nop 0
Tv 42,v5,49152
Tv 44,v5,55296
Mavv 4,30,100
E_ 133
Mavv 8,30,104
E_ 134
Mavv 28,34,104
E_ 135
Mavv 32,34,108
C_ 135,134,135
C_ 134,132,133
Mavv 12,30,108
E_ 76
Mavv 16,30,112
E_ 77
Mavv 36,34,112
W_ 0
E_ 78
Mavv 56,38,112
s_nop 0
Tv 30,v2,50176
Tv 32,v2,56320
Mavv 76,42,112
E_ 79
Mavv 72,42,108
s_nop 0
Tv 34,v3,50176
Tv 36,v3,56320
Mavv 52,38,108
C_ 116,76,77
C_ 117,78,79
Mavv 48,38,104
E_ 80
Mavv 68,42,104
E_ 81
Mavv 64,42,100
E_ 82
Mavv 44,38,100
E_ 83
Mavv 40,38,96
C_ 120,80,81
C_ 121,82,83
Mavv 60,42,96
W_ 0
E_ 84
Mavv 80,30,96
s_nop 0
Tv 38,v4,50176
Tv 40,v4,56320
Mavv 100,34,96
E_ 85
Mavv 104,34,100
s_nop 0
Tv 42,v5,50176
Tv 44,v5,56320
Mavv 84,30,100
E_ 86
Mavv 88,30,104
E_ 87
Mavv 108,34,104
C_ 124,84,85
C_ 125,86,87
Mavv 112,34,108
E_ 88
Mavv 92,30,108
E_ 89
Mavv 96,30,112
E_ 90
Mavv 116,34,112
W_ 0
E_ 91
Mavv 136,38,112
s_nop 0
Tv 30,v2,51200
Tv 32,v2,57344
Mavv 156,42,112
C_ 128,88,89
C_ 129,90,91
Mavv 152,42,108
s_nop 0
Tv 34,v3,51200
Tv 36,v3,57344
Mavv 132,38,108
E_ 92
Mavv 128,38,104
E_ 93
Mavv 148,42,104
E_ 94
Mavv 144,42,100
E_ 95
Mavv 124,38,100
C_ 132,92,93
C_ 133,94,95
Mavv 120,38,96
v_mov_b32 v92, 0x3f803f80
v_mov_b32 v93, 0x3f803f80
Mavv 140,42,96
W_ 0
v_mov_b32 v94, 0x3f803f80
v_mov_b32 v95, 0x3f803f80
Mavv 160,30,96
s_nop 0
Tv 38,v4,51200
Tv 40,v4,57344
Mavv 180,34,96
Mavv 184,34,100
s_nop 0
Tv 42,v5,51200
Tv 44,v5,57344
Mavv 164,30,100
Mavv 168,30,104
Mavv 188,34,104
S_ s65,s61,73728
Mavv 192,34,108
S_ m0,s51,49152
Mavv 172,30,108
s_waitcnt vmcnt(18) lgkmcnt(0)
s_barrier
Mavv 176,30,112
Ln v7,32,s65,0
Mavv 196,34,112
Mavv 216,38,112
Dv 30,%0,30720
Mavv 236,42,112
Dv 34,v1,30720
Mavv 232,42,108
Ln v8,32,s65,3072
S_ m0,s51,50048
Mavv 212,38,108
Mavv 208,38,104
Ln v7,32,s65,128
Mavv 228,42,104
Mavv 224,42,100
Ln v8,32,s65,3200
S_ m0,s51,50944
Mavv 204,38,100
Mavv 200,38,96
Ln v7,32,s65,256
Mavv 220,42,96
.Lstep0_4:
Zvvv 76,92,116
Dv 38,%0,31744
Zvvv 80,92,120
Dv 42,v1,31744
Zvvv 84,92,124
Zvvv 88,92,128
Zvvv 92,92,132
W_ 2
Nvvv 96,30,136,56
Mvvv 96,34,156
Nvvv 100,30,140,60
s_nop 1
v_max3_f32 v10, v76, v80, v84
s_nop 0
v_max3_f32 v10, v10, v88, v92
Mvvv 100,34,160
v_cmp_lt_f32 vcc, 0x42000000, v10
s_cbranch_vccnz .Ltramp0_3
Nvvv 104,30,144,64
A_ v46,v46,v76
A_ v47,v47,v80
Mvvv 104,34,164
A_ v48,v48,v84
A_ v49,v49,v88
Nvvv 108,30,148,68
A_ v50,v50,v92
Mvvv 108,34,168
Ln v8,32,s65,3328
Nvvv 112,30,152,72
Mvvv 112,34,172
W_ 0
Mvvv 112,38,192
Dv 30,%0,32768
Mvvv 112,42,212
Dv 34,v1,32768
Mvvv 108,38,188
Mvvv 108,42,208
Mvvv 104,38,184
Mvvv 104,42,204
Mvvv 100,38,180
Mvvv 100,42,200
Mvvv 96,38,176
Mvvv 96,42,196
W_ 0
Mvvv 96,30,216
Dv 38,%0,24576
Mvvv 96,34,236
Dv 42,v1,24576
Mvvv 100,30,220
Mvvv 100,34,240
Mvvv 104,30,224
Mvvv 104,34,244
Mvvv 108,30,228
Mvvv 108,34,248
s_nop 0
E_ 96
Mvvv 112,30,232
E_ 97
Mvvv 112,34,252
W_ 0
E_ 98
Nvvv 76,38,136,56
Dv 30,%0,25600
Mvvv 76,42,156
Dv 34,v1,25600
Nvvv 80,38,140,60
E_ 99
Mvvv 80,42,160
C_ 99,98,99
C_ 98,96,97
Nvvv 84,38,144,64
E_ 100
Mvvv 84,42,164
E_ 101
Nvvv 88,38,148,68
E_ 102
Mvvv 88,42,168
E_ 103
Nvvv 92,38,152,72
C_ 103,102,103
C_ 102,100,101
Mvvv 92,42,172
W_ 0
E_ 104
Mvvv 92,30,192
Dv 38,%0,26624
Mvvv 92,34,212
Dv 42,v1,26624
Mvvv 88,30,188
E_ 105
Mvvv 88,34,208
E_ 106
Mvvv 84,30,184
S_ s65,s61,61440
E_ 107
Mvvv 84,34,204
S_ m0,s51,24576
C_ 107,106,107
C_ 106,104,105
Mvvv 80,30,180
s_waitcnt vmcnt(18) lgkmcnt(0)
s_barrier
Mvvv 80,34,200
L_ v9,36,s65,0
Mvvv 76,30,176
E_ 108
Mvvv 76,34,196
L_ v9,36,s65,3072
S_ m0,s51,25472
Mvvv 76,38,216
Tv 30,v2,0
Tv 32,v2,6144
Mvvv 76,42,236
L_ v9,36,s65,128
Mvvv 80,38,220
s_nop 0
Tv 34,v3,0
Tv 36,v3,6144
Mvvv 80,42,240
L_ v9,36,s65,3200
S_ m0,s51,26368
Mvvv 84,38,224
E_ 109
Mvvv 84,42,244
L_ v9,36,s65,256
Mvvv 88,38,228
E_ 110
Mvvv 88,42,248
L_ v9,36,s65,3328
Mvvv 92,38,232
E_ 111
Mvvv 92,42,252
W_ 0
C_ 111,110,111
C_ 110,108,109
Mavv 0,30,116
s_nop 0
Tv 38,v4,0
Tv 40,v4,6144
Mavv 20,34,116
E_ 112
Mavv 24,34,120
s_nop 0
Tv 42,v5,0
Tv 44,v5,6144
Mavv 4,30,120
E_ 113
Mavv 8,30,124
E_ 114
Mavv 28,34,124
E_ 115
Mavv 32,34,128
C_ 115,114,115
C_ 114,112,113
Mavv 12,30,128
E_ 76
Mavv 16,30,132
E_ 77
Mavv 36,34,132
W_ 0
E_ 78
Mavv 56,38,132
s_nop 0
Tv 30,v2,1024
Tv 32,v2,7168
Mavv 76,42,132
E_ 79
Mavv 72,42,128
s_nop 0
Tv 34,v3,1024
Tv 36,v3,7168
Mavv 52,38,128
C_ 96,76,77
C_ 97,78,79
Mavv 48,38,124
E_ 80
Mavv 68,42,124
E_ 81
Mavv 64,42,120
E_ 82
Mavv 44,38,120
E_ 83
Mavv 40,38,116
C_ 100,80,81
C_ 101,82,83
Mavv 60,42,116
W_ 0
E_ 84
Mavv 80,30,116
s_nop 0
Tv 38,v4,1024
Tv 40,v4,7168
Mavv 100,34,116
E_ 85
Mavv 104,34,120
s_nop 0
Tv 42,v5,1024
Tv 44,v5,7168
Mavv 84,30,120
E_ 86
Mavv 88,30,124
E_ 87
Mavv 108,34,124
C_ 104,84,85
C_ 105,86,87
Mavv 112,34,128
E_ 88
Mavv 92,30,128
E_ 89
Mavv 96,30,132
E_ 90
Mavv 116,34,132
W_ 0
E_ 91
Mavv 136,38,132
s_nop 0
Tv 30,v2,2048
Tv 32,v2,8192
Mavv 156,42,132
C_ 108,88,89
C_ 109,90,91
Mavv 152,42,128
s_nop 0
Tv 34,v3,2048
Tv 36,v3,8192
Mavv 132,38,128
E_ 92
Mavv 128,38,124
E_ 93
Mavv 148,42,124
E_ 94
Mavv 144,42,120
E_ 95
Mavv 124,38,120
C_ 112,92,93
C_ 113,94,95
Mavv 120,38,116
v_mov_b32 v92, 0x3f803f80
v_mov_b32 v93, 0x3f803f80
Mavv 140,42,116
W_ 0
v_mov_b32 v94, 0x3f803f80
v_mov_b32 v95, 0x3f803f80
Mavv 160,30,116
s_nop 0
Tv 38,v4,2048
Tv 40,v4,8192
Mavv 180,34,116
Mavv 184,34,120
s_nop 0
Tv 42,v5,2048
Tv 44,v5,8192
Mavv 164,30,120
Mavv 168,30,124
Mavv 188,34,124
S_ s65,s61,86016
Mavv 192,34,128
S_ m0,s51,0
Mavv 172,30,128
s_waitcnt vmcnt(18) lgkmcnt(0)
s_barrier
Mavv 176,30,132
Ln v7,32,s65,0
Mavv 196,34,132
Mavv 216,38,132
Dv 30,%0,18432
Mavv 236,42,132
Dv 34,v1,18432
Mavv 232,42,128
Ln v8,32,s65,3072
S_ m0,s51,896
Mavv 212,38,128
Mavv 208,38,124
Ln v7,32,s65,128
Mavv 228,42,124
Mavv 224,42,120
Ln v8,32,s65,3200
S_ m0,s51,1792
Mavv 204,38,120
Mavv 200,38,116
Ln v7,32,s65,256
Mavv 220,42,116
.Lstep0_5:
Zvvv 76,92,96
Dv 38,%0,19456
Zvvv 80,92,100
Dv 42,v1,19456
Zvvv 84,92,104
Zvvv 88,92,108
Zvvv 92,92,112
W_ 2
Nvvv 116,30,136,56
Mvvv 116,34,156
Nvvv 120,30,140,60
s_nop 1
v_max3_f32 v10, v76, v80, v84
s_nop 0
v_max3_f32 v10, v10, v88, v92
Mvvv 120,34,160
v_cmp_lt_f32 vcc, 0x42000000, v10
s_cbranch_vccnz .Ltramp0_4
Nvvv 124,30,144,64
A_ v46,v46,v76
A_ v47,v47,v80
Mvvv 124,34,164
A_ v48,v48,v84
A_ v49,v49,v88
Nvvv 128,30,148,68
A_ v50,v50,v92
Mvvv 128,34,168
Ln v8,32,s65,3328
Nvvv 132,30,152,72
Mvvv 132,34,172
W_ 0
Mvvv 132,38,192
Dv 30,%0,20480
Mvvv 132,42,212
Dv 34,v1,20480
Mvvv 128,38,188
Mvvv 128,42,208
Mvvv 124,38,184
Mvvv 124,42,204
Mvvv 120,38,180
Mvvv 120,42,200
Mvvv 116,38,176
Mvvv 116,42,196
W_ 0
Mvvv 116,30,216
Dv 38,%0,12288
Mvvv 116,34,236
Dv 42,v1,12288
Mvvv 120,30,220
Mvvv 120,34,240
Mvvv 124,30,224
Mvvv 124,34,244
Mvvv 128,30,228
Mvvv 128,34,248
s_nop 0
E_ 116
Mvvv 132,30,232
E_ 117
Mvvv 132,34,252
W_ 0
E_ 118
Nvvv 76,38,136,56
Dv 30,%0,13312
Mvvv 76,42,156
Dv 34,v1,13312
Nvvv 80,38,140,60
E_ 119
Mvvv 80,42,160
C_ 119,118,119
C_ 118,116,117
Nvvv 84,38,144,64
E_ 120
Mvvv 84,42,164
E_ 121
Nvvv 88,38,148,68
E_ 122
Mvvv 88,42,168
E_ 123
Nvvv 92,38,152,72
C_ 123,122,123
C_ 122,120,121
Mvvv 92,42,172
W_ 0
E_ 124
Mvvv 92,30,192
Dv 38,%0,14336
Mvvv 92,34,212
Dv 42,v1,14336
Mvvv 88,30,188
E_ 125
Mvvv 88,34,208
E_ 126
Mvvv 84,30,184
S_ s65,s61,73728
E_ 127
Mvvv 84,34,204
S_ m0,s51,12288
C_ 127,126,127
C_ 126,124,125
Mvvv 80,30,180
s_waitcnt vmcnt(18) lgkmcnt(0)
s_barrier
Mvvv 80,34,200
L_ v9,36,s65,0
Mvvv 76,30,176
E_ 128
Mvvv 76,34,196
L_ v9,36,s65,3072
S_ m0,s51,13184
Mvvv 76,38,216
Tv 30,v2,36864
Tv 32,v2,43008
Mvvv 76,42,236
L_ v9,36,s65,128
Mvvv 80,38,220
s_nop 0
Tv 34,v3,36864
Tv 36,v3,43008
Mvvv 80,42,240
L_ v9,36,s65,3200
S_ m0,s51,14080
Mvvv 84,38,224
E_ 129
Mvvv 84,42,244
L_ v9,36,s65,256
Mvvv 88,38,228
E_ 130
Mvvv 88,42,248
L_ v9,36,s65,3328
Mvvv 92,38,232
E_ 131
Mvvv 92,42,252
W_ 0
C_ 131,130,131
C_ 130,128,129
Mavv 0,30,96
s_nop 0
Tv 38,v4,36864
Tv 40,v4,43008
Mavv 20,34,96
E_ 132
Mavv 24,34,100
s_nop 0
Tv 42,v5,36864
Tv 44,v5,43008
Mavv 4,30,100
E_ 133
Mavv 8,30,104
E_ 134
Mavv 28,34,104
E_ 135
Mavv 32,34,108
C_ 135,134,135
C_ 134,132,133
Mavv 12,30,108
E_ 76
Mavv 16,30,112
E_ 77
Mavv 36,34,112
W_ 0
E_ 78
Mavv 56,38,112
s_nop 0
Tv 30,v2,37888
Tv 32,v2,44032
Mavv 76,42,112
)A"
R"A(E_ 79
Mavv 72,42,108
s_nop 0
Tv 34,v3,37888
Tv 36,v3,44032
Mavv 52,38,108
C_ 116,76,77
C_ 117,78,79
Mavv 48,38,104
E_ 80
Mavv 68,42,104
E_ 81
Mavv 64,42,100
E_ 82
Mavv 44,38,100
E_ 83
Mavv 40,38,96
C_ 120,80,81
C_ 121,82,83
Mavv 60,42,96
W_ 0
E_ 84
Mavv 80,30,96
s_nop 0
Tv 38,v4,37888
Tv 40,v4,44032
Mavv 100,34,96
E_ 85
Mavv 104,34,100
s_nop 0
Tv 42,v5,37888
Tv 44,v5,44032
Mavv 84,30,100
E_ 86
Mavv 88,30,104
E_ 87
Mavv 108,34,104
C_ 124,84,85
C_ 125,86,87
Mavv 112,34,108
E_ 88
Mavv 92,30,108
E_ 89
Mavv 96,30,112
E_ 90
Mavv 116,34,112
W_ 0
E_ 91
Mavv 136,38,112
s_nop 0
Tv 30,v2,38912
Tv 32,v2,45056
Mavv 156,42,112
C_ 128,88,89
C_ 129,90,91
Mavv 152,42,108
s_nop 0
Tv 34,v3,38912
Tv 36,v3,45056
Mavv 132,38,108
E_ 92
Mavv 128,38,104
E_ 93
Mavv 148,42,104
E_ 94
Mavv 144,42,100
E_ 95
Mavv 124,38,100
C_ 132,92,93
C_ 133,94,95
Mavv 120,38,96
v_mov_b32 v92, 0x3f803f80
v_mov_b32 v93, 0x3f803f80
Mavv 140,42,96
W_ 0
v_mov_b32 v94, 0x3f803f80
v_mov_b32 v95, 0x3f803f80
Mavv 160,30,96
s_nop 0
Tv 38,v4,38912
Tv 40,v4,45056
Mavv 180,34,96
Mavv 184,34,100
s_nop 0
Tv 42,v5,38912
Tv 44,v5,45056
Mavv 164,30,100
Mavv 168,30,104
Mavv 188,34,104
S_ s65,s61,98304
Mavv 192,34,108
S_ m0,s51,36864
Mavv 172,30,108
s_waitcnt vmcnt(18) lgkmcnt(0)
s_barrier
Mavv 176,30,112
Ln v7,32,s65,0
Mavv 196,34,112
Mavv 216,38,112
Dv 30,%0,55296
Mavv 236,42,112
Dv 34,v1,55296
Mavv 232,42,108
Ln v8,32,s65,3072
S_ m0,s51,37760
Mavv 212,38,108
Mavv 208,38,104
Ln v7,32,s65,128
Mavv 228,42,104
Mavv 224,42,100
Ln v8,32,s65,3200
S_ m0,s51,38656
Mavv 204,38,100
Mavv 200,38,96
Ln v7,32,s65,256
Mavv 220,42,96
.Lstep0_6:
Zvvv 76,92,116
Dv 38,%0,56320
Zvvv 80,92,120
Dv 42,v1,56320
Zvvv 84,92,124
Zvvv 88,92,128
Zvvv 92,92,132
W_ 2
Nvvv 96,30,136,56
Mvvv 96,34,156
Nvvv 100,30,140,60
s_nop 1
v_max3_f32 v10, v76, v80, v84
s_nop 0
v_max3_f32 v10, v10, v88, v92
Mvvv 100,34,160
v_cmp_lt_f32 vcc, 0x42000000, v10
s_cbranch_vccnz .Ltramp0_5
Nvvv 104,30,144,64
A_ v46,v46,v76
A_ v47,v47,v80
Mvvv 104,34,164
A_ v48,v48,v84
A_ v49,v49,v88
Nvvv 108,30,148,68
A_ v50,v50,v92
Mvvv 108,34,168
Ln v8,32,s65,3328
Nvvv 112,30,152,72
R_ v11,a248
Mvvv 112,34,172
W_ 0
v_lshlrev_b32 v11, 10, v11
Mvvv 112,38,192
Dv 30,%0,57344
Mvvv 112,42,212
Dv 34,v1,57344
Mvvv 108,38,188
buffer_load_dword a254, v11, s[44:47], s66 offen
Mvvv 108,42,208
Mvvv 104,38,184
Mvvv 104,42,204
Mvvv 100,38,180
Mvvv 100,42,200
Mvvv 96,38,176
Mvvv 96,42,196
W_ 0
Mvvv 96,30,216
Dv 38,%0,49152
Mvvv 96,34,236
Dv 42,v1,49152
Mvvv 100,30,220
Mvvv 100,34,240
Mvvv 104,30,224
Mvvv 104,34,244
Mvvv 108,30,228
Mvvv 108,34,248
s_nop 0
E_ 96
Mvvv 112,30,232
E_ 97
Mvvv 112,34,252
W_ 0
E_ 98
Nvvv 76,38,136,56
Dv 30,%0,50176
Mvvv 76,42,156
Dv 34,v1,50176
Nvvv 80,38,140,60
E_ 99
Mvvv 80,42,160
C_ 99,98,99
C_ 98,96,97
Nvvv 84,38,144,64
E_ 100
Mvvv 84,42,164
E_ 101
Nvvv 88,38,148,68
E_ 102
Mvvv 88,42,168
E_ 103
Nvvv 92,38,152,72
C_ 103,102,103
C_ 102,100,101
Mvvv 92,42,172
W_ 0
E_ 104
Mvvv 92,30,192
Dv 38,%0,51200
Mvvv 92,34,212
Dv 42,v1,51200
Mvvv 88,30,188
E_ 105
Mvvv 88,34,208
E_ 106
Mvvv 84,30,184
S_ s65,s61,86016
E_ 107
Mvvv 84,34,204
S_ m0,s51,49152
C_ 107,106,107
C_ 106,104,105
Mvvv 80,30,180
s_waitcnt vmcnt(19) lgkmcnt(0)
s_barrier
Mvvv 80,34,200
L_ v9,36,s65,0
Mvvv 76,30,176
E_ 108
Mvvv 76,34,196
L_ v9,36,s65,3072
S_ m0,s51,50048
Mvvv 76,38,216
Tv 30,v2,24576
Tv 32,v2,30720
Mvvv 76,42,236
L_ v9,36,s65,128
Mvvv 80,38,220
s_nop 0
Tv 34,v3,24576
Tv 36,v3,30720
Mvvv 80,42,240
L_ v9,36,s65,3200
S_ m0,s51,50944
Mvvv 84,38,224
E_ 109
Mvvv 84,42,244
L_ v9,36,s65,256
Mvvv 88,38,228
E_ 110
Mvvv 88,42,248
L_ v9,36,s65,3328
Mvvv 92,38,232
E_ 111
Mvvv 92,42,252
W_ 0
C_ 111,110,111
C_ 110,108,109
Mavv 0,30,116
s_nop 0
Tv 38,v4,24576
Tv 40,v4,30720
Mavv 20,34,116
E_ 112
Mavv 24,34,120
s_nop 0
Tv 42,v5,24576
Tv 44,v5,30720
Mavv 4,30,120
E_ 113
Mavv 8,30,124
E_ 114
Mavv 28,34,124
E_ 115
Mavv 32,34,128
C_ 115,114,115
C_ 114,112,113
Mavv 12,30,128
E_ 76
Mavv 16,30,132
E_ 77
Mavv 36,34,132
W_ 0
E_ 78
Mavv 56,38,132
s_nop 0
Tv 30,v2,25600
Tv 32,v2,31744
Mavv 76,42,132
E_ 79
Mavv 72,42,128
s_nop 0
Tv 34,v3,25600
Tv 36,v3,31744
Mavv 52,38,128
C_ 96,76,77
C_ 97,78,79
Mavv 48,38,124
E_ 80
Mavv 68,42,124
E_ 81
Mavv 64,42,120
E_ 82
Mavv 44,38,120
E_ 83
Mavv 40,38,116
C_ 100,80,81
C_ 101,82,83
Mavv 60,42,116
W_ 0
E_ 84
Mavv 80,30,116
s_nop 0
Tv 38,v4,25600
Tv 40,v4,31744
Mavv 100,34,116
E_ 85
Mavv 104,34,120
s_nop 0
Tv 42,v5,25600
Tv 44,v5,31744
Mavv 84,30,120
E_ 86
Mavv 88,30,124
E_ 87
Mavv 108,34,124
C_ 104,84,85
C_ 105,86,87
Mavv 112,34,128
E_ 88
Mavv 92,30,128
E_ 89
Mavv 96,30,132
E_ 90
Mavv 116,34,132
W_ 0
E_ 91
Mavv 136,38,132
s_nop 0
Tv 30,v2,26624
Tv 32,v2,32768
Mavv 156,42,132
C_ 108,88,89
C_ 109,90,91
Mavv 152,42,128
s_nop 0
Tv 34,v3,26624
Tv 36,v3,32768
Mavv 132,38,128
E_ 92
Mavv 128,38,124
E_ 93
Mavv 148,42,124
E_ 94
Mavv 144,42,120
E_ 95
Mavv 124,38,120
C_ 112,92,93
C_ 113,94,95
Mavv 120,38,116
v_mov_b32 v92, 0x3f803f80
v_mov_b32 v93, 0x3f803f80
Mavv 140,42,116
W_ 0
v_mov_b32 v94, 0x3f803f80
v_mov_b32 v95, 0x3f803f80
Mavv 160,30,116
s_nop 0
Tv 38,v4,26624
Tv 40,v4,32768
Mavv 180,34,116
Mavv 184,34,120
s_nop 0
Tv 42,v5,26624
Tv 44,v5,32768
Mavv 164,30,120
Mavv 168,30,124
Mavv 188,34,124
S_ s65,s61,110592
Mavv 192,34,128
S_ m0,s51,24576
Mavv 172,30,128
s_waitcnt vmcnt(19) lgkmcnt(0)
s_barrier
Mavv 176,30,132
Ln v7,32,s65,0
Mavv 196,34,132
Mavv 216,38,132
Dv 30,%0,6144
Mavv 236,42,132
Dv 34,v1,6144
Mavv 232,42,128
Ln v8,32,s65,3072
S_ m0,s51,25472
Mavv 212,38,128
Mavv 208,38,124
Ln v7,32,s65,128
Mavv 228,42,124
Mavv 224,42,120
Ln v8,32,s65,3200
S_ m0,s51,26368
Mavv 204,38,120
Mavv 200,38,116
Ln v7,32,s65,256
Mavv 220,42,116
s_cmp_eq_u32 s4, 0
s_cbranch_scc1 .Lhs_c0
.Lhs_c0_back:
.Lstep0_7:
Zvvv 76,92,96
Dv 38,%0,7168
Zvvv 80,92,100
Dv 42,v1,7168
Zvvv 84,92,104
Zvvv 88,92,108
Zvvv 92,92,112
W_ 2
Nvvv 116,30,136,56
Mvvv 116,34,156
Nvvv 120,30,140,60
s_nop 1
v_max3_f32 v10, v76, v80, v84
s_nop 0
v_max3_f32 v10, v10, v88, v92
Mvvv 120,34,160
v_cmp_lt_f32 vcc, 0x42000000, v10
s_cbranch_vccnz .Ltramp0_6
Nvvv 124,30,144,64
A_ v46,v46,v76
A_ v47,v47,v80
Mvvv 124,34,164
A_ v48,v48,v84
A_ v49,v49,v88
Nvvv 128,30,148,68
A_ v50,v50,v92
Mvvv 128,34,168
Ln v8,32,s65,3328
Nvvv 132,30,152,72
Mvvv 132,34,172
W_ 0
Mvvv 132,38,192
Dv 30,%0,8192
Mvvv 132,42,212
Dv 34,v1,8192
Mvvv 128,38,188
Mvvv 128,42,208
Mvvv 124,38,184
Mvvv 124,42,204
Mvvv 120,38,180
Mvvv 120,42,200
Mvvv 116,38,176
Mvvv 116,42,196
W_ 0
Mvvv 116,30,216
Dv 38,%0,0
Mvvv 116,34,236
Dv 42,v1,0
Mvvv 120,30,220
Mvvv 120,34,240
Mvvv 124,30,224
Mvvv 124,34,244
Mvvv 128,30,228
Mvvv 128,34,248
s_nop 0
E_ 116
Mvvv 132,30,232
E_ 117
Mvvv 132,34,252
W_ 0
E_ 118
Nvvv 76,38,136,56
Dv 30,%0,1024
Mvvv 76,42,156
Dv 34,v1,1024
Nvvv 80,38,140,60
E_ 119
Mvvv 80,42,160
C_ 119,118,119
C_ 118,116,117
Nvvv 84,38,144,64
E_ 120
Mvvv 84,42,164
E_ 121
Nvvv 88,38,148,68
E_ 122
Mvvv 88,42,168
E_ 123
Nvvv 92,38,152,72
C_ 123,122,123
C_ 122,120,121
Mvvv 92,42,172
W_ 0
E_ 124
Mvvv 92,30,192
Dv 38,%0,2048
Mvvv 92,34,212
Dv 42,v1,2048
Mvvv 88,30,188
E_ 125
Mvvv 88,34,208
E_ 126
Mvvv 84,30,184
S_ s65,s61,98304
E_ 127
Mvvv 84,34,204
S_ m0,s51,0
C_ 127,126,127
C_ 126,124,125
Mvvv 80,30,180
s_waitcnt vmcnt(19) lgkmcnt(0)
s_barrier
Mvvv 80,34,200
L_ v9,36,s65,0
Mvvv 76,30,176
E_ 128
Mvvv 76,34,196
L_ v9,36,s65,3072
S_ m0,s51,896
Mvvv 76,38,216
Tv 30,v2,12288
Tv 32,v2,18432
Mvvv 76,42,236
L_ v9,36,s65,128
Mvvv 80,38,220
s_nop 0
Tv 34,v3,12288
Tv 36,v3,18432
Mvvv 80,42,240
L_ v9,36,s65,3200
S_ m0,s51,1792
Mvvv 84,38,224
E_ 129
Mvvv 84,42,244
L_ v9,36,s65,256
Mvvv 88,38,228
E_ 130
Mvvv 88,42,248
L_ v9,36,s65,3328
Mvvv 92,38,232
E_ 131
Mvvv 92,42,252
W_ 0
C_ 131,130,131
C_ 130,128,129
Mavv 0,30,96
s_nop 0
Tv 38,v4,12288
Tv 40,v4,18432
Mavv 20,34,96
E_ 132
Mavv 24,34,100
s_nop 0
Tv 42,v5,12288
Tv 44,v5,18432
Mavv 4,30,100
E_ 133
Mavv 8,30,104
E_ 134
Mavv 28,34,104
E_ 135
Mavv 32,34,108
C_ 135,134,135
C_ 134,132,133
Mavv 12,30,108
E_ 76
Mavv 16,30,112
E_ 77
Mavv 36,34,112
W_ 0
E_ 78
Mavv 56,38,112
s_nop 0
Tv 30,v2,13312
Tv 32,v2,19456
Mavv 76,42,112
E_ 79
Mavv 72,42,108
s_nop 0
Tv 34,v3,13312
Tv 36,v3,19456
Mavv 52,38,108
C_ 116,76,77
C_ 117,78,79
Mavv 48,38,104
E_ 80
Mavv 68,42,104
E_ 81
Mavv 64,42,100
E_ 82
Mavv 44,38,100
E_ 83
Mavv 40,38,96
C_ 120,80,81
C_ 121,82,83
Mavv 60,42,96
W_ 0
E_ 84
Mavv 80,30,96
s_nop 0
Tv 38,v4,13312
Tv 40,v4,19456
Mavv 100,34,96
E_ 85
Mavv 104,34,100
s_nop 0
Tv 42,v5,13312
Tv 44,v5,19456
Mavv 84,30,100
E_ 86
Mavv 88,30,104
E_ 87
Mavv 108,34,104
C_ 124,84,85
C_ 125,86,87
Mavv 112,34,108
E_ 88
Mavv 92,30,108
E_ 89
Mavv 96,30,112
E_ 90
Mavv 116,34,112
W_ 0
E_ 91
Mavv 136,38,112
s_nop 0
Tv 30,v2,14336
Tv 32,v2,20480
Mavv 156,42,112
C_ 128,88,89
C_ 129,90,91
Mavv 152,42,108
s_nop 0
Tv 34,v3,14336
Tv 36,v3,20480
Mavv 132,38,108
E_ 92
Mavv 128,38,104
E_ 93
Mavv 148,42,104
E_ 94
Mavv 144,42,100
E_ 95
Mavv 124,38,100
C_ 132,92,93
C_ 133,94,95
Mavv 120,38,96
v_mov_b32 v92, 0x3f803f80
v_mov_b32 v93, 0x3f803f80
Mavv 140,42,96
W_ 0
v_mov_b32 v94, 0x3f803f80
v_mov_b32 v95, 0x3f803f80
Mavv 160,30,96
s_nop 0
Tv 38,v4,14336
Tv 40,v4,20480
Mavv 180,34,96
Mavv 184,34,100
s_nop 0
Tv 42,v5,14336
Tv 44,v5,20480
Mavv 164,30,100
Mavv 168,30,104
Mavv 188,34,104
S_ s65,s61,122880
Mavv 192,34,108
S_ m0,s51,12288
Mavv 172,30,108
s_waitcnt vmcnt(19) lgkmcnt(0)
s_barrier
Mavv 176,30,112
Ln v7,32,s65,0
Mavv 196,34,112
Mavv 216,38,112
Dv 30,%0,43008
Mavv 236,42,112
Dv 34,v1,43008
Mavv 232,42,108
Ln v8,32,s65,3072
S_ m0,s51,13184
Mavv 212,38,108
Mavv 208,38,104
Ln v7,32,s65,128
Mavv 228,42,104
Mavv 224,42,100
Ln v8,32,s65,3200
S_ m0,s51,14080
Mavv 204,38,100
Mavv 200,38,96
Ln v7,32,s65,256
Mavv 220,42,96
.Lstep0_8:
Zvvv 76,92,116
Dv 38,%0,44032
Zvvv 80,92,120
Dv 42,v1,44032
Zvvv 84,92,124
Zvvv 88,92,128
Zvvv 92,92,132
W_ 2
Nvvv 96,30,136,56
Mvvv 96,34,156
Nvvv 100,30,140,60
s_nop 1
v_max3_f32 v10, v76, v80, v84
s_nop 0
v_max3_f32 v10, v10, v88, v92
Mvvv 100,34,160
v_cmp_lt_f32 vcc, 0x42000000, v10
s_cbranch_vccnz .Ltramp0_7
Nvvv 104,30,144,64
A_ v46,v46,v76
A_ v47,v47,v80
Mvvv 104,34,164
A_ v48,v48,v84
A_ v49,v49,v88
Nvvv 108,30,148,68
A_ v50,v50,v92
Mvvv 108,34,168
Ln v8,32,s65,3328
Nvvv 112,30,152,72
Mvvv 112,34,172
W_ 0
Mvvv 112,38,192
Dv 30,%0,45056
Mvvv 112,42,212
Dv 34,v1,45056
Mvvv 108,38,188
Mvvv 108,42,208
Mvvv 104,38,184
Mvvv 104,42,204
Mvvv 100,38,180
Mvvv 100,42,200
Mvvv 96,38,176
Mvvv 96,42,196
W_ 0
Mvvv 96,30,216
Dv 38,%0,36864
Mvvv 96,34,236
Dv 42,v1,36864
Mvvv 100,30,220
Mvvv 100,34,240
Mvvv 104,30,224
Mvvv 104,34,244
Mvvv 108,30,228
Mvvv 108,34,248
s_nop 0
E_ 96
Mvvv 112,30,232
E_ 97
Mvvv 112,34,252
W_ 0
E_ 98
Nvvv 76,38,136,56
Dv 30,%0,37888
Mvvv 76,42,156
Dv 34,v1,37888
Nvvv 80,38,140,60
E_ 99
Mvvv 80,42,160
C_ 99,98,99
C_ 98,96,97
Nvvv 84,38,144,64
E_ 100
Mvvv 84,42,164
E_ 101
Nvvv 88,38,148,68
E_ 102
Mvvv 88,42,168
E_ 103
Nvvv 92,38,152,72
C_ 103,102,103
C_ 102,100,101
Mvvv 92,42,172
W_ 0
E_ 104
Mvvv 92,30,192
Dv 38,%0,38912
Mvvv 92,34,212
Dv 42,v1,38912
Mvvv 88,30,188
E_ 105
Mvvv 88,34,208
E_ 106
Mvvv 84,30,184
S_ s65,s61,110592
E_ 107
Mvvv 84,34,204
S_ m0,s51,36864
C_ 107,106,107
C_ 106,104,105
Mvvv 80,30,180
s_waitcnt vmcnt(18) lgkmcnt(0)
s_barrier
Mvvv 80,34,200
L_ v9,36,s65,0
Mvvv 76,30,176
E_ 108
Mvvv 76,34,196
L_ v9,36,s65,3072
S_ m0,s51,37760
Mvvv 76,38,216
Tv 30,v2,49152
Tv 32,v2,55296
Mvvv 76,42,236
L_ v9,36,s65,128
Mvvv 80,38,220
s_nop 0
Tv 34,v3,49152
Tv 36,v3,55296
Mvvv 80,42,240
L_ v9,36,s65,3200
S_ m0,s51,38656
Mvvv 84,38,224
E_ 109
Mvvv 84,42,244
L_ v9,36,s65,256
Mvvv 88,38,228
E_ 110
Mvvv 88,42,248
L_ v9,36,s65,3328
Mvvv 92,38,232
E_ 111
Mvvv 92,42,252
W_ 0
C_ 111,110,111
C_ 110,108,109
Mavv 0,30,116
s_nop 0
Tv 38,v4,49152
Tv 40,v4,55296
Mavv 20,34,116
E_ 112
Mavv 24,34,120
s_nop 0
Tv 42,v5,49152
Tv 44,v5,55296
Mavv 4,30,120
E_ 113
Mavv 8,30,124
E_ 114
Mavv 28,34,124
E_ 115
Mavv 32,34,128
C_ 115,114,115
C_ 114,112,113
Mavv 12,30,128
E_ 76
Mavv 16,30,132
E_ 77
Mavv 36,34,132
W_ 0
E_ 78
Mavv 56,38,132
s_nop 0
Tv 30,v2,50176
Tv 32,v2,56320
Mavv 76,42,132
E_ 79
Mavv 72,42,128
s_nop 0
Tv 34,v3,50176
Tv 36,v3,56320
Mavv 52,38,128
C_ 96,76,77
C_ 97,78,79
Mavv 48,38,124
E_ 80
Mavv 68,42,124
E_ 81
Mavv 64,42,120
E_ 82
Mavv 44,38,120
E_ 83
Mavv 40,38,116
C_ 100,80,81
C_ 101,82,83
Mavv 60,42,116
W_ 0
E_ 84
Mavv 80,30,116
s_nop 0
Tv 38,v4,50176
Tv 40,v4,56320
Mavv 100,34,116
E_ 85
Mavv 104,34,120
s_nop 0
Tv 42,v5,50176
Tv 44,v5,56320
Mavv 84,30,120
E_ 86
Mavv 88,30,124
E_ 87
Mavv 108,34,124
C_ 104,84,85
C_ 105,86,87
Mavv 112,34,128
E_ 88
Mavv 92,30,128
E_ 89
Mavv 96,30,132
E_ 90
Mavv 116,34,132
W_ 0
E_ 91
Mavv 136,38,132
s_nop 0
Tv 30,v2,51200
Tv 32,v2,57344
Mavv 156,42,132
C_ 108,88,89
C_ 109,90,91
Mavv 152,42,128
s_nop 0
Tv 34,v3,51200
Tv 36,v3,57344
Mavv 132,38,128
E_ 92
Mavv 128,38,124
E_ 93
Mavv 148,42,124
E_ 94
Mavv 144,42,120
E_ 95
Mavv 124,38,120
C_ 112,92,93
C_ 113,94,95
Mavv 120,38,116
v_mov_b32 v92, 0x3f803f80
v_mov_b32 v93, 0x3f803f80
Mavv 140,42,116
W_ 0
v_mov_b32 v94, 0x3f803f80
v_mov_b32 v95, 0x3f803f80
Mavv 160,30,116
s_nop 0
Tv 38,v4,51200
Tv 40,v4,57344
Mavv 180,34,116
Mavv 184,34,120
s_nop 0
Tv 42,v5,51200
Tv 44,v5,57344
Mavv 164,30,120
Mavv 168,30,124
Mavv 188,34,124
S_ s65,s61,135168
Mavv 192,34,128
S_ m0,s51,49152
Mavv 172,30,128
s_waitcnt vmcnt(18) lgkmcnt(0)
s_barrier
Mavv 176,30,132
Ln v7,32,s65,0
Mavv 196,34,132
Mavv 216,38,132
Dv 30,%0,30720
Mavv 236,42,132
Dv 34,v1,30720
Mavv 232,42,128
Ln v8,32,s65,3072
S_ m0,s51,50048
Mavv 212,38,128
Mavv 208,38,124
Ln v7,32,s65,128
Mavv 228,42,124
Mavv 224,42,120
Ln v8,32,s65,3200
S_ m0,s51,50944
Mavv 204,38,120
Mavv 200,38,116
Ln v7,32,s65,256
Mavv 220,42,116
s_cmp_eq_u32 s4, 0
s_cbranch_scc1 .Lhs_d0
.Lhs_d0_back:
.Lstep0_9:
Zvvv 76,92,96
Dv 38,%0,31744
Zvvv 80,92,100
Dv 42,v1,31744
Zvvv 84,92,104
Zvvv 88,92,108
Zvvv 92,92,112
W_ 2
Nvvv 116,30,136,56
Mvvv 116,34,156
Nvvv 120,30,140,60
s_nop 1
v_max3_f32 v10, v76, v80, v84
s_nop 0
v_max3_f32 v10, v10, v88, v92
Mvvv 120,34,160
v_cmp_lt_f32 vcc, 0x42000000, v10
s_cbranch_vccnz .Ltramp0_8
Nvvv 124,30,144,64
A_ v46,v46,v76
A_ v47,v47,v80
Mvvv 124,34,164
A_ v48,v48,v84
A_ v49,v49,v88
Nvvv 128,30,148,68
A_ v50,v50,v92
Mvvv 128,34,168
Ln v8,32,s65,3328
Nvvv 132,30,152,72
Mvvv 132,34,172
W_ 0
Mvvv 132,38,192
Dv 30,%0,32768
Mvvv 132,42,212
Dv 34,v1,32768
Mvvv 128,38,188
Mvvv 128,42,208
Mvvv 124,38,184
Mvvv 124,42,204
Mvvv 120,38,180
Mvvv 120,42,200
Mvvv 116,38,176
Mvvv 116,42,196
W_ 0
Mvvv 116,30,216
Dv 38,%0,24576
Mvvv 116,34,236
Dv 42,v1,24576
Mvvv 120,30,220
Mvvv 120,34,240
Mvvv 124,30,224
Mvvv 124,34,244
Mvvv 128,30,228
Mvvv 128,34,248
s_nop 0
E_ 116
Mvvv 132,30,232
E_ 117
Mvvv 132,34,252
W_ 0
E_ 118
Nvvv 76,38,136,56
Dv 30,%0,25600
Mvvv 76,42,156
Dv 34,v1,25600
Nvvv 80,38,140,60
E_ 119
Mvvv 80,42,160
C_ 119,118,119
C_ 118,116,117
Nvvv 84,38,144,64
E_ 120
Mvvv 84,42,164
E_ 121
Nvvv 88,38,148,68
E_ 122
Mvvv 88,42,168
E_ 123
Nvvv 92,38,152,72
C_ 123,122,123
C_ 122,120,121
Mvvv 92,42,172
W_ 0
E_ 124
Mvvv 92,30,192
Dv 38,%0,26624
Mvvv 92,34,212
Dv 42,v1,26624
Mvvv 88,30,188
E_ 125
Mvvv 88,34,208
E_ 126
Mvvv 84,30,184
S_ s65,s61,122880
E_ 127
Mvvv 84,34,204
S_ m0,s51,24576
C_ 127,126,127
C_ 126,124,125
Mvvv 80,30,180
s_waitcnt vmcnt(18) lgkmcnt(0)
s_barrier
Mvvv 80,34,200
L_ v9,36,s65,0
Mvvv 76,30,176
E_ 128
Mvvv 76,34,196
L_ v9,36,s65,3072
S_ m0,s51,25472
Mvvv 76,38,216
Tv 30,v2,0
Tv 32,v2,6144
Mvvv 76,42,236
L_ v9,36,s65,128
Mvvv 80,38,220
s_nop 0
Tv 34,v3,0
Tv 36,v3,6144
Mvvv 80,42,240
L_ v9,36,s65,3200
S_ m0,s51,26368
Mvvv 84,38,224
E_ 129
Mvvv 84,42,244
L_ v9,36,s65,256
Mvvv 88,38,228
E_ 130
Mvvv 88,42,248
L_ v9,36,s65,3328
Mvvv 92,38,232
E_ 131
Mvvv 92,42,252
W_ 0
C_ 131,130,131
C_ 130,128,129
Mavv 0,30,96
s_nop 0
Tv 38,v4,0
Tv 40,v4,6144
Mavv 20,34,96
E_ 132
Mavv 24,34,100
s_nop 0
Tv 42,v5,0
Tv 44,v5,6144
Mavv 4,30,100
E_ 133
Mavv 8,30,104
E_ 134
Mavv 28,34,104
E_ 135
Mavv 32,34,108
C_ 135,134,135
C_ 134,132,133
Mavv 12,30,108
E_ 76
Mavv 16,30,112
E_ 77
Mavv 36,34,112
W_ 0
E_ 78
Mavv 56,38,112
s_nop 0
Tv 30,v2,1024
Tv 32,v2,7168
Mavv 76,42,112
E_ 79
Mavv 72,42,108
s_nop 0
Tv 34,v3,1024
Tv 36,v3,7168
Mavv 52,38,108
C_ 116,76,77
C_ 117,78,79
Mavv 48,38,104
E_ 80
Mavv 68,42,104
E_ 81
Mavv 64,42,100
E_ 82
Mavv 44,38,100
E_ 83
Mavv 40,38,96
C_ 120,80,81
C_ 121,82,83
Mavv 60,42,96
W_ 0
E_ 84
Mavv 80,30,96
s_nop 0
Tv 38,v4,1024
Tv 40,v4,7168
Mavv 100,34,96
E_ 85
Mavv 104,34,100
s_nop 0
Tv 42,v5,1024
Tv 44,v5,7168
Mavv 84,30,100
E_ 86
Mavv 88,30,104
E_ 87
Mavv 108,34,104
C_ 124,84,85
C_ 125,86,87
Mavv 112,34,108
E_ 88
Mavv 92,30,108
E_ 89
Mavv 96,30,112
E_ 90
Mavv 116,34,112
W_ 0
E_ 91
Mavv 136,38,112
s_nop 0
Tv 30,v2,2048
Tv 32,v2,8192
Mavv 156,42,112
C_ 128,88,89
C_ 129,90,91
Mavv 152,42,108
s_nop 0
Tv 34,v3,2048
Tv 36,v3,8192
Mavv 132,38,108
E_ 92
Mavv 128,38,104
E_ 93
Mavv 148,42,104
E_ 94
Mavv 144,42,100
E_ 95
Mavv 124,38,100
C_ 132,92,93
C_ 133,94,95
Mavv 120,38,96
v_mov_b32 v92, 0x3f803f80
v_mov_b32 v93, 0x3f803f80
Mavv 140,42,96
W_ 0
v_mov_b32 v94, 0x3f803f80
v_mov_b32 v95, 0x3f803f80
Mavv 160,30,96
s_nop 0
Tv 38,v4,2048
Tv 40,v4,8192
Mavv 180,34,96
Mavv 184,34,100
s_nop 0
Tv 42,v5,2048
Tv 44,v5,8192
Mavv 164,30,100
Mavv 168,30,104
Mavv 188,34,104
S_ s65,s61,147456
Mavv 192,34,108
S_ m0,s51,0
Mavv 172,30,108
s_waitcnt vmcnt(18) lgkmcnt(0)
s_barrier
Mavv 176,30,112
Ln v7,32,s65,0
Mavv 196,34,112
Mavv 216,38,112
Dv 30,%0,18432
Mavv 236,42,112
Dv 34,v1,18432
Mavv 232,42,108
Ln v8,32,s65,3072
S_ m0,s51,896
Mavv 212,38,108
Mavv 208,38,104
Ln v7,32,s65,128
Mavv 228,42,104
Mavv 224,42,100
Ln v8,32,s65,3200
S_ m0,s51,1792
Mavv 204,38,100
Mavv 200,38,96
Ln v7,32,s65,256
Mavv 220,42,96
.Lstep0_10:
Zvvv 76,92,116
Dv 38,%0,19456
Zvvv 80,92,120
Dv 42,v1,19456
Zvvv 84,92,124
Zvvv 88,92,128
Zvvv 92,92,132
W_ 2
Nvvv 96,30,136,56
Mvvv 96,34,156
Nvvv 100,30,140,60
s_nop 1
v_max3_f32 v10, v76, v80, v84
s_nop 0
v_max3_f32 v10, v10, v88, v92
Mvvv 100,34,160
v_cmp_lt_f32 vcc, 0x42000000, v10
s_cbranch_vccnz .Ltramp0_9
Nvvv 104,30,144,64
A_ v46,v46,v76
A_ v47,v47,v80
Mvvv 104,34,164
A_ v48,v48,v84
A_ v49,v49,v88
Nvvv 108,30,148,68
A_ v50,v50,v92
Mvvv 108,34,168
Ln v8,32,s65,3328
Nvvv 112,30,152,72
Mvvv 112,34,172
W_ 0
Mvvv 112,38,192
Dv 30,%0,20480
Mvvv 112,42,212
Dv 34,v1,20480
Mvvv 108,38,188
Mvvv 108,42,208
Mvvv 104,38,184
Mvvv 104,42,204
Mvvv 100,38,180
Mvvv 100,42,200
Mvvv 96,38,176
Mvvv 96,42,196
W_ 0
Mvvv 96,30,216
Dv 38,%0,12288
Mvvv 96,34,236
Dv 42,v1,12288
Mvvv 100,30,220
Mvvv 100,34,240
Mvvv 104,30,224
Mvvv 104,34,244
Mvvv 108,30,228
Mvvv 108,34,248
s_nop 0
E_ 96
Mvvv 112,30,232
E_ 97
Mvvv 112,34,252
W_ 0
E_ 98
Nvvv 76,38,136,56
Dv 30,%0,13312
Mvvv 76,42,156
Dv 34,v1,13312
Nvvv 80,38,140,60
E_ 99
Mvvv 80,42,160
C_ 99,98,99
C_ 98,96,97
Nvvv 84,38,144,64
E_ 100
Mvvv 84,42,164
E_ 101
Nvvv 88,38,148,68
E_ 102
Mvvv 88,42,168
E_ 103
Nvvv 92,38,152,72
C_ 103,102,103
C_ 102,100,101
Mvvv 92,42,172
W_ 0
E_ 104
Mvvv 92,30,192
Dv 38,%0,14336
Mvvv 92,34,212
Dv 42,v1,14336
Mvvv 88,30,188
E_ 105
Mvvv 88,34,208
E_ 106
Mvvv 84,30,184
S_ s65,s61,135168
E_ 107
Mvvv 84,34,204
S_ m0,s51,12288
C_ 107,106,107
C_ 106,104,105
Mvvv 80,30,180
s_waitcnt vmcnt(18) lgkmcnt(0)
s_barrier
Mvvv 80,34,200
L_ v9,36,s65,0
Mvvv 76,30,176
E_ 108
Mvvv 76,34,196
L_ v9,36,s65,3072
S_ m0,s51,13184
Mvvv 76,38,216
Tv 30,v2,36864
Tv 32,v2,43008
Mvvv 76,42,236
L_ v9,36,s65,128
Mvvv 80,38,220
s_nop 0
Tv 34,v3,36864
Tv 36,v3,43008
Mvvv 80,42,240
L_ v9,36,s65,3200
S_ m0,s51,14080
Mvvv 84,38,224
E_ 109
Mvvv 84,42,244
L_ v9,36,s65,256
Mvvv 88,38,228
E_ 110
Mvvv 88,42,248
L_ v9,36,s65,3328
Mvvv 92,38,232
E_ 111
Mvvv 92,42,252
W_ 0
C_ 111,110,111
C_ 110,108,109
Mavv 0,30,116
s_nop 0
Tv 38,v4,36864
Tv 40,v4,43008
Mavv 20,34,116
E_ 112
Mavv 24,34,120
s_nop 0
Tv 42,v5,36864
Tv 44,v5,43008
Mavv 4,30,120
E_ 113
Mavv 8,30,124
E_ 114
Mavv 28,34,124
E_ 115
Mavv 32,34,128
C_ 115,114,115
C_ 114,112,113
Mavv 12,30,128
E_ 76
Mavv 16,30,132
E_ 77
Mavv 36,34,132
W_ 0
E_ 78
Mavv 56,38,132
s_nop 0
Tv 30,v2,37888
Tv 32,v2,44032
Mavv 76,42,132
E_ 79
Mavv 72,42,128
s_nop 0
Tv 34,v3,37888
Tv 36,v3,44032
Mavv 52,38,128
C_ 96,76,77
C_ 97,78,79
Mavv 48,38,124
E_ 80
Mavv 68,42,124
E_ 81
Mavv 64,42,120
E_ 82
Mavv 44,38,120
E_ 83
Mavv 40,38,116
C_ 100,80,81
C_ 101,82,83
Mavv 60,42,116
W_ 0
E_ 84
Mavv 80,30,116
s_nop 0
Tv 38,v4,37888
Tv 40,v4,44032
Mavv 100,34,116
E_ 85
Mavv 104,34,120
s_nop 0
Tv 42,v5,37888
Tv 44,v5,44032
Mavv 84,30,120
E_ 86
Mavv 88,30,124
E_ 87
Mavv 108,34,124
C_ 104,84,85
C_ 105,86,87
Mavv 112,34,128
E_ 88
Mavv 92,30,128
E_ 89
Mavv 96,30,132
E_ 90
Mavv 116,34,132
W_ 0
E_ 91
Mavv 136,38,132
s_nop 0
Tv 30,v2,38912
Tv 32,v2,45056
Mavv 156,42,132
C_ 108,88,89
C_ 109,90,91
Mavv 152,42,128
s_nop 0
Tv 34,v3,38912
Tv 36,v3,45056
Mavv 132,38,128
E_ 92
Mavv 128,38,124
E_ 93
Mavv 148,42,124
E_ 94
Mavv 144,42,120
E_ 95
Mavv 124,38,120
C_ 112,92,93
C_ 113,94,95
Mavv 120,38,116
v_mov_b32 v92, 0x3f803f80
v_mov_b32 v93, 0x3f803f80
Mavv 140,42,116
W_ 0
v_mov_b32 v94, 0x3f803f80
v_mov_b32 v95, 0x3f803f80
Mavv 160,30,116
s_nop 0
Tv 38,v4,38912
Tv 40,v4,45056
Mavv 180,34,116
Mavv 184,34,120
s_nop 0
Tv 42,v5,38912
Tv 44,v5,45056
Mavv 164,30,120
Mavv 168,30,124
Mavv 188,34,124
S_ s65,s61,159744
Mavv 192,34,128
S_ m0,s51,36864
Mavv 172,30,128
s_waitcnt vmcnt(18) lgkmcnt(0)
s_barrier
Mavv 176,30,132
Ln v7,32,s65,0
Mavv 196,34,132
Mavv 216,38,132
Dv 30,%0,55296
Mavv 236,42,132
Dv 34,v1,55296
Mavv 232,42,128
Ln v8,32,s65,3072
S_ m0,s51,37760
Mavv 212,38,128
Mavv 208,38,124
Ln v7,32,s65,128
Mavv 228,42,124
Mavv 224,42,120
Ln v8,32,s65,3200
S_ m0,s51,38656
Mavv 204,38,120
Mavv 200,38,116
Ln v7,32,s65,256
Mavv 220,42,116
.Lstep0_11:
Zvvv 76,92,96
Dv 38,%0,56320
Zvvv 80,92,100
Dv 42,v1,56320
Zvvv 84,92,104
Zvvv 88,92,108
Zvvv 92,92,112
W_ 2
Nvvv 116,30,136,56
Mvvv 116,34,156
Nvvv 120,30,140,60
s_nop 1
v_max3_f32 v10, v76, v80, v84
s_nop 0
v_max3_f32 v10, v10, v88, v92
Mvvv 120,34,160
v_cmp_lt_f32 vcc, 0x42000000, v10
s_cbranch_vccnz .Ltramp0_10
Nvvv 124,30,144,64
A_ v46,v46,v76
A_ v47,v47,v80
Mvvv 124,34,164
A_ v48,v48,v84
A_ v49,v49,v88
Nvvv 128,30,148,68
A_ v50,v50,v92
Mvvv 128,34,168
Ln v8,32,s65,3328
Nvvv 132,30,152,72
Mvvv 132,34,172
W_ 0
Mvvv 132,38,192
Dv 30,%0,57344
Mvvv 132,42,212
Dv 34,v1,57344
Mvvv 128,38,188
Mvvv 128,42,208
Mvvv 124,38,184
Mvvv 124,42,204
Mvvv 120,38,180
Mvvv 120,42,200
Mvvv 116,38,176
Mvvv 116,42,196
W_ 0
Mvvv 116,30,216
Dv 38,%0,49152
Mvvv 116,34,236
Dv 42,v1,49152
Mvvv 120,30,220
Mvvv 120,34,240
Mvvv 124,30,224
Mvvv 124,34,244
Mvvv 128,30,228
Mvvv 128,34,248
s_nop 0
E_ 116
Mvvv 132,30,232
E_ 117
Mvvv 132,34,252
W_ 0
E_ 118
Nvvv 76,38,136,56
Dv 30,%0,50176
Mvvv 76,42,156
Dv 34,v1,50176
Nvvv 80,38,140,60
E_ 119
Mvvv 80,42,160
C_ 119,118,119
C_ 118,116,117
Nvvv 84,38,144,64
E_ 120
Mvvv 84,42,164
E_ 121
Nvvv 88,38,148,68
E_ 122
Mvvv 88,42,168
E_ 123
Nvvv 92,38,152,72
C_ 123,122,123
C_ 122,120,121
Mvvv 92,42,172
W_ 0
E_ 124
Mvvv 92,30,192
Dv 38,%0,51200
Mvvv 92,34,212
Dv 42,v1,51200
Mvvv 88,30,188
E_ 125
Mvvv 88,34,208
E_ 126
Mvvv 84,30,184
S_ s65,s61,147456
E_ 127
Mvvv 84,34,204
S_ m0,s51,49152
C_ 127,126,127
C_ 126,124,125
Mvvv 80,30,180
s_waitcnt vmcnt(18) lgkmcnt(0)
s_barrier
Mvvv 80,34,200
L_ v9,36,s65,0
Mvvv 76,30,176
E_ 128
Mvvv 76,34,196
L_ v9,36,s65,3072
S_ m0,s51,50048
Mvvv 76,38,216
Tv 30,v2,24576
Tv 32,v2,30720
Mvvv 76,42,236
L_ v9,36,s65,128
Mvvv 80,38,220
s_nop 0
Tv 34,v3,24576
Tv 36,v3,30720
Mvvv 80,42,240
L_ v9,36,s65,3200
S_ m0,s51,50944
Mvvv 84,38,224
E_ 129
Mvvv 84,42,244
L_ v9,36,s65,256
Mvvv 88,38,228
E_ 130
Mvvv 88,42,248
L_ v9,36,s65,3328
Mvvv 92,38,232
E_ 131
Mvvv 92,42,252
W_ 0
C_ 131,130,131
C_ 130,128,129
Mavv 0,30,96
s_nop 0
Tv 38,v4,24576
Tv 40,v4,30720
Mavv 20,34,96
E_ 132
Mavv 24,34,100
s_nop 0
Tv 42,v5,24576
Tv 44,v5,30720
Mavv 4,30,100
E_ 133
Mavv 8,30,104
E_ 134
Mavv 28,34,104
E_ 135
Mavv 32,34,108
C_ 135,134,135
C_ 134,132,133
Mavv 12,30,108
E_ 76
Mavv 16,30,112
E_ 77
Mavv 36,34,112
W_ 0
E_ 78
Mavv 56,38,112
s_nop 0
Tv 30,v2,25600
Tv 32,v2,31744
Mavv 76,42,112
E_ 79
Mavv 72,42,108
s_nop 0
Tv 34,v3,25600
Tv 36,v3,31744
Mavv 52,38,108
C_ 116,76,77
C_ 117,78,79
Mavv 48,38,104
E_ 80
Mavv 68,42,104
E_ 81
Mavv 64,42,100
E_ 82
Mavv 44,38,100
E_ 83
Mavv 40,38,96
C_ 120,80,81
C_ 121,82,83
Mavv 60,42,96
W_ 0
E_ 84
Mavv 80,30,96
s_nop 0
Tv 38,v4,25600
Tv 40,v4,31744
Mavv 100,34,96
E_ 85
Mavv 104,34,100
s_nop 0
Tv 42,v5,25600
Tv 44,v5,31744
Mavv 84,30,100
E_ 86
Mavv 88,30,104
E_ 87
Mavv 108,34,104
C_ 124,84,85
C_ 125,86,87
Mavv 112,34,108
E_ 88
Mavv 92,30,108
E_ 89
Mavv 96,30,112
E_ 90
Mavv 116,34,112
W_ 0
E_ 91
Mavv 136,38,112
s_nop 0
Tv 30,v2,26624
Tv 32,v2,32768
Mavv 156,42,112
C_ 128,88,89
C_ 129,90,91
Mavv 152,42,108
s_nop 0
Tv 34,v3,26624
Tv 36,v3,32768
Mavv 132,38,108
E_ 92
Mavv 128,38,104
E_ 93
Mavv 148,42,104
E_ 94
Mavv 144,42,100
E_ 95
Mavv 124,38,100
C_ 132,92,93
C_ 133,94,95
Mavv 120,38,96
v_mov_b32 v92, 0x3f803f80
v_mov_b32 v93, 0x3f803f80
Mavv 140,42,96
W_ 0
v_mov_b32 v94, 0x3f803f80
v_mov_b32 v95, 0x3f803f80
Mavv 160,30,96
s_nop 0
Tv 38,v4,26624
Tv 40,v4,32768
Mavv 180,34,96
Mavv 184,34,100
s_nop 0
Tv 42,v5,26624
Tv 44,v5,32768
Mavv 164,30,100
Mavv 168,30,104
Mavv 188,34,104
S_ s65,s61,172032
Mavv 192,34,108
S_ m0,s51,24576
Mavv 172,30,108
s_waitcnt vmcnt(18) lgkmcnt(0)
s_barrier
Mavv 176,30,112
Ln v7,32,s65,0
Mavv 196,34,112
Mavv 216,38,112
Dv 30,%0,6144
Mavv 236,42,112
Dv 34,v1,6144
Mavv 232,42,108
Ln v8,32,s65,3072
S_ m0,s51,25472
Mavv 212,38,108
Mavv 208,38,104
Ln v7,32,s65,128
Mavv 228,42,104
Mavv 224,42,100
Ln v8,32,s65,3200
S_ m0,s51,26368
Mavv 204,38,100
Mavv 200,38,96
Ln v7,32,s65,256
Mavv 220,42,96
.Lstep0_12:
Zvvv 76,92,116
Dv 38,%0,7168
Zvvv 80,92,120
Dv 42,v1,7168
Zvvv 84,92,124
Zvvv 88,92,128
Zvvv 92,92,132
W_ 2
Nvvv 96,30,136,56
Mvvv 96,34,156
Nvvv 100,30,140,60
s_nop 1
v_max3_f32 v10, v76, v80, v84
s_nop 0
v_max3_f32 v10, v10, v88, v92
Mvvv 100,34,160
v_cmp_lt_f32 vcc, 0x42000000, v10
s_cbranch_vccnz .Ltramp0_11
Nvvv 104,30,144,64
A_ v46,v46,v76
A_ v47,v47,v80
Mvvv 104,34,164
A_ v48,v48,v84
A_ v49,v49,v88
Nvvv 108,30,148,68
A_ v50,v50,v92
Mvvv 108,34,168
Ln v8,32,s65,3328
Nvvv 112,30,152,72
R_ v11,a248
Mvvv 112,34,172
W_ 0
v_lshlrev_b32 v11, 10, v11
Mvvv 112,38,192
Dv 30,%0,8192
Mvvv 112,42,212
Dv 34,v1,8192
Mvvv 108,38,188
buffer_load_dword a254, v11, s[44:47], s66 offen
Mvvv 108,42,208
Mvvv 104,38,184
Mvvv 104,42,204
Mvvv 100,38,180
Mvvv 100,42,200
Mvvv 96,38,176
Mvvv 96,42,196
W_ 0
Mvvv 96,30,216
Dv 38,%0,0
Mvvv 96,34,236
Dv 42,v1,0
Mvvv 100,30,220
Mvvv 100,34,240
Mvvv 104,30,224
Mvvv 104,34,244
Mvvv 108,30,228
Mvvv 108,34,248
s_nop 0
E_ 96
Mvvv 112,30,232
E_ 97
Mvvv 112,34,252
W_ 0
E_ 98
Nvvv 76,38,136,56
Dv 30,%0,1024
Mvvv 76,42,156
Dv 34,v1,1024
Nvvv 80,38,140,60
E_ 99
Mvvv 80,42,160
C_ 99,98,99
C_ 98,96,97
Nvvv 84,38,144,64
E_ 100
Mvvv 84,42,164
E_ 101
Nvvv 88,38,148,68
E_ 102
Mvvv 88,42,168
E_ 103
Nvvv 92,38,152,72
C_ 103,102,103
C_ 102,100,101
Mvvv 92,42,172
W_ 0
E_ 104
Mvvv 92,30,192
Dv 38,%0,2048
Mvvv 92,34,212
Dv 42,v1,2048
Mvvv 88,30,188
E_ 105
Mvvv 88,34,208
E_ 106
Mvvv 84,30,184
S_ s65,s61,159744
E_ 107
Mvvv 84,34,204
S_ m0,s51,0
C_ 107,106,107
C_ 106,104,105
Mvvv 80,30,180
s_waitcnt vmcnt(19) lgkmcnt(0)
s_barrier
Mvvv 80,34,200
L_ v9,36,s65,0
Mvvv 76,30,176
E_ 108
Mvvv 76,34,196
L_ v9,36,s65,3072
S_ m0,s51,896
Mvvv 76,38,216
Tv 30,v2,12288
Tv 32,v2,18432
Mvvv 76,42,236
L_ v9,36,s65,128
Mvvv 80,38,220
s_nop 0
Tv 34,v3,12288
Tv 36,v3,18432
Mvvv 80,42,240
L_ v9,36,s65,3200
S_ m0,s51,1792
Mvvv 84,38,224
E_ 109
Mvvv 84,42,244
L_ v9,36,s65,256
Mvvv 88,38,228
E_ 110
Mvvv 88,42,248
L_ v9,36,s65,3328
Mvvv 92,38,232
E_ 111
Mvvv 92,42,252
W_ 0
C_ 111,110,111
C_ 110,108,109
Mavv 0,30,116
s_nop 0
Tv 38,v4,12288
Tv 40,v4,18432
Mavv 20,34,116
E_ 112
Mavv 24,34,120
s_nop 0
Tv 42,v5,12288
Tv 44,v5,18432
Mavv 4,30,120
E_ 113
Mavv 8,30,124
E_ 114
Mavv 28,34,124
E_ 115
Mavv 32,34,128
C_ 115,114,115
C_ 114,112,113
Mavv 12,30,128
E_ 76
Mavv 16,30,132
E_ 77
Mavv 36,34,132
W_ 0
E_ 78
Mavv 56,38,132
s_nop 0
Tv 30,v2,13312
Tv 32,v2,19456
Mavv 76,42,132
E_ 79
Mavv 72,42,128
s_nop 0
Tv 34,v3,13312
Tv 36,v3,19456
Mavv 52,38,128
C_ 96,76,77
)A"
R"A(C_ 97,78,79
Mavv 48,38,124
E_ 80
Mavv 68,42,124
E_ 81
Mavv 64,42,120
E_ 82
Mavv 44,38,120
E_ 83
Mavv 40,38,116
C_ 100,80,81
C_ 101,82,83
Mavv 60,42,116
W_ 0
E_ 84
Mavv 80,30,116
s_nop 0
Tv 38,v4,13312
Tv 40,v4,19456
Mavv 100,34,116
E_ 85
Mavv 104,34,120
s_nop 0
Tv 42,v5,13312
Tv 44,v5,19456
Mavv 84,30,120
E_ 86
Mavv 88,30,124
E_ 87
Mavv 108,34,124
C_ 104,84,85
C_ 105,86,87
Mavv 112,34,128
E_ 88
Mavv 92,30,128
E_ 89
Mavv 96,30,132
E_ 90
Mavv 116,34,132
W_ 0
E_ 91
Mavv 136,38,132
s_nop 0
Tv 30,v2,14336
Tv 32,v2,20480
Mavv 156,42,132
C_ 108,88,89
C_ 109,90,91
Mavv 152,42,128
s_nop 0
Tv 34,v3,14336
Tv 36,v3,20480
Mavv 132,38,128
E_ 92
Mavv 128,38,124
E_ 93
Mavv 148,42,124
E_ 94
Mavv 144,42,120
E_ 95
Mavv 124,38,120
C_ 112,92,93
C_ 113,94,95
Mavv 120,38,116
v_mov_b32 v92, 0x3f803f80
v_mov_b32 v93, 0x3f803f80
Mavv 140,42,116
W_ 0
v_mov_b32 v94, 0x3f803f80
v_mov_b32 v95, 0x3f803f80
Mavv 160,30,116
s_nop 0
Tv 38,v4,14336
Tv 40,v4,20480
Mavv 180,34,116
Mavv 184,34,120
s_nop 0
Tv 42,v5,14336
Tv 44,v5,20480
Mavv 164,30,120
Mavv 168,30,124
Mavv 188,34,124
S_ s65,s61,184320
Mavv 192,34,128
S_ m0,s51,12288
Mavv 172,30,128
s_waitcnt vmcnt(19) lgkmcnt(0)
s_barrier
Mavv 176,30,132
Ln v7,32,s65,0
Mavv 196,34,132
Mavv 216,38,132
Dv 30,%0,43008
Mavv 236,42,132
Dv 34,v1,43008
Mavv 232,42,128
Ln v8,32,s65,3072
S_ m0,s51,13184
Mavv 212,38,128
Mavv 208,38,124
Ln v7,32,s65,128
Mavv 228,42,124
Mavv 224,42,120
Ln v8,32,s65,3200
S_ m0,s51,14080
Mavv 204,38,120
Mavv 200,38,116
Ln v7,32,s65,256
Mavv 220,42,116
.Lstep0_13:
Zvvv 76,92,96
Dv 38,%0,44032
Zvvv 80,92,100
Dv 42,v1,44032
Zvvv 84,92,104
Zvvv 88,92,108
Zvvv 92,92,112
W_ 2
Nvvv 116,30,136,56
Mvvv 116,34,156
Nvvv 120,30,140,60
s_nop 1
v_max3_f32 v10, v76, v80, v84
s_nop 0
v_max3_f32 v10, v10, v88, v92
Mvvv 120,34,160
v_cmp_lt_f32 vcc, 0x42000000, v10
s_cbranch_vccnz .Ltramp0_12
Nvvv 124,30,144,64
A_ v46,v46,v76
A_ v47,v47,v80
Mvvv 124,34,164
A_ v48,v48,v84
A_ v49,v49,v88
Nvvv 128,30,148,68
A_ v50,v50,v92
Mvvv 128,34,168
Ln v8,32,s65,3328
Nvvv 132,30,152,72
Mvvv 132,34,172
W_ 0
Mvvv 132,38,192
Dv 30,%0,45056
Mvvv 132,42,212
Dv 34,v1,45056
Mvvv 128,38,188
Mvvv 128,42,208
Mvvv 124,38,184
Mvvv 124,42,204
Mvvv 120,38,180
Mvvv 120,42,200
Mvvv 116,38,176
Mvvv 116,42,196
W_ 0
Mvvv 116,30,216
Dv 38,%0,36864
Mvvv 116,34,236
Dv 42,v1,36864
Mvvv 120,30,220
Mvvv 120,34,240
Mvvv 124,30,224
Mvvv 124,34,244
Mvvv 128,30,228
Mvvv 128,34,248
s_nop 0
E_ 116
Mvvv 132,30,232
E_ 117
Mvvv 132,34,252
W_ 0
E_ 118
Nvvv 76,38,136,56
Dv 30,%0,37888
Mvvv 76,42,156
Dv 34,v1,37888
Nvvv 80,38,140,60
E_ 119
Mvvv 80,42,160
C_ 119,118,119
C_ 118,116,117
Nvvv 84,38,144,64
E_ 120
Mvvv 84,42,164
E_ 121
Nvvv 88,38,148,68
E_ 122
Mvvv 88,42,168
E_ 123
Nvvv 92,38,152,72
C_ 123,122,123
C_ 122,120,121
Mvvv 92,42,172
W_ 0
E_ 124
Mvvv 92,30,192
Dv 38,%0,38912
Mvvv 92,34,212
Dv 42,v1,38912
Mvvv 88,30,188
E_ 125
Mvvv 88,34,208
E_ 126
Mvvv 84,30,184
S_ s65,s61,172032
E_ 127
Mvvv 84,34,204
S_ m0,s51,36864
C_ 127,126,127
C_ 126,124,125
Mvvv 80,30,180
s_waitcnt vmcnt(19) lgkmcnt(0)
s_barrier
Mvvv 80,34,200
L_ v9,36,s65,0
Mvvv 76,30,176
E_ 128
Mvvv 76,34,196
L_ v9,36,s65,3072
S_ m0,s51,37760
Mvvv 76,38,216
Tv 30,v2,49152
Tv 32,v2,55296
Mvvv 76,42,236
L_ v9,36,s65,128
Mvvv 80,38,220
s_nop 0
Tv 34,v3,49152
Tv 36,v3,55296
Mvvv 80,42,240
L_ v9,36,s65,3200
S_ m0,s51,38656
Mvvv 84,38,224
E_ 129
Mvvv 84,42,244
L_ v9,36,s65,256
Mvvv 88,38,228
E_ 130
Mvvv 88,42,248
L_ v9,36,s65,3328
Mvvv 92,38,232
E_ 131
Mvvv 92,42,252
W_ 0
C_ 131,130,131
C_ 130,128,129
Mavv 0,30,96
s_nop 0
Tv 38,v4,49152
Tv 40,v4,55296
Mavv 20,34,96
E_ 132
Mavv 24,34,100
s_nop 0
Tv 42,v5,49152
Tv 44,v5,55296
Mavv 4,30,100
E_ 133
Mavv 8,30,104
E_ 134
Mavv 28,34,104
E_ 135
Mavv 32,34,108
C_ 135,134,135
C_ 134,132,133
Mavv 12,30,108
E_ 76
Mavv 16,30,112
E_ 77
Mavv 36,34,112
W_ 0
E_ 78
Mavv 56,38,112
s_nop 0
Tv 30,v2,50176
Tv 32,v2,56320
Mavv 76,42,112
E_ 79
Mavv 72,42,108
s_nop 0
Tv 34,v3,50176
Tv 36,v3,56320
Mavv 52,38,108
C_ 116,76,77
C_ 117,78,79
Mavv 48,38,104
E_ 80
Mavv 68,42,104
E_ 81
Mavv 64,42,100
E_ 82
Mavv 44,38,100
E_ 83
Mavv 40,38,96
C_ 120,80,81
C_ 121,82,83
Mavv 60,42,96
W_ 0
E_ 84
Mavv 80,30,96
s_nop 0
Tv 38,v4,50176
Tv 40,v4,56320
Mavv 100,34,96
E_ 85
Mavv 104,34,100
s_nop 0
Tv 42,v5,50176
Tv 44,v5,56320
Mavv 84,30,100
E_ 86
Mavv 88,30,104
E_ 87
Mavv 108,34,104
C_ 124,84,85
C_ 125,86,87
Mavv 112,34,108
E_ 88
Mavv 92,30,108
E_ 89
Mavv 96,30,112
E_ 90
Mavv 116,34,112
W_ 0
E_ 91
Mavv 136,38,112
s_nop 0
Tv 30,v2,51200
Tv 32,v2,57344
Mavv 156,42,112
C_ 128,88,89
C_ 129,90,91
Mavv 152,42,108
s_nop 0
Tv 34,v3,51200
Tv 36,v3,57344
Mavv 132,38,108
E_ 92
Mavv 128,38,104
E_ 93
Mavv 148,42,104
E_ 94
Mavv 144,42,100
E_ 95
Mavv 124,38,100
C_ 132,92,93
C_ 133,94,95
Mavv 120,38,96
v_mov_b32 v92, 0x3f803f80
v_mov_b32 v93, 0x3f803f80
Mavv 140,42,96
W_ 0
v_mov_b32 v94, 0x3f803f80
v_mov_b32 v95, 0x3f803f80
Mavv 160,30,96
s_nop 0
Tv 38,v4,51200
Tv 40,v4,57344
Mavv 180,34,96
Mavv 184,34,100
s_nop 0
Tv 42,v5,51200
Tv 44,v5,57344
Mavv 164,30,100
Mavv 168,30,104
Mavv 188,34,104
S_ s65,s61,184320
Mavv 192,34,108
S_ m0,s51,49152
Mavv 172,30,108
s_waitcnt vmcnt(19) lgkmcnt(0)
s_barrier
Mavv 176,30,112
L_ v9,36,s65,0
Mavv 196,34,112
Mavv 216,38,112
Dv 30,%0,30720
Mavv 236,42,112
Dv 34,v1,30720
Mavv 232,42,108
L_ v9,36,s65,3072
S_ m0,s51,50048
Mavv 212,38,108
Mavv 208,38,104
L_ v9,36,s65,128
Mavv 228,42,104
Mavv 224,42,100
L_ v9,36,s65,3200
S_ m0,s51,50944
Mavv 204,38,100
Mavv 200,38,96
L_ v9,36,s65,256
Mavv 220,42,96
.Lstep0_14:
Zvvv 76,92,116
Dv 38,%0,31744
Zvvv 80,92,120
Dv 42,v1,31744
Zvvv 84,92,124
Zvvv 88,92,128
Zvvv 92,92,132
W_ 2
Nvvv 96,30,136,56
Mvvv 96,34,156
Nvvv 100,30,140,60
s_nop 1
v_max3_f32 v10, v76, v80, v84
s_nop 0
v_max3_f32 v10, v10, v88, v92
Mvvv 100,34,160
v_cmp_lt_f32 vcc, 0x42000000, v10
s_cbranch_vccnz .Ltramp0_13
Nvvv 104,30,144,64
A_ v46,v46,v76
A_ v47,v47,v80
Mvvv 104,34,164
A_ v48,v48,v84
A_ v49,v49,v88
Nvvv 108,30,148,68
A_ v50,v50,v92
Mvvv 108,34,168
L_ v9,36,s65,3328
Nvvv 112,30,152,72
R_ v20,a240
Mvvv 112,34,172
W_ 0
v_xor_b32 v21, 64, v20
Mvvv 112,38,192
S_ s65,s60,0
Dv 30,%0,32768
Mvvv 112,42,212
S_ m0,s50,0
Dv 34,v1,32768
Mvvv 108,38,188
L_ v20,24,s65,0
Mvvv 108,42,208
S_ s65,s60,10240
Mvvv 104,38,184
S_ m0,s50,1024
Mvvv 104,42,204
L_ v21,24,s65,0
Mvvv 100,38,180
S_ s65,s60,20480
Mvvv 100,42,200
S_ m0,s50,2048
Mvvv 96,38,176
L_ v20,24,s65,0
Mvvv 96,42,196
S_ s65,s60,30720
W_ 0
Mvvv 96,30,216
S_ m0,s50,3072
Dv 38,%0,24576
Mvvv 96,34,236
Dv 42,v1,24576
Mvvv 100,30,220
L_ v21,24,s65,0
Mvvv 100,34,240
S_ s65,s60,40960
Mvvv 104,30,224
S_ m0,s50,4096
Mvvv 104,34,244
L_ v20,24,s65,0
Mvvv 108,30,228
Mvvv 108,34,248
E_ 96
Mvvv 112,30,232
E_ 97
Mvvv 112,34,252
W_ 0
E_ 98
Nvvv 76,38,136,56
Dv 30,%0,25600
Mvvv 76,42,156
Dv 34,v1,25600
Nvvv 80,38,140,60
E_ 99
Mvvv 80,42,160
C_ 99,98,99
C_ 98,96,97
Nvvv 84,38,144,64
E_ 100
Mvvv 84,42,164
E_ 101
Nvvv 88,38,148,68
E_ 102
Mvvv 88,42,168
E_ 103
Nvvv 92,38,152,72
C_ 103,102,103
C_ 102,100,101
Mvvv 92,42,172
W_ 0
E_ 104
Mvvv 92,30,192
Dv 38,%0,26624
Mvvv 92,34,212
Dv 42,v1,26624
Mvvv 88,30,188
E_ 105
Mvvv 88,34,208
E_ 106
Mvvv 84,30,184
E_ 107
Mvvv 84,34,204
C_ 107,106,107
C_ 106,104,105
Mvvv 80,30,180
s_waitcnt vmcnt(23) lgkmcnt(0)
s_barrier
Mvvv 80,34,200
E_ 108
Mvvv 76,30,176
E_ 109
Mvvv 76,34,196
E_ 110
Mvvv 76,38,216
s_nop 0
Tv 30,v2,0
Tv 32,v2,6144
Mvvv 76,42,236
E_ 111
Mvvv 80,38,220
s_nop 0
Tv 34,v3,0
Tv 36,v3,6144
Mvvv 80,42,240
C_ 111,110,111
C_ 110,108,109
Mvvv 84,38,224
E_ 112
Mvvv 84,42,244
E_ 113
Mvvv 88,38,228
E_ 114
Mvvv 88,42,248
E_ 115
Mvvv 92,38,232
C_ 115,114,115
C_ 114,112,113
Mvvv 92,42,252
W_ 0
E_ 76
Mavv 0,30,116
s_nop 0
Tv 38,v4,0
Tv 40,v4,6144
Mavv 20,34,116
E_ 77
Mavv 24,34,120
s_nop 0
Tv 42,v5,0
Tv 44,v5,6144
Mavv 4,30,120
E_ 78
Mavv 8,30,124
E_ 79
Mavv 28,34,124
C_ 96,76,77
C_ 97,78,79
Mavv 32,34,128
E_ 80
Mavv 12,30,128
E_ 81
Mavv 16,30,132
E_ 82
Mavv 36,34,132
W_ 0
E_ 83
Mavv 56,38,132
s_nop 0
Tv 30,v2,1024
Tv 32,v2,7168
Mavv 76,42,132
C_ 100,80,81
C_ 101,82,83
Mavv 72,42,128
s_nop 0
Tv 34,v3,1024
Tv 36,v3,7168
Mavv 52,38,128
E_ 84
Mavv 48,38,124
E_ 85
Mavv 68,42,124
E_ 86
Mavv 64,42,120
E_ 87
Mavv 44,38,120
C_ 104,84,85
C_ 105,86,87
Mavv 40,38,116
E_ 88
Mavv 60,42,116
W_ 0
E_ 89
Mavv 80,30,116
s_nop 0
Tv 38,v4,1024
Tv 40,v4,7168
Mavv 100,34,116
E_ 90
Mavv 104,34,120
s_nop 0
Tv 42,v5,1024
Tv 44,v5,7168
Mavv 84,30,120
E_ 91
Mavv 88,30,124
C_ 108,88,89
C_ 109,90,91
Mavv 108,34,124
E_ 92
Mavv 112,34,128
E_ 93
Mavv 92,30,128
E_ 94
Mavv 96,30,132
E_ 95
Mavv 116,34,132
W_ 0
C_ 112,92,93
C_ 113,94,95
Mavv 136,38,132
s_nop 0
Tv 30,v2,2048
Tv 32,v2,8192
Mavv 156,42,132
v_mov_b32 v92, 0x3f803f80
v_mov_b32 v93, 0x3f803f80
Mavv 152,42,128
s_nop 0
Tv 34,v3,2048
Tv 36,v3,8192
Mavv 132,38,128
v_mov_b32 v94, 0x3f803f80
v_mov_b32 v95, 0x3f803f80
Mavv 128,38,124
Mavv 148,42,124
Mavv 144,42,120
Mavv 124,38,120
Mavv 120,38,116
Mavv 140,42,116
W_ 0
Mavv 160,30,116
s_nop 0
Tv 38,v4,2048
Tv 40,v4,8192
Mavv 180,34,116
Mavv 184,34,120
s_nop 0
Tv 42,v5,2048
Tv 44,v5,8192
Mavv 164,30,120
Mavv 168,30,124
Mavv 188,34,124
Mavv 192,34,128
Mavv 172,30,128
s_waitcnt vmcnt(17) lgkmcnt(0)
s_barrier
Mavv 176,30,132
S_ s65,s59,0
Mavv 196,34,132
S_ m0,s52,24320
Mavv 216,38,132
Dv 30,%0,18432
Mavv 236,42,132
Dv 34,v1,18432
Mavv 232,42,128
L_ v6,28,s65,256
Mavv 212,38,128
S_ s65,s59,98304
Mavv 208,38,124
S_ m0,s52,25344
Mavv 228,42,124
L_ v6,28,s65,256
Mavv 224,42,120
S_ s65,s59,196608
Mavv 204,38,120
S_ m0,s52,26368
Mavv 200,38,116
L_ v6,28,s65,256
Mavv 220,42,116
S_ s65,s59,294912
.Lstep0_15:
Zvvv 76,92,96
Dv 38,%0,19456
Zvvv 80,92,100
Dv 42,v1,19456
Zvvv 84,92,104
Zvvv 88,92,108
Zvvv 92,92,112
W_ 2
Nvvv 116,30,136,56
Mvvv 116,34,156
Nvvv 120,30,140,60
s_nop 1
v_max3_f32 v10, v76, v80, v84
s_nop 0
v_max3_f32 v10, v10, v88, v92
Mvvv 120,34,160
v_cmp_lt_f32 vcc, 0x42000000, v10
s_cbranch_vccnz .Ltramp0_14
Nvvv 124,30,144,64
S_ m0,s52,27392
A_ v46,v46,v76
A_ v47,v47,v80
Mvvv 124,34,164
A_ v48,v48,v84
A_ v49,v49,v88
Nvvv 128,30,148,68
A_ v50,v50,v92
Mvvv 128,34,168
L_ v6,28,s65,256
Nvvv 132,30,152,72
S_ s65,s59,786432
Mvvv 132,34,172
S_ m0,s52,0
W_ 0
Mvvv 132,38,192
Dv 30,%0,20480
Mvvv 132,42,212
Dv 34,v1,20480
Mvvv 128,38,188
L_ v6,28,s65,0
Mvvv 128,42,208
S_ s65,s59,884736
Mvvv 124,38,184
S_ m0,s52,1024
Mvvv 124,42,204
L_ v6,28,s65,0
Mvvv 120,38,180
S_ s65,s59,983040
Mvvv 120,42,200
S_ m0,s52,2048
Mvvv 116,38,176
L_ v6,28,s65,0
Mvvv 116,42,196
S_ s65,s59,1081344
W_ 0
Mvvv 116,30,216
S_ m0,s52,3072
Dv 38,%0,12288
Mvvv 116,34,236
Dv 42,v1,12288
Mvvv 120,30,220
L_ v6,28,s65,0
Mvvv 120,34,240
S_ s65,s60,128
Mvvv 124,30,224
S_ m0,s50,20480
Mvvv 124,34,244
L_ v20,24,s65,0
Mvvv 128,30,228
S_ s65,s60,10368
Mvvv 128,34,248
S_ m0,s50,21504
E_ 116
Mvvv 132,30,232
L_ v21,24,s65,0
Mvvv 132,34,252
S_ s65,s60,20608
E_ 117
W_ 1
Nvvv 76,38,136,56
S_ m0,s50,22528
Dv 30,%0,13312
W_ 1
Mvvv 76,42,156
Dv 34,v1,13312
Nvvv 80,38,140,60
L_ v20,24,s65,0
Mvvv 80,42,160
S_ s65,s60,30848
E_ 118
Nvvv 84,38,144,64
S_ m0,s50,23552
E_ 119
Mvvv 84,42,164
L_ v21,24,s65,0
Nvvv 88,38,148,68
S_ s65,s60,41088
C_ 119,118,119
C_ 118,116,117
Mvvv 88,42,168
S_ m0,s50,24576
E_ 120
Nvvv 92,38,152,72
L_ v20,24,s65,0
Mvvv 92,42,172
W_ 0
E_ 121
Mvvv 92,30,192
Dv 38,%0,14336
Mvvv 92,34,212
Dv 42,v1,14336
Mvvv 88,30,188
E_ 122
Mvvv 88,34,208
E_ 123
Mvvv 84,30,184
C_ 123,122,123
C_ 122,120,121
Mvvv 84,34,204
E_ 124
Mvvv 80,30,180
s_waitcnt vmcnt(24) lgkmcnt(0)
s_barrier
Mvvv 80,34,200
S_ s65,s59,0
E_ 125
Mvvv 76,30,176
S_ m0,s52,8192
E_ 126
Mvvv 76,34,196
L_ v6,28,s65,0
Mvvv 76,38,216
S_ s65,s59,98304
Tv 30,v2,36864
Tv 32,v2,43008
Mvvv 76,42,236
S_ m0,s52,9216
E_ 127
Mvvv 80,38,220
Tv 34,v3,36864
Tv 36,v3,43008
Mvvv 80,42,240
L_ v6,28,s65,0
Mvvv 84,38,224
S_ s65,s59,196608
C_ 127,126,127
C_ 126,124,125
Mvvv 84,42,244
S_ m0,s52,10240
E_ 128
Mvvv 88,38,228
L_ v6,28,s65,0
Mvvv 88,42,248
S_ s65,s59,294912
E_ 129
Mvvv 92,38,232
S_ m0,s52,11264
E_ 130
Mvvv 92,42,252
L_ v6,28,s65,0
W_ 2
Mavv 0,30,96
S_ s65,s59,0
Tv 38,v4,36864
Tv 40,v4,43008
W_ 2
Mavv 20,34,96
S_ m0,s52,16256
E_ 131
Mavv 24,34,100
Tv 42,v5,36864
Tv 44,v5,43008
Mavv 4,30,100
L_ v6,28,s65,128
Mavv 8,30,104
S_ s65,s59,98304
C_ 131,130,131
C_ 130,128,129
Mavv 28,34,104
S_ m0,s52,17280
E_ 132
Mavv 32,34,108
L_ v6,28,s65,128
Mavv 12,30,108
S_ s65,s59,196608
E_ 133
Mavv 16,30,112
S_ m0,s52,18304
E_ 134
Mavv 36,34,112
L_ v6,28,s65,128
W_ 2
Mavv 56,38,112
S_ s65,s59,294912
Tv 30,v2,37888
Tv 32,v2,44032
W_ 2
Mavv 76,42,112
S_ m0,s52,19328
E_ 135
Mavv 72,42,108
Tv 34,v3,37888
Tv 36,v3,44032
Mavv 52,38,108
L_ v6,28,s65,128
Mavv 48,38,104
C_ 135,134,135
C_ 134,132,133
Mavv 68,42,104
E_ 76
Mavv 64,42,100
E_ 77
Mavv 44,38,100
E_ 78
Mavv 40,38,96
E_ 79
Mavv 60,42,96
W_ 0
C_ 116,76,77
C_ 117,78,79
Mavv 80,30,96
s_nop 0
Tv 38,v4,37888
Tv 40,v4,44032
Mavv 100,34,96
E_ 80
Mavv 104,34,100
s_nop 0
Tv 42,v5,37888
Tv 44,v5,44032
Mavv 84,30,100
E_ 81
Mavv 88,30,104
E_ 82
Mavv 108,34,104
E_ 83
Mavv 112,34,108
C_ 120,80,81
C_ 121,82,83
Mavv 92,30,108
E_ 84
Mavv 96,30,112
E_ 85
Mavv 116,34,112
W_ 0
E_ 86
Mavv 136,38,112
s_nop 0
Tv 30,v2,38912
Tv 32,v2,45056
Mavv 156,42,112
E_ 87
Mavv 152,42,108
s_nop 0
Tv 34,v3,38912
Tv 36,v3,45056
Mavv 132,38,108
C_ 124,84,85
C_ 125,86,87
Mavv 128,38,104
E_ 88
Mavv 148,42,104
E_ 89
Mavv 144,42,100
E_ 90
Mavv 124,38,100
E_ 91
Mavv 120,38,96
C_ 128,88,89
C_ 129,90,91
Mavv 140,42,96
W_ 0
E_ 92
Mavv 160,30,96
s_nop 0
Tv 38,v4,38912
Tv 40,v4,45056
Mavv 180,34,96
E_ 93
Mavv 184,34,100
s_nop 0
Tv 42,v5,38912
Tv 44,v5,45056
Mavv 164,30,100
E_ 94
Mavv 168,30,104
E_ 95
Mavv 188,34,104
C_ 132,92,93
C_ 133,94,95
Mavv 192,34,108
v_mov_b32 v92, 0x3f803f80
v_mov_b32 v93, 0x3f803f80
Mavv 172,30,108
s_waitcnt vmcnt(26) lgkmcnt(0)
s_barrier
v_mov_b32 v94, 0x3f803f80
Mavv 176,30,112
v_mov_b32 v95, 0x3f803f80
Mavv 196,34,112
Mavv 216,38,112
s_nop 0
Tv 30,v2,49152
Tv 32,v2,55296
Zvvv 76,92,116
Zvvv 80,92,120
s_nop 0
Tv 34,v3,49152
Tv 36,v3,55296
Zvvv 84,92,124
Zvvv 88,92,128
Zvvv 92,92,132
Mavv 236,42,112
Mavv 232,42,108
Mavv 212,38,108
Mavv 208,38,104
s_nop 1
v_max3_f32 v10, v76, v80, v84
Mavv 228,42,104
v_max3_f32 v10, v10, v88, v92
v_cmp_lt_f32 vcc, 0x42000000, v10
Mavv 224,42,100
Mavv 204,38,100
Mavv 200,38,96
Mavv 220,42,96
s_cbranch_vccnz .Ltramp0_15
.Ljoin0:
W_ 2
Mavv 0,30,116
s_mov_b32 s58, 1
Tv 38,v4,49152
Tv 40,v4,55296
W_ 2
Mavv 20,34,116
S_ s61,s61,393216
R_ v12,a248
s_mov_b32 s85, 4026531840
Mavv 24,34,120
s_nop 0
Tv 42,v5,49152
Tv 44,v5,55296
Mavv 4,30,120
A_ v46,v46,v76
A_ v47,v47,v80
Mavv 8,30,124
A_ v48,v48,v84
A_ v49,v49,v88
Mavv 28,34,124
A_ v50,v50,v92
Mavv 32,34,128
v_rcp_f32 v46, v46
Mavv 12,30,128
v_rcp_f32 v47, v47
Mavv 16,30,132
v_rcp_f32 v48, v48
Mavv 36,34,132
W_ 0
v_rcp_f32 v49, v49
Mavv 56,38,132
s_nop 0
Tv 30,v2,50176
Tv 32,v2,56320
Mavv 76,42,132
v_rcp_f32 v50, v50
Mavv 72,42,128
s_nop 0
Tv 34,v3,50176
Tv 36,v3,56320
Mavv 52,38,128
R_ v96,a0
R_ v97,a1
Mavv 48,38,124
R_ v98,a2
R_ v99,a3
Mavv 68,42,124
R_ v100,a20
R_ v101,a21
Mavv 64,42,120
R_ v102,a22
R_ v103,a23
Mavv 44,38,120
U_ v96,v96,v46
U_ v97,v97,v46
Mavv 40,38,116
U_ v98,v98,v46
U_ v99,v99,v46
Mavv 60,42,116
W_ 0
U_ v100,v100,v46
U_ v101,v101,v46
Mavv 80,30,116
s_nop 0
Tv 38,v4,50176
Tv 40,v4,56320
Mavv 100,34,116
U_ v102,v102,v46
U_ v103,v103,v46
Mavv 104,34,120
s_nop 0
Tv 42,v5,50176
Tv 44,v5,56320
Mavv 84,30,120
C_ 76,96,97
C_ 77,98,99
Mavv 88,30,124
C_ 78,100,101
C_ 79,102,103
Mavv 108,34,124
X_ a0,v76
X_ a1,v77
Mavv 112,34,128
X_ a2,v78
X_ a3,v79
Mavv 92,30,128
R_ v104,a4
R_ v105,a5
Mavv 96,30,132
R_ v106,a6
R_ v107,a7
Mavv 116,34,132
W_ 0
R_ v108,a24
R_ v109,a25
Mavv 136,38,132
s_nop 0
Tv 30,v2,51200
Tv 32,v2,57344
Mavv 156,42,132
R_ v110,a26
R_ v111,a27
Mavv 152,42,128
s_nop 0
Tv 34,v3,51200
Tv 36,v3,57344
Mavv 132,38,128
U_ v104,v104,v47
U_ v105,v105,v47
Mavv 128,38,124
U_ v106,v106,v47
U_ v107,v107,v47
Mavv 148,42,124
U_ v108,v108,v47
U_ v109,v109,v47
Mavv 144,42,120
U_ v110,v110,v47
U_ v111,v111,v47
Mavv 124,38,120
C_ 80,104,105
C_ 81,106,107
Mavv 120,38,116
C_ 82,108,109
C_ 83,110,111
Mavv 140,42,116
W_ 0
X_ a4,v80
X_ a5,v81
Mavv 160,30,116
s_nop 0
Tv 38,v4,51200
Tv 40,v4,57344
Mavv 180,34,116
X_ a6,v82
X_ a7,v83
Mavv 184,34,120
s_nop 0
Tv 42,v5,51200
Tv 44,v5,57344
Mavv 164,30,120
R_ v96,a8
R_ v97,a9
Mavv 168,30,124
R_ v98,a10
R_ v99,a11
Mavv 188,34,124
R_ v100,a28
R_ v101,a29
Mavv 192,34,128
R_ v102,a30
R_ v103,a31
Mavv 172,30,128
U_ v96,v96,v48
U_ v97,v97,v48
Mavv 176,30,132
U_ v98,v98,v48
U_ v99,v99,v48
Mavv 196,34,132
W_ 0
U_ v100,v100,v48
U_ v101,v101,v48
Mavv 216,38,132
U_ v102,v102,v48
U_ v103,v103,v48
Mavv 236,42,132
C_ 84,96,97
C_ 85,98,99
Mavv 232,42,128
C_ 86,100,101
C_ 87,102,103
Mavv 212,38,128
X_ a8,v84
X_ a9,v85
Mavv 208,38,124
X_ a10,v86
X_ a11,v87
Mavv 228,42,124
Mavv 224,42,120
Mavv 204,38,120
Mavv 200,38,116
Mavv 220,42,116
.p2align 3
R_ v104,a12
R_ v105,a13
R_ v106,a14
R_ v107,a15
R_ v108,a32
R_ v109,a33
R_ v110,a34
R_ v111,a35
v_pk_mul_f32 v[104:105], v[104:105], v[48:49] op_sel:[0,1]
v_pk_mul_f32 v[106:107], v[106:107], v[48:49] op_sel:[0,1]
v_pk_mul_f32 v[108:109], v[108:109], v[48:49] op_sel:[0,1]
v_pk_mul_f32 v[110:111], v[110:111], v[48:49] op_sel:[0,1]
C_ 88,104,105
C_ 89,106,107
C_ 90,108,109
C_ 91,110,111
X_ a12,v88
X_ a13,v89
X_ a14,v90
X_ a15,v91
.p2align 3
R_ v96,a16
R_ v97,a17
R_ v98,a18
R_ v99,a19
R_ v100,a36
R_ v101,a37
R_ v102,a38
R_ v103,a39
v_pk_mul_f32 v[96:97], v[96:97], v[50:51] op_sel_hi:[1,0]
v_pk_mul_f32 v[98:99], v[98:99], v[50:51] op_sel_hi:[1,0]
v_pk_mul_f32 v[100:101], v[100:101], v[50:51] op_sel_hi:[1,0]
v_pk_mul_f32 v[102:103], v[102:103], v[50:51] op_sel_hi:[1,0]
C_ 92,96,97
C_ 93,98,99
C_ 94,100,101
C_ 95,102,103
X_ a16,v92
X_ a17,v93
X_ a18,v94
X_ a19,v95
.p2align 3
R_ v104,a40
R_ v105,a41
R_ v106,a42
R_ v107,a43
R_ v108,a60
R_ v109,a61
R_ v110,a62
R_ v111,a63
v_pk_mul_f32 v[104:105], v[104:105], v[46:47] op_sel_hi:[1,0]
v_pk_mul_f32 v[106:107], v[106:107], v[46:47] op_sel_hi:[1,0]
v_pk_mul_f32 v[108:109], v[108:109], v[46:47] op_sel_hi:[1,0]
v_pk_mul_f32 v[110:111], v[110:111], v[46:47] op_sel_hi:[1,0]
C_ 76,104,105
C_ 77,106,107
C_ 78,108,109
C_ 79,110,111
X_ a40,v76
X_ a41,v77
X_ a42,v78
X_ a43,v79
.p2align 3
R_ v96,a44
R_ v97,a45
R_ v98,a46
R_ v99,a47
R_ v100,a64
R_ v101,a65
R_ v102,a66
R_ v103,a67
v_pk_mul_f32 v[96:97], v[96:97], v[46:47] op_sel:[0,1]
v_pk_mul_f32 v[98:99], v[98:99], v[46:47] op_sel:[0,1]
v_pk_mul_f32 v[100:101], v[100:101], v[46:47] op_sel:[0,1]
v_pk_mul_f32 v[102:103], v[102:103], v[46:47] op_sel:[0,1]
C_ 80,96,97
C_ 81,98,99
C_ 82,100,101
C_ 83,102,103
X_ a44,v80
X_ a45,v81
X_ a46,v82
X_ a47,v83
.p2align 3
R_ v104,a48
R_ v105,a49
R_ v106,a50
R_ v107,a51
R_ v108,a68
R_ v109,a69
R_ v110,a70
R_ v111,a71
v_pk_mul_f32 v[104:105], v[104:105], v[48:49] op_sel_hi:[1,0]
v_pk_mul_f32 v[106:107], v[106:107], v[48:49] op_sel_hi:[1,0]
v_pk_mul_f32 v[108:109], v[108:109], v[48:49] op_sel_hi:[1,0]
v_pk_mul_f32 v[110:111], v[110:111], v[48:49] op_sel_hi:[1,0]
C_ 84,104,105
C_ 85,106,107
C_ 86,108,109
C_ 87,110,111
X_ a48,v84
X_ a49,v85
X_ a50,v86
X_ a51,v87
.p2align 3
R_ v96,a52
R_ v97,a53
R_ v98,a54
R_ v99,a55
R_ v100,a72
R_ v101,a73
R_ v102,a74
R_ v103,a75
v_pk_mul_f32 v[96:97], v[96:97], v[48:49] op_sel:[0,1]
v_pk_mul_f32 v[98:99], v[98:99], v[48:49] op_sel:[0,1]
v_pk_mul_f32 v[100:101], v[100:101], v[48:49] op_sel:[0,1]
v_pk_mul_f32 v[102:103], v[102:103], v[48:49] op_sel:[0,1]
C_ 88,96,97
C_ 89,98,99
C_ 90,100,101
C_ 91,102,103
X_ a52,v88
X_ a53,v89
X_ a54,v90
X_ a55,v91
.p2align 3
R_ v104,a56
R_ v105,a57
R_ v106,a58
R_ v107,a59
R_ v108,a76
R_ v109,a77
R_ v110,a78
R_ v111,a79
v_pk_mul_f32 v[104:105], v[104:105], v[50:51] op_sel_hi:[1,0]
v_pk_mul_f32 v[106:107], v[106:107], v[50:51] op_sel_hi:[1,0]
v_pk_mul_f32 v[108:109], v[108:109], v[50:51] op_sel_hi:[1,0]
v_pk_mul_f32 v[110:111], v[110:111], v[50:51] op_sel_hi:[1,0]
C_ 92,104,105
C_ 93,106,107
C_ 94,108,109
C_ 95,110,111
X_ a56,v92
X_ a57,v93
X_ a58,v94
X_ a59,v95
.p2align 3
R_ v96,a80
R_ v97,a81
R_ v98,a82
R_ v99,a83
R_ v100,a100
R_ v101,a101
R_ v102,a102
R_ v103,a103
v_pk_mul_f32 v[96:97], v[96:97], v[46:47] op_sel_hi:[1,0]
v_pk_mul_f32 v[98:99], v[98:99], v[46:47] op_sel_hi:[1,0]
v_pk_mul_f32 v[100:101], v[100:101], v[46:47] op_sel_hi:[1,0]
v_pk_mul_f32 v[102:103], v[102:103], v[46:47] op_sel_hi:[1,0]
C_ 76,96,97
C_ 77,98,99
C_ 78,100,101
C_ 79,102,103
X_ a80,v76
X_ a81,v77
X_ a82,v78
X_ a83,v79
.p2align 3
R_ v104,a84
R_ v105,a85
R_ v106,a86
R_ v107,a87
R_ v108,a104
R_ v109,a105
R_ v110,a106
R_ v111,a107
v_pk_mul_f32 v[104:105], v[104:105], v[46:47] op_sel:[0,1]
v_pk_mul_f32 v[106:107], v[106:107], v[46:47] op_sel:[0,1]
v_pk_mul_f32 v[108:109], v[108:109], v[46:47] op_sel:[0,1]
v_pk_mul_f32 v[110:111], v[110:111], v[46:47] op_sel:[0,1]
C_ 80,104,105
C_ 81,106,107
C_ 82,108,109
C_ 83,110,111
X_ a84,v80
X_ a85,v81
X_ a86,v82
X_ a87,v83
.p2align 3
R_ v96,a88
R_ v97,a89
R_ v98,a90
R_ v99,a91
R_ v100,a108
R_ v101,a109
R_ v102,a110
R_ v103,a111
v_pk_mul_f32 v[96:97], v[96:97], v[48:49] op_sel_hi:[1,0]
v_pk_mul_f32 v[98:99], v[98:99], v[48:49] op_sel_hi:[1,0]
v_pk_mul_f32 v[100:101], v[100:101], v[48:49] op_sel_hi:[1,0]
v_pk_mul_f32 v[102:103], v[102:103], v[48:49] op_sel_hi:[1,0]
C_ 84,96,97
C_ 85,98,99
C_ 86,100,101
C_ 87,102,103
X_ a88,v84
X_ a89,v85
X_ a90,v86
X_ a91,v87
.p2align 3
R_ v104,a92
R_ v105,a93
R_ v106,a94
R_ v107,a95
R_ v108,a112
R_ v109,a113
R_ v110,a114
R_ v111,a115
v_pk_mul_f32 v[104:105], v[104:105], v[48:49] op_sel:[0,1]
v_pk_mul_f32 v[106:107], v[106:107], v[48:49] op_sel:[0,1]
v_pk_mul_f32 v[108:109], v[108:109], v[48:49] op_sel:[0,1]
v_pk_mul_f32 v[110:111], v[110:111], v[48:49] op_sel:[0,1]
C_ 88,104,105
C_ 89,106,107
C_ 90,108,109
C_ 91,110,111
X_ a92,v88
X_ a93,v89
X_ a94,v90
X_ a95,v91
.p2align 3
R_ v96,a96
R_ v97,a97
R_ v98,a98
R_ v99,a99
R_ v100,a116
R_ v101,a117
R_ v102,a118
R_ v103,a119
v_pk_mul_f32 v[96:97], v[96:97], v[50:51] op_sel_hi:[1,0]
v_pk_mul_f32 v[98:99], v[98:99], v[50:51] op_sel_hi:[1,0]
v_pk_mul_f32 v[100:101], v[100:101], v[50:51] op_sel_hi:[1,0]
v_pk_mul_f32 v[102:103], v[102:103], v[50:51] op_sel_hi:[1,0]
C_ 92,96,97
C_ 93,98,99
C_ 94,100,101
C_ 95,102,103
X_ a96,v92
X_ a97,v93
X_ a98,v94
X_ a99,v95
.p2align 3
R_ v104,a120
R_ v105,a121
R_ v106,a122
R_ v107,a123
R_ v108,a140
R_ v109,a141
R_ v110,a142
R_ v111,a143
v_pk_mul_f32 v[104:105], v[104:105], v[46:47] op_sel_hi:[1,0]
v_pk_mul_f32 v[106:107], v[106:107], v[46:47] op_sel_hi:[1,0]
v_pk_mul_f32 v[108:109], v[108:109], v[46:47] op_sel_hi:[1,0]
v_pk_mul_f32 v[110:111], v[110:111], v[46:47] op_sel_hi:[1,0]
C_ 76,104,105
C_ 77,106,107
C_ 78,108,109
C_ 79,110,111
X_ a120,v76
X_ a121,v77
X_ a122,v78
X_ a123,v79
.p2align 3
R_ v96,a124
R_ v97,a125
R_ v98,a126
R_ v99,a127
R_ v100,a144
R_ v101,a145
R_ v102,a146
R_ v103,a147
v_pk_mul_f32 v[96:97], v[96:97], v[46:47] op_sel:[0,1]
v_pk_mul_f32 v[98:99], v[98:99], v[46:47] op_sel:[0,1]
v_pk_mul_f32 v[100:101], v[100:101], v[46:47] op_sel:[0,1]
v_pk_mul_f32 v[102:103], v[102:103], v[46:47] op_sel:[0,1]
C_ 80,96,97
C_ 81,98,99
C_ 82,100,101
C_ 83,102,103
X_ a124,v80
X_ a125,v81
X_ a126,v82
X_ a127,v83
.p2align 3
R_ v104,a128
R_ v105,a129
R_ v106,a130
R_ v107,a131
R_ v108,a148
R_ v109,a149
R_ v110,a150
R_ v111,a151
v_pk_mul_f32 v[104:105], v[104:105], v[48:49] op_sel_hi:[1,0]
v_pk_mul_f32 v[106:107], v[106:107], v[48:49] op_sel_hi:[1,0]
v_pk_mul_f32 v[108:109], v[108:109], v[48:49] op_sel_hi:[1,0]
v_pk_mul_f32 v[110:111], v[110:111], v[48:49] op_sel_hi:[1,0]
C_ 84,104,105
C_ 85,106,107
C_ 86,108,109
C_ 87,110,111
X_ a128,v84
X_ a129,v85
X_ a130,v86
X_ a131,v87
.p2align 3
R_ v96,a132
R_ v97,a133
R_ v98,a134
R_ v99,a135
R_ v100,a152
R_ v101,a153
R_ v102,a154
R_ v103,a155
v_pk_mul_f32 v[96:97], v[96:97], v[48:49] op_sel:[0,1]
v_pk_mul_f32 v[98:99], v[98:99], v[48:49] op_sel:[0,1]
v_pk_mul_f32 v[100:101], v[100:101], v[48:49] op_sel:[0,1]
v_pk_mul_f32 v[102:103], v[102:103], v[48:49] op_sel:[0,1]
C_ 88,96,97
C_ 89,98,99
C_ 90,100,101
C_ 91,102,103
X_ a132,v88
X_ a133,v89
X_ a134,v90
X_ a135,v91
.p2align 3
R_ v104,a136
R_ v105,a137
R_ v106,a138
R_ v107,a139
R_ v108,a156
R_ v109,a157
R_ v110,a158
R_ v111,a159
v_pk_mul_f32 v[104:105], v[104:105], v[50:51] op_sel_hi:[1,0]
v_pk_mul_f32 v[106:107], v[106:107], v[50:51] op_sel_hi:[1,0]
v_pk_mul_f32 v[108:109], v[108:109], v[50:51] op_sel_hi:[1,0]
v_pk_mul_f32 v[110:111], v[110:111], v[50:51] op_sel_hi:[1,0]
C_ 92,104,105
C_ 93,106,107
C_ 94,108,109
C_ 95,110,111
X_ a136,v92
X_ a137,v93
X_ a138,v94
X_ a139,v95
.p2align 3
R_ v96,a160
R_ v97,a161
R_ v98,a162
R_ v99,a163
R_ v100,a180
R_ v101,a181
R_ v102,a182
R_ v103,a183
v_pk_mul_f32 v[96:97], v[96:97], v[46:47] op_sel_hi:[1,0]
v_pk_mul_f32 v[98:99], v[98:99], v[46:47] op_sel_hi:[1,0]
v_pk_mul_f32 v[100:101], v[100:101], v[46:47] op_sel_hi:[1,0]
v_pk_mul_f32 v[102:103], v[102:103], v[46:47] op_sel_hi:[1,0]
C_ 76,96,97
C_ 77,98,99
C_ 78,100,101
C_ 79,102,103
X_ a160,v76
X_ a161,v77
X_ a162,v78
X_ a163,v79
.p2align 3
R_ v104,a164
R_ v105,a165
R_ v106,a166
R_ v107,a167
R_ v108,a184
R_ v109,a185
R_ v110,a186
R_ v111,a187
v_pk_mul_f32 v[104:105], v[104:105], v[46:47] op_sel:[0,1]
v_pk_mul_f32 v[106:107], v[106:107], v[46:47] op_sel:[0,1]
v_pk_mul_f32 v[108:109], v[108:109], v[46:47] op_sel:[0,1]
v_pk_mul_f32 v[110:111], v[110:111], v[46:47] op_sel:[0,1]
C_ 80,104,105
C_ 81,106,107
C_ 82,108,109
C_ 83,110,111
X_ a164,v80
X_ a165,v81
X_ a166,v82
X_ a167,v83
.p2align 3
R_ v96,a168
R_ v97,a169
R_ v98,a170
R_ v99,a171
R_ v100,a188
R_ v101,a189
R_ v102,a190
R_ v103,a191
v_pk_mul_f32 v[96:97], v[96:97], v[48:49] op_sel_hi:[1,0]
v_pk_mul_f32 v[98:99], v[98:99], v[48:49] op_sel_hi:[1,0]
v_pk_mul_f32 v[100:101], v[100:101], v[48:49] op_sel_hi:[1,0]
v_pk_mul_f32 v[102:103], v[102:103], v[48:49] op_sel_hi:[1,0]
C_ 84,96,97
C_ 85,98,99
C_ 86,100,101
C_ 87,102,103
X_ a168,v84
X_ a169,v85
X_ a170,v86
X_ a171,v87
.p2align 3
R_ v104,a172
R_ v105,a173
R_ v106,a174
R_ v107,a175
R_ v108,a192
R_ v109,a193
R_ v110,a194
R_ v111,a195
v_pk_mul_f32 v[104:105], v[104:105], v[48:49] op_sel:[0,1]
v_pk_mul_f32 v[106:107], v[106:107], v[48:49] op_sel:[0,1]
v_pk_mul_f32 v[108:109], v[108:109], v[48:49] op_sel:[0,1]
v_pk_mul_f32 v[110:111], v[110:111], v[48:49] op_sel:[0,1]
C_ 88,104,105
C_ 89,106,107
C_ 90,108,109
C_ 91,110,111
X_ a172,v88
X_ a173,v89
X_ a174,v90
X_ a175,v91
.p2align 3
R_ v96,a176
R_ v97,a177
R_ v98,a178
R_ v99,a179
R_ v100,a196
R_ v101,a197
R_ v102,a198
R_ v103,a199
v_pk_mul_f32 v[96:97], v[96:97], v[50:51] op_sel_hi:[1,0]
v_pk_mul_f32 v[98:99], v[98:99], v[50:51] op_sel_hi:[1,0]
v_pk_mul_f32 v[100:101], v[100:101], v[50:51] op_sel_hi:[1,0]
v_pk_mul_f32 v[102:103], v[102:103], v[50:51] op_sel_hi:[1,0]
C_ 92,96,97
C_ 93,98,99
C_ 94,100,101
C_ 95,102,103
X_ a176,v92
X_ a177,v93
X_ a178,v94
X_ a179,v95
.p2align 3
R_ v104,a200
R_ v105,a201
R_ v106,a202
R_ v107,a203
R_ v108,a220
R_ v109,a221
R_ v110,a222
R_ v111,a223
v_pk_mul_f32 v[104:105], v[104:105], v[46:47] op_sel_hi:[1,0]
v_pk_mul_f32 v[106:107], v[106:107], v[46:47] op_sel_hi:[1,0]
v_pk_mul_f32 v[108:109], v[108:109], v[46:47] op_sel_hi:[1,0]
v_pk_mul_f32 v[110:111], v[110:111], v[46:47] op_sel_hi:[1,0]
C_ 76,104,105
C_ 77,106,107
C_ 78,108,109
C_ 79,110,111
X_ a200,v76
X_ a201,v77
X_ a202,v78
X_ a203,v79
.p2align 3
R_ v96,a204
R_ v97,a205
R_ v98,a206
R_ v99,a207
R_ v100,a224
R_ v101,a225
R_ v102,a226
R_ v103,a227
v_pk_mul_f32 v[96:97], v[96:97], v[46:47] op_sel:[0,1]
v_pk_mul_f32 v[98:99], v[98:99], v[46:47] op_sel:[0,1]
v_pk_mul_f32 v[100:101], v[100:101], v[46:47] op_sel:[0,1]
v_pk_mul_f32 v[102:103], v[102:103], v[46:47] op_sel:[0,1]
C_ 80,96,97
C_ 81,98,99
C_ 82,100,101
C_ 83,102,103
X_ a204,v80
X_ a205,v81
X_ a206,v82
X_ a207,v83
v_mov_b32 v4, v20
v_mov_b32 v5, v21
R_ %0,a242
R_ v2,a243
v_xor_b32 v1, 64, %0
v_xor_b32 v3, 64, v2
s_waitcnt vmcnt(0) lgkmcnt(0)
s_barrier
Ta 20,%0,8192
Ta 22,%0,8704
Ta 24,%0,12288
Ta 26,%0,12800
Da 60,v2,0
Da 100,v3,0
Da 64,v2,2048
Da 104,v3,2048
Da 68,v2,4096
Da 108,v3,4096
Da 72,v2,6144
Da 112,v3,6144
Da 76,v2,8192
Da 116,v3,8192
Ta 28,%0,8200
Ta 30,%0,8712
Ta 32,%0,12296
Ta 34,%0,12808
.p2align 3
R_ v104,a208
R_ v105,a209
R_ v106,a210
R_ v107,a211
R_ v108,a228
R_ v109,a229
R_ v110,a230
R_ v111,a231
v_pk_mul_f32 v[104:105], v[104:105], v[48:49] op_sel_hi:[1,0]
v_pk_mul_f32 v[106:107], v[106:107], v[48:49] op_sel_hi:[1,0]
v_pk_mul_f32 v[108:109], v[108:109], v[48:49] op_sel_hi:[1,0]
v_pk_mul_f32 v[110:111], v[110:111], v[48:49] op_sel_hi:[1,0]
C_ 84,104,105
C_ 85,106,107
C_ 86,108,109
C_ 87,110,111
X_ a208,v84
X_ a209,v85
X_ a210,v86
X_ a211,v87
.p2align 3
R_ v96,a212
R_ v97,a213
R_ v98,a214
R_ v99,a215
R_ v100,a232
R_ v101,a233
R_ v102,a234
R_ v103,a235
v_pk_mul_f32 v[96:97], v[96:97], v[48:49] op_sel:[0,1]
v_pk_mul_f32 v[98:99], v[98:99], v[48:49] op_sel:[0,1]
v_pk_mul_f32 v[100:101], v[100:101], v[48:49] op_sel:[0,1]
v_pk_mul_f32 v[102:103], v[102:103], v[48:49] op_sel:[0,1]
C_ 88,96,97
C_ 89,98,99
C_ 90,100,101
C_ 91,102,103
X_ a212,v88
X_ a213,v89
X_ a214,v90
X_ a215,v91
.p2align 3
R_ v104,a216
R_ v105,a217
R_ v106,a218
R_ v107,a219
R_ v108,a236
R_ v109,a237
R_ v110,a238
R_ v111,a239
v_pk_mul_f32 v[104:105], v[104:105], v[50:51] op_sel_hi:[1,0]
v_pk_mul_f32 v[106:107], v[106:107], v[50:51] op_sel_hi:[1,0]
v_pk_mul_f32 v[108:109], v[108:109], v[50:51] op_sel_hi:[1,0]
v_pk_mul_f32 v[110:111], v[110:111], v[50:51] op_sel_hi:[1,0]
C_ 92,104,105
C_ 93,106,107
C_ 94,108,109
C_ 95,110,111
X_ a216,v92
X_ a217,v93
X_ a218,v94
X_ a219,v95
s_branch .Lq
.Ltramp0_1:
s_mov_b32 s63, 1
s_branch .Lslow1
.Ltramp0_2:
s_mov_b32 s63, 2
s_branch .Lslow0
.Ltramp0_3:
s_mov_b32 s63, 3
s_branch .Lslow1
.Ltramp0_4:
s_mov_b32 s63, 4
s_branch .Lslow0
.Ltramp0_5:
s_mov_b32 s63, 5
s_branch .Lslow1
.Ltramp0_6:
s_mov_b32 s63, 6
s_branch .Lslow0
.Ltramp0_7:
s_mov_b32 s63, 7
s_branch .Lslow1
.Ltramp0_8:
s_mov_b32 s63, 8
s_branch .Lslow0
.Ltramp0_9:
s_mov_b32 s63, 9
s_branch .Lslow1
.Ltramp0_10:
s_mov_b32 s63, 10
s_branch .Lslow0
.Ltramp0_11:
s_mov_b32 s63, 11
s_branch .Lslow1
.Ltramp0_12:
s_mov_b32 s63, 12
s_branch .Lslow0
.Ltramp0_13:
s_mov_b32 s63, 13
s_branch .Lslow1
.Ltramp0_14:
s_mov_b32 s63, 14
s_branch .Lslow0
.Ltramp0_15:
s_mov_b32 s63, 15
s_branch .Lslow1
.Lstep0_16:
Zvvv 76,92,116
Zvvv 80,92,120
Zvvv 84,92,124
Zvvv 88,92,128
Zvvv 92,92,132
s_nop 5
v_max3_f32 v10, v76, v80, v84
s_nop 0
v_max3_f32 v10, v10, v88, v92
v_cmp_lt_f32 vcc, 0x42000000, v10
s_cbranch_vccnz .Ltramp0_15
s_branch .Ljoin0
.Ltramp1_1:
s_mov_b32 s63, 1
s_branch .Lslow1
.Ltramp1_2:
s_mov_b32 s63, 2
s_branch .Lslow0
.Ltramp1_3:
s_mov_b32 s63, 3
s_branch .Lslow1
.Ltramp1_4:
s_mov_b32 s63, 4
s_branch .Lslow0
.Ltramp1_5:
s_mov_b32 s63, 5
s_branch .Lslow1
.Ltramp1_6:
s_mov_b32 s63, 6
s_branch .Lslow0
.Ltramp1_7:
s_mov_b32 s63, 7
s_branch .Lslow1
.Ltramp1_8:
s_mov_b32 s63, 8
s_branch .Lslow0
.Ltramp1_9:
s_mov_b32 s63, 9
s_branch .Lslow1
.Ltramp1_10:
s_mov_b32 s63, 10
s_branch .Lslow0
.Ltramp1_11:
s_mov_b32 s63, 11
s_branch .Lslow1
.Ltramp1_12:
s_mov_b32 s63, 12
s_branch .Lslow0
.Ltramp1_13:
s_mov_b32 s63, 13
s_branch .Lslow1
.Ltramp1_14:
s_mov_b32 s63, 14
s_branch .Lslow0
.Ltramp1_15:
s_mov_b32 s63, 15
s_branch .Lslow1
.Lstep1_16:
Zvvv 76,92,116
Zvvv 80,92,120
Zvvv 84,92,124
Zvvv 88,92,128
Zvvv 92,92,132
s_nop 5
v_max3_f32 v10, v76, v80, v84
s_nop 0
v_max3_f32 v10, v10, v88, v92
v_cmp_lt_f32 vcc, 0x42000000, v10
s_cbranch_vccnz .Ltramp1_15
s_branch .Ljoin1
.Ltramp2_1:
s_mov_b32 s63, 1
s_branch .Lslow1
.Ltramp2_2:
s_mov_b32 s63, 2
s_branch .Lslow0
.Ltramp2_3:
s_mov_b32 s63, 3
s_branch .Lslow1
.Ltramp2_4:
s_mov_b32 s63, 4
s_branch .Lslow0
.Ltramp2_5:
s_mov_b32 s63, 5
s_branch .Lslow1
.Ltramp2_6:
s_mov_b32 s63, 6
s_branch .Lslow0
.Ltramp2_7:
s_mov_b32 s63, 7
s_branch .Lslow1
.Ltramp2_8:
s_mov_b32 s63, 8
s_branch .Lslow0
.Ltramp2_9:
s_mov_b32 s63, 9
s_branch .Lslow1
.Ltramp2_10:
s_mov_b32 s63, 10
s_branch .Lslow0
.Ltramp2_11:
s_mov_b32 s63, 11
s_branch .Lslow1
.Ltramp2_12:
s_mov_b32 s63, 12
s_branch .Lslow0
.Ltramp2_13:
s_mov_b32 s63, 13
s_branch .Lslow1
.Ltramp2_14:
s_mov_b32 s63, 14
s_branch .Lslow0
.Ltramp2_15:
s_mov_b32 s63, 15
s_branch .Lslow1
.Lstep2_16:
Zvvv 76,92,116
Zvvv 80,92,120
Zvvv 84,92,124
Zvvv 88,92,128
Zvvv 92,92,132
s_nop 5
v_max3_f32 v10, v76, v80, v84
s_nop 0
v_max3_f32 v10, v10, v88, v92
v_cmp_lt_f32 vcc, 0x42000000, v10
s_cbranch_vccnz .Ltramp2_15
s_branch .Ljoin2
.Lslow0:
W_ 0
s_nop 7
s_nop 7
s_and_b32 s64, s61, 4294959104
s_mul_i32 s53, s63, 12288
S_ s64,s64,s53
R_ v11,a248
v_and_b32 v10, 0xf0, v11
v_mul_u32_u24 v10, 24, v10
v_lshrrev_b32 v11, 8, v11
v_lshl_add_u32 v10, v11, 4, v10
S_ s53,s64,6144
buffer_load_dwordx4 v[116:119], v10, s[32:35], s53 offen offset:0
S_ s53,s64,6144
buffer_load_dwordx4 v[120:123], v10, s[32:35], s53 offen offset:64
S_ s53,s64,6144
buffer_load_dwordx4 v[124:127], v10, s[32:35], s53 offen offset:128
S_ s53,s64,6144
buffer_load_dwordx4 v[128:131], v10, s[32:35], s53 offen offset:192
s_waitcnt vmcnt(0)
Nvvv 96,116,136,56
Nvvv 100,116,140,60
Nvvv 104,116,144,64
Nvvv 108,116,148,68
Nvvv 112,116,152,72
Mvvv 96,120,156
Mvvv 100,120,160
Mvvv 104,120,164
Mvvv 108,120,168
Mvvv 112,120,172
Mvvv 96,124,176
Mvvv 100,124,180
Mvvv 104,124,184
Mvvv 108,124,188
Mvvv 112,124,192
Mvvv 96,128,196
Mvvv 100,128,200
Mvvv 104,128,204
)A"
R"A(Mvvv 108,128,208
Mvvv 112,128,212
s_nop 7
S_ s53,s64,6144
buffer_load_dwordx4 v[116:119], v10, s[32:35], s53 offen offset:256
S_ s53,s64,6144
buffer_load_dwordx4 v[120:123], v10, s[32:35], s53 offen offset:320
S_ s53,s64,0
buffer_load_dwordx4 v[124:127], v10, s[32:35], s53 offen offset:0
S_ s53,s64,0
buffer_load_dwordx4 v[128:131], v10, s[32:35], s53 offen offset:64
s_waitcnt vmcnt(0)
Mvvv 96,116,216
Mvvv 100,116,220
Mvvv 104,116,224
Mvvv 108,116,228
Mvvv 112,116,232
Mvvv 96,120,236
Mvvv 100,120,240
Mvvv 104,120,244
Mvvv 108,120,248
Mvvv 112,120,252
Nvvv 76,124,136,56
Nvvv 80,124,140,60
Nvvv 84,124,144,64
Nvvv 88,124,148,68
Nvvv 92,124,152,72
Mvvv 76,128,156
Mvvv 80,128,160
Mvvv 84,128,164
Mvvv 88,128,168
Mvvv 92,128,172
s_nop 7
S_ s53,s64,0
buffer_load_dwordx4 v[116:119], v10, s[32:35], s53 offen offset:128
S_ s53,s64,0
buffer_load_dwordx4 v[120:123], v10, s[32:35], s53 offen offset:192
S_ s53,s64,0
buffer_load_dwordx4 v[124:127], v10, s[32:35], s53 offen offset:256
S_ s53,s64,0
buffer_load_dwordx4 v[128:131], v10, s[32:35], s53 offen offset:320
s_waitcnt vmcnt(0)
Mvvv 76,116,176
Mvvv 80,116,180
Mvvv 84,116,184
Mvvv 88,116,188
Mvvv 92,116,192
Mvvv 76,120,196
Mvvv 80,120,200
Mvvv 84,120,204
Mvvv 88,120,208
Mvvv 92,120,212
Mvvv 76,124,216
Mvvv 80,124,220
Mvvv 84,124,224
Mvvv 88,124,228
Mvvv 92,124,232
Mvvv 76,128,236
Mvvv 80,128,240
Mvvv 84,128,244
Mvvv 88,128,248
Mvvv 92,128,252
s_nop 7
s_nop 2
v_max3_f32 v10, v76, v77, v78
v_max3_f32 v10, v10, v79, v96
v_max3_f32 v10, v10, v97, v98
v_max_f32 v10, v10, v99
v_mov_b32 v11, v10
s_nop 1
v_permlane16_swap_b32 v10, v11
v_max_f32 v10, v10, v11
v_mov_b32 v11, v10
s_nop 1
v_permlane32_swap_b32 v10, v11
v_max_f32 v10, v10, v11
A_ v10,0x40e00000,v10
v_max_f32 v10, 0, v10
v_exp_f32 v11, -v10
s_nop 0
U_ v46,v46,v11
R_ v12,a0
U_ v12,v12,v11
X_ a0,v12
R_ v13,a1
U_ v13,v13,v11
X_ a1,v13
R_ v12,a2
U_ v12,v12,v11
X_ a2,v12
R_ v13,a3
U_ v13,v13,v11
X_ a3,v13
R_ v12,a20
U_ v12,v12,v11
X_ a20,v12
R_ v13,a21
U_ v13,v13,v11
X_ a21,v13
R_ v12,a22
U_ v12,v12,v11
X_ a22,v12
R_ v13,a23
U_ v13,v13,v11
X_ a23,v13
R_ v12,a40
U_ v12,v12,v11
X_ a40,v12
R_ v13,a41
U_ v13,v13,v11
X_ a41,v13
R_ v12,a42
U_ v12,v12,v11
X_ a42,v12
R_ v13,a43
U_ v13,v13,v11
X_ a43,v13
R_ v12,a60
U_ v12,v12,v11
X_ a60,v12
R_ v13,a61
U_ v13,v13,v11
X_ a61,v13
R_ v12,a62
U_ v12,v12,v11
X_ a62,v12
R_ v13,a63
U_ v13,v13,v11
X_ a63,v13
R_ v12,a80
U_ v12,v12,v11
X_ a80,v12
R_ v13,a81
U_ v13,v13,v11
X_ a81,v13
R_ v12,a82
U_ v12,v12,v11
X_ a82,v12
R_ v13,a83
U_ v13,v13,v11
X_ a83,v13
R_ v12,a100
U_ v12,v12,v11
X_ a100,v12
R_ v13,a101
U_ v13,v13,v11
X_ a101,v13
R_ v12,a102
U_ v12,v12,v11
X_ a102,v12
R_ v13,a103
U_ v13,v13,v11
X_ a103,v13
R_ v12,a120
U_ v12,v12,v11
X_ a120,v12
R_ v13,a121
U_ v13,v13,v11
X_ a121,v13
R_ v12,a122
U_ v12,v12,v11
X_ a122,v12
R_ v13,a123
U_ v13,v13,v11
X_ a123,v13
R_ v12,a140
U_ v12,v12,v11
X_ a140,v12
R_ v13,a141
U_ v13,v13,v11
X_ a141,v13
R_ v12,a142
U_ v12,v12,v11
X_ a142,v12
R_ v13,a143
U_ v13,v13,v11
X_ a143,v13
R_ v12,a160
U_ v12,v12,v11
X_ a160,v12
R_ v13,a161
U_ v13,v13,v11
X_ a161,v13
R_ v12,a162
U_ v12,v12,v11
X_ a162,v12
R_ v13,a163
U_ v13,v13,v11
X_ a163,v13
R_ v12,a180
U_ v12,v12,v11
X_ a180,v12
R_ v13,a181
U_ v13,v13,v11
X_ a181,v13
R_ v12,a182
U_ v12,v12,v11
X_ a182,v12
R_ v13,a183
U_ v13,v13,v11
X_ a183,v13
R_ v12,a200
U_ v12,v12,v11
X_ a200,v12
R_ v13,a201
U_ v13,v13,v11
X_ a201,v13
R_ v12,a202
U_ v12,v12,v11
X_ a202,v12
R_ v13,a203
U_ v13,v13,v11
X_ a203,v13
R_ v12,a220
U_ v12,v12,v11
X_ a220,v12
R_ v13,a221
U_ v13,v13,v11
X_ a221,v13
R_ v12,a222
U_ v12,v12,v11
X_ a222,v12
R_ v13,a223
U_ v13,v13,v11
X_ a223,v13
B_ v56,v56,v10
B_ v57,v57,v10
B_ v58,v58,v10
B_ v59,v59,v10
B_ v76,v76,v10
B_ v77,v77,v10
B_ v78,v78,v10
B_ v79,v79,v10
B_ v96,v96,v10
B_ v97,v97,v10
B_ v98,v98,v10
B_ v99,v99,v10
E_ 76
E_ 77
E_ 78
E_ 79
E_ 96
E_ 97
E_ 98
E_ 99
s_nop 0
C_ 99,98,99
C_ 98,96,97
C_ 96,76,77
C_ 97,78,79
v_max3_f32 v10, v80, v81, v82
v_max3_f32 v10, v10, v83, v100
v_max3_f32 v10, v10, v101, v102
v_max_f32 v10, v10, v103
v_mov_b32 v11, v10
s_nop 1
v_permlane16_swap_b32 v10, v11
v_max_f32 v10, v10, v11
v_mov_b32 v11, v10
s_nop 1
v_permlane32_swap_b32 v10, v11
v_max_f32 v10, v10, v11
A_ v10,0x40e00000,v10
v_max_f32 v10, 0, v10
v_exp_f32 v11, -v10
s_nop 0
U_ v47,v47,v11
R_ v12,a4
U_ v12,v12,v11
X_ a4,v12
R_ v13,a5
U_ v13,v13,v11
X_ a5,v13
R_ v12,a6
U_ v12,v12,v11
X_ a6,v12
R_ v13,a7
U_ v13,v13,v11
X_ a7,v13
R_ v12,a24
U_ v12,v12,v11
X_ a24,v12
R_ v13,a25
U_ v13,v13,v11
X_ a25,v13
R_ v12,a26
U_ v12,v12,v11
X_ a26,v12
R_ v13,a27
U_ v13,v13,v11
X_ a27,v13
R_ v12,a44
U_ v12,v12,v11
X_ a44,v12
R_ v13,a45
U_ v13,v13,v11
X_ a45,v13
R_ v12,a46
U_ v12,v12,v11
X_ a46,v12
R_ v13,a47
U_ v13,v13,v11
X_ a47,v13
R_ v12,a64
U_ v12,v12,v11
X_ a64,v12
R_ v13,a65
U_ v13,v13,v11
X_ a65,v13
R_ v12,a66
U_ v12,v12,v11
X_ a66,v12
R_ v13,a67
U_ v13,v13,v11
X_ a67,v13
R_ v12,a84
U_ v12,v12,v11
X_ a84,v12
R_ v13,a85
U_ v13,v13,v11
X_ a85,v13
R_ v12,a86
U_ v12,v12,v11
X_ a86,v12
R_ v13,a87
U_ v13,v13,v11
X_ a87,v13
R_ v12,a104
U_ v12,v12,v11
X_ a104,v12
R_ v13,a105
U_ v13,v13,v11
X_ a105,v13
R_ v12,a106
U_ v12,v12,v11
X_ a106,v12
R_ v13,a107
U_ v13,v13,v11
X_ a107,v13
R_ v12,a124
U_ v12,v12,v11
X_ a124,v12
R_ v13,a125
U_ v13,v13,v11
X_ a125,v13
R_ v12,a126
U_ v12,v12,v11
X_ a126,v12
R_ v13,a127
U_ v13,v13,v11
X_ a127,v13
R_ v12,a144
U_ v12,v12,v11
X_ a144,v12
R_ v13,a145
U_ v13,v13,v11
X_ a145,v13
R_ v12,a146
U_ v12,v12,v11
X_ a146,v12
R_ v13,a147
U_ v13,v13,v11
X_ a147,v13
R_ v12,a164
U_ v12,v12,v11
X_ a164,v12
R_ v13,a165
U_ v13,v13,v11
X_ a165,v13
R_ v12,a166
U_ v12,v12,v11
X_ a166,v12
R_ v13,a167
U_ v13,v13,v11
X_ a167,v13
R_ v12,a184
U_ v12,v12,v11
X_ a184,v12
R_ v13,a185
U_ v13,v13,v11
X_ a185,v13
R_ v12,a186
U_ v12,v12,v11
X_ a186,v12
R_ v13,a187
U_ v13,v13,v11
X_ a187,v13
R_ v12,a204
U_ v12,v12,v11
X_ a204,v12
R_ v13,a205
U_ v13,v13,v11
X_ a205,v13
R_ v12,a206
U_ v12,v12,v11
X_ a206,v12
R_ v13,a207
U_ v13,v13,v11
X_ a207,v13
R_ v12,a224
U_ v12,v12,v11
X_ a224,v12
R_ v13,a225
U_ v13,v13,v11
X_ a225,v13
R_ v12,a226
U_ v12,v12,v11
X_ a226,v12
R_ v13,a227
U_ v13,v13,v11
X_ a227,v13
B_ v60,v60,v10
B_ v61,v61,v10
B_ v62,v62,v10
B_ v63,v63,v10
B_ v80,v80,v10
B_ v81,v81,v10
B_ v82,v82,v10
B_ v83,v83,v10
B_ v100,v100,v10
B_ v101,v101,v10
B_ v102,v102,v10
B_ v103,v103,v10
E_ 80
E_ 81
E_ 82
E_ 83
E_ 100
E_ 101
E_ 102
E_ 103
s_nop 0
C_ 103,102,103
C_ 102,100,101
C_ 100,80,81
C_ 101,82,83
v_max3_f32 v10, v84, v85, v86
v_max3_f32 v10, v10, v87, v104
v_max3_f32 v10, v10, v105, v106
v_max_f32 v10, v10, v107
v_mov_b32 v11, v10
s_nop 1
v_permlane16_swap_b32 v10, v11
v_max_f32 v10, v10, v11
v_mov_b32 v11, v10
s_nop 1
v_permlane32_swap_b32 v10, v11
v_max_f32 v10, v10, v11
A_ v10,0x40e00000,v10
v_max_f32 v10, 0, v10
v_exp_f32 v11, -v10
s_nop 0
U_ v48,v48,v11
R_ v12,a8
U_ v12,v12,v11
X_ a8,v12
R_ v13,a9
U_ v13,v13,v11
X_ a9,v13
R_ v12,a10
U_ v12,v12,v11
X_ a10,v12
R_ v13,a11
U_ v13,v13,v11
X_ a11,v13
R_ v12,a28
U_ v12,v12,v11
X_ a28,v12
R_ v13,a29
U_ v13,v13,v11
X_ a29,v13
R_ v12,a30
U_ v12,v12,v11
X_ a30,v12
R_ v13,a31
U_ v13,v13,v11
X_ a31,v13
R_ v12,a48
U_ v12,v12,v11
X_ a48,v12
R_ v13,a49
U_ v13,v13,v11
X_ a49,v13
R_ v12,a50
U_ v12,v12,v11
X_ a50,v12
R_ v13,a51
U_ v13,v13,v11
X_ a51,v13
R_ v12,a68
U_ v12,v12,v11
X_ a68,v12
R_ v13,a69
U_ v13,v13,v11
X_ a69,v13
R_ v12,a70
U_ v12,v12,v11
X_ a70,v12
R_ v13,a71
U_ v13,v13,v11
X_ a71,v13
R_ v12,a88
U_ v12,v12,v11
X_ a88,v12
R_ v13,a89
U_ v13,v13,v11
X_ a89,v13
R_ v12,a90
U_ v12,v12,v11
X_ a90,v12
R_ v13,a91
U_ v13,v13,v11
X_ a91,v13
R_ v12,a108
U_ v12,v12,v11
X_ a108,v12
R_ v13,a109
U_ v13,v13,v11
X_ a109,v13
R_ v12,a110
U_ v12,v12,v11
X_ a110,v12
R_ v13,a111
U_ v13,v13,v11
X_ a111,v13
R_ v12,a128
U_ v12,v12,v11
X_ a128,v12
R_ v13,a129
U_ v13,v13,v11
X_ a129,v13
R_ v12,a130
U_ v12,v12,v11
X_ a130,v12
R_ v13,a131
U_ v13,v13,v11
X_ a131,v13
R_ v12,a148
U_ v12,v12,v11
X_ a148,v12
R_ v13,a149
U_ v13,v13,v11
X_ a149,v13
R_ v12,a150
U_ v12,v12,v11
X_ a150,v12
R_ v13,a151
U_ v13,v13,v11
X_ a151,v13
R_ v12,a168
U_ v12,v12,v11
X_ a168,v12
R_ v13,a169
U_ v13,v13,v11
X_ a169,v13
R_ v12,a170
U_ v12,v12,v11
X_ a170,v12
R_ v13,a171
U_ v13,v13,v11
X_ a171,v13
R_ v12,a188
U_ v12,v12,v11
X_ a188,v12
R_ v13,a189
U_ v13,v13,v11
X_ a189,v13
R_ v12,a190
U_ v12,v12,v11
X_ a190,v12
R_ v13,a191
U_ v13,v13,v11
X_ a191,v13
R_ v12,a208
U_ v12,v12,v11
X_ a208,v12
R_ v13,a209
U_ v13,v13,v11
X_ a209,v13
R_ v12,a210
U_ v12,v12,v11
X_ a210,v12
R_ v13,a211
U_ v13,v13,v11
X_ a211,v13
R_ v12,a228
U_ v12,v12,v11
X_ a228,v12
R_ v13,a229
U_ v13,v13,v11
X_ a229,v13
R_ v12,a230
U_ v12,v12,v11
X_ a230,v12
R_ v13,a231
U_ v13,v13,v11
X_ a231,v13
B_ v64,v64,v10
B_ v65,v65,v10
B_ v66,v66,v10
B_ v67,v67,v10
B_ v84,v84,v10
B_ v85,v85,v10
B_ v86,v86,v10
B_ v87,v87,v10
B_ v104,v104,v10
B_ v105,v105,v10
B_ v106,v106,v10
B_ v107,v107,v10
E_ 84
E_ 85
E_ 86
E_ 87
E_ 104
E_ 105
E_ 106
E_ 107
s_nop 0
C_ 107,106,107
C_ 106,104,105
C_ 104,84,85
C_ 105,86,87
v_max3_f32 v10, v88, v89, v90
v_max3_f32 v10, v10, v91, v108
v_max3_f32 v10, v10, v109, v110
v_max_f32 v10, v10, v111
v_mov_b32 v11, v10
s_nop 1
v_permlane16_swap_b32 v10, v11
v_max_f32 v10, v10, v11
v_mov_b32 v11, v10
s_nop 1
v_permlane32_swap_b32 v10, v11
v_max_f32 v10, v10, v11
A_ v10,0x40e00000,v10
v_max_f32 v10, 0, v10
v_exp_f32 v11, -v10
s_nop 0
U_ v49,v49,v11
R_ v12,a12
U_ v12,v12,v11
X_ a12,v12
R_ v13,a13
U_ v13,v13,v11
X_ a13,v13
R_ v12,a14
U_ v12,v12,v11
X_ a14,v12
R_ v13,a15
U_ v13,v13,v11
X_ a15,v13
R_ v12,a32
U_ v12,v12,v11
X_ a32,v12
R_ v13,a33
U_ v13,v13,v11
X_ a33,v13
R_ v12,a34
U_ v12,v12,v11
X_ a34,v12
R_ v13,a35
U_ v13,v13,v11
X_ a35,v13
R_ v12,a52
U_ v12,v12,v11
X_ a52,v12
R_ v13,a53
U_ v13,v13,v11
X_ a53,v13
R_ v12,a54
U_ v12,v12,v11
X_ a54,v12
R_ v13,a55
U_ v13,v13,v11
X_ a55,v13
R_ v12,a72
U_ v12,v12,v11
X_ a72,v12
R_ v13,a73
U_ v13,v13,v11
X_ a73,v13
R_ v12,a74
U_ v12,v12,v11
X_ a74,v12
R_ v13,a75
U_ v13,v13,v11
X_ a75,v13
R_ v12,a92
U_ v12,v12,v11
X_ a92,v12
R_ v13,a93
U_ v13,v13,v11
X_ a93,v13
R_ v12,a94
U_ v12,v12,v11
X_ a94,v12
R_ v13,a95
U_ v13,v13,v11
X_ a95,v13
R_ v12,a112
U_ v12,v12,v11
X_ a112,v12
R_ v13,a113
U_ v13,v13,v11
X_ a113,v13
R_ v12,a114
U_ v12,v12,v11
X_ a114,v12
R_ v13,a115
U_ v13,v13,v11
X_ a115,v13
R_ v12,a132
U_ v12,v12,v11
X_ a132,v12
R_ v13,a133
U_ v13,v13,v11
X_ a133,v13
R_ v12,a134
U_ v12,v12,v11
X_ a134,v12
R_ v13,a135
U_ v13,v13,v11
X_ a135,v13
R_ v12,a152
U_ v12,v12,v11
X_ a152,v12
R_ v13,a153
U_ v13,v13,v11
X_ a153,v13
R_ v12,a154
U_ v12,v12,v11
X_ a154,v12
R_ v13,a155
U_ v13,v13,v11
X_ a155,v13
R_ v12,a172
U_ v12,v12,v11
X_ a172,v12
R_ v13,a173
U_ v13,v13,v11
X_ a173,v13
R_ v12,a174
U_ v12,v12,v11
X_ a174,v12
R_ v13,a175
U_ v13,v13,v11
X_ a175,v13
R_ v12,a192
U_ v12,v12,v11
X_ a192,v12
R_ v13,a193
U_ v13,v13,v11
X_ a193,v13
R_ v12,a194
U_ v12,v12,v11
X_ a194,v12
R_ v13,a195
U_ v13,v13,v11
X_ a195,v13
R_ v12,a212
U_ v12,v12,v11
X_ a212,v12
R_ v13,a213
U_ v13,v13,v11
X_ a213,v13
R_ v12,a214
U_ v12,v12,v11
X_ a214,v12
R_ v13,a215
U_ v13,v13,v11
X_ a215,v13
R_ v12,a232
U_ v12,v12,v11
X_ a232,v12
R_ v13,a233
U_ v13,v13,v11
X_ a233,v13
R_ v12,a234
U_ v12,v12,v11
X_ a234,v12
R_ v13,a235
U_ v13,v13,v11
X_ a235,v13
B_ v68,v68,v10
B_ v69,v69,v10
B_ v70,v70,v10
B_ v71,v71,v10
B_ v88,v88,v10
B_ v89,v89,v10
B_ v90,v90,v10
B_ v91,v91,v10
B_ v108,v108,v10
B_ v109,v109,v10
B_ v110,v110,v10
B_ v111,v111,v10
E_ 88
E_ 89
E_ 90
E_ 91
E_ 108
E_ 109
E_ 110
E_ 111
s_nop 0
C_ 111,110,111
C_ 110,108,109
C_ 108,88,89
C_ 109,90,91
v_max3_f32 v10, v92, v93, v94
v_max3_f32 v10, v10, v95, v112
v_max3_f32 v10, v10, v113, v114
v_max_f32 v10, v10, v115
v_mov_b32 v11, v10
s_nop 1
v_permlane16_swap_b32 v10, v11
v_max_f32 v10, v10, v11
v_mov_b32 v11, v10
s_nop 1
v_permlane32_swap_b32 v10, v11
v_max_f32 v10, v10, v11
A_ v10,0x40e00000,v10
v_max_f32 v10, 0, v10
v_exp_f32 v11, -v10
s_nop 0
U_ v50,v50,v11
R_ v12,a16
U_ v12,v12,v11
X_ a16,v12
R_ v13,a17
U_ v13,v13,v11
X_ a17,v13
R_ v12,a18
U_ v12,v12,v11
X_ a18,v12
R_ v13,a19
U_ v13,v13,v11
X_ a19,v13
R_ v12,a36
U_ v12,v12,v11
X_ a36,v12
R_ v13,a37
U_ v13,v13,v11
X_ a37,v13
R_ v12,a38
U_ v12,v12,v11
X_ a38,v12
R_ v13,a39
U_ v13,v13,v11
X_ a39,v13
R_ v12,a56
U_ v12,v12,v11
X_ a56,v12
R_ v13,a57
U_ v13,v13,v11
X_ a57,v13
R_ v12,a58
U_ v12,v12,v11
X_ a58,v12
R_ v13,a59
U_ v13,v13,v11
X_ a59,v13
R_ v12,a76
U_ v12,v12,v11
X_ a76,v12
R_ v13,a77
U_ v13,v13,v11
X_ a77,v13
R_ v12,a78
U_ v12,v12,v11
X_ a78,v12
R_ v13,a79
U_ v13,v13,v11
X_ a79,v13
R_ v12,a96
U_ v12,v12,v11
X_ a96,v12
R_ v13,a97
U_ v13,v13,v11
X_ a97,v13
R_ v12,a98
U_ v12,v12,v11
X_ a98,v12
R_ v13,a99
U_ v13,v13,v11
X_ a99,v13
R_ v12,a116
U_ v12,v12,v11
X_ a116,v12
R_ v13,a117
U_ v13,v13,v11
X_ a117,v13
R_ v12,a118
U_ v12,v12,v11
X_ a118,v12
R_ v13,a119
U_ v13,v13,v11
X_ a119,v13
R_ v12,a136
U_ v12,v12,v11
X_ a136,v12
R_ v13,a137
U_ v13,v13,v11
X_ a137,v13
R_ v12,a138
U_ v12,v12,v11
X_ a138,v12
R_ v13,a139
U_ v13,v13,v11
X_ a139,v13
R_ v12,a156
U_ v12,v12,v11
X_ a156,v12
R_ v13,a157
U_ v13,v13,v11
X_ a157,v13
R_ v12,a158
U_ v12,v12,v11
X_ a158,v12
R_ v13,a159
U_ v13,v13,v11
X_ a159,v13
R_ v12,a176
U_ v12,v12,v11
X_ a176,v12
R_ v13,a177
U_ v13,v13,v11
X_ a177,v13
R_ v12,a178
U_ v12,v12,v11
X_ a178,v12
R_ v13,a179
U_ v13,v13,v11
X_ a179,v13
R_ v12,a196
U_ v12,v12,v11
X_ a196,v12
R_ v13,a197
U_ v13,v13,v11
X_ a197,v13
R_ v12,a198
U_ v12,v12,v11
X_ a198,v12
R_ v13,a199
U_ v13,v13,v11
X_ a199,v13
R_ v12,a216
U_ v12,v12,v11
X_ a216,v12
R_ v13,a217
U_ v13,v13,v11
X_ a217,v13
R_ v12,a218
U_ v12,v12,v11
X_ a218,v12
R_ v13,a219
U_ v13,v13,v11
X_ a219,v13
R_ v12,a236
U_ v12,v12,v11
X_ a236,v12
R_ v13,a237
U_ v13,v13,v11
X_ a237,v13
R_ v12,a238
U_ v12,v12,v11
X_ a238,v12
R_ v13,a239
U_ v13,v13,v11
X_ a239,v13
B_ v72,v72,v10
B_ v73,v73,v10
B_ v74,v74,v10
B_ v75,v75,v10
B_ v92,v92,v10
B_ v93,v93,v10
B_ v94,v94,v10
B_ v95,v95,v10
B_ v112,v112,v10
B_ v113,v113,v10
B_ v114,v114,v10
B_ v115,v115,v10
E_ 92
E_ 93
E_ 94
E_ 95
E_ 112
E_ 113
E_ 114
E_ 115
s_nop 0
C_ 115,114,115
C_ 114,112,113
C_ 112,92,93
C_ 113,94,95
v_mov_b32 v92, 0x3f803f80
v_mov_b32 v93, 0x3f803f80
v_mov_b32 v94, 0x3f803f80
v_mov_b32 v95, 0x3f803f80
s_nop 7
s_lshl_b32 s53, s58, 4
S_ s53,s53,s63
s_cmp_eq_u32 s53, 2
s_cbranch_scc1 .Lstep0_3
s_cmp_eq_u32 s53, 4
s_cbranch_scc1 .Lstep0_5
s_cmp_eq_u32 s53, 6
s_cbranch_scc1 .Lstep0_7
s_cmp_eq_u32 s53, 8
s_cbranch_scc1 .Lstep0_9
s_cmp_eq_u32 s53, 10
s_cbranch_scc1 .Lstep0_11
s_cmp_eq_u32 s53, 12
s_cbranch_scc1 .Lstep0_13
s_cmp_eq_u32 s53, 14
s_cbranch_scc1 .Lstep0_15
s_cmp_eq_u32 s53, 18
s_cbranch_scc1 .Lstep1_3
s_cmp_eq_u32 s53, 20
s_cbranch_scc1 .Lstep1_5
s_cmp_eq_u32 s53, 22
s_cbranch_scc1 .Lstep1_7
s_cmp_eq_u32 s53, 24
s_cbranch_scc1 .Lstep1_9
s_cmp_eq_u32 s53, 26
s_cbranch_scc1 .Lstep1_11
s_cmp_eq_u32 s53, 28
s_cbranch_scc1 .Lstep1_13
s_cmp_eq_u32 s53, 30
s_cbranch_scc1 .Lstep1_15
s_cmp_eq_u32 s53, 34
s_cbranch_scc1 .Lstep2_3
s_cmp_eq_u32 s53, 36
s_cbranch_scc1 .Lstep2_5
s_cmp_eq_u32 s53, 38
s_cbranch_scc1 .Lstep2_7
s_cmp_eq_u32 s53, 40
s_cbranch_scc1 .Lstep2_9
s_cmp_eq_u32 s53, 42
s_cbranch_scc1 .Lstep2_11
s_cmp_eq_u32 s53, 44
s_cbranch_scc1 .Lstep2_13
s_cmp_eq_u32 s53, 46
s_cbranch_scc1 .Lstep2_15
s_endpgm
.Lslow1:
W_ 0
s_nop 7
s_nop 7
s_and_b32 s64, s61, 4294959104
s_mul_i32 s53, s63, 12288
S_ s64,s64,s53
R_ v11,a248
v_and_b32 v10, 0xf0, v11
v_mul_u32_u24 v10, 24, v10
v_lshrrev_b32 v11, 8, v11
v_lshl_add_u32 v10, v11, 4, v10
S_ s53,s64,6144
buffer_load_dwordx4 v[96:99], v10, s[32:35], s53 offen offset:0
S_ s53,s64,6144
buffer_load_dwordx4 v[100:103], v10, s[32:35], s53 offen offset:64
S_ s53,s64,6144
buffer_load_dwordx4 v[104:107], v10, s[32:35], s53 offen offset:128
S_ s53,s64,6144
buffer_load_dwordx4 v[108:111], v10, s[32:35], s53 offen offset:192
s_waitcnt vmcnt(0)
Nvvv 116,96,136,56
Nvvv 120,96,140,60
Nvvv 124,96,144,64
Nvvv 128,96,148,68
Nvvv 132,96,152,72
Mvvv 116,100,156
Mvvv 120,100,160
Mvvv 124,100,164
Mvvv 128,100,168
Mvvv 132,100,172
Mvvv 116,104,176
Mvvv 120,104,180
Mvvv 124,104,184
Mvvv 128,104,188
Mvvv 132,104,192
Mvvv 116,108,196
Mvvv 120,108,200
Mvvv 124,108,204
Mvvv 128,108,208
Mvvv 132,108,212
s_nop 7
S_ s53,s64,6144
buffer_load_dwordx4 v[96:99], v10, s[32:35], s53 offen offset:256
S_ s53,s64,6144
buffer_load_dwordx4 v[100:103], v10, s[32:35], s53 offen offset:320
S_ s53,s64,0
buffer_load_dwordx4 v[104:107], v10, s[32:35], s53 offen offset:0
S_ s53,s64,0
buffer_load_dwordx4 v[108:111], v10, s[32:35], s53 offen offset:64
s_waitcnt vmcnt(0)
Mvvv 116,96,216
Mvvv 120,96,220
Mvvv 124,96,224
Mvvv 128,96,228
Mvvv 132,96,232
Mvvv 116,100,236
Mvvv 120,100,240
Mvvv 124,100,244
Mvvv 128,100,248
Mvvv 132,100,252
Nvvv 76,104,136,56
Nvvv 80,104,140,60
Nvvv 84,104,144,64
Nvvv 88,104,148,68
Nvvv 92,104,152,72
Mvvv 76,108,156
Mvvv 80,108,160
Mvvv 84,108,164
Mvvv 88,108,168
Mvvv 92,108,172
s_nop 7
S_ s53,s64,0
buffer_load_dwordx4 v[96:99], v10, s[32:35], s53 offen offset:128
S_ s53,s64,0
buffer_load_dwordx4 v[100:103], v10, s[32:35], s53 offen offset:192
S_ s53,s64,0
buffer_load_dwordx4 v[104:107], v10, s[32:35], s53 offen offset:256
S_ s53,s64,0
buffer_load_dwordx4 v[108:111], v10, s[32:35], s53 offen offset:320
s_waitcnt vmcnt(0)
Mvvv 76,96,176
Mvvv 80,96,180
Mvvv 84,96,184
Mvvv 88,96,188
Mvvv 92,96,192
Mvvv 76,100,196
Mvvv 80,100,200
Mvvv 84,100,204
Mvvv 88,100,208
Mvvv 92,100,212
Mvvv 76,104,216
Mvvv 80,104,220
Mvvv 84,104,224
Mvvv 88,104,228
Mvvv 92,104,232
Mvvv 76,108,236
Mvvv 80,108,240
Mvvv 84,108,244
Mvvv 88,108,248
Mvvv 92,108,252
s_nop 7
s_nop 2
v_max3_f32 v10, v76, v77, v78
v_max3_f32 v10, v10, v79, v116
v_max3_f32 v10, v10, v117, v118
v_max_f32 v10, v10, v119
v_mov_b32 v11, v10
s_nop 1
v_permlane16_swap_b32 v10, v11
v_max_f32 v10, v10, v11
v_mov_b32 v11, v10
s_nop 1
v_permlane32_swap_b32 v10, v11
v_max_f32 v10, v10, v11
A_ v10,0x40e00000,v10
v_max_f32 v10, 0, v10
v_exp_f32 v11, -v10
s_nop 0
U_ v46,v46,v11
R_ v12,a0
U_ v12,v12,v11
X_ a0,v12
R_ v13,a1
U_ v13,v13,v11
X_ a1,v13
R_ v12,a2
U_ v12,v12,v11
X_ a2,v12
R_ v13,a3
U_ v13,v13,v11
X_ a3,v13
R_ v12,a20
U_ v12,v12,v11
X_ a20,v12
R_ v13,a21
U_ v13,v13,v11
X_ a21,v13
R_ v12,a22
U_ v12,v12,v11
X_ a22,v12
R_ v13,a23
U_ v13,v13,v11
X_ a23,v13
R_ v12,a40
U_ v12,v12,v11
X_ a40,v12
R_ v13,a41
U_ v13,v13,v11
X_ a41,v13
R_ v12,a42
U_ v12,v12,v11
X_ a42,v12
R_ v13,a43
U_ v13,v13,v11
X_ a43,v13
R_ v12,a60
U_ v12,v12,v11
X_ a60,v12
R_ v13,a61
U_ v13,v13,v11
X_ a61,v13
R_ v12,a62
U_ v12,v12,v11
X_ a62,v12
R_ v13,a63
U_ v13,v13,v11
X_ a63,v13
R_ v12,a80
U_ v12,v12,v11
X_ a80,v12
R_ v13,a81
U_ v13,v13,v11
X_ a81,v13
R_ v12,a82
U_ v12,v12,v11
X_ a82,v12
R_ v13,a83
U_ v13,v13,v11
X_ a83,v13
R_ v12,a100
U_ v12,v12,v11
X_ a100,v12
R_ v13,a101
U_ v13,v13,v11
X_ a101,v13
R_ v12,a102
U_ v12,v12,v11
X_ a102,v12
R_ v13,a103
U_ v13,v13,v11
X_ a103,v13
R_ v12,a120
U_ v12,v12,v11
X_ a120,v12
R_ v13,a121
U_ v13,v13,v11
X_ a121,v13
R_ v12,a122
U_ v12,v12,v11
X_ a122,v12
R_ v13,a123
U_ v13,v13,v11
X_ a123,v13
R_ v12,a140
U_ v12,v12,v11
X_ a140,v12
R_ v13,a141
U_ v13,v13,v11
X_ a141,v13
R_ v12,a142
U_ v12,v12,v11
X_ a142,v12
R_ v13,a143
U_ v13,v13,v11
X_ a143,v13
R_ v12,a160
U_ v12,v12,v11
X_ a160,v12
R_ v13,a161
U_ v13,v13,v11
X_ a161,v13
R_ v12,a162
U_ v12,v12,v11
X_ a162,v12
R_ v13,a163
U_ v13,v13,v11
X_ a163,v13
R_ v12,a180
U_ v12,v12,v11
X_ a180,v12
R_ v13,a181
U_ v13,v13,v11
X_ a181,v13
R_ v12,a182
U_ v12,v12,v11
X_ a182,v12
R_ v13,a183
U_ v13,v13,v11
X_ a183,v13
R_ v12,a200
U_ v12,v12,v11
X_ a200,v12
R_ v13,a201
U_ v13,v13,v11
X_ a201,v13
R_ v12,a202
U_ v12,v12,v11
X_ a202,v12
R_ v13,a203
U_ v13,v13,v11
X_ a203,v13
R_ v12,a220
U_ v12,v12,v11
X_ a220,v12
R_ v13,a221
U_ v13,v13,v11
X_ a221,v13
R_ v12,a222
U_ v12,v12,v11
X_ a222,v12
R_ v13,a223
U_ v13,v13,v11
X_ a223,v13
B_ v56,v56,v10
B_ v57,v57,v10
B_ v58,v58,v10
B_ v59,v59,v10
B_ v76,v76,v10
B_ v77,v77,v10
B_ v78,v78,v10
B_ v79,v79,v10
B_ v116,v116,v10
B_ v117,v117,v10
B_ v118,v118,v10
B_ v119,v119,v10
E_ 76
E_ 77
E_ 78
E_ 79
E_ 116
E_ 117
E_ 118
E_ 119
s_nop 0
C_ 119,118,119
C_ 118,116,117
C_ 116,76,77
C_ 117,78,79
v_max3_f32 v10, v80, v81, v82
v_max3_f32 v10, v10, v83, v120
v_max3_f32 v10, v10, v121, v122
v_max_f32 v10, v10, v123
v_mov_b32 v11, v10
s_nop 1
v_permlane16_swap_b32 v10, v11
v_max_f32 v10, v10, v11
v_mov_b32 v11, v10
s_nop 1
v_permlane32_swap_b32 v10, v11
v_max_f32 v10, v10, v11
A_ v10,0x40e00000,v10
v_max_f32 v10, 0, v10
v_exp_f32 v11, -v10
s_nop 0
U_ v47,v47,v11
R_ v12,a4
U_ v12,v12,v11
X_ a4,v12
R_ v13,a5
U_ v13,v13,v11
X_ a5,v13
R_ v12,a6
U_ v12,v12,v11
X_ a6,v12
R_ v13,a7
U_ v13,v13,v11
X_ a7,v13
R_ v12,a24
U_ v12,v12,v11
X_ a24,v12
R_ v13,a25
U_ v13,v13,v11
X_ a25,v13
R_ v12,a26
U_ v12,v12,v11
X_ a26,v12
R_ v13,a27
U_ v13,v13,v11
X_ a27,v13
R_ v12,a44
U_ v12,v12,v11
X_ a44,v12
R_ v13,a45
U_ v13,v13,v11
X_ a45,v13
R_ v12,a46
U_ v12,v12,v11
X_ a46,v12
R_ v13,a47
U_ v13,v13,v11
X_ a47,v13
R_ v12,a64
U_ v12,v12,v11
X_ a64,v12
R_ v13,a65
U_ v13,v13,v11
X_ a65,v13
R_ v12,a66
U_ v12,v12,v11
X_ a66,v12
R_ v13,a67
U_ v13,v13,v11
X_ a67,v13
R_ v12,a84
U_ v12,v12,v11
X_ a84,v12
R_ v13,a85
U_ v13,v13,v11
X_ a85,v13
R_ v12,a86
U_ v12,v12,v11
X_ a86,v12
R_ v13,a87
U_ v13,v13,v11
X_ a87,v13
R_ v12,a104
U_ v12,v12,v11
X_ a104,v12
R_ v13,a105
U_ v13,v13,v11
X_ a105,v13
R_ v12,a106
U_ v12,v12,v11
X_ a106,v12
R_ v13,a107
U_ v13,v13,v11
X_ a107,v13
R_ v12,a124
U_ v12,v12,v11
X_ a124,v12
R_ v13,a125
U_ v13,v13,v11
X_ a125,v13
R_ v12,a126
U_ v12,v12,v11
X_ a126,v12
R_ v13,a127
U_ v13,v13,v11
X_ a127,v13
R_ v12,a144
U_ v12,v12,v11
X_ a144,v12
R_ v13,a145
U_ v13,v13,v11
X_ a145,v13
R_ v12,a146
U_ v12,v12,v11
X_ a146,v12
R_ v13,a147
U_ v13,v13,v11
X_ a147,v13
R_ v12,a164
U_ v12,v12,v11
X_ a164,v12
R_ v13,a165
U_ v13,v13,v11
X_ a165,v13
R_ v12,a166
U_ v12,v12,v11
X_ a166,v12
R_ v13,a167
U_ v13,v13,v11
X_ a167,v13
R_ v12,a184
U_ v12,v12,v11
X_ a184,v12
R_ v13,a185
U_ v13,v13,v11
X_ a185,v13
R_ v12,a186
U_ v12,v12,v11
X_ a186,v12
R_ v13,a187
U_ v13,v13,v11
X_ a187,v13
R_ v12,a204
U_ v12,v12,v11
X_ a204,v12
R_ v13,a205
U_ v13,v13,v11
X_ a205,v13
R_ v12,a206
U_ v12,v12,v11
X_ a206,v12
R_ v13,a207
U_ v13,v13,v11
X_ a207,v13
R_ v12,a224
U_ v12,v12,v11
X_ a224,v12
R_ v13,a225
U_ v13,v13,v11
X_ a225,v13
R_ v12,a226
U_ v12,v12,v11
X_ a226,v12
R_ v13,a227
U_ v13,v13,v11
X_ a227,v13
B_ v60,v60,v10
B_ v61,v61,v10
B_ v62,v62,v10
B_ v63,v63,v10
B_ v80,v80,v10
B_ v81,v81,v10
B_ v82,v82,v10
B_ v83,v83,v10
B_ v120,v120,v10
B_ v121,v121,v10
B_ v122,v122,v10
B_ v123,v123,v10
E_ 80
E_ 81
E_ 82
E_ 83
E_ 120
E_ 121
E_ 122
E_ 123
s_nop 0
C_ 123,122,123
C_ 122,120,121
C_ 120,80,81
C_ 121,82,83
v_max3_f32 v10, v84, v85, v86
v_max3_f32 v10, v10, v87, v124
v_max3_f32 v10, v10, v125, v126
v_max_f32 v10, v10, v127
v_mov_b32 v11, v10
s_nop 1
v_permlane16_swap_b32 v10, v11
v_max_f32 v10, v10, v11
v_mov_b32 v11, v10
s_nop 1
v_permlane32_swap_b32 v10, v11
v_max_f32 v10, v10, v11
A_ v10,0x40e00000,v10
v_max_f32 v10, 0, v10
v_exp_f32 v11, -v10
s_nop 0
U_ v48,v48,v11
R_ v12,a8
U_ v12,v12,v11
X_ a8,v12
R_ v13,a9
U_ v13,v13,v11
X_ a9,v13
R_ v12,a10
U_ v12,v12,v11
X_ a10,v12
R_ v13,a11
U_ v13,v13,v11
X_ a11,v13
R_ v12,a28
U_ v12,v12,v11
X_ a28,v12
R_ v13,a29
U_ v13,v13,v11
X_ a29,v13
R_ v12,a30
U_ v12,v12,v11
X_ a30,v12
R_ v13,a31
U_ v13,v13,v11
X_ a31,v13
R_ v12,a48
U_ v12,v12,v11
X_ a48,v12
R_ v13,a49
U_ v13,v13,v11
X_ a49,v13
R_ v12,a50
U_ v12,v12,v11
X_ a50,v12
R_ v13,a51
U_ v13,v13,v11
X_ a51,v13
R_ v12,a68
U_ v12,v12,v11
X_ a68,v12
R_ v13,a69
U_ v13,v13,v11
X_ a69,v13
R_ v12,a70
U_ v12,v12,v11
X_ a70,v12
R_ v13,a71
U_ v13,v13,v11
X_ a71,v13
R_ v12,a88
U_ v12,v12,v11
X_ a88,v12
R_ v13,a89
U_ v13,v13,v11
X_ a89,v13
R_ v12,a90
U_ v12,v12,v11
X_ a90,v12
R_ v13,a91
U_ v13,v13,v11
X_ a91,v13
R_ v12,a108
U_ v12,v12,v11
X_ a108,v12
R_ v13,a109
U_ v13,v13,v11
X_ a109,v13
R_ v12,a110
U_ v12,v12,v11
X_ a110,v12
R_ v13,a111
U_ v13,v13,v11
X_ a111,v13
R_ v12,a128
U_ v12,v12,v11
X_ a128,v12
R_ v13,a129
U_ v13,v13,v11
X_ a129,v13
R_ v12,a130
U_ v12,v12,v11
X_ a130,v12
R_ v13,a131
U_ v13,v13,v11
X_ a131,v13
R_ v12,a148
U_ v12,v12,v11
X_ a148,v12
R_ v13,a149
U_ v13,v13,v11
X_ a149,v13
R_ v12,a150
U_ v12,v12,v11
X_ a150,v12
R_ v13,a151
U_ v13,v13,v11
X_ a151,v13
R_ v12,a168
U_ v12,v12,v11
X_ a168,v12
R_ v13,a169
U_ v13,v13,v11
X_ a169,v13
R_ v12,a170
U_ v12,v12,v11
X_ a170,v12
R_ v13,a171
U_ v13,v13,v11
X_ a171,v13
R_ v12,a188
U_ v12,v12,v11
X_ a188,v12
R_ v13,a189
U_ v13,v13,v11
X_ a189,v13
R_ v12,a190
U_ v12,v12,v11
X_ a190,v12
R_ v13,a191
U_ v13,v13,v11
X_ a191,v13
R_ v12,a208
U_ v12,v12,v11
X_ a208,v12
R_ v13,a209
U_ v13,v13,v11
X_ a209,v13
R_ v12,a210
U_ v12,v12,v11
X_ a210,v12
R_ v13,a211
U_ v13,v13,v11
X_ a211,v13
R_ v12,a228
U_ v12,v12,v11
X_ a228,v12
R_ v13,a229
U_ v13,v13,v11
X_ a229,v13
R_ v12,a230
U_ v12,v12,v11
X_ a230,v12
R_ v13,a231
U_ v13,v13,v11
X_ a231,v13
B_ v64,v64,v10
B_ v65,v65,v10
B_ v66,v66,v10
B_ v67,v67,v10
B_ v84,v84,v10
B_ v85,v85,v10
B_ v86,v86,v10
B_ v87,v87,v10
B_ v124,v124,v10
B_ v125,v125,v10
B_ v126,v126,v10
B_ v127,v127,v10
E_ 84
E_ 85
E_ 86
E_ 87
E_ 124
E_ 125
E_ 126
E_ 127
s_nop 0
C_ 127,126,127
C_ 126,124,125
C_ 124,84,85
C_ 125,86,87
v_max3_f32 v10, v88, v89, v90
v_max3_f32 v10, v10, v91, v128
v_max3_f32 v10, v10, v129, v130
v_max_f32 v10, v10, v131
v_mov_b32 v11, v10
s_nop 1
v_permlane16_swap_b32 v10, v11
v_max_f32 v10, v10, v11
v_mov_b32 v11, v10
s_nop 1
v_permlane32_swap_b32 v10, v11
v_max_f32 v10, v10, v11
A_ v10,0x40e00000,v10
v_max_f32 v10, 0, v10
v_exp_f32 v11, -v10
s_nop 0
U_ v49,v49,v11
R_ v12,a12
U_ v12,v12,v11
X_ a12,v12
R_ v13,a13
U_ v13,v13,v11
X_ a13,v13
R_ v12,a14
U_ v12,v12,v11
X_ a14,v12
R_ v13,a15
U_ v13,v13,v11
X_ a15,v13
R_ v12,a32
U_ v12,v12,v11
X_ a32,v12
R_ v13,a33
U_ v13,v13,v11
X_ a33,v13
R_ v12,a34
U_ v12,v12,v11
X_ a34,v12
R_ v13,a35
U_ v13,v13,v11
X_ a35,v13
R_ v12,a52
U_ v12,v12,v11
X_ a52,v12
R_ v13,a53
U_ v13,v13,v11
X_ a53,v13
R_ v12,a54
U_ v12,v12,v11
X_ a54,v12
R_ v13,a55
U_ v13,v13,v11
X_ a55,v13
R_ v12,a72
U_ v12,v12,v11
X_ a72,v12
R_ v13,a73
U_ v13,v13,v11
X_ a73,v13
R_ v12,a74
U_ v12,v12,v11
X_ a74,v12
R_ v13,a75
U_ v13,v13,v11
X_ a75,v13
R_ v12,a92
U_ v12,v12,v11
X_ a92,v12
R_ v13,a93
U_ v13,v13,v11
X_ a93,v13
R_ v12,a94
U_ v12,v12,v11
X_ a94,v12
R_ v13,a95
U_ v13,v13,v11
X_ a95,v13
R_ v12,a112
U_ v12,v12,v11
X_ a112,v12
R_ v13,a113
U_ v13,v13,v11
X_ a113,v13
R_ v12,a114
U_ v12,v12,v11
X_ a114,v12
R_ v13,a115
U_ v13,v13,v11
X_ a115,v13
R_ v12,a132
U_ v12,v12,v11
X_ a132,v12
R_ v13,a133
U_ v13,v13,v11
X_ a133,v13
R_ v12,a134
U_ v12,v12,v11
X_ a134,v12
R_ v13,a135
U_ v13,v13,v11
X_ a135,v13
R_ v12,a152
U_ v12,v12,v11
X_ a152,v12
R_ v13,a153
U_ v13,v13,v11
X_ a153,v13
R_ v12,a154
U_ v12,v12,v11
X_ a154,v12
R_ v13,a155
U_ v13,v13,v11
X_ a155,v13
R_ v12,a172
U_ v12,v12,v11
X_ a172,v12
R_ v13,a173
U_ v13,v13,v11
X_ a173,v13
R_ v12,a174
U_ v12,v12,v11
X_ a174,v12
R_ v13,a175
U_ v13,v13,v11
X_ a175,v13
R_ v12,a192
U_ v12,v12,v11
X_ a192,v12
R_ v13,a193
U_ v13,v13,v11
X_ a193,v13
R_ v12,a194
U_ v12,v12,v11
X_ a194,v12
R_ v13,a195
U_ v13,v13,v11
X_ a195,v13
R_ v12,a212
U_ v12,v12,v11
X_ a212,v12
R_ v13,a213
U_ v13,v13,v11
X_ a213,v13
R_ v12,a214
U_ v12,v12,v11
X_ a214,v12
R_ v13,a215
U_ v13,v13,v11
X_ a215,v13
R_ v12,a232
U_ v12,v12,v11
X_ a232,v12
R_ v13,a233
U_ v13,v13,v11
X_ a233,v13
R_ v12,a234
U_ v12,v12,v11
X_ a234,v12
R_ v13,a235
U_ v13,v13,v11
X_ a235,v13
B_ v68,v68,v10
B_ v69,v69,v10
B_ v70,v70,v10
B_ v71,v71,v10
B_ v88,v88,v10
B_ v89,v89,v10
B_ v90,v90,v10
B_ v91,v91,v10
B_ v128,v128,v10
B_ v129,v129,v10
B_ v130,v130,v10
B_ v131,v131,v10
E_ 88
E_ 89
E_ 90
E_ 91
E_ 128
E_ 129
E_ 130
E_ 131
s_nop 0
C_ 131,130,131
C_ 130,128,129
C_ 128,88,89
C_ 129,90,91
v_max3_f32 v10, v92, v93, v94
v_max3_f32 v10, v10, v95, v132
v_max3_f32 v10, v10, v133, v134
v_max_f32 v10, v10, v135
v_mov_b32 v11, v10
s_nop 1
v_permlane16_swap_b32 v10, v11
v_max_f32 v10, v10, v11
v_mov_b32 v11, v10
s_nop 1
v_permlane32_swap_b32 v10, v11
v_max_f32 v10, v10, v11
A_ v10,0x40e00000,v10
v_max_f32 v10, 0, v10
v_exp_f32 v11, -v10
s_nop 0
U_ v50,v50,v11
R_ v12,a16
U_ v12,v12,v11
X_ a16,v12
R_ v13,a17
U_ v13,v13,v11
X_ a17,v13
R_ v12,a18
U_ v12,v12,v11
X_ a18,v12
R_ v13,a19
U_ v13,v13,v11
X_ a19,v13
R_ v12,a36
U_ v12,v12,v11
X_ a36,v12
R_ v13,a37
U_ v13,v13,v11
X_ a37,v13
R_ v12,a38
U_ v12,v12,v11
X_ a38,v12
R_ v13,a39
U_ v13,v13,v11
X_ a39,v13
R_ v12,a56
U_ v12,v12,v11
X_ a56,v12
R_ v13,a57
U_ v13,v13,v11
X_ a57,v13
R_ v12,a58
U_ v12,v12,v11
X_ a58,v12
R_ v13,a59
U_ v13,v13,v11
X_ a59,v13
R_ v12,a76
U_ v12,v12,v11
X_ a76,v12
R_ v13,a77
U_ v13,v13,v11
X_ a77,v13
R_ v12,a78
U_ v12,v12,v11
X_ a78,v12
R_ v13,a79
U_ v13,v13,v11
X_ a79,v13
R_ v12,a96
U_ v12,v12,v11
X_ a96,v12
R_ v13,a97
U_ v13,v13,v11
X_ a97,v13
R_ v12,a98
U_ v12,v12,v11
X_ a98,v12
R_ v13,a99
U_ v13,v13,v11
X_ a99,v13
R_ v12,a116
U_ v12,v12,v11
X_ a116,v12
R_ v13,a117
U_ v13,v13,v11
X_ a117,v13
R_ v12,a118
U_ v12,v12,v11
X_ a118,v12
R_ v13,a119
U_ v13,v13,v11
X_ a119,v13
R_ v12,a136
U_ v12,v12,v11
X_ a136,v12
R_ v13,a137
U_ v13,v13,v11
X_ a137,v13
R_ v12,a138
U_ v12,v12,v11
X_ a138,v12
R_ v13,a139
U_ v13,v13,v11
X_ a139,v13
R_ v12,a156
U_ v12,v12,v11
X_ a156,v12
R_ v13,a157
U_ v13,v13,v11
X_ a157,v13
R_ v12,a158
U_ v12,v12,v11
)A"
R"A(X_ a158,v12
R_ v13,a159
U_ v13,v13,v11
X_ a159,v13
R_ v12,a176
U_ v12,v12,v11
X_ a176,v12
R_ v13,a177
U_ v13,v13,v11
X_ a177,v13
R_ v12,a178
U_ v12,v12,v11
X_ a178,v12
R_ v13,a179
U_ v13,v13,v11
X_ a179,v13
R_ v12,a196
U_ v12,v12,v11
X_ a196,v12
R_ v13,a197
U_ v13,v13,v11
X_ a197,v13
R_ v12,a198
U_ v12,v12,v11
X_ a198,v12
R_ v13,a199
U_ v13,v13,v11
X_ a199,v13
R_ v12,a216
U_ v12,v12,v11
X_ a216,v12
R_ v13,a217
U_ v13,v13,v11
X_ a217,v13
R_ v12,a218
U_ v12,v12,v11
X_ a218,v12
R_ v13,a219
U_ v13,v13,v11
X_ a219,v13
R_ v12,a236
U_ v12,v12,v11
X_ a236,v12
R_ v13,a237
U_ v13,v13,v11
X_ a237,v13
R_ v12,a238
U_ v12,v12,v11
X_ a238,v12
R_ v13,a239
U_ v13,v13,v11
X_ a239,v13
B_ v72,v72,v10
B_ v73,v73,v10
B_ v74,v74,v10
B_ v75,v75,v10
B_ v92,v92,v10
B_ v93,v93,v10
B_ v94,v94,v10
B_ v95,v95,v10
B_ v132,v132,v10
B_ v133,v133,v10
B_ v134,v134,v10
B_ v135,v135,v10
E_ 92
E_ 93
E_ 94
E_ 95
E_ 132
E_ 133
E_ 134
E_ 135
s_nop 0
C_ 135,134,135
C_ 134,132,133
C_ 132,92,93
C_ 133,94,95
v_mov_b32 v92, 0x3f803f80
v_mov_b32 v93, 0x3f803f80
v_mov_b32 v94, 0x3f803f80
v_mov_b32 v95, 0x3f803f80
s_nop 7
s_lshl_b32 s53, s58, 4
S_ s53,s53,s63
s_cmp_eq_u32 s53, 1
s_cbranch_scc1 .Lstep0_2
s_cmp_eq_u32 s53, 3
s_cbranch_scc1 .Lstep0_4
s_cmp_eq_u32 s53, 5
s_cbranch_scc1 .Lstep0_6
s_cmp_eq_u32 s53, 7
s_cbranch_scc1 .Lstep0_8
s_cmp_eq_u32 s53, 9
s_cbranch_scc1 .Lstep0_10
s_cmp_eq_u32 s53, 11
s_cbranch_scc1 .Lstep0_12
s_cmp_eq_u32 s53, 13
s_cbranch_scc1 .Lstep0_14
s_cmp_eq_u32 s53, 15
s_cbranch_scc1 .Lstep0_16
s_cmp_eq_u32 s53, 17
s_cbranch_scc1 .Lstep1_2
s_cmp_eq_u32 s53, 19
s_cbranch_scc1 .Lstep1_4
s_cmp_eq_u32 s53, 21
s_cbranch_scc1 .Lstep1_6
s_cmp_eq_u32 s53, 23
s_cbranch_scc1 .Lstep1_8
s_cmp_eq_u32 s53, 25
s_cbranch_scc1 .Lstep1_10
s_cmp_eq_u32 s53, 27
s_cbranch_scc1 .Lstep1_12
s_cmp_eq_u32 s53, 29
s_cbranch_scc1 .Lstep1_14
s_cmp_eq_u32 s53, 31
s_cbranch_scc1 .Lstep1_16
s_cmp_eq_u32 s53, 33
s_cbranch_scc1 .Lstep2_2
s_cmp_eq_u32 s53, 35
s_cbranch_scc1 .Lstep2_4
s_cmp_eq_u32 s53, 37
s_cbranch_scc1 .Lstep2_6
s_cmp_eq_u32 s53, 39
s_cbranch_scc1 .Lstep2_8
s_cmp_eq_u32 s53, 41
s_cbranch_scc1 .Lstep2_10
s_cmp_eq_u32 s53, 43
s_cbranch_scc1 .Lstep2_12
s_cmp_eq_u32 s53, 45
s_cbranch_scc1 .Lstep2_14
s_cmp_eq_u32 s53, 47
s_cbranch_scc1 .Lstep2_16
s_endpgm
.Lpace1:
S_ s12,s12,s13
.Lpace1_l:
s_memrealtime s[100:101]
W_ 0
s_sub_u32 s100, s100, s98
s_cmp_ge_u32 s100, s12
s_cbranch_scc1 .Lpace1_back
s_sleep 2
s_branch .Lpace1_l
.Lpace3:
S_ s12,s12,s13
.Lpace3_l:
s_memrealtime s[100:101]
W_ 0
s_sub_u32 s100, s100, s98
s_cmp_ge_u32 s100, s12
s_cbranch_scc1 .Lpace3_back
s_sleep 2
s_branch .Lpace3_l
.Lpace5:
S_ s12,s12,s13
.Lpace5_l:
s_memrealtime s[100:101]
W_ 0
s_sub_u32 s100, s100, s98
s_cmp_ge_u32 s100, s12
s_cbranch_scc1 .Lpace5_back
s_sleep 2
s_branch .Lpace5_l
.Lpace7:
S_ s12,s12,s13
.Lpace7_l:
s_memrealtime s[100:101]
W_ 0
s_sub_u32 s100, s100, s98
s_cmp_ge_u32 s100, s12
s_cbranch_scc1 .Lpace7_back
s_sleep 2
s_branch .Lpace7_l
.Lpace9:
S_ s12,s12,s13
.Lpace9_l:
s_memrealtime s[100:101]
W_ 0
s_sub_u32 s100, s100, s98
s_cmp_ge_u32 s100, s12
s_cbranch_scc1 .Lpace9_back
s_sleep 2
s_branch .Lpace9_l
.Lpace11:
S_ s12,s12,s13
.Lpace11_l:
s_memrealtime s[100:101]
W_ 0
s_sub_u32 s100, s100, s98
s_cmp_ge_u32 s100, s12
s_cbranch_scc1 .Lpace11_back
s_sleep 2
s_branch .Lpace11_l
.Lpace13:
S_ s12,s12,s13
.Lpace13_l:
s_memrealtime s[100:101]
W_ 0
s_sub_u32 s100, s100, s98
s_cmp_ge_u32 s100, s12
s_cbranch_scc1 .Lpace13_back
s_sleep 2
s_branch .Lpace13_l
.Lpace15:
S_ s12,s12,s13
.Lpace15_l:
s_memrealtime s[100:101]
W_ 0
s_sub_u32 s100, s100, s98
s_cmp_ge_u32 s100, s12
s_cbranch_scc1 .Lpace15_back
s_sleep 2
s_branch .Lpace15_l
.Lpace17:
S_ s12,s12,s13
.Lpace17_l:
s_memrealtime s[100:101]
W_ 0
s_sub_u32 s100, s100, s98
s_cmp_ge_u32 s100, s12
s_cbranch_scc1 .Lpace17_back
s_sleep 2
s_branch .Lpace17_l
.Lpace19:
S_ s12,s12,s13
.Lpace19_l:
s_memrealtime s[100:101]
W_ 0
s_sub_u32 s100, s100, s98
s_cmp_ge_u32 s100, s12
s_cbranch_scc1 .Lpace19_back
s_sleep 2
s_branch .Lpace19_l
.Lpace21:
S_ s12,s12,s13
.Lpace21_l:
s_memrealtime s[100:101]
W_ 0
s_sub_u32 s100, s100, s98
s_cmp_ge_u32 s100, s12
s_cbranch_scc1 .Lpace21_back
s_sleep 2
s_branch .Lpace21_l
.Lpace23:
S_ s12,s12,s13
.Lpace23_l:
s_memrealtime s[100:101]
W_ 0
s_sub_u32 s100, s100, s98
s_cmp_ge_u32 s100, s12
s_cbranch_scc1 .Lpace23_back
s_sleep 2
s_branch .Lpace23_l
.Lpace25:
S_ s12,s12,s13
.Lpace25_l:
s_memrealtime s[100:101]
W_ 0
s_sub_u32 s100, s100, s98
s_cmp_ge_u32 s100, s12
s_cbranch_scc1 .Lpace25_back
s_sleep 2
s_branch .Lpace25_l
.Lpace27:
S_ s12,s12,s13
.Lpace27_l:
s_memrealtime s[100:101]
W_ 0
s_sub_u32 s100, s100, s98
s_cmp_ge_u32 s100, s12
s_cbranch_scc1 .Lpace27_back
s_sleep 2
s_branch .Lpace27_l
.Lpace29:
S_ s12,s12,s13
.Lpace29_l:
s_memrealtime s[100:101]
W_ 0
s_sub_u32 s100, s100, s98
s_cmp_ge_u32 s100, s12
s_cbranch_scc1 .Lpace29_back
s_sleep 2
s_branch .Lpace29_l
.Lhs_a0:
s_nop 4
s_waitcnt vmcnt(12)
R_ v10,a251
buffer_store_dword a255, v10, s[92:95], 0 offen offset:128 sc1
s_branch .Lhs_a0_back
.Lhs_a1:
s_nop 4
R_ v11,a248
v_lshrrev_b32 v11, 2, v11
buffer_load_dword a254, v11, s[92:95], s96 offen offset:128 sc1
s_branch .Lhs_a1_back
.Lhs_a2:
s_nop 4
s_waitcnt vmcnt(12)
R_ v10,a251
buffer_store_dword a255, v10, s[92:95], 0 offen offset:128 sc1
s_branch .Lhs_a2_back
.Lhs_b1:
s_nop 4
s_waitcnt vmcnt(12)
R_ v11,a254
v_cmp_eq_u32 vcc, s62, v11
R_ v10,a251
v_mov_b32 v11, 0x7fff0000
v_cndmask_b32 v10, v11, v10, vcc
R_ v11,a253
v_add_u32 v11, 1, v11
buffer_store_dword v11, v10, s[92:95], 0 offen offset:256 sc1
s_and_b32 s53, vcc_lo, 255
s_cmp_eq_u32 s53, 0xFF
s_cselect_b32 s97, 1, 0
s_branch .Lhs_b1_back
.Lhs_c0:
s_nop 4
R_ v11,a248
v_lshrrev_b32 v11, 2, v11
buffer_load_dword a254, v11, s[92:95], s96 offen offset:128 sc1
s_branch .Lhs_c0_back
.Lhs_c1:
s_nop 4
R_ v11,a248
v_lshrrev_b32 v11, 2, v11
buffer_load_dword a254, v11, s[92:95], s96 offen offset:256 sc1
s_branch .Lhs_c1_back
.Lhs_c2:
s_nop 4
R_ v11,a248
v_lshrrev_b32 v11, 2, v11
buffer_load_dword a254, v11, s[92:95], s96 offen offset:128 sc1
s_branch .Lhs_c2_back
.Lhs_d0:
s_nop 4
s_waitcnt vmcnt(12)
R_ v11,a254
v_cmp_eq_u32 vcc, s62, v11
R_ v10,a251
v_mov_b32 v11, 0x7fff0000
v_cndmask_b32 v10, v11, v10, vcc
R_ v11,a253
v_add_u32 v11, 1, v11
buffer_store_dword v11, v10, s[92:95], 0 offen offset:256 sc1
s_and_b32 s53, vcc_lo, 255
s_cmp_eq_u32 s53, 0xFF
s_cselect_b32 s97, 1, 0
s_branch .Lhs_d0_back
.Lhs_d2:
s_nop 4
s_waitcnt vmcnt(12)
R_ v11,a254
v_cmp_eq_u32 vcc, s62, v11
R_ v10,a251
v_mov_b32 v11, 0x7fff0000
v_cndmask_b32 v10, v11, v10, vcc
R_ v11,a253
v_add_u32 v11, 1, v11
buffer_store_dword v11, v10, s[92:95], 0 offen offset:256 sc1
s_and_b32 s53, vcc_lo, 255
s_cmp_eq_u32 s53, 0xFF
s_cselect_b32 s97, 1, 0
s_branch .Lhs_d2_back
.Lhs_d1:
s_nop 4
s_waitcnt vmcnt(12)
R_ v11,a254
R_ v10,a252
v_cmp_ne_u32 vcc, v11, v10
s_nop 1
s_and_b32 s53, vcc_lo, 255
s_cmp_eq_u32 s53, 0xFF
s_cselect_b32 s53, s97, 0
s_cmp_eq_u32 s53, 1
s_cbranch_scc1 .Lhs_ok
.Lhs_spin:
s_cmp_eq_u32 s57, 0
s_cbranch_scc1 .Lhs_ok
s_sub_u32 s57, s57, 1
s_sleep 8
R_ v10,a251
s_lshl_b32 s53, s6, 2
v_subrev_u32 v10, s53, v10
buffer_load_dword v11, v10, s[92:95], 0 offen offset:0 sc1
R_ v10,a248
v_lshrrev_b32 v10, 2, v10
buffer_load_dword v12, v10, s[92:95], s96 offen offset:128 sc1
buffer_load_dword v13, v10, s[92:95], s96 offen offset:256 sc1
s_waitcnt vmcnt(0)
X_ a255,v11
R_ v10,a251
buffer_store_dword v11, v10, s[92:95], 0 offen offset:128 sc1
s_nop 1
v_cmp_eq_u32 vcc, s62, v12
R_ v10,a251
v_mov_b32 v12, 0x7fff0000
v_cndmask_b32 v10, v12, v10, vcc
R_ v12,a253
v_add_u32 v12, 1, v12
buffer_store_dword v12, v10, s[92:95], 0 offen offset:256 sc1
s_and_b32 s53, vcc_lo, 255
s_cmp_eq_u32 s53, 0xFF
s_cselect_b32 s97, 1, 0
s_nop 1
R_ v10,a252
v_cmp_ne_u32 vcc, v13, v10
s_nop 1
s_and_b32 s53, vcc_lo, 255
s_cmp_eq_u32 s53, 0xFF
s_cselect_b32 s53, s97, 0
s_cmp_eq_u32 s53, 1
s_cbranch_scc0 .Lhs_spin
.Lhs_ok:
s_branch .Lhs_d1_back
.Latt2:
R_ v13,a248
S_ s53,s51,36864
v_add_u32 v12, s53, v13
s_mul_i32 s53, s4, 10240
S_ s53,s53,0
v_add_u32 v13, s53, v13
s_cmp_eq_u32 s4, 0
s_cbranch_scc0 .Lhs_tok2
R_ v11,a241
buffer_load_dword a255, v11, s[92:95], 0 offen offset:0 sc1
.Lhs_tok2:
W_ 2
Zvvv 96,30,136
Dv 42,v1,44032
R_ v2,a245
Zvvv 100,30,140
ds_write_b128 v13, a[0:3] offset:0
Zvvv 104,30,144
v_xor_b32 v3, 32, v2
v_xor_b32 v4, 64, v2
Zvvv 108,30,148
ds_write_b128 v13, a[4:7] offset:1024
Zvvv 112,30,152
W_ 4
v_xor_b32 v5, 96, v2
s_mov_b64 s[24:25], s[16:17]
Mvvv 96,34,156
s_mov_b32 s26, 7864320
Dv 30,%0,45056
s_mov_b64 s[28:29], s[22:23]
Mvvv 100,34,160
ds_write_b128 v13, a[8:11] offset:2048
Mvvv 104,34,164
s_mov_b32 s30, 1638400
R_ v20,a80
s_mul_i32 s52, s4, 10240
Mvvv 108,34,168
ds_write_b128 v13, a[12:15] offset:3072
Mvvv 112,34,172
W_ 6
R_ v21,a81
R_ v22,a82
Mvvv 96,38,176
Dv 34,v1,45056
R_ v23,a83
Mvvv 100,38,180
ds_write_b128 v13, a[16:19] offset:4096
Mvvv 104,38,184
R_ v24,a84
R_ v25,a85
Mvvv 108,38,188
ds_write_b128 v13, a[40:43] offset:5120
Mvvv 112,38,192
W_ 8
R_ v26,a86
R_ v27,a87
Mvvv 96,42,196
Dv 38,%0,36864
R_ v52,a88
Mvvv 100,42,200
ds_write_b128 v13, a[44:47] offset:6144
Mvvv 104,42,204
R_ v53,a89
R_ v54,a90
Mvvv 108,42,208
ds_write_b128 v13, a[48:51] offset:7168
Mvvv 112,42,212
W_ 8
R_ v55,a91
v_accvgpr_mov_b32 a240, a92
Mvvv 96,30,216
Dv 42,v1,36864
v_accvgpr_mov_b32 a241, a93
Mvvv 100,30,220
ds_write_b128 v13, a[52:55] offset:8192
Mvvv 104,30,224
v_accvgpr_mov_b32 a242, a94
v_accvgpr_mov_b32 a243, a95
Mvvv 108,30,228
ds_write_b128 v13, a[56:59] offset:9216
Mvvv 112,30,232
W_ 8
v_accvgpr_mov_b32 a244, a96
v_accvgpr_mov_b32 a245, a97
Mvvv 96,34,236
Dv 30,%0,37888
v_accvgpr_mov_b32 a246, a98
Mvvv 100,34,240
v_accvgpr_mov_b32 a247, a99
R_ v16,a120
Mvvv 104,34,244
R_ v17,a121
R_ v18,a122
Mvvv 108,34,248
R_ v19,a123
R_ v28,a124
Mvvv 112,34,252
W_ 6
R_ v29,a125
R_ v51,a126
Zvvv 76,38,136
Dv 34,v1,37888
R_ v6,a127
Zvvv 80,38,140
R_ v14,a128
R_ v15,a129
Zvvv 84,38,144
v_accvgpr_mov_b32 a249, a130
v_accvgpr_mov_b32 a250, a131
Zvvv 88,38,148
v_max3_f32 v56, v96, v97, v98
v_max_f32 v56, v56, v99
Zvvv 92,38,152
W_ 4
v_max3_f32 v60, v100, v101, v102
v_max_f32 v60, v60, v103
Mvvv 76,42,156
Dv 38,%0,38912
v_max3_f32 v64, v104, v105, v106
Mvvv 80,42,160
v_max_f32 v64, v64, v107
Mvvv 84,42,164
v_max3_f32 v68, v108, v109, v110
v_max_f32 v68, v68, v111
Mvvv 88,42,168
v_max3_f32 v72, v112, v113, v114
v_max_f32 v72, v72, v115
Mvvv 92,42,172
W_ 2
Mvvv 76,30,176
Dv 42,v1,38912
Mvvv 80,30,180
Mvvv 84,30,184
Mvvv 88,30,188
Mvvv 92,30,192
s_waitcnt vmcnt(34) lgkmcnt(0)
s_barrier
Mvvv 76,34,196
Dv 30,%0,55296
Mvvv 76,38,216
Mvvv 76,42,236
Mvvv 80,34,200
Mvvv 80,38,220
Mvvv 80,42,240
Mvvv 84,34,204
s_nop 3
v_max3_f32 v56, v56, v76, v77
v_max3_f32 v56, v56, v78, v79
Mvvv 84,38,224
Mvvv 84,42,244
Mvvv 88,34,208
v_max3_f32 v60, v60, v80, v81
v_max3_f32 v60, v60, v82, v83
Mvvv 88,38,228
Mvvv 88,42,248
Mvvv 92,34,212
s_nop 1
v_max3_f32 v64, v64, v84, v85
v_max3_f32 v64, v64, v86, v87
Mvvv 92,38,232
Mvvv 92,42,252
Dv 34,v1,55296
.p2align 3
v_mov_b32 v11, 0x3f803f80
v_permlane32_swap_b32 v56, v60
v_max3_f32 v68, v68, v88, v89
v_max3_f32 v68, v68, v90, v91
v_max_f32 v56, v56, v60
s_nop 1
v_max3_f32 v72, v72, v92, v93
v_max3_f32 v72, v72, v94, v95
v_mov_b32 v73, v72
v_permlane32_swap_b32 v64, v68
v_max_f32 v64, v64, v68
v_permlane32_swap_b32 v72, v73
v_max_f32 v72, v72, v73
v_mov_b32 v73, v72
v_permlane16_swap_b32 v56, v64
v_max_f32 v56, v56, v64
v_permlane16_swap_b32 v72, v73
B_ v56,0xc0e00000,v56
v_max_f32 v72, v72, v73
v_mov_b32 v64, v56
B_ v72,0xc0e00000,v72
v_mov_b32 v73, v72
v_permlane16_swap_b32 v56, v64
v_mov_b32 v60, v56
v_mov_b32 v68, v64
v_mov_b64 v[74:75], v[72:73]
v_permlane32_swap_b32 v56, v60
v_permlane32_swap_b32 v64, v68
v_mov_b32 v57, v56
v_mov_b32 v61, v60
v_mov_b32 v65, v64
v_mov_b32 v69, v68
v_pk_add_f32 v[96:97], v[96:97], v[56:57]
v_pk_add_f32 v[98:99], v[98:99], v[56:57]
v_pk_add_f32 v[76:77], v[76:77], v[56:57]
v_pk_add_f32 v[78:79], v[78:79], v[56:57]
v_mov_b64 v[58:59], v[56:57]
v_pk_add_f32 v[100:101], v[100:101], v[60:61]
v_pk_add_f32 v[102:103], v[102:103], v[60:61]
v_pk_add_f32 v[80:81], v[80:81], v[60:61]
v_pk_add_f32 v[82:83], v[82:83], v[60:61]
v_mov_b64 v[62:63], v[60:61]
v_pk_add_f32 v[104:105], v[104:105], v[64:65]
v_pk_add_f32 v[106:107], v[106:107], v[64:65]
v_pk_add_f32 v[84:85], v[84:85], v[64:65]
v_pk_add_f32 v[86:87], v[86:87], v[64:65]
v_mov_b64 v[66:67], v[64:65]
v_pk_add_f32 v[108:109], v[108:109], v[68:69]
v_pk_add_f32 v[110:111], v[110:111], v[68:69]
v_pk_add_f32 v[88:89], v[88:89], v[68:69]
v_pk_add_f32 v[90:91], v[90:91], v[68:69]
v_mov_b64 v[70:71], v[68:69]
v_pk_add_f32 v[112:113], v[112:113], v[72:73]
v_pk_add_f32 v[114:115], v[114:115], v[72:73]
v_pk_add_f32 v[92:93], v[92:93], v[72:73]
v_pk_add_f32 v[94:95], v[94:95], v[72:73]
E_ 96
E_ 97
E_ 98
E_ 99
E_ 76
E_ 77
E_ 78
E_ 79
C_ 99,98,99
C_ 98,96,97
C_ 96,76,77
C_ 97,78,79
E_ 100
E_ 101
E_ 102
E_ 103
E_ 80
E_ 81
E_ 82
E_ 83
C_ 103,102,103
C_ 102,100,101
C_ 100,80,81
C_ 101,82,83
.Lstep2_1:
W_ 1
Nvvv 116,30,136,56
s_nop 0
Dv 38,%0,56320
Dv 42,v1,56320
W_ 2
Mvvv 116,34,156
E_ 104
Nvvv 120,30,140,60
E_ 105
Mvvv 120,34,160
E_ 106
Nvvv 124,30,144,64
E_ 107
Mvvv 124,34,164
E_ 84
Nvvv 128,30,148,68
E_ 85
Mvvv 128,34,168
E_ 86
Nvvv 132,30,152,72
E_ 87
Mvvv 132,34,172
W_ 0
C_ 107,106,107
C_ 106,104,105
Mvvv 132,38,192
s_nop 0
Dv 30,%0,57344
Dv 34,v1,57344
Mvvv 132,42,212
C_ 104,84,85
C_ 105,86,87
Mvvv 128,38,188
E_ 108
Mvvv 128,42,208
E_ 109
Mvvv 124,38,184
E_ 110
Mvvv 124,42,204
E_ 111
Mvvv 120,38,180
E_ 88
Mvvv 120,42,200
E_ 89
Mvvv 116,38,176
E_ 90
Mvvv 116,42,196
W_ 0
E_ 91
Mvvv 116,30,216
s_nop 0
Dv 38,%0,49152
Dv 42,v1,49152
Mvvv 116,34,236
C_ 111,110,111
C_ 110,108,109
Mvvv 120,30,220
C_ 108,88,89
C_ 109,90,91
Mvvv 120,34,240
E_ 112
Mvvv 124,30,224
E_ 113
Mvvv 124,34,244
E_ 114
Mvvv 128,30,228
E_ 115
Mvvv 128,34,248
E_ 92
Mvvv 132,30,232
E_ 93
Mvvv 132,34,252
W_ 0
E_ 94
Nvvv 76,38,136,56
s_nop 0
Dv 30,%0,50176
Dv 34,v1,50176
Mvvv 76,42,156
E_ 95
Nvvv 80,38,140,60
C_ 115,114,115
C_ 114,112,113
Mvvv 80,42,160
C_ 112,92,93
C_ 113,94,95
Nvvv 84,38,144,64
E_ 116
Mvvv 84,42,164
E_ 117
Nvvv 88,38,148,68
E_ 118
Mvvv 88,42,168
E_ 119
Nvvv 92,38,152,72
C_ 119,118,119
C_ 118,116,117
Mvvv 92,42,172
W_ 0
E_ 120
Mvvv 92,30,192
s_nop 0
Dv 38,%0,51200
Dv 42,v1,51200
Mvvv 92,34,212
X_ a180,v11
X_ a181,v11
Mvvv 88,30,188
X_ a182,v11
X_ a183,v11
Mvvv 88,34,208
ds_write_b128 v12, a[132:135] offset:0
Mvvv 84,30,184
E_ 121
Mvvv 84,34,204
E_ 122
Mvvv 80,30,180
ds_write_b128 v12, a[136:139] offset:1024
Mvvv 80,34,200
E_ 123
Mvvv 76,30,176
C_ 123,122,123
C_ 122,120,121
Mvvv 76,34,196
ds_write_b128 v12, a[160:163] offset:2048
W_ 4
Mvvv 76,38,216
W_ 3
E_ 124
Mvvv 76,42,236
s_waitcnt vmcnt(12) lgkmcnt(0)
s_barrier
Mvvv 80,38,220
ds_write_b128 v12, a[164:167] offset:3072
Mvvv 80,42,240
E_ 125
Mvvv 84,38,224
E_ 126
Mvvv 84,42,244
s_nop 0
Tv 30,v2,24576
Tv 32,v2,30720
Mvvv 88,38,228
ds_write_b128 v12, a[168:171] offset:4096
Mvvv 88,42,248
s_nop 0
Tv 34,v3,24576
Tv 36,v3,30720
Mvvv 92,38,232
E_ 127
Mvvv 92,42,252
ds_write_b128 v12, a[172:175] offset:5120
Zaav 220,180,96
C_ 127,126,127
C_ 126,124,125
Zaav 224,180,100
E_ 128
Zaav 228,180,104
ds_write_b128 v12, a[176:179] offset:12288
Zaav 232,180,108
E_ 129
Zaav 236,180,112
W_ 2
E_ 130
Zavv 0,30,96
s_nop 0
Tv 38,v4,24576
Tv 40,v4,30720
Zavv 20,34,96
ds_write_b128 v12, a[200:203] offset:13312
Zavv 24,34,100
s_nop 0
Tv 42,v5,24576
Tv 44,v5,30720
Zavv 4,30,100
E_ 131
Zavv 8,30,104
ds_write_b128 v12, a[204:207] offset:14336
Zavv 28,34,104
R_ v46,a220
R_ v47,a224
Zavv 32,34,108
R_ v48,a228
R_ v49,a232
Zavv 12,30,108
R_ v50,a236
C_ 131,130,131
Zavv 16,30,112
ds_write_b128 v12, a[208:211] offset:15360
Zavv 36,34,112
W_ 2
C_ 130,128,129
Zavv 56,38,112
s_nop 0
Tv 30,v2,25600
Tv 32,v2,31744
Zavv 76,42,112
ds_write_b128 v12, a[212:215] offset:16384
Zavv 72,42,108
s_nop 0
Tv 34,v3,25600
Tv 36,v3,31744
Zavv 52,38,108
E_ 132
Zavv 48,38,104
ds_write_b128 v12, a[216:219] offset:17408
Zavv 68,42,104
E_ 133
Zavv 64,42,100
E_ 134
Zavv 44,38,100
E_ 135
Zavv 40,38,96
C_ 135,134,135
C_ 134,132,133
Zavv 60,42,96
W_ 1
E_ 76
Zavv 80,30,96
s_nop 0
Tv 38,v4,25600
Tv 40,v4,31744
Zavv 100,34,96
E_ 77
Zavv 104,34,100
s_nop 0
Tv 42,v5,25600
Tv 44,v5,31744
Zavv 84,30,100
E_ 78
Zavv 88,30,104
E_ 79
Zavv 108,34,104
C_ 116,76,77
C_ 117,78,79
Zavv 112,34,108
E_ 80
Zavv 92,30,108
E_ 81
Zavv 96,30,112
E_ 82
Zavv 116,34,112
W_ 0
E_ 83
Zavv 136,38,112
s_nop 0
Tv 30,v2,26624
Tv 32,v2,32768
Zavv 156,42,112
C_ 120,80,81
C_ 121,82,83
Zavv 152,42,108
s_nop 0
Tv 34,v3,26624
Tv 36,v3,32768
Zavv 132,38,108
E_ 84
Zavv 128,38,104
E_ 85
Zavv 148,42,104
E_ 86
Zavv 144,42,100
E_ 87
Zavv 124,38,100
C_ 124,84,85
C_ 125,86,87
Zavv 120,38,96
E_ 88
Zavv 140,42,96
W_ 0
E_ 89
Zavv 160,30,96
s_nop 0
Tv 38,v4,26624
Tv 40,v4,32768
Zavv 180,34,96
E_ 90
Zavv 184,34,100
s_nop 0
Tv 42,v5,26624
Tv 44,v5,32768
Zavv 164,30,100
E_ 91
Zavv 168,30,104
C_ 128,88,89
C_ 129,90,91
Zavv 188,34,104
S_ s65,s61,36864
E_ 92
Zavv 192,34,108
S_ m0,s51,24576
E_ 93
Zavv 172,30,108
s_waitcnt vmcnt(6) lgkmcnt(0)
s_barrier
Zavv 176,30,112
Ln v7,32,s65,0
Zavv 196,34,112
E_ 94
Zavv 216,38,112
Dv 30,%0,6144
Zavv 236,42,112
Dv 34,v1,6144
Zavv 232,42,108
Ln v8,32,s65,3072
S_ m0,s51,25472
Zavv 212,38,108
E_ 95
Zavv 208,38,104
Ln v7,32,s65,128
Zavv 228,42,104
C_ 132,92,93
C_ 133,94,95
Zavv 224,42,100
Ln v8,32,s65,3200
S_ m0,s51,26368
Zavv 204,38,100
v_mov_b32 v92, 0x3f803f80
v_mov_b32 v93, 0x3f803f80
Zavv 200,38,96
Ln v7,32,s65,256
Zavv 220,42,96
v_mov_b32 v94, 0x3f803f80
v_mov_b32 v95, 0x3f803f80
Ln v8,32,s65,3328
.Lstep2_2:
s_nop 0
Zvvv 76,92,116
Dv 38,%0,7168
Zvvv 80,92,120
Dv 42,v1,7168
Zvvv 84,92,124
Zvvv 88,92,128
Zvvv 92,92,132
W_ 2
Nvvv 96,30,136,56
Mvvv 96,34,156
Nvvv 100,30,140,60
s_nop 1
v_max3_f32 v10, v76, v80, v84
s_nop 0
v_max3_f32 v10, v10, v88, v92
Mvvv 100,34,160
v_cmp_lt_f32 vcc, 0x42000000, v10
s_cbranch_vccnz .Ltramp2_1
Nvvv 104,30,144,64
A_ v46,v46,v76
A_ v47,v47,v80
Mvvv 104,34,164
A_ v48,v48,v84
A_ v49,v49,v88
Nvvv 108,30,148,68
A_ v50,v50,v92
Mvvv 108,34,168
Nvvv 112,30,152,72
Mvvv 112,34,172
W_ 0
Mvvv 112,38,192
Dv 30,%0,8192
Mvvv 112,42,212
Dv 34,v1,8192
Mvvv 108,38,188
Mvvv 108,42,208
Mvvv 104,38,184
Mvvv 104,42,204
Mvvv 100,38,180
Mvvv 100,42,200
Mvvv 96,38,176
Mvvv 96,42,196
W_ 0
Mvvv 96,30,216
Dv 38,%0,0
Mvvv 96,34,236
Dv 42,v1,0
Mvvv 100,30,220
Mvvv 100,34,240
Mvvv 104,30,224
Mvvv 104,34,244
Mvvv 108,30,228
Mvvv 108,34,248
s_nop 0
E_ 96
Mvvv 112,30,232
E_ 97
Mvvv 112,34,252
W_ 0
E_ 98
Nvvv 76,38,136,56
Dv 30,%0,1024
Mvvv 76,42,156
Dv 34,v1,1024
Nvvv 80,38,140,60
E_ 99
Mvvv 80,42,160
C_ 99,98,99
C_ 98,96,97
Nvvv 84,38,144,64
E_ 100
Mvvv 84,42,164
E_ 101
Nvvv 88,38,148,68
E_ 102
Mvvv 88,42,168
E_ 103
Nvvv 92,38,152,72
C_ 103,102,103
C_ 102,100,101
Mvvv 92,42,172
W_ 0
E_ 104
Mvvv 92,30,192
Dv 38,%0,2048
Mvvv 92,34,212
Dv 42,v1,2048
Mvvv 88,30,188
E_ 105
Mvvv 88,34,208
E_ 106
Mvvv 84,30,184
S_ s65,s61,24576
E_ 107
Mvvv 84,34,204
S_ m0,s51,0
C_ 107,106,107
C_ 106,104,105
Mvvv 80,30,180
s_waitcnt vmcnt(6) lgkmcnt(0)
s_barrier
Mvvv 80,34,200
L_ v9,36,s65,0
Mvvv 76,30,176
E_ 108
Mvvv 76,34,196
L_ v9,36,s65,3072
S_ m0,s51,896
Mvvv 76,38,216
Tv 30,v2,12288
Tv 32,v2,18432
Mvvv 76,42,236
L_ v9,36,s65,128
Mvvv 80,38,220
s_nop 0
Tv 34,v3,12288
Tv 36,v3,18432
Mvvv 80,42,240
L_ v9,36,s65,3200
S_ m0,s51,1792
Mvvv 84,38,224
E_ 109
Mvvv 84,42,244
L_ v9,36,s65,256
Mvvv 88,38,228
E_ 110
Mvvv 88,42,248
L_ v9,36,s65,3328
Mvvv 92,38,232
E_ 111
Mvvv 92,42,252
W_ 0
C_ 111,110,111
C_ 110,108,109
Mavv 0,30,116
s_nop 0
Tv 38,v4,12288
Tv 40,v4,18432
Mavv 20,34,116
E_ 112
Mavv 24,34,120
s_nop 0
Tv 42,v5,12288
Tv 44,v5,18432
Mavv 4,30,120
E_ 113
Mavv 8,30,124
E_ 114
Mavv 28,34,124
E_ 115
Mavv 32,34,128
C_ 115,114,115
C_ 114,112,113
Mavv 12,30,128
E_ 76
Mavv 16,30,132
E_ 77
Mavv 36,34,132
W_ 0
E_ 78
Mavv 56,38,132
s_nop 0
Tv 30,v2,13312
Tv 32,v2,19456
Mavv 76,42,132
E_ 79
Mavv 72,42,128
s_nop 0
Tv 34,v3,13312
Tv 36,v3,19456
Mavv 52,38,128
C_ 96,76,77
C_ 97,78,79
Mavv 48,38,124
E_ 80
Mavv 68,42,124
E_ 81
Mavv 64,42,120
E_ 82
Mavv 44,38,120
E_ 83
Mavv 40,38,116
C_ 100,80,81
C_ 101,82,83
Mavv 60,42,116
W_ 0
E_ 84
Mavv 80,30,116
s_nop 0
Tv 38,v4,13312
Tv 40,v4,19456
Mavv 100,34,116
E_ 85
Mavv 104,34,120
s_nop 0
Tv 42,v5,13312
Tv 44,v5,19456
Mavv 84,30,120
E_ 86
Mavv 88,30,124
E_ 87
Mavv 108,34,124
C_ 104,84,85
C_ 105,86,87
Mavv 112,34,128
E_ 88
Mavv 92,30,128
E_ 89
Mavv 96,30,132
E_ 90
Mavv 116,34,132
W_ 0
E_ 91
Mavv 136,38,132
s_nop 0
Tv 30,v2,14336
Tv 32,v2,20480
Mavv 156,42,132
C_ 108,88,89
C_ 109,90,91
Mavv 152,42,128
s_nop 0
Tv 34,v3,14336
Tv 36,v3,20480
Mavv 132,38,128
E_ 92
Mavv 128,38,124
E_ 93
Mavv 148,42,124
E_ 94
Mavv 144,42,120
E_ 95
Mavv 124,38,120
C_ 112,92,93
C_ 113,94,95
Mavv 120,38,116
v_mov_b32 v92, 0x3f803f80
v_mov_b32 v93, 0x3f803f80
Mavv 140,42,116
W_ 0
v_mov_b32 v94, 0x3f803f80
v_mov_b32 v95, 0x3f803f80
Mavv 160,30,116
s_nop 0
Tv 38,v4,14336
Tv 40,v4,20480
Mavv 180,34,116
Mavv 184,34,120
s_nop 0
Tv 42,v5,14336
Tv 44,v5,20480
Mavv 164,30,120
Mavv 168,30,124
Mavv 188,34,124
S_ s65,s61,49152
Mavv 192,34,128
S_ m0,s51,12288
Mavv 172,30,128
s_waitcnt vmcnt(6) lgkmcnt(0)
s_barrier
Mavv 176,30,132
Ln v7,32,s65,0
Mavv 196,34,132
Mavv 216,38,132
Dv 30,%0,30720
Mavv 236,42,132
Dv 34,v1,30720
Mavv 232,42,128
Ln v8,32,s65,3072
S_ m0,s51,13184
Mavv 212,38,128
Mavv 208,38,124
Ln v7,32,s65,128
Mavv 228,42,124
Mavv 224,42,120
Ln v8,32,s65,3200
S_ m0,s51,14080
Mavv 204,38,120
Mavv 200,38,116
Ln v7,32,s65,256
Mavv 220,42,116
s_cmp_eq_u32 s4, 0
s_cbranch_scc1 .Lhs_a2
.Lhs_a2_back:
.Lstep2_3:
Zvvv 76,92,96
Dv 38,%0,31744
Zvvv 80,92,100
Dv 42,v1,31744
Zvvv 84,92,104
Zvvv 88,92,108
Zvvv 92,92,112
W_ 2
Nvvv 116,30,136,56
Mvvv 116,34,156
Nvvv 120,30,140,60
s_nop 1
v_max3_f32 v10, v76, v80, v84
s_nop 0
v_max3_f32 v10, v10, v88, v92
Mvvv 120,34,160
v_cmp_lt_f32 vcc, 0x42000000, v10
s_cbranch_vccnz .Ltramp2_2
Nvvv 124,30,144,64
A_ v46,v46,v76
A_ v47,v47,v80
Mvvv 124,34,164
A_ v48,v48,v84
A_ v49,v49,v88
Nvvv 128,30,148,68
A_ v50,v50,v92
Mvvv 128,34,168
Ln v8,32,s65,3328
Nvvv 132,30,152,72
Mvvv 132,34,172
W_ 0
Mvvv 132,38,192
Dv 30,%0,32768
Mvvv 132,42,212
Dv 34,v1,32768
Mvvv 128,38,188
Mvvv 128,42,208
Mvvv 124,38,184
Mvvv 124,42,204
Mvvv 120,38,180
Mvvv 120,42,200
Mvvv 116,38,176
Mvvv 116,42,196
W_ 0
Mvvv 116,30,216
Dv 38,%0,24576
Mvvv 116,34,236
Dv 42,v1,24576
Mvvv 120,30,220
Mvvv 120,34,240
Mvvv 124,30,224
Mvvv 124,34,244
Mvvv 128,30,228
Mvvv 128,34,248
s_nop 0
E_ 116
Mvvv 132,30,232
E_ 117
Mvvv 132,34,252
W_ 0
E_ 118
Nvvv 76,38,136,56
Dv 30,%0,25600
Mvvv 76,42,156
Dv 34,v1,25600
Nvvv 80,38,140,60
E_ 119
Mvvv 80,42,160
C_ 119,118,119
C_ 118,116,117
Nvvv 84,38,144,64
E_ 120
Mvvv 84,42,164
E_ 121
Nvvv 88,38,148,68
E_ 122
Mvvv 88,42,168
E_ 123
Nvvv 92,38,152,72
C_ 123,122,123
C_ 122,120,121
Mvvv 92,42,172
W_ 0
E_ 124
Mvvv 92,30,192
Dv 38,%0,26624
Mvvv 92,34,212
Dv 42,v1,26624
Mvvv 88,30,188
E_ 125
Mvvv 88,34,208
E_ 126
Mvvv 84,30,184
S_ s65,s61,36864
E_ 127
Mvvv 84,34,204
S_ m0,s51,24576
C_ 127,126,127
C_ 126,124,125
Mvvv 80,30,180
s_waitcnt vmcnt(6) lgkmcnt(0)
s_barrier
Mvvv 80,34,200
L_ v9,36,s65,0
Mvvv 76,30,176
E_ 128
Mvvv 76,34,196
L_ v9,36,s65,3072
S_ m0,s51,25472
Mvvv 76,38,216
Tv 30,v2,0
Tv 32,v2,6144
Mvvv 76,42,236
L_ v9,36,s65,128
Mvvv 80,38,220
s_nop 0
Tv 34,v3,0
Tv 36,v3,6144
Mvvv 80,42,240
L_ v9,36,s65,3200
S_ m0,s51,26368
Mvvv 84,38,224
E_ 129
Mvvv 84,42,244
L_ v9,36,s65,256
Mvvv 88,38,228
E_ 130
Mvvv 88,42,248
L_ v9,36,s65,3328
Mvvv 92,38,232
E_ 131
Mvvv 92,42,252
W_ 0
C_ 131,130,131
C_ 130,128,129
Mavv 0,30,96
s_nop 0
Tv 38,v4,0
Tv 40,v4,6144
Mavv 20,34,96
E_ 132
Mavv 24,34,100
s_nop 0
Tv 42,v5,0
Tv 44,v5,6144
Mavv 4,30,100
E_ 133
Mavv 8,30,104
E_ 134
Mavv 28,34,104
E_ 135
Mavv 32,34,108
C_ 135,134,135
C_ 134,132,133
Mavv 12,30,108
E_ 76
Mavv 16,30,112
E_ 77
Mavv 36,34,112
W_ 0
E_ 78
Mavv 56,38,112
s_nop 0
Tv 30,v2,1024
Tv 32,v2,7168
Mavv 76,42,112
E_ 79
Mavv 72,42,108
s_nop 0
Tv 34,v3,1024
Tv 36,v3,7168
Mavv 52,38,108
C_ 116,76,77
C_ 117,78,79
Mavv 48,38,104
E_ 80
Mavv 68,42,104
E_ 81
Mavv 64,42,100
E_ 82
Mavv 44,38,100
E_ 83
Mavv 40,38,96
C_ 120,80,81
C_ 121,82,83
Mavv 60,42,96
W_ 0
E_ 84
Mavv 80,30,96
s_nop 0
Tv 38,v4,1024
Tv 40,v4,7168
Mavv 100,34,96
E_ 85
Mavv 104,34,100
s_nop 0
Tv 42,v5,1024
Tv 44,v5,7168
Mavv 84,30,100
E_ 86
Mavv 88,30,104
E_ 87
Mavv 108,34,104
C_ 124,84,85
C_ 125,86,87
Mavv 112,34,108
E_ 88
Mavv 92,30,108
E_ 89
Mavv 96,30,112
E_ 90
Mavv 116,34,112
W_ 0
E_ 91
Mavv 136,38,112
s_nop 0
Tv 30,v2,2048
Tv 32,v2,8192
Mavv 156,42,112
C_ 128,88,89
C_ 129,90,91
Mavv 152,42,108
s_nop 0
Tv 34,v3,2048
Tv 36,v3,8192
Mavv 132,38,108
E_ 92
Mavv 128,38,104
E_ 93
Mavv 148,42,104
E_ 94
Mavv 144,42,100
E_ 95
Mavv 124,38,100
C_ 132,92,93
C_ 133,94,95
Mavv 120,38,96
v_mov_b32 v92, 0x3f803f80
v_mov_b32 v93, 0x3f803f80
Mavv 140,42,96
W_ 0
v_mov_b32 v94, 0x3f803f80
v_mov_b32 v95, 0x3f803f80
Mavv 160,30,96
s_nop 0
Tv 38,v4,2048
Tv 40,v4,8192
Mavv 180,34,96
Mavv 184,34,100
s_nop 0
Tv 42,v5,2048
Tv 44,v5,8192
Mavv 164,30,100
Mavv 168,30,104
Mavv 188,34,104
S_ s65,s61,61440
Mavv 192,34,108
S_ m0,s51,0
Mavv 172,30,108
s_waitcnt vmcnt(6) lgkmcnt(0)
s_barrier
Mavv 176,30,112
Ln v7,32,s65,0
Mavv 196,34,112
Mavv 216,38,112
Dv 30,%0,18432
Mavv 236,42,112
Dv 34,v1,18432
Mavv 232,42,108
Ln v8,32,s65,3072
S_ m0,s51,896
Mavv 212,38,108
Mavv 208,38,104
Ln v7,32,s65,128
Mavv 228,42,104
Mavv 224,42,100
Ln v8,32,s65,3200
S_ m0,s51,1792
Mavv 204,38,100
Mavv 200,38,96
Ln v7,32,s65,256
Mavv 220,42,96
.Lstep2_4:
Zvvv 76,92,116
Dv 38,%0,19456
Zvvv 80,92,120
Dv 42,v1,19456
Zvvv 84,92,124
Zvvv 88,92,128
Zvvv 92,92,132
W_ 2
Nvvv 96,30,136,56
Mvvv 96,34,156
Nvvv 100,30,140,60
s_nop 1
v_max3_f32 v10, v76, v80, v84
s_nop 0
v_max3_f32 v10, v10, v88, v92
Mvvv 100,34,160
v_cmp_lt_f32 vcc, 0x42000000, v10
s_cbranch_vccnz .Ltramp2_3
Nvvv 104,30,144,64
A_ v46,v46,v76
A_ v47,v47,v80
Mvvv 104,34,164
A_ v48,v48,v84
A_ v49,v49,v88
Nvvv 108,30,148,68
A_ v50,v50,v92
Mvvv 108,34,168
Ln v8,32,s65,3328
Nvvv 112,30,152,72
Mvvv 112,34,172
W_ 0
Mvvv 112,38,192
Dv 30,%0,20480
Mvvv 112,42,212
Dv 34,v1,20480
Mvvv 108,38,188
Mvvv 108,42,208
Mvvv 104,38,184
Mvvv 104,42,204
Mvvv 100,38,180
Mvvv 100,42,200
Mvvv 96,38,176
Mvvv 96,42,196
W_ 0
Mvvv 96,30,216
Dv 38,%0,12288
Mvvv 96,34,236
Dv 42,v1,12288
Mvvv 100,30,220
Mvvv 100,34,240
Mvvv 104,30,224
Mvvv 104,34,244
Mvvv 108,30,228
Mvvv 108,34,248
s_nop 0
E_ 96
Mvvv 112,30,232
E_ 97
Mvvv 112,34,252
W_ 0
E_ 98
Nvvv 76,38,136,56
Dv 30,%0,13312
Mvvv 76,42,156
Dv 34,v1,13312
Nvvv 80,38,140,60
E_ 99
Mvvv 80,42,160
C_ 99,98,99
C_ 98,96,97
Nvvv 84,38,144,64
E_ 100
Mvvv 84,42,164
E_ 101
Nvvv 88,38,148,68
E_ 102
Mvvv 88,42,168
E_ 103
Nvvv 92,38,152,72
C_ 103,102,103
C_ 102,100,101
Mvvv 92,42,172
W_ 0
E_ 104
Mvvv 92,30,192
Dv 38,%0,14336
Mvvv 92,34,212
Dv 42,v1,14336
Mvvv 88,30,188
E_ 105
Mvvv 88,34,208
E_ 106
Mvvv 84,30,184
S_ s65,s61,49152
E_ 107
Mvvv 84,34,204
S_ m0,s51,12288
C_ 107,106,107
C_ 106,104,105
Mvvv 80,30,180
s_waitcnt vmcnt(6) lgkmcnt(0)
s_barrier
Mvvv 80,34,200
L_ v9,36,s65,0
Mvvv 76,30,176
E_ 108
Mvvv 76,34,196
L_ v9,36,s65,3072
S_ m0,s51,13184
Mvvv 76,38,216
Tv 30,v2,24576
Tv 32,v2,30720
Mvvv 76,42,236
L_ v9,36,s65,128
Mvvv 80,38,220
s_nop 0
Tv 34,v3,24576
Tv 36,v3,30720
Mvvv 80,42,240
L_ v9,36,s65,3200
S_ m0,s51,14080
Mvvv 84,38,224
E_ 109
Mvvv 84,42,244
L_ v9,36,s65,256
Mvvv 88,38,228
E_ 110
Mvvv 88,42,248
L_ v9,36,s65,3328
Mvvv 92,38,232
E_ 111
Mvvv 92,42,252
W_ 0
C_ 111,110,111
C_ 110,108,109
Mavv 0,30,116
s_nop 0
Tv 38,v4,24576
Tv 40,v4,30720
Mavv 20,34,116
E_ 112
Mavv 24,34,120
s_nop 0
Tv 42,v5,24576
Tv 44,v5,30720
Mavv 4,30,120
E_ 113
Mavv 8,30,124
E_ 114
Mavv 28,34,124
E_ 115
Mavv 32,34,128
C_ 115,114,115
C_ 114,112,113
Mavv 12,30,128
E_ 76
Mavv 16,30,132
E_ 77
Mavv 36,34,132
W_ 0
E_ 78
Mavv 56,38,132
s_nop 0
Tv 30,v2,25600
Tv 32,v2,31744
Mavv 76,42,132
E_ 79
Mavv 72,42,128
s_nop 0
Tv 34,v3,25600
Tv 36,v3,31744
Mavv 52,38,128
C_ 96,76,77
C_ 97,78,79
Mavv 48,38,124
E_ 80
Mavv 68,42,124
E_ 81
Mavv 64,42,120
E_ 82
Mavv 44,38,120
E_ 83
Mavv 40,38,116
C_ 100,80,81
C_ 101,82,83
Mavv 60,42,116
W_ 0
E_ 84
Mavv 80,30,116
s_nop 0
Tv 38,v4,25600
Tv 40,v4,31744
Mavv 100,34,116
E_ 85
Mavv 104,34,120
s_nop 0
Tv 42,v5,25600
Tv 44,v5,31744
Mavv 84,30,120
E_ 86
Mavv 88,30,124
E_ 87
Mavv 108,34,124
C_ 104,84,85
C_ 105,86,87
Mavv 112,34,128
E_ 88
Mavv 92,30,128
E_ 89
Mavv 96,30,132
E_ 90
Mavv 116,34,132
W_ 0
E_ 91
Mavv 136,38,132
s_nop 0
Tv 30,v2,26624
Tv 32,v2,32768
Mavv 156,42,132
C_ 108,88,89
C_ 109,90,91
Mavv 152,42,128
s_nop 0
Tv 34,v3,26624
Tv 36,v3,32768
Mavv 132,38,128
E_ 92
Mavv 128,38,124
E_ 93
Mavv 148,42,124
E_ 94
Mavv 144,42,120
E_ 95
Mavv 124,38,120
C_ 112,92,93
C_ 113,94,95
Mavv 120,38,116
v_mov_b32 v92, 0x3f803f80
v_mov_b32 v93, 0x3f803f80
Mavv 140,42,116
W_ 0
v_mov_b32 v94, 0x3f803f80
v_mov_b32 v95, 0x3f803f80
Mavv 160,30,116
s_nop 0
Tv 38,v4,26624
Tv 40,v4,32768
Mavv 180,34,116
Mavv 184,34,120
s_nop 0
Tv 42,v5,26624
Tv 44,v5,32768
Mavv 164,30,120
Mavv 168,30,124
Mavv 188,34,124
S_ s65,s61,73728
Mavv 192,34,128
S_ m0,s51,24576
Mavv 172,30,128
s_waitcnt vmcnt(6) lgkmcnt(0)
s_barrier
Mavv 176,30,132
Ln v7,32,s65,0
Mavv 196,34,132
Mavv 216,38,132
Dv 30,%0,6144
Mavv 236,42,132
Dv 34,v1,6144
Mavv 232,42,128
Ln v8,32,s65,3072
S_ m0,s51,25472
Mavv 212,38,128
Mavv 208,38,124
Ln v7,32,s65,128
Mavv 228,42,124
Mavv 224,42,120
Ln v8,32,s65,3200
S_ m0,s51,26368
Mavv 204,38,120
Mavv 200,38,116
Ln v7,32,s65,256
Mavv 220,42,116
.Lstep2_5:
Zvvv 76,92,96
Dv 38,%0,7168
Zvvv 80,92,100
Dv 42,v1,7168
Zvvv 84,92,104
Zvvv 88,92,108
Zvvv 92,92,112
W_ 2
Nvvv 116,30,136,56
Mvvv 116,34,156
Nvvv 120,30,140,60
s_nop 1
v_max3_f32 v10, v76, v80, v84
s_nop 0
v_max3_f32 v10, v10, v88, v92
Mvvv 120,34,160
v_cmp_lt_f32 vcc, 0x42000000, v10
s_cbranch_vccnz .Ltramp2_4
Nvvv 124,30,144,64
A_ v46,v46,v76
A_ v47,v47,v80
Mvvv 124,34,164
A_ v48,v48,v84
A_ v49,v49,v88
Nvvv 128,30,148,68
A_ v50,v50,v92
Mvvv 128,34,168
Ln v8,32,s65,3328
Nvvv 132,30,152,72
Mvvv 132,34,172
W_ 0
Mvvv 132,38,192
Dv 30,%0,8192
Mvvv 132,42,212
Dv 34,v1,8192
Mvvv 128,38,188
Mvvv 128,42,208
Mvvv 124,38,184
Mvvv 124,42,204
Mvvv 120,38,180
Mvvv 120,42,200
Mvvv 116,38,176
Mvvv 116,42,196
W_ 0
Mvvv 116,30,216
Dv 38,%0,0
Mvvv 116,34,236
Dv 42,v1,0
Mvvv 120,30,220
Mvvv 120,34,240
Mvvv 124,30,224
Mvvv 124,34,244
Mvvv 128,30,228
Mvvv 128,34,248
s_nop 0
E_ 116
Mvvv 132,30,232
E_ 117
Mvvv 132,34,252
W_ 0
E_ 118
Nvvv 76,38,136,56
Dv 30,%0,1024
Mvvv 76,42,156
Dv 34,v1,1024
Nvvv 80,38,140,60
E_ 119
Mvvv 80,42,160
C_ 119,118,119
C_ 118,116,117
Nvvv 84,38,144,64
E_ 120
Mvvv 84,42,164
E_ 121
Nvvv 88,38,148,68
E_ 122
Mvvv 88,42,168
E_ 123
Nvvv 92,38,152,72
C_ 123,122,123
C_ 122,120,121
Mvvv 92,42,172
W_ 0
E_ 124
Mvvv 92,30,192
Dv 38,%0,2048
Mvvv 92,34,212
Dv 42,v1,2048
Mvvv 88,30,188
E_ 125
Mvvv 88,34,208
E_ 126
Mvvv 84,30,184
S_ s65,s61,61440
E_ 127
Mvvv 84,34,204
S_ m0,s51,0
C_ 127,126,127
C_ 126,124,125
Mvvv 80,30,180
s_waitcnt vmcnt(6) lgkmcnt(0)
s_barrier
Mvvv 80,34,200
L_ v9,36,s65,0
Mvvv 76,30,176
E_ 128
Mvvv 76,34,196
L_ v9,36,s65,3072
S_ m0,s51,896
Mvvv 76,38,216
Tv 30,v2,12288
Tv 32,v2,18432
Mvvv 76,42,236
L_ v9,36,s65,128
Mvvv 80,38,220
s_nop 0
Tv 34,v3,12288
Tv 36,v3,18432
Mvvv 80,42,240
L_ v9,36,s65,3200
S_ m0,s51,1792
Mvvv 84,38,224
E_ 129
Mvvv 84,42,244
L_ v9,36,s65,256
Mvvv 88,38,228
E_ 130
Mvvv 88,42,248
L_ v9,36,s65,3328
Mvvv 92,38,232
E_ 131
Mvvv 92,42,252
W_ 0
C_ 131,130,131
C_ 130,128,129
Mavv 0,30,96
s_nop 0
Tv 38,v4,12288
Tv 40,v4,18432
Mavv 20,34,96
E_ 132
Mavv 24,34,100
s_nop 0
Tv 42,v5,12288
Tv 44,v5,18432
Mavv 4,30,100
E_ 133
Mavv 8,30,104
E_ 134
Mavv 28,34,104
E_ 135
Mavv 32,34,108
C_ 135,134,135
C_ 134,132,133
Mavv 12,30,108
E_ 76
Mavv 16,30,112
E_ 77
Mavv 36,34,112
W_ 0
E_ 78
)A"
R"A(Mavv 56,38,112
s_nop 0
Tv 30,v2,13312
Tv 32,v2,19456
Mavv 76,42,112
E_ 79
Mavv 72,42,108
s_nop 0
Tv 34,v3,13312
Tv 36,v3,19456
Mavv 52,38,108
C_ 116,76,77
C_ 117,78,79
Mavv 48,38,104
E_ 80
Mavv 68,42,104
E_ 81
Mavv 64,42,100
E_ 82
Mavv 44,38,100
E_ 83
Mavv 40,38,96
C_ 120,80,81
C_ 121,82,83
Mavv 60,42,96
W_ 0
E_ 84
Mavv 80,30,96
s_nop 0
Tv 38,v4,13312
Tv 40,v4,19456
Mavv 100,34,96
E_ 85
Mavv 104,34,100
s_nop 0
Tv 42,v5,13312
Tv 44,v5,19456
Mavv 84,30,100
E_ 86
Mavv 88,30,104
E_ 87
Mavv 108,34,104
C_ 124,84,85
C_ 125,86,87
Mavv 112,34,108
E_ 88
Mavv 92,30,108
E_ 89
Mavv 96,30,112
E_ 90
Mavv 116,34,112
W_ 0
E_ 91
Mavv 136,38,112
s_nop 0
Tv 30,v2,14336
Tv 32,v2,20480
Mavv 156,42,112
C_ 128,88,89
C_ 129,90,91
Mavv 152,42,108
s_nop 0
Tv 34,v3,14336
Tv 36,v3,20480
Mavv 132,38,108
E_ 92
Mavv 128,38,104
E_ 93
Mavv 148,42,104
E_ 94
Mavv 144,42,100
E_ 95
Mavv 124,38,100
C_ 132,92,93
C_ 133,94,95
Mavv 120,38,96
v_mov_b32 v92, 0x3f803f80
v_mov_b32 v93, 0x3f803f80
Mavv 140,42,96
W_ 0
v_mov_b32 v94, 0x3f803f80
v_mov_b32 v95, 0x3f803f80
Mavv 160,30,96
s_nop 0
Tv 38,v4,14336
Tv 40,v4,20480
Mavv 180,34,96
Mavv 184,34,100
s_nop 0
Tv 42,v5,14336
Tv 44,v5,20480
Mavv 164,30,100
Mavv 168,30,104
Mavv 188,34,104
S_ s65,s61,86016
Mavv 192,34,108
S_ m0,s51,12288
Mavv 172,30,108
s_waitcnt vmcnt(6) lgkmcnt(0)
s_barrier
Mavv 176,30,112
Ln v7,32,s65,0
Mavv 196,34,112
Mavv 216,38,112
Dv 30,%0,30720
Mavv 236,42,112
Dv 34,v1,30720
Mavv 232,42,108
Ln v8,32,s65,3072
S_ m0,s51,13184
Mavv 212,38,108
Mavv 208,38,104
Ln v7,32,s65,128
Mavv 228,42,104
Mavv 224,42,100
Ln v8,32,s65,3200
S_ m0,s51,14080
Mavv 204,38,100
Mavv 200,38,96
Ln v7,32,s65,256
Mavv 220,42,96
.Lstep2_6:
Zvvv 76,92,116
Dv 38,%0,31744
Zvvv 80,92,120
Dv 42,v1,31744
Zvvv 84,92,124
Zvvv 88,92,128
Zvvv 92,92,132
W_ 2
Nvvv 96,30,136,56
Mvvv 96,34,156
Nvvv 100,30,140,60
s_nop 1
v_max3_f32 v10, v76, v80, v84
s_nop 0
v_max3_f32 v10, v10, v88, v92
Mvvv 100,34,160
v_cmp_lt_f32 vcc, 0x42000000, v10
s_cbranch_vccnz .Ltramp2_5
Nvvv 104,30,144,64
A_ v46,v46,v76
A_ v47,v47,v80
Mvvv 104,34,164
A_ v48,v48,v84
A_ v49,v49,v88
Nvvv 108,30,148,68
A_ v50,v50,v92
Mvvv 108,34,168
Ln v8,32,s65,3328
Nvvv 112,30,152,72
R_ v11,a248
Mvvv 112,34,172
W_ 0
v_lshlrev_b32 v11, 10, v11
Mvvv 112,38,192
Dv 30,%0,32768
Mvvv 112,42,212
Dv 34,v1,32768
Mvvv 108,38,188
buffer_load_dword a254, v11, s[44:47], s66 offen
Mvvv 108,42,208
Mvvv 104,38,184
Mvvv 104,42,204
Mvvv 100,38,180
Mvvv 100,42,200
Mvvv 96,38,176
Mvvv 96,42,196
W_ 0
Mvvv 96,30,216
Dv 38,%0,24576
Mvvv 96,34,236
Dv 42,v1,24576
Mvvv 100,30,220
Mvvv 100,34,240
Mvvv 104,30,224
Mvvv 104,34,244
Mvvv 108,30,228
Mvvv 108,34,248
s_nop 0
E_ 96
Mvvv 112,30,232
E_ 97
Mvvv 112,34,252
W_ 0
E_ 98
Nvvv 76,38,136,56
Dv 30,%0,25600
Mvvv 76,42,156
Dv 34,v1,25600
Nvvv 80,38,140,60
E_ 99
Mvvv 80,42,160
C_ 99,98,99
C_ 98,96,97
Nvvv 84,38,144,64
E_ 100
Mvvv 84,42,164
E_ 101
Nvvv 88,38,148,68
E_ 102
Mvvv 88,42,168
E_ 103
Nvvv 92,38,152,72
C_ 103,102,103
C_ 102,100,101
Mvvv 92,42,172
W_ 0
E_ 104
Mvvv 92,30,192
Dv 38,%0,26624
Mvvv 92,34,212
Dv 42,v1,26624
Mvvv 88,30,188
E_ 105
Mvvv 88,34,208
E_ 106
Mvvv 84,30,184
S_ s65,s61,73728
E_ 107
Mvvv 84,34,204
S_ m0,s51,24576
C_ 107,106,107
C_ 106,104,105
Mvvv 80,30,180
s_waitcnt vmcnt(7) lgkmcnt(0)
s_barrier
Mvvv 80,34,200
L_ v9,36,s65,0
Mvvv 76,30,176
E_ 108
Mvvv 76,34,196
L_ v9,36,s65,3072
S_ m0,s51,25472
Mvvv 76,38,216
Tv 30,v2,0
Tv 32,v2,6144
Mvvv 76,42,236
L_ v9,36,s65,128
Mvvv 80,38,220
s_nop 0
Tv 34,v3,0
Tv 36,v3,6144
Mvvv 80,42,240
L_ v9,36,s65,3200
S_ m0,s51,26368
Mvvv 84,38,224
E_ 109
Mvvv 84,42,244
L_ v9,36,s65,256
Mvvv 88,38,228
E_ 110
Mvvv 88,42,248
L_ v9,36,s65,3328
Mvvv 92,38,232
E_ 111
Mvvv 92,42,252
W_ 0
C_ 111,110,111
C_ 110,108,109
Mavv 0,30,116
s_nop 0
Tv 38,v4,0
Tv 40,v4,6144
Mavv 20,34,116
E_ 112
Mavv 24,34,120
s_nop 0
Tv 42,v5,0
Tv 44,v5,6144
Mavv 4,30,120
E_ 113
Mavv 8,30,124
E_ 114
Mavv 28,34,124
E_ 115
Mavv 32,34,128
C_ 115,114,115
C_ 114,112,113
Mavv 12,30,128
E_ 76
Mavv 16,30,132
E_ 77
Mavv 36,34,132
W_ 0
E_ 78
Mavv 56,38,132
s_nop 0
Tv 30,v2,1024
Tv 32,v2,7168
Mavv 76,42,132
E_ 79
Mavv 72,42,128
s_nop 0
Tv 34,v3,1024
Tv 36,v3,7168
Mavv 52,38,128
C_ 96,76,77
C_ 97,78,79
Mavv 48,38,124
E_ 80
Mavv 68,42,124
E_ 81
Mavv 64,42,120
E_ 82
Mavv 44,38,120
E_ 83
Mavv 40,38,116
C_ 100,80,81
C_ 101,82,83
Mavv 60,42,116
W_ 0
E_ 84
Mavv 80,30,116
s_nop 0
Tv 38,v4,1024
Tv 40,v4,7168
Mavv 100,34,116
E_ 85
Mavv 104,34,120
s_nop 0
Tv 42,v5,1024
Tv 44,v5,7168
Mavv 84,30,120
E_ 86
Mavv 88,30,124
E_ 87
Mavv 108,34,124
C_ 104,84,85
C_ 105,86,87
Mavv 112,34,128
E_ 88
Mavv 92,30,128
E_ 89
Mavv 96,30,132
E_ 90
Mavv 116,34,132
W_ 0
E_ 91
Mavv 136,38,132
s_nop 0
Tv 30,v2,2048
Tv 32,v2,8192
Mavv 156,42,132
C_ 108,88,89
C_ 109,90,91
Mavv 152,42,128
s_nop 0
Tv 34,v3,2048
Tv 36,v3,8192
Mavv 132,38,128
E_ 92
Mavv 128,38,124
E_ 93
Mavv 148,42,124
E_ 94
Mavv 144,42,120
E_ 95
Mavv 124,38,120
C_ 112,92,93
C_ 113,94,95
Mavv 120,38,116
v_mov_b32 v92, 0x3f803f80
v_mov_b32 v93, 0x3f803f80
Mavv 140,42,116
W_ 0
v_mov_b32 v94, 0x3f803f80
v_mov_b32 v95, 0x3f803f80
Mavv 160,30,116
s_nop 0
Tv 38,v4,2048
Tv 40,v4,8192
Mavv 180,34,116
Mavv 184,34,120
s_nop 0
Tv 42,v5,2048
Tv 44,v5,8192
Mavv 164,30,120
Mavv 168,30,124
Mavv 188,34,124
S_ s65,s61,98304
Mavv 192,34,128
S_ m0,s51,0
Mavv 172,30,128
s_waitcnt vmcnt(7) lgkmcnt(0)
s_barrier
Mavv 176,30,132
Ln v7,32,s65,0
Mavv 196,34,132
Mavv 216,38,132
Dv 30,%0,18432
Mavv 236,42,132
Dv 34,v1,18432
Mavv 232,42,128
Ln v8,32,s65,3072
S_ m0,s51,896
Mavv 212,38,128
Mavv 208,38,124
Ln v7,32,s65,128
Mavv 228,42,124
Mavv 224,42,120
Ln v8,32,s65,3200
S_ m0,s51,1792
Mavv 204,38,120
Mavv 200,38,116
Ln v7,32,s65,256
Mavv 220,42,116
s_cmp_eq_u32 s4, 0
s_cbranch_scc1 .Lhs_c2
.Lhs_c2_back:
.Lstep2_7:
Zvvv 76,92,96
Dv 38,%0,19456
Zvvv 80,92,100
Dv 42,v1,19456
Zvvv 84,92,104
Zvvv 88,92,108
Zvvv 92,92,112
W_ 2
Nvvv 116,30,136,56
Mvvv 116,34,156
Nvvv 120,30,140,60
s_nop 1
v_max3_f32 v10, v76, v80, v84
s_nop 0
v_max3_f32 v10, v10, v88, v92
Mvvv 120,34,160
v_cmp_lt_f32 vcc, 0x42000000, v10
s_cbranch_vccnz .Ltramp2_6
Nvvv 124,30,144,64
A_ v46,v46,v76
A_ v47,v47,v80
Mvvv 124,34,164
A_ v48,v48,v84
A_ v49,v49,v88
Nvvv 128,30,148,68
A_ v50,v50,v92
Mvvv 128,34,168
Ln v8,32,s65,3328
Nvvv 132,30,152,72
Mvvv 132,34,172
W_ 0
Mvvv 132,38,192
Dv 30,%0,20480
Mvvv 132,42,212
Dv 34,v1,20480
Mvvv 128,38,188
Mvvv 128,42,208
Mvvv 124,38,184
Mvvv 124,42,204
Mvvv 120,38,180
Mvvv 120,42,200
Mvvv 116,38,176
Mvvv 116,42,196
W_ 0
Mvvv 116,30,216
Dv 38,%0,12288
Mvvv 116,34,236
Dv 42,v1,12288
Mvvv 120,30,220
Mvvv 120,34,240
Mvvv 124,30,224
Mvvv 124,34,244
Mvvv 128,30,228
Mvvv 128,34,248
s_nop 0
E_ 116
Mvvv 132,30,232
E_ 117
Mvvv 132,34,252
W_ 0
E_ 118
Nvvv 76,38,136,56
Dv 30,%0,13312
Mvvv 76,42,156
Dv 34,v1,13312
Nvvv 80,38,140,60
E_ 119
Mvvv 80,42,160
C_ 119,118,119
C_ 118,116,117
Nvvv 84,38,144,64
E_ 120
Mvvv 84,42,164
E_ 121
Nvvv 88,38,148,68
E_ 122
Mvvv 88,42,168
E_ 123
Nvvv 92,38,152,72
C_ 123,122,123
C_ 122,120,121
Mvvv 92,42,172
W_ 0
E_ 124
Mvvv 92,30,192
Dv 38,%0,14336
Mvvv 92,34,212
Dv 42,v1,14336
Mvvv 88,30,188
E_ 125
Mvvv 88,34,208
E_ 126
Mvvv 84,30,184
S_ s65,s61,86016
E_ 127
Mvvv 84,34,204
S_ m0,s51,12288
C_ 127,126,127
C_ 126,124,125
Mvvv 80,30,180
s_waitcnt vmcnt(6) lgkmcnt(0)
s_barrier
Mvvv 80,34,200
L_ v9,36,s65,0
Mvvv 76,30,176
E_ 128
Mvvv 76,34,196
L_ v9,36,s65,3072
S_ m0,s51,13184
Mvvv 76,38,216
Tv 30,v2,24576
Tv 32,v2,30720
Mvvv 76,42,236
L_ v9,36,s65,128
Mvvv 80,38,220
s_nop 0
Tv 34,v3,24576
Tv 36,v3,30720
Mvvv 80,42,240
L_ v9,36,s65,3200
S_ m0,s51,14080
Mvvv 84,38,224
E_ 129
Mvvv 84,42,244
L_ v9,36,s65,256
Mvvv 88,38,228
E_ 130
Mvvv 88,42,248
L_ v9,36,s65,3328
Mvvv 92,38,232
E_ 131
Mvvv 92,42,252
W_ 0
C_ 131,130,131
C_ 130,128,129
Mavv 0,30,96
s_nop 0
Tv 38,v4,24576
Tv 40,v4,30720
Mavv 20,34,96
E_ 132
Mavv 24,34,100
s_nop 0
Tv 42,v5,24576
Tv 44,v5,30720
Mavv 4,30,100
E_ 133
Mavv 8,30,104
E_ 134
Mavv 28,34,104
E_ 135
Mavv 32,34,108
C_ 135,134,135
C_ 134,132,133
Mavv 12,30,108
E_ 76
Mavv 16,30,112
E_ 77
Mavv 36,34,112
W_ 0
E_ 78
Mavv 56,38,112
s_nop 0
Tv 30,v2,25600
Tv 32,v2,31744
Mavv 76,42,112
E_ 79
Mavv 72,42,108
s_nop 0
Tv 34,v3,25600
Tv 36,v3,31744
Mavv 52,38,108
C_ 116,76,77
C_ 117,78,79
Mavv 48,38,104
E_ 80
Mavv 68,42,104
E_ 81
Mavv 64,42,100
E_ 82
Mavv 44,38,100
E_ 83
Mavv 40,38,96
C_ 120,80,81
C_ 121,82,83
Mavv 60,42,96
W_ 0
E_ 84
Mavv 80,30,96
s_nop 0
Tv 38,v4,25600
Tv 40,v4,31744
Mavv 100,34,96
E_ 85
Mavv 104,34,100
s_nop 0
Tv 42,v5,25600
Tv 44,v5,31744
Mavv 84,30,100
E_ 86
Mavv 88,30,104
E_ 87
Mavv 108,34,104
C_ 124,84,85
C_ 125,86,87
Mavv 112,34,108
E_ 88
Mavv 92,30,108
E_ 89
Mavv 96,30,112
E_ 90
Mavv 116,34,112
W_ 0
E_ 91
Mavv 136,38,112
s_nop 0
Tv 30,v2,26624
Tv 32,v2,32768
Mavv 156,42,112
C_ 128,88,89
C_ 129,90,91
Mavv 152,42,108
s_nop 0
Tv 34,v3,26624
Tv 36,v3,32768
Mavv 132,38,108
E_ 92
Mavv 128,38,104
E_ 93
Mavv 148,42,104
E_ 94
Mavv 144,42,100
E_ 95
Mavv 124,38,100
C_ 132,92,93
C_ 133,94,95
Mavv 120,38,96
v_mov_b32 v92, 0x3f803f80
v_mov_b32 v93, 0x3f803f80
Mavv 140,42,96
W_ 0
v_mov_b32 v94, 0x3f803f80
v_mov_b32 v95, 0x3f803f80
Mavv 160,30,96
s_nop 0
Tv 38,v4,26624
Tv 40,v4,32768
Mavv 180,34,96
Mavv 184,34,100
s_nop 0
Tv 42,v5,26624
Tv 44,v5,32768
Mavv 164,30,100
Mavv 168,30,104
Mavv 188,34,104
S_ s65,s61,110592
Mavv 192,34,108
S_ m0,s51,24576
Mavv 172,30,108
s_waitcnt vmcnt(6) lgkmcnt(0)
s_barrier
Mavv 176,30,112
Ln v7,32,s65,0
Mavv 196,34,112
Mavv 216,38,112
Dv 30,%0,6144
Mavv 236,42,112
Dv 34,v1,6144
Mavv 232,42,108
Ln v8,32,s65,3072
S_ m0,s51,25472
Mavv 212,38,108
Mavv 208,38,104
Ln v7,32,s65,128
Mavv 228,42,104
Mavv 224,42,100
Ln v8,32,s65,3200
S_ m0,s51,26368
Mavv 204,38,100
Mavv 200,38,96
Ln v7,32,s65,256
Mavv 220,42,96
.Lstep2_8:
Zvvv 76,92,116
Dv 38,%0,7168
Zvvv 80,92,120
Dv 42,v1,7168
Zvvv 84,92,124
Zvvv 88,92,128
Zvvv 92,92,132
W_ 2
Nvvv 96,30,136,56
Mvvv 96,34,156
Nvvv 100,30,140,60
s_nop 1
v_max3_f32 v10, v76, v80, v84
s_nop 0
v_max3_f32 v10, v10, v88, v92
Mvvv 100,34,160
v_cmp_lt_f32 vcc, 0x42000000, v10
s_cbranch_vccnz .Ltramp2_7
Nvvv 104,30,144,64
A_ v46,v46,v76
A_ v47,v47,v80
Mvvv 104,34,164
A_ v48,v48,v84
A_ v49,v49,v88
Nvvv 108,30,148,68
A_ v50,v50,v92
Mvvv 108,34,168
Ln v8,32,s65,3328
Nvvv 112,30,152,72
Mvvv 112,34,172
W_ 0
Mvvv 112,38,192
Dv 30,%0,8192
Mvvv 112,42,212
Dv 34,v1,8192
Mvvv 108,38,188
Mvvv 108,42,208
Mvvv 104,38,184
Mvvv 104,42,204
Mvvv 100,38,180
Mvvv 100,42,200
Mvvv 96,38,176
Mvvv 96,42,196
W_ 0
Mvvv 96,30,216
Dv 38,%0,0
Mvvv 96,34,236
Dv 42,v1,0
Mvvv 100,30,220
Mvvv 100,34,240
Mvvv 104,30,224
Mvvv 104,34,244
Mvvv 108,30,228
Mvvv 108,34,248
s_nop 0
E_ 96
Mvvv 112,30,232
E_ 97
Mvvv 112,34,252
W_ 0
E_ 98
Nvvv 76,38,136,56
Dv 30,%0,1024
Mvvv 76,42,156
Dv 34,v1,1024
Nvvv 80,38,140,60
E_ 99
Mvvv 80,42,160
C_ 99,98,99
C_ 98,96,97
Nvvv 84,38,144,64
E_ 100
Mvvv 84,42,164
E_ 101
Nvvv 88,38,148,68
E_ 102
Mvvv 88,42,168
E_ 103
Nvvv 92,38,152,72
C_ 103,102,103
C_ 102,100,101
Mvvv 92,42,172
W_ 0
E_ 104
Mvvv 92,30,192
Dv 38,%0,2048
Mvvv 92,34,212
Dv 42,v1,2048
Mvvv 88,30,188
E_ 105
Mvvv 88,34,208
E_ 106
Mvvv 84,30,184
S_ s65,s61,98304
E_ 107
Mvvv 84,34,204
S_ m0,s51,0
C_ 107,106,107
C_ 106,104,105
Mvvv 80,30,180
s_waitcnt vmcnt(6) lgkmcnt(0)
s_barrier
Mvvv 80,34,200
L_ v9,36,s65,0
Mvvv 76,30,176
E_ 108
Mvvv 76,34,196
L_ v9,36,s65,3072
S_ m0,s51,896
Mvvv 76,38,216
Tv 30,v2,12288
Tv 32,v2,18432
Mvvv 76,42,236
L_ v9,36,s65,128
Mvvv 80,38,220
s_nop 0
Tv 34,v3,12288
Tv 36,v3,18432
Mvvv 80,42,240
L_ v9,36,s65,3200
S_ m0,s51,1792
Mvvv 84,38,224
E_ 109
Mvvv 84,42,244
L_ v9,36,s65,256
Mvvv 88,38,228
E_ 110
Mvvv 88,42,248
L_ v9,36,s65,3328
Mvvv 92,38,232
E_ 111
Mvvv 92,42,252
W_ 0
C_ 111,110,111
C_ 110,108,109
Mavv 0,30,116
s_nop 0
Tv 38,v4,12288
Tv 40,v4,18432
Mavv 20,34,116
E_ 112
Mavv 24,34,120
s_nop 0
Tv 42,v5,12288
Tv 44,v5,18432
Mavv 4,30,120
E_ 113
Mavv 8,30,124
E_ 114
Mavv 28,34,124
E_ 115
Mavv 32,34,128
C_ 115,114,115
C_ 114,112,113
Mavv 12,30,128
E_ 76
Mavv 16,30,132
E_ 77
Mavv 36,34,132
W_ 0
E_ 78
Mavv 56,38,132
s_nop 0
Tv 30,v2,13312
Tv 32,v2,19456
Mavv 76,42,132
E_ 79
Mavv 72,42,128
s_nop 0
Tv 34,v3,13312
Tv 36,v3,19456
Mavv 52,38,128
C_ 96,76,77
C_ 97,78,79
Mavv 48,38,124
E_ 80
Mavv 68,42,124
E_ 81
Mavv 64,42,120
E_ 82
Mavv 44,38,120
E_ 83
Mavv 40,38,116
C_ 100,80,81
C_ 101,82,83
Mavv 60,42,116
W_ 0
E_ 84
Mavv 80,30,116
s_nop 0
Tv 38,v4,13312
Tv 40,v4,19456
Mavv 100,34,116
E_ 85
Mavv 104,34,120
s_nop 0
Tv 42,v5,13312
Tv 44,v5,19456
Mavv 84,30,120
E_ 86
Mavv 88,30,124
E_ 87
Mavv 108,34,124
C_ 104,84,85
C_ 105,86,87
Mavv 112,34,128
E_ 88
Mavv 92,30,128
E_ 89
Mavv 96,30,132
E_ 90
Mavv 116,34,132
W_ 0
E_ 91
Mavv 136,38,132
s_nop 0
Tv 30,v2,14336
Tv 32,v2,20480
Mavv 156,42,132
C_ 108,88,89
C_ 109,90,91
Mavv 152,42,128
s_nop 0
Tv 34,v3,14336
Tv 36,v3,20480
Mavv 132,38,128
E_ 92
Mavv 128,38,124
E_ 93
Mavv 148,42,124
E_ 94
Mavv 144,42,120
E_ 95
Mavv 124,38,120
C_ 112,92,93
C_ 113,94,95
Mavv 120,38,116
v_mov_b32 v92, 0x3f803f80
v_mov_b32 v93, 0x3f803f80
Mavv 140,42,116
W_ 0
v_mov_b32 v94, 0x3f803f80
v_mov_b32 v95, 0x3f803f80
Mavv 160,30,116
s_nop 0
Tv 38,v4,14336
Tv 40,v4,20480
Mavv 180,34,116
Mavv 184,34,120
s_nop 0
Tv 42,v5,14336
Tv 44,v5,20480
Mavv 164,30,120
Mavv 168,30,124
Mavv 188,34,124
S_ s65,s61,122880
Mavv 192,34,128
S_ m0,s51,12288
Mavv 172,30,128
s_waitcnt vmcnt(6) lgkmcnt(0)
s_barrier
Mavv 176,30,132
Ln v7,32,s65,0
Mavv 196,34,132
Mavv 216,38,132
Dv 30,%0,30720
Mavv 236,42,132
Dv 34,v1,30720
Mavv 232,42,128
Ln v8,32,s65,3072
S_ m0,s51,13184
Mavv 212,38,128
Mavv 208,38,124
Ln v7,32,s65,128
Mavv 228,42,124
Mavv 224,42,120
Ln v8,32,s65,3200
S_ m0,s51,14080
Mavv 204,38,120
Mavv 200,38,116
Ln v7,32,s65,256
Mavv 220,42,116
s_cmp_eq_u32 s4, 0
s_cbranch_scc1 .Lhs_d2
.Lhs_d2_back:
.Lstep2_9:
Zvvv 76,92,96
Dv 38,%0,31744
Zvvv 80,92,100
Dv 42,v1,31744
Zvvv 84,92,104
Zvvv 88,92,108
Zvvv 92,92,112
W_ 2
Nvvv 116,30,136,56
Mvvv 116,34,156
Nvvv 120,30,140,60
s_nop 1
v_max3_f32 v10, v76, v80, v84
s_nop 0
v_max3_f32 v10, v10, v88, v92
Mvvv 120,34,160
v_cmp_lt_f32 vcc, 0x42000000, v10
s_cbranch_vccnz .Ltramp2_8
Nvvv 124,30,144,64
A_ v46,v46,v76
A_ v47,v47,v80
Mvvv 124,34,164
A_ v48,v48,v84
A_ v49,v49,v88
Nvvv 128,30,148,68
A_ v50,v50,v92
Mvvv 128,34,168
Ln v8,32,s65,3328
Nvvv 132,30,152,72
Mvvv 132,34,172
W_ 0
Mvvv 132,38,192
Dv 30,%0,32768
Mvvv 132,42,212
Dv 34,v1,32768
Mvvv 128,38,188
Mvvv 128,42,208
Mvvv 124,38,184
Mvvv 124,42,204
Mvvv 120,38,180
Mvvv 120,42,200
Mvvv 116,38,176
Mvvv 116,42,196
W_ 0
Mvvv 116,30,216
Dv 38,%0,24576
Mvvv 116,34,236
Dv 42,v1,24576
Mvvv 120,30,220
Mvvv 120,34,240
Mvvv 124,30,224
Mvvv 124,34,244
Mvvv 128,30,228
Mvvv 128,34,248
s_nop 0
E_ 116
Mvvv 132,30,232
E_ 117
Mvvv 132,34,252
W_ 0
E_ 118
Nvvv 76,38,136,56
Dv 30,%0,25600
Mvvv 76,42,156
Dv 34,v1,25600
Nvvv 80,38,140,60
E_ 119
Mvvv 80,42,160
C_ 119,118,119
C_ 118,116,117
Nvvv 84,38,144,64
E_ 120
Mvvv 84,42,164
E_ 121
Nvvv 88,38,148,68
E_ 122
Mvvv 88,42,168
E_ 123
Nvvv 92,38,152,72
C_ 123,122,123
C_ 122,120,121
Mvvv 92,42,172
W_ 0
E_ 124
Mvvv 92,30,192
Dv 38,%0,26624
Mvvv 92,34,212
Dv 42,v1,26624
Mvvv 88,30,188
E_ 125
Mvvv 88,34,208
E_ 126
Mvvv 84,30,184
S_ s65,s61,110592
E_ 127
Mvvv 84,34,204
S_ m0,s51,24576
C_ 127,126,127
C_ 126,124,125
Mvvv 80,30,180
s_waitcnt vmcnt(6) lgkmcnt(0)
s_barrier
Mvvv 80,34,200
Ln v9,36,s65,0
Mvvv 76,30,176
E_ 128
Mvvv 76,34,196
Ln v9,36,s65,3072
S_ m0,s51,25472
Mvvv 76,38,216
Tv 30,v2,0
Tv 32,v2,6144
Mvvv 76,42,236
Ln v9,36,s65,128
Mvvv 80,38,220
s_nop 0
Tv 34,v3,0
Tv 36,v3,6144
Mvvv 80,42,240
Ln v9,36,s65,3200
S_ m0,s51,26368
Mvvv 84,38,224
E_ 129
Mvvv 84,42,244
Ln v9,36,s65,256
Mvvv 88,38,228
E_ 130
Mvvv 88,42,248
Ln v9,36,s65,3328
Mvvv 92,38,232
E_ 131
Mvvv 92,42,252
W_ 0
C_ 131,130,131
C_ 130,128,129
Mavv 0,30,96
s_nop 0
Tv 38,v4,0
Tv 40,v4,6144
Mavv 20,34,96
E_ 132
Mavv 24,34,100
s_nop 0
Tv 42,v5,0
Tv 44,v5,6144
Mavv 4,30,100
E_ 133
Mavv 8,30,104
E_ 134
Mavv 28,34,104
E_ 135
Mavv 32,34,108
C_ 135,134,135
C_ 134,132,133
Mavv 12,30,108
E_ 76
Mavv 16,30,112
E_ 77
Mavv 36,34,112
W_ 0
E_ 78
Mavv 56,38,112
s_nop 0
Tv 30,v2,1024
Tv 32,v2,7168
Mavv 76,42,112
E_ 79
Mavv 72,42,108
s_nop 0
Tv 34,v3,1024
Tv 36,v3,7168
Mavv 52,38,108
C_ 116,76,77
C_ 117,78,79
Mavv 48,38,104
E_ 80
Mavv 68,42,104
E_ 81
Mavv 64,42,100
E_ 82
Mavv 44,38,100
E_ 83
Mavv 40,38,96
C_ 120,80,81
C_ 121,82,83
Mavv 60,42,96
W_ 0
E_ 84
Mavv 80,30,96
s_nop 0
Tv 38,v4,1024
Tv 40,v4,7168
Mavv 100,34,96
E_ 85
Mavv 104,34,100
s_nop 0
Tv 42,v5,1024
Tv 44,v5,7168
Mavv 84,30,100
E_ 86
Mavv 88,30,104
E_ 87
Mavv 108,34,104
C_ 124,84,85
C_ 125,86,87
Mavv 112,34,108
E_ 88
Mavv 92,30,108
E_ 89
Mavv 96,30,112
E_ 90
Mavv 116,34,112
W_ 0
E_ 91
Mavv 136,38,112
s_nop 0
Tv 30,v2,2048
Tv 32,v2,8192
Mavv 156,42,112
C_ 128,88,89
C_ 129,90,91
Mavv 152,42,108
s_nop 0
Tv 34,v3,2048
Tv 36,v3,8192
Mavv 132,38,108
E_ 92
Mavv 128,38,104
E_ 93
Mavv 148,42,104
E_ 94
Mavv 144,42,100
E_ 95
Mavv 124,38,100
C_ 132,92,93
C_ 133,94,95
Mavv 120,38,96
v_mov_b32 v92, 0x3f803f80
v_mov_b32 v93, 0x3f803f80
Mavv 140,42,96
W_ 0
v_mov_b32 v94, 0x3f803f80
v_mov_b32 v95, 0x3f803f80
Mavv 160,30,96
s_nop 0
Tv 38,v4,2048
Tv 40,v4,8192
Mavv 180,34,96
Mavv 184,34,100
s_nop 0
Tv 42,v5,2048
Tv 44,v5,8192
Mavv 164,30,100
Mavv 168,30,104
Mavv 188,34,104
S_ s65,s61,135168
Mavv 192,34,108
S_ m0,s51,0
Mavv 172,30,108
s_waitcnt vmcnt(6) lgkmcnt(0)
s_barrier
Mavv 176,30,112
Ln v7,32,s65,0
Mavv 196,34,112
Mavv 216,38,112
Dv 30,%0,18432
Mavv 236,42,112
Dv 34,v1,18432
Mavv 232,42,108
Ln v8,32,s65,3072
S_ m0,s51,896
Mavv 212,38,108
Mavv 208,38,104
Ln v7,32,s65,128
Mavv 228,42,104
Mavv 224,42,100
Ln v8,32,s65,3200
S_ m0,s51,1792
Mavv 204,38,100
Mavv 200,38,96
Ln v7,32,s65,256
Mavv 220,42,96
.Lstep2_10:
Zvvv 76,92,116
Dv 38,%0,19456
Zvvv 80,92,120
Dv 42,v1,19456
Zvvv 84,92,124
Zvvv 88,92,128
Zvvv 92,92,132
W_ 2
Nvvv 96,30,136,56
Mvvv 96,34,156
Nvvv 100,30,140,60
s_nop 1
v_max3_f32 v10, v76, v80, v84
s_nop 0
v_max3_f32 v10, v10, v88, v92
Mvvv 100,34,160
v_cmp_lt_f32 vcc, 0x42000000, v10
s_cbranch_vccnz .Ltramp2_9
Nvvv 104,30,144,64
A_ v46,v46,v76
A_ v47,v47,v80
Mvvv 104,34,164
A_ v48,v48,v84
A_ v49,v49,v88
Nvvv 108,30,148,68
A_ v50,v50,v92
Mvvv 108,34,168
Ln v8,32,s65,3328
Nvvv 112,30,152,72
Mvvv 112,34,172
W_ 0
Mvvv 112,38,192
Dv 30,%0,20480
Mvvv 112,42,212
Dv 34,v1,20480
Mvvv 108,38,188
Mvvv 108,42,208
Mvvv 104,38,184
Mvvv 104,42,204
Mvvv 100,38,180
Mvvv 100,42,200
Mvvv 96,38,176
Mvvv 96,42,196
W_ 0
Mvvv 96,30,216
Dv 38,%0,12288
Mvvv 96,34,236
Dv 42,v1,12288
Mvvv 100,30,220
Mvvv 100,34,240
Mvvv 104,30,224
Mvvv 104,34,244
Mvvv 108,30,228
Mvvv 108,34,248
s_nop 0
E_ 96
Mvvv 112,30,232
E_ 97
Mvvv 112,34,252
W_ 0
E_ 98
Nvvv 76,38,136,56
Dv 30,%0,13312
Mvvv 76,42,156
Dv 34,v1,13312
Nvvv 80,38,140,60
E_ 99
Mvvv 80,42,160
C_ 99,98,99
C_ 98,96,97
Nvvv 84,38,144,64
E_ 100
Mvvv 84,42,164
E_ 101
Nvvv 88,38,148,68
E_ 102
Mvvv 88,42,168
E_ 103
Nvvv 92,38,152,72
C_ 103,102,103
C_ 102,100,101
Mvvv 92,42,172
W_ 0
E_ 104
Mvvv 92,30,192
Dv 38,%0,14336
Mvvv 92,34,212
Dv 42,v1,14336
Mvvv 88,30,188
E_ 105
Mvvv 88,34,208
E_ 106
Mvvv 84,30,184
S_ s65,s61,122880
E_ 107
Mvvv 84,34,204
S_ m0,s51,12288
C_ 107,106,107
C_ 106,104,105
Mvvv 80,30,180
s_waitcnt vmcnt(6) lgkmcnt(0)
s_barrier
Mvvv 80,34,200
Ln v9,36,s65,0
Mvvv 76,30,176
E_ 108
Mvvv 76,34,196
Ln v9,36,s65,3072
S_ m0,s51,13184
Mvvv 76,38,216
Tv 30,v2,24576
Tv 32,v2,30720
Mvvv 76,42,236
Ln v9,36,s65,128
Mvvv 80,38,220
s_nop 0
Tv 34,v3,24576
Tv 36,v3,30720
Mvvv 80,42,240
Ln v9,36,s65,3200
S_ m0,s51,14080
Mvvv 84,38,224
E_ 109
Mvvv 84,42,244
Ln v9,36,s65,256
Mvvv 88,38,228
E_ 110
Mvvv 88,42,248
Ln v9,36,s65,3328
Mvvv 92,38,232
E_ 111
Mvvv 92,42,252
W_ 0
C_ 111,110,111
C_ 110,108,109
Mavv 0,30,116
s_nop 0
Tv 38,v4,24576
Tv 40,v4,30720
Mavv 20,34,116
E_ 112
Mavv 24,34,120
s_nop 0
Tv 42,v5,24576
Tv 44,v5,30720
Mavv 4,30,120
E_ 113
Mavv 8,30,124
E_ 114
Mavv 28,34,124
E_ 115
Mavv 32,34,128
C_ 115,114,115
C_ 114,112,113
Mavv 12,30,128
E_ 76
Mavv 16,30,132
E_ 77
Mavv 36,34,132
W_ 0
E_ 78
Mavv 56,38,132
s_nop 0
Tv 30,v2,25600
Tv 32,v2,31744
Mavv 76,42,132
E_ 79
Mavv 72,42,128
s_nop 0
Tv 34,v3,25600
Tv 36,v3,31744
Mavv 52,38,128
C_ 96,76,77
C_ 97,78,79
Mavv 48,38,124
E_ 80
Mavv 68,42,124
E_ 81
Mavv 64,42,120
E_ 82
Mavv 44,38,120
E_ 83
Mavv 40,38,116
C_ 100,80,81
C_ 101,82,83
Mavv 60,42,116
W_ 0
E_ 84
Mavv 80,30,116
s_nop 0
Tv 38,v4,25600
Tv 40,v4,31744
Mavv 100,34,116
E_ 85
Mavv 104,34,120
s_nop 0
Tv 42,v5,25600
Tv 44,v5,31744
Mavv 84,30,120
E_ 86
Mavv 88,30,124
E_ 87
Mavv 108,34,124
C_ 104,84,85
C_ 105,86,87
Mavv 112,34,128
E_ 88
Mavv 92,30,128
E_ 89
Mavv 96,30,132
E_ 90
Mavv 116,34,132
W_ 0
E_ 91
Mavv 136,38,132
s_nop 0
Tv 30,v2,26624
Tv 32,v2,32768
Mavv 156,42,132
C_ 108,88,89
C_ 109,90,91
Mavv 152,42,128
s_nop 0
Tv 34,v3,26624
Tv 36,v3,32768
Mavv 132,38,128
E_ 92
Mavv 128,38,124
E_ 93
Mavv 148,42,124
E_ 94
Mavv 144,42,120
E_ 95
Mavv 124,38,120
C_ 112,92,93
C_ 113,94,95
Mavv 120,38,116
v_mov_b32 v92, 0x3f803f80
v_mov_b32 v93, 0x3f803f80
Mavv 140,42,116
W_ 0
v_mov_b32 v94, 0x3f803f80
v_mov_b32 v95, 0x3f803f80
Mavv 160,30,116
s_nop 0
Tv 38,v4,26624
Tv 40,v4,32768
Mavv 180,34,116
Mavv 184,34,120
s_nop 0
Tv 42,v5,26624
Tv 44,v5,32768
Mavv 164,30,120
Mavv 168,30,124
Mavv 188,34,124
S_ s65,s61,147456
Mavv 192,34,128
S_ m0,s51,24576
Mavv 172,30,128
s_waitcnt vmcnt(6) lgkmcnt(0)
s_barrier
Mavv 176,30,132
Ln v7,32,s65,0
Mavv 196,34,132
Mavv 216,38,132
Dv 30,%0,6144
Mavv 236,42,132
Dv 34,v1,6144
Mavv 232,42,128
Ln v8,32,s65,3072
S_ m0,s51,25472
Mavv 212,38,128
Mavv 208,38,124
Ln v7,32,s65,128
Mavv 228,42,124
Mavv 224,42,120
Ln v8,32,s65,3200
S_ m0,s51,26368
Mavv 204,38,120
Mavv 200,38,116
Ln v7,32,s65,256
Mavv 220,42,116
.Lstep2_11:
Zvvv 76,92,96
Dv 38,%0,7168
Zvvv 80,92,100
Dv 42,v1,7168
Zvvv 84,92,104
Zvvv 88,92,108
Zvvv 92,92,112
W_ 2
Nvvv 116,30,136,56
Mvvv 116,34,156
Nvvv 120,30,140,60
s_nop 1
v_max3_f32 v10, v76, v80, v84
s_nop 0
v_max3_f32 v10, v10, v88, v92
Mvvv 120,34,160
v_cmp_lt_f32 vcc, 0x42000000, v10
s_cbranch_vccnz .Ltramp2_10
Nvvv 124,30,144,64
A_ v46,v46,v76
A_ v47,v47,v80
Mvvv 124,34,164
A_ v48,v48,v84
A_ v49,v49,v88
Nvvv 128,30,148,68
A_ v50,v50,v92
Mvvv 128,34,168
Ln v8,32,s65,3328
Nvvv 132,30,152,72
Mvvv 132,34,172
W_ 0
Mvvv 132,38,192
Dv 30,%0,8192
Mvvv 132,42,212
Dv 34,v1,8192
Mvvv 128,38,188
Mvvv 128,42,208
Mvvv 124,38,184
Mvvv 124,42,204
Mvvv 120,38,180
Mvvv 120,42,200
Mvvv 116,38,176
Mvvv 116,42,196
W_ 0
Mvvv 116,30,216
Dv 38,%0,0
Mvvv 116,34,236
Dv 42,v1,0
Mvvv 120,30,220
Mvvv 120,34,240
Mvvv 124,30,224
Mvvv 124,34,244
Mvvv 128,30,228
Mvvv 128,34,248
s_nop 0
E_ 116
Mvvv 132,30,232
E_ 117
Mvvv 132,34,252
W_ 0
E_ 118
Nvvv 76,38,136,56
Dv 30,%0,1024
Mvvv 76,42,156
Dv 34,v1,1024
Nvvv 80,38,140,60
E_ 119
Mvvv 80,42,160
C_ 119,118,119
C_ 118,116,117
Nvvv 84,38,144,64
E_ 120
Mvvv 84,42,164
E_ 121
Nvvv 88,38,148,68
E_ 122
Mvvv 88,42,168
E_ 123
Nvvv 92,38,152,72
C_ 123,122,123
C_ 122,120,121
Mvvv 92,42,172
W_ 0
E_ 124
Mvvv 92,30,192
Dv 38,%0,2048
Mvvv 92,34,212
Dv 42,v1,2048
Mvvv 88,30,188
E_ 125
Mvvv 88,34,208
E_ 126
Mvvv 84,30,184
S_ s65,s61,135168
E_ 127
Mvvv 84,34,204
S_ m0,s51,0
C_ 127,126,127
C_ 126,124,125
Mvvv 80,30,180
s_waitcnt vmcnt(6) lgkmcnt(0)
s_barrier
Mvvv 80,34,200
Ln v9,36,s65,0
Mvvv 76,30,176
E_ 128
Mvvv 76,34,196
Ln v9,36,s65,3072
S_ m0,s51,896
Mvvv 76,38,216
Tv 30,v2,12288
Tv 32,v2,18432
Mvvv 76,42,236
Ln v9,36,s65,128
Mvvv 80,38,220
s_nop 0
Tv 34,v3,12288
Tv 36,v3,18432
Mvvv 80,42,240
Ln v9,36,s65,3200
S_ m0,s51,1792
Mvvv 84,38,224
E_ 129
Mvvv 84,42,244
Ln v9,36,s65,256
Mvvv 88,38,228
E_ 130
Mvvv 88,42,248
Ln v9,36,s65,3328
Mvvv 92,38,232
E_ 131
Mvvv 92,42,252
W_ 0
C_ 131,130,131
C_ 130,128,129
Mavv 0,30,96
s_nop 0
Tv 38,v4,12288
Tv 40,v4,18432
Mavv 20,34,96
E_ 132
Mavv 24,34,100
s_nop 0
Tv 42,v5,12288
Tv 44,v5,18432
Mavv 4,30,100
E_ 133
Mavv 8,30,104
E_ 134
Mavv 28,34,104
E_ 135
Mavv 32,34,108
C_ 135,134,135
C_ 134,132,133
Mavv 12,30,108
E_ 76
Mavv 16,30,112
E_ 77
Mavv 36,34,112
W_ 0
E_ 78
Mavv 56,38,112
s_nop 0
Tv 30,v2,13312
Tv 32,v2,19456
Mavv 76,42,112
E_ 79
Mavv 72,42,108
s_nop 0
Tv 34,v3,13312
Tv 36,v3,19456
Mavv 52,38,108
C_ 116,76,77
C_ 117,78,79
Mavv 48,38,104
E_ 80
Mavv 68,42,104
E_ 81
Mavv 64,42,100
E_ 82
Mavv 44,38,100
E_ 83
Mavv 40,38,96
C_ 120,80,81
C_ 121,82,83
Mavv 60,42,96
W_ 0
E_ 84
Mavv 80,30,96
s_nop 0
Tv 38,v4,13312
Tv 40,v4,19456
Mavv 100,34,96
E_ 85
Mavv 104,34,100
s_nop 0
Tv 42,v5,13312
Tv 44,v5,19456
Mavv 84,30,100
E_ 86
Mavv 88,30,104
E_ 87
Mavv 108,34,104
C_ 124,84,85
C_ 125,86,87
Mavv 112,34,108
E_ 88
Mavv 92,30,108
E_ 89
Mavv 96,30,112
E_ 90
Mavv 116,34,112
W_ 0
E_ 91
Mavv 136,38,112
s_nop 0
Tv 30,v2,14336
Tv 32,v2,20480
Mavv 156,42,112
C_ 128,88,89
C_ 129,90,91
Mavv 152,42,108
s_nop 0
Tv 34,v3,14336
Tv 36,v3,20480
Mavv 132,38,108
E_ 92
Mavv 128,38,104
E_ 93
Mavv 148,42,104
E_ 94
Mavv 144,42,100
E_ 95
Mavv 124,38,100
C_ 132,92,93
C_ 133,94,95
Mavv 120,38,96
v_mov_b32 v92, 0x3f803f80
v_mov_b32 v93, 0x3f803f80
Mavv 140,42,96
W_ 0
v_mov_b32 v94, 0x3f803f80
v_mov_b32 v95, 0x3f803f80
Mavv 160,30,96
s_nop 0
Tv 38,v4,14336
Tv 40,v4,20480
Mavv 180,34,96
Mavv 184,34,100
s_nop 0
Tv 42,v5,14336
Tv 44,v5,20480
Mavv 164,30,100
Mavv 168,30,104
Mavv 188,34,104
S_ s65,s61,159744
Mavv 192,34,108
S_ m0,s51,12288
Mavv 172,30,108
s_waitcnt vmcnt(6) lgkmcnt(0)
s_barrier
Mavv 176,30,112
Ln v7,32,s65,0
Mavv 196,34,112
Mavv 216,38,112
Dv 30,%0,30720
Mavv 236,42,112
Dv 34,v1,30720
Mavv 232,42,108
Ln v8,32,s65,3072
S_ m0,s51,13184
Mavv 212,38,108
Mavv 208,38,104
Ln v7,32,s65,128
Mavv 228,42,104
Mavv 224,42,100
Ln v8,32,s65,3200
S_ m0,s51,14080
Mavv 204,38,100
Mavv 200,38,96
Ln v7,32,s65,256
Mavv 220,42,96
.Lstep2_12:
Zvvv 76,92,116
Dv 38,%0,31744
Zvvv 80,92,120
Dv 42,v1,31744
Zvvv 84,92,124
Zvvv 88,92,128
Zvvv 92,92,132
W_ 2
Nvvv 96,30,136,56
Mvvv 96,34,156
Nvvv 100,30,140,60
s_nop 1
v_max3_f32 v10, v76, v80, v84
s_nop 0
v_max3_f32 v10, v10, v88, v92
Mvvv 100,34,160
v_cmp_lt_f32 vcc, 0x42000000, v10
s_cbranch_vccnz .Ltramp2_11
Nvvv 104,30,144,64
A_ v46,v46,v76
A_ v47,v47,v80
Mvvv 104,34,164
A_ v48,v48,v84
A_ v49,v49,v88
Nvvv 108,30,148,68
A_ v50,v50,v92
Mvvv 108,34,168
Ln v8,32,s65,3328
Nvvv 112,30,152,72
R_ v11,a248
Mvvv 112,34,172
W_ 0
v_lshlrev_b32 v11, 10, v11
Mvvv 112,38,192
Dv 30,%0,32768
Mvvv 112,42,212
Dv 34,v1,32768
Mvvv 108,38,188
buffer_load_dword a254, v11, s[44:47], s66 offen
Mvvv 108,42,208
Mvvv 104,38,184
Mvvv 104,42,204
Mvvv 100,38,180
Mvvv 100,42,200
Mvvv 96,38,176
Mvvv 96,42,196
W_ 0
Mvvv 96,30,216
Dv 38,%0,24576
Mvvv 96,34,236
Dv 42,v1,24576
Mvvv 100,30,220
Mvvv 100,34,240
Mvvv 104,30,224
Mvvv 104,34,244
Mvvv 108,30,228
Mvvv 108,34,248
s_nop 0
E_ 96
Mvvv 112,30,232
E_ 97
Mvvv 112,34,252
W_ 0
E_ 98
Nvvv 76,38,136,56
Dv 30,%0,25600
Mvvv 76,42,156
Dv 34,v1,25600
Nvvv 80,38,140,60
E_ 99
Mvvv 80,42,160
C_ 99,98,99
C_ 98,96,97
Nvvv 84,38,144,64
E_ 100
Mvvv 84,42,164
E_ 101
Nvvv 88,38,148,68
E_ 102
Mvvv 88,42,168
E_ 103
Nvvv 92,38,152,72
C_ 103,102,103
C_ 102,100,101
Mvvv 92,42,172
W_ 0
E_ 104
Mvvv 92,30,192
Dv 38,%0,26624
Mvvv 92,34,212
Dv 42,v1,26624
Mvvv 88,30,188
E_ 105
Mvvv 88,34,208
E_ 106
Mvvv 84,30,184
S_ s65,s61,147456
E_ 107
Mvvv 84,34,204
S_ m0,s51,24576
C_ 107,106,107
C_ 106,104,105
Mvvv 80,30,180
s_waitcnt vmcnt(7) lgkmcnt(0)
s_barrier
Mvvv 80,34,200
Ln v9,36,s65,0
Mvvv 76,30,176
E_ 108
Mvvv 76,34,196
Ln v9,36,s65,3072
S_ m0,s51,25472
Mvvv 76,38,216
Tv 30,v2,0
Tv 32,v2,6144
Mvvv 76,42,236
Ln v9,36,s65,128
Mvvv 80,38,220
s_nop 0
Tv 34,v3,0
Tv 36,v3,6144
Mvvv 80,42,240
Ln v9,36,s65,3200
S_ m0,s51,26368
Mvvv 84,38,224
E_ 109
Mvvv 84,42,244
Ln v9,36,s65,256
Mvvv 88,38,228
E_ 110
Mvvv 88,42,248
Ln v9,36,s65,3328
Mvvv 92,38,232
E_ 111
Mvvv 92,42,252
W_ 0
C_ 111,110,111
C_ 110,108,109
Mavv 0,30,116
s_nop 0
Tv 38,v4,0
Tv 40,v4,6144
Mavv 20,34,116
E_ 112
Mavv 24,34,120
s_nop 0
Tv 42,v5,0
Tv 44,v5,6144
Mavv 4,30,120
E_ 113
Mavv 8,30,124
E_ 114
Mavv 28,34,124
E_ 115
Mavv 32,34,128
C_ 115,114,115
C_ 114,112,113
Mavv 12,30,128
E_ 76
Mavv 16,30,132
E_ 77
Mavv 36,34,132
W_ 0
E_ 78
Mavv 56,38,132
s_nop 0
Tv 30,v2,1024
Tv 32,v2,7168
Mavv 76,42,132
E_ 79
Mavv 72,42,128
)A"
R"A(s_nop 0
Tv 34,v3,1024
Tv 36,v3,7168
Mavv 52,38,128
C_ 96,76,77
C_ 97,78,79
Mavv 48,38,124
E_ 80
Mavv 68,42,124
E_ 81
Mavv 64,42,120
E_ 82
Mavv 44,38,120
E_ 83
Mavv 40,38,116
C_ 100,80,81
C_ 101,82,83
Mavv 60,42,116
W_ 0
E_ 84
Mavv 80,30,116
s_nop 0
Tv 38,v4,1024
Tv 40,v4,7168
Mavv 100,34,116
E_ 85
Mavv 104,34,120
s_nop 0
Tv 42,v5,1024
Tv 44,v5,7168
Mavv 84,30,120
E_ 86
Mavv 88,30,124
E_ 87
Mavv 108,34,124
C_ 104,84,85
C_ 105,86,87
Mavv 112,34,128
E_ 88
Mavv 92,30,128
E_ 89
Mavv 96,30,132
E_ 90
Mavv 116,34,132
W_ 0
E_ 91
Mavv 136,38,132
s_nop 0
Tv 30,v2,2048
Tv 32,v2,8192
Mavv 156,42,132
C_ 108,88,89
C_ 109,90,91
Mavv 152,42,128
s_nop 0
Tv 34,v3,2048
Tv 36,v3,8192
Mavv 132,38,128
E_ 92
Mavv 128,38,124
E_ 93
Mavv 148,42,124
E_ 94
Mavv 144,42,120
E_ 95
Mavv 124,38,120
C_ 112,92,93
C_ 113,94,95
Mavv 120,38,116
v_mov_b32 v92, 0x3f803f80
v_mov_b32 v93, 0x3f803f80
Mavv 140,42,116
W_ 0
v_mov_b32 v94, 0x3f803f80
v_mov_b32 v95, 0x3f803f80
Mavv 160,30,116
s_nop 0
Tv 38,v4,2048
Tv 40,v4,8192
Mavv 180,34,116
Mavv 184,34,120
s_nop 0
Tv 42,v5,2048
Tv 44,v5,8192
Mavv 164,30,120
Mavv 168,30,124
Mavv 188,34,124
S_ s65,s61,172032
Mavv 192,34,128
S_ m0,s51,0
Mavv 172,30,128
s_waitcnt vmcnt(7) lgkmcnt(0)
s_barrier
Mavv 176,30,132
Ln v7,32,s65,0
Mavv 196,34,132
Mavv 216,38,132
Dv 30,%0,18432
Mavv 236,42,132
Dv 34,v1,18432
Mavv 232,42,128
Ln v8,32,s65,3072
S_ m0,s51,896
Mavv 212,38,128
Mavv 208,38,124
Ln v7,32,s65,128
Mavv 228,42,124
Mavv 224,42,120
Ln v8,32,s65,3200
S_ m0,s51,1792
Mavv 204,38,120
Mavv 200,38,116
Ln v7,32,s65,256
Mavv 220,42,116
.Lstep2_13:
Zvvv 76,92,96
Dv 38,%0,19456
Zvvv 80,92,100
Dv 42,v1,19456
Zvvv 84,92,104
Zvvv 88,92,108
Zvvv 92,92,112
W_ 2
Nvvv 116,30,136,56
Mvvv 116,34,156
Nvvv 120,30,140,60
s_nop 1
v_max3_f32 v10, v76, v80, v84
s_nop 0
v_max3_f32 v10, v10, v88, v92
Mvvv 120,34,160
v_cmp_lt_f32 vcc, 0x42000000, v10
s_cbranch_vccnz .Ltramp2_12
Nvvv 124,30,144,64
A_ v46,v46,v76
A_ v47,v47,v80
Mvvv 124,34,164
A_ v48,v48,v84
A_ v49,v49,v88
Nvvv 128,30,148,68
A_ v50,v50,v92
Mvvv 128,34,168
Ln v8,32,s65,3328
Nvvv 132,30,152,72
Mvvv 132,34,172
W_ 0
Mvvv 132,38,192
Dv 30,%0,20480
Mvvv 132,42,212
Dv 34,v1,20480
Mvvv 128,38,188
Mvvv 128,42,208
Mvvv 124,38,184
Mvvv 124,42,204
Mvvv 120,38,180
Mvvv 120,42,200
Mvvv 116,38,176
Mvvv 116,42,196
W_ 0
Mvvv 116,30,216
Dv 38,%0,12288
Mvvv 116,34,236
Dv 42,v1,12288
Mvvv 120,30,220
Mvvv 120,34,240
Mvvv 124,30,224
Mvvv 124,34,244
Mvvv 128,30,228
Mvvv 128,34,248
s_nop 0
E_ 116
Mvvv 132,30,232
E_ 117
Mvvv 132,34,252
W_ 0
E_ 118
Nvvv 76,38,136,56
Dv 30,%0,13312
Mvvv 76,42,156
Dv 34,v1,13312
Nvvv 80,38,140,60
E_ 119
Mvvv 80,42,160
C_ 119,118,119
C_ 118,116,117
Nvvv 84,38,144,64
E_ 120
Mvvv 84,42,164
E_ 121
Nvvv 88,38,148,68
E_ 122
Mvvv 88,42,168
E_ 123
Nvvv 92,38,152,72
C_ 123,122,123
C_ 122,120,121
Mvvv 92,42,172
W_ 0
E_ 124
Mvvv 92,30,192
Dv 38,%0,14336
Mvvv 92,34,212
Dv 42,v1,14336
Mvvv 88,30,188
E_ 125
Mvvv 88,34,208
E_ 126
Mvvv 84,30,184
S_ s65,s61,159744
E_ 127
Mvvv 84,34,204
S_ m0,s51,12288
C_ 127,126,127
C_ 126,124,125
Mvvv 80,30,180
s_waitcnt vmcnt(6) lgkmcnt(0)
s_barrier
Mvvv 80,34,200
Ln v9,36,s65,0
Mvvv 76,30,176
E_ 128
Mvvv 76,34,196
Ln v9,36,s65,3072
S_ m0,s51,13184
Mvvv 76,38,216
Tv 30,v2,24576
Tv 32,v2,30720
Mvvv 76,42,236
Ln v9,36,s65,128
Mvvv 80,38,220
s_nop 0
Tv 34,v3,24576
Tv 36,v3,30720
Mvvv 80,42,240
Ln v9,36,s65,3200
S_ m0,s51,14080
Mvvv 84,38,224
E_ 129
Mvvv 84,42,244
Ln v9,36,s65,256
Mvvv 88,38,228
E_ 130
Mvvv 88,42,248
Ln v9,36,s65,3328
Mvvv 92,38,232
E_ 131
Mvvv 92,42,252
W_ 0
C_ 131,130,131
C_ 130,128,129
Mavv 0,30,96
s_nop 0
Tv 38,v4,24576
Tv 40,v4,30720
Mavv 20,34,96
E_ 132
Mavv 24,34,100
s_nop 0
Tv 42,v5,24576
Tv 44,v5,30720
Mavv 4,30,100
E_ 133
Mavv 8,30,104
E_ 134
Mavv 28,34,104
E_ 135
Mavv 32,34,108
C_ 135,134,135
C_ 134,132,133
Mavv 12,30,108
E_ 76
Mavv 16,30,112
E_ 77
Mavv 36,34,112
W_ 0
E_ 78
Mavv 56,38,112
s_nop 0
Tv 30,v2,25600
Tv 32,v2,31744
Mavv 76,42,112
E_ 79
Mavv 72,42,108
s_nop 0
Tv 34,v3,25600
Tv 36,v3,31744
Mavv 52,38,108
C_ 116,76,77
C_ 117,78,79
Mavv 48,38,104
E_ 80
Mavv 68,42,104
E_ 81
Mavv 64,42,100
E_ 82
Mavv 44,38,100
E_ 83
Mavv 40,38,96
C_ 120,80,81
C_ 121,82,83
Mavv 60,42,96
W_ 0
E_ 84
Mavv 80,30,96
s_nop 0
Tv 38,v4,25600
Tv 40,v4,31744
Mavv 100,34,96
E_ 85
Mavv 104,34,100
s_nop 0
Tv 42,v5,25600
Tv 44,v5,31744
Mavv 84,30,100
E_ 86
Mavv 88,30,104
E_ 87
Mavv 108,34,104
C_ 124,84,85
C_ 125,86,87
Mavv 112,34,108
E_ 88
Mavv 92,30,108
E_ 89
Mavv 96,30,112
E_ 90
Mavv 116,34,112
W_ 0
E_ 91
Mavv 136,38,112
s_nop 0
Tv 30,v2,26624
Tv 32,v2,32768
Mavv 156,42,112
C_ 128,88,89
C_ 129,90,91
Mavv 152,42,108
s_nop 0
Tv 34,v3,26624
Tv 36,v3,32768
Mavv 132,38,108
E_ 92
Mavv 128,38,104
E_ 93
Mavv 148,42,104
E_ 94
Mavv 144,42,100
E_ 95
Mavv 124,38,100
C_ 132,92,93
C_ 133,94,95
Mavv 120,38,96
v_mov_b32 v92, 0x3f803f80
v_mov_b32 v93, 0x3f803f80
Mavv 140,42,96
W_ 0
v_mov_b32 v94, 0x3f803f80
v_mov_b32 v95, 0x3f803f80
Mavv 160,30,96
s_nop 0
Tv 38,v4,26624
Tv 40,v4,32768
Mavv 180,34,96
Mavv 184,34,100
s_nop 0
Tv 42,v5,26624
Tv 44,v5,32768
Mavv 164,30,100
Mavv 168,30,104
Mavv 188,34,104
S_ s65,s61,184320
Mavv 192,34,108
S_ m0,s51,24576
Mavv 172,30,108
s_waitcnt vmcnt(6) lgkmcnt(0)
s_barrier
Mavv 176,30,112
Ln v7,32,s65,0
Mavv 196,34,112
Mavv 216,38,112
Dv 30,%0,6144
Mavv 236,42,112
Dv 34,v1,6144
Mavv 232,42,108
Ln v8,32,s65,3072
S_ m0,s51,25472
Mavv 212,38,108
Mavv 208,38,104
Ln v7,32,s65,128
Mavv 228,42,104
Mavv 224,42,100
Ln v8,32,s65,3200
S_ m0,s51,26368
Mavv 204,38,100
Mavv 200,38,96
Ln v7,32,s65,256
Mavv 220,42,96
.Lstep2_14:
Zvvv 76,92,116
Dv 38,%0,7168
Zvvv 80,92,120
Dv 42,v1,7168
Zvvv 84,92,124
Zvvv 88,92,128
Zvvv 92,92,132
W_ 2
Nvvv 96,30,136,56
Mvvv 96,34,156
Nvvv 100,30,140,60
s_nop 1
v_max3_f32 v10, v76, v80, v84
s_nop 0
v_max3_f32 v10, v10, v88, v92
Mvvv 100,34,160
v_cmp_lt_f32 vcc, 0x42000000, v10
s_cbranch_vccnz .Ltramp2_13
Nvvv 104,30,144,64
A_ v46,v46,v76
A_ v47,v47,v80
Mvvv 104,34,164
A_ v48,v48,v84
A_ v49,v49,v88
Nvvv 108,30,148,68
A_ v50,v50,v92
Mvvv 108,34,168
Ln v8,32,s65,3328
Nvvv 112,30,152,72
Mvvv 112,34,172
W_ 0
Mvvv 112,38,192
Dv 30,%0,8192
Mvvv 112,42,212
Dv 34,v1,8192
Mvvv 108,38,188
Mvvv 108,42,208
Mvvv 104,38,184
Mvvv 104,42,204
Mvvv 100,38,180
Mvvv 100,42,200
Mvvv 96,38,176
Mvvv 96,42,196
W_ 0
Mvvv 96,30,216
Dv 38,%0,0
Mvvv 96,34,236
Dv 42,v1,0
Mvvv 100,30,220
Mvvv 100,34,240
Mvvv 104,30,224
Mvvv 104,34,244
Mvvv 108,30,228
Mvvv 108,34,248
s_nop 0
E_ 96
Mvvv 112,30,232
E_ 97
Mvvv 112,34,252
W_ 0
E_ 98
Nvvv 76,38,136,56
Dv 30,%0,1024
Mvvv 76,42,156
Dv 34,v1,1024
Nvvv 80,38,140,60
E_ 99
Mvvv 80,42,160
C_ 99,98,99
C_ 98,96,97
Nvvv 84,38,144,64
E_ 100
Mvvv 84,42,164
E_ 101
Nvvv 88,38,148,68
E_ 102
Mvvv 88,42,168
E_ 103
Nvvv 92,38,152,72
C_ 103,102,103
C_ 102,100,101
Mvvv 92,42,172
W_ 0
E_ 104
Mvvv 92,30,192
Dv 38,%0,2048
Mvvv 92,34,212
Dv 42,v1,2048
Mvvv 88,30,188
E_ 105
Mvvv 88,34,208
E_ 106
Mvvv 84,30,184
S_ s65,s61,172032
E_ 107
Mvvv 84,34,204
S_ m0,s51,0
C_ 107,106,107
C_ 106,104,105
Mvvv 80,30,180
s_waitcnt vmcnt(6) lgkmcnt(0)
s_barrier
Mvvv 80,34,200
Ln v9,36,s65,0
Mvvv 76,30,176
E_ 108
Mvvv 76,34,196
Ln v9,36,s65,3072
S_ m0,s51,896
Mvvv 76,38,216
Tv 30,v2,12288
Tv 32,v2,18432
Mvvv 76,42,236
Ln v9,36,s65,128
Mvvv 80,38,220
s_nop 0
Tv 34,v3,12288
Tv 36,v3,18432
Mvvv 80,42,240
Ln v9,36,s65,3200
S_ m0,s51,1792
Mvvv 84,38,224
E_ 109
Mvvv 84,42,244
Ln v9,36,s65,256
Mvvv 88,38,228
E_ 110
Mvvv 88,42,248
Ln v9,36,s65,3328
Mvvv 92,38,232
E_ 111
Mvvv 92,42,252
W_ 0
C_ 111,110,111
C_ 110,108,109
Mavv 0,30,116
s_nop 0
Tv 38,v4,12288
Tv 40,v4,18432
Mavv 20,34,116
E_ 112
Mavv 24,34,120
s_nop 0
Tv 42,v5,12288
Tv 44,v5,18432
Mavv 4,30,120
E_ 113
Mavv 8,30,124
E_ 114
Mavv 28,34,124
E_ 115
Mavv 32,34,128
C_ 115,114,115
C_ 114,112,113
Mavv 12,30,128
E_ 76
Mavv 16,30,132
E_ 77
Mavv 36,34,132
W_ 0
E_ 78
Mavv 56,38,132
s_nop 0
Tv 30,v2,13312
Tv 32,v2,19456
Mavv 76,42,132
E_ 79
Mavv 72,42,128
s_nop 0
Tv 34,v3,13312
Tv 36,v3,19456
Mavv 52,38,128
C_ 96,76,77
C_ 97,78,79
Mavv 48,38,124
E_ 80
Mavv 68,42,124
E_ 81
Mavv 64,42,120
E_ 82
Mavv 44,38,120
E_ 83
Mavv 40,38,116
C_ 100,80,81
C_ 101,82,83
Mavv 60,42,116
W_ 0
E_ 84
Mavv 80,30,116
s_nop 0
Tv 38,v4,13312
Tv 40,v4,19456
Mavv 100,34,116
E_ 85
Mavv 104,34,120
s_nop 0
Tv 42,v5,13312
Tv 44,v5,19456
Mavv 84,30,120
E_ 86
Mavv 88,30,124
E_ 87
Mavv 108,34,124
C_ 104,84,85
C_ 105,86,87
Mavv 112,34,128
E_ 88
Mavv 92,30,128
E_ 89
Mavv 96,30,132
E_ 90
Mavv 116,34,132
W_ 0
E_ 91
Mavv 136,38,132
s_nop 0
Tv 30,v2,14336
Tv 32,v2,20480
Mavv 156,42,132
C_ 108,88,89
C_ 109,90,91
Mavv 152,42,128
s_nop 0
Tv 34,v3,14336
Tv 36,v3,20480
Mavv 132,38,128
E_ 92
Mavv 128,38,124
E_ 93
Mavv 148,42,124
E_ 94
Mavv 144,42,120
E_ 95
Mavv 124,38,120
C_ 112,92,93
C_ 113,94,95
Mavv 120,38,116
v_mov_b32 v92, 0x3f803f80
v_mov_b32 v93, 0x3f803f80
Mavv 140,42,116
W_ 0
v_mov_b32 v94, 0x3f803f80
v_mov_b32 v95, 0x3f803f80
Mavv 160,30,116
s_nop 0
Tv 38,v4,14336
Tv 40,v4,20480
Mavv 180,34,116
Mavv 184,34,120
s_nop 0
Tv 42,v5,14336
Tv 44,v5,20480
Mavv 164,30,120
Mavv 168,30,124
Mavv 188,34,124
S_ s65,s61,184320
Mavv 192,34,128
S_ m0,s51,12288
Mavv 172,30,128
s_waitcnt vmcnt(6) lgkmcnt(0)
s_barrier
Mavv 176,30,132
Ln v9,36,s65,0
Mavv 196,34,132
Mavv 216,38,132
Dv 30,%0,30720
Mavv 236,42,132
Dv 34,v1,30720
Mavv 232,42,128
Ln v9,36,s65,3072
S_ m0,s51,13184
Mavv 212,38,128
Mavv 208,38,124
Ln v9,36,s65,128
Mavv 228,42,124
Mavv 224,42,120
Ln v9,36,s65,3200
S_ m0,s51,14080
Mavv 204,38,120
Mavv 200,38,116
Ln v9,36,s65,256
Mavv 220,42,116
.Lstep2_15:
Zvvv 76,92,96
Dv 38,%0,31744
Zvvv 80,92,100
Dv 42,v1,31744
Zvvv 84,92,104
Zvvv 88,92,108
Zvvv 92,92,112
W_ 2
Nvvv 116,30,136,56
Mvvv 116,34,156
Nvvv 120,30,140,60
s_nop 1
v_max3_f32 v10, v76, v80, v84
s_nop 0
v_max3_f32 v10, v10, v88, v92
Mvvv 120,34,160
v_cmp_lt_f32 vcc, 0x42000000, v10
s_cbranch_vccnz .Ltramp2_14
Nvvv 124,30,144,64
A_ v46,v46,v76
A_ v47,v47,v80
Mvvv 124,34,164
A_ v48,v48,v84
A_ v49,v49,v88
Nvvv 128,30,148,68
A_ v50,v50,v92
Mvvv 128,34,168
Ln v9,36,s65,3328
Nvvv 132,30,152,72
Mvvv 132,34,172
W_ 0
Mvvv 132,38,192
Dv 30,%0,32768
Mvvv 132,42,212
Dv 34,v1,32768
Mvvv 128,38,188
Mvvv 128,42,208
Mvvv 124,38,184
Mvvv 124,42,204
Mvvv 120,38,180
Mvvv 120,42,200
Mvvv 116,38,176
Mvvv 116,42,196
W_ 0
Mvvv 116,30,216
Dv 38,%0,24576
Mvvv 116,34,236
Dv 42,v1,24576
Mvvv 120,30,220
Mvvv 120,34,240
Mvvv 124,30,224
Mvvv 124,34,244
Mvvv 128,30,228
Mvvv 128,34,248
s_nop 0
E_ 116
Mvvv 132,30,232
E_ 117
Mvvv 132,34,252
W_ 0
E_ 118
Nvvv 76,38,136,56
Dv 30,%0,25600
Mvvv 76,42,156
Dv 34,v1,25600
Nvvv 80,38,140,60
E_ 119
Mvvv 80,42,160
C_ 119,118,119
C_ 118,116,117
Nvvv 84,38,144,64
E_ 120
Mvvv 84,42,164
E_ 121
Nvvv 88,38,148,68
E_ 122
Mvvv 88,42,168
E_ 123
Nvvv 92,38,152,72
C_ 123,122,123
C_ 122,120,121
Mvvv 92,42,172
W_ 0
E_ 124
Mvvv 92,30,192
Dv 38,%0,26624
Mvvv 92,34,212
Dv 42,v1,26624
Mvvv 88,30,188
E_ 125
Mvvv 88,34,208
E_ 126
Mvvv 84,30,184
S_ s65,s61,393216
E_ 127
Mvvv 84,34,204
S_ m0,s51,24576
C_ 127,126,127
C_ 126,124,125
Mvvv 80,30,180
s_waitcnt vmcnt(6) lgkmcnt(0)
s_barrier
Mvvv 80,34,200
L_ v9,36,s65,0
Mvvv 76,30,176
E_ 128
Mvvv 76,34,196
L_ v9,36,s65,3072
S_ m0,s51,25472
Mvvv 76,38,216
Tv 30,v2,0
Tv 32,v2,6144
Mvvv 76,42,236
L_ v9,36,s65,128
Mvvv 80,38,220
s_nop 0
Tv 34,v3,0
Tv 36,v3,6144
Mvvv 80,42,240
L_ v9,36,s65,3200
S_ m0,s51,26368
Mvvv 84,38,224
E_ 129
Mvvv 84,42,244
L_ v9,36,s65,256
Mvvv 88,38,228
E_ 130
Mvvv 88,42,248
L_ v9,36,s65,3328
Mvvv 92,38,232
E_ 131
Mvvv 92,42,252
W_ 0
C_ 131,130,131
C_ 130,128,129
Mavv 0,30,96
s_nop 0
Tv 38,v4,0
Tv 40,v4,6144
Mavv 20,34,96
E_ 132
Mavv 24,34,100
s_nop 0
Tv 42,v5,0
Tv 44,v5,6144
Mavv 4,30,100
E_ 133
Mavv 8,30,104
E_ 134
Mavv 28,34,104
E_ 135
Mavv 32,34,108
C_ 135,134,135
C_ 134,132,133
Mavv 12,30,108
E_ 76
Mavv 16,30,112
E_ 77
Mavv 36,34,112
W_ 0
E_ 78
Mavv 56,38,112
s_nop 0
Tv 30,v2,1024
Tv 32,v2,7168
Mavv 76,42,112
E_ 79
Mavv 72,42,108
s_nop 0
Tv 34,v3,1024
Tv 36,v3,7168
Mavv 52,38,108
C_ 116,76,77
C_ 117,78,79
Mavv 48,38,104
E_ 80
Mavv 68,42,104
E_ 81
Mavv 64,42,100
E_ 82
Mavv 44,38,100
E_ 83
Mavv 40,38,96
C_ 120,80,81
C_ 121,82,83
Mavv 60,42,96
W_ 0
E_ 84
Mavv 80,30,96
s_nop 0
Tv 38,v4,1024
Tv 40,v4,7168
Mavv 100,34,96
E_ 85
Mavv 104,34,100
s_nop 0
Tv 42,v5,1024
Tv 44,v5,7168
Mavv 84,30,100
E_ 86
Mavv 88,30,104
E_ 87
Mavv 108,34,104
C_ 124,84,85
C_ 125,86,87
Mavv 112,34,108
E_ 88
Mavv 92,30,108
E_ 89
Mavv 96,30,112
E_ 90
Mavv 116,34,112
W_ 0
E_ 91
Mavv 136,38,112
s_nop 0
Tv 30,v2,2048
Tv 32,v2,8192
Mavv 156,42,112
C_ 128,88,89
C_ 129,90,91
Mavv 152,42,108
s_nop 0
Tv 34,v3,2048
Tv 36,v3,8192
Mavv 132,38,108
E_ 92
Mavv 128,38,104
E_ 93
Mavv 148,42,104
E_ 94
Mavv 144,42,100
E_ 95
Mavv 124,38,100
C_ 132,92,93
C_ 133,94,95
Mavv 120,38,96
v_mov_b32 v92, 0x3f803f80
v_mov_b32 v93, 0x3f803f80
Mavv 140,42,96
W_ 0
v_mov_b32 v94, 0x3f803f80
v_mov_b32 v95, 0x3f803f80
Mavv 160,30,96
s_nop 0
Tv 38,v4,2048
Tv 40,v4,8192
Mavv 180,34,96
Mavv 184,34,100
s_nop 0
Tv 42,v5,2048
Tv 44,v5,8192
Mavv 164,30,100
Mavv 168,30,104
Mavv 188,34,104
S_ s65,s61,393216
Mavv 192,34,108
S_ m0,s51,0
Mavv 172,30,108
s_waitcnt vmcnt(6) lgkmcnt(0)
s_barrier
Mavv 176,30,112
Ln v7,32,s65,0
Mavv 196,34,112
Mavv 216,38,112
s_nop 0
Tv 30,v2,12288
Tv 32,v2,18432
Zvvv 76,92,116
Ln v8,32,s65,3072
S_ m0,s51,896
Zvvv 80,92,120
s_nop 0
Tv 34,v3,12288
Tv 36,v3,18432
Zvvv 84,92,124
Ln v7,32,s65,128
Zvvv 88,92,128
Zvvv 92,92,132
Ln v8,32,s65,3200
S_ m0,s51,1792
Mavv 236,42,112
Mavv 232,42,108
Ln v7,32,s65,256
Mavv 212,38,108
Mavv 208,38,104
v_max3_f32 v10, v76, v80, v84
Mavv 228,42,104
v_max3_f32 v10, v10, v88, v92
v_cmp_lt_f32 vcc, 0x42000000, v10
Mavv 224,42,100
Ln v8,32,s65,3328
Mavv 204,38,100
Mavv 200,38,96
Mavv 220,42,96
s_cbranch_vccnz .Ltramp2_15
.Ljoin2:
W_ 2
Mavv 0,30,116
s_mov_b32 s58, 1
Tv 38,v4,12288
Tv 40,v4,18432
W_ 2
Mavv 20,34,116
R_ v11,a248
R_ v13,a248
Mavv 24,34,120
S_ s53,s51,36864
Tv 42,v5,12288
Tv 44,v5,18432
Mavv 4,30,120
v_add_u32 v12, s53, v13
s_mul_i32 s53, s4, 10240
Mavv 8,30,124
v_add_u32 v13, s53, v13
A_ v46,v46,v76
Mavv 28,34,124
A_ v47,v47,v80
Mavv 32,34,128
A_ v48,v48,v84
A_ v49,v49,v88
Mavv 12,30,128
A_ v50,v50,v92
Mavv 16,30,132
v_rcp_f32 v46, v46
Mavv 36,34,132
W_ 0
v_rcp_f32 v47, v47
Mavv 56,38,132
s_nop 0
Tv 30,v2,13312
Tv 32,v2,19456
Mavv 76,42,132
v_rcp_f32 v48, v48
Mavv 72,42,128
s_nop 0
Tv 34,v3,13312
Tv 36,v3,19456
Mavv 52,38,128
v_rcp_f32 v49, v49
Mavv 48,38,124
v_rcp_f32 v50, v50
Mavv 68,42,124
s_mov_b32 s56, 12288
v_mov_b32 v176, v20
v_mov_b32 v177, v21
Mavv 64,42,120
Dv 136,v13,0
v_mov_b32 v178, v22
Mavv 44,38,120
Dv 140,v13,1024
v_mov_b32 v179, v23
Mavv 40,38,116
Dv 144,v13,2048
v_mov_b32 v180, v24
Mavv 60,42,116
W_ 3
Dv 148,v13,3072
v_mov_b32 v181, v25
Mavv 80,30,116
s_nop 0
Tv 38,v4,13312
Tv 40,v4,19456
Mavv 100,34,116
Dv 152,v13,4096
v_mov_b32 v182, v26
Mavv 104,34,120
s_nop 0
Tv 42,v5,13312
Tv 44,v5,19456
Mavv 84,30,120
Dv 156,v13,5120
v_mov_b32 v183, v27
Mavv 88,30,124
Dv 160,v13,6144
v_mov_b32 v184, v52
Mavv 108,34,124
Dv 164,v13,7168
v_mov_b32 v185, v53
Mavv 112,34,128
Dv 168,v13,8192
v_mov_b32 v186, v54
Mavv 92,30,128
Dv 172,v13,9216
v_mov_b32 v187, v55
Mavv 96,30,132
Dv 208,v12,0
R_ v188,a240
Mavv 116,34,132
W_ 6
Dv 212,v12,1024
R_ v189,a241
Mavv 136,38,132
s_nop 0
Tv 30,v2,14336
Tv 32,v2,20480
Mavv 156,42,132
Dv 216,v12,2048
R_ v190,a242
Mavv 152,42,128
s_nop 0
Tv 34,v3,14336
Tv 36,v3,20480
Mavv 132,38,128
Dv 220,v12,3072
R_ v191,a243
Mavv 128,38,124
Dv 224,v12,4096
R_ v192,a244
Mavv 148,42,124
Dv 228,v12,5120
R_ v193,a245
Mavv 144,42,120
Dv 232,v12,12288
R_ v194,a246
Mavv 124,38,120
Dv 236,v12,13312
R_ v195,a247
Mavv 120,38,116
Dv 240,v12,14336
v_mov_b32 v196, v16
Mavv 140,42,116
W_ 6
Dv 244,v12,15360
v_mov_b32 v197, v17
Mavv 160,30,116
s_nop 0
Tv 38,v4,14336
Tv 40,v4,20480
Mavv 180,34,116
Dv 248,v12,16384
v_mov_b32 v198, v18
Mavv 184,34,120
s_nop 0
Tv 42,v5,14336
Tv 44,v5,20480
Mavv 164,30,120
Dv 252,v12,17408
v_mov_b32 v199, v19
Mavv 168,30,124
v_mov_b32 v200, v28
v_mov_b32 v201, v29
Mavv 188,34,124
v_mov_b32 v202, v51
v_mov_b32 v203, v6
Mavv 192,34,128
v_mov_b32 v204, v14
v_mov_b32 v205, v15
Mavv 172,30,128
S_ s65,s61,405504
R_ v206,a249
R_ v207,a250
Mavv 176,30,132
S_ m0,s51,12288
R_ v96,a0
R_ v97,a1
Mavv 196,34,132
s_waitcnt vmcnt(63) lgkmcnt(0)
s_barrier
R_ v98,a2
Mavv 216,38,132
Ln v7,32,s65,0
Mavv 236,42,132
R_ v99,a3
Mavv 232,42,128
Ln v8,32,s65,3072
S_ m0,s51,13184
Mavv 212,38,128
R_ v100,a20
R_ v101,a21
Mavv 208,38,124
Ln v7,32,s65,128
Mavv 228,42,124
R_ v102,a22
R_ v103,a23
Mavv 224,42,120
Ln v8,32,s65,3200
S_ m0,s51,14080
Mavv 204,38,120
U_ v96,v96,v46
U_ v97,v97,v46
Mavv 200,38,116
Ln v7,32,s65,256
Mavv 220,42,116
U_ v98,v98,v46
U_ v99,v99,v46
U_ v100,v100,v46
U_ v101,v101,v46
U_ v102,v102,v46
U_ v103,v103,v46
C_ 76,96,97
C_ 77,98,99
C_ 78,100,101
C_ 79,102,103
ds_write_b128 v13, v[76:79] offset:0
Ln v8,32,s65,3328
S_ s61,s61,393216
.p2align 3
R_ v104,a4
R_ v105,a5
R_ v106,a6
R_ v107,a7
R_ v108,a24
R_ v109,a25
R_ v110,a26
R_ v111,a27
v_pk_mul_f32 v[104:105], v[104:105], v[46:47] op_sel:[0,1]
v_pk_mul_f32 v[106:107], v[106:107], v[46:47] op_sel:[0,1]
v_pk_mul_f32 v[108:109], v[108:109], v[46:47] op_sel:[0,1]
v_pk_mul_f32 v[110:111], v[110:111], v[46:47] op_sel:[0,1]
C_ 80,104,105
C_ 81,106,107
C_ 82,108,109
C_ 83,110,111
ds_write_b128 v13, v[80:83] offset:1024
.p2align 3
R_ v96,a8
R_ v97,a9
R_ v98,a10
R_ v99,a11
R_ v100,a28
R_ v101,a29
R_ v102,a30
R_ v103,a31
v_pk_mul_f32 v[96:97], v[96:97], v[48:49] op_sel_hi:[1,0]
v_pk_mul_f32 v[98:99], v[98:99], v[48:49] op_sel_hi:[1,0]
v_pk_mul_f32 v[100:101], v[100:101], v[48:49] op_sel_hi:[1,0]
v_pk_mul_f32 v[102:103], v[102:103], v[48:49] op_sel_hi:[1,0]
C_ 84,96,97
C_ 85,98,99
C_ 86,100,101
C_ 87,102,103
ds_write_b128 v13, v[84:87] offset:2048
.p2align 3
R_ v104,a12
R_ v105,a13
R_ v106,a14
R_ v107,a15
R_ v108,a32
R_ v109,a33
R_ v110,a34
R_ v111,a35
v_pk_mul_f32 v[104:105], v[104:105], v[48:49] op_sel:[0,1]
v_pk_mul_f32 v[106:107], v[106:107], v[48:49] op_sel:[0,1]
v_pk_mul_f32 v[108:109], v[108:109], v[48:49] op_sel:[0,1]
v_pk_mul_f32 v[110:111], v[110:111], v[48:49] op_sel:[0,1]
C_ 88,104,105
C_ 89,106,107
C_ 90,108,109
C_ 91,110,111
ds_write_b128 v13, v[88:91] offset:3072
.p2align 3
R_ v96,a16
R_ v97,a17
R_ v98,a18
R_ v99,a19
R_ v100,a36
R_ v101,a37
R_ v102,a38
R_ v103,a39
v_pk_mul_f32 v[96:97], v[96:97], v[50:51] op_sel_hi:[1,0]
v_pk_mul_f32 v[98:99], v[98:99], v[50:51] op_sel_hi:[1,0]
v_pk_mul_f32 v[100:101], v[100:101], v[50:51] op_sel_hi:[1,0]
v_pk_mul_f32 v[102:103], v[102:103], v[50:51] op_sel_hi:[1,0]
C_ 92,96,97
C_ 93,98,99
C_ 94,100,101
C_ 95,102,103
ds_write_b128 v13, v[92:95] offset:4096
.p2align 3
R_ v104,a40
R_ v105,a41
R_ v106,a42
R_ v107,a43
R_ v108,a60
R_ v109,a61
R_ v110,a62
R_ v111,a63
v_pk_mul_f32 v[104:105], v[104:105], v[46:47] op_sel_hi:[1,0]
v_pk_mul_f32 v[106:107], v[106:107], v[46:47] op_sel_hi:[1,0]
v_pk_mul_f32 v[108:109], v[108:109], v[46:47] op_sel_hi:[1,0]
v_pk_mul_f32 v[110:111], v[110:111], v[46:47] op_sel_hi:[1,0]
C_ 76,104,105
C_ 77,106,107
C_ 78,108,109
C_ 79,110,111
ds_write_b128 v13, v[76:79] offset:5120
.p2align 3
R_ v96,a44
R_ v97,a45
R_ v98,a46
R_ v99,a47
R_ v100,a64
R_ v101,a65
R_ v102,a66
R_ v103,a67
v_pk_mul_f32 v[96:97], v[96:97], v[46:47] op_sel:[0,1]
v_pk_mul_f32 v[98:99], v[98:99], v[46:47] op_sel:[0,1]
v_pk_mul_f32 v[100:101], v[100:101], v[46:47] op_sel:[0,1]
v_pk_mul_f32 v[102:103], v[102:103], v[46:47] op_sel:[0,1]
C_ 80,96,97
C_ 81,98,99
C_ 82,100,101
C_ 83,102,103
ds_write_b128 v13, v[80:83] offset:6144
.p2align 3
R_ v104,a48
R_ v105,a49
R_ v106,a50
R_ v107,a51
R_ v108,a68
R_ v109,a69
R_ v110,a70
R_ v111,a71
v_pk_mul_f32 v[104:105], v[104:105], v[48:49] op_sel_hi:[1,0]
v_pk_mul_f32 v[106:107], v[106:107], v[48:49] op_sel_hi:[1,0]
v_pk_mul_f32 v[108:109], v[108:109], v[48:49] op_sel_hi:[1,0]
v_pk_mul_f32 v[110:111], v[110:111], v[48:49] op_sel_hi:[1,0]
C_ 84,104,105
C_ 85,106,107
C_ 86,108,109
C_ 87,110,111
ds_write_b128 v13, v[84:87] offset:7168
.p2align 3
R_ v96,a52
R_ v97,a53
R_ v98,a54
R_ v99,a55
R_ v100,a72
R_ v101,a73
R_ v102,a74
R_ v103,a75
v_pk_mul_f32 v[96:97], v[96:97], v[48:49] op_sel:[0,1]
v_pk_mul_f32 v[98:99], v[98:99], v[48:49] op_sel:[0,1]
v_pk_mul_f32 v[100:101], v[100:101], v[48:49] op_sel:[0,1]
v_pk_mul_f32 v[102:103], v[102:103], v[48:49] op_sel:[0,1]
C_ 88,96,97
C_ 89,98,99
C_ 90,100,101
C_ 91,102,103
ds_write_b128 v13, v[88:91] offset:8192
.p2align 3
R_ v104,a56
R_ v105,a57
R_ v106,a58
R_ v107,a59
R_ v108,a76
R_ v109,a77
R_ v110,a78
R_ v111,a79
v_pk_mul_f32 v[104:105], v[104:105], v[50:51] op_sel_hi:[1,0]
v_pk_mul_f32 v[106:107], v[106:107], v[50:51] op_sel_hi:[1,0]
v_pk_mul_f32 v[108:109], v[108:109], v[50:51] op_sel_hi:[1,0]
v_pk_mul_f32 v[110:111], v[110:111], v[50:51] op_sel_hi:[1,0]
C_ 92,104,105
C_ 93,106,107
C_ 94,108,109
C_ 95,110,111
ds_write_b128 v13, v[92:95] offset:9216
.p2align 3
R_ v104,a92
R_ v105,a93
R_ v106,a94
R_ v107,a95
R_ v108,a112
R_ v109,a113
R_ v110,a114
R_ v111,a115
v_pk_mul_f32 v[104:105], v[104:105], v[48:49] op_sel:[0,1]
v_pk_mul_f32 v[106:107], v[106:107], v[48:49] op_sel:[0,1]
v_pk_mul_f32 v[108:109], v[108:109], v[48:49] op_sel:[0,1]
v_pk_mul_f32 v[110:111], v[110:111], v[48:49] op_sel:[0,1]
C_ 88,104,105
C_ 89,106,107
C_ 90,108,109
C_ 91,110,111
X_ a240,v88
X_ a241,v89
X_ a242,v90
X_ a243,v91
.p2align 3
R_ v96,a96
R_ v97,a97
R_ v98,a98
R_ v99,a99
R_ v100,a116
R_ v101,a117
R_ v102,a118
R_ v103,a119
v_pk_mul_f32 v[96:97], v[96:97], v[50:51] op_sel_hi:[1,0]
v_pk_mul_f32 v[98:99], v[98:99], v[50:51] op_sel_hi:[1,0]
v_pk_mul_f32 v[100:101], v[100:101], v[50:51] op_sel_hi:[1,0]
v_pk_mul_f32 v[102:103], v[102:103], v[50:51] op_sel_hi:[1,0]
C_ 92,96,97
C_ 93,98,99
C_ 94,100,101
C_ 95,102,103
X_ a244,v92
X_ a245,v93
X_ a246,v94
X_ a247,v95
.p2align 3
R_ v96,a124
R_ v97,a125
R_ v98,a126
R_ v99,a127
R_ v100,a144
R_ v101,a145
R_ v102,a146
R_ v103,a147
v_pk_mul_f32 v[96:97], v[96:97], v[46:47] op_sel:[0,1]
v_pk_mul_f32 v[98:99], v[98:99], v[46:47] op_sel:[0,1]
v_pk_mul_f32 v[100:101], v[100:101], v[46:47] op_sel:[0,1]
v_pk_mul_f32 v[102:103], v[102:103], v[46:47] op_sel:[0,1]
C_ 80,96,97
C_ 81,98,99
C_ 82,100,101
C_ 83,102,103
v_mov_b32 v28, v80
v_mov_b32 v29, v81
v_mov_b32 v51, v82
v_mov_b32 v6, v83
.p2align 3
R_ v104,a128
R_ v105,a129
R_ v106,a130
R_ v107,a131
R_ v108,a148
R_ v109,a149
R_ v110,a150
R_ v111,a151
v_pk_mul_f32 v[104:105], v[104:105], v[48:49] op_sel_hi:[1,0]
v_pk_mul_f32 v[106:107], v[106:107], v[48:49] op_sel_hi:[1,0]
v_pk_mul_f32 v[108:109], v[108:109], v[48:49] op_sel_hi:[1,0]
v_pk_mul_f32 v[110:111], v[110:111], v[48:49] op_sel_hi:[1,0]
C_ 84,104,105
C_ 85,106,107
C_ 86,108,109
C_ 87,110,111
v_mov_b32 v14, v84
v_mov_b32 v15, v85
X_ a249,v86
X_ a250,v87
.p2align 3
R_ v96,a132
R_ v97,a133
R_ v98,a134
R_ v99,a135
R_ v100,a152
R_ v101,a153
R_ v102,a154
R_ v103,a155
v_pk_mul_f32 v[96:97], v[96:97], v[48:49] op_sel:[0,1]
v_pk_mul_f32 v[98:99], v[98:99], v[48:49] op_sel:[0,1]
v_pk_mul_f32 v[100:101], v[100:101], v[48:49] op_sel:[0,1]
v_pk_mul_f32 v[102:103], v[102:103], v[48:49] op_sel:[0,1]
C_ 88,96,97
C_ 89,98,99
C_ 90,100,101
C_ 91,102,103
ds_write_b128 v12, v[88:91] offset:0
.p2align 3
R_ v104,a136
R_ v105,a137
R_ v106,a138
R_ v107,a139
R_ v108,a156
R_ v109,a157
R_ v110,a158
R_ v111,a159
v_pk_mul_f32 v[104:105], v[104:105], v[50:51] op_sel_hi:[1,0]
v_pk_mul_f32 v[106:107], v[106:107], v[50:51] op_sel_hi:[1,0]
v_pk_mul_f32 v[108:109], v[108:109], v[50:51] op_sel_hi:[1,0]
v_pk_mul_f32 v[110:111], v[110:111], v[50:51] op_sel_hi:[1,0]
C_ 92,104,105
C_ 93,106,107
C_ 94,108,109
C_ 95,110,111
ds_write_b128 v12, v[92:95] offset:1024
.p2align 3
R_ v96,a160
R_ v97,a161
R_ v98,a162
R_ v99,a163
R_ v100,a180
R_ v101,a181
R_ v102,a182
R_ v103,a183
v_pk_mul_f32 v[96:97], v[96:97], v[46:47] op_sel_hi:[1,0]
v_pk_mul_f32 v[98:99], v[98:99], v[46:47] op_sel_hi:[1,0]
v_pk_mul_f32 v[100:101], v[100:101], v[46:47] op_sel_hi:[1,0]
v_pk_mul_f32 v[102:103], v[102:103], v[46:47] op_sel_hi:[1,0]
C_ 76,96,97
C_ 77,98,99
C_ 78,100,101
C_ 79,102,103
ds_write_b128 v12, v[76:79] offset:2048
.p2align 3
R_ v104,a164
R_ v105,a165
R_ v106,a166
R_ v107,a167
R_ v108,a184
R_ v109,a185
R_ v110,a186
R_ v111,a187
v_pk_mul_f32 v[104:105], v[104:105], v[46:47] op_sel:[0,1]
v_pk_mul_f32 v[106:107], v[106:107], v[46:47] op_sel:[0,1]
v_pk_mul_f32 v[108:109], v[108:109], v[46:47] op_sel:[0,1]
v_pk_mul_f32 v[110:111], v[110:111], v[46:47] op_sel:[0,1]
C_ 80,104,105
C_ 81,106,107
C_ 82,108,109
C_ 83,110,111
ds_write_b128 v12, v[80:83] offset:3072
.p2align 3
R_ v96,a168
R_ v97,a169
R_ v98,a170
R_ v99,a171
R_ v100,a188
R_ v101,a189
R_ v102,a190
R_ v103,a191
v_pk_mul_f32 v[96:97], v[96:97], v[48:49] op_sel_hi:[1,0]
v_pk_mul_f32 v[98:99], v[98:99], v[48:49] op_sel_hi:[1,0]
v_pk_mul_f32 v[100:101], v[100:101], v[48:49] op_sel_hi:[1,0]
v_pk_mul_f32 v[102:103], v[102:103], v[48:49] op_sel_hi:[1,0]
C_ 84,96,97
C_ 85,98,99
C_ 86,100,101
C_ 87,102,103
ds_write_b128 v12, v[84:87] offset:4096
.p2align 3
R_ v104,a172
R_ v105,a173
R_ v106,a174
R_ v107,a175
R_ v108,a192
R_ v109,a193
R_ v110,a194
R_ v111,a195
v_pk_mul_f32 v[104:105], v[104:105], v[48:49] op_sel:[0,1]
v_pk_mul_f32 v[106:107], v[106:107], v[48:49] op_sel:[0,1]
v_pk_mul_f32 v[108:109], v[108:109], v[48:49] op_sel:[0,1]
v_pk_mul_f32 v[110:111], v[110:111], v[48:49] op_sel:[0,1]
C_ 88,104,105
C_ 89,106,107
C_ 90,108,109
C_ 91,110,111
ds_write_b128 v12, v[88:91] offset:5120
.p2align 3
R_ v96,a176
R_ v97,a177
R_ v98,a178
R_ v99,a179
R_ v100,a196
R_ v101,a197
R_ v102,a198
R_ v103,a199
v_pk_mul_f32 v[96:97], v[96:97], v[50:51] op_sel_hi:[1,0]
v_pk_mul_f32 v[98:99], v[98:99], v[50:51] op_sel_hi:[1,0]
v_pk_mul_f32 v[100:101], v[100:101], v[50:51] op_sel_hi:[1,0]
v_pk_mul_f32 v[102:103], v[102:103], v[50:51] op_sel_hi:[1,0]
C_ 92,96,97
C_ 93,98,99
C_ 94,100,101
C_ 95,102,103
ds_write_b128 v12, v[92:95] offset:12288
.p2align 3
R_ v104,a200
R_ v105,a201
R_ v106,a202
R_ v107,a203
R_ v108,a220
R_ v109,a221
R_ v110,a222
R_ v111,a223
v_pk_mul_f32 v[104:105], v[104:105], v[46:47] op_sel_hi:[1,0]
v_pk_mul_f32 v[106:107], v[106:107], v[46:47] op_sel_hi:[1,0]
v_pk_mul_f32 v[108:109], v[108:109], v[46:47] op_sel_hi:[1,0]
v_pk_mul_f32 v[110:111], v[110:111], v[46:47] op_sel_hi:[1,0]
C_ 76,104,105
C_ 77,106,107
C_ 78,108,109
C_ 79,110,111
ds_write_b128 v12, v[76:79] offset:13312
.p2align 3
R_ v96,a204
R_ v97,a205
R_ v98,a206
R_ v99,a207
R_ v100,a224
R_ v101,a225
R_ v102,a226
R_ v103,a227
v_pk_mul_f32 v[96:97], v[96:97], v[46:47] op_sel:[0,1]
v_pk_mul_f32 v[98:99], v[98:99], v[46:47] op_sel:[0,1]
v_pk_mul_f32 v[100:101], v[100:101], v[46:47] op_sel:[0,1]
v_pk_mul_f32 v[102:103], v[102:103], v[46:47] op_sel:[0,1]
C_ 80,96,97
C_ 81,98,99
C_ 82,100,101
C_ 83,102,103
ds_write_b128 v12, v[80:83] offset:14336
.p2align 3
R_ v104,a208
R_ v105,a209
R_ v106,a210
R_ v107,a211
R_ v108,a228
R_ v109,a229
R_ v110,a230
R_ v111,a231
v_pk_mul_f32 v[104:105], v[104:105], v[48:49] op_sel_hi:[1,0]
v_pk_mul_f32 v[106:107], v[106:107], v[48:49] op_sel_hi:[1,0]
v_pk_mul_f32 v[108:109], v[108:109], v[48:49] op_sel_hi:[1,0]
v_pk_mul_f32 v[110:111], v[110:111], v[48:49] op_sel_hi:[1,0]
C_ 84,104,105
C_ 85,106,107
C_ 86,108,109
C_ 87,110,111
ds_write_b128 v12, v[84:87] offset:15360
s_waitcnt vmcnt(6) lgkmcnt(0)
s_barrier
Dv 30,%0,6144
Dv 34,v1,6144
Dv 38,%0,7168
.p2align 3
R_ v96,a212
R_ v97,a213
R_ v98,a214
R_ v99,a215
R_ v100,a232
R_ v101,a233
R_ v102,a234
R_ v103,a235
v_pk_mul_f32 v[96:97], v[96:97], v[48:49] op_sel:[0,1]
v_pk_mul_f32 v[98:99], v[98:99], v[48:49] op_sel:[0,1]
v_pk_mul_f32 v[100:101], v[100:101], v[48:49] op_sel:[0,1]
v_pk_mul_f32 v[102:103], v[102:103], v[48:49] op_sel:[0,1]
C_ 88,96,97
C_ 89,98,99
C_ 90,100,101
C_ 91,102,103
ds_write_b128 v12, v[88:91] offset:16384
.p2align 3
R_ v104,a216
R_ v105,a217
R_ v106,a218
R_ v107,a219
R_ v108,a236
R_ v109,a237
R_ v110,a238
R_ v111,a239
v_pk_mul_f32 v[104:105], v[104:105], v[50:51] op_sel_hi:[1,0]
v_pk_mul_f32 v[106:107], v[106:107], v[50:51] op_sel_hi:[1,0]
v_pk_mul_f32 v[108:109], v[108:109], v[50:51] op_sel_hi:[1,0]
v_pk_mul_f32 v[110:111], v[110:111], v[50:51] op_sel_hi:[1,0]
C_ 92,104,105
C_ 93,106,107
C_ 94,108,109
C_ 95,110,111
ds_write_b128 v12, v[92:95] offset:17408
.Latt3:
W_ 4
Zvvv 96,30,136
Dv 42,v1,7168
R_ v116,a80
Zvvv 100,30,140
R_ v117,a81
R_ v118,a82
Zvvv 104,30,144
R_ v119,a83
R_ v120,a100
Zvvv 108,30,148
R_ v121,a101
R_ v122,a102
Zvvv 112,30,152
W_ 4
R_ v123,a103
U_ v116,v116,v46
Mvvv 96,34,156
Dv 30,%0,8192
U_ v117,v117,v46
Mvvv 100,34,160
U_ v118,v118,v46
U_ v119,v119,v46
Mvvv 104,34,164
U_ v120,v120,v46
U_ v121,v121,v46
Mvvv 108,34,168
U_ v122,v122,v46
U_ v123,v123,v46
Mvvv 112,34,172
W_ 4
C_ 20,116,117
C_ 21,118,119
Mvvv 96,38,176
Dv 34,v1,8192
C_ 22,120,121
Mvvv 100,38,180
C_ 23,122,123
R_ v124,a84
Mvvv 104,38,184
R_ v125,a85
R_ v126,a86
Mvvv 108,38,188
R_ v127,a87
R_ v128,a104
Mvvv 112,38,192
W_ 2
R_ v129,a105
R_ v130,a106
Mvvv 96,42,196
Dv 38,%0,0
R_ v131,a107
Mvvv 100,42,200
U_ v124,v124,v47
U_ v125,v125,v47
Mvvv 104,42,204
U_ v126,v126,v47
U_ v127,v127,v47
Mvvv 108,42,208
U_ v128,v128,v47
U_ v129,v129,v47
Mvvv 112,42,212
W_ 2
U_ v130,v130,v47
U_ v131,v131,v47
Mvvv 96,30,216
Dv 42,v1,0
C_ 24,124,125
Mvvv 100,30,220
C_ 25,126,127
C_ 26,128,129
Mvvv 104,30,224
C_ 27,130,131
R_ v116,a88
Mvvv 108,30,228
R_ v117,a89
R_ v118,a90
Mvvv 112,30,232
W_ 2
R_ v119,a91
R_ v120,a108
Mvvv 96,34,236
Dv 30,%0,1024
R_ v121,a109
Mvvv 100,34,240
R_ v122,a110
R_ v123,a111
Mvvv 104,34,244
U_ v116,v116,v48
U_ v117,v117,v48
Mvvv 108,34,248
U_ v118,v118,v48
U_ v119,v119,v48
Mvvv 112,34,252
W_ 2
U_ v120,v120,v48
U_ v121,v121,v48
Zvvv 76,38,136
Dv 34,v1,1024
U_ v122,v122,v48
Zvvv 80,38,140
U_ v123,v123,v48
C_ 52,116,117
Zvvv 84,38,144
C_ 53,118,119
C_ 54,120,121
Zvvv 88,38,148
C_ 55,122,123
R_ v124,a120
Zvvv 92,38,152
W_ 2
R_ v125,a121
R_ v126,a122
Mvvv 76,42,156
Dv 38,%0,2048
R_ v127,a123
Mvvv 80,42,160
R_ v128,a140
R_ v129,a141
Mvvv 84,42,164
R_ v130,a142
R_ v131,a143
Mvvv 88,42,168
U_ v124,v124,v46
U_ v125,v125,v46
Mvvv 92,42,172
W_ 2
U_ v126,v126,v46
U_ v127,v127,v46
Mvvv 76,30,176
Dv 42,v1,2048
U_ v128,v128,v46
Mvvv 80,30,180
U_ v129,v129,v46
U_ v130,v130,v46
Mvvv 84,30,184
U_ v131,v131,v46
C_ 16,124,125
Mvvv 88,30,188
C_ 17,126,127
C_ 18,128,129
Mvvv 92,30,192
s_waitcnt vmcnt(0) lgkmcnt(0)
s_barrier
Mvvv 76,34,196
S_ s65,s61,24576
Dv 30,%0,18432
C_ 19,130,131
Mvvv 76,38,216
S_ m0,s51,0
v_max3_f32 v56, v96, v97, v98
v_max_f32 v56, v56, v99
Mvvv 76,42,236
Ln v7,32,s65,0
Mvvv 80,34,200
v_max3_f32 v60, v100, v101, v102
v_max_f32 v60, v60, v103
Mvvv 80,38,220
Ln v8,32,s65,3072
Mvvv 80,42,240
S_ m0,s51,896
v_max3_f32 v64, v104, v105, v106
v_max_f32 v64, v64, v107
Mvvv 84,34,204
Ln v7,32,s65,128
Mvvv 84,38,224
v_max3_f32 v68, v108, v109, v110
v_max_f32 v68, v68, v111
Mvvv 84,42,244
Ln v8,32,s65,3200
Mvvv 88,34,208
S_ m0,s51,1792
v_max3_f32 v72, v112, v113, v114
v_max_f32 v72, v72, v115
Mvvv 88,38,228
Ln v7,32,s65,256
Mvvv 88,42,248
v_max3_f32 v56, v56, v76, v77
v_max3_f32 v56, v56, v78, v79
Mvvv 92,34,212
Ln v8,32,s65,3328
Mvvv 92,38,232
v_max3_f32 v60, v60, v80, v81
v_max3_f32 v60, v60, v82, v83
Mvvv 92,42,252
v_max3_f32 v64, v64, v84, v85
v_max3_f32 v64, v64, v86, v87
Dv 34,v1,18432
.p2align 3
v_mov_b32 v11, 0x3f803f80
v_permlane32_swap_b32 v56, v60
v_max3_f32 v68, v68, v88, v89
v_max3_f32 v68, v68, v90, v91
v_max_f32 v56, v56, v60
v_max3_f32 v72, v72, v92, v93
v_max3_f32 v72, v72, v94, v95
v_mov_b32 v73, v72
v_permlane32_swap_b32 v64, v68
v_max_f32 v64, v64, v68
v_permlane32_swap_b32 v72, v73
v_max_f32 v72, v72, v73
v_mov_b32 v73, v72
v_permlane16_swap_b32 v56, v64
v_max_f32 v56, v56, v64
v_permlane16_swap_b32 v72, v73
B_ v56,0xc0e00000,v56
v_max_f32 v72, v72, v73
v_mov_b32 v64, v56
B_ v72,0xc0e00000,v72
v_mov_b32 v73, v72
v_permlane16_swap_b32 v56, v64
v_mov_b32 v60, v56
v_mov_b32 v68, v64
v_mov_b64 v[74:75], v[72:73]
v_permlane32_swap_b32 v56, v60
v_permlane32_swap_b32 v64, v68
v_mov_b32 v57, v56
v_mov_b32 v61, v60
v_mov_b32 v65, v64
v_mov_b32 v69, v68
v_pk_add_f32 v[96:97], v[96:97], v[56:57]
v_pk_add_f32 v[98:99], v[98:99], v[56:57]
v_pk_add_f32 v[76:77], v[76:77], v[56:57]
v_pk_add_f32 v[78:79], v[78:79], v[56:57]
v_mov_b64 v[58:59], v[56:57]
v_pk_add_f32 v[100:101], v[100:101], v[60:61]
v_pk_add_f32 v[102:103], v[102:103], v[60:61]
v_pk_add_f32 v[80:81], v[80:81], v[60:61]
v_pk_add_f32 v[82:83], v[82:83], v[60:61]
v_mov_b64 v[62:63], v[60:61]
v_pk_add_f32 v[104:105], v[104:105], v[64:65]
v_pk_add_f32 v[106:107], v[106:107], v[64:65]
v_pk_add_f32 v[84:85], v[84:85], v[64:65]
v_pk_add_f32 v[86:87], v[86:87], v[64:65]
v_mov_b64 v[66:67], v[64:65]
v_pk_add_f32 v[108:109], v[108:109], v[68:69]
v_pk_add_f32 v[110:111], v[110:111], v[68:69]
v_pk_add_f32 v[88:89], v[88:89], v[68:69]
v_pk_add_f32 v[90:91], v[90:91], v[68:69]
v_mov_b64 v[70:71], v[68:69]
v_pk_add_f32 v[112:113], v[112:113], v[72:73]
v_pk_add_f32 v[114:115], v[114:115], v[72:73]
v_pk_add_f32 v[92:93], v[92:93], v[72:73]
v_pk_add_f32 v[94:95], v[94:95], v[72:73]
E_ 96
E_ 97
E_ 98
E_ 99
E_ 76
E_ 77
E_ 78
E_ 79
C_ 99,98,99
C_ 98,96,97
C_ 96,76,77
C_ 97,78,79
E_ 100
E_ 101
E_ 102
E_ 103
E_ 80
E_ 81
E_ 82
E_ 83
C_ 103,102,103
C_ 102,100,101
C_ 100,80,81
C_ 101,82,83
.Lstep3_1:
W_ 1
Nvvv 116,30,136,56
s_nop 0
Dv 38,%0,19456
Dv 42,v1,19456
W_ 2
Mvvv 116,34,156
E_ 104
Nvvv 120,30,140,60
E_ 105
Mvvv 120,34,160
E_ 106
Nvvv 124,30,144,64
E_ 107
Mvvv 124,34,164
E_ 84
Nvvv 128,30,148,68
E_ 85
Mvvv 128,34,168
E_ 86
Nvvv 132,30,152,72
E_ 87
Mvvv 132,34,172
W_ 0
C_ 107,106,107
C_ 106,104,105
Mvvv 132,38,192
s_nop 0
Dv 30,%0,20480
Dv 34,v1,20480
Mvvv 132,42,212
C_ 104,84,85
C_ 105,86,87
Mvvv 128,38,188
E_ 108
Mvvv 128,42,208
E_ 109
Mvvv 124,38,184
E_ 110
Mvvv 124,42,204
E_ 111
Mvvv 120,38,180
E_ 88
Mvvv 120,42,200
E_ 89
Mvvv 116,38,176
E_ 90
Mvvv 116,42,196
W_ 0
E_ 91
Mvvv 116,30,216
s_nop 0
Dv 38,%0,12288
Dv 42,v1,12288
Mvvv 116,34,236
C_ 111,110,111
C_ 110,108,109
Mvvv 120,30,220
C_ 108,88,89
C_ 109,90,91
Mvvv 120,34,240
E_ 112
Mvvv 124,30,224
E_ 113
Mvvv 124,34,244
E_ 114
Mvvv 128,30,228
E_ 115
Mvvv 128,34,248
E_ 92
Mvvv 132,30,232
E_ 93
Mvvv 132,34,252
W_ 0
E_ 94
Nvvv 76,38,136,56
s_nop 0
Dv 30,%0,13312
Dv 34,v1,13312
Mvvv 76,42,156
E_ 95
Nvvv 80,38,140,60
C_ 115,114,115
C_ 114,112,113
Mvvv 80,42,160
)A"
R"A(C_ 112,92,93
C_ 113,94,95
Nvvv 84,38,144,64
E_ 116
Mvvv 84,42,164
E_ 117
Nvvv 88,38,148,68
E_ 118
Mvvv 88,42,168
E_ 119
Nvvv 92,38,152,72
C_ 119,118,119
C_ 118,116,117
Mvvv 92,42,172
W_ 0
E_ 120
Mvvv 92,30,192
s_nop 0
Dv 38,%0,14336
Dv 42,v1,14336
Mvvv 92,34,212
X_ a180,v11
X_ a181,v11
Mvvv 88,30,188
X_ a182,v11
X_ a183,v11
Mvvv 88,34,208
E_ 121
Mvvv 84,30,184
E_ 122
Mvvv 84,34,204
E_ 123
Mvvv 80,30,180
C_ 123,122,123
C_ 122,120,121
Mvvv 80,34,200
E_ 124
Mvvv 76,30,176
E_ 125
Mvvv 76,34,196
S_ s65,s61,12288
E_ 126
W_ 1
Mvvv 76,38,216
S_ m0,s51,12288
E_ 127
W_ 0
Mvvv 76,42,236
s_waitcnt vmcnt(18) lgkmcnt(0)
s_barrier
C_ 127,126,127
Mvvv 80,38,220
L_ v9,36,s65,0
Mvvv 80,42,240
C_ 126,124,125
Mvvv 84,38,224
L_ v9,36,s65,3072
S_ m0,s51,13184
Mvvv 84,42,244
s_nop 0
Tv 30,v2,24576
Tv 32,v2,30720
Mvvv 88,38,228
L_ v9,36,s65,128
Mvvv 88,42,248
s_nop 0
Tv 34,v3,24576
Tv 36,v3,30720
Mvvv 92,38,232
L_ v9,36,s65,3200
S_ m0,s51,14080
Mvvv 92,42,252
E_ 128
Zaav 220,180,96
L_ v9,36,s65,256
Zaav 224,180,100
E_ 129
Zaav 228,180,104
L_ v9,36,s65,3328
Zaav 232,180,108
E_ 130
Zaav 236,180,112
W_ 0
E_ 131
Zavv 0,30,96
s_nop 0
Tv 38,v4,24576
Tv 40,v4,30720
Zavv 20,34,96
C_ 131,130,131
C_ 130,128,129
Zavv 24,34,100
s_nop 0
Tv 42,v5,24576
Tv 44,v5,30720
Zavv 4,30,100
E_ 132
Zavv 8,30,104
E_ 133
Zavv 28,34,104
R_ v46,a220
R_ v47,a224
Zavv 32,34,108
R_ v48,a228
R_ v49,a232
Zavv 12,30,108
R_ v50,a236
Zavv 16,30,112
E_ 134
Zavv 36,34,112
W_ 0
E_ 135
Zavv 56,38,112
s_nop 0
Tv 30,v2,25600
Tv 32,v2,31744
Zavv 76,42,112
C_ 135,134,135
C_ 134,132,133
Zavv 72,42,108
s_nop 0
Tv 34,v3,25600
Tv 36,v3,31744
Zavv 52,38,108
E_ 76
Zavv 48,38,104
E_ 77
Zavv 68,42,104
E_ 78
Zavv 64,42,100
E_ 79
Zavv 44,38,100
C_ 116,76,77
C_ 117,78,79
Zavv 40,38,96
E_ 80
Zavv 60,42,96
W_ 0
E_ 81
Zavv 80,30,96
s_nop 0
Tv 38,v4,25600
Tv 40,v4,31744
Zavv 100,34,96
E_ 82
Zavv 104,34,100
s_nop 0
Tv 42,v5,25600
Tv 44,v5,31744
Zavv 84,30,100
E_ 83
Zavv 88,30,104
C_ 120,80,81
C_ 121,82,83
Zavv 108,34,104
E_ 84
Zavv 112,34,108
E_ 85
Zavv 92,30,108
E_ 86
Zavv 96,30,112
E_ 87
Zavv 116,34,112
W_ 0
C_ 124,84,85
C_ 125,86,87
Zavv 136,38,112
s_nop 0
Tv 30,v2,26624
Tv 32,v2,32768
Zavv 156,42,112
E_ 88
Zavv 152,42,108
s_nop 0
Tv 34,v3,26624
Tv 36,v3,32768
Zavv 132,38,108
E_ 89
Zavv 128,38,104
E_ 90
Zavv 148,42,104
E_ 91
Zavv 144,42,100
C_ 128,88,89
C_ 129,90,91
Zavv 124,38,100
E_ 92
Zavv 120,38,96
E_ 93
Zavv 140,42,96
W_ 0
E_ 94
Zavv 160,30,96
s_nop 0
Tv 38,v4,26624
Tv 40,v4,32768
Zavv 180,34,96
E_ 95
Zavv 184,34,100
s_nop 0
Tv 42,v5,26624
Tv 44,v5,32768
Zavv 164,30,100
C_ 132,92,93
C_ 133,94,95
Zavv 168,30,104
v_mov_b32 v92, 0x3f803f80
v_mov_b32 v93, 0x3f803f80
Zavv 188,34,104
S_ s65,s61,36864
v_mov_b32 v94, 0x3f803f80
v_mov_b32 v95, 0x3f803f80
Zavv 192,34,108
S_ m0,s51,24576
Zavv 172,30,108
s_waitcnt vmcnt(6) lgkmcnt(0)
s_barrier
Zavv 176,30,112
Ln v7,32,s65,0
Zavv 196,34,112
Zavv 216,38,112
Dv 30,%0,6144
Zavv 236,42,112
Dv 34,v1,6144
Zavv 232,42,108
Ln v8,32,s65,3072
S_ m0,s51,25472
Zavv 212,38,108
Zavv 208,38,104
Ln v7,32,s65,128
Zavv 228,42,104
Zavv 224,42,100
Ln v8,32,s65,3200
S_ m0,s51,26368
Zavv 204,38,100
Zavv 200,38,96
Ln v7,32,s65,256
Zavv 220,42,96
Ln v8,32,s65,3328
W_ 0
s_nop 7
s_branch .Lstep1_2
.Latt1:
R_ v13,a248
S_ s53,s51,36864
v_add_u32 v12, s53, v13
s_mul_i32 s53, s4, 10240
S_ s53,s53,0
v_add_u32 v13, s53, v13
W_ 2
Zvvv 96,30,136
Dv 42,v1,44032
R_ v2,a245
Zvvv 100,30,140
ds_write_b128 v13, a[0:3] offset:0
Zvvv 104,30,144
v_xor_b32 v3, 32, v2
v_xor_b32 v4, 64, v2
Zvvv 108,30,148
ds_write_b128 v13, a[4:7] offset:1024
Zvvv 112,30,152
W_ 4
v_xor_b32 v5, 96, v2
s_mov_b64 s[24:25], s[16:17]
Mvvv 96,34,156
s_mov_b32 s26, 7864320
Dv 30,%0,45056
s_mov_b64 s[28:29], s[22:23]
Mvvv 100,34,160
ds_write_b128 v13, a[8:11] offset:2048
Mvvv 104,34,164
s_mov_b32 s30, 1638400
R_ v20,a80
s_mul_i32 s52, s4, 10240
Mvvv 108,34,168
ds_write_b128 v13, a[12:15] offset:3072
Mvvv 112,34,172
W_ 6
R_ v21,a81
R_ v22,a82
Mvvv 96,38,176
Dv 34,v1,45056
R_ v23,a83
Mvvv 100,38,180
ds_write_b128 v13, a[16:19] offset:4096
Mvvv 104,38,184
R_ v24,a84
R_ v25,a85
Mvvv 108,38,188
ds_write_b128 v13, a[40:43] offset:5120
Mvvv 112,38,192
W_ 8
R_ v26,a86
R_ v27,a87
Mvvv 96,42,196
Dv 38,%0,36864
R_ v52,a88
Mvvv 100,42,200
ds_write_b128 v13, a[44:47] offset:6144
Mvvv 104,42,204
R_ v53,a89
R_ v54,a90
Mvvv 108,42,208
ds_write_b128 v13, a[48:51] offset:7168
Mvvv 112,42,212
W_ 8
R_ v55,a91
v_accvgpr_mov_b32 a240, a92
Mvvv 96,30,216
Dv 42,v1,36864
v_accvgpr_mov_b32 a241, a93
Mvvv 100,30,220
ds_write_b128 v13, a[52:55] offset:8192
Mvvv 104,30,224
v_accvgpr_mov_b32 a242, a94
v_accvgpr_mov_b32 a243, a95
Mvvv 108,30,228
ds_write_b128 v13, a[56:59] offset:9216
Mvvv 112,30,232
W_ 8
v_accvgpr_mov_b32 a244, a96
v_accvgpr_mov_b32 a245, a97
Mvvv 96,34,236
Dv 30,%0,37888
v_accvgpr_mov_b32 a246, a98
Mvvv 100,34,240
v_accvgpr_mov_b32 a247, a99
R_ v16,a120
Mvvv 104,34,244
R_ v17,a121
R_ v18,a122
Mvvv 108,34,248
R_ v19,a123
R_ v28,a124
Mvvv 112,34,252
W_ 6
R_ v29,a125
R_ v51,a126
Zvvv 76,38,136
Dv 34,v1,37888
R_ v6,a127
Zvvv 80,38,140
R_ v14,a128
R_ v15,a129
Zvvv 84,38,144
v_accvgpr_mov_b32 a249, a130
v_accvgpr_mov_b32 a250, a131
Zvvv 88,38,148
v_max3_f32 v56, v96, v97, v98
v_max_f32 v56, v56, v99
Zvvv 92,38,152
W_ 4
v_max3_f32 v60, v100, v101, v102
v_max_f32 v60, v60, v103
Mvvv 76,42,156
Dv 38,%0,38912
v_max3_f32 v64, v104, v105, v106
Mvvv 80,42,160
v_max_f32 v64, v64, v107
Mvvv 84,42,164
v_max3_f32 v68, v108, v109, v110
v_max_f32 v68, v68, v111
Mvvv 88,42,168
v_max3_f32 v72, v112, v113, v114
v_max_f32 v72, v72, v115
Mvvv 92,42,172
W_ 2
Mvvv 76,30,176
Dv 42,v1,38912
Mvvv 80,30,180
Mvvv 84,30,184
Mvvv 88,30,188
Mvvv 92,30,192
s_waitcnt vmcnt(34) lgkmcnt(0)
s_barrier
Mvvv 76,34,196
Dv 30,%0,55296
Mvvv 76,38,216
Mvvv 76,42,236
Mvvv 80,34,200
Mvvv 80,38,220
Mvvv 80,42,240
Mvvv 84,34,204
s_nop 3
v_max3_f32 v56, v56, v76, v77
v_max3_f32 v56, v56, v78, v79
Mvvv 84,38,224
Mvvv 84,42,244
Mvvv 88,34,208
v_max3_f32 v60, v60, v80, v81
v_max3_f32 v60, v60, v82, v83
Mvvv 88,38,228
Mvvv 88,42,248
Mvvv 92,34,212
s_nop 1
v_max3_f32 v64, v64, v84, v85
v_max3_f32 v64, v64, v86, v87
Mvvv 92,38,232
Mvvv 92,42,252
Dv 34,v1,55296
.p2align 3
v_mov_b32 v11, 0x3f803f80
v_permlane32_swap_b32 v56, v60
v_max3_f32 v68, v68, v88, v89
v_max3_f32 v68, v68, v90, v91
v_max_f32 v56, v56, v60
s_nop 1
v_max3_f32 v72, v72, v92, v93
v_max3_f32 v72, v72, v94, v95
v_mov_b32 v73, v72
v_permlane32_swap_b32 v64, v68
v_max_f32 v64, v64, v68
v_permlane32_swap_b32 v72, v73
v_max_f32 v72, v72, v73
v_mov_b32 v73, v72
v_permlane16_swap_b32 v56, v64
v_max_f32 v56, v56, v64
v_permlane16_swap_b32 v72, v73
B_ v56,0xc0e00000,v56
v_max_f32 v72, v72, v73
v_mov_b32 v64, v56
B_ v72,0xc0e00000,v72
v_mov_b32 v73, v72
v_permlane16_swap_b32 v56, v64
v_mov_b32 v60, v56
v_mov_b32 v68, v64
v_mov_b64 v[74:75], v[72:73]
v_permlane32_swap_b32 v56, v60
v_permlane32_swap_b32 v64, v68
v_mov_b32 v57, v56
v_mov_b32 v61, v60
v_mov_b32 v65, v64
v_mov_b32 v69, v68
v_pk_add_f32 v[96:97], v[96:97], v[56:57]
v_pk_add_f32 v[98:99], v[98:99], v[56:57]
v_pk_add_f32 v[76:77], v[76:77], v[56:57]
v_pk_add_f32 v[78:79], v[78:79], v[56:57]
v_mov_b64 v[58:59], v[56:57]
v_pk_add_f32 v[100:101], v[100:101], v[60:61]
v_pk_add_f32 v[102:103], v[102:103], v[60:61]
v_pk_add_f32 v[80:81], v[80:81], v[60:61]
v_pk_add_f32 v[82:83], v[82:83], v[60:61]
v_mov_b64 v[62:63], v[60:61]
v_pk_add_f32 v[104:105], v[104:105], v[64:65]
v_pk_add_f32 v[106:107], v[106:107], v[64:65]
v_pk_add_f32 v[84:85], v[84:85], v[64:65]
v_pk_add_f32 v[86:87], v[86:87], v[64:65]
v_mov_b64 v[66:67], v[64:65]
v_pk_add_f32 v[108:109], v[108:109], v[68:69]
v_pk_add_f32 v[110:111], v[110:111], v[68:69]
v_pk_add_f32 v[88:89], v[88:89], v[68:69]
v_pk_add_f32 v[90:91], v[90:91], v[68:69]
v_mov_b64 v[70:71], v[68:69]
v_pk_add_f32 v[112:113], v[112:113], v[72:73]
v_pk_add_f32 v[114:115], v[114:115], v[72:73]
v_pk_add_f32 v[92:93], v[92:93], v[72:73]
v_pk_add_f32 v[94:95], v[94:95], v[72:73]
E_ 96
E_ 97
E_ 98
E_ 99
E_ 76
E_ 77
E_ 78
E_ 79
C_ 99,98,99
C_ 98,96,97
C_ 96,76,77
C_ 97,78,79
E_ 100
E_ 101
E_ 102
E_ 103
E_ 80
E_ 81
E_ 82
E_ 83
C_ 103,102,103
C_ 102,100,101
C_ 100,80,81
C_ 101,82,83
.Lstep1_1:
W_ 1
Nvvv 116,30,136,56
s_nop 0
Dv 38,%0,56320
Dv 42,v1,56320
W_ 2
Mvvv 116,34,156
E_ 104
Nvvv 120,30,140,60
E_ 105
Mvvv 120,34,160
E_ 106
Nvvv 124,30,144,64
E_ 107
Mvvv 124,34,164
E_ 84
Nvvv 128,30,148,68
E_ 85
Mvvv 128,34,168
E_ 86
Nvvv 132,30,152,72
E_ 87
Mvvv 132,34,172
W_ 0
C_ 107,106,107
C_ 106,104,105
Mvvv 132,38,192
s_nop 0
Dv 30,%0,57344
Dv 34,v1,57344
Mvvv 132,42,212
C_ 104,84,85
C_ 105,86,87
Mvvv 128,38,188
E_ 108
Mvvv 128,42,208
E_ 109
Mvvv 124,38,184
E_ 110
Mvvv 124,42,204
E_ 111
Mvvv 120,38,180
E_ 88
Mvvv 120,42,200
E_ 89
Mvvv 116,38,176
E_ 90
Mvvv 116,42,196
W_ 0
E_ 91
Mvvv 116,30,216
s_nop 0
Dv 38,%0,49152
Dv 42,v1,49152
Mvvv 116,34,236
C_ 111,110,111
C_ 110,108,109
Mvvv 120,30,220
C_ 108,88,89
C_ 109,90,91
Mvvv 120,34,240
E_ 112
Mvvv 124,30,224
E_ 113
Mvvv 124,34,244
E_ 114
Mvvv 128,30,228
E_ 115
Mvvv 128,34,248
E_ 92
Mvvv 132,30,232
E_ 93
Mvvv 132,34,252
W_ 0
E_ 94
Nvvv 76,38,136,56
s_nop 0
Dv 30,%0,50176
Dv 34,v1,50176
Mvvv 76,42,156
E_ 95
Nvvv 80,38,140,60
C_ 115,114,115
C_ 114,112,113
Mvvv 80,42,160
C_ 112,92,93
C_ 113,94,95
Nvvv 84,38,144,64
E_ 116
Mvvv 84,42,164
E_ 117
Nvvv 88,38,148,68
E_ 118
Mvvv 88,42,168
E_ 119
Nvvv 92,38,152,72
C_ 119,118,119
C_ 118,116,117
Mvvv 92,42,172
W_ 0
E_ 120
Mvvv 92,30,192
s_nop 0
Dv 38,%0,51200
Dv 42,v1,51200
Mvvv 92,34,212
X_ a180,v11
X_ a181,v11
Mvvv 88,30,188
X_ a182,v11
X_ a183,v11
Mvvv 88,34,208
ds_write_b128 v12, a[132:135] offset:0
Mvvv 84,30,184
E_ 121
Mvvv 84,34,204
E_ 122
Mvvv 80,30,180
ds_write_b128 v12, a[136:139] offset:1024
Mvvv 80,34,200
E_ 123
Mvvv 76,30,176
C_ 123,122,123
C_ 122,120,121
Mvvv 76,34,196
ds_write_b128 v12, a[160:163] offset:2048
W_ 4
Mvvv 76,38,216
W_ 3
E_ 124
Mvvv 76,42,236
s_waitcnt vmcnt(12) lgkmcnt(0)
s_barrier
Mvvv 80,38,220
ds_write_b128 v12, a[164:167] offset:3072
Mvvv 80,42,240
E_ 125
Mvvv 84,38,224
E_ 126
Mvvv 84,42,244
s_nop 0
Tv 30,v2,24576
Tv 32,v2,30720
Mvvv 88,38,228
ds_write_b128 v12, a[168:171] offset:4096
Mvvv 88,42,248
s_nop 0
Tv 34,v3,24576
Tv 36,v3,30720
Mvvv 92,38,232
E_ 127
Mvvv 92,42,252
ds_write_b128 v12, a[172:175] offset:5120
Zaav 220,180,96
C_ 127,126,127
C_ 126,124,125
Zaav 224,180,100
E_ 128
Zaav 228,180,104
ds_write_b128 v12, a[176:179] offset:12288
Zaav 232,180,108
E_ 129
Zaav 236,180,112
W_ 2
E_ 130
Zavv 0,30,96
s_nop 0
Tv 38,v4,24576
Tv 40,v4,30720
Zavv 20,34,96
ds_write_b128 v12, a[200:203] offset:13312
Zavv 24,34,100
s_nop 0
Tv 42,v5,24576
Tv 44,v5,30720
Zavv 4,30,100
E_ 131
Zavv 8,30,104
ds_write_b128 v12, a[204:207] offset:14336
Zavv 28,34,104
R_ v46,a220
R_ v47,a224
Zavv 32,34,108
R_ v48,a228
R_ v49,a232
Zavv 12,30,108
R_ v50,a236
C_ 131,130,131
Zavv 16,30,112
ds_write_b128 v12, a[208:211] offset:15360
Zavv 36,34,112
W_ 2
C_ 130,128,129
Zavv 56,38,112
s_nop 0
Tv 30,v2,25600
Tv 32,v2,31744
Zavv 76,42,112
ds_write_b128 v12, a[212:215] offset:16384
Zavv 72,42,108
s_nop 0
Tv 34,v3,25600
Tv 36,v3,31744
Zavv 52,38,108
E_ 132
Zavv 48,38,104
ds_write_b128 v12, a[216:219] offset:17408
Zavv 68,42,104
E_ 133
Zavv 64,42,100
E_ 134
Zavv 44,38,100
E_ 135
Zavv 40,38,96
C_ 135,134,135
C_ 134,132,133
Zavv 60,42,96
W_ 1
E_ 76
Zavv 80,30,96
s_nop 0
Tv 38,v4,25600
Tv 40,v4,31744
Zavv 100,34,96
E_ 77
Zavv 104,34,100
s_nop 0
Tv 42,v5,25600
Tv 44,v5,31744
Zavv 84,30,100
E_ 78
Zavv 88,30,104
E_ 79
Zavv 108,34,104
C_ 116,76,77
C_ 117,78,79
Zavv 112,34,108
E_ 80
Zavv 92,30,108
E_ 81
Zavv 96,30,112
E_ 82
Zavv 116,34,112
W_ 0
E_ 83
Zavv 136,38,112
s_nop 0
Tv 30,v2,26624
Tv 32,v2,32768
Zavv 156,42,112
C_ 120,80,81
C_ 121,82,83
Zavv 152,42,108
s_nop 0
Tv 34,v3,26624
Tv 36,v3,32768
Zavv 132,38,108
E_ 84
Zavv 128,38,104
E_ 85
Zavv 148,42,104
E_ 86
Zavv 144,42,100
E_ 87
Zavv 124,38,100
C_ 124,84,85
C_ 125,86,87
Zavv 120,38,96
E_ 88
Zavv 140,42,96
W_ 0
E_ 89
Zavv 160,30,96
s_nop 0
Tv 38,v4,26624
Tv 40,v4,32768
Zavv 180,34,96
E_ 90
Zavv 184,34,100
s_nop 0
Tv 42,v5,26624
Tv 44,v5,32768
Zavv 164,30,100
E_ 91
Zavv 168,30,104
C_ 128,88,89
C_ 129,90,91
Zavv 188,34,104
S_ s65,s61,36864
E_ 92
Zavv 192,34,108
S_ m0,s51,24576
E_ 93
Zavv 172,30,108
s_waitcnt vmcnt(6) lgkmcnt(0)
s_barrier
Zavv 176,30,112
Ln v7,32,s65,0
Zavv 196,34,112
E_ 94
Zavv 216,38,112
Dv 30,%0,6144
Zavv 236,42,112
Dv 34,v1,6144
Zavv 232,42,108
Ln v8,32,s65,3072
S_ m0,s51,25472
Zavv 212,38,108
E_ 95
Zavv 208,38,104
Ln v7,32,s65,128
Zavv 228,42,104
C_ 132,92,93
C_ 133,94,95
Zavv 224,42,100
Ln v8,32,s65,3200
S_ m0,s51,26368
Zavv 204,38,100
v_mov_b32 v92, 0x3f803f80
v_mov_b32 v93, 0x3f803f80
Zavv 200,38,96
Ln v7,32,s65,256
Zavv 220,42,96
v_mov_b32 v94, 0x3f803f80
v_mov_b32 v95, 0x3f803f80
Ln v8,32,s65,3328
.Lstep1_2:
s_nop 0
Zvvv 76,92,116
Dv 38,%0,7168
Zvvv 80,92,120
Dv 42,v1,7168
Zvvv 84,92,124
Zvvv 88,92,128
Zvvv 92,92,132
W_ 2
Nvvv 96,30,136,56
Mvvv 96,34,156
Nvvv 100,30,140,60
s_nop 1
v_max3_f32 v10, v76, v80, v84
s_nop 0
v_max3_f32 v10, v10, v88, v92
Mvvv 100,34,160
v_cmp_lt_f32 vcc, 0x42000000, v10
s_cbranch_vccnz .Ltramp1_1
Nvvv 104,30,144,64
A_ v46,v46,v76
A_ v47,v47,v80
Mvvv 104,34,164
A_ v48,v48,v84
A_ v49,v49,v88
Nvvv 108,30,148,68
A_ v50,v50,v92
Mvvv 108,34,168
Nvvv 112,30,152,72
Mvvv 112,34,172
W_ 0
Mvvv 112,38,192
Dv 30,%0,8192
Mvvv 112,42,212
Dv 34,v1,8192
Mvvv 108,38,188
Mvvv 108,42,208
Mvvv 104,38,184
Mvvv 104,42,204
Mvvv 100,38,180
Mvvv 100,42,200
Mvvv 96,38,176
Mvvv 96,42,196
W_ 0
Mvvv 96,30,216
Dv 38,%0,0
Mvvv 96,34,236
Dv 42,v1,0
Mvvv 100,30,220
Mvvv 100,34,240
Mvvv 104,30,224
Mvvv 104,34,244
Mvvv 108,30,228
Mvvv 108,34,248
s_nop 0
E_ 96
Mvvv 112,30,232
E_ 97
Mvvv 112,34,252
W_ 0
E_ 98
Nvvv 76,38,136,56
Dv 30,%0,1024
Mvvv 76,42,156
Dv 34,v1,1024
Nvvv 80,38,140,60
E_ 99
Mvvv 80,42,160
C_ 99,98,99
C_ 98,96,97
Nvvv 84,38,144,64
E_ 100
Mvvv 84,42,164
E_ 101
Nvvv 88,38,148,68
E_ 102
Mvvv 88,42,168
E_ 103
Nvvv 92,38,152,72
C_ 103,102,103
C_ 102,100,101
Mvvv 92,42,172
W_ 0
E_ 104
Mvvv 92,30,192
Dv 38,%0,2048
Mvvv 92,34,212
Dv 42,v1,2048
Mvvv 88,30,188
E_ 105
Mvvv 88,34,208
E_ 106
Mvvv 84,30,184
S_ s65,s61,24576
E_ 107
Mvvv 84,34,204
S_ m0,s51,0
C_ 107,106,107
C_ 106,104,105
Mvvv 80,30,180
s_waitcnt vmcnt(6) lgkmcnt(0)
s_barrier
Mvvv 80,34,200
L_ v9,36,s65,0
Mvvv 76,30,176
E_ 108
Mvvv 76,34,196
L_ v9,36,s65,3072
S_ m0,s51,896
Mvvv 76,38,216
Tv 30,v2,12288
Tv 32,v2,18432
Mvvv 76,42,236
L_ v9,36,s65,128
Mvvv 80,38,220
s_nop 0
Tv 34,v3,12288
Tv 36,v3,18432
Mvvv 80,42,240
L_ v9,36,s65,3200
S_ m0,s51,1792
Mvvv 84,38,224
E_ 109
Mvvv 84,42,244
L_ v9,36,s65,256
Mvvv 88,38,228
E_ 110
Mvvv 88,42,248
L_ v9,36,s65,3328
Mvvv 92,38,232
E_ 111
Mvvv 92,42,252
W_ 0
C_ 111,110,111
C_ 110,108,109
Mavv 0,30,116
s_nop 0
Tv 38,v4,12288
Tv 40,v4,18432
Mavv 20,34,116
E_ 112
Mavv 24,34,120
s_nop 0
Tv 42,v5,12288
Tv 44,v5,18432
Mavv 4,30,120
E_ 113
Mavv 8,30,124
E_ 114
Mavv 28,34,124
E_ 115
Mavv 32,34,128
C_ 115,114,115
C_ 114,112,113
Mavv 12,30,128
E_ 76
Mavv 16,30,132
E_ 77
Mavv 36,34,132
W_ 0
E_ 78
Mavv 56,38,132
s_nop 0
Tv 30,v2,13312
Tv 32,v2,19456
Mavv 76,42,132
E_ 79
Mavv 72,42,128
s_nop 0
Tv 34,v3,13312
Tv 36,v3,19456
Mavv 52,38,128
C_ 96,76,77
C_ 97,78,79
Mavv 48,38,124
E_ 80
Mavv 68,42,124
E_ 81
Mavv 64,42,120
E_ 82
Mavv 44,38,120
E_ 83
Mavv 40,38,116
C_ 100,80,81
C_ 101,82,83
Mavv 60,42,116
W_ 0
E_ 84
Mavv 80,30,116
s_nop 0
Tv 38,v4,13312
Tv 40,v4,19456
Mavv 100,34,116
E_ 85
Mavv 104,34,120
s_nop 0
Tv 42,v5,13312
Tv 44,v5,19456
Mavv 84,30,120
E_ 86
Mavv 88,30,124
E_ 87
Mavv 108,34,124
C_ 104,84,85
C_ 105,86,87
Mavv 112,34,128
E_ 88
Mavv 92,30,128
E_ 89
Mavv 96,30,132
E_ 90
Mavv 116,34,132
W_ 0
E_ 91
Mavv 136,38,132
s_nop 0
Tv 30,v2,14336
Tv 32,v2,20480
Mavv 156,42,132
C_ 108,88,89
C_ 109,90,91
Mavv 152,42,128
s_nop 0
Tv 34,v3,14336
Tv 36,v3,20480
Mavv 132,38,128
E_ 92
Mavv 128,38,124
E_ 93
Mavv 148,42,124
E_ 94
Mavv 144,42,120
E_ 95
Mavv 124,38,120
C_ 112,92,93
C_ 113,94,95
Mavv 120,38,116
v_mov_b32 v92, 0x3f803f80
v_mov_b32 v93, 0x3f803f80
Mavv 140,42,116
W_ 0
v_mov_b32 v94, 0x3f803f80
v_mov_b32 v95, 0x3f803f80
Mavv 160,30,116
s_nop 0
Tv 38,v4,14336
Tv 40,v4,20480
Mavv 180,34,116
Mavv 184,34,120
s_nop 0
Tv 42,v5,14336
Tv 44,v5,20480
Mavv 164,30,120
Mavv 168,30,124
Mavv 188,34,124
S_ s65,s61,49152
Mavv 192,34,128
S_ m0,s51,12288
Mavv 172,30,128
s_waitcnt vmcnt(6) lgkmcnt(0)
s_barrier
Mavv 176,30,132
Ln v7,32,s65,0
Mavv 196,34,132
Mavv 216,38,132
Dv 30,%0,30720
Mavv 236,42,132
Dv 34,v1,30720
Mavv 232,42,128
Ln v8,32,s65,3072
S_ m0,s51,13184
Mavv 212,38,128
Mavv 208,38,124
Ln v7,32,s65,128
Mavv 228,42,124
Mavv 224,42,120
Ln v8,32,s65,3200
S_ m0,s51,14080
Mavv 204,38,120
Mavv 200,38,116
Ln v7,32,s65,256
Mavv 220,42,116
s_cmp_eq_u32 s4, 0
s_cbranch_scc1 .Lhs_a1
.Lhs_a1_back:
.Lstep1_3:
Zvvv 76,92,96
Dv 38,%0,31744
Zvvv 80,92,100
Dv 42,v1,31744
Zvvv 84,92,104
Zvvv 88,92,108
Zvvv 92,92,112
W_ 2
Nvvv 116,30,136,56
Mvvv 116,34,156
Nvvv 120,30,140,60
s_nop 1
v_max3_f32 v10, v76, v80, v84
s_nop 0
v_max3_f32 v10, v10, v88, v92
Mvvv 120,34,160
v_cmp_lt_f32 vcc, 0x42000000, v10
s_cbranch_vccnz .Ltramp1_2
Nvvv 124,30,144,64
A_ v46,v46,v76
A_ v47,v47,v80
Mvvv 124,34,164
A_ v48,v48,v84
A_ v49,v49,v88
Nvvv 128,30,148,68
A_ v50,v50,v92
Mvvv 128,34,168
Ln v8,32,s65,3328
Nvvv 132,30,152,72
Mvvv 132,34,172
W_ 0
Mvvv 132,38,192
Dv 30,%0,32768
Mvvv 132,42,212
Dv 34,v1,32768
Mvvv 128,38,188
Mvvv 128,42,208
Mvvv 124,38,184
Mvvv 124,42,204
Mvvv 120,38,180
Mvvv 120,42,200
Mvvv 116,38,176
Mvvv 116,42,196
W_ 0
Mvvv 116,30,216
Dv 38,%0,24576
Mvvv 116,34,236
Dv 42,v1,24576
Mvvv 120,30,220
Mvvv 120,34,240
Mvvv 124,30,224
Mvvv 124,34,244
Mvvv 128,30,228
Mvvv 128,34,248
s_nop 0
E_ 116
Mvvv 132,30,232
E_ 117
Mvvv 132,34,252
W_ 0
E_ 118
Nvvv 76,38,136,56
Dv 30,%0,25600
Mvvv 76,42,156
Dv 34,v1,25600
Nvvv 80,38,140,60
E_ 119
Mvvv 80,42,160
C_ 119,118,119
C_ 118,116,117
Nvvv 84,38,144,64
E_ 120
Mvvv 84,42,164
E_ 121
Nvvv 88,38,148,68
E_ 122
Mvvv 88,42,168
E_ 123
Nvvv 92,38,152,72
C_ 123,122,123
C_ 122,120,121
Mvvv 92,42,172
W_ 0
E_ 124
Mvvv 92,30,192
Dv 38,%0,26624
Mvvv 92,34,212
Dv 42,v1,26624
Mvvv 88,30,188
E_ 125
Mvvv 88,34,208
E_ 126
Mvvv 84,30,184
S_ s65,s61,36864
E_ 127
Mvvv 84,34,204
S_ m0,s51,24576
C_ 127,126,127
C_ 126,124,125
Mvvv 80,30,180
s_waitcnt vmcnt(6) lgkmcnt(0)
s_barrier
Mvvv 80,34,200
L_ v9,36,s65,0
Mvvv 76,30,176
E_ 128
Mvvv 76,34,196
L_ v9,36,s65,3072
S_ m0,s51,25472
Mvvv 76,38,216
Tv 30,v2,0
Tv 32,v2,6144
Mvvv 76,42,236
L_ v9,36,s65,128
Mvvv 80,38,220
s_nop 0
Tv 34,v3,0
Tv 36,v3,6144
Mvvv 80,42,240
L_ v9,36,s65,3200
S_ m0,s51,26368
Mvvv 84,38,224
E_ 129
Mvvv 84,42,244
L_ v9,36,s65,256
Mvvv 88,38,228
E_ 130
Mvvv 88,42,248
L_ v9,36,s65,3328
Mvvv 92,38,232
E_ 131
Mvvv 92,42,252
W_ 0
C_ 131,130,131
C_ 130,128,129
Mavv 0,30,96
s_nop 0
Tv 38,v4,0
Tv 40,v4,6144
Mavv 20,34,96
E_ 132
Mavv 24,34,100
s_nop 0
Tv 42,v5,0
Tv 44,v5,6144
Mavv 4,30,100
E_ 133
Mavv 8,30,104
E_ 134
Mavv 28,34,104
E_ 135
Mavv 32,34,108
C_ 135,134,135
C_ 134,132,133
Mavv 12,30,108
E_ 76
Mavv 16,30,112
E_ 77
Mavv 36,34,112
W_ 0
E_ 78
Mavv 56,38,112
s_nop 0
Tv 30,v2,1024
Tv 32,v2,7168
Mavv 76,42,112
E_ 79
Mavv 72,42,108
s_nop 0
Tv 34,v3,1024
Tv 36,v3,7168
Mavv 52,38,108
C_ 116,76,77
C_ 117,78,79
Mavv 48,38,104
E_ 80
Mavv 68,42,104
E_ 81
Mavv 64,42,100
E_ 82
Mavv 44,38,100
E_ 83
Mavv 40,38,96
C_ 120,80,81
C_ 121,82,83
Mavv 60,42,96
W_ 0
E_ 84
Mavv 80,30,96
s_nop 0
Tv 38,v4,1024
Tv 40,v4,7168
Mavv 100,34,96
E_ 85
Mavv 104,34,100
s_nop 0
Tv 42,v5,1024
Tv 44,v5,7168
Mavv 84,30,100
E_ 86
Mavv 88,30,104
E_ 87
Mavv 108,34,104
C_ 124,84,85
C_ 125,86,87
Mavv 112,34,108
E_ 88
Mavv 92,30,108
E_ 89
Mavv 96,30,112
E_ 90
Mavv 116,34,112
W_ 0
E_ 91
Mavv 136,38,112
s_nop 0
Tv 30,v2,2048
Tv 32,v2,8192
Mavv 156,42,112
C_ 128,88,89
C_ 129,90,91
Mavv 152,42,108
s_nop 0
Tv 34,v3,2048
Tv 36,v3,8192
Mavv 132,38,108
E_ 92
Mavv 128,38,104
E_ 93
Mavv 148,42,104
E_ 94
Mavv 144,42,100
E_ 95
Mavv 124,38,100
C_ 132,92,93
C_ 133,94,95
Mavv 120,38,96
v_mov_b32 v92, 0x3f803f80
v_mov_b32 v93, 0x3f803f80
Mavv 140,42,96
W_ 0
v_mov_b32 v94, 0x3f803f80
v_mov_b32 v95, 0x3f803f80
Mavv 160,30,96
s_nop 0
Tv 38,v4,2048
Tv 40,v4,8192
Mavv 180,34,96
Mavv 184,34,100
s_nop 0
Tv 42,v5,2048
Tv 44,v5,8192
Mavv 164,30,100
Mavv 168,30,104
Mavv 188,34,104
S_ s65,s61,61440
Mavv 192,34,108
S_ m0,s51,0
Mavv 172,30,108
s_waitcnt vmcnt(6) lgkmcnt(0)
s_barrier
Mavv 176,30,112
Ln v7,32,s65,0
Mavv 196,34,112
Mavv 216,38,112
Dv 30,%0,18432
Mavv 236,42,112
Dv 34,v1,18432
Mavv 232,42,108
Ln v8,32,s65,3072
S_ m0,s51,896
Mavv 212,38,108
Mavv 208,38,104
Ln v7,32,s65,128
Mavv 228,42,104
Mavv 224,42,100
Ln v8,32,s65,3200
S_ m0,s51,1792
Mavv 204,38,100
Mavv 200,38,96
Ln v7,32,s65,256
Mavv 220,42,96
.Lstep1_4:
Zvvv 76,92,116
Dv 38,%0,19456
Zvvv 80,92,120
Dv 42,v1,19456
Zvvv 84,92,124
Zvvv 88,92,128
Zvvv 92,92,132
W_ 2
Nvvv 96,30,136,56
Mvvv 96,34,156
Nvvv 100,30,140,60
s_nop 1
v_max3_f32 v10, v76, v80, v84
s_nop 0
v_max3_f32 v10, v10, v88, v92
Mvvv 100,34,160
v_cmp_lt_f32 vcc, 0x42000000, v10
s_cbranch_vccnz .Ltramp1_3
Nvvv 104,30,144,64
A_ v46,v46,v76
A_ v47,v47,v80
Mvvv 104,34,164
A_ v48,v48,v84
A_ v49,v49,v88
Nvvv 108,30,148,68
A_ v50,v50,v92
Mvvv 108,34,168
Ln v8,32,s65,3328
Nvvv 112,30,152,72
Mvvv 112,34,172
W_ 0
Mvvv 112,38,192
Dv 30,%0,20480
Mvvv 112,42,212
Dv 34,v1,20480
Mvvv 108,38,188
Mvvv 108,42,208
Mvvv 104,38,184
Mvvv 104,42,204
Mvvv 100,38,180
Mvvv 100,42,200
Mvvv 96,38,176
Mvvv 96,42,196
W_ 0
Mvvv 96,30,216
Dv 38,%0,12288
Mvvv 96,34,236
Dv 42,v1,12288
Mvvv 100,30,220
Mvvv 100,34,240
Mvvv 104,30,224
Mvvv 104,34,244
Mvvv 108,30,228
Mvvv 108,34,248
s_nop 0
E_ 96
Mvvv 112,30,232
E_ 97
Mvvv 112,34,252
W_ 0
E_ 98
Nvvv 76,38,136,56
Dv 30,%0,13312
Mvvv 76,42,156
Dv 34,v1,13312
Nvvv 80,38,140,60
E_ 99
Mvvv 80,42,160
C_ 99,98,99
C_ 98,96,97
Nvvv 84,38,144,64
E_ 100
Mvvv 84,42,164
E_ 101
Nvvv 88,38,148,68
E_ 102
Mvvv 88,42,168
E_ 103
Nvvv 92,38,152,72
C_ 103,102,103
C_ 102,100,101
Mvvv 92,42,172
W_ 0
E_ 104
Mvvv 92,30,192
Dv 38,%0,14336
Mvvv 92,34,212
Dv 42,v1,14336
Mvvv 88,30,188
E_ 105
Mvvv 88,34,208
E_ 106
Mvvv 84,30,184
S_ s65,s61,49152
E_ 107
Mvvv 84,34,204
S_ m0,s51,12288
C_ 107,106,107
C_ 106,104,105
Mvvv 80,30,180
s_waitcnt vmcnt(6) lgkmcnt(0)
s_barrier
Mvvv 80,34,200
L_ v9,36,s65,0
Mvvv 76,30,176
E_ 108
Mvvv 76,34,196
L_ v9,36,s65,3072
S_ m0,s51,13184
Mvvv 76,38,216
Tv 30,v2,24576
Tv 32,v2,30720
Mvvv 76,42,236
L_ v9,36,s65,128
Mvvv 80,38,220
s_nop 0
Tv 34,v3,24576
Tv 36,v3,30720
Mvvv 80,42,240
L_ v9,36,s65,3200
S_ m0,s51,14080
Mvvv 84,38,224
E_ 109
Mvvv 84,42,244
L_ v9,36,s65,256
Mvvv 88,38,228
E_ 110
Mvvv 88,42,248
L_ v9,36,s65,3328
Mvvv 92,38,232
E_ 111
Mvvv 92,42,252
W_ 0
C_ 111,110,111
C_ 110,108,109
Mavv 0,30,116
s_nop 0
Tv 38,v4,24576
Tv 40,v4,30720
Mavv 20,34,116
E_ 112
Mavv 24,34,120
s_nop 0
Tv 42,v5,24576
Tv 44,v5,30720
Mavv 4,30,120
E_ 113
Mavv 8,30,124
E_ 114
Mavv 28,34,124
E_ 115
Mavv 32,34,128
C_ 115,114,115
C_ 114,112,113
Mavv 12,30,128
E_ 76
Mavv 16,30,132
E_ 77
Mavv 36,34,132
W_ 0
E_ 78
Mavv 56,38,132
s_nop 0
Tv 30,v2,25600
Tv 32,v2,31744
Mavv 76,42,132
E_ 79
Mavv 72,42,128
s_nop 0
Tv 34,v3,25600
Tv 36,v3,31744
Mavv 52,38,128
C_ 96,76,77
C_ 97,78,79
Mavv 48,38,124
E_ 80
Mavv 68,42,124
E_ 81
Mavv 64,42,120
E_ 82
Mavv 44,38,120
E_ 83
Mavv 40,38,116
C_ 100,80,81
C_ 101,82,83
Mavv 60,42,116
W_ 0
E_ 84
Mavv 80,30,116
s_nop 0
Tv 38,v4,25600
Tv 40,v4,31744
Mavv 100,34,116
E_ 85
Mavv 104,34,120
s_nop 0
Tv 42,v5,25600
Tv 44,v5,31744
Mavv 84,30,120
E_ 86
Mavv 88,30,124
E_ 87
Mavv 108,34,124
C_ 104,84,85
C_ 105,86,87
Mavv 112,34,128
E_ 88
Mavv 92,30,128
E_ 89
Mavv 96,30,132
E_ 90
Mavv 116,34,132
W_ 0
E_ 91
Mavv 136,38,132
s_nop 0
Tv 30,v2,26624
Tv 32,v2,32768
Mavv 156,42,132
C_ 108,88,89
C_ 109,90,91
Mavv 152,42,128
s_nop 0
Tv 34,v3,26624
Tv 36,v3,32768
Mavv 132,38,128
E_ 92
Mavv 128,38,124
E_ 93
Mavv 148,42,124
E_ 94
Mavv 144,42,120
E_ 95
Mavv 124,38,120
C_ 112,92,93
C_ 113,94,95
Mavv 120,38,116
v_mov_b32 v92, 0x3f803f80
v_mov_b32 v93, 0x3f803f80
Mavv 140,42,116
W_ 0
v_mov_b32 v94, 0x3f803f80
v_mov_b32 v95, 0x3f803f80
Mavv 160,30,116
s_nop 0
Tv 38,v4,26624
Tv 40,v4,32768
Mavv 180,34,116
Mavv 184,34,120
s_nop 0
Tv 42,v5,26624
Tv 44,v5,32768
Mavv 164,30,120
Mavv 168,30,124
Mavv 188,34,124
S_ s65,s61,73728
Mavv 192,34,128
S_ m0,s51,24576
Mavv 172,30,128
s_waitcnt vmcnt(6) lgkmcnt(0)
s_barrier
Mavv 176,30,132
Ln v7,32,s65,0
Mavv 196,34,132
Mavv 216,38,132
Dv 30,%0,6144
Mavv 236,42,132
Dv 34,v1,6144
Mavv 232,42,128
Ln v8,32,s65,3072
S_ m0,s51,25472
Mavv 212,38,128
Mavv 208,38,124
Ln v7,32,s65,128
Mavv 228,42,124
Mavv 224,42,120
Ln v8,32,s65,3200
S_ m0,s51,26368
Mavv 204,38,120
Mavv 200,38,116
Ln v7,32,s65,256
Mavv 220,42,116
s_cmp_eq_u32 s4, 0
s_cbranch_scc1 .Lhs_b1
.Lhs_b1_back:
.Lstep1_5:
Zvvv 76,92,96
Dv 38,%0,7168
Zvvv 80,92,100
Dv 42,v1,7168
Zvvv 84,92,104
Zvvv 88,92,108
Zvvv 92,92,112
W_ 2
Nvvv 116,30,136,56
Mvvv 116,34,156
Nvvv 120,30,140,60
s_nop 1
v_max3_f32 v10, v76, v80, v84
s_nop 0
v_max3_f32 v10, v10, v88, v92
Mvvv 120,34,160
v_cmp_lt_f32 vcc, 0x42000000, v10
s_cbranch_vccnz .Ltramp1_4
Nvvv 124,30,144,64
A_ v46,v46,v76
A_ v47,v47,v80
Mvvv 124,34,164
A_ v48,v48,v84
A_ v49,v49,v88
Nvvv 128,30,148,68
A_ v50,v50,v92
Mvvv 128,34,168
Ln v8,32,s65,3328
Nvvv 132,30,152,72
Mvvv 132,34,172
W_ 0
Mvvv 132,38,192
Dv 30,%0,8192
Mvvv 132,42,212
Dv 34,v1,8192
Mvvv 128,38,188
Mvvv 128,42,208
Mvvv 124,38,184
Mvvv 124,42,204
Mvvv 120,38,180
Mvvv 120,42,200
Mvvv 116,38,176
Mvvv 116,42,196
W_ 0
Mvvv 116,30,216
Dv 38,%0,0
Mvvv 116,34,236
Dv 42,v1,0
Mvvv 120,30,220
Mvvv 120,34,240
Mvvv 124,30,224
Mvvv 124,34,244
Mvvv 128,30,228
Mvvv 128,34,248
s_nop 0
E_ 116
Mvvv 132,30,232
E_ 117
Mvvv 132,34,252
W_ 0
E_ 118
Nvvv 76,38,136,56
Dv 30,%0,1024
Mvvv 76,42,156
Dv 34,v1,1024
Nvvv 80,38,140,60
E_ 119
Mvvv 80,42,160
C_ 119,118,119
C_ 118,116,117
Nvvv 84,38,144,64
E_ 120
Mvvv 84,42,164
E_ 121
Nvvv 88,38,148,68
E_ 122
Mvvv 88,42,168
E_ 123
Nvvv 92,38,152,72
C_ 123,122,123
C_ 122,120,121
Mvvv 92,42,172
W_ 0
E_ 124
Mvvv 92,30,192
Dv 38,%0,2048
Mvvv 92,34,212
Dv 42,v1,2048
Mvvv 88,30,188
E_ 125
Mvvv 88,34,208
E_ 126
Mvvv 84,30,184
S_ s65,s61,61440
E_ 127
Mvvv 84,34,204
S_ m0,s51,0
C_ 127,126,127
C_ 126,124,125
Mvvv 80,30,180
s_waitcnt vmcnt(6) lgkmcnt(0)
s_barrier
Mvvv 80,34,200
L_ v9,36,s65,0
Mvvv 76,30,176
E_ 128
Mvvv 76,34,196
L_ v9,36,s65,3072
S_ m0,s51,896
Mvvv 76,38,216
Tv 30,v2,12288
Tv 32,v2,18432
Mvvv 76,42,236
L_ v9,36,s65,128
Mvvv 80,38,220
s_nop 0
Tv 34,v3,12288
Tv 36,v3,18432
Mvvv 80,42,240
L_ v9,36,s65,3200
S_ m0,s51,1792
Mvvv 84,38,224
E_ 129
Mvvv 84,42,244
L_ v9,36,s65,256
Mvvv 88,38,228
E_ 130
Mvvv 88,42,248
L_ v9,36,s65,3328
Mvvv 92,38,232
E_ 131
Mvvv 92,42,252
W_ 0
C_ 131,130,131
C_ 130,128,129
Mavv 0,30,96
s_nop 0
Tv 38,v4,12288
Tv 40,v4,18432
Mavv 20,34,96
E_ 132
Mavv 24,34,100
s_nop 0
Tv 42,v5,12288
Tv 44,v5,18432
Mavv 4,30,100
E_ 133
Mavv 8,30,104
E_ 134
Mavv 28,34,104
E_ 135
Mavv 32,34,108
C_ 135,134,135
C_ 134,132,133
Mavv 12,30,108
E_ 76
Mavv 16,30,112
E_ 77
Mavv 36,34,112
W_ 0
E_ 78
Mavv 56,38,112
s_nop 0
Tv 30,v2,13312
Tv 32,v2,19456
Mavv 76,42,112
E_ 79
Mavv 72,42,108
s_nop 0
Tv 34,v3,13312
Tv 36,v3,19456
Mavv 52,38,108
C_ 116,76,77
C_ 117,78,79
Mavv 48,38,104
E_ 80
Mavv 68,42,104
E_ 81
Mavv 64,42,100
E_ 82
Mavv 44,38,100
E_ 83
Mavv 40,38,96
C_ 120,80,81
C_ 121,82,83
Mavv 60,42,96
W_ 0
E_ 84
Mavv 80,30,96
s_nop 0
Tv 38,v4,13312
Tv 40,v4,19456
Mavv 100,34,96
E_ 85
Mavv 104,34,100
s_nop 0
Tv 42,v5,13312
Tv 44,v5,19456
Mavv 84,30,100
E_ 86
Mavv 88,30,104
E_ 87
Mavv 108,34,104
C_ 124,84,85
C_ 125,86,87
Mavv 112,34,108
E_ 88
Mavv 92,30,108
E_ 89
Mavv 96,30,112
E_ 90
Mavv 116,34,112
W_ 0
E_ 91
Mavv 136,38,112
s_nop 0
Tv 30,v2,14336
Tv 32,v2,20480
Mavv 156,42,112
C_ 128,88,89
C_ 129,90,91
Mavv 152,42,108
s_nop 0
Tv 34,v3,14336
Tv 36,v3,20480
Mavv 132,38,108
E_ 92
Mavv 128,38,104
E_ 93
Mavv 148,42,104
E_ 94
Mavv 144,42,100
E_ 95
Mavv 124,38,100
C_ 132,92,93
C_ 133,94,95
Mavv 120,38,96
v_mov_b32 v92, 0x3f803f80
v_mov_b32 v93, 0x3f803f80
Mavv 140,42,96
W_ 0
v_mov_b32 v94, 0x3f803f80
v_mov_b32 v95, 0x3f803f80
Mavv 160,30,96
s_nop 0
Tv 38,v4,14336
Tv 40,v4,20480
Mavv 180,34,96
Mavv 184,34,100
s_nop 0
Tv 42,v5,14336
Tv 44,v5,20480
Mavv 164,30,100
Mavv 168,30,104
Mavv 188,34,104
S_ s65,s61,86016
Mavv 192,34,108
S_ m0,s51,12288
Mavv 172,30,108
s_waitcnt vmcnt(6) lgkmcnt(0)
s_barrier
Mavv 176,30,112
Ln v7,32,s65,0
Mavv 196,34,112
Mavv 216,38,112
Dv 30,%0,30720
Mavv 236,42,112
Dv 34,v1,30720
Mavv 232,42,108
Ln v8,32,s65,3072
S_ m0,s51,13184
Mavv 212,38,108
Mavv 208,38,104
Ln v7,32,s65,128
Mavv 228,42,104
Mavv 224,42,100
Ln v8,32,s65,3200
S_ m0,s51,14080
Mavv 204,38,100
Mavv 200,38,96
Ln v7,32,s65,256
Mavv 220,42,96
.Lstep1_6:
Zvvv 76,92,116
Dv 38,%0,31744
Zvvv 80,92,120
Dv 42,v1,31744
Zvvv 84,92,124
Zvvv 88,92,128
Zvvv 92,92,132
W_ 2
Nvvv 96,30,136,56
Mvvv 96,34,156
Nvvv 100,30,140,60
s_nop 1
v_max3_f32 v10, v76, v80, v84
s_nop 0
v_max3_f32 v10, v10, v88, v92
Mvvv 100,34,160
v_cmp_lt_f32 vcc, 0x42000000, v10
s_cbranch_vccnz .Ltramp1_5
Nvvv 104,30,144,64
A_ v46,v46,v76
A_ v47,v47,v80
Mvvv 104,34,164
A_ v48,v48,v84
A_ v49,v49,v88
Nvvv 108,30,148,68
A_ v50,v50,v92
Mvvv 108,34,168
Ln v8,32,s65,3328
Nvvv 112,30,152,72
R_ v11,a248
Mvvv 112,34,172
W_ 0
v_lshlrev_b32 v11, 10, v11
Mvvv 112,38,192
Dv 30,%0,32768
Mvvv 112,42,212
Dv 34,v1,32768
Mvvv 108,38,188
buffer_load_dword a254, v11, s[44:47], s66 offen
Mvvv 108,42,208
Mvvv 104,38,184
Mvvv 104,42,204
Mvvv 100,38,180
Mvvv 100,42,200
Mvvv 96,38,176
Mvvv 96,42,196
W_ 0
Mvvv 96,30,216
Dv 38,%0,24576
Mvvv 96,34,236
Dv 42,v1,24576
Mvvv 100,30,220
Mvvv 100,34,240
)A"
R"A(Mvvv 104,30,224
Mvvv 104,34,244
Mvvv 108,30,228
Mvvv 108,34,248
s_nop 0
E_ 96
Mvvv 112,30,232
E_ 97
Mvvv 112,34,252
W_ 0
E_ 98
Nvvv 76,38,136,56
Dv 30,%0,25600
Mvvv 76,42,156
Dv 34,v1,25600
Nvvv 80,38,140,60
E_ 99
Mvvv 80,42,160
C_ 99,98,99
C_ 98,96,97
Nvvv 84,38,144,64
E_ 100
Mvvv 84,42,164
E_ 101
Nvvv 88,38,148,68
E_ 102
Mvvv 88,42,168
E_ 103
Nvvv 92,38,152,72
C_ 103,102,103
C_ 102,100,101
Mvvv 92,42,172
W_ 0
E_ 104
Mvvv 92,30,192
Dv 38,%0,26624
Mvvv 92,34,212
Dv 42,v1,26624
Mvvv 88,30,188
E_ 105
Mvvv 88,34,208
E_ 106
Mvvv 84,30,184
S_ s65,s61,73728
E_ 107
Mvvv 84,34,204
S_ m0,s51,24576
C_ 107,106,107
C_ 106,104,105
Mvvv 80,30,180
s_waitcnt vmcnt(7) lgkmcnt(0)
s_barrier
Mvvv 80,34,200
L_ v9,36,s65,0
Mvvv 76,30,176
E_ 108
Mvvv 76,34,196
L_ v9,36,s65,3072
S_ m0,s51,25472
Mvvv 76,38,216
Tv 30,v2,0
Tv 32,v2,6144
Mvvv 76,42,236
L_ v9,36,s65,128
Mvvv 80,38,220
s_nop 0
Tv 34,v3,0
Tv 36,v3,6144
Mvvv 80,42,240
L_ v9,36,s65,3200
S_ m0,s51,26368
Mvvv 84,38,224
E_ 109
Mvvv 84,42,244
L_ v9,36,s65,256
Mvvv 88,38,228
E_ 110
Mvvv 88,42,248
L_ v9,36,s65,3328
Mvvv 92,38,232
E_ 111
Mvvv 92,42,252
W_ 0
C_ 111,110,111
C_ 110,108,109
Mavv 0,30,116
s_nop 0
Tv 38,v4,0
Tv 40,v4,6144
Mavv 20,34,116
E_ 112
Mavv 24,34,120
s_nop 0
Tv 42,v5,0
Tv 44,v5,6144
Mavv 4,30,120
E_ 113
Mavv 8,30,124
E_ 114
Mavv 28,34,124
E_ 115
Mavv 32,34,128
C_ 115,114,115
C_ 114,112,113
Mavv 12,30,128
E_ 76
Mavv 16,30,132
E_ 77
Mavv 36,34,132
W_ 0
E_ 78
Mavv 56,38,132
s_nop 0
Tv 30,v2,1024
Tv 32,v2,7168
Mavv 76,42,132
E_ 79
Mavv 72,42,128
s_nop 0
Tv 34,v3,1024
Tv 36,v3,7168
Mavv 52,38,128
C_ 96,76,77
C_ 97,78,79
Mavv 48,38,124
E_ 80
Mavv 68,42,124
E_ 81
Mavv 64,42,120
E_ 82
Mavv 44,38,120
E_ 83
Mavv 40,38,116
C_ 100,80,81
C_ 101,82,83
Mavv 60,42,116
W_ 0
E_ 84
Mavv 80,30,116
s_nop 0
Tv 38,v4,1024
Tv 40,v4,7168
Mavv 100,34,116
E_ 85
Mavv 104,34,120
s_nop 0
Tv 42,v5,1024
Tv 44,v5,7168
Mavv 84,30,120
E_ 86
Mavv 88,30,124
E_ 87
Mavv 108,34,124
C_ 104,84,85
C_ 105,86,87
Mavv 112,34,128
E_ 88
Mavv 92,30,128
E_ 89
Mavv 96,30,132
E_ 90
Mavv 116,34,132
W_ 0
E_ 91
Mavv 136,38,132
s_nop 0
Tv 30,v2,2048
Tv 32,v2,8192
Mavv 156,42,132
C_ 108,88,89
C_ 109,90,91
Mavv 152,42,128
s_nop 0
Tv 34,v3,2048
Tv 36,v3,8192
Mavv 132,38,128
E_ 92
Mavv 128,38,124
E_ 93
Mavv 148,42,124
E_ 94
Mavv 144,42,120
E_ 95
Mavv 124,38,120
C_ 112,92,93
C_ 113,94,95
Mavv 120,38,116
v_mov_b32 v92, 0x3f803f80
v_mov_b32 v93, 0x3f803f80
Mavv 140,42,116
W_ 0
v_mov_b32 v94, 0x3f803f80
v_mov_b32 v95, 0x3f803f80
Mavv 160,30,116
s_nop 0
Tv 38,v4,2048
Tv 40,v4,8192
Mavv 180,34,116
Mavv 184,34,120
s_nop 0
Tv 42,v5,2048
Tv 44,v5,8192
Mavv 164,30,120
Mavv 168,30,124
Mavv 188,34,124
S_ s65,s61,98304
Mavv 192,34,128
S_ m0,s51,0
Mavv 172,30,128
s_waitcnt vmcnt(7) lgkmcnt(0)
s_barrier
Mavv 176,30,132
Ln v7,32,s65,0
Mavv 196,34,132
Mavv 216,38,132
Dv 30,%0,18432
Mavv 236,42,132
Dv 34,v1,18432
Mavv 232,42,128
Ln v8,32,s65,3072
S_ m0,s51,896
Mavv 212,38,128
Mavv 208,38,124
Ln v7,32,s65,128
Mavv 228,42,124
Mavv 224,42,120
Ln v8,32,s65,3200
S_ m0,s51,1792
Mavv 204,38,120
Mavv 200,38,116
Ln v7,32,s65,256
Mavv 220,42,116
s_cmp_eq_u32 s4, 0
s_cbranch_scc1 .Lhs_c1
.Lhs_c1_back:
.Lstep1_7:
Zvvv 76,92,96
Dv 38,%0,19456
Zvvv 80,92,100
Dv 42,v1,19456
Zvvv 84,92,104
Zvvv 88,92,108
Zvvv 92,92,112
W_ 2
Nvvv 116,30,136,56
Mvvv 116,34,156
Nvvv 120,30,140,60
s_nop 1
v_max3_f32 v10, v76, v80, v84
s_nop 0
v_max3_f32 v10, v10, v88, v92
Mvvv 120,34,160
v_cmp_lt_f32 vcc, 0x42000000, v10
s_cbranch_vccnz .Ltramp1_6
Nvvv 124,30,144,64
A_ v46,v46,v76
A_ v47,v47,v80
Mvvv 124,34,164
A_ v48,v48,v84
A_ v49,v49,v88
Nvvv 128,30,148,68
A_ v50,v50,v92
Mvvv 128,34,168
Ln v8,32,s65,3328
Nvvv 132,30,152,72
Mvvv 132,34,172
W_ 0
Mvvv 132,38,192
Dv 30,%0,20480
Mvvv 132,42,212
Dv 34,v1,20480
Mvvv 128,38,188
Mvvv 128,42,208
Mvvv 124,38,184
Mvvv 124,42,204
Mvvv 120,38,180
Mvvv 120,42,200
Mvvv 116,38,176
Mvvv 116,42,196
W_ 0
Mvvv 116,30,216
Dv 38,%0,12288
Mvvv 116,34,236
Dv 42,v1,12288
Mvvv 120,30,220
Mvvv 120,34,240
Mvvv 124,30,224
Mvvv 124,34,244
Mvvv 128,30,228
Mvvv 128,34,248
s_nop 0
E_ 116
Mvvv 132,30,232
E_ 117
Mvvv 132,34,252
W_ 0
E_ 118
Nvvv 76,38,136,56
Dv 30,%0,13312
Mvvv 76,42,156
Dv 34,v1,13312
Nvvv 80,38,140,60
E_ 119
Mvvv 80,42,160
C_ 119,118,119
C_ 118,116,117
Nvvv 84,38,144,64
E_ 120
Mvvv 84,42,164
E_ 121
Nvvv 88,38,148,68
E_ 122
Mvvv 88,42,168
E_ 123
Nvvv 92,38,152,72
C_ 123,122,123
C_ 122,120,121
Mvvv 92,42,172
W_ 0
E_ 124
Mvvv 92,30,192
Dv 38,%0,14336
Mvvv 92,34,212
Dv 42,v1,14336
Mvvv 88,30,188
E_ 125
Mvvv 88,34,208
E_ 126
Mvvv 84,30,184
S_ s65,s61,86016
E_ 127
Mvvv 84,34,204
S_ m0,s51,12288
C_ 127,126,127
C_ 126,124,125
Mvvv 80,30,180
s_waitcnt vmcnt(6) lgkmcnt(0)
s_barrier
Mvvv 80,34,200
L_ v9,36,s65,0
Mvvv 76,30,176
E_ 128
Mvvv 76,34,196
L_ v9,36,s65,3072
S_ m0,s51,13184
Mvvv 76,38,216
Tv 30,v2,24576
Tv 32,v2,30720
Mvvv 76,42,236
L_ v9,36,s65,128
Mvvv 80,38,220
s_nop 0
Tv 34,v3,24576
Tv 36,v3,30720
Mvvv 80,42,240
L_ v9,36,s65,3200
S_ m0,s51,14080
Mvvv 84,38,224
E_ 129
Mvvv 84,42,244
L_ v9,36,s65,256
Mvvv 88,38,228
E_ 130
Mvvv 88,42,248
L_ v9,36,s65,3328
Mvvv 92,38,232
E_ 131
Mvvv 92,42,252
W_ 0
C_ 131,130,131
C_ 130,128,129
Mavv 0,30,96
s_nop 0
Tv 38,v4,24576
Tv 40,v4,30720
Mavv 20,34,96
E_ 132
Mavv 24,34,100
s_nop 0
Tv 42,v5,24576
Tv 44,v5,30720
Mavv 4,30,100
E_ 133
Mavv 8,30,104
E_ 134
Mavv 28,34,104
E_ 135
Mavv 32,34,108
C_ 135,134,135
C_ 134,132,133
Mavv 12,30,108
E_ 76
Mavv 16,30,112
E_ 77
Mavv 36,34,112
W_ 0
E_ 78
Mavv 56,38,112
s_nop 0
Tv 30,v2,25600
Tv 32,v2,31744
Mavv 76,42,112
E_ 79
Mavv 72,42,108
s_nop 0
Tv 34,v3,25600
Tv 36,v3,31744
Mavv 52,38,108
C_ 116,76,77
C_ 117,78,79
Mavv 48,38,104
E_ 80
Mavv 68,42,104
E_ 81
Mavv 64,42,100
E_ 82
Mavv 44,38,100
E_ 83
Mavv 40,38,96
C_ 120,80,81
C_ 121,82,83
Mavv 60,42,96
W_ 0
E_ 84
Mavv 80,30,96
s_nop 0
Tv 38,v4,25600
Tv 40,v4,31744
Mavv 100,34,96
E_ 85
Mavv 104,34,100
s_nop 0
Tv 42,v5,25600
Tv 44,v5,31744
Mavv 84,30,100
E_ 86
Mavv 88,30,104
E_ 87
Mavv 108,34,104
C_ 124,84,85
C_ 125,86,87
Mavv 112,34,108
E_ 88
Mavv 92,30,108
E_ 89
Mavv 96,30,112
E_ 90
Mavv 116,34,112
W_ 0
E_ 91
Mavv 136,38,112
s_nop 0
Tv 30,v2,26624
Tv 32,v2,32768
Mavv 156,42,112
C_ 128,88,89
C_ 129,90,91
Mavv 152,42,108
s_nop 0
Tv 34,v3,26624
Tv 36,v3,32768
Mavv 132,38,108
E_ 92
Mavv 128,38,104
E_ 93
Mavv 148,42,104
E_ 94
Mavv 144,42,100
E_ 95
Mavv 124,38,100
C_ 132,92,93
C_ 133,94,95
Mavv 120,38,96
v_mov_b32 v92, 0x3f803f80
v_mov_b32 v93, 0x3f803f80
Mavv 140,42,96
W_ 0
v_mov_b32 v94, 0x3f803f80
v_mov_b32 v95, 0x3f803f80
Mavv 160,30,96
s_nop 0
Tv 38,v4,26624
Tv 40,v4,32768
Mavv 180,34,96
Mavv 184,34,100
s_nop 0
Tv 42,v5,26624
Tv 44,v5,32768
Mavv 164,30,100
Mavv 168,30,104
Mavv 188,34,104
S_ s65,s61,110592
Mavv 192,34,108
S_ m0,s51,24576
Mavv 172,30,108
s_waitcnt vmcnt(6) lgkmcnt(0)
s_barrier
Mavv 176,30,112
Ln v7,32,s65,0
Mavv 196,34,112
Mavv 216,38,112
Dv 30,%0,6144
Mavv 236,42,112
Dv 34,v1,6144
Mavv 232,42,108
Ln v8,32,s65,3072
S_ m0,s51,25472
Mavv 212,38,108
Mavv 208,38,104
Ln v7,32,s65,128
Mavv 228,42,104
Mavv 224,42,100
Ln v8,32,s65,3200
S_ m0,s51,26368
Mavv 204,38,100
Mavv 200,38,96
Ln v7,32,s65,256
Mavv 220,42,96
.Lstep1_8:
Zvvv 76,92,116
Dv 38,%0,7168
Zvvv 80,92,120
Dv 42,v1,7168
Zvvv 84,92,124
Zvvv 88,92,128
Zvvv 92,92,132
W_ 2
Nvvv 96,30,136,56
Mvvv 96,34,156
Nvvv 100,30,140,60
s_nop 1
v_max3_f32 v10, v76, v80, v84
s_nop 0
v_max3_f32 v10, v10, v88, v92
Mvvv 100,34,160
v_cmp_lt_f32 vcc, 0x42000000, v10
s_cbranch_vccnz .Ltramp1_7
Nvvv 104,30,144,64
A_ v46,v46,v76
A_ v47,v47,v80
Mvvv 104,34,164
A_ v48,v48,v84
A_ v49,v49,v88
Nvvv 108,30,148,68
A_ v50,v50,v92
Mvvv 108,34,168
Ln v8,32,s65,3328
Nvvv 112,30,152,72
Mvvv 112,34,172
W_ 0
Mvvv 112,38,192
Dv 30,%0,8192
Mvvv 112,42,212
Dv 34,v1,8192
Mvvv 108,38,188
Mvvv 108,42,208
Mvvv 104,38,184
Mvvv 104,42,204
Mvvv 100,38,180
Mvvv 100,42,200
Mvvv 96,38,176
Mvvv 96,42,196
W_ 0
Mvvv 96,30,216
Dv 38,%0,0
Mvvv 96,34,236
Dv 42,v1,0
Mvvv 100,30,220
Mvvv 100,34,240
Mvvv 104,30,224
Mvvv 104,34,244
Mvvv 108,30,228
Mvvv 108,34,248
s_nop 0
E_ 96
Mvvv 112,30,232
E_ 97
Mvvv 112,34,252
W_ 0
E_ 98
Nvvv 76,38,136,56
Dv 30,%0,1024
Mvvv 76,42,156
Dv 34,v1,1024
Nvvv 80,38,140,60
E_ 99
Mvvv 80,42,160
C_ 99,98,99
C_ 98,96,97
Nvvv 84,38,144,64
E_ 100
Mvvv 84,42,164
E_ 101
Nvvv 88,38,148,68
E_ 102
Mvvv 88,42,168
E_ 103
Nvvv 92,38,152,72
C_ 103,102,103
C_ 102,100,101
Mvvv 92,42,172
W_ 0
E_ 104
Mvvv 92,30,192
Dv 38,%0,2048
Mvvv 92,34,212
Dv 42,v1,2048
Mvvv 88,30,188
E_ 105
Mvvv 88,34,208
E_ 106
Mvvv 84,30,184
S_ s65,s61,98304
E_ 107
Mvvv 84,34,204
S_ m0,s51,0
C_ 107,106,107
C_ 106,104,105
Mvvv 80,30,180
s_waitcnt vmcnt(6) lgkmcnt(0)
s_barrier
Mvvv 80,34,200
L_ v9,36,s65,0
Mvvv 76,30,176
E_ 108
Mvvv 76,34,196
L_ v9,36,s65,3072
S_ m0,s51,896
Mvvv 76,38,216
Tv 30,v2,12288
Tv 32,v2,18432
Mvvv 76,42,236
L_ v9,36,s65,128
Mvvv 80,38,220
s_nop 0
Tv 34,v3,12288
Tv 36,v3,18432
Mvvv 80,42,240
L_ v9,36,s65,3200
S_ m0,s51,1792
Mvvv 84,38,224
E_ 109
Mvvv 84,42,244
L_ v9,36,s65,256
Mvvv 88,38,228
E_ 110
Mvvv 88,42,248
L_ v9,36,s65,3328
Mvvv 92,38,232
E_ 111
Mvvv 92,42,252
W_ 0
C_ 111,110,111
C_ 110,108,109
Mavv 0,30,116
s_nop 0
Tv 38,v4,12288
Tv 40,v4,18432
Mavv 20,34,116
E_ 112
Mavv 24,34,120
s_nop 0
Tv 42,v5,12288
Tv 44,v5,18432
Mavv 4,30,120
E_ 113
Mavv 8,30,124
E_ 114
Mavv 28,34,124
E_ 115
Mavv 32,34,128
C_ 115,114,115
C_ 114,112,113
Mavv 12,30,128
E_ 76
Mavv 16,30,132
E_ 77
Mavv 36,34,132
W_ 0
E_ 78
Mavv 56,38,132
s_nop 0
Tv 30,v2,13312
Tv 32,v2,19456
Mavv 76,42,132
E_ 79
Mavv 72,42,128
s_nop 0
Tv 34,v3,13312
Tv 36,v3,19456
Mavv 52,38,128
C_ 96,76,77
C_ 97,78,79
Mavv 48,38,124
E_ 80
Mavv 68,42,124
E_ 81
Mavv 64,42,120
E_ 82
Mavv 44,38,120
E_ 83
Mavv 40,38,116
C_ 100,80,81
C_ 101,82,83
Mavv 60,42,116
W_ 0
E_ 84
Mavv 80,30,116
s_nop 0
Tv 38,v4,13312
Tv 40,v4,19456
Mavv 100,34,116
E_ 85
Mavv 104,34,120
s_nop 0
Tv 42,v5,13312
Tv 44,v5,19456
Mavv 84,30,120
E_ 86
Mavv 88,30,124
E_ 87
Mavv 108,34,124
C_ 104,84,85
C_ 105,86,87
Mavv 112,34,128
E_ 88
Mavv 92,30,128
E_ 89
Mavv 96,30,132
E_ 90
Mavv 116,34,132
W_ 0
E_ 91
Mavv 136,38,132
s_nop 0
Tv 30,v2,14336
Tv 32,v2,20480
Mavv 156,42,132
C_ 108,88,89
C_ 109,90,91
Mavv 152,42,128
s_nop 0
Tv 34,v3,14336
Tv 36,v3,20480
Mavv 132,38,128
E_ 92
Mavv 128,38,124
E_ 93
Mavv 148,42,124
E_ 94
Mavv 144,42,120
E_ 95
Mavv 124,38,120
C_ 112,92,93
C_ 113,94,95
Mavv 120,38,116
v_mov_b32 v92, 0x3f803f80
v_mov_b32 v93, 0x3f803f80
Mavv 140,42,116
W_ 0
v_mov_b32 v94, 0x3f803f80
v_mov_b32 v95, 0x3f803f80
Mavv 160,30,116
s_nop 0
Tv 38,v4,14336
Tv 40,v4,20480
Mavv 180,34,116
Mavv 184,34,120
s_nop 0
Tv 42,v5,14336
Tv 44,v5,20480
Mavv 164,30,120
Mavv 168,30,124
Mavv 188,34,124
S_ s65,s61,122880
Mavv 192,34,128
S_ m0,s51,12288
Mavv 172,30,128
s_waitcnt vmcnt(6) lgkmcnt(0)
s_barrier
Mavv 176,30,132
Ln v7,32,s65,0
Mavv 196,34,132
Mavv 216,38,132
Dv 30,%0,30720
Mavv 236,42,132
Dv 34,v1,30720
Mavv 232,42,128
Ln v8,32,s65,3072
S_ m0,s51,13184
Mavv 212,38,128
Mavv 208,38,124
Ln v7,32,s65,128
Mavv 228,42,124
Mavv 224,42,120
Ln v8,32,s65,3200
S_ m0,s51,14080
Mavv 204,38,120
Mavv 200,38,116
Ln v7,32,s65,256
Mavv 220,42,116
s_cmp_eq_u32 s4, 0
s_cbranch_scc1 .Lhs_d1
.Lhs_d1_back:
.Lstep1_9:
Zvvv 76,92,96
Dv 38,%0,31744
Zvvv 80,92,100
Dv 42,v1,31744
Zvvv 84,92,104
Zvvv 88,92,108
Zvvv 92,92,112
W_ 2
Nvvv 116,30,136,56
Mvvv 116,34,156
Nvvv 120,30,140,60
s_nop 1
v_max3_f32 v10, v76, v80, v84
s_nop 0
v_max3_f32 v10, v10, v88, v92
Mvvv 120,34,160
v_cmp_lt_f32 vcc, 0x42000000, v10
s_cbranch_vccnz .Ltramp1_8
Nvvv 124,30,144,64
A_ v46,v46,v76
A_ v47,v47,v80
Mvvv 124,34,164
A_ v48,v48,v84
A_ v49,v49,v88
Nvvv 128,30,148,68
A_ v50,v50,v92
Mvvv 128,34,168
Ln v8,32,s65,3328
Nvvv 132,30,152,72
Mvvv 132,34,172
W_ 0
Mvvv 132,38,192
Dv 30,%0,32768
Mvvv 132,42,212
Dv 34,v1,32768
Mvvv 128,38,188
Mvvv 128,42,208
Mvvv 124,38,184
Mvvv 124,42,204
Mvvv 120,38,180
Mvvv 120,42,200
Mvvv 116,38,176
Mvvv 116,42,196
W_ 0
Mvvv 116,30,216
Dv 38,%0,24576
Mvvv 116,34,236
Dv 42,v1,24576
Mvvv 120,30,220
Mvvv 120,34,240
Mvvv 124,30,224
Mvvv 124,34,244
Mvvv 128,30,228
Mvvv 128,34,248
s_nop 0
E_ 116
Mvvv 132,30,232
E_ 117
Mvvv 132,34,252
W_ 0
E_ 118
Nvvv 76,38,136,56
Dv 30,%0,25600
Mvvv 76,42,156
Dv 34,v1,25600
Nvvv 80,38,140,60
E_ 119
Mvvv 80,42,160
C_ 119,118,119
C_ 118,116,117
Nvvv 84,38,144,64
E_ 120
Mvvv 84,42,164
E_ 121
Nvvv 88,38,148,68
E_ 122
Mvvv 88,42,168
E_ 123
Nvvv 92,38,152,72
C_ 123,122,123
C_ 122,120,121
Mvvv 92,42,172
W_ 0
E_ 124
Mvvv 92,30,192
Dv 38,%0,26624
Mvvv 92,34,212
Dv 42,v1,26624
Mvvv 88,30,188
E_ 125
Mvvv 88,34,208
E_ 126
Mvvv 84,30,184
S_ s65,s61,110592
E_ 127
Mvvv 84,34,204
S_ m0,s51,24576
C_ 127,126,127
C_ 126,124,125
Mvvv 80,30,180
s_waitcnt vmcnt(6) lgkmcnt(0)
s_barrier
Mvvv 80,34,200
L_ v9,36,s65,0
Mvvv 76,30,176
E_ 128
Mvvv 76,34,196
L_ v9,36,s65,3072
S_ m0,s51,25472
Mvvv 76,38,216
Tv 30,v2,0
Tv 32,v2,6144
Mvvv 76,42,236
L_ v9,36,s65,128
Mvvv 80,38,220
s_nop 0
Tv 34,v3,0
Tv 36,v3,6144
Mvvv 80,42,240
L_ v9,36,s65,3200
S_ m0,s51,26368
Mvvv 84,38,224
E_ 129
Mvvv 84,42,244
L_ v9,36,s65,256
Mvvv 88,38,228
E_ 130
Mvvv 88,42,248
L_ v9,36,s65,3328
Mvvv 92,38,232
E_ 131
Mvvv 92,42,252
W_ 0
C_ 131,130,131
C_ 130,128,129
Mavv 0,30,96
s_nop 0
Tv 38,v4,0
Tv 40,v4,6144
Mavv 20,34,96
E_ 132
Mavv 24,34,100
s_nop 0
Tv 42,v5,0
Tv 44,v5,6144
Mavv 4,30,100
E_ 133
Mavv 8,30,104
E_ 134
Mavv 28,34,104
E_ 135
Mavv 32,34,108
C_ 135,134,135
C_ 134,132,133
Mavv 12,30,108
E_ 76
Mavv 16,30,112
E_ 77
Mavv 36,34,112
W_ 0
E_ 78
Mavv 56,38,112
s_nop 0
Tv 30,v2,1024
Tv 32,v2,7168
Mavv 76,42,112
E_ 79
Mavv 72,42,108
s_nop 0
Tv 34,v3,1024
Tv 36,v3,7168
Mavv 52,38,108
C_ 116,76,77
C_ 117,78,79
Mavv 48,38,104
E_ 80
Mavv 68,42,104
E_ 81
Mavv 64,42,100
E_ 82
Mavv 44,38,100
E_ 83
Mavv 40,38,96
C_ 120,80,81
C_ 121,82,83
Mavv 60,42,96
W_ 0
E_ 84
Mavv 80,30,96
s_nop 0
Tv 38,v4,1024
Tv 40,v4,7168
Mavv 100,34,96
E_ 85
Mavv 104,34,100
s_nop 0
Tv 42,v5,1024
Tv 44,v5,7168
Mavv 84,30,100
E_ 86
Mavv 88,30,104
E_ 87
Mavv 108,34,104
C_ 124,84,85
C_ 125,86,87
Mavv 112,34,108
E_ 88
Mavv 92,30,108
E_ 89
Mavv 96,30,112
E_ 90
Mavv 116,34,112
W_ 0
E_ 91
Mavv 136,38,112
s_nop 0
Tv 30,v2,2048
Tv 32,v2,8192
Mavv 156,42,112
C_ 128,88,89
C_ 129,90,91
Mavv 152,42,108
s_nop 0
Tv 34,v3,2048
Tv 36,v3,8192
Mavv 132,38,108
E_ 92
Mavv 128,38,104
E_ 93
Mavv 148,42,104
E_ 94
Mavv 144,42,100
E_ 95
Mavv 124,38,100
C_ 132,92,93
C_ 133,94,95
Mavv 120,38,96
v_mov_b32 v92, 0x3f803f80
v_mov_b32 v93, 0x3f803f80
Mavv 140,42,96
W_ 0
v_mov_b32 v94, 0x3f803f80
v_mov_b32 v95, 0x3f803f80
Mavv 160,30,96
s_nop 0
Tv 38,v4,2048
Tv 40,v4,8192
Mavv 180,34,96
Mavv 184,34,100
s_nop 0
Tv 42,v5,2048
Tv 44,v5,8192
Mavv 164,30,100
Mavv 168,30,104
Mavv 188,34,104
S_ s65,s61,135168
Mavv 192,34,108
S_ m0,s51,0
Mavv 172,30,108
s_waitcnt vmcnt(6) lgkmcnt(0)
s_barrier
Mavv 176,30,112
Ln v7,32,s65,0
Mavv 196,34,112
Mavv 216,38,112
Dv 30,%0,18432
Mavv 236,42,112
Dv 34,v1,18432
Mavv 232,42,108
Ln v8,32,s65,3072
S_ m0,s51,896
Mavv 212,38,108
Mavv 208,38,104
Ln v7,32,s65,128
Mavv 228,42,104
Mavv 224,42,100
Ln v8,32,s65,3200
S_ m0,s51,1792
Mavv 204,38,100
Mavv 200,38,96
Ln v7,32,s65,256
Mavv 220,42,96
.Lstep1_10:
Zvvv 76,92,116
Dv 38,%0,19456
Zvvv 80,92,120
Dv 42,v1,19456
Zvvv 84,92,124
Zvvv 88,92,128
Zvvv 92,92,132
W_ 2
Nvvv 96,30,136,56
Mvvv 96,34,156
Nvvv 100,30,140,60
s_nop 1
v_max3_f32 v10, v76, v80, v84
s_nop 0
v_max3_f32 v10, v10, v88, v92
Mvvv 100,34,160
v_cmp_lt_f32 vcc, 0x42000000, v10
s_cbranch_vccnz .Ltramp1_9
Nvvv 104,30,144,64
A_ v46,v46,v76
A_ v47,v47,v80
Mvvv 104,34,164
A_ v48,v48,v84
A_ v49,v49,v88
Nvvv 108,30,148,68
A_ v50,v50,v92
Mvvv 108,34,168
Ln v8,32,s65,3328
Nvvv 112,30,152,72
Mvvv 112,34,172
W_ 0
Mvvv 112,38,192
Dv 30,%0,20480
Mvvv 112,42,212
Dv 34,v1,20480
Mvvv 108,38,188
Mvvv 108,42,208
Mvvv 104,38,184
Mvvv 104,42,204
Mvvv 100,38,180
Mvvv 100,42,200
Mvvv 96,38,176
Mvvv 96,42,196
W_ 0
Mvvv 96,30,216
Dv 38,%0,12288
Mvvv 96,34,236
Dv 42,v1,12288
Mvvv 100,30,220
Mvvv 100,34,240
Mvvv 104,30,224
Mvvv 104,34,244
Mvvv 108,30,228
Mvvv 108,34,248
s_nop 0
E_ 96
Mvvv 112,30,232
E_ 97
Mvvv 112,34,252
W_ 0
E_ 98
Nvvv 76,38,136,56
Dv 30,%0,13312
Mvvv 76,42,156
Dv 34,v1,13312
Nvvv 80,38,140,60
E_ 99
Mvvv 80,42,160
C_ 99,98,99
C_ 98,96,97
Nvvv 84,38,144,64
E_ 100
Mvvv 84,42,164
E_ 101
Nvvv 88,38,148,68
E_ 102
Mvvv 88,42,168
E_ 103
Nvvv 92,38,152,72
C_ 103,102,103
C_ 102,100,101
Mvvv 92,42,172
W_ 0
E_ 104
Mvvv 92,30,192
Dv 38,%0,14336
Mvvv 92,34,212
Dv 42,v1,14336
Mvvv 88,30,188
E_ 105
Mvvv 88,34,208
E_ 106
Mvvv 84,30,184
S_ s65,s61,122880
E_ 107
Mvvv 84,34,204
S_ m0,s51,12288
C_ 107,106,107
C_ 106,104,105
Mvvv 80,30,180
s_waitcnt vmcnt(6) lgkmcnt(0)
s_barrier
Mvvv 80,34,200
L_ v9,36,s65,0
Mvvv 76,30,176
E_ 108
Mvvv 76,34,196
L_ v9,36,s65,3072
S_ m0,s51,13184
Mvvv 76,38,216
Tv 30,v2,24576
Tv 32,v2,30720
Mvvv 76,42,236
L_ v9,36,s65,128
Mvvv 80,38,220
s_nop 0
Tv 34,v3,24576
Tv 36,v3,30720
Mvvv 80,42,240
L_ v9,36,s65,3200
S_ m0,s51,14080
Mvvv 84,38,224
E_ 109
Mvvv 84,42,244
L_ v9,36,s65,256
Mvvv 88,38,228
E_ 110
Mvvv 88,42,248
L_ v9,36,s65,3328
Mvvv 92,38,232
E_ 111
Mvvv 92,42,252
W_ 0
C_ 111,110,111
C_ 110,108,109
Mavv 0,30,116
s_nop 0
Tv 38,v4,24576
Tv 40,v4,30720
Mavv 20,34,116
E_ 112
Mavv 24,34,120
s_nop 0
Tv 42,v5,24576
Tv 44,v5,30720
Mavv 4,30,120
E_ 113
Mavv 8,30,124
E_ 114
Mavv 28,34,124
E_ 115
Mavv 32,34,128
C_ 115,114,115
C_ 114,112,113
Mavv 12,30,128
E_ 76
Mavv 16,30,132
E_ 77
Mavv 36,34,132
W_ 0
E_ 78
Mavv 56,38,132
s_nop 0
Tv 30,v2,25600
Tv 32,v2,31744
Mavv 76,42,132
E_ 79
Mavv 72,42,128
s_nop 0
Tv 34,v3,25600
Tv 36,v3,31744
Mavv 52,38,128
C_ 96,76,77
C_ 97,78,79
Mavv 48,38,124
E_ 80
Mavv 68,42,124
E_ 81
Mavv 64,42,120
E_ 82
Mavv 44,38,120
E_ 83
Mavv 40,38,116
C_ 100,80,81
C_ 101,82,83
Mavv 60,42,116
W_ 0
E_ 84
Mavv 80,30,116
s_nop 0
Tv 38,v4,25600
Tv 40,v4,31744
Mavv 100,34,116
E_ 85
Mavv 104,34,120
s_nop 0
Tv 42,v5,25600
Tv 44,v5,31744
Mavv 84,30,120
E_ 86
Mavv 88,30,124
E_ 87
Mavv 108,34,124
C_ 104,84,85
C_ 105,86,87
Mavv 112,34,128
E_ 88
Mavv 92,30,128
E_ 89
Mavv 96,30,132
E_ 90
Mavv 116,34,132
W_ 0
E_ 91
Mavv 136,38,132
s_nop 0
Tv 30,v2,26624
Tv 32,v2,32768
Mavv 156,42,132
C_ 108,88,89
C_ 109,90,91
Mavv 152,42,128
s_nop 0
Tv 34,v3,26624
Tv 36,v3,32768
Mavv 132,38,128
E_ 92
Mavv 128,38,124
E_ 93
Mavv 148,42,124
E_ 94
Mavv 144,42,120
E_ 95
Mavv 124,38,120
C_ 112,92,93
C_ 113,94,95
Mavv 120,38,116
v_mov_b32 v92, 0x3f803f80
v_mov_b32 v93, 0x3f803f80
Mavv 140,42,116
W_ 0
v_mov_b32 v94, 0x3f803f80
v_mov_b32 v95, 0x3f803f80
Mavv 160,30,116
s_nop 0
Tv 38,v4,26624
Tv 40,v4,32768
Mavv 180,34,116
Mavv 184,34,120
s_nop 0
Tv 42,v5,26624
Tv 44,v5,32768
Mavv 164,30,120
Mavv 168,30,124
Mavv 188,34,124
S_ s65,s61,147456
Mavv 192,34,128
S_ m0,s51,24576
Mavv 172,30,128
s_waitcnt vmcnt(6) lgkmcnt(0)
s_barrier
Mavv 176,30,132
Ln v7,32,s65,0
Mavv 196,34,132
Mavv 216,38,132
Dv 30,%0,6144
Mavv 236,42,132
Dv 34,v1,6144
Mavv 232,42,128
Ln v8,32,s65,3072
S_ m0,s51,25472
Mavv 212,38,128
Mavv 208,38,124
Ln v7,32,s65,128
Mavv 228,42,124
Mavv 224,42,120
Ln v8,32,s65,3200
S_ m0,s51,26368
Mavv 204,38,120
Mavv 200,38,116
Ln v7,32,s65,256
Mavv 220,42,116
.Lstep1_11:
Zvvv 76,92,96
Dv 38,%0,7168
Zvvv 80,92,100
Dv 42,v1,7168
Zvvv 84,92,104
Zvvv 88,92,108
Zvvv 92,92,112
W_ 2
Nvvv 116,30,136,56
Mvvv 116,34,156
Nvvv 120,30,140,60
s_nop 1
v_max3_f32 v10, v76, v80, v84
s_nop 0
v_max3_f32 v10, v10, v88, v92
Mvvv 120,34,160
v_cmp_lt_f32 vcc, 0x42000000, v10
s_cbranch_vccnz .Ltramp1_10
Nvvv 124,30,144,64
A_ v46,v46,v76
A_ v47,v47,v80
Mvvv 124,34,164
A_ v48,v48,v84
A_ v49,v49,v88
Nvvv 128,30,148,68
A_ v50,v50,v92
Mvvv 128,34,168
Ln v8,32,s65,3328
Nvvv 132,30,152,72
Mvvv 132,34,172
W_ 0
Mvvv 132,38,192
Dv 30,%0,8192
Mvvv 132,42,212
Dv 34,v1,8192
Mvvv 128,38,188
Mvvv 128,42,208
Mvvv 124,38,184
Mvvv 124,42,204
Mvvv 120,38,180
Mvvv 120,42,200
Mvvv 116,38,176
Mvvv 116,42,196
W_ 0
Mvvv 116,30,216
Dv 38,%0,0
Mvvv 116,34,236
Dv 42,v1,0
Mvvv 120,30,220
Mvvv 120,34,240
Mvvv 124,30,224
Mvvv 124,34,244
Mvvv 128,30,228
Mvvv 128,34,248
s_nop 0
E_ 116
Mvvv 132,30,232
E_ 117
Mvvv 132,34,252
W_ 0
E_ 118
Nvvv 76,38,136,56
Dv 30,%0,1024
Mvvv 76,42,156
Dv 34,v1,1024
Nvvv 80,38,140,60
E_ 119
Mvvv 80,42,160
C_ 119,118,119
C_ 118,116,117
Nvvv 84,38,144,64
E_ 120
Mvvv 84,42,164
E_ 121
Nvvv 88,38,148,68
E_ 122
Mvvv 88,42,168
E_ 123
Nvvv 92,38,152,72
C_ 123,122,123
C_ 122,120,121
Mvvv 92,42,172
W_ 0
E_ 124
Mvvv 92,30,192
Dv 38,%0,2048
Mvvv 92,34,212
Dv 42,v1,2048
Mvvv 88,30,188
E_ 125
Mvvv 88,34,208
E_ 126
Mvvv 84,30,184
S_ s65,s61,135168
E_ 127
Mvvv 84,34,204
S_ m0,s51,0
C_ 127,126,127
C_ 126,124,125
Mvvv 80,30,180
s_waitcnt vmcnt(6) lgkmcnt(0)
s_barrier
Mvvv 80,34,200
L_ v9,36,s65,0
Mvvv 76,30,176
E_ 128
Mvvv 76,34,196
L_ v9,36,s65,3072
S_ m0,s51,896
Mvvv 76,38,216
Tv 30,v2,12288
Tv 32,v2,18432
Mvvv 76,42,236
L_ v9,36,s65,128
Mvvv 80,38,220
s_nop 0
Tv 34,v3,12288
Tv 36,v3,18432
Mvvv 80,42,240
L_ v9,36,s65,3200
S_ m0,s51,1792
Mvvv 84,38,224
E_ 129
Mvvv 84,42,244
L_ v9,36,s65,256
Mvvv 88,38,228
E_ 130
Mvvv 88,42,248
L_ v9,36,s65,3328
Mvvv 92,38,232
E_ 131
Mvvv 92,42,252
W_ 0
C_ 131,130,131
C_ 130,128,129
Mavv 0,30,96
s_nop 0
Tv 38,v4,12288
Tv 40,v4,18432
Mavv 20,34,96
E_ 132
Mavv 24,34,100
s_nop 0
Tv 42,v5,12288
Tv 44,v5,18432
Mavv 4,30,100
E_ 133
Mavv 8,30,104
E_ 134
Mavv 28,34,104
E_ 135
Mavv 32,34,108
C_ 135,134,135
C_ 134,132,133
Mavv 12,30,108
E_ 76
Mavv 16,30,112
E_ 77
Mavv 36,34,112
W_ 0
E_ 78
Mavv 56,38,112
s_nop 0
Tv 30,v2,13312
Tv 32,v2,19456
Mavv 76,42,112
E_ 79
Mavv 72,42,108
s_nop 0
Tv 34,v3,13312
Tv 36,v3,19456
Mavv 52,38,108
C_ 116,76,77
C_ 117,78,79
Mavv 48,38,104
E_ 80
Mavv 68,42,104
E_ 81
Mavv 64,42,100
E_ 82
Mavv 44,38,100
E_ 83
Mavv 40,38,96
C_ 120,80,81
C_ 121,82,83
Mavv 60,42,96
W_ 0
E_ 84
Mavv 80,30,96
s_nop 0
Tv 38,v4,13312
Tv 40,v4,19456
Mavv 100,34,96
E_ 85
Mavv 104,34,100
s_nop 0
Tv 42,v5,13312
Tv 44,v5,19456
Mavv 84,30,100
E_ 86
Mavv 88,30,104
E_ 87
Mavv 108,34,104
C_ 124,84,85
C_ 125,86,87
Mavv 112,34,108
E_ 88
Mavv 92,30,108
E_ 89
Mavv 96,30,112
E_ 90
Mavv 116,34,112
W_ 0
E_ 91
Mavv 136,38,112
s_nop 0
Tv 30,v2,14336
Tv 32,v2,20480
Mavv 156,42,112
C_ 128,88,89
C_ 129,90,91
Mavv 152,42,108
s_nop 0
Tv 34,v3,14336
Tv 36,v3,20480
Mavv 132,38,108
E_ 92
Mavv 128,38,104
E_ 93
Mavv 148,42,104
E_ 94
Mavv 144,42,100
E_ 95
Mavv 124,38,100
C_ 132,92,93
C_ 133,94,95
Mavv 120,38,96
v_mov_b32 v92, 0x3f803f80
v_mov_b32 v93, 0x3f803f80
Mavv 140,42,96
W_ 0
v_mov_b32 v94, 0x3f803f80
v_mov_b32 v95, 0x3f803f80
Mavv 160,30,96
s_nop 0
Tv 38,v4,14336
Tv 40,v4,20480
Mavv 180,34,96
Mavv 184,34,100
s_nop 0
Tv 42,v5,14336
Tv 44,v5,20480
Mavv 164,30,100
Mavv 168,30,104
Mavv 188,34,104
S_ s65,s61,159744
Mavv 192,34,108
S_ m0,s51,12288
Mavv 172,30,108
s_waitcnt vmcnt(6) lgkmcnt(0)
s_barrier
Mavv 176,30,112
Ln v7,32,s65,0
Mavv 196,34,112
Mavv 216,38,112
Dv 30,%0,30720
Mavv 236,42,112
Dv 34,v1,30720
Mavv 232,42,108
Ln v8,32,s65,3072
S_ m0,s51,13184
Mavv 212,38,108
Mavv 208,38,104
Ln v7,32,s65,128
Mavv 228,42,104
Mavv 224,42,100
Ln v8,32,s65,3200
S_ m0,s51,14080
Mavv 204,38,100
Mavv 200,38,96
Ln v7,32,s65,256
Mavv 220,42,96
.Lstep1_12:
Zvvv 76,92,116
Dv 38,%0,31744
Zvvv 80,92,120
Dv 42,v1,31744
Zvvv 84,92,124
Zvvv 88,92,128
Zvvv 92,92,132
W_ 2
Nvvv 96,30,136,56
Mvvv 96,34,156
Nvvv 100,30,140,60
s_nop 1
v_max3_f32 v10, v76, v80, v84
s_nop 0
v_max3_f32 v10, v10, v88, v92
Mvvv 100,34,160
v_cmp_lt_f32 vcc, 0x42000000, v10
s_cbranch_vccnz .Ltramp1_11
Nvvv 104,30,144,64
A_ v46,v46,v76
A_ v47,v47,v80
Mvvv 104,34,164
A_ v48,v48,v84
A_ v49,v49,v88
Nvvv 108,30,148,68
A_ v50,v50,v92
Mvvv 108,34,168
Ln v8,32,s65,3328
Nvvv 112,30,152,72
R_ v11,a248
Mvvv 112,34,172
W_ 0
v_lshlrev_b32 v11, 10, v11
Mvvv 112,38,192
Dv 30,%0,32768
Mvvv 112,42,212
Dv 34,v1,32768
Mvvv 108,38,188
buffer_load_dword a254, v11, s[44:47], s66 offen
Mvvv 108,42,208
Mvvv 104,38,184
Mvvv 104,42,204
Mvvv 100,38,180
Mvvv 100,42,200
Mvvv 96,38,176
Mvvv 96,42,196
W_ 0
Mvvv 96,30,216
Dv 38,%0,24576
Mvvv 96,34,236
Dv 42,v1,24576
Mvvv 100,30,220
Mvvv 100,34,240
Mvvv 104,30,224
Mvvv 104,34,244
Mvvv 108,30,228
Mvvv 108,34,248
s_nop 0
E_ 96
Mvvv 112,30,232
E_ 97
Mvvv 112,34,252
W_ 0
E_ 98
Nvvv 76,38,136,56
Dv 30,%0,25600
Mvvv 76,42,156
Dv 34,v1,25600
Nvvv 80,38,140,60
E_ 99
Mvvv 80,42,160
C_ 99,98,99
C_ 98,96,97
Nvvv 84,38,144,64
E_ 100
Mvvv 84,42,164
E_ 101
Nvvv 88,38,148,68
E_ 102
Mvvv 88,42,168
E_ 103
Nvvv 92,38,152,72
C_ 103,102,103
C_ 102,100,101
Mvvv 92,42,172
W_ 0
E_ 104
Mvvv 92,30,192
Dv 38,%0,26624
Mvvv 92,34,212
Dv 42,v1,26624
Mvvv 88,30,188
E_ 105
Mvvv 88,34,208
E_ 106
Mvvv 84,30,184
S_ s65,s61,147456
E_ 107
Mvvv 84,34,204
S_ m0,s51,24576
C_ 107,106,107
C_ 106,104,105
Mvvv 80,30,180
s_waitcnt vmcnt(7) lgkmcnt(0)
s_barrier
Mvvv 80,34,200
Ln v9,36,s65,0
Mvvv 76,30,176
E_ 108
Mvvv 76,34,196
Ln v9,36,s65,3072
S_ m0,s51,25472
Mvvv 76,38,216
Tv 30,v2,0
Tv 32,v2,6144
Mvvv 76,42,236
Ln v9,36,s65,128
Mvvv 80,38,220
s_nop 0
Tv 34,v3,0
Tv 36,v3,6144
Mvvv 80,42,240
Ln v9,36,s65,3200
S_ m0,s51,26368
Mvvv 84,38,224
E_ 109
Mvvv 84,42,244
Ln v9,36,s65,256
Mvvv 88,38,228
E_ 110
Mvvv 88,42,248
Ln v9,36,s65,3328
Mvvv 92,38,232
E_ 111
Mvvv 92,42,252
W_ 0
C_ 111,110,111
C_ 110,108,109
Mavv 0,30,116
s_nop 0
Tv 38,v4,0
Tv 40,v4,6144
Mavv 20,34,116
E_ 112
Mavv 24,34,120
s_nop 0
Tv 42,v5,0
Tv 44,v5,6144
Mavv 4,30,120
E_ 113
Mavv 8,30,124
E_ 114
Mavv 28,34,124
E_ 115
Mavv 32,34,128
C_ 115,114,115
C_ 114,112,113
Mavv 12,30,128
E_ 76
Mavv 16,30,132
E_ 77
Mavv 36,34,132
W_ 0
E_ 78
Mavv 56,38,132
s_nop 0
Tv 30,v2,1024
Tv 32,v2,7168
Mavv 76,42,132
E_ 79
Mavv 72,42,128
s_nop 0
Tv 34,v3,1024
Tv 36,v3,7168
Mavv 52,38,128
C_ 96,76,77
C_ 97,78,79
Mavv 48,38,124
E_ 80
Mavv 68,42,124
E_ 81
Mavv 64,42,120
E_ 82
Mavv 44,38,120
E_ 83
Mavv 40,38,116
C_ 100,80,81
C_ 101,82,83
Mavv 60,42,116
W_ 0
E_ 84
Mavv 80,30,116
s_nop 0
Tv 38,v4,1024
Tv 40,v4,7168
Mavv 100,34,116
E_ 85
Mavv 104,34,120
s_nop 0
Tv 42,v5,1024
Tv 44,v5,7168
Mavv 84,30,120
E_ 86
Mavv 88,30,124
E_ 87
Mavv 108,34,124
C_ 104,84,85
C_ 105,86,87
Mavv 112,34,128
E_ 88
Mavv 92,30,128
E_ 89
Mavv 96,30,132
E_ 90
Mavv 116,34,132
W_ 0
E_ 91
Mavv 136,38,132
s_nop 0
Tv 30,v2,2048
Tv 32,v2,8192
Mavv 156,42,132
C_ 108,88,89
C_ 109,90,91
Mavv 152,42,128
s_nop 0
Tv 34,v3,2048
Tv 36,v3,8192
Mavv 132,38,128
E_ 92
Mavv 128,38,124
E_ 93
Mavv 148,42,124
E_ 94
Mavv 144,42,120
E_ 95
Mavv 124,38,120
C_ 112,92,93
C_ 113,94,95
Mavv 120,38,116
v_mov_b32 v92, 0x3f803f80
v_mov_b32 v93, 0x3f803f80
Mavv 140,42,116
W_ 0
v_mov_b32 v94, 0x3f803f80
v_mov_b32 v95, 0x3f803f80
Mavv 160,30,116
s_nop 0
Tv 38,v4,2048
Tv 40,v4,8192
Mavv 180,34,116
Mavv 184,34,120
s_nop 0
Tv 42,v5,2048
Tv 44,v5,8192
Mavv 164,30,120
Mavv 168,30,124
Mavv 188,34,124
S_ s65,s61,172032
Mavv 192,34,128
S_ m0,s51,0
Mavv 172,30,128
s_waitcnt vmcnt(7) lgkmcnt(0)
s_barrier
Mavv 176,30,132
Ln v7,32,s65,0
Mavv 196,34,132
Mavv 216,38,132
Dv 30,%0,18432
Mavv 236,42,132
Dv 34,v1,18432
Mavv 232,42,128
Ln v8,32,s65,3072
S_ m0,s51,896
Mavv 212,38,128
Mavv 208,38,124
Ln v7,32,s65,128
Mavv 228,42,124
Mavv 224,42,120
Ln v8,32,s65,3200
S_ m0,s51,1792
Mavv 204,38,120
Mavv 200,38,116
Ln v7,32,s65,256
Mavv 220,42,116
.Lstep1_13:
Zvvv 76,92,96
Dv 38,%0,19456
Zvvv 80,92,100
Dv 42,v1,19456
Zvvv 84,92,104
Zvvv 88,92,108
Zvvv 92,92,112
W_ 2
Nvvv 116,30,136,56
Mvvv 116,34,156
Nvvv 120,30,140,60
s_nop 1
v_max3_f32 v10, v76, v80, v84
s_nop 0
v_max3_f32 v10, v10, v88, v92
Mvvv 120,34,160
v_cmp_lt_f32 vcc, 0x42000000, v10
s_cbranch_vccnz .Ltramp1_12
Nvvv 124,30,144,64
A_ v46,v46,v76
A_ v47,v47,v80
Mvvv 124,34,164
A_ v48,v48,v84
A_ v49,v49,v88
Nvvv 128,30,148,68
A_ v50,v50,v92
Mvvv 128,34,168
Ln v8,32,s65,3328
Nvvv 132,30,152,72
Mvvv 132,34,172
W_ 0
Mvvv 132,38,192
Dv 30,%0,20480
Mvvv 132,42,212
Dv 34,v1,20480
Mvvv 128,38,188
Mvvv 128,42,208
Mvvv 124,38,184
Mvvv 124,42,204
Mvvv 120,38,180
Mvvv 120,42,200
Mvvv 116,38,176
Mvvv 116,42,196
W_ 0
Mvvv 116,30,216
Dv 38,%0,12288
Mvvv 116,34,236
Dv 42,v1,12288
Mvvv 120,30,220
Mvvv 120,34,240
Mvvv 124,30,224
Mvvv 124,34,244
Mvvv 128,30,228
Mvvv 128,34,248
s_nop 0
E_ 116
Mvvv 132,30,232
E_ 117
Mvvv 132,34,252
W_ 0
)A"
R"A(E_ 118
Nvvv 76,38,136,56
Dv 30,%0,13312
Mvvv 76,42,156
Dv 34,v1,13312
Nvvv 80,38,140,60
E_ 119
Mvvv 80,42,160
C_ 119,118,119
C_ 118,116,117
Nvvv 84,38,144,64
E_ 120
Mvvv 84,42,164
E_ 121
Nvvv 88,38,148,68
E_ 122
Mvvv 88,42,168
E_ 123
Nvvv 92,38,152,72
C_ 123,122,123
C_ 122,120,121
Mvvv 92,42,172
W_ 0
E_ 124
Mvvv 92,30,192
Dv 38,%0,14336
Mvvv 92,34,212
Dv 42,v1,14336
Mvvv 88,30,188
E_ 125
Mvvv 88,34,208
E_ 126
Mvvv 84,30,184
S_ s65,s61,159744
E_ 127
Mvvv 84,34,204
S_ m0,s51,12288
C_ 127,126,127
C_ 126,124,125
Mvvv 80,30,180
s_waitcnt vmcnt(6) lgkmcnt(0)
s_barrier
Mvvv 80,34,200
Ln v9,36,s65,0
Mvvv 76,30,176
E_ 128
Mvvv 76,34,196
Ln v9,36,s65,3072
S_ m0,s51,13184
Mvvv 76,38,216
Tv 30,v2,24576
Tv 32,v2,30720
Mvvv 76,42,236
Ln v9,36,s65,128
Mvvv 80,38,220
s_nop 0
Tv 34,v3,24576
Tv 36,v3,30720
Mvvv 80,42,240
Ln v9,36,s65,3200
S_ m0,s51,14080
Mvvv 84,38,224
E_ 129
Mvvv 84,42,244
Ln v9,36,s65,256
Mvvv 88,38,228
E_ 130
Mvvv 88,42,248
Ln v9,36,s65,3328
Mvvv 92,38,232
E_ 131
Mvvv 92,42,252
W_ 0
C_ 131,130,131
C_ 130,128,129
Mavv 0,30,96
s_nop 0
Tv 38,v4,24576
Tv 40,v4,30720
Mavv 20,34,96
E_ 132
Mavv 24,34,100
s_nop 0
Tv 42,v5,24576
Tv 44,v5,30720
Mavv 4,30,100
E_ 133
Mavv 8,30,104
E_ 134
Mavv 28,34,104
E_ 135
Mavv 32,34,108
C_ 135,134,135
C_ 134,132,133
Mavv 12,30,108
E_ 76
Mavv 16,30,112
E_ 77
Mavv 36,34,112
W_ 0
E_ 78
Mavv 56,38,112
s_nop 0
Tv 30,v2,25600
Tv 32,v2,31744
Mavv 76,42,112
E_ 79
Mavv 72,42,108
s_nop 0
Tv 34,v3,25600
Tv 36,v3,31744
Mavv 52,38,108
C_ 116,76,77
C_ 117,78,79
Mavv 48,38,104
E_ 80
Mavv 68,42,104
E_ 81
Mavv 64,42,100
E_ 82
Mavv 44,38,100
E_ 83
Mavv 40,38,96
C_ 120,80,81
C_ 121,82,83
Mavv 60,42,96
W_ 0
E_ 84
Mavv 80,30,96
s_nop 0
Tv 38,v4,25600
Tv 40,v4,31744
Mavv 100,34,96
E_ 85
Mavv 104,34,100
s_nop 0
Tv 42,v5,25600
Tv 44,v5,31744
Mavv 84,30,100
E_ 86
Mavv 88,30,104
E_ 87
Mavv 108,34,104
C_ 124,84,85
C_ 125,86,87
Mavv 112,34,108
E_ 88
Mavv 92,30,108
E_ 89
Mavv 96,30,112
E_ 90
Mavv 116,34,112
W_ 0
E_ 91
Mavv 136,38,112
s_nop 0
Tv 30,v2,26624
Tv 32,v2,32768
Mavv 156,42,112
C_ 128,88,89
C_ 129,90,91
Mavv 152,42,108
s_nop 0
Tv 34,v3,26624
Tv 36,v3,32768
Mavv 132,38,108
E_ 92
Mavv 128,38,104
E_ 93
Mavv 148,42,104
E_ 94
Mavv 144,42,100
E_ 95
Mavv 124,38,100
C_ 132,92,93
C_ 133,94,95
Mavv 120,38,96
v_mov_b32 v92, 0x3f803f80
v_mov_b32 v93, 0x3f803f80
Mavv 140,42,96
W_ 0
v_mov_b32 v94, 0x3f803f80
v_mov_b32 v95, 0x3f803f80
Mavv 160,30,96
s_nop 0
Tv 38,v4,26624
Tv 40,v4,32768
Mavv 180,34,96
Mavv 184,34,100
s_nop 0
Tv 42,v5,26624
Tv 44,v5,32768
Mavv 164,30,100
Mavv 168,30,104
Mavv 188,34,104
S_ s65,s61,184320
Mavv 192,34,108
S_ m0,s51,24576
Mavv 172,30,108
s_waitcnt vmcnt(6) lgkmcnt(0)
s_barrier
Mavv 176,30,112
Ln v7,32,s65,0
Mavv 196,34,112
Mavv 216,38,112
Dv 30,%0,6144
Mavv 236,42,112
Dv 34,v1,6144
Mavv 232,42,108
Ln v8,32,s65,3072
S_ m0,s51,25472
Mavv 212,38,108
Mavv 208,38,104
Ln v7,32,s65,128
Mavv 228,42,104
Mavv 224,42,100
Ln v8,32,s65,3200
S_ m0,s51,26368
Mavv 204,38,100
Mavv 200,38,96
Ln v7,32,s65,256
Mavv 220,42,96
.Lstep1_14:
Zvvv 76,92,116
Dv 38,%0,7168
Zvvv 80,92,120
Dv 42,v1,7168
Zvvv 84,92,124
Zvvv 88,92,128
Zvvv 92,92,132
W_ 2
Nvvv 96,30,136,56
Mvvv 96,34,156
Nvvv 100,30,140,60
s_nop 1
v_max3_f32 v10, v76, v80, v84
s_nop 0
v_max3_f32 v10, v10, v88, v92
Mvvv 100,34,160
v_cmp_lt_f32 vcc, 0x42000000, v10
s_cbranch_vccnz .Ltramp1_13
Nvvv 104,30,144,64
A_ v46,v46,v76
A_ v47,v47,v80
Mvvv 104,34,164
A_ v48,v48,v84
A_ v49,v49,v88
Nvvv 108,30,148,68
A_ v50,v50,v92
Mvvv 108,34,168
Ln v8,32,s65,3328
Nvvv 112,30,152,72
Mvvv 112,34,172
W_ 0
Mvvv 112,38,192
Dv 30,%0,8192
Mvvv 112,42,212
Dv 34,v1,8192
Mvvv 108,38,188
Mvvv 108,42,208
Mvvv 104,38,184
Mvvv 104,42,204
Mvvv 100,38,180
Mvvv 100,42,200
Mvvv 96,38,176
Mvvv 96,42,196
W_ 0
Mvvv 96,30,216
Dv 38,%0,0
Mvvv 96,34,236
Dv 42,v1,0
Mvvv 100,30,220
Mvvv 100,34,240
Mvvv 104,30,224
Mvvv 104,34,244
Mvvv 108,30,228
Mvvv 108,34,248
s_nop 0
E_ 96
Mvvv 112,30,232
E_ 97
Mvvv 112,34,252
W_ 0
E_ 98
Nvvv 76,38,136,56
Dv 30,%0,1024
Mvvv 76,42,156
Dv 34,v1,1024
Nvvv 80,38,140,60
E_ 99
Mvvv 80,42,160
C_ 99,98,99
C_ 98,96,97
Nvvv 84,38,144,64
E_ 100
Mvvv 84,42,164
E_ 101
Nvvv 88,38,148,68
E_ 102
Mvvv 88,42,168
E_ 103
Nvvv 92,38,152,72
C_ 103,102,103
C_ 102,100,101
Mvvv 92,42,172
W_ 0
E_ 104
Mvvv 92,30,192
Dv 38,%0,2048
Mvvv 92,34,212
Dv 42,v1,2048
Mvvv 88,30,188
E_ 105
Mvvv 88,34,208
E_ 106
Mvvv 84,30,184
S_ s65,s61,172032
E_ 107
Mvvv 84,34,204
S_ m0,s51,0
C_ 107,106,107
C_ 106,104,105
Mvvv 80,30,180
s_waitcnt vmcnt(6) lgkmcnt(0)
s_barrier
Mvvv 80,34,200
Ln v9,36,s65,0
Mvvv 76,30,176
E_ 108
Mvvv 76,34,196
Ln v9,36,s65,3072
S_ m0,s51,896
Mvvv 76,38,216
Tv 30,v2,12288
Tv 32,v2,18432
Mvvv 76,42,236
Ln v9,36,s65,128
Mvvv 80,38,220
s_nop 0
Tv 34,v3,12288
Tv 36,v3,18432
Mvvv 80,42,240
Ln v9,36,s65,3200
S_ m0,s51,1792
Mvvv 84,38,224
E_ 109
Mvvv 84,42,244
Ln v9,36,s65,256
Mvvv 88,38,228
E_ 110
Mvvv 88,42,248
Ln v9,36,s65,3328
Mvvv 92,38,232
E_ 111
Mvvv 92,42,252
W_ 0
C_ 111,110,111
C_ 110,108,109
Mavv 0,30,116
s_nop 0
Tv 38,v4,12288
Tv 40,v4,18432
Mavv 20,34,116
E_ 112
Mavv 24,34,120
s_nop 0
Tv 42,v5,12288
Tv 44,v5,18432
Mavv 4,30,120
E_ 113
Mavv 8,30,124
E_ 114
Mavv 28,34,124
E_ 115
Mavv 32,34,128
C_ 115,114,115
C_ 114,112,113
Mavv 12,30,128
E_ 76
Mavv 16,30,132
E_ 77
Mavv 36,34,132
W_ 0
E_ 78
Mavv 56,38,132
s_nop 0
Tv 30,v2,13312
Tv 32,v2,19456
Mavv 76,42,132
E_ 79
Mavv 72,42,128
s_nop 0
Tv 34,v3,13312
Tv 36,v3,19456
Mavv 52,38,128
C_ 96,76,77
C_ 97,78,79
Mavv 48,38,124
E_ 80
Mavv 68,42,124
E_ 81
Mavv 64,42,120
E_ 82
Mavv 44,38,120
E_ 83
Mavv 40,38,116
C_ 100,80,81
C_ 101,82,83
Mavv 60,42,116
W_ 0
E_ 84
Mavv 80,30,116
s_nop 0
Tv 38,v4,13312
Tv 40,v4,19456
Mavv 100,34,116
E_ 85
Mavv 104,34,120
s_nop 0
Tv 42,v5,13312
Tv 44,v5,19456
Mavv 84,30,120
E_ 86
Mavv 88,30,124
E_ 87
Mavv 108,34,124
C_ 104,84,85
C_ 105,86,87
Mavv 112,34,128
E_ 88
Mavv 92,30,128
E_ 89
Mavv 96,30,132
E_ 90
Mavv 116,34,132
W_ 0
E_ 91
Mavv 136,38,132
s_nop 0
Tv 30,v2,14336
Tv 32,v2,20480
Mavv 156,42,132
C_ 108,88,89
C_ 109,90,91
Mavv 152,42,128
s_nop 0
Tv 34,v3,14336
Tv 36,v3,20480
Mavv 132,38,128
E_ 92
Mavv 128,38,124
E_ 93
Mavv 148,42,124
E_ 94
Mavv 144,42,120
E_ 95
Mavv 124,38,120
C_ 112,92,93
C_ 113,94,95
Mavv 120,38,116
v_mov_b32 v92, 0x3f803f80
v_mov_b32 v93, 0x3f803f80
Mavv 140,42,116
W_ 0
v_mov_b32 v94, 0x3f803f80
v_mov_b32 v95, 0x3f803f80
Mavv 160,30,116
s_nop 0
Tv 38,v4,14336
Tv 40,v4,20480
Mavv 180,34,116
Mavv 184,34,120
s_nop 0
Tv 42,v5,14336
Tv 44,v5,20480
Mavv 164,30,120
Mavv 168,30,124
Mavv 188,34,124
S_ s65,s61,184320
Mavv 192,34,128
S_ m0,s51,12288
Mavv 172,30,128
s_waitcnt vmcnt(6) lgkmcnt(0)
s_barrier
Mavv 176,30,132
Ln v9,36,s65,0
Mavv 196,34,132
Mavv 216,38,132
Dv 30,%0,30720
Mavv 236,42,132
Dv 34,v1,30720
Mavv 232,42,128
Ln v9,36,s65,3072
S_ m0,s51,13184
Mavv 212,38,128
Mavv 208,38,124
Ln v9,36,s65,128
Mavv 228,42,124
Mavv 224,42,120
Ln v9,36,s65,3200
S_ m0,s51,14080
Mavv 204,38,120
Mavv 200,38,116
Ln v9,36,s65,256
Mavv 220,42,116
.Lstep1_15:
Zvvv 76,92,96
Dv 38,%0,31744
Zvvv 80,92,100
Dv 42,v1,31744
Zvvv 84,92,104
Zvvv 88,92,108
Zvvv 92,92,112
W_ 2
Nvvv 116,30,136,56
Mvvv 116,34,156
Nvvv 120,30,140,60
s_nop 1
v_max3_f32 v10, v76, v80, v84
s_nop 0
v_max3_f32 v10, v10, v88, v92
Mvvv 120,34,160
v_cmp_lt_f32 vcc, 0x42000000, v10
s_cbranch_vccnz .Ltramp1_14
Nvvv 124,30,144,64
A_ v46,v46,v76
A_ v47,v47,v80
Mvvv 124,34,164
A_ v48,v48,v84
A_ v49,v49,v88
Nvvv 128,30,148,68
A_ v50,v50,v92
Mvvv 128,34,168
Ln v9,36,s65,3328
Nvvv 132,30,152,72
Mvvv 132,34,172
W_ 0
Mvvv 132,38,192
Dv 30,%0,32768
Mvvv 132,42,212
Dv 34,v1,32768
Mvvv 128,38,188
Mvvv 128,42,208
Mvvv 124,38,184
Mvvv 124,42,204
Mvvv 120,38,180
Mvvv 120,42,200
Mvvv 116,38,176
Mvvv 116,42,196
W_ 0
Mvvv 116,30,216
Dv 38,%0,24576
Mvvv 116,34,236
Dv 42,v1,24576
Mvvv 120,30,220
Mvvv 120,34,240
Mvvv 124,30,224
Mvvv 124,34,244
Mvvv 128,30,228
Mvvv 128,34,248
s_nop 0
E_ 116
Mvvv 132,30,232
E_ 117
Mvvv 132,34,252
W_ 0
E_ 118
Nvvv 76,38,136,56
Dv 30,%0,25600
Mvvv 76,42,156
Dv 34,v1,25600
Nvvv 80,38,140,60
E_ 119
Mvvv 80,42,160
C_ 119,118,119
C_ 118,116,117
Nvvv 84,38,144,64
E_ 120
Mvvv 84,42,164
E_ 121
Nvvv 88,38,148,68
E_ 122
Mvvv 88,42,168
E_ 123
Nvvv 92,38,152,72
C_ 123,122,123
C_ 122,120,121
Mvvv 92,42,172
W_ 0
E_ 124
Mvvv 92,30,192
Dv 38,%0,26624
Mvvv 92,34,212
Dv 42,v1,26624
Mvvv 88,30,188
E_ 125
Mvvv 88,34,208
E_ 126
Mvvv 84,30,184
E_ 127
Mvvv 84,34,204
C_ 127,126,127
C_ 126,124,125
Mvvv 80,30,180
s_waitcnt vmcnt(6) lgkmcnt(0)
s_barrier
Mvvv 80,34,200
R_ v7,a248
Mvvv 76,30,176
v_lshrrev_b32 v7, 7, v7
Mvvv 76,34,196
v_mul_u32_u24 v7, 896, v7
Mvvv 76,38,216
s_nop 0
Tv 30,v2,0
Tv 32,v2,6144
Mvvv 76,42,236
v_add_u32 v7, v7, v9
Mvvv 80,38,220
S_ s71,s72,1024
Tv 34,v3,0
Tv 36,v3,6144
Mvvv 80,42,240
S_ s55,s71,491520
E_ 128
Mvvv 84,38,224
S_ m0,s51,24576
E_ 129
Mvvv 84,42,244
L_ v7,24,s55,0
Mvvv 88,38,228
S_ s55,s71,501760
E_ 130
Mvvv 88,42,248
S_ m0,s51,25600
E_ 131
Mvvv 92,38,232
L_ v7,24,s55,0
Mvvv 92,42,252
S_ s55,s71,512000
C_ 131,130,131
C_ 130,128,129
W_ 2
Mavv 0,30,96
S_ m0,s51,26624
Tv 38,v4,0
Tv 40,v4,6144
W_ 2
Mavv 20,34,96
L_ v7,24,s55,0
Mavv 24,34,100
S_ s55,s71,522240
Tv 42,v5,0
Tv 44,v5,6144
Mavv 4,30,100
S_ m0,s51,27648
E_ 132
Mavv 8,30,104
L_ v7,24,s55,0
Mavv 28,34,104
S_ s55,s71,532480
E_ 133
Mavv 32,34,108
S_ m0,s51,28672
E_ 134
Mavv 12,30,108
L_ v7,24,s55,0
Mavv 16,30,112
S_ s55,s71,542720
E_ 135
Mavv 36,34,112
S_ m0,s51,29696
C_ 135,134,135
C_ 134,132,133
W_ 2
Mavv 56,38,112
Tv 30,v2,1024
Tv 32,v2,7168
W_ 2
Mavv 76,42,112
L_ v7,24,s55,0
Mavv 72,42,108
Tv 34,v3,1024
Tv 36,v3,7168
Mavv 52,38,108
E_ 76
Mavv 48,38,104
E_ 77
Mavv 68,42,104
E_ 78
Mavv 64,42,100
E_ 79
Mavv 44,38,100
C_ 116,76,77
C_ 117,78,79
Mavv 40,38,96
E_ 80
Mavv 60,42,96
W_ 0
E_ 81
Mavv 80,30,96
s_nop 0
Tv 38,v4,1024
Tv 40,v4,7168
Mavv 100,34,96
E_ 82
Mavv 104,34,100
s_nop 0
Tv 42,v5,1024
Tv 44,v5,7168
Mavv 84,30,100
E_ 83
Mavv 88,30,104
C_ 120,80,81
C_ 121,82,83
Mavv 108,34,104
E_ 84
Mavv 112,34,108
E_ 85
Mavv 92,30,108
E_ 86
Mavv 96,30,112
E_ 87
Mavv 116,34,112
W_ 0
C_ 124,84,85
C_ 125,86,87
Mavv 136,38,112
s_nop 0
Tv 30,v2,2048
Tv 32,v2,8192
Mavv 156,42,112
E_ 88
Mavv 152,42,108
s_nop 0
Tv 34,v3,2048
Tv 36,v3,8192
Mavv 132,38,108
E_ 89
Mavv 128,38,104
E_ 90
Mavv 148,42,104
E_ 91
Mavv 144,42,100
C_ 128,88,89
C_ 129,90,91
Mavv 124,38,100
E_ 92
Mavv 120,38,96
E_ 93
Mavv 140,42,96
W_ 0
E_ 94
Mavv 160,30,96
s_nop 0
Tv 38,v4,2048
Tv 40,v4,8192
Mavv 180,34,96
E_ 95
Mavv 184,34,100
s_nop 0
Tv 42,v5,2048
Tv 44,v5,8192
Mavv 164,30,100
C_ 132,92,93
C_ 133,94,95
Mavv 168,30,104
v_mov_b32 v92, 0x3f803f80
v_mov_b32 v93, 0x3f803f80
Mavv 188,34,104
v_mov_b32 v94, 0x3f803f80
v_mov_b32 v95, 0x3f803f80
Mavv 192,34,108
R_ v11,a248
Mavv 172,30,108
s_waitcnt vmcnt(6) lgkmcnt(0)
s_barrier
v_mul_u32_u24 v11, 80, v11
Mavv 176,30,112
S_ s55,s71,614400
v_min_u32 v11, 29440, v11
Mavv 196,34,112
S_ m0,s51,0
S_ s85,s87,1024
Mavv 216,38,112
s_nop 0
Tv 30,v2,12288
Tv 32,v2,18432
Zvvv 76,92,116
L_ v7,24,s55,0
Zvvv 80,92,120
S_ s55,s71,624640
Tv 34,v3,12288
Tv 36,v3,18432
Zvvv 84,92,124
buffer_load_dword a254, v11, s[24:27], s85 offen
Zvvv 88,92,128
S_ m0,s51,1024
S_ s85,s87,1152
Zvvv 92,92,132
L_ v7,24,s55,0
Mavv 236,42,112
S_ s55,s71,634880
Mavv 232,42,108
buffer_load_dword a254, v11, s[24:27], s85 offen
Mavv 212,38,108
S_ m0,s51,2048
S_ s85,s6,1
Mavv 208,38,104
s_and_b32 s85, s85, 7
v_max3_f32 v10, v76, v80, v84
Mavv 228,42,104
s_lshl_b32 s85, s85, 7
v_max3_f32 v10, v10, v88, v92
v_cmp_lt_f32 vcc, 0x42000000, v10
Mavv 224,42,100
L_ v7,24,s55,0
Mavv 204,38,100
S_ s55,s71,645120
S_ s85,s85,s87
Mavv 200,38,96
buffer_load_dword a254, v11, s[24:27], s85 offen
Mavv 220,42,96
S_ m0,s51,3072
s_cbranch_vccnz .Ltramp1_15
.Ljoin1:
W_ 2
Mavv 0,30,116
s_nop 0
Tv 38,v4,12288
Tv 40,v4,18432
W_ 2
Mavv 20,34,116
L_ v7,24,s55,0
Mavv 24,34,120
S_ s55,s71,655360
Tv 42,v5,12288
Tv 44,v5,18432
Mavv 4,30,120
S_ m0,s51,4096
R_ v13,a248
S_ s53,s51,36864
Mavv 8,30,124
L_ v7,24,s55,0
Mavv 28,34,124
S_ s55,s71,665600
v_add_u32 v12, s53, v13
s_mul_i32 s53, s4, 10240
Mavv 32,34,128
S_ m0,s51,5120
v_add_u32 v13, s53, v13
R_ v10,a248
Mavv 12,30,128
L_ v7,24,s55,0
Mavv 16,30,132
A_ v46,v46,v76
A_ v47,v47,v80
Mavv 36,34,132
buffer_load_dword v11, off, s[92:95], s96 offset:0 sc1
W_ 2
Mavv 56,38,132
W_ 0
Tv 30,v2,13312
Tv 32,v2,19456
Mavv 76,42,132
A_ v48,v48,v84
A_ v49,v49,v88
Mavv 72,42,128
s_nop 0
Tv 34,v3,13312
Tv 36,v3,19456
Mavv 52,38,128
A_ v50,v50,v92
Mavv 48,38,124
v_rcp_f32 v46, v46
Mavv 68,42,124
v_rcp_f32 v47, v47
Mavv 64,42,120
v_rcp_f32 v48, v48
Mavv 44,38,120
v_rcp_f32 v49, v49
Mavv 40,38,116
v_rcp_f32 v50, v50
Mavv 60,42,116
W_ 0
R_ v96,a0
R_ v97,a1
Mavv 80,30,116
s_nop 0
Tv 38,v4,13312
Tv 40,v4,19456
Mavv 100,34,116
R_ v98,a2
R_ v99,a3
Mavv 104,34,120
s_nop 0
Tv 42,v5,13312
Tv 44,v5,19456
Mavv 84,30,120
R_ v100,a20
R_ v101,a21
Mavv 88,30,124
R_ v102,a22
R_ v103,a23
Mavv 108,34,124
U_ v96,v96,v46
U_ v97,v97,v46
Mavv 112,34,128
U_ v98,v98,v46
U_ v99,v99,v46
Mavv 92,30,128
U_ v100,v100,v46
U_ v101,v101,v46
Mavv 96,30,132
U_ v102,v102,v46
U_ v103,v103,v46
Mavv 116,34,132
W_ 0
C_ 136,96,97
C_ 137,98,99
Mavv 136,38,132
s_nop 0
Tv 30,v2,14336
Tv 32,v2,20480
Mavv 156,42,132
C_ 138,100,101
C_ 139,102,103
Mavv 152,42,128
s_nop 0
Tv 34,v3,14336
Tv 36,v3,20480
Mavv 132,38,128
Da 0,v13,0
Mavv 128,38,124
R_ v104,a4
R_ v105,a5
Mavv 148,42,124
R_ v106,a6
R_ v107,a7
Mavv 144,42,120
R_ v108,a24
R_ v109,a25
Mavv 124,38,120
R_ v110,a26
R_ v111,a27
Mavv 120,38,116
U_ v104,v104,v47
U_ v105,v105,v47
Mavv 140,42,116
W_ 1
U_ v106,v106,v47
U_ v107,v107,v47
Mavv 160,30,116
s_nop 0
Tv 38,v4,14336
Tv 40,v4,20480
Mavv 180,34,116
U_ v108,v108,v47
U_ v109,v109,v47
Mavv 184,34,120
s_nop 0
Tv 42,v5,14336
Tv 44,v5,20480
Mavv 164,30,120
U_ v110,v110,v47
U_ v111,v111,v47
Mavv 168,30,124
C_ 140,104,105
C_ 141,106,107
Mavv 188,34,124
C_ 142,108,109
C_ 143,110,111
Mavv 192,34,128
Da 4,v13,1024
Mavv 172,30,128
S_ s55,s71,0
R_ v96,a8
R_ v97,a9
Mavv 176,30,132
S_ m0,s51,12288
R_ v98,a10
R_ v99,a11
Mavv 196,34,132
s_waitcnt vmcnt(63) lgkmcnt(0)
s_barrier
R_ v100,a28
Mavv 216,38,132
L_ v7,24,s55,0
S_ s55,s71,10240
S_ m0,s51,13312
Mavv 236,42,132
R_ v101,a29
Mavv 232,42,128
L_ v7,24,s55,0
S_ s55,s71,20480
S_ m0,s51,14336
Mavv 212,38,128
R_ v102,a30
R_ v103,a31
Mavv 208,38,124
L_ v7,24,s55,0
S_ s55,s71,30720
S_ m0,s51,15360
Mavv 228,42,124
U_ v96,v96,v48
U_ v97,v97,v48
Mavv 224,42,120
L_ v7,24,s55,0
S_ s55,s71,40960
S_ m0,s51,16384
Mavv 204,38,120
U_ v98,v98,v48
U_ v99,v99,v48
Mavv 200,38,116
L_ v7,24,s55,0
S_ s55,s71,51200
S_ m0,s51,17408
Mavv 220,42,116
U_ v100,v100,v48
U_ v101,v101,v48
U_ v102,v102,v48
U_ v103,v103,v48
C_ 144,96,97
C_ 145,98,99
C_ 146,100,101
C_ 147,102,103
Da 8,v13,2048
L_ v7,24,s55,0
.p2align 3
R_ v104,a12
R_ v105,a13
R_ v106,a14
R_ v107,a15
R_ v108,a32
R_ v109,a33
R_ v110,a34
R_ v111,a35
v_pk_mul_f32 v[104:105], v[104:105], v[48:49] op_sel:[0,1]
v_pk_mul_f32 v[106:107], v[106:107], v[48:49] op_sel:[0,1]
v_pk_mul_f32 v[108:109], v[108:109], v[48:49] op_sel:[0,1]
v_pk_mul_f32 v[110:111], v[110:111], v[48:49] op_sel:[0,1]
C_ 148,104,105
C_ 149,106,107
C_ 150,108,109
C_ 151,110,111
Da 12,v13,3072
.p2align 3
R_ v96,a16
R_ v97,a17
R_ v98,a18
R_ v99,a19
R_ v100,a36
R_ v101,a37
R_ v102,a38
R_ v103,a39
v_pk_mul_f32 v[96:97], v[96:97], v[50:51] op_sel_hi:[1,0]
v_pk_mul_f32 v[98:99], v[98:99], v[50:51] op_sel_hi:[1,0]
v_pk_mul_f32 v[100:101], v[100:101], v[50:51] op_sel_hi:[1,0]
v_pk_mul_f32 v[102:103], v[102:103], v[50:51] op_sel_hi:[1,0]
C_ 152,96,97
C_ 153,98,99
C_ 154,100,101
C_ 155,102,103
Da 16,v13,4096
.p2align 3
R_ v104,a40
R_ v105,a41
R_ v106,a42
R_ v107,a43
R_ v108,a60
R_ v109,a61
R_ v110,a62
R_ v111,a63
v_pk_mul_f32 v[104:105], v[104:105], v[46:47] op_sel_hi:[1,0]
v_pk_mul_f32 v[106:107], v[106:107], v[46:47] op_sel_hi:[1,0]
v_pk_mul_f32 v[108:109], v[108:109], v[46:47] op_sel_hi:[1,0]
v_pk_mul_f32 v[110:111], v[110:111], v[46:47] op_sel_hi:[1,0]
C_ 156,104,105
C_ 157,106,107
C_ 158,108,109
C_ 159,110,111
Da 40,v13,5120
.p2align 3
R_ v96,a44
R_ v97,a45
R_ v98,a46
R_ v99,a47
R_ v100,a64
R_ v101,a65
R_ v102,a66
R_ v103,a67
v_pk_mul_f32 v[96:97], v[96:97], v[46:47] op_sel:[0,1]
v_pk_mul_f32 v[98:99], v[98:99], v[46:47] op_sel:[0,1]
v_pk_mul_f32 v[100:101], v[100:101], v[46:47] op_sel:[0,1]
v_pk_mul_f32 v[102:103], v[102:103], v[46:47] op_sel:[0,1]
C_ 160,96,97
C_ 161,98,99
C_ 162,100,101
C_ 163,102,103
Da 44,v13,6144
.p2align 3
R_ v104,a48
R_ v105,a49
R_ v106,a50
R_ v107,a51
R_ v108,a68
R_ v109,a69
R_ v110,a70
R_ v111,a71
v_pk_mul_f32 v[104:105], v[104:105], v[48:49] op_sel_hi:[1,0]
v_pk_mul_f32 v[106:107], v[106:107], v[48:49] op_sel_hi:[1,0]
v_pk_mul_f32 v[108:109], v[108:109], v[48:49] op_sel_hi:[1,0]
v_pk_mul_f32 v[110:111], v[110:111], v[48:49] op_sel_hi:[1,0]
C_ 164,104,105
C_ 165,106,107
C_ 166,108,109
C_ 167,110,111
Da 48,v13,7168
.p2align 3
R_ v96,a52
R_ v97,a53
R_ v98,a54
R_ v99,a55
R_ v100,a72
R_ v101,a73
R_ v102,a74
R_ v103,a75
v_pk_mul_f32 v[96:97], v[96:97], v[48:49] op_sel:[0,1]
v_pk_mul_f32 v[98:99], v[98:99], v[48:49] op_sel:[0,1]
v_pk_mul_f32 v[100:101], v[100:101], v[48:49] op_sel:[0,1]
v_pk_mul_f32 v[102:103], v[102:103], v[48:49] op_sel:[0,1]
C_ 168,96,97
C_ 169,98,99
C_ 170,100,101
C_ 171,102,103
Da 52,v13,8192
.p2align 3
R_ v104,a56
R_ v105,a57
R_ v106,a58
R_ v107,a59
R_ v108,a76
R_ v109,a77
R_ v110,a78
R_ v111,a79
v_pk_mul_f32 v[104:105], v[104:105], v[50:51] op_sel_hi:[1,0]
v_pk_mul_f32 v[106:107], v[106:107], v[50:51] op_sel_hi:[1,0]
v_pk_mul_f32 v[108:109], v[108:109], v[50:51] op_sel_hi:[1,0]
v_pk_mul_f32 v[110:111], v[110:111], v[50:51] op_sel_hi:[1,0]
C_ 172,104,105
C_ 173,106,107
C_ 174,108,109
C_ 175,110,111
Da 56,v13,9216
.p2align 3
R_ v96,a80
R_ v97,a81
R_ v98,a82
R_ v99,a83
R_ v100,a100
R_ v101,a101
R_ v102,a102
R_ v103,a103
v_pk_mul_f32 v[96:97], v[96:97], v[46:47] op_sel_hi:[1,0]
v_pk_mul_f32 v[98:99], v[98:99], v[46:47] op_sel_hi:[1,0]
v_pk_mul_f32 v[100:101], v[100:101], v[46:47] op_sel_hi:[1,0]
v_pk_mul_f32 v[102:103], v[102:103], v[46:47] op_sel_hi:[1,0]
C_ 176,96,97
C_ 177,98,99
C_ 178,100,101
C_ 179,102,103
X_ a80,v20
X_ a81,v21
X_ a82,v22
X_ a83,v23
.p2align 3
R_ v104,a84
R_ v105,a85
R_ v106,a86
R_ v107,a87
R_ v108,a104
R_ v109,a105
R_ v110,a106
R_ v111,a107
v_pk_mul_f32 v[104:105], v[104:105], v[46:47] op_sel:[0,1]
v_pk_mul_f32 v[106:107], v[106:107], v[46:47] op_sel:[0,1]
v_pk_mul_f32 v[108:109], v[108:109], v[46:47] op_sel:[0,1]
v_pk_mul_f32 v[110:111], v[110:111], v[46:47] op_sel:[0,1]
C_ 180,104,105
C_ 181,106,107
C_ 182,108,109
C_ 183,110,111
X_ a84,v24
X_ a85,v25
X_ a86,v26
X_ a87,v27
.p2align 3
R_ v96,a88
R_ v97,a89
R_ v98,a90
R_ v99,a91
R_ v100,a108
R_ v101,a109
R_ v102,a110
R_ v103,a111
v_pk_mul_f32 v[96:97], v[96:97], v[48:49] op_sel_hi:[1,0]
v_pk_mul_f32 v[98:99], v[98:99], v[48:49] op_sel_hi:[1,0]
v_pk_mul_f32 v[100:101], v[100:101], v[48:49] op_sel_hi:[1,0]
v_pk_mul_f32 v[102:103], v[102:103], v[48:49] op_sel_hi:[1,0]
C_ 184,96,97
C_ 185,98,99
C_ 186,100,101
C_ 187,102,103
X_ a88,v52
X_ a89,v53
X_ a90,v54
X_ a91,v55
.p2align 3
R_ v104,a92
R_ v105,a93
R_ v106,a94
R_ v107,a95
R_ v108,a112
R_ v109,a113
R_ v110,a114
R_ v111,a115
v_pk_mul_f32 v[104:105], v[104:105], v[48:49] op_sel:[0,1]
v_pk_mul_f32 v[106:107], v[106:107], v[48:49] op_sel:[0,1]
v_pk_mul_f32 v[108:109], v[108:109], v[48:49] op_sel:[0,1]
v_pk_mul_f32 v[110:111], v[110:111], v[48:49] op_sel:[0,1]
C_ 188,104,105
C_ 189,106,107
C_ 190,108,109
C_ 191,110,111
.p2align 3
R_ v96,a96
R_ v97,a97
R_ v98,a98
R_ v99,a99
R_ v100,a116
R_ v101,a117
R_ v102,a118
R_ v103,a119
v_pk_mul_f32 v[96:97], v[96:97], v[50:51] op_sel_hi:[1,0]
v_pk_mul_f32 v[98:99], v[98:99], v[50:51] op_sel_hi:[1,0]
v_pk_mul_f32 v[100:101], v[100:101], v[50:51] op_sel_hi:[1,0]
v_pk_mul_f32 v[102:103], v[102:103], v[50:51] op_sel_hi:[1,0]
C_ 192,96,97
C_ 193,98,99
C_ 194,100,101
C_ 195,102,103
.p2align 3
R_ v104,a120
R_ v105,a121
R_ v106,a122
R_ v107,a123
R_ v108,a140
R_ v109,a141
R_ v110,a142
R_ v111,a143
v_pk_mul_f32 v[104:105], v[104:105], v[46:47] op_sel_hi:[1,0]
v_pk_mul_f32 v[106:107], v[106:107], v[46:47] op_sel_hi:[1,0]
v_pk_mul_f32 v[108:109], v[108:109], v[46:47] op_sel_hi:[1,0]
v_pk_mul_f32 v[110:111], v[110:111], v[46:47] op_sel_hi:[1,0]
C_ 196,104,105
C_ 197,106,107
C_ 198,108,109
C_ 199,110,111
X_ a120,v16
X_ a121,v17
X_ a122,v18
X_ a123,v19
.p2align 3
R_ v96,a124
R_ v97,a125
R_ v98,a126
R_ v99,a127
R_ v100,a144
R_ v101,a145
R_ v102,a146
R_ v103,a147
v_pk_mul_f32 v[96:97], v[96:97], v[46:47] op_sel:[0,1]
v_pk_mul_f32 v[98:99], v[98:99], v[46:47] op_sel:[0,1]
v_pk_mul_f32 v[100:101], v[100:101], v[46:47] op_sel:[0,1]
v_pk_mul_f32 v[102:103], v[102:103], v[46:47] op_sel:[0,1]
C_ 200,96,97
C_ 201,98,99
C_ 202,100,101
C_ 203,102,103
X_ a124,v28
X_ a125,v29
X_ a126,v51
X_ a127,v6
.p2align 3
R_ v104,a128
R_ v105,a129
R_ v106,a130
R_ v107,a131
R_ v108,a148
R_ v109,a149
R_ v110,a150
R_ v111,a151
v_pk_mul_f32 v[104:105], v[104:105], v[48:49] op_sel_hi:[1,0]
v_pk_mul_f32 v[106:107], v[106:107], v[48:49] op_sel_hi:[1,0]
v_pk_mul_f32 v[108:109], v[108:109], v[48:49] op_sel_hi:[1,0]
v_pk_mul_f32 v[110:111], v[110:111], v[48:49] op_sel_hi:[1,0]
C_ 204,104,105
C_ 205,106,107
C_ 206,108,109
C_ 207,110,111
X_ a128,v14
X_ a129,v15
v_accvgpr_mov_b32 a130, a249
v_accvgpr_mov_b32 a131, a250
.p2align 3
R_ v96,a132
R_ v97,a133
R_ v98,a134
R_ v99,a135
R_ v100,a152
R_ v101,a153
R_ v102,a154
R_ v103,a155
v_pk_mul_f32 v[96:97], v[96:97], v[48:49] op_sel:[0,1]
v_pk_mul_f32 v[98:99], v[98:99], v[48:49] op_sel:[0,1]
v_pk_mul_f32 v[100:101], v[100:101], v[48:49] op_sel:[0,1]
v_pk_mul_f32 v[102:103], v[102:103], v[48:49] op_sel:[0,1]
C_ 208,96,97
C_ 209,98,99
C_ 210,100,101
C_ 211,102,103
Da 132,v12,0
.p2align 3
R_ v104,a136
R_ v105,a137
R_ v106,a138
R_ v107,a139
R_ v108,a156
R_ v109,a157
R_ v110,a158
R_ v111,a159
v_pk_mul_f32 v[104:105], v[104:105], v[50:51] op_sel_hi:[1,0]
v_pk_mul_f32 v[106:107], v[106:107], v[50:51] op_sel_hi:[1,0]
v_pk_mul_f32 v[108:109], v[108:109], v[50:51] op_sel_hi:[1,0]
v_pk_mul_f32 v[110:111], v[110:111], v[50:51] op_sel_hi:[1,0]
C_ 212,104,105
C_ 213,106,107
C_ 214,108,109
C_ 215,110,111
Da 136,v12,1024
.p2align 3
R_ v96,a160
R_ v97,a161
R_ v98,a162
R_ v99,a163
R_ v100,a180
R_ v101,a181
R_ v102,a182
R_ v103,a183
v_pk_mul_f32 v[96:97], v[96:97], v[46:47] op_sel_hi:[1,0]
v_pk_mul_f32 v[98:99], v[98:99], v[46:47] op_sel_hi:[1,0]
v_pk_mul_f32 v[100:101], v[100:101], v[46:47] op_sel_hi:[1,0]
v_pk_mul_f32 v[102:103], v[102:103], v[46:47] op_sel_hi:[1,0]
C_ 216,96,97
C_ 217,98,99
C_ 218,100,101
C_ 219,102,103
Da 160,v12,2048
.p2align 3
R_ v104,a164
R_ v105,a165
R_ v106,a166
R_ v107,a167
R_ v108,a184
R_ v109,a185
R_ v110,a186
R_ v111,a187
v_pk_mul_f32 v[104:105], v[104:105], v[46:47] op_sel:[0,1]
v_pk_mul_f32 v[106:107], v[106:107], v[46:47] op_sel:[0,1]
v_pk_mul_f32 v[108:109], v[108:109], v[46:47] op_sel:[0,1]
v_pk_mul_f32 v[110:111], v[110:111], v[46:47] op_sel:[0,1]
C_ 220,104,105
C_ 221,106,107
C_ 222,108,109
C_ 223,110,111
Da 164,v12,3072
.p2align 3
R_ v96,a168
R_ v97,a169
R_ v98,a170
R_ v99,a171
R_ v100,a188
R_ v101,a189
R_ v102,a190
R_ v103,a191
v_pk_mul_f32 v[96:97], v[96:97], v[48:49] op_sel_hi:[1,0]
v_pk_mul_f32 v[98:99], v[98:99], v[48:49] op_sel_hi:[1,0]
v_pk_mul_f32 v[100:101], v[100:101], v[48:49] op_sel_hi:[1,0]
v_pk_mul_f32 v[102:103], v[102:103], v[48:49] op_sel_hi:[1,0]
C_ 224,96,97
C_ 225,98,99
C_ 226,100,101
C_ 227,102,103
Da 168,v12,4096
.p2align 3
R_ v104,a172
R_ v105,a173
R_ v106,a174
R_ v107,a175
R_ v108,a192
R_ v109,a193
R_ v110,a194
R_ v111,a195
v_pk_mul_f32 v[104:105], v[104:105], v[48:49] op_sel:[0,1]
v_pk_mul_f32 v[106:107], v[106:107], v[48:49] op_sel:[0,1]
v_pk_mul_f32 v[108:109], v[108:109], v[48:49] op_sel:[0,1]
v_pk_mul_f32 v[110:111], v[110:111], v[48:49] op_sel:[0,1]
C_ 228,104,105
C_ 229,106,107
C_ 230,108,109
C_ 231,110,111
Da 172,v12,5120
.p2align 3
R_ v96,a176
R_ v97,a177
R_ v98,a178
R_ v99,a179
R_ v100,a196
R_ v101,a197
R_ v102,a198
R_ v103,a199
v_pk_mul_f32 v[96:97], v[96:97], v[50:51] op_sel_hi:[1,0]
v_pk_mul_f32 v[98:99], v[98:99], v[50:51] op_sel_hi:[1,0]
v_pk_mul_f32 v[100:101], v[100:101], v[50:51] op_sel_hi:[1,0]
v_pk_mul_f32 v[102:103], v[102:103], v[50:51] op_sel_hi:[1,0]
C_ 232,96,97
C_ 233,98,99
C_ 234,100,101
C_ 235,102,103
Da 176,v12,12288
.p2align 3
R_ v104,a200
R_ v105,a201
R_ v106,a202
R_ v107,a203
R_ v108,a220
R_ v109,a221
R_ v110,a222
R_ v111,a223
v_pk_mul_f32 v[104:105], v[104:105], v[46:47] op_sel_hi:[1,0]
v_pk_mul_f32 v[106:107], v[106:107], v[46:47] op_sel_hi:[1,0]
v_pk_mul_f32 v[108:109], v[108:109], v[46:47] op_sel_hi:[1,0]
v_pk_mul_f32 v[110:111], v[110:111], v[46:47] op_sel_hi:[1,0]
C_ 236,104,105
C_ 237,106,107
C_ 238,108,109
C_ 239,110,111
Da 200,v12,13312
.p2align 3
R_ v96,a204
R_ v97,a205
R_ v98,a206
R_ v99,a207
R_ v100,a224
R_ v101,a225
R_ v102,a226
R_ v103,a227
v_pk_mul_f32 v[96:97], v[96:97], v[46:47] op_sel:[0,1]
v_pk_mul_f32 v[98:99], v[98:99], v[46:47] op_sel:[0,1]
v_pk_mul_f32 v[100:101], v[100:101], v[46:47] op_sel:[0,1]
v_pk_mul_f32 v[102:103], v[102:103], v[46:47] op_sel:[0,1]
C_ 240,96,97
C_ 241,98,99
C_ 242,100,101
C_ 243,102,103
Da 204,v12,14336
v_mov_b32 v6, v10
v_lshrrev_b32 v10, 9, v6
v_lshlrev_b32 v10, 11, v10
v_sub_u32 %0, v2, v10
v_xor_b32 %0, s83, %0
s_waitcnt vmcnt(16) lgkmcnt(0)
s_barrier
Ta 180,%0,24576
Ta 182,%0,26624
Ta 184,%0,28672
Ta 186,%0,30720
Ta 188,%0,32768
Ta 190,%0,34816
.p2align 3
R_ v104,a208
R_ v105,a209
R_ v106,a210
R_ v107,a211
R_ v108,a228
R_ v109,a229
R_ v110,a230
R_ v111,a231
v_pk_mul_f32 v[104:105], v[104:105], v[48:49] op_sel_hi:[1,0]
v_pk_mul_f32 v[106:107], v[106:107], v[48:49] op_sel_hi:[1,0]
v_pk_mul_f32 v[108:109], v[108:109], v[48:49] op_sel_hi:[1,0]
v_pk_mul_f32 v[110:111], v[110:111], v[48:49] op_sel_hi:[1,0]
C_ 244,104,105
C_ 245,106,107
C_ 246,108,109
C_ 247,110,111
Da 208,v12,15360
.p2align 3
R_ v96,a212
R_ v97,a213
R_ v98,a214
R_ v99,a215
R_ v100,a232
R_ v101,a233
R_ v102,a234
R_ v103,a235
v_pk_mul_f32 v[96:97], v[96:97], v[48:49] op_sel:[0,1]
v_pk_mul_f32 v[98:99], v[98:99], v[48:49] op_sel:[0,1]
v_pk_mul_f32 v[100:101], v[100:101], v[48:49] op_sel:[0,1]
v_pk_mul_f32 v[102:103], v[102:103], v[48:49] op_sel:[0,1]
C_ 248,96,97
C_ 249,98,99
C_ 250,100,101
C_ 251,102,103
Da 212,v12,16384
.p2align 3
R_ v104,a216
R_ v105,a217
R_ v106,a218
R_ v107,a219
R_ v108,a236
R_ v109,a237
R_ v110,a238
R_ v111,a239
v_pk_mul_f32 v[104:105], v[104:105], v[50:51] op_sel_hi:[1,0]
v_pk_mul_f32 v[106:107], v[106:107], v[50:51] op_sel_hi:[1,0]
v_pk_mul_f32 v[108:109], v[108:109], v[50:51] op_sel_hi:[1,0]
v_pk_mul_f32 v[110:111], v[110:111], v[50:51] op_sel_hi:[1,0]
C_ 252,104,105
C_ 253,106,107
C_ 254,108,109
C_ 255,110,111
Da 216,v12,17408
v_xor_b32 v1, 32, %0
W_ 7
Zvav 56,180,136
s_mul_i32 s76, s6, 204800
Ta 192,v1,24576
Ta 194,v1,26624
W_ 7
Mvav 56,184,156
v_xor_b32 v2, 64, %0
v_xor_b32 v3, 96, %0
W_ 5
Mvav 56,188,176
s_nop 0
Ta 196,v1,28672
Ta 198,v1,30720
Zvav 60,180,140
W_ 4
R_ v4,a248
v_mul_u32_u24 v4, 80, v4
Mvav 60,184,160
S_ s55,s71,122880
Ta 220,v1,32768
Ta 222,v1,34816
Mvav 60,188,180
S_ m0,s51,49152
v_min_u32 v4, 29440, v4
s_waitcnt vmcnt(6)
Zvav 64,180,144
v_readfirstlane_b32 s62, v11
v_lshrrev_b32 v8, 2, v6
Mvav 64,184,164
v_add_u32 v5, s75, v8
R_ v9,a251
Mvav 64,188,184
v_or_b32 v9, s73, v9
v_lshrrev_b32 v10, 8, v6
Zvav 68,180,148
v_bfe_u32 v12, v6, 6, 2
v_cmp_eq_u32 vcc, v10, v12
Mvav 68,184,168
v_and_b32 v10, 16, v6
v_mov_b32 v12, 0x3f80
Mvav 68,188,188
v_lshlrev_b32 v12, v10, v12
v_cndmask_b32 v12, 0, v12, vcc
Zvav 72,180,152
v_and_b32 v10, 32, v6
v_cmp_eq_u32 vcc, 0, v10
Mvav 72,184,172
X_ a68,0
X_ a69,0
Mvav 72,188,192
W_ 0
X_ a72,0
X_ a73,0
Zvav 92,220,192
s_nop 0
Ta 180,v2,24576
Ta 182,v2,26624
Mvav 92,196,172
v_cndmask_b32 v10, 0, v12, vcc
v_sub_u32 v12, v12, v10
Mvav 92,192,152
s_nop 0
Ta 184,v2,28672
Ta 186,v2,30720
Zvav 88,220,188
X_ a70,v10
X_ a71,v12
Mvav 88,196,168
s_mov_b64 s[32:33], s[20:21]
Ta 188,v2,32768
Ta 190,v2,34816
Mvav 88,192,148
s_mov_b32 s34, 409600
S_ s85,s6,2
Zvav 84,220,184
L_ v7,24,s55,0
Mvav 84,196,164
S_ s55,s71,133120
s_and_b32 s85, s85, 7
Mvav 84,192,144
S_ m0,s51,50176
s_lshl_b32 s85, s85, 7
Zvav 80,220,180
S_ s85,s85,s87
Mvav 80,196,160
L_ v7,24,s55,0
Mvav 80,192,140
S_ s55,s71,143360
Zvav 76,220,176
S_ m0,s51,51200
Mvav 76,196,156
Mvav 76,192,136
W_ 0
Zvav 16,180,136
s_nop 0
Ta 192,v3,24576
Ta 194,v3,26624
Mvav 16,184,156
L_ v7,24,s55,0
Mvav 16,188,176
S_ s55,s71,153600
Ta 196,v3,28672
Ta 198,v3,30720
Zvav 20,180,140
S_ m0,s51,52224
Mvav 20,184,160
s_nop 0
Ta 220,v3,32768
Ta 222,v3,34816
Mvav 20,188,180
L_ v7,24,s55,0
Zvav 24,180,144
S_ s55,s71,163840
Mvav 24,184,164
S_ m0,s51,53248
Mvav 24,188,184
Zvav 28,180,148
L_ v7,24,s55,0
Mvav 28,184,168
S_ s55,s71,174080
Mvav 28,188,188
S_ m0,s51,54272
Zvav 32,180,152
Mvav 32,184,172
L_ v7,24,s55,0
Mvav 32,188,192
s_waitcnt vmcnt(13) lgkmcnt(0)
s_barrier
S_ s71,s72,1152
Zvav 52,220,192
S_ s55,s71,491520
Ta 180,%0,0
Ta 182,%0,2048
Mvav 52,196,172
S_ m0,s51,36864
Mvav 52,192,152
s_nop 0
Ta 184,%0,4096
Ta 186,%0,6144
Zvav 48,220,188
L_ v7,24,s55,0
Mvav 48,196,168
S_ s55,s71,501760
Ta 188,%0,8192
Ta 190,%0,10240
Mvav 48,192,148
S_ m0,s51,37888
Zvav 44,220,184
Mvav 44,196,164
L_ v7,24,s55,0
Mvav 44,192,144
S_ s55,s71,512000
Zvav 40,220,180
S_ m0,s51,38912
Mvav 40,196,160
Mvav 40,192,140
L_ v7,24,s55,0
Zvav 36,220,176
S_ s55,s71,522240
Mvav 36,196,156
S_ m0,s51,39936
Mvav 36,192,136
W_ 0
Mvav 56,180,196
s_nop 0
Ta 192,v1,0
Ta 194,v1,2048
Mvav 56,184,216
L_ v7,24,s55,0
Mvav 56,188,236
S_ s55,s71,532480
Ta 196,v1,4096
Ta 198,v1,6144
Mvav 60,180,200
S_ m0,s51,40960
Mvav 60,184,220
s_nop 0
Ta 220,v1,8192
Ta 222,v1,10240
Mvav 60,188,240
L_ v7,24,s55,0
Mvav 64,180,204
S_ s55,s71,542720
Mvav 64,184,224
S_ m0,s51,41984
Mvav 64,188,244
Mvav 68,180,208
L_ v7,24,s55,0
Mvav 68,184,228
S_ s55,s71,614400
Mvav 68,188,248
S_ m0,s51,24576
Mvav 72,180,212
Mvav 72,184,232
L_ v7,24,s55,0
Mvav 72,188,252
W_ 0
S_ s55,s71,624640
Mvav 92,220,252
S_ m0,s51,25600
Ta 180,v2,0
Ta 182,v2,2048
Mvav 92,196,232
Mvav 92,192,212
s_nop 0
Ta 184,v2,4096
Ta 186,v2,6144
Mvav 88,220,248
L_ v7,24,s55,0
Mvav 88,196,228
S_ s55,s71,634880
Ta 188,v2,8192
Ta 190,v2,10240
Mvav 88,192,208
S_ m0,s51,26624
Mvav 84,220,244
Mvav 84,196,224
L_ v7,24,s55,0
Mvav 84,192,204
S_ s55,s71,645120
Mvav 80,220,240
S_ m0,s51,27648
Mvav 80,196,220
Mvav 80,192,200
L_ v7,24,s55,0
Mvav 76,220,236
S_ s55,s71,655360
Mvav 76,196,216
S_ m0,s51,28672
Mvav 76,192,196
W_ 0
Mvav 16,180,196
s_nop 0
Ta 192,v3,0
Ta 194,v3,2048
Mvav 16,184,216
L_ v7,24,s55,0
Mvav 16,188,236
S_ s55,s71,665600
Ta 196,v3,4096
Ta 198,v3,6144
Mvav 20,180,200
S_ m0,s51,29696
Mvav 20,184,220
s_nop 0
Ta 220,v3,8192
Ta 222,v3,10240
Mvav 20,188,240
L_ v7,24,s55,0
Mvav 24,180,204
Mvav 24,184,224
Mvav 24,188,244
Mvav 28,180,208
buffer_load_dword a74, v4, s[24:27], s85 offen
Mvav 28,184,228
Mvav 28,188,248
Mvav 32,180,212
Mvav 32,184,232
Mvav 32,188,252
s_waitcnt vmcnt(19) lgkmcnt(0)
s_barrier
Mvav 52,220,252
S_ s55,s71,0
Ta 180,%0,12288
Ta 182,%0,14336
Mvav 52,196,232
S_ m0,s51,0
Mvav 52,192,212
s_nop 0
Ta 184,%0,16384
Ta 186,%0,18432
Mvav 48,220,248
L_ v7,24,s55,0
Mvav 48,196,228
S_ s55,s71,10240
Ta 188,%0,20480
Ta 190,%0,22528
Mvav 48,192,208
S_ m0,s51,1024
Mvav 44,220,244
Mvav 44,196,224
L_ v7,24,s55,0
Mvav 44,192,204
S_ s55,s71,20480
Mvav 40,220,240
S_ m0,s51,2048
Mvav 40,196,220
Mvav 40,192,200
L_ v7,24,s55,0
Mvav 36,220,236
S_ s55,s71,30720
Mvav 36,196,216
S_ m0,s51,3072
Mvav 36,192,196
W_ 0
Mvaa 56,180,0
s_nop 0
Ta 192,v1,12288
Ta 194,v1,14336
Mvaa 56,184,40
L_ v7,24,s55,0
Mvaa 56,188,80
S_ s55,s71,40960
Ta 196,v1,16384
Ta 198,v1,18432
Mvaa 60,180,4
S_ m0,s51,4096
Mvaa 60,184,44
s_nop 0
Ta 220,v1,20480
Ta 222,v1,22528
Mvaa 60,188,84
L_ v7,24,s55,0
Mvaa 64,180,8
S_ s55,s71,51200
Mvaa 64,184,48
S_ m0,s51,5120
Mvaa 64,188,88
Mvaa 68,180,12
L_ v7,24,s55,0
Mvaa 68,184,52
Mvaa 68,188,240
Mvaa 72,180,16
Mvaa 72,184,56
Mvaa 72,188,244
W_ 0
Mvaa 92,220,244
s_nop 0
Ta 180,v2,12288
Ta 182,v2,14336
Mvaa 92,196,56
Mvaa 92,192,16
s_nop 0
Ta 184,v2,16384
Ta 186,v2,18432
Mvaa 88,220,240
Mvaa 88,196,52
s_nop 0
Ta 188,v2,20480
Ta 190,v2,22528
Mvaa 88,192,12
Mvaa 84,220,88
Mvaa 84,196,48
Mvaa 84,192,8
Mvaa 80,220,84
Mvaa 80,196,44
Mvaa 80,192,4
Mvaa 76,220,80
Mvaa 76,196,40
Mvaa 76,192,0
W_ 0
Mvaa 16,180,0
s_nop 0
Ta 192,v3,12288
Ta 194,v3,14336
Mvaa 16,184,40
Mvaa 16,188,80
s_nop 0
Ta 196,v3,16384
Ta 198,v3,18432
Mvaa 20,180,4
Mvaa 20,184,44
s_nop 0
Ta 220,v3,20480
Ta 222,v3,22528
Mvaa 20,188,84
Mvaa 24,180,8
Mvaa 24,184,48
Mvaa 24,188,88
Mvaa 28,180,12
Mvaa 28,184,52
Mvaa 28,188,240
Mvaa 32,180,16
Mvaa 32,184,56
Mvaa 32,188,244
s_waitcnt vmcnt(19) lgkmcnt(0)
s_barrier
Mvaa 52,220,244
s_nop 0
Ta 180,%0,49152
Ta 182,%0,51200
Mvaa 52,196,56
Mvaa 52,192,16
s_nop 0
Ta 184,%0,53248
Ta 186,%0,55296
Mvaa 48,220,240
Mvaa 48,196,52
s_nop 0
Ta 188,%0,57344
Ta 190,%0,59392
Mvaa 48,192,12
Mvaa 44,220,88
Mvaa 44,196,48
Mvaa 44,192,8
Mvaa 40,220,84
Mvaa 40,196,44
Mvaa 40,192,4
Mvaa 36,220,80
Mvaa 36,196,40
Mvaa 36,192,0
W_ 0
Mvaa 56,180,120
S_ s73,s78,163840
Ta 192,v1,49152
Ta 194,v1,51200
Mvaa 56,184,160
S_ s73,s73,4096
Mvaa 56,188,200
s_nop 0
Ta 196,v1,53248
Ta 198,v1,55296
Mvaa 60,180,124
s_mov_b32 m0, s52
Mvaa 60,184,164
s_nop 0
Ta 220,v1,57344
Ta 222,v1,59392
Mvaa 60,188,204
s_nop 0
Y_ 56,0
Y_ 57,256
Mvaa 64,180,128
s_nop 0
Y_ 58,512
Y_ 59,768
Mvaa 64,184,168
Mvaa 64,188,208
s_nop 0
Y_ 60,1024
Y_ 61,1280
Mvaa 68,180,132
s_nop 0
Y_ 62,1536
Y_ 63,1792
Mvaa 68,184,172
Mvaa 68,188,212
s_nop 0
Y_ 64,2048
Y_ 65,2304
Mvaa 72,180,136
s_nop 0
Y_ 66,2560
Y_ 67,2816
Mvaa 72,184,176
Mvaa 72,188,216
W_ 12
Y_ 68,3072
Y_ 69,3328
Mvaa 92,220,216
s_nop 0
Ta 180,v2,49152
Ta 182,v2,51200
Mvaa 92,196,176
s_nop 0
)A"
R"A(Y_ 70,3584
Y_ 71,3840
Mvaa 92,192,136
s_nop 0
Ta 184,v2,53248
Ta 186,v2,55296
Mvaa 88,220,212
s_nop 0
Y_ 72,4096
Y_ 73,4352
Mvaa 88,196,172
s_nop 0
Ta 188,v2,57344
Ta 190,v2,59392
Mvaa 88,192,132
s_nop 0
Y_ 74,4608
Y_ 75,4864
Mvaa 84,220,208
s_nop 0
Y_ 92,9216
Y_ 93,9472
Mvaa 84,196,168
s_nop 0
Y_ 94,9728
Y_ 95,9984
Mvaa 84,192,128
s_nop 0
Y_ 88,8192
Y_ 89,8448
Mvaa 80,220,204
s_nop 0
Y_ 90,8704
Y_ 91,8960
Mvaa 80,196,164
Mvaa 80,192,124
s_nop 0
Y_ 84,7168
Y_ 85,7424
Mvaa 76,220,200
s_nop 0
Y_ 86,7680
Y_ 87,7936
Mvaa 76,196,160
Mvaa 76,192,120
W_ 14
Y_ 80,6144
Y_ 81,6400
Mvaa 16,180,120
s_nop 0
Ta 192,v3,49152
Ta 194,v3,51200
Mvaa 16,184,160
s_nop 0
Y_ 82,6656
Y_ 83,6912
Mvaa 16,188,200
s_nop 0
Ta 196,v3,53248
Ta 198,v3,55296
Mvaa 20,180,124
s_nop 0
Y_ 76,5120
Y_ 77,5376
Mvaa 20,184,164
s_nop 0
Ta 220,v3,57344
Ta 222,v3,59392
Mvaa 20,188,204
s_nop 0
Y_ 78,5632
Y_ 79,5888
Mvaa 24,180,128
Mvaa 24,184,168
Mvaa 24,188,208
s_waitcnt vmcnt(13) lgkmcnt(0)
s_barrier
Mvaa 28,180,132
S_ s55,s71,122880
ds_read2st64_b32 v[96:97], v5 offset0:0 offset1:1
Mvaa 28,184,172
S_ m0,s51,12288
ds_read2st64_b32 v[98:99], v5 offset0:2 offset1:3
Mvaa 28,188,212
L_ v7,24,s55,0
Mvaa 32,180,136
S_ s55,s71,133120
ds_read2st64_b32 v[104:105], v5 offset0:4 offset1:5
Mvaa 32,184,176
S_ m0,s51,13312
ds_read2st64_b32 v[106:107], v5 offset0:6 offset1:7
Mvaa 32,188,216
ds_read2st64_b32 v[112:113], v5 offset0:8 offset1:9
Mvaa 52,220,216
s_nop 0
Ta 180,%0,36864
Ta 182,%0,38912
Mvaa 52,196,176
L_ v7,24,s55,0
Mvaa 52,192,136
W_ 5
Ta 184,%0,40960
Ta 186,%0,43008
Mvaa 48,220,212
S_ s55,s71,143360
ds_read2st64_b32 v[114:115], v5 offset0:10 offset1:11
A_ v96,v16,v96
Mvaa 48,196,172
S_ m0,s51,14336
Ta 188,%0,45056
Ta 190,%0,47104
Mvaa 48,192,132
L_ v7,24,s55,0
Mvaa 44,220,208
S_ s55,s71,153600
ds_read2st64_b32 v[120:121], v5 offset0:12 offset1:13
A_ v97,v17,v97
Mvaa 44,196,168
S_ m0,s51,15360
ds_read2st64_b32 v[122:123], v5 offset0:14 offset1:15
A_ v98,v18,v98
Mvaa 44,192,128
ds_read2st64_b32 v[128:129], v5 offset0:16 offset1:17
A_ v99,v19,v99
Mvaa 40,220,204
L_ v7,24,s55,0
Mvaa 40,196,164
W_ 11
ds_read2st64_b32 v[130:131], v5 offset0:18 offset1:19
A_ v104,v20,v104
Mvaa 40,192,124
S_ s55,s71,163840
ds_read2st64_b32 v[132:133], v5 offset0:36 offset1:37
A_ v105,v21,v105
Mvaa 36,220,200
S_ m0,s51,16384
ds_read2st64_b32 v[134:135], v5 offset0:38 offset1:39
A_ v106,v22,v106
Mvaa 36,196,160
L_ v7,24,s55,0
Mvaa 36,192,120
W_ 6
ds_read2st64_b32 v[124:125], v5 offset0:32 offset1:33
A_ v107,v23,v107
Zvav 56,180,136
Ta 192,v1,36864
Ta 194,v1,38912
Mvav 56,184,156
S_ s55,s71,174080
ds_read2st64_b32 v[126:127], v5 offset0:34 offset1:35
A_ v112,v24,v112
Mvav 56,188,176
S_ m0,s51,17408
Ta 196,v1,40960
Ta 198,v1,43008
Zvav 60,180,140
L_ v7,24,s55,0
Mvav 60,184,160
S_ s79,s6,1
Ta 220,v1,45056
Ta 222,v1,47104
Mvav 60,188,180
s_and_b32 s79, s79, 7
ds_read2st64_b32 v[116:117], v5 offset0:28 offset1:29
A_ v113,v25,v113
Zvav 64,180,144
s_lshl_b32 s79, s79, 7
ds_read2st64_b32 v[118:119], v5 offset0:30 offset1:31
A_ v114,v26,v114
Mvav 64,184,164
S_ s71,s72,s79
ds_read2st64_b32 v[108:109], v5 offset0:24 offset1:25
A_ v115,v27,v115
Mvav 64,188,184
W_ 14
ds_read2st64_b32 v[110:111], v5 offset0:26 offset1:27
A_ v120,v28,v120
Zvav 68,180,148
S_ s55,s71,491520
ds_read2st64_b32 v[100:101], v5 offset0:20 offset1:21
A_ v121,v29,v121
Mvav 68,184,168
S_ m0,s51,49152
ds_read2st64_b32 v[102:103], v5 offset0:22 offset1:23
A_ v122,v30,v122
Mvav 68,188,188
L_ v7,24,s55,0
Zvav 72,180,152
S_ s55,s71,501760
A_ v123,v31,v123
R_ v12,a248
Mvav 72,184,172
W_ 14
A_ v128,v32,v128
A_ v129,v33,v129
Mvav 72,188,192
W_ 6
A_ v130,v34,v130
A_ v131,v35,v131
Zvav 92,220,192
Ta 180,v2,36864
Ta 182,v2,38912
Mvav 92,196,172
S_ m0,s51,50176
A_ v132,v52,v132
A_ v133,v53,v133
Mvav 92,192,152
s_nop 0
Ta 184,v2,40960
Ta 186,v2,43008
Zvav 88,220,188
L_ v7,24,s55,0
Mvav 88,196,168
S_ s55,s71,512000
Ta 188,v2,45056
Ta 190,v2,47104
Mvav 88,192,148
S_ m0,s51,51200
A_ v134,v54,v134
A_ v135,v55,v135
Zvav 84,220,184
A_ v124,v48,v124
A_ v125,v49,v125
Mvav 84,196,164
L_ v7,24,s55,0
Mvav 84,192,144
S_ s55,s71,522240
A_ v126,v50,v126
A_ v127,v51,v127
Zvav 80,220,180
W_ 10
A_ v116,v44,v116
A_ v117,v45,v117
Mvav 80,196,160
S_ m0,s51,52224
A_ v118,v46,v118
A_ v119,v47,v119
Mvav 80,192,140
L_ v7,24,s55,0
Zvav 76,220,176
W_ 8
A_ v108,v40,v108
A_ v109,v41,v109
Mvav 76,196,156
S_ s55,s71,532480
A_ v110,v42,v110
A_ v111,v43,v111
Mvav 76,192,136
W_ 0
A_ v100,v36,v100
Zvav 16,180,136
S_ m0,s51,53248
Ta 192,v3,36864
Ta 194,v3,38912
Mvav 16,184,156
L_ v7,24,s55,0
Mvav 16,188,176
S_ s55,s71,542720
Ta 196,v3,40960
Ta 198,v3,43008
Zvav 20,180,140
S_ m0,s51,54272
A_ v101,v37,v101
A_ v102,v38,v102
Mvav 20,184,160
S_ s85,s6,3
Ta 220,v3,45056
Ta 222,v3,47104
Mvav 20,188,180
L_ v7,24,s55,0
Zvav 24,180,144
s_and_b32 s85, s85, 7
A_ v103,v39,v103
C_ 131,130,131
Mvav 24,184,164
s_lshl_b32 s85, s85, 7
C_ 130,128,129
C_ 132,132,133
Mvav 24,188,184
S_ s85,s85,s87
C_ 133,134,135
v_lshlrev_b32 v12, 10, v12
Zvav 28,180,148
buffer_store_dwordx4 v[130:133], v6, s[28:31], s73 offen offset:0 sc1
Mvav 28,184,168
s_sub_u32 s73, s73, 4096
Mvav 28,188,188
C_ 123,122,123
C_ 122,120,121
Zvav 32,180,152
C_ 124,124,125
C_ 125,126,127
Mvav 32,184,172
buffer_store_dwordx4 v[122:125], v6, s[28:31], s73 offen offset:3072 sc1
Mvav 32,188,192
s_waitcnt vmcnt(21) lgkmcnt(0)
s_barrier
C_ 115,114,115
Zvav 52,220,192
S_ s55,s71,614400
Ta 180,%0,24576
Ta 182,%0,26624
Mvav 52,196,172
S_ m0,s51,36864
C_ 114,112,113
C_ 116,116,117
Mvav 52,192,152
s_nop 0
Ta 184,%0,28672
Ta 186,%0,30720
Zvav 48,220,188
L_ v7,24,s55,0
Mvav 48,196,168
S_ s55,s71,624640
Ta 188,%0,32768
Ta 190,%0,34816
Mvav 48,192,148
S_ m0,s51,37888
C_ 117,118,119
Zvav 44,220,184
Mvav 44,196,164
buffer_store_dwordx4 v[114:117], v6, s[28:31], s73 offen offset:2048 sc1
Mvav 44,192,144
C_ 107,106,107
C_ 106,104,105
Zvav 40,220,180
C_ 108,108,109
C_ 109,110,111
Mvav 40,196,160
Mvav 40,192,140
buffer_store_dwordx4 v[106:109], v6, s[28:31], s73 offen offset:1024 sc1
Zvav 36,220,176
C_ 99,98,99
C_ 98,96,97
Mvav 36,196,156
C_ 100,100,101
C_ 101,102,103
Mvav 36,192,136
W_ 0
Mvav 56,180,196
s_nop 0
Ta 192,v1,24576
Ta 194,v1,26624
Mvav 56,184,216
buffer_store_dwordx4 v[98:101], v6, s[28:31], s73 offen offset:0 sc1
Mvav 56,188,236
s_nop 0
Ta 196,v1,28672
Ta 198,v1,30720
Mvav 60,180,200
Mvav 60,184,220
s_nop 0
Ta 220,v1,32768
Ta 222,v1,34816
Mvav 60,188,240
L_ v7,24,s55,0
Mvav 64,180,204
S_ s55,s71,634880
Mvav 64,184,224
S_ m0,s51,38912
Mvav 64,188,244
Mvav 68,180,208
L_ v7,24,s55,0
Mvav 68,184,228
S_ s55,s71,645120
Mvav 68,188,248
S_ m0,s51,39936
Mvav 72,180,212
Mvav 72,184,232
L_ v7,24,s55,0
Mvav 72,188,252
W_ 0
S_ s55,s71,655360
Mvav 92,220,252
S_ m0,s51,40960
Ta 180,v2,24576
Ta 182,v2,26624
Mvav 92,196,232
Mvav 92,192,212
s_nop 0
Ta 184,v2,28672
Ta 186,v2,30720
Mvav 88,220,248
L_ v7,24,s55,0
Mvav 88,196,228
S_ s55,s71,665600
Ta 188,v2,32768
Ta 190,v2,34816
Mvav 88,192,208
S_ m0,s51,41984
Mvav 84,220,244
Mvav 84,196,224
L_ v7,24,s55,0
Mvav 84,192,204
Mvav 80,220,240
Mvav 80,196,220
Mvav 80,192,200
buffer_load_dword a74, v12, s[44:47], s66 offen
Mvav 76,220,236
Mvav 76,196,216
Mvav 76,192,196
W_ 0
Mvav 16,180,196
s_nop 0
Ta 192,v3,24576
Ta 194,v3,26624
Mvav 16,184,216
buffer_load_dword a74, v4, s[24:27], s85 offen
Mvav 16,188,236
s_nop 0
Ta 196,v3,28672
Ta 198,v3,30720
Mvav 20,180,200
Mvav 20,184,220
s_nop 0
Ta 220,v3,32768
Ta 222,v3,34816
Mvav 20,188,240
Mvav 24,180,204
Mvav 24,184,224
Mvav 24,188,244
Mvav 28,180,208
Mvav 28,184,228
Mvav 28,188,248
Mvav 32,180,212
Mvav 32,184,232
Mvav 32,188,252
s_waitcnt vmcnt(25) lgkmcnt(0)
s_barrier
Mvav 52,220,252
S_ s55,s71,0
Ta 180,%0,0
Ta 182,%0,2048
Mvav 52,196,232
S_ m0,s51,24576
Mvav 52,192,212
s_nop 0
Ta 184,%0,4096
Ta 186,%0,6144
Mvav 48,220,248
L_ v7,24,s55,0
Mvav 48,196,228
S_ s55,s71,10240
Ta 188,%0,8192
Ta 190,%0,10240
Mvav 48,192,208
S_ m0,s51,25600
Mvav 44,220,244
Mvav 44,196,224
L_ v7,24,s55,0
Mvav 44,192,204
S_ s55,s71,20480
Mvav 40,220,240
S_ m0,s51,26624
Mvav 40,196,220
Mvav 40,192,200
L_ v7,24,s55,0
Mvav 36,220,236
S_ s55,s71,30720
Mvav 36,196,216
S_ m0,s51,27648
Mvav 36,192,196
W_ 0
Mvaa 56,180,0
s_nop 0
Ta 192,v1,0
Ta 194,v1,2048
Mvaa 56,184,40
L_ v7,24,s55,0
Mvaa 56,188,80
S_ s55,s71,40960
Ta 196,v1,4096
Ta 198,v1,6144
Mvaa 60,180,4
S_ m0,s51,28672
Mvaa 60,184,44
s_nop 0
Ta 220,v1,8192
Ta 222,v1,10240
Mvaa 60,188,84
L_ v7,24,s55,0
Mvaa 64,180,8
S_ s55,s71,51200
Mvaa 64,184,48
S_ m0,s51,29696
Mvaa 64,188,88
Mvaa 68,180,12
L_ v7,24,s55,0
Mvaa 68,184,52
Mvaa 68,188,240
Mvaa 72,180,16
Mvaa 72,184,56
Mvaa 72,188,244
W_ 0
Mvaa 92,220,244
s_nop 0
Ta 180,v2,0
Ta 182,v2,2048
Mvaa 92,196,56
Mvaa 92,192,16
s_nop 0
Ta 184,v2,4096
Ta 186,v2,6144
Mvaa 88,220,240
Mvaa 88,196,52
s_nop 0
Ta 188,v2,8192
Ta 190,v2,10240
Mvaa 88,192,12
Mvaa 84,220,88
Mvaa 84,196,48
Mvaa 84,192,8
Mvaa 80,220,84
Mvaa 80,196,44
Mvaa 80,192,4
Mvaa 76,220,80
Mvaa 76,196,40
Mvaa 76,192,0
W_ 0
Mvaa 16,180,0
s_nop 0
Ta 192,v3,0
Ta 194,v3,2048
Mvaa 16,184,40
Mvaa 16,188,80
s_nop 0
Ta 196,v3,4096
Ta 198,v3,6144
Mvaa 20,180,4
Mvaa 20,184,44
s_nop 0
Ta 220,v3,8192
Ta 222,v3,10240
Mvaa 20,188,84
Mvaa 24,180,8
Mvaa 24,184,48
Mvaa 24,188,88
Mvaa 28,180,12
Mvaa 28,184,52
Mvaa 28,188,240
Mvaa 32,180,16
Mvaa 32,184,56
Mvaa 32,188,244
s_waitcnt vmcnt(25) lgkmcnt(0)
s_barrier
Mvaa 52,220,244
s_nop 0
Ta 180,%0,12288
Ta 182,%0,14336
Mvaa 52,196,56
Mvaa 52,192,16
s_nop 0
Ta 184,%0,16384
Ta 186,%0,18432
Mvaa 48,220,240
Mvaa 48,196,52
s_nop 0
Ta 188,%0,20480
Ta 190,%0,22528
Mvaa 48,192,12
Mvaa 44,220,88
Mvaa 44,196,48
Mvaa 44,192,8
Mvaa 40,220,84
Mvaa 40,196,44
Mvaa 40,192,4
Mvaa 36,220,80
Mvaa 36,196,40
Mvaa 36,192,0
W_ 0
Mvaa 56,180,120
S_ s73,s78,184320
Ta 192,v1,12288
Ta 194,v1,14336
Mvaa 56,184,160
S_ s73,s73,4096
Mvaa 56,188,200
s_nop 0
Ta 196,v1,16384
Ta 198,v1,18432
Mvaa 60,180,124
s_mov_b32 m0, s52
Mvaa 60,184,164
s_nop 0
Ta 220,v1,20480
Ta 222,v1,22528
Mvaa 60,188,204
s_nop 0
Y_ 56,0
Y_ 57,256
Mvaa 64,180,128
s_nop 0
Y_ 58,512
Y_ 59,768
Mvaa 64,184,168
Mvaa 64,188,208
s_nop 0
Y_ 60,1024
Y_ 61,1280
Mvaa 68,180,132
s_nop 0
Y_ 62,1536
Y_ 63,1792
Mvaa 68,184,172
Mvaa 68,188,212
s_nop 0
Y_ 64,2048
Y_ 65,2304
Mvaa 72,180,136
s_nop 0
Y_ 66,2560
Y_ 67,2816
Mvaa 72,184,176
Mvaa 72,188,216
W_ 12
Y_ 68,3072
Y_ 69,3328
Mvaa 92,220,216
s_nop 0
Ta 180,v2,12288
Ta 182,v2,14336
Mvaa 92,196,176
s_nop 0
Y_ 70,3584
Y_ 71,3840
Mvaa 92,192,136
s_nop 0
Ta 184,v2,16384
Ta 186,v2,18432
Mvaa 88,220,212
s_nop 0
Y_ 72,4096
Y_ 73,4352
Mvaa 88,196,172
s_nop 0
Ta 188,v2,20480
Ta 190,v2,22528
Mvaa 88,192,132
s_nop 0
Y_ 74,4608
Y_ 75,4864
Mvaa 84,220,208
s_nop 0
Y_ 92,9216
Y_ 93,9472
Mvaa 84,196,168
s_nop 0
Y_ 94,9728
Y_ 95,9984
Mvaa 84,192,128
s_nop 0
Y_ 88,8192
Y_ 89,8448
Mvaa 80,220,204
s_nop 0
Y_ 90,8704
Y_ 91,8960
Mvaa 80,196,164
Mvaa 80,192,124
s_nop 0
Y_ 84,7168
Y_ 85,7424
Mvaa 76,220,200
s_nop 0
Y_ 86,7680
Y_ 87,7936
Mvaa 76,196,160
Mvaa 76,192,120
W_ 14
Y_ 80,6144
Y_ 81,6400
Mvaa 16,180,120
s_nop 0
Ta 192,v3,12288
Ta 194,v3,14336
Mvaa 16,184,160
s_nop 0
Y_ 82,6656
Y_ 83,6912
Mvaa 16,188,200
s_nop 0
Ta 196,v3,16384
Ta 198,v3,18432
Mvaa 20,180,124
s_nop 0
Y_ 76,5120
Y_ 77,5376
Mvaa 20,184,164
s_nop 0
Ta 220,v3,20480
Ta 222,v3,22528
Mvaa 20,188,204
s_nop 0
Y_ 78,5632
Y_ 79,5888
Mvaa 24,180,128
Mvaa 24,184,168
Mvaa 24,188,208
s_waitcnt vmcnt(19) lgkmcnt(0)
s_barrier
Mvaa 28,180,132
S_ s55,s71,122880
ds_read2st64_b32 v[96:97], v5 offset0:0 offset1:1
Mvaa 28,184,172
S_ m0,s51,0
ds_read2st64_b32 v[98:99], v5 offset0:2 offset1:3
Mvaa 28,188,212
L_ v7,24,s55,0
Mvaa 32,180,136
S_ s55,s71,133120
ds_read2st64_b32 v[104:105], v5 offset0:4 offset1:5
Mvaa 32,184,176
S_ m0,s51,1024
ds_read2st64_b32 v[106:107], v5 offset0:6 offset1:7
Mvaa 32,188,216
ds_read2st64_b32 v[112:113], v5 offset0:8 offset1:9
Mvaa 52,220,216
s_nop 0
Ta 180,%0,49152
Ta 182,%0,51200
Mvaa 52,196,176
L_ v7,24,s55,0
Mvaa 52,192,136
W_ 5
Ta 184,%0,53248
Ta 186,%0,55296
Mvaa 48,220,212
S_ s55,s71,143360
ds_read2st64_b32 v[114:115], v5 offset0:10 offset1:11
A_ v96,v16,v96
Mvaa 48,196,172
S_ m0,s51,2048
Ta 188,%0,57344
Ta 190,%0,59392
Mvaa 48,192,132
L_ v7,24,s55,0
Mvaa 44,220,208
S_ s55,s71,153600
ds_read2st64_b32 v[120:121], v5 offset0:12 offset1:13
A_ v97,v17,v97
Mvaa 44,196,168
S_ m0,s51,3072
ds_read2st64_b32 v[122:123], v5 offset0:14 offset1:15
A_ v98,v18,v98
Mvaa 44,192,128
ds_read2st64_b32 v[128:129], v5 offset0:16 offset1:17
A_ v99,v19,v99
Mvaa 40,220,204
L_ v7,24,s55,0
Mvaa 40,196,164
W_ 11
ds_read2st64_b32 v[130:131], v5 offset0:18 offset1:19
A_ v104,v20,v104
Mvaa 40,192,124
S_ s55,s71,163840
ds_read2st64_b32 v[132:133], v5 offset0:36 offset1:37
A_ v105,v21,v105
Mvaa 36,220,200
S_ m0,s51,4096
ds_read2st64_b32 v[134:135], v5 offset0:38 offset1:39
A_ v106,v22,v106
Mvaa 36,196,160
L_ v7,24,s55,0
Mvaa 36,192,120
W_ 6
ds_read2st64_b32 v[124:125], v5 offset0:32 offset1:33
A_ v107,v23,v107
Zvav 56,180,136
Ta 192,v1,49152
Ta 194,v1,51200
Mvav 56,184,156
S_ s55,s71,174080
ds_read2st64_b32 v[126:127], v5 offset0:34 offset1:35
A_ v112,v24,v112
Mvav 56,188,176
S_ m0,s51,5120
Ta 196,v1,53248
Ta 198,v1,55296
Zvav 60,180,140
L_ v7,24,s55,0
Mvav 60,184,160
S_ s79,s6,2
Ta 220,v1,57344
Ta 222,v1,59392
Mvav 60,188,180
s_and_b32 s79, s79, 7
ds_read2st64_b32 v[116:117], v5 offset0:28 offset1:29
A_ v113,v25,v113
Zvav 64,180,144
s_lshl_b32 s79, s79, 7
ds_read2st64_b32 v[118:119], v5 offset0:30 offset1:31
A_ v114,v26,v114
Mvav 64,184,164
S_ s71,s72,s79
ds_read2st64_b32 v[108:109], v5 offset0:24 offset1:25
A_ v115,v27,v115
Mvav 64,188,184
W_ 14
ds_read2st64_b32 v[110:111], v5 offset0:26 offset1:27
A_ v120,v28,v120
Zvav 68,180,148
S_ s55,s71,491520
ds_read2st64_b32 v[100:101], v5 offset0:20 offset1:21
A_ v121,v29,v121
Mvav 68,184,168
S_ m0,s51,12288
ds_read2st64_b32 v[102:103], v5 offset0:22 offset1:23
A_ v122,v30,v122
Mvav 68,188,188
L_ v7,24,s55,0
Zvav 72,180,152
S_ s55,s71,501760
A_ v123,v31,v123
Mvav 72,184,172
W_ 14
A_ v128,v32,v128
A_ v129,v33,v129
Mvav 72,188,192
W_ 6
A_ v130,v34,v130
A_ v131,v35,v131
Zvav 92,220,192
Ta 180,v2,49152
Ta 182,v2,51200
Mvav 92,196,172
S_ m0,s51,13312
A_ v132,v52,v132
A_ v133,v53,v133
Mvav 92,192,152
s_nop 0
Ta 184,v2,53248
Ta 186,v2,55296
Zvav 88,220,188
L_ v7,24,s55,0
Mvav 88,196,168
S_ s55,s71,512000
Ta 188,v2,57344
Ta 190,v2,59392
Mvav 88,192,148
S_ m0,s51,14336
A_ v134,v54,v134
A_ v135,v55,v135
Zvav 84,220,184
A_ v124,v48,v124
A_ v125,v49,v125
Mvav 84,196,164
L_ v7,24,s55,0
Mvav 84,192,144
S_ s55,s71,522240
A_ v126,v50,v126
A_ v127,v51,v127
Zvav 80,220,180
W_ 10
A_ v116,v44,v116
A_ v117,v45,v117
Mvav 80,196,160
S_ m0,s51,15360
A_ v118,v46,v118
A_ v119,v47,v119
Mvav 80,192,140
L_ v7,24,s55,0
Zvav 76,220,176
W_ 8
A_ v108,v40,v108
A_ v109,v41,v109
Mvav 76,196,156
S_ s55,s71,532480
A_ v110,v42,v110
A_ v111,v43,v111
Mvav 76,192,136
W_ 0
A_ v100,v36,v100
Zvav 16,180,136
S_ m0,s51,16384
Ta 192,v3,49152
Ta 194,v3,51200
Mvav 16,184,156
L_ v7,24,s55,0
Mvav 16,188,176
S_ s55,s71,542720
Ta 196,v3,53248
Ta 198,v3,55296
Zvav 20,180,140
S_ m0,s51,17408
A_ v101,v37,v101
A_ v102,v38,v102
Mvav 20,184,160
S_ s85,s6,4
Ta 220,v3,57344
Ta 222,v3,59392
Mvav 20,188,180
L_ v7,24,s55,0
Zvav 24,180,144
s_and_b32 s85, s85, 7
A_ v103,v39,v103
C_ 131,130,131
Mvav 24,184,164
s_lshl_b32 s85, s85, 7
C_ 130,128,129
C_ 132,132,133
Mvav 24,188,184
S_ s85,s85,s87
C_ 133,134,135
Zvav 28,180,148
buffer_store_dwordx4 v[130:133], v6, s[28:31], s73 offen offset:0 sc1
Mvav 28,184,168
s_sub_u32 s73, s73, 4096
Mvav 28,188,188
C_ 123,122,123
C_ 122,120,121
Zvav 32,180,152
C_ 124,124,125
C_ 125,126,127
Mvav 32,184,172
buffer_store_dwordx4 v[122:125], v6, s[28:31], s73 offen offset:3072 sc1
Mvav 32,188,192
s_waitcnt vmcnt(22) lgkmcnt(0)
s_barrier
C_ 115,114,115
Zvav 52,220,192
S_ s55,s71,614400
Ta 180,%0,36864
Ta 182,%0,38912
Mvav 52,196,172
S_ m0,s51,49152
C_ 114,112,113
C_ 116,116,117
Mvav 52,192,152
s_nop 0
Ta 184,%0,40960
Ta 186,%0,43008
Zvav 48,220,188
L_ v7,24,s55,0
Mvav 48,196,168
S_ s55,s71,624640
Ta 188,%0,45056
Ta 190,%0,47104
Mvav 48,192,148
S_ m0,s51,50176
C_ 117,118,119
Zvav 44,220,184
Mvav 44,196,164
buffer_store_dwordx4 v[114:117], v6, s[28:31], s73 offen offset:2048 sc1
Mvav 44,192,144
C_ 107,106,107
C_ 106,104,105
Zvav 40,220,180
C_ 108,108,109
C_ 109,110,111
Mvav 40,196,160
Mvav 40,192,140
buffer_store_dwordx4 v[106:109], v6, s[28:31], s73 offen offset:1024 sc1
Zvav 36,220,176
C_ 99,98,99
C_ 98,96,97
Mvav 36,196,156
C_ 100,100,101
C_ 101,102,103
Mvav 36,192,136
W_ 0
Mvav 56,180,196
s_nop 0
Ta 192,v1,36864
Ta 194,v1,38912
Mvav 56,184,216
buffer_store_dwordx4 v[98:101], v6, s[28:31], s73 offen offset:0 sc1
Mvav 56,188,236
s_nop 0
Ta 196,v1,40960
Ta 198,v1,43008
Mvav 60,180,200
X_ a20,-1
X_ a21,-1
Mvav 60,184,220
s_nop 0
Ta 220,v1,45056
Ta 222,v1,47104
Mvav 60,188,240
L_ v7,24,s55,0
Mvav 64,180,204
S_ s55,s71,634880
X_ a22,-1
X_ a23,-1
Mvav 64,184,224
S_ s81,s6,7
S_ m0,s51,51200
Mvav 64,188,244
s_and_b32 s81, s81, 7
Mvav 68,180,208
L_ v7,24,s55,0
Mvav 68,184,228
s_mul_i32 s81, s81, 20480
S_ s55,s71,645120
Mvav 68,188,248
S_ s81,s81,s78
S_ m0,s51,52224
Mvav 72,180,212
Mvav 72,184,232
buffer_store_dwordx4 a[20:23], v6, s[28:31], s81 offen offset:0 sc1
Mvav 72,188,252
W_ 0
Mvav 92,220,252
s_nop 0
Ta 180,v2,36864
Ta 182,v2,38912
Mvav 92,196,232
Mvav 92,192,212
s_nop 0
Ta 184,v2,40960
Ta 186,v2,43008
Mvav 88,220,248
buffer_store_dwordx4 a[20:23], v6, s[28:31], s81 offen offset:1024 sc1
Mvav 88,196,228
s_nop 0
Ta 188,v2,45056
Ta 190,v2,47104
Mvav 88,192,208
Mvav 84,220,244
Mvav 84,196,224
buffer_store_dwordx4 a[20:23], v6, s[28:31], s81 offen offset:2048 sc1
Mvav 84,192,204
Mvav 80,220,240
Mvav 80,196,220
Mvav 80,192,200
buffer_store_dwordx4 a[20:23], v6, s[28:31], s81 offen offset:3072 sc1
Mvav 76,220,236
S_ s81,s81,4096
Mvav 76,196,216
Mvav 76,192,196
W_ 0
Mvav 16,180,196
s_nop 0
Ta 192,v3,36864
Ta 194,v3,38912
Mvav 16,184,216
buffer_store_dwordx4 a[20:23], v6, s[28:31], s81 offen offset:0 sc1
Mvav 16,188,236
s_nop 0
Ta 196,v3,40960
Ta 198,v3,43008
Mvav 20,180,200
Mvav 20,184,220
s_nop 0
Ta 220,v3,45056
Ta 222,v3,47104
Mvav 20,188,240
L_ v7,24,s55,0
Mvav 24,180,204
S_ s55,s71,655360
Mvav 24,184,224
S_ m0,s51,53248
Mvav 24,188,244
Mvav 28,180,208
L_ v7,24,s55,0
Mvav 28,184,228
S_ s55,s71,665600
Mvav 28,188,248
S_ m0,s51,54272
Mvav 32,180,212
Mvav 32,184,232
L_ v7,24,s55,0
Mvav 32,188,252
s_waitcnt vmcnt(28) lgkmcnt(0)
s_barrier
Mvav 52,220,252
S_ s55,s71,0
Ta 180,%0,24576
Ta 182,%0,26624
Mvav 52,196,232
S_ m0,s51,36864
Mvav 52,192,212
s_nop 0
Ta 184,%0,28672
Ta 186,%0,30720
Mvav 48,220,248
L_ v7,24,s55,0
Mvav 48,196,228
S_ s55,s71,10240
Ta 188,%0,32768
Ta 190,%0,34816
Mvav 48,192,208
S_ m0,s51,37888
Mvav 44,220,244
Mvav 44,196,224
L_ v7,24,s55,0
Mvav 44,192,204
S_ s55,s71,20480
Mvav 40,220,240
S_ m0,s51,38912
Mvav 40,196,220
Mvav 40,192,200
L_ v7,24,s55,0
Mvav 36,220,236
S_ s55,s71,30720
Mvav 36,196,216
S_ m0,s51,39936
Mvav 36,192,196
W_ 0
Mvaa 56,180,0
s_nop 0
Ta 192,v1,24576
Ta 194,v1,26624
Mvaa 56,184,40
L_ v7,24,s55,0
Mvaa 56,188,80
S_ s55,s71,40960
Ta 196,v1,28672
Ta 198,v1,30720
Mvaa 60,180,4
S_ m0,s51,40960
Mvaa 60,184,44
s_nop 0
Ta 220,v1,32768
Ta 222,v1,34816
Mvaa 60,188,84
L_ v7,24,s55,0
Mvaa 64,180,8
S_ s55,s71,51200
Mvaa 64,184,48
S_ m0,s51,41984
Mvaa 64,188,88
Mvaa 68,180,12
L_ v7,24,s55,0
Mvaa 68,184,52
Mvaa 68,188,240
Mvaa 72,180,16
Mvaa 72,184,56
buffer_load_dword a74, v4, s[24:27], s85 offen
Mvaa 72,188,244
W_ 0
Mvaa 92,220,244
s_nop 0
Ta 180,v2,24576
Ta 182,v2,26624
Mvaa 92,196,56
Mvaa 92,192,16
s_nop 0
Ta 184,v2,28672
Ta 186,v2,30720
Mvaa 88,220,240
Mvaa 88,196,52
s_nop 0
Ta 188,v2,32768
Ta 190,v2,34816
Mvaa 88,192,12
Mvaa 84,220,88
Mvaa 84,196,48
Mvaa 84,192,8
Mvaa 80,220,84
Mvaa 80,196,44
Mvaa 80,192,4
Mvaa 76,220,80
Mvaa 76,196,40
Mvaa 76,192,0
W_ 0
Mvaa 16,180,0
s_nop 0
Ta 192,v3,24576
Ta 194,v3,26624
Mvaa 16,184,40
Mvaa 16,188,80
s_nop 0
Ta 196,v3,28672
Ta 198,v3,30720
Mvaa 20,180,4
Mvaa 20,184,44
s_nop 0
Ta 220,v3,32768
Ta 222,v3,34816
Mvaa 20,188,84
Mvaa 24,180,8
Mvaa 24,184,48
Mvaa 24,188,88
Mvaa 28,180,12
Mvaa 28,184,52
Mvaa 28,188,240
Mvaa 32,180,16
Mvaa 32,184,56
Mvaa 32,188,244
s_waitcnt vmcnt(29) lgkmcnt(0)
s_barrier
Mvaa 52,220,244
s_nop 0
Ta 180,%0,0
Ta 182,%0,2048
Mvaa 52,196,56
Mvaa 52,192,16
s_nop 0
Ta 184,%0,4096
Ta 186,%0,6144
Mvaa 48,220,240
Mvaa 48,196,52
s_nop 0
Ta 188,%0,8192
Ta 190,%0,10240
Mvaa 48,192,12
Mvaa 44,220,88
Mvaa 44,196,48
Mvaa 44,192,8
Mvaa 40,220,84
Mvaa 40,196,44
Mvaa 40,192,4
Mvaa 36,220,80
Mvaa 36,196,40
Mvaa 36,192,0
W_ 0
Mvaa 56,180,120
S_ s81,s6,1
Ta 192,v1,0
Ta 194,v1,2048
Mvaa 56,184,160
s_and_b32 s81, s81, 7
Mvaa 56,188,200
s_mul_i32 s81, s81, 20480
Ta 196,v1,4096
Ta 198,v1,6144
Mvaa 60,180,124
s_mov_b32 m0, s52
S_ s73,s81,s78
Mvaa 60,184,164
S_ s73,s73,4096
Ta 220,v1,8192
Ta 222,v1,10240
Mvaa 60,188,204
s_nop 0
Y_ 56,0
Y_ 57,256
Mvaa 64,180,128
s_nop 0
Y_ 58,512
Y_ 59,768
Mvaa 64,184,168
Mvaa 64,188,208
s_nop 0
Y_ 60,1024
Y_ 61,1280
Mvaa 68,180,132
s_nop 0
Y_ 62,1536
Y_ 63,1792
Mvaa 68,184,172
Mvaa 68,188,212
s_nop 0
Y_ 64,2048
Y_ 65,2304
Mvaa 72,180,136
s_nop 0
Y_ 66,2560
Y_ 67,2816
Mvaa 72,184,176
Mvaa 72,188,216
W_ 12
Y_ 68,3072
Y_ 69,3328
Mvaa 92,220,216
s_nop 0
Ta 180,v2,0
Ta 182,v2,2048
Mvaa 92,196,176
s_nop 0
Y_ 70,3584
Y_ 71,3840
Mvaa 92,192,136
s_nop 0
Ta 184,v2,4096
Ta 186,v2,6144
Mvaa 88,220,212
s_nop 0
Y_ 72,4096
Y_ 73,4352
Mvaa 88,196,172
s_nop 0
Ta 188,v2,8192
Ta 190,v2,10240
Mvaa 88,192,132
s_nop 0
Y_ 74,4608
Y_ 75,4864
Mvaa 84,220,208
s_nop 0
Y_ 92,9216
Y_ 93,9472
Mvaa 84,196,168
s_nop 0
Y_ 94,9728
Y_ 95,9984
Mvaa 84,192,128
s_nop 0
Y_ 88,8192
Y_ 89,8448
Mvaa 80,220,204
s_nop 0
Y_ 90,8704
Y_ 91,8960
Mvaa 80,196,164
Mvaa 80,192,124
s_nop 0
Y_ 84,7168
Y_ 85,7424
Mvaa 76,220,200
s_nop 0
Y_ 86,7680
Y_ 87,7936
Mvaa 76,196,160
Mvaa 76,192,120
W_ 14
Y_ 80,6144
Y_ 81,6400
Mvaa 16,180,120
s_nop 0
Ta 192,v3,0
Ta 194,v3,2048
Mvaa 16,184,160
s_nop 0
Y_ 82,6656
Y_ 83,6912
Mvaa 16,188,200
s_nop 0
Ta 196,v3,4096
Ta 198,v3,6144
Mvaa 20,180,124
s_nop 0
Y_ 76,5120
Y_ 77,5376
Mvaa 20,184,164
s_nop 0
Ta 220,v3,8192
Ta 222,v3,10240
Mvaa 20,188,204
s_nop 0
Y_ 78,5632
Y_ 79,5888
Mvaa 24,180,128
Mvaa 24,184,168
Mvaa 24,188,208
s_waitcnt vmcnt(23) lgkmcnt(0)
s_barrier
Mvaa 28,180,132
S_ s55,s71,122880
ds_read2st64_b32 v[96:97], v5 offset0:0 offset1:1
Mvaa 28,184,172
S_ m0,s51,24576
ds_read2st64_b32 v[98:99], v5 offset0:2 offset1:3
Mvaa 28,188,212
L_ v7,24,s55,0
Mvaa 32,180,136
S_ s55,s71,133120
ds_read2st64_b32 v[104:105], v5 offset0:4 offset1:5
Mvaa 32,184,176
S_ m0,s51,25600
ds_read2st64_b32 v[106:107], v5 offset0:6 offset1:7
Mvaa 32,188,216
ds_read2st64_b32 v[112:113], v5 offset0:8 offset1:9
Mvaa 52,220,216
s_nop 0
Ta 180,%0,12288
Ta 182,%0,14336
Mvaa 52,196,176
L_ v7,24,s55,0
Mvaa 52,192,136
W_ 5
Ta 184,%0,16384
Ta 186,%0,18432
Mvaa 48,220,212
S_ s55,s71,143360
ds_read2st64_b32 v[114:115], v5 offset0:10 offset1:11
A_ v96,v16,v96
Mvaa 48,196,172
S_ m0,s51,26624
Ta 188,%0,20480
Ta 190,%0,22528
Mvaa 48,192,132
L_ v7,24,s55,0
Mvaa 44,220,208
S_ s55,s71,153600
ds_read2st64_b32 v[120:121], v5 offset0:12 offset1:13
A_ v97,v17,v97
Mvaa 44,196,168
S_ m0,s51,27648
ds_read2st64_b32 v[122:123], v5 offset0:14 offset1:15
A_ v98,v18,v98
Mvaa 44,192,128
ds_read2st64_b32 v[128:129], v5 offset0:16 offset1:17
A_ v99,v19,v99
Mvaa 40,220,204
L_ v7,24,s55,0
Mvaa 40,196,164
W_ 11
ds_read2st64_b32 v[130:131], v5 offset0:18 offset1:19
A_ v104,v20,v104
Mvaa 40,192,124
S_ s55,s71,163840
ds_read2st64_b32 v[132:133], v5 offset0:36 offset1:37
A_ v105,v21,v105
Mvaa 36,220,200
S_ m0,s51,28672
ds_read2st64_b32 v[134:135], v5 offset0:38 offset1:39
A_ v106,v22,v106
Mvaa 36,196,160
L_ v7,24,s55,0
Mvaa 36,192,120
W_ 6
ds_read2st64_b32 v[124:125], v5 offset0:32 offset1:33
A_ v107,v23,v107
Zvav 56,180,136
Ta 192,v1,12288
Ta 194,v1,14336
Mvav 56,184,156
S_ s55,s71,174080
ds_read2st64_b32 v[126:127], v5 offset0:34 offset1:35
A_ v112,v24,v112
Mvav 56,188,176
S_ m0,s51,29696
Ta 196,v1,16384
Ta 198,v1,18432
Zvav 60,180,140
L_ v7,24,s55,0
Mvav 60,184,160
S_ s79,s6,3
Ta 220,v1,20480
Ta 222,v1,22528
Mvav 60,188,180
s_and_b32 s79, s79, 7
ds_read2st64_b32 v[116:117], v5 offset0:28 offset1:29
A_ v113,v25,v113
Zvav 64,180,144
s_lshl_b32 s79, s79, 7
ds_read2st64_b32 v[118:119], v5 offset0:30 offset1:31
A_ v114,v26,v114
Mvav 64,184,164
S_ s71,s72,s79
ds_read2st64_b32 v[108:109], v5 offset0:24 offset1:25
A_ v115,v27,v115
Mvav 64,188,184
W_ 14
ds_read2st64_b32 v[110:111], v5 offset0:26 offset1:27
A_ v120,v28,v120
Zvav 68,180,148
S_ s55,s71,491520
ds_read2st64_b32 v[100:101], v5 offset0:20 offset1:21
A_ v121,v29,v121
Mvav 68,184,168
S_ m0,s51,0
ds_read2st64_b32 v[102:103], v5 offset0:22 offset1:23
A_ v122,v30,v122
Mvav 68,188,188
L_ v7,24,s55,0
Zvav 72,180,152
S_ s55,s71,501760
A_ v123,v31,v123
Mvav 72,184,172
W_ 14
A_ v128,v32,v128
A_ v129,v33,v129
Mvav 72,188,192
W_ 6
A_ v130,v34,v130
A_ v131,v35,v131
Zvav 92,220,192
Ta 180,v2,12288
Ta 182,v2,14336
Mvav 92,196,172
S_ m0,s51,1024
A_ v132,v52,v132
A_ v133,v53,v133
Mvav 92,192,152
s_nop 0
Ta 184,v2,16384
Ta 186,v2,18432
Zvav 88,220,188
L_ v7,24,s55,0
Mvav 88,196,168
S_ s55,s71,512000
Ta 188,v2,20480
Ta 190,v2,22528
Mvav 88,192,148
S_ m0,s51,2048
A_ v134,v54,v134
A_ v135,v55,v135
Zvav 84,220,184
A_ v124,v48,v124
A_ v125,v49,v125
Mvav 84,196,164
L_ v7,24,s55,0
Mvav 84,192,144
S_ s55,s71,522240
A_ v126,v50,v126
A_ v127,v51,v127
Zvav 80,220,180
W_ 10
A_ v116,v44,v116
A_ v117,v45,v117
Mvav 80,196,160
S_ m0,s51,3072
A_ v118,v46,v118
A_ v119,v47,v119
Mvav 80,192,140
L_ v7,24,s55,0
Zvav 76,220,176
W_ 8
A_ v108,v40,v108
A_ v109,v41,v109
Mvav 76,196,156
S_ s55,s71,532480
A_ v110,v42,v110
A_ v111,v43,v111
Mvav 76,192,136
W_ 0
A_ v100,v36,v100
Zvav 16,180,136
S_ m0,s51,4096
Ta 192,v3,12288
Ta 194,v3,14336
Mvav 16,184,156
L_ v7,24,s55,0
Mvav 16,188,176
S_ s55,s71,542720
Ta 196,v3,16384
Ta 198,v3,18432
Zvav 20,180,140
S_ m0,s51,5120
A_ v101,v37,v101
A_ v102,v38,v102
Mvav 20,184,160
S_ s85,s6,5
Ta 220,v3,20480
Ta 222,v3,22528
Mvav 20,188,180
L_ v7,24,s55,0
Zvav 24,180,144
s_and_b32 s85, s85, 7
A_ v103,v39,v103
C_ 131,130,131
Mvav 24,184,164
s_lshl_b32 s85, s85, 7
C_ 130,128,129
C_ 132,132,133
Mvav 24,188,184
S_ s85,s85,s87
C_ 133,134,135
Zvav 28,180,148
buffer_store_dwordx4 v[130:133], v6, s[28:31], s73 offen offset:0 sc1
Mvav 28,184,168
s_sub_u32 s73, s73, 4096
Mvav 28,188,188
C_ 123,122,123
C_ 122,120,121
Zvav 32,180,152
C_ 124,124,125
C_ 125,126,127
Mvav 32,184,172
buffer_store_dwordx4 v[122:125], v6, s[28:31], s73 offen offset:3072 sc1
Mvav 32,188,192
s_waitcnt vmcnt(21) lgkmcnt(0)
s_barrier
C_ 115,114,115
Zvav 52,220,192
S_ s56,s76,s74
Ta 180,%0,49152
Ta 182,%0,51200
Mvav 52,196,172
S_ s55,s71,614400
C_ 114,112,113
C_ 116,116,117
Mvav 52,192,152
S_ m0,s51,12288
Ta 184,%0,53248
Ta 186,%0,55296
Zvav 48,220,188
buffer_load_dwordx4 a[60:63], v6, s[28:31], s56 offen offset:0 sc1
Mvav 48,196,168
S_ s56,s76,s82
Ta 188,%0,57344
Ta 190,%0,59392
Mvav 48,192,148
C_ 117,118,119
Zvav 44,220,184
Mvav 44,196,164
buffer_load_dwordx4 a[64:67], v6, s[28:31], s56 offen offset:0 sc1
Mvav 44,192,144
Zvav 40,220,180
Mvav 40,196,160
Mvav 40,192,140
buffer_store_dwordx4 v[114:117], v6, s[28:31], s73 offen offset:2048 sc1
Zvav 36,220,176
C_ 107,106,107
C_ 106,104,105
Mvav 36,196,156
C_ 108,108,109
C_ 109,110,111
Mvav 36,192,136
W_ 0
Mvav 56,180,196
s_nop 0
Ta 192,v1,49152
Ta 194,v1,51200
Mvav 56,184,216
buffer_store_dwordx4 v[106:109], v6, s[28:31], s73 offen offset:1024 sc1
Mvav 56,188,236
s_nop 0
Ta 196,v1,53248
Ta 198,v1,55296
Mvav 60,180,200
C_ 99,98,99
C_ 98,96,97
Mvav 60,184,220
s_nop 0
Ta 220,v1,57344
Ta 222,v1,59392
Mvav 60,188,240
L_ v7,24,s55,0
Mvav 64,180,204
S_ s55,s71,624640
C_ 100,100,101
C_ 101,102,103
Mvav 64,184,224
S_ m0,s51,13312
Mvav 64,188,244
Mvav 68,180,208
buffer_store_dwordx4 v[98:101], v6, s[28:31], s73 offen offset:0 sc1
Mvav 68,184,228
Mvav 68,188,248
Mvav 72,180,212
Mvav 72,184,232
L_ v7,24,s55,0
Mvav 72,188,252
W_ 0
S_ s55,s71,634880
Mvav 92,220,252
S_ m0,s51,14336
Ta 180,v2,49152
Ta 182,v2,51200
Mvav 92,196,232
Mvav 92,192,212
s_nop 0
Ta 184,v2,53248
Ta 186,v2,55296
Mvav 88,220,248
L_ v7,24,s55,0
Mvav 88,196,228
S_ s55,s71,645120
Ta 188,v2,57344
Ta 190,v2,59392
Mvav 88,192,208
S_ m0,s51,15360
Mvav 84,220,244
Mvav 84,196,224
L_ v7,24,s55,0
Mvav 84,192,204
S_ s55,s71,655360
Mvav 80,220,240
S_ m0,s51,16384
Mvav 80,196,220
S_ s83,s6,7
Mvav 80,192,200
L_ v7,24,s55,0
Mvav 76,220,236
s_and_b32 s83, s83, 7
S_ s55,s71,665600
Mvav 76,196,216
s_lshl_b32 s83, s83, 2
S_ m0,s51,17408
Mvav 76,192,196
W_ 0
v_cmp_eq_u32_e64 s[36:37], s83, v8
Mvav 16,180,196
s_nop 0
Ta 192,v3,49152
Ta 194,v3,51200
Mvav 16,184,216
L_ v7,24,s55,0
Mvav 16,188,236
s_nop 0
Ta 196,v3,53248
Ta 198,v3,55296
Mvav 20,180,200
v_mov_b32 v10, 0x7fff0000
Mvav 20,184,220
s_nop 0
Ta 220,v3,57344
Ta 222,v3,59392
Mvav 20,188,240
buffer_load_dword a74, v4, s[24:27], s85 offen
Mvav 24,180,204
v_cndmask_b32_e64 v10, v10, v9, s[36:37]
Mvav 24,184,224
Mvav 24,188,244
Mvav 28,180,208
Mvav 28,184,228
Mvav 28,188,248
Mvav 32,180,212
Mvav 32,184,232
Mvav 32,188,252
s_waitcnt vmcnt(27) lgkmcnt(0)
s_barrier
Mvav 52,220,252
S_ s55,s71,0
Ta 180,%0,36864
Ta 182,%0,38912
Mvav 52,196,232
buffer_store_dword a255, v10, s[92:95], 0 offen offset:384 sc1
Mvav 52,192,212
S_ m0,s51,49152
Ta 184,%0,40960
Ta 186,%0,43008
Mvav 48,220,248
Mvav 48,196,228
s_nop 0
Ta 188,%0,45056
Ta 190,%0,47104
Mvav 48,192,208
L_ v7,24,s55,0
Mvav 44,220,244
S_ s55,s71,10240
Mvav 44,196,224
S_ m0,s51,50176
Mvav 44,192,204
Mvav 40,220,240
L_ v7,24,s55,0
Mvav 40,196,220
S_ s55,s71,20480
Mvav 40,192,200
S_ m0,s51,51200
Mvav 36,220,236
Mvav 36,196,216
L_ v7,24,s55,0
Mvav 36,192,196
W_ 0
S_ s55,s71,30720
Mvaa 56,180,0
S_ m0,s51,52224
Ta 192,v1,36864
Ta 194,v1,38912
Mvaa 56,184,40
Mvaa 56,188,80
s_nop 0
Ta 196,v1,40960
Ta 198,v1,43008
Mvaa 60,180,4
L_ v7,24,s55,0
Mvaa 60,184,44
S_ s55,s71,40960
Ta 220,v1,45056
Ta 222,v1,47104
Mvaa 60,188,84
S_ m0,s51,53248
Mvaa 64,180,8
Mvaa 64,184,48
L_ v7,24,s55,0
Mvaa 64,188,88
S_ s55,s71,51200
Mvaa 68,180,12
S_ m0,s51,54272
Mvaa 68,184,52
Mvaa 68,188,240
L_ v7,24,s55,0
Mvaa 72,180,16
Mvaa 72,184,56
Mvaa 72,188,244
W_ 0
Mvaa 92,220,244
s_nop 0
Ta 180,v2,36864
Ta 182,v2,38912
Mvaa 92,196,56
Mvaa 92,192,16
s_nop 0
Ta 184,v2,40960
Ta 186,v2,43008
Mvaa 88,220,240
Mvaa 88,196,52
s_nop 0
Ta 188,v2,45056
Ta 190,v2,47104
Mvaa 88,192,12
Mvaa 84,220,88
Mvaa 84,196,48
Mvaa 84,192,8
Mvaa 80,220,84
Mvaa 80,196,44
Mvaa 80,192,4
Mvaa 76,220,80
Mvaa 76,196,40
Mvaa 76,192,0
W_ 0
Mvaa 16,180,0
s_nop 0
Ta 192,v3,36864
Ta 194,v3,38912
Mvaa 16,184,40
Mvaa 16,188,80
s_nop 0
Ta 196,v3,40960
Ta 198,v3,43008
Mvaa 20,180,4
Mvaa 20,184,44
s_nop 0
Ta 220,v3,45056
Ta 222,v3,47104
Mvaa 20,188,84
Mvaa 24,180,8
Mvaa 24,184,48
Mvaa 24,188,88
Mvaa 28,180,12
Mvaa 28,184,52
Mvaa 28,188,240
Mvaa 32,180,16
Mvaa 32,184,56
Mvaa 32,188,244
s_waitcnt vmcnt(27) lgkmcnt(0)
s_barrier
Mvaa 52,220,244
s_nop 0
Ta 180,%0,24576
Ta 182,%0,26624
Mvaa 52,196,56
Mvaa 52,192,16
s_nop 0
Ta 184,%0,28672
Ta 186,%0,30720
Mvaa 48,220,240
Mvaa 48,196,52
s_nop 0
Ta 188,%0,32768
Ta 190,%0,34816
Mvaa 48,192,12
Mvaa 44,220,88
Mvaa 44,196,48
Mvaa 44,192,8
Mvaa 40,220,84
Mvaa 40,196,44
Mvaa 40,192,4
Mvaa 36,220,80
Mvaa 36,196,40
Mvaa 36,192,0
W_ 0
Mvaa 56,180,120
S_ s81,s6,2
Ta 192,v1,24576
Ta 194,v1,26624
Mvaa 56,184,160
s_and_b32 s81, s81, 7
Mvaa 56,188,200
s_mul_i32 s81, s81, 20480
Ta 196,v1,28672
Ta 198,v1,30720
Mvaa 60,180,124
s_mov_b32 m0, s52
S_ s73,s81,s78
Mvaa 60,184,164
S_ s73,s73,4096
Ta 220,v1,32768
Ta 222,v1,34816
Mvaa 60,188,204
s_nop 0
Y_ 56,0
Y_ 57,256
Mvaa 64,180,128
s_nop 0
Y_ 58,512
Y_ 59,768
Mvaa 64,184,168
Mvaa 64,188,208
s_nop 0
Y_ 60,1024
Y_ 61,1280
Mvaa 68,180,132
s_nop 0
Y_ 62,1536
Y_ 63,1792
Mvaa 68,184,172
Mvaa 68,188,212
s_nop 0
Y_ 64,2048
Y_ 65,2304
Mvaa 72,180,136
s_nop 0
Y_ 66,2560
Y_ 67,2816
Mvaa 72,184,176
Mvaa 72,188,216
W_ 12
Y_ 68,3072
Y_ 69,3328
Mvaa 92,220,216
s_nop 0
Ta 180,v2,24576
Ta 182,v2,26624
Mvaa 92,196,176
s_nop 0
Y_ 70,3584
Y_ 71,3840
Mvaa 92,192,136
s_nop 0
Ta 184,v2,28672
Ta 186,v2,30720
Mvaa 88,220,212
s_nop 0
Y_ 72,4096
Y_ 73,4352
Mvaa 88,196,172
S_ s83,s6,1
Ta 188,v2,32768
Ta 190,v2,34816
Mvaa 88,192,132
s_and_b32 s83, s83, 7
Y_ 74,4608
Y_ 75,4864
Mvaa 84,220,208
s_lshl_b32 s83, s83, 2
Y_ 92,9216
Y_ 93,9472
Mvaa 84,196,168
s_nop 0
Y_ 94,9728
Y_ 95,9984
Mvaa 84,192,128
s_nop 0
Y_ 88,8192
Y_ 89,8448
Mvaa 80,220,204
s_nop 0
Y_ 90,8704
Y_ 91,8960
Mvaa 80,196,164
v_cmp_eq_u32_e64 s[36:37], s83, v8
Mvaa 80,192,124
s_nop 0
Y_ 84,7168
Y_ 85,7424
Mvaa 76,220,200
s_nop 0
Y_ 86,7680
Y_ 87,7936
Mvaa 76,196,160
v_mov_b32 v10, 0x7fff0000
Mvaa 76,192,120
W_ 14
Y_ 80,6144
Y_ 81,6400
Mvaa 16,180,120
s_nop 0
Ta 192,v3,24576
Ta 194,v3,26624
Mvaa 16,184,160
s_nop 0
Y_ 82,6656
Y_ 83,6912
Mvaa 16,188,200
s_nop 0
Ta 196,v3,28672
Ta 198,v3,30720
Mvaa 20,180,124
s_nop 0
Y_ 76,5120
Y_ 77,5376
Mvaa 20,184,164
s_nop 0
Ta 220,v3,32768
Ta 222,v3,34816
Mvaa 20,188,204
s_nop 0
Y_ 78,5632
Y_ 79,5888
Mvaa 24,180,128
v_cndmask_b32_e64 v10, v10, v9, s[36:37]
Mvaa 24,184,168
Mvaa 24,188,208
s_waitcnt vmcnt(13) lgkmcnt(0)
s_barrier
Mvaa 28,180,132
buffer_store_dword a255, v10, s[92:95], 0 offen offset:384 sc1
Mvaa 28,184,172
S_ s55,s71,122880
ds_read2st64_b32 v[96:97], v5 offset0:0 offset1:1
Mvaa 28,188,212
S_ m0,s51,36864
ds_read2st64_b32 v[98:99], v5 offset0:2 offset1:3
Mvaa 32,180,136
ds_read2st64_b32 v[104:105], v5 offset0:4 offset1:5
Mvaa 32,184,176
L_ v7,24,s55,0
Mvaa 32,188,216
S_ s55,s71,133120
ds_read2st64_b32 v[106:107], v5 offset0:6 offset1:7
Mvaa 52,220,216
S_ m0,s51,37888
Ta 180,%0,0
Ta 182,%0,2048
Mvaa 52,196,176
ds_read2st64_b32 v[112:113], v5 offset0:8 offset1:9
Mvaa 52,192,136
W_ 5
Ta 184,%0,4096
Ta 186,%0,6144
Mvaa 48,220,212
L_ v7,24,s55,0
Mvaa 48,196,172
S_ s55,s71,143360
Ta 188,%0,8192
)A"
R"A(Ta 190,%0,10240
Mvaa 48,192,132
S_ m0,s51,38912
ds_read2st64_b32 v[114:115], v5 offset0:10 offset1:11
A_ v96,v16,v96
Mvaa 44,220,208
ds_read2st64_b32 v[120:121], v5 offset0:12 offset1:13
A_ v97,v17,v97
Mvaa 44,196,168
L_ v7,24,s55,0
Mvaa 44,192,128
S_ s55,s71,153600
ds_read2st64_b32 v[122:123], v5 offset0:14 offset1:15
A_ v98,v18,v98
Mvaa 40,220,204
S_ m0,s51,39936
ds_read2st64_b32 v[128:129], v5 offset0:16 offset1:17
A_ v99,v19,v99
Mvaa 40,196,164
W_ 11
ds_read2st64_b32 v[130:131], v5 offset0:18 offset1:19
A_ v104,v20,v104
Mvaa 40,192,124
L_ v7,24,s55,0
Mvaa 36,220,200
S_ s55,s71,163840
ds_read2st64_b32 v[132:133], v5 offset0:36 offset1:37
A_ v105,v21,v105
Mvaa 36,196,160
S_ m0,s51,40960
ds_read2st64_b32 v[134:135], v5 offset0:38 offset1:39
A_ v106,v22,v106
Mvaa 36,192,120
ds_read2st64_b32 v[124:125], v5 offset0:32 offset1:33
A_ v107,v23,v107
s_waitcnt vmcnt(22)
Zaaa 224,70,60
L_ v7,24,s55,0
Zaaa 228,68,60
W_ 7
ds_read2st64_b32 v[126:127], v5 offset0:34 offset1:35
A_ v112,v24,v112
Zaaa 232,70,64
S_ s55,s71,174080
ds_read2st64_b32 v[116:117], v5 offset0:28 offset1:29
A_ v113,v25,v113
Zaaa 236,68,64
ds_read2st64_b32 v[118:119], v5 offset0:30 offset1:31
A_ v114,v26,v114
Zvav 56,180,136
S_ m0,s51,41984
Ta 192,v1,0
Ta 194,v1,2048
Mvav 56,184,156
L_ v7,24,s55,0
Mvav 56,188,176
S_ s79,s6,4
Ta 196,v1,4096
Ta 198,v1,6144
Zvav 60,180,140
s_and_b32 s79, s79, 7
ds_read2st64_b32 v[108:109], v5 offset0:24 offset1:25
A_ v115,v27,v115
Mvav 60,184,160
W_ 13
Ta 220,v1,8192
Ta 222,v1,10240
Mvav 60,188,180
s_lshl_b32 s79, s79, 7
ds_read2st64_b32 v[110:111], v5 offset0:26 offset1:27
A_ v120,v28,v120
Zvav 64,180,144
S_ s71,s72,s79
ds_read2st64_b32 v[100:101], v5 offset0:20 offset1:21
A_ v121,v29,v121
Mvav 64,184,164
S_ s55,s71,491520
ds_read2st64_b32 v[102:103], v5 offset0:22 offset1:23
A_ v122,v30,v122
Mvav 64,188,184
S_ m0,s51,24576
A_ v123,v31,v123
R_ v12,a248
Zvav 68,180,148
L_ v7,24,s55,0
Mvav 68,184,168
W_ 14
A_ v128,v32,v128
A_ v129,v33,v129
Mvav 68,188,188
S_ s55,s71,501760
A_ v130,v34,v130
A_ v131,v35,v131
Zvav 72,180,152
A_ v132,v52,v132
A_ v133,v53,v133
Mvav 72,184,172
S_ m0,s51,25600
A_ v134,v54,v134
A_ v135,v55,v135
Mvav 72,188,192
W_ 3
A_ v124,v48,v124
Zvav 92,220,192
s_nop 0
Ta 180,v2,0
Ta 182,v2,2048
Mvav 92,196,172
L_ v7,24,s55,0
Mvav 92,192,152
S_ s55,s71,512000
Ta 184,v2,4096
Ta 186,v2,6144
Zvav 88,220,188
S_ m0,s51,26624
A_ v125,v49,v125
A_ v126,v50,v126
Mvav 88,196,168
s_nop 0
Ta 188,v2,8192
Ta 190,v2,10240
Mvav 88,192,148
L_ v7,24,s55,0
Zvav 84,220,184
S_ s55,s71,522240
A_ v127,v51,v127
C_ 131,130,131
Mvav 84,196,164
A_ v116,v44,v116
A_ v117,v45,v117
Mvav 84,192,144
S_ m0,s51,27648
A_ v118,v46,v118
A_ v119,v47,v119
Zvav 80,220,180
L_ v7,24,s55,0
Mvav 80,196,160
W_ 8
A_ v108,v40,v108
A_ v109,v41,v109
Mvav 80,192,140
S_ s55,s71,532480
A_ v110,v42,v110
A_ v111,v43,v111
Zvav 76,220,176
W_ 6
A_ v100,v36,v100
A_ v101,v37,v101
Mvav 76,196,156
S_ m0,s51,28672
A_ v102,v38,v102
A_ v103,v39,v103
Mvav 76,192,136
W_ 0
C_ 130,128,129
C_ 132,132,133
Zvav 16,180,136
S_ s85,s6,6
Ta 192,v3,0
Ta 194,v3,2048
Mvav 16,184,156
L_ v7,24,s55,0
Mvav 16,188,176
S_ s55,s71,542720
Ta 196,v3,4096
Ta 198,v3,6144
Zvav 20,180,140
S_ m0,s51,29696
C_ 133,134,135
v_lshlrev_b32 v12, 10, v12
Mvav 20,184,160
s_and_b32 s85, s85, 7
Ta 220,v3,8192
Ta 222,v3,10240
Mvav 20,188,180
buffer_store_dwordx4 v[130:133], v6, s[28:31], s73 offen offset:0 sc1
Zvav 24,180,144
s_sub_u32 s73, s73, 4096
s_lshl_b32 s85, s85, 7
Mvav 24,184,164
S_ s85,s85,s87
C_ 123,122,123
C_ 122,120,121
Mvav 24,188,184
C_ 124,124,125
C_ 125,126,127
Zvav 28,180,148
buffer_store_dwordx4 v[122:125], v6, s[28:31], s73 offen offset:3072 sc1
Mvav 28,184,168
C_ 115,114,115
C_ 114,112,113
Mvav 28,188,188
C_ 116,116,117
C_ 117,118,119
Zvav 32,180,152
Mvav 32,184,172
buffer_store_dwordx4 v[114:117], v6, s[28:31], s73 offen offset:2048 sc1
Mvav 32,188,192
s_waitcnt vmcnt(23) lgkmcnt(0)
s_barrier
C_ 107,106,107
Zvav 52,220,192
s_nop 0
Ta 180,%0,12288
Ta 182,%0,14336
Mvav 52,196,172
C_ 106,104,105
C_ 108,108,109
Mvav 52,192,152
s_nop 0
Ta 184,%0,16384
Ta 186,%0,18432
Zvav 48,220,188
L_ v7,24,s55,0
Mvav 48,196,168
S_ s55,s71,614400
Ta 188,%0,20480
Ta 190,%0,22528
Mvav 48,192,148
S_ m0,s51,0
C_ 109,110,111
Zvav 44,220,184
Mvav 44,196,164
buffer_store_dwordx4 v[106:109], v6, s[28:31], s73 offen offset:1024 sc1
Mvav 44,192,144
C_ 99,98,99
C_ 98,96,97
Zvav 40,220,180
C_ 100,100,101
C_ 101,102,103
Mvav 40,196,160
Mvav 40,192,140
buffer_store_dwordx4 v[98:101], v6, s[28:31], s73 offen offset:0 sc1
Zvav 36,220,176
Mvav 36,196,156
Mvav 36,192,136
W_ 0
Mvav 56,180,196
s_nop 0
Ta 192,v1,12288
Ta 194,v1,14336
Mvav 56,184,216
L_ v7,24,s55,0
Mvav 56,188,236
S_ s55,s71,624640
Ta 196,v1,16384
Ta 198,v1,18432
Mvav 60,180,200
S_ m0,s51,1024
Mvav 60,184,220
s_nop 0
Ta 220,v1,20480
Ta 222,v1,22528
Mvav 60,188,240
L_ v7,24,s55,0
Mvav 64,180,204
S_ s55,s71,634880
Mvav 64,184,224
S_ m0,s51,2048
Mvav 64,188,244
Mvav 68,180,208
L_ v7,24,s55,0
Mvav 68,184,228
S_ s55,s71,645120
Mvav 68,188,248
S_ m0,s51,3072
Mvav 72,180,212
Mvav 72,184,232
L_ v7,24,s55,0
Mvav 72,188,252
W_ 0
S_ s55,s71,655360
Mvav 92,220,252
S_ m0,s51,4096
Ta 180,v2,12288
Ta 182,v2,14336
Mvav 92,196,232
Mvav 92,192,212
s_nop 0
Ta 184,v2,16384
Ta 186,v2,18432
Mvav 88,220,248
L_ v7,24,s55,0
Mvav 88,196,228
S_ s55,s71,665600
Ta 188,v2,20480
Ta 190,v2,22528
Mvav 88,192,208
S_ m0,s51,5120
Mvav 84,220,244
Mvav 84,196,224
L_ v7,24,s55,0
Mvav 84,192,204
Mvav 80,220,240
Mvav 80,196,220
Mvav 80,192,200
buffer_load_dword a74, v12, s[44:47], s66 offen
Mvav 76,220,236
Mvav 76,196,216
Mvav 76,192,196
W_ 0
Mvav 16,180,196
s_nop 0
Ta 192,v3,12288
Ta 194,v3,14336
Mvav 16,184,216
buffer_load_dword a74, v4, s[24:27], s85 offen
Mvav 16,188,236
s_nop 0
Ta 196,v3,16384
Ta 198,v3,18432
Mvav 20,180,200
Mvav 20,184,220
s_nop 0
Ta 220,v3,20480
Ta 222,v3,22528
Mvav 20,188,240
Mvav 24,180,204
Mvav 24,184,224
Mvav 24,188,244
Mvav 28,180,208
Mvav 28,184,228
Mvav 28,188,248
Mvav 32,180,212
Mvav 32,184,232
Mvav 32,188,252
s_waitcnt vmcnt(26) lgkmcnt(0)
s_barrier
Mvav 52,220,252
S_ s55,s71,0
Ta 180,%0,49152
Ta 182,%0,51200
Mvav 52,196,232
S_ m0,s51,12288
Mvav 52,192,212
s_nop 0
Ta 184,%0,53248
Ta 186,%0,55296
Mvav 48,220,248
L_ v7,24,s55,0
Mvav 48,196,228
S_ s55,s71,10240
Ta 188,%0,57344
Ta 190,%0,59392
Mvav 48,192,208
S_ m0,s51,13312
Mvav 44,220,244
Mvav 44,196,224
L_ v7,24,s55,0
Mvav 44,192,204
S_ s55,s71,20480
Mvav 40,220,240
S_ m0,s51,14336
Mvav 40,196,220
Mvav 40,192,200
L_ v7,24,s55,0
Mvav 36,220,236
S_ s55,s71,30720
Mvav 36,196,216
S_ m0,s51,15360
Mvav 36,192,196
W_ 0
Mvaa 56,180,0
s_nop 0
Ta 192,v1,49152
Ta 194,v1,51200
Mvaa 56,184,40
L_ v7,24,s55,0
Mvaa 56,188,80
S_ s55,s71,40960
Ta 196,v1,53248
Ta 198,v1,55296
Mvaa 60,180,4
S_ m0,s51,16384
Mvaa 60,184,44
s_nop 0
Ta 220,v1,57344
Ta 222,v1,59392
Mvaa 60,188,84
L_ v7,24,s55,0
Mvaa 64,180,8
S_ s55,s71,51200
Mvaa 64,184,48
S_ m0,s51,17408
Mvaa 64,188,88
Mvaa 68,180,12
L_ v7,24,s55,0
Mvaa 68,184,52
Mvaa 68,188,240
Mvaa 72,180,16
Mvaa 72,184,56
Mvaa 72,188,244
W_ 0
Mvaa 92,220,244
s_nop 0
Ta 180,v2,49152
Ta 182,v2,51200
Mvaa 92,196,56
Mvaa 92,192,16
s_nop 0
Ta 184,v2,53248
Ta 186,v2,55296
Mvaa 88,220,240
Mvaa 88,196,52
s_nop 0
Ta 188,v2,57344
Ta 190,v2,59392
Mvaa 88,192,12
Mvaa 84,220,88
Mvaa 84,196,48
Mvaa 84,192,8
Mvaa 80,220,84
Mvaa 80,196,44
Mvaa 80,192,4
Mvaa 76,220,80
Mvaa 76,196,40
Mvaa 76,192,0
W_ 0
Mvaa 16,180,0
s_nop 0
Ta 192,v3,49152
Ta 194,v3,51200
Mvaa 16,184,40
Mvaa 16,188,80
s_nop 0
Ta 196,v3,53248
Ta 198,v3,55296
Mvaa 20,180,4
Mvaa 20,184,44
s_nop 0
Ta 220,v3,57344
Ta 222,v3,59392
Mvaa 20,188,84
Mvaa 24,180,8
Mvaa 24,184,48
Mvaa 24,188,88
Mvaa 28,180,12
Mvaa 28,184,52
Mvaa 28,188,240
Mvaa 32,180,16
Mvaa 32,184,56
Mvaa 32,188,244
s_waitcnt vmcnt(25) lgkmcnt(0)
s_barrier
Mvaa 52,220,244
s_nop 0
Ta 180,%0,36864
Ta 182,%0,38912
Mvaa 52,196,56
Mvaa 52,192,16
s_nop 0
Ta 184,%0,40960
Ta 186,%0,43008
Mvaa 48,220,240
Mvaa 48,196,52
s_nop 0
Ta 188,%0,45056
Ta 190,%0,47104
Mvaa 48,192,12
Mvaa 44,220,88
Mvaa 44,196,48
Mvaa 44,192,8
Mvaa 40,220,84
Mvaa 40,196,44
Mvaa 40,192,4
Mvaa 36,220,80
Mvaa 36,196,40
Mvaa 36,192,0
W_ 0
Mvaa 56,180,120
S_ s81,s6,3
Ta 192,v1,36864
Ta 194,v1,38912
Mvaa 56,184,160
s_and_b32 s81, s81, 7
Mvaa 56,188,200
s_mul_i32 s81, s81, 20480
Ta 196,v1,40960
Ta 198,v1,43008
Mvaa 60,180,124
s_mov_b32 m0, s52
S_ s73,s81,s78
Mvaa 60,184,164
S_ s73,s73,4096
Ta 220,v1,45056
Ta 222,v1,47104
Mvaa 60,188,204
s_nop 0
Y_ 56,0
Y_ 57,256
Mvaa 64,180,128
s_nop 0
Y_ 58,512
Y_ 59,768
Mvaa 64,184,168
Mvaa 64,188,208
s_nop 0
Y_ 60,1024
Y_ 61,1280
Mvaa 68,180,132
s_nop 0
Y_ 62,1536
Y_ 63,1792
Mvaa 68,184,172
Mvaa 68,188,212
s_nop 0
Y_ 64,2048
Y_ 65,2304
Mvaa 72,180,136
s_nop 0
Y_ 66,2560
Y_ 67,2816
Mvaa 72,184,176
Mvaa 72,188,216
W_ 12
Y_ 68,3072
Y_ 69,3328
Mvaa 92,220,216
s_nop 0
Ta 180,v2,36864
Ta 182,v2,38912
Mvaa 92,196,176
s_nop 0
Y_ 70,3584
Y_ 71,3840
Mvaa 92,192,136
s_nop 0
Ta 184,v2,40960
Ta 186,v2,43008
Mvaa 88,220,212
s_nop 0
Y_ 72,4096
Y_ 73,4352
Mvaa 88,196,172
S_ s83,s6,2
Ta 188,v2,45056
Ta 190,v2,47104
Mvaa 88,192,132
s_and_b32 s83, s83, 7
Y_ 74,4608
Y_ 75,4864
Mvaa 84,220,208
s_lshl_b32 s83, s83, 2
Y_ 92,9216
Y_ 93,9472
Mvaa 84,196,168
s_nop 0
Y_ 94,9728
Y_ 95,9984
Mvaa 84,192,128
s_nop 0
Y_ 88,8192
Y_ 89,8448
Mvaa 80,220,204
s_nop 0
Y_ 90,8704
Y_ 91,8960
Mvaa 80,196,164
v_cmp_eq_u32_e64 s[36:37], s83, v8
Mvaa 80,192,124
s_nop 0
Y_ 84,7168
Y_ 85,7424
Mvaa 76,220,200
s_nop 0
Y_ 86,7680
Y_ 87,7936
Mvaa 76,196,160
v_mov_b32 v10, 0x7fff0000
Mvaa 76,192,120
W_ 14
Y_ 80,6144
Y_ 81,6400
Mvaa 16,180,120
s_nop 0
Ta 192,v3,36864
Ta 194,v3,38912
Mvaa 16,184,160
s_nop 0
Y_ 82,6656
Y_ 83,6912
Mvaa 16,188,200
s_nop 0
Ta 196,v3,40960
Ta 198,v3,43008
Mvaa 20,180,124
s_nop 0
Y_ 76,5120
Y_ 77,5376
Mvaa 20,184,164
s_nop 0
Ta 220,v3,45056
Ta 222,v3,47104
Mvaa 20,188,204
s_nop 0
Y_ 78,5632
Y_ 79,5888
Mvaa 24,180,128
v_cndmask_b32_e64 v10, v10, v9, s[36:37]
Mvaa 24,184,168
Mvaa 24,188,208
s_waitcnt vmcnt(14) lgkmcnt(0)
s_barrier
Mvaa 28,180,132
buffer_store_dword a255, v10, s[92:95], 0 offen offset:384 sc1
Mvaa 28,184,172
s_sub_u32 s75, s6, 1
ds_read2st64_b32 v[96:97], v5 offset0:0 offset1:1
S_ s55,s71,122880
Mvaa 28,188,212
s_and_b32 s75, s75, 7
ds_read2st64_b32 v[98:99], v5 offset0:2 offset1:3
S_ m0,s51,49152
Mvaa 32,180,136
ds_read2st64_b32 v[104:105], v5 offset0:4 offset1:5
Mvaa 32,184,176
buffer_load_dword v11, v8, s[92:95], s96 offen offset:384 sc1
Mvaa 32,188,216
ds_read2st64_b32 v[106:107], v5 offset0:6 offset1:7
Mvaa 52,220,216
s_nop 0
Ta 180,%0,24576
Ta 182,%0,26624
Mvaa 52,196,176
ds_read2st64_b32 v[112:113], v5 offset0:8 offset1:9
Mvaa 52,192,136
W_ 5
Ta 184,%0,28672
Ta 186,%0,30720
Mvaa 48,220,212
L_ v7,24,s55,0
Mvaa 48,196,172
S_ s55,s71,133120
Ta 188,%0,32768
Ta 190,%0,34816
Mvaa 48,192,132
S_ m0,s51,50176
ds_read2st64_b32 v[114:115], v5 offset0:10 offset1:11
A_ v96,v16,v96
Mvaa 44,220,208
ds_read2st64_b32 v[120:121], v5 offset0:12 offset1:13
A_ v97,v17,v97
Mvaa 44,196,168
L_ v7,24,s55,0
Mvaa 44,192,128
S_ s55,s71,143360
ds_read2st64_b32 v[122:123], v5 offset0:14 offset1:15
A_ v98,v18,v98
Mvaa 40,220,204
S_ m0,s51,51200
ds_read2st64_b32 v[128:129], v5 offset0:16 offset1:17
A_ v99,v19,v99
Mvaa 40,196,164
W_ 11
ds_read2st64_b32 v[130:131], v5 offset0:18 offset1:19
A_ v104,v20,v104
Mvaa 40,192,124
L_ v7,24,s55,0
Mvaa 36,220,200
S_ s55,s71,153600
ds_read2st64_b32 v[132:133], v5 offset0:36 offset1:37
A_ v105,v21,v105
Mvaa 36,196,160
S_ m0,s51,52224
ds_read2st64_b32 v[134:135], v5 offset0:38 offset1:39
A_ v106,v22,v106
Mvaa 36,192,120
W_ 7
ds_read2st64_b32 v[124:125], v5 offset0:32 offset1:33
A_ v107,v23,v107
Zvav 56,180,136
W_ 7
Ta 192,v1,24576
Ta 194,v1,26624
Mvav 56,184,156
L_ v7,24,s55,0
Mvav 56,188,176
S_ s55,s71,163840
Ta 196,v1,28672
Ta 198,v1,30720
Zvav 60,180,140
S_ m0,s51,53248
ds_read2st64_b32 v[126:127], v5 offset0:34 offset1:35
A_ v112,v24,v112
Mvav 60,184,160
s_nop 0
Ta 220,v1,32768
Ta 222,v1,34816
Mvav 60,188,180
L_ v7,24,s55,0
Zvav 64,180,144
S_ s55,s71,174080
ds_read2st64_b32 v[116:117], v5 offset0:28 offset1:29
A_ v113,v25,v113
Mvav 64,184,164
S_ m0,s51,54272
ds_read2st64_b32 v[118:119], v5 offset0:30 offset1:31
A_ v114,v26,v114
Mvav 64,188,184
ds_read2st64_b32 v[108:109], v5 offset0:24 offset1:25
A_ v115,v27,v115
Zvav 68,180,148
L_ v7,24,s55,0
Mvav 68,184,168
W_ 14
ds_read2st64_b32 v[110:111], v5 offset0:26 offset1:27
A_ v120,v28,v120
Mvav 68,188,188
S_ s79,s6,5
ds_read2st64_b32 v[100:101], v5 offset0:20 offset1:21
A_ v121,v29,v121
Zvav 72,180,152
s_and_b32 s79, s79, 7
ds_read2st64_b32 v[102:103], v5 offset0:22 offset1:23
A_ v122,v30,v122
Mvav 72,184,172
s_lshl_b32 s79, s79, 7
A_ v123,v31,v123
Mvav 72,188,192
W_ 6
A_ v128,v32,v128
Zvav 92,220,192
S_ s71,s72,s79
Ta 180,v2,24576
Ta 182,v2,26624
Mvav 92,196,172
S_ s55,s71,491520
A_ v129,v33,v129
A_ v130,v34,v130
Mvav 92,192,152
S_ m0,s51,36864
Ta 184,v2,28672
Ta 186,v2,30720
Zvav 88,220,188
L_ v7,24,s55,0
Mvav 88,196,168
S_ s55,s71,501760
Ta 188,v2,32768
Ta 190,v2,34816
Mvav 88,192,148
S_ m0,s51,37888
A_ v131,v35,v131
S_ s85,s6,7
Zvav 84,220,184
A_ v132,v52,v132
A_ v133,v53,v133
Mvav 84,196,164
L_ v7,24,s55,0
Mvav 84,192,144
S_ s55,s71,512000
A_ v134,v54,v134
A_ v135,v55,v135
Zvav 80,220,180
A_ v124,v48,v124
A_ v125,v49,v125
Mvav 80,196,160
S_ m0,s51,38912
A_ v126,v50,v126
A_ v127,v51,v127
Mvav 80,192,140
L_ v7,24,s55,0
Zvav 76,220,176
W_ 10
A_ v116,v44,v116
A_ v117,v45,v117
Mvav 76,196,156
S_ s55,s71,522240
A_ v118,v46,v118
A_ v119,v47,v119
Mvav 76,192,136
W_ 0
A_ v108,v40,v108
Zvav 16,180,136
S_ m0,s51,39936
Ta 192,v3,24576
Ta 194,v3,26624
Mvav 16,184,156
L_ v7,24,s55,0
Mvav 16,188,176
S_ s55,s71,532480
Ta 196,v3,28672
Ta 198,v3,30720
Zvav 20,180,140
S_ m0,s51,40960
A_ v109,v41,v109
A_ v110,v42,v110
Mvav 20,184,160
s_and_b32 s85, s85, 7
Ta 220,v3,32768
Ta 222,v3,34816
Mvav 20,188,180
L_ v7,24,s55,0
Zvav 24,180,144
S_ s55,s71,542720
A_ v111,v43,v111
C_ 131,130,131
Mvav 24,184,164
A_ v100,v36,v100
A_ v101,v37,v101
Mvav 24,188,184
S_ m0,s51,41984
A_ v102,v38,v102
A_ v103,v39,v103
Zvav 28,180,148
L_ v7,24,s55,0
Mvav 28,184,168
s_lshl_b32 s85, s85, 7
C_ 130,128,129
C_ 132,132,133
Mvav 28,188,188
S_ s85,s85,s87
C_ 133,134,135
Zvav 32,180,152
Mvav 32,184,172
buffer_store_dwordx4 v[130:133], v6, s[28:31], s73 offen offset:0 sc1
Mvav 32,188,192
s_waitcnt vmcnt(23) lgkmcnt(0)
s_barrier
s_sub_u32 s73, s73, 4096
Zvav 52,220,192
S_ s55,s71,614400
Ta 180,%0,0
Ta 182,%0,2048
Mvav 52,196,172
S_ m0,s51,24576
C_ 123,122,123
C_ 122,120,121
Mvav 52,192,152
s_nop 0
Ta 184,%0,4096
Ta 186,%0,6144
Zvav 48,220,188
L_ v7,24,s55,0
Mvav 48,196,168
S_ s55,s71,624640
Ta 188,%0,8192
Ta 190,%0,10240
Mvav 48,192,148
S_ m0,s51,25600
C_ 124,124,125
C_ 125,126,127
Zvav 44,220,184
Mvav 44,196,164
buffer_store_dwordx4 v[122:125], v6, s[28:31], s73 offen offset:3072 sc1
Mvav 44,192,144
C_ 115,114,115
C_ 114,112,113
Zvav 40,220,180
C_ 116,116,117
C_ 117,118,119
Mvav 40,196,160
Mvav 40,192,140
buffer_store_dwordx4 v[114:117], v6, s[28:31], s73 offen offset:2048 sc1
Zvav 36,220,176
C_ 107,106,107
C_ 106,104,105
Mvav 36,196,156
C_ 108,108,109
C_ 109,110,111
Mvav 36,192,136
W_ 0
Mvav 56,180,196
s_nop 0
Ta 192,v1,0
Ta 194,v1,2048
Mvav 56,184,216
buffer_store_dwordx4 v[106:109], v6, s[28:31], s73 offen offset:1024 sc1
Mvav 56,188,236
s_nop 0
Ta 196,v1,4096
Ta 198,v1,6144
Mvav 60,180,200
C_ 99,98,99
C_ 98,96,97
Mvav 60,184,220
s_nop 0
Ta 220,v1,8192
Ta 222,v1,10240
Mvav 60,188,240
L_ v7,24,s55,0
Mvav 64,180,204
S_ s55,s71,634880
C_ 100,100,101
C_ 101,102,103
Mvav 64,184,224
S_ m0,s51,26624
Mvav 64,188,244
Mvav 68,180,208
buffer_store_dwordx4 v[98:101], v6, s[28:31], s73 offen offset:0 sc1
Mvav 68,184,228
Mvav 68,188,248
Mvav 72,180,212
Mvav 72,184,232
L_ v7,24,s55,0
Mvav 72,188,252
W_ 0
S_ s55,s71,645120
Mvav 92,220,252
S_ m0,s51,27648
Ta 180,v2,0
Ta 182,v2,2048
Mvav 92,196,232
Mvav 92,192,212
s_nop 0
Ta 184,v2,4096
Ta 186,v2,6144
Mvav 88,220,248
L_ v7,24,s55,0
Mvav 88,196,228
s_waitcnt vmcnt(21)
Ta 188,v2,8192
Ta 190,v2,10240
Mvav 88,192,208
S_ s55,s71,655360
v_cmp_eq_u32 vcc, s62, v11
Mvav 84,220,244
s_mul_i32 s76, s75, 204800
S_ m0,s51,28672
Mvav 84,196,224
L_ v7,24,s55,0
Mvav 84,192,204
s_bitcmp1_b32 vcc_lo, s75
s_cbranch_scc0 .Lspin0
.Lspun0:
S_ s55,s71,665600
Mvav 80,220,240
S_ s56,s76,s84
S_ m0,s51,29696
Mvav 80,196,220
Mvav 80,192,200
buffer_load_dwordx4 a[20:23], v6, s[28:31], s56 offen offset:0 sc1
Mvav 76,220,236
Mvav 76,196,216
Mvav 76,192,196
W_ 0
Mvav 16,180,196
s_nop 0
Ta 192,v3,0
Ta 194,v3,2048
Mvav 16,184,216
buffer_load_dwordx4 a[24:27], v6, s[28:31], s56 offen offset:1024 sc1
Mvav 16,188,236
s_nop 0
Ta 196,v3,4096
Ta 198,v3,6144
Mvav 20,180,200
Mvav 20,184,220
s_nop 0
Ta 220,v3,8192
Ta 222,v3,10240
Mvav 20,188,240
buffer_load_dwordx4 a[28:31], v6, s[28:31], s56 offen offset:2048 sc1
Mvav 24,180,204
Mvav 24,184,224
Mvav 24,188,244
Mvav 28,180,208
buffer_load_dwordx4 a[32:35], v6, s[28:31], s56 offen offset:3072 sc1
Mvav 28,184,228
S_ s56,s56,4096
Mvav 28,188,248
Mvav 32,180,212
Mvav 32,184,232
buffer_load_dwordx4 a[36:39], v6, s[28:31], s56 offen offset:0 sc1
Mvav 32,188,252
s_waitcnt vmcnt(29) lgkmcnt(0)
s_barrier
S_ s56,s76,s74
Mvav 52,220,252
s_nop 0
Ta 180,%0,12288
Ta 182,%0,14336
Mvav 52,196,232
Mvav 52,192,212
s_nop 0
Ta 184,%0,16384
Ta 186,%0,18432
Mvav 48,220,248
buffer_load_dwordx4 a[60:63], v6, s[28:31], s56 offen offset:0 sc1
Mvav 48,196,228
S_ s56,s76,s82
Ta 188,%0,20480
Ta 190,%0,22528
Mvav 48,192,208
Mvav 44,220,244
Mvav 44,196,224
buffer_load_dwordx4 a[64:67], v6, s[28:31], s56 offen offset:0 sc1
Mvav 44,192,204
Mvav 40,220,240
Mvav 40,196,220
Mvav 40,192,200
L_ v7,24,s55,0
Mvav 36,220,236
S_ s55,s71,0
Mvav 36,196,216
S_ m0,s51,0
Mvav 36,192,196
W_ 0
Mvaa 56,180,0
s_nop 0
Ta 192,v1,12288
Ta 194,v1,14336
Mvaa 56,184,40
L_ v7,24,s55,0
Mvaa 56,188,80
S_ s55,s71,10240
Ta 196,v1,16384
Ta 198,v1,18432
Mvaa 60,180,4
S_ m0,s51,1024
Mvaa 60,184,44
s_nop 0
Ta 220,v1,20480
Ta 222,v1,22528
Mvaa 60,188,84
L_ v7,24,s55,0
Mvaa 64,180,8
S_ s55,s71,20480
Mvaa 64,184,48
S_ m0,s51,2048
Mvaa 64,188,88
Mvaa 68,180,12
L_ v7,24,s55,0
Mvaa 68,184,52
S_ s55,s71,30720
Mvaa 68,188,240
S_ m0,s51,3072
Mvaa 72,180,16
Mvaa 72,184,56
L_ v7,24,s55,0
Mvaa 72,188,244
W_ 0
S_ s55,s71,40960
Mvaa 92,220,244
S_ m0,s51,4096
Ta 180,v2,12288
Ta 182,v2,14336
Mvaa 92,196,56
Mvaa 92,192,16
s_nop 0
Ta 184,v2,16384
Ta 186,v2,18432
Mvaa 88,220,240
L_ v7,24,s55,0
Mvaa 88,196,52
S_ s55,s71,51200
Ta 188,v2,20480
Ta 190,v2,22528
Mvaa 88,192,12
S_ m0,s51,5120
Mvaa 84,220,88
Mvaa 84,196,48
L_ v7,24,s55,0
Mvaa 84,192,8
Mvaa 80,220,84
Mvaa 80,196,44
Mvaa 80,192,4
buffer_load_dword a74, v4, s[24:27], s85 offen
Mvaa 76,220,80
Mvaa 76,196,40
Mvaa 76,192,0
W_ 0
Mvaa 16,180,0
s_nop 0
Ta 192,v3,12288
Ta 194,v3,14336
Mvaa 16,184,40
Mvaa 16,188,80
s_nop 0
Ta 196,v3,16384
Ta 198,v3,18432
Mvaa 20,180,4
Mvaa 20,184,44
s_nop 0
Ta 220,v3,20480
Ta 222,v3,22528
Mvaa 20,188,84
Mvaa 24,180,8
Mvaa 24,184,48
Mvaa 24,188,88
Mvaa 28,180,12
Mvaa 28,184,52
Mvaa 28,188,240
Mvaa 32,180,16
Mvaa 32,184,56
Mvaa 32,188,244
s_waitcnt vmcnt(31) lgkmcnt(0)
s_barrier
Mvaa 52,220,244
s_nop 0
Ta 180,%0,49152
Ta 182,%0,51200
Mvaa 52,196,56
Mvaa 52,192,16
s_nop 0
Ta 184,%0,53248
Ta 186,%0,55296
Mvaa 48,220,240
Mvaa 48,196,52
s_nop 0
Ta 188,%0,57344
Ta 190,%0,59392
Mvaa 48,192,12
Mvaa 44,220,88
Mvaa 44,196,48
Mvaa 44,192,8
Mvaa 40,220,84
Mvaa 40,196,44
Mvaa 40,192,4
Mvaa 36,220,80
Mvaa 36,196,40
Mvaa 36,192,0
W_ 0
Mvaa 56,180,120
S_ s81,s6,4
Ta 192,v1,49152
Ta 194,v1,51200
Mvaa 56,184,160
s_and_b32 s81, s81, 7
Mvaa 56,188,200
s_mul_i32 s81, s81, 20480
Ta 196,v1,53248
Ta 198,v1,55296
Mvaa 60,180,124
s_mov_b32 m0, s52
S_ s73,s81,s78
Mvaa 60,184,164
S_ s73,s73,4096
Ta 220,v1,57344
Ta 222,v1,59392
Mvaa 60,188,204
s_nop 0
Y_ 56,0
Y_ 57,256
Mvaa 64,180,128
s_nop 0
Y_ 58,512
Y_ 59,768
Mvaa 64,184,168
Mvaa 64,188,208
s_nop 0
Y_ 60,1024
Y_ 61,1280
Mvaa 68,180,132
s_nop 0
Y_ 62,1536
Y_ 63,1792
Mvaa 68,184,172
Mvaa 68,188,212
s_nop 0
Y_ 64,2048
Y_ 65,2304
Mvaa 72,180,136
s_nop 0
Y_ 66,2560
Y_ 67,2816
Mvaa 72,184,176
Mvaa 72,188,216
W_ 12
Y_ 68,3072
Y_ 69,3328
Mvaa 92,220,216
s_nop 0
Ta 180,v2,49152
Ta 182,v2,51200
Mvaa 92,196,176
s_nop 0
Y_ 70,3584
Y_ 71,3840
Mvaa 92,192,136
s_nop 0
Ta 184,v2,53248
Ta 186,v2,55296
Mvaa 88,220,212
s_nop 0
Y_ 72,4096
Y_ 73,4352
Mvaa 88,196,172
S_ s83,s6,3
Ta 188,v2,57344
Ta 190,v2,59392
Mvaa 88,192,132
s_and_b32 s83, s83, 7
Y_ 74,4608
Y_ 75,4864
Mvaa 84,220,208
s_lshl_b32 s83, s83, 2
Y_ 92,9216
Y_ 93,9472
Mvaa 84,196,168
s_nop 0
Y_ 94,9728
Y_ 95,9984
Mvaa 84,192,128
s_nop 0
Y_ 88,8192
Y_ 89,8448
Mvaa 80,220,204
s_nop 0
Y_ 90,8704
Y_ 91,8960
Mvaa 80,196,164
v_cmp_eq_u32_e64 s[36:37], s83, v8
Mvaa 80,192,124
s_nop 0
Y_ 84,7168
Y_ 85,7424
Mvaa 76,220,200
s_nop 0
Y_ 86,7680
Y_ 87,7936
Mvaa 76,196,160
v_mov_b32 v10, 0x7fff0000
Mvaa 76,192,120
W_ 14
Y_ 80,6144
Y_ 81,6400
Mvaa 16,180,120
s_nop 0
Ta 192,v3,49152
Ta 194,v3,51200
Mvaa 16,184,160
s_nop 0
Y_ 82,6656
Y_ 83,6912
Mvaa 16,188,200
s_nop 0
Ta 196,v3,53248
Ta 198,v3,55296
Mvaa 20,180,124
s_nop 0
Y_ 76,5120
Y_ 77,5376
Mvaa 20,184,164
s_nop 0
Ta 220,v3,57344
Ta 222,v3,59392
Mvaa 20,188,204
s_nop 0
Y_ 78,5632
Y_ 79,5888
Mvaa 24,180,128
v_cndmask_b32_e64 v10, v10, v9, s[36:37]
Mvaa 24,184,168
Mvaa 24,188,208
s_waitcnt vmcnt(18) lgkmcnt(0)
s_barrier
Mvaa 28,180,132
buffer_store_dword a255, v10, s[92:95], 0 offen offset:384 sc1
Mvaa 28,184,172
s_sub_u32 s75, s6, 2
ds_read2st64_b32 v[96:97], v5 offset0:0 offset1:1
S_ s55,s71,122880
Mvaa 28,188,212
s_and_b32 s75, s75, 7
ds_read2st64_b32 v[98:99], v5 offset0:2 offset1:3
S_ m0,s51,12288
Mvaa 32,180,136
ds_read2st64_b32 v[104:105], v5 offset0:4 offset1:5
Mvaa 32,184,176
buffer_load_dword v11, v8, s[92:95], s96 offen offset:384 sc1
Mvaa 32,188,216
ds_read2st64_b32 v[106:107], v5 offset0:6 offset1:7
Mvaa 52,220,216
s_nop 0
Ta 180,%0,36864
Ta 182,%0,38912
Mvaa 52,196,176
ds_read2st64_b32 v[112:113], v5 offset0:8 offset1:9
Mvaa 52,192,136
W_ 5
Ta 184,%0,40960
Ta 186,%0,43008
Mvaa 48,220,212
L_ v7,24,s55,0
Mvaa 48,196,172
S_ s55,s71,133120
Ta 188,%0,45056
Ta 190,%0,47104
Mvaa 48,192,132
S_ m0,s51,13312
ds_read2st64_b32 v[114:115], v5 offset0:10 offset1:11
A_ v96,v16,v96
Mvaa 44,220,208
ds_read2st64_b32 v[120:121], v5 offset0:12 offset1:13
A_ v97,v17,v97
Mvaa 44,196,168
L_ v7,24,s55,0
Mvaa 44,192,128
S_ s55,s71,143360
ds_read2st64_b32 v[122:123], v5 offset0:14 offset1:15
A_ v98,v18,v98
Mvaa 40,220,204
S_ m0,s51,14336
ds_read2st64_b32 v[128:129], v5 offset0:16 offset1:17
A_ v99,v19,v99
Mvaa 40,196,164
W_ 11
ds_read2st64_b32 v[130:131], v5 offset0:18 offset1:19
A_ v104,v20,v104
Mvaa 40,192,124
L_ v7,24,s55,0
Mvaa 36,220,200
S_ s55,s71,153600
ds_read2st64_b32 v[132:133], v5 offset0:36 offset1:37
A_ v105,v21,v105
Mvaa 36,196,160
S_ m0,s51,15360
ds_read2st64_b32 v[134:135], v5 offset0:38 offset1:39
A_ v106,v22,v106
Mvaa 36,192,120
ds_read2st64_b32 v[124:125], v5 offset0:32 offset1:33
A_ v107,v23,v107
s_waitcnt vmcnt(13)
Zaaa 100,70,20
L_ v7,24,s55,0
Zaaa 140,68,20
W_ 7
ds_read2st64_b32 v[126:127], v5 offset0:34 offset1:35
A_ v112,v24,v112
Zaaa 104,70,24
S_ s55,s71,163840
ds_read2st64_b32 v[116:117], v5 offset0:28 offset1:29
A_ v113,v25,v113
Zaaa 144,68,24
S_ m0,s51,16384
ds_read2st64_b32 v[118:119], v5 offset0:30 offset1:31
A_ v114,v26,v114
Zaaa 108,70,28
L_ v7,24,s55,0
Zaaa 148,68,28
S_ s55,s71,174080
ds_read2st64_b32 v[108:109], v5 offset0:24 offset1:25
A_ v115,v27,v115
Zaaa 112,70,32
W_ 9
ds_read2st64_b32 v[110:111], v5 offset0:26 offset1:27
A_ v120,v28,v120
Zaaa 152,68,32
S_ m0,s51,17408
ds_read2st64_b32 v[100:101], v5 offset0:20 offset1:21
A_ v121,v29,v121
Zaaa 116,70,36
L_ v7,24,s55,0
Zaaa 156,68,36
S_ s79,s6,6
ds_read2st64_b32 v[102:103], v5 offset0:22 offset1:23
A_ v122,v30,v122
Maaa 224,70,60
s_and_b32 s79, s79, 7
A_ v123,v31,v123
Maaa 228,68,60
W_ 10
A_ v128,v32,v128
A_ v129,v33,v129
Maaa 232,70,64
s_lshl_b32 s79, s79, 7
A_ v130,v34,v130
A_ v131,v35,v131
Maaa 236,68,64
W_ 9
A_ v132,v52,v132
W_ 8
Zvav 56,180,136
S_ s71,s72,s79
Ta 192,v1,36864
Ta 194,v1,38912
Mvav 56,184,156
S_ s55,s71,491520
A_ v133,v53,v133
A_ v134,v54,v134
Mvav 56,188,176
S_ m0,s51,49152
Ta 196,v1,40960
Ta 198,v1,43008
Zvav 60,180,140
L_ v7,24,s55,0
Mvav 60,184,160
S_ s55,s71,501760
Ta 220,v1,45056
Ta 222,v1,47104
Mvav 60,188,180
S_ m0,s51,50176
A_ v135,v55,v135
Zvav 64,180,144
W_ 12
A_ v124,v48,v124
A_ v125,v49,v125
Mvav 64,184,164
L_ v7,24,s55,0
Mvav 64,188,184
S_ s55,s71,512000
A_ v126,v50,v126
A_ v127,v51,v127
Zvav 68,180,148
W_ 10
A_ v116,v44,v116
A_ v117,v45,v117
Mvav 68,184,168
S_ m0,s51,51200
A_ v118,v46,v118
A_ v119,v47,v119
Mvav 68,188,188
L_ v7,24,s55,0
Zvav 72,180,152
W_ 8
A_ v108,v40,v108
A_ v109,v41,v109
Mvav 72,184,172
S_ s55,s71,522240
A_ v110,v42,v110
A_ v111,v43,v111
Mvav 72,188,192
W_ 0
A_ v100,v36,v100
Zvav 92,220,192
S_ m0,s51,52224
Ta 180,v2,36864
Ta 182,v2,38912
Mvav 92,196,172
L_ v7,24,s55,0
Mvav 92,192,152
S_ s55,s71,532480
Ta 184,v2,40960
Ta 186,v2,43008
Zvav 88,220,188
S_ m0,s51,53248
A_ v101,v37,v101
A_ v102,v38,v102
Mvav 88,196,168
s_nop 0
Ta 188,v2,45056
Ta 190,v2,47104
Mvav 88,192,148
L_ v7,24,s55,0
Zvav 84,220,184
S_ s55,s71,542720
A_ v103,v39,v103
C_ 131,130,131
Mvav 84,196,164
S_ m0,s51,54272
C_ 130,128,129
C_ 132,132,133
Mvav 84,192,144
S_ s85,s6,8
C_ 133,134,135
Zvav 80,220,180
buffer_store_dwordx4 v[130:133], v6, s[28:31], s73 offen offset:0 sc1
Mvav 80,196,160
s_sub_u32 s73, s73, 4096
s_and_b32 s85, s85, 7
Mvav 80,192,140
s_lshl_b32 s85, s85, 7
C_ 123,122,123
C_ 122,120,121
Zvav 76,220,176
S_ s85,s85,s87
C_ 124,124,125
C_ 125,126,127
Mvav 76,196,156
buffer_store_dwordx4 v[122:125], v6, s[28:31], s73 offen offset:3072 sc1
Mvav 76,192,136
W_ 0
C_ 115,114,115
C_ 114,112,113
Zvav 16,180,136
s_nop 0
Ta 192,v3,36864
Ta 194,v3,38912
Mvav 16,184,156
C_ 116,116,117
C_ 117,118,119
Mvav 16,188,176
s_nop 0
Ta 196,v3,40960
Ta 198,v3,43008
Zvav 20,180,140
buffer_store_dwordx4 v[114:117], v6, s[28:31], s73 offen offset:2048 sc1
Mvav 20,184,160
s_nop 0
Ta 220,v3,45056
Ta 222,v3,47104
Mvav 20,188,180
C_ 107,106,107
C_ 106,104,105
Zvav 24,180,144
C_ 108,108,109
C_ 109,110,111
Mvav 24,184,164
buffer_store_dwordx4 v[106:109], v6, s[28:31], s73 offen offset:1024 sc1
Mvav 24,188,184
C_ 99,98,99
C_ 98,96,97
Zvav 28,180,148
C_ 100,100,101
C_ 101,102,103
Mvav 28,184,168
Mvav 28,188,188
buffer_store_dwordx4 v[98:101], v6, s[28:31], s73 offen offset:0 sc1
Zvav 32,180,152
Mvav 32,184,172
Mvav 32,188,192
s_waitcnt vmcnt(25) lgkmcnt(0)
s_barrier
Zvav 52,220,192
s_nop 0
Ta 180,%0,24576
Ta 182,%0,26624
Mvav 52,196,172
L_ v7,24,s55,0
Mvav 52,192,152
S_ s55,s71,614400
Ta 184,%0,28672
Ta 186,%0,30720
Zvav 48,220,188
S_ m0,s51,36864
Mvav 48,196,168
s_nop 0
Ta 188,%0,32768
Ta 190,%0,34816
Mvav 48,192,148
L_ v7,24,s55,0
Zvav 44,220,184
S_ s55,s71,624640
Mvav 44,196,164
S_ m0,s51,37888
Mvav 44,192,144
Zvav 40,220,180
L_ v7,24,s55,0
Mvav 40,196,160
S_ s55,s71,634880
Mvav 40,192,140
S_ m0,s51,38912
Zvav 36,220,176
Mvav 36,196,156
L_ v7,24,s55,0
Mvav 36,192,136
W_ 0
S_ s55,s71,645120
Mvav 56,180,196
S_ m0,s51,39936
Ta 192,v1,24576
Ta 194,v1,26624
Mvav 56,184,216
Mvav 56,188,236
s_nop 0
Ta 196,v1,28672
Ta 198,v1,30720
Mvav 60,180,200
L_ v7,24,s55,0
Mvav 60,184,220
S_ s55,s71,655360
Ta 220,v1,32768
Ta 222,v1,34816
Mvav 60,188,240
S_ m0,s51,40960
Mvav 64,180,204
Mvav 64,184,224
L_ v7,24,s55,0
Mvav 64,188,244
S_ s55,s71,665600
Mvav 68,180,208
S_ m0,s51,41984
Mvav 68,184,228
Mvav 68,188,248
L_ v7,24,s55,0
Mvav 72,180,212
Mvav 72,184,232
Mvav 72,188,252
W_ 0
Mvav 92,220,252
s_nop 0
Ta 180,v2,24576
Ta 182,v2,26624
Mvav 92,196,232
buffer_load_dword a74, v4, s[24:27], s85 offen
Mvav 92,192,212
s_nop 0
Ta 184,v2,28672
Ta 186,v2,30720
Mvav 88,220,248
s_waitcnt vmcnt(24)
Mvav 88,196,228
s_nop 0
Ta 188,v2,32768
Ta 190,v2,34816
Mvav 88,192,208
v_cmp_eq_u32 vcc, s62, v11
Mvav 84,220,244
s_mul_i32 s76, s75, 204800
Mvav 84,196,224
s_bitcmp1_b32 vcc_lo, s75
s_cbranch_scc0 .Lspin1
.Lspun1:
Mvav 84,192,204
S_ s56,s76,s84
Mvav 80,220,240
buffer_load_dwordx4 a[20:23], v6, s[28:31], s56 offen offset:0 sc1
Mvav 80,196,220
Mvav 80,192,200
Mvav 76,220,236
Mvav 76,196,216
buffer_load_dwordx4 a[24:27], v6, s[28:31], s56 offen offset:1024 sc1
Mvav 76,192,196
W_ 0
Mvav 16,180,196
s_nop 0
Ta 192,v3,24576
Ta 194,v3,26624
Mvav 16,184,216
Mvav 16,188,236
s_nop 0
Ta 196,v3,28672
Ta 198,v3,30720
Mvav 20,180,200
buffer_load_dwordx4 a[28:31], v6, s[28:31], s56 offen offset:2048 sc1
Mvav 20,184,220
s_nop 0
Ta 220,v3,32768
Ta 222,v3,34816
Mvav 20,188,240
Mvav 24,180,204
Mvav 24,184,224
buffer_load_dwordx4 a[32:35], v6, s[28:31], s56 offen offset:3072 sc1
Mvav 24,188,244
S_ s56,s56,4096
Mvav 28,180,208
Mvav 28,184,228
Mvav 28,188,248
buffer_load_dwordx4 a[36:39], v6, s[28:31], s56 offen offset:0 sc1
Mvav 32,180,212
S_ s56,s76,s74
Mvav 32,184,232
Mvav 32,188,252
s_waitcnt vmcnt(32) lgkmcnt(0)
s_barrier
Mvav 52,220,252
S_ s55,s71,0
Ta 180,%0,0
Ta 182,%0,2048
Mvav 52,196,232
buffer_load_dwordx4 a[60:63], v6, s[28:31], s56 offen offset:0 sc1
Mvav 52,192,212
S_ s56,s76,s82
Ta 184,%0,4096
Ta 186,%0,6144
Mvav 48,220,248
S_ m0,s51,24576
Mvav 48,196,228
s_nop 0
Ta 188,%0,8192
Ta 190,%0,10240
Mvav 48,192,208
buffer_load_dwordx4 a[64:67], v6, s[28:31], s56 offen offset:0 sc1
Mvav 44,220,244
Mvav 44,196,224
Mvav 44,192,204
Mvav 40,220,240
L_ v7,24,s55,0
Mvav 40,196,220
S_ s55,s71,10240
Mvav 40,192,200
S_ m0,s51,25600
Mvav 36,220,236
Mvav 36,196,216
L_ v7,24,s55,0
Mvav 36,192,196
W_ 0
S_ s55,s71,20480
Mvaa 56,180,0
S_ m0,s51,26624
Ta 192,v1,0
Ta 194,v1,2048
Mvaa 56,184,40
Mvaa 56,188,80
s_nop 0
Ta 196,v1,4096
Ta 198,v1,6144
Mvaa 60,180,4
L_ v7,24,s55,0
Mvaa 60,184,44
S_ s55,s71,30720
Ta 220,v1,8192
Ta 222,v1,10240
Mvaa 60,188,84
S_ m0,s51,27648
Mvaa 64,180,8
Mvaa 64,184,48
L_ v7,24,s55,0
Mvaa 64,188,88
S_ s55,s71,40960
Mvaa 68,180,12
S_ m0,s51,28672
Mvaa 68,184,52
Mvaa 68,188,240
L_ v7,24,s55,0
Mvaa 72,180,16
S_ s55,s71,51200
Mvaa 72,184,56
S_ m0,s51,29696
Mvaa 72,188,244
W_ 0
Mvaa 92,220,244
s_nop 0
Ta 180,v2,0
Ta 182,v2,2048
Mvaa 92,196,56
L_ v7,24,s55,0
Mvaa 92,192,16
s_nop 0
Ta 184,v2,4096
Ta 186,v2,6144
Mvaa 88,220,240
Mvaa 88,196,52
s_nop 0
Ta 188,v2,8192
Ta 190,v2,10240
Mvaa 88,192,12
Mvaa 84,220,88
Mvaa 84,196,48
Mvaa 84,192,8
Mvaa 80,220,84
Mvaa 80,196,44
Mvaa 80,192,4
Mvaa 76,220,80
Mvaa 76,196,40
Mvaa 76,192,0
W_ 0
Mvaa 16,180,0
s_nop 0
Ta 192,v3,0
Ta 194,v3,2048
Mvaa 16,184,40
Mvaa 16,188,80
s_nop 0
Ta 196,v3,4096
Ta 198,v3,6144
Mvaa 20,180,4
Mvaa 20,184,44
s_nop 0
Ta 220,v3,8192
Ta 222,v3,10240
Mvaa 20,188,84
Mvaa 24,180,8
Mvaa 24,184,48
Mvaa 24,188,88
Mvaa 28,180,12
Mvaa 28,184,52
Mvaa 28,188,240
Mvaa 32,180,16
Mvaa 32,184,56
Mvaa 32,188,244
s_waitcnt vmcnt(31) lgkmcnt(0)
s_barrier
Mvaa 52,220,244
s_nop 0
Ta 180,%0,12288
Ta 182,%0,14336
Mvaa 52,196,56
Mvaa 52,192,16
s_nop 0
Ta 184,%0,16384
Ta 186,%0,18432
Mvaa 48,220,240
Mvaa 48,196,52
s_nop 0
Ta 188,%0,20480
Ta 190,%0,22528
Mvaa 48,192,12
Mvaa 44,220,88
Mvaa 44,196,48
Mvaa 44,192,8
Mvaa 40,220,84
Mvaa 40,196,44
Mvaa 40,192,4
Mvaa 36,220,80
Mvaa 36,196,40
Mvaa 36,192,0
W_ 0
Mvaa 56,180,120
S_ s81,s6,5
Ta 192,v1,12288
Ta 194,v1,14336
Mvaa 56,184,160
s_and_b32 s81, s81, 7
Mvaa 56,188,200
s_mul_i32 s81, s81, 20480
Ta 196,v1,16384
Ta 198,v1,18432
Mvaa 60,180,124
s_mov_b32 m0, s52
S_ s73,s81,s78
Mvaa 60,184,164
S_ s73,s73,4096
Ta 220,v1,20480
Ta 222,v1,22528
Mvaa 60,188,204
s_nop 0
Y_ 56,0
Y_ 57,256
Mvaa 64,180,128
s_nop 0
Y_ 58,512
Y_ 59,768
Mvaa 64,184,168
Mvaa 64,188,208
s_nop 0
Y_ 60,1024
Y_ 61,1280
Mvaa 68,180,132
s_nop 0
Y_ 62,1536
Y_ 63,1792
Mvaa 68,184,172
Mvaa 68,188,212
s_nop 0
Y_ 64,2048
Y_ 65,2304
Mvaa 72,180,136
s_nop 0
Y_ 66,2560
Y_ 67,2816
Mvaa 72,184,176
Mvaa 72,188,216
W_ 12
Y_ 68,3072
Y_ 69,3328
Mvaa 92,220,216
s_nop 0
Ta 180,v2,12288
Ta 182,v2,14336
Mvaa 92,196,176
s_nop 0
Y_ 70,3584
Y_ 71,3840
Mvaa 92,192,136
s_nop 0
Ta 184,v2,16384
Ta 186,v2,18432
Mvaa 88,220,212
s_nop 0
Y_ 72,4096
Y_ 73,4352
Mvaa 88,196,172
S_ s83,s6,4
Ta 188,v2,20480
Ta 190,v2,22528
Mvaa 88,192,132
s_and_b32 s83, s83, 7
Y_ 74,4608
Y_ 75,4864
Mvaa 84,220,208
s_lshl_b32 s83, s83, 2
Y_ 92,9216
Y_ 93,9472
Mvaa 84,196,168
s_nop 0
Y_ 94,9728
Y_ 95,9984
Mvaa 84,192,128
s_nop 0
Y_ 88,8192
Y_ 89,8448
Mvaa 80,220,204
s_nop 0
Y_ 90,8704
Y_ 91,8960
Mvaa 80,196,164
v_cmp_eq_u32_e64 s[36:37], s83, v8
Mvaa 80,192,124
s_nop 0
Y_ 84,7168
Y_ 85,7424
Mvaa 76,220,200
s_nop 0
Y_ 86,7680
Y_ 87,7936
Mvaa 76,196,160
v_mov_b32 v10, 0x7fff0000
Mvaa 76,192,120
W_ 14
Y_ 80,6144
Y_ 81,6400
Mvaa 16,180,120
s_nop 0
Ta 192,v3,12288
Ta 194,v3,14336
Mvaa 16,184,160
s_nop 0
Y_ 82,6656
Y_ 83,6912
Mvaa 16,188,200
s_nop 0
Ta 196,v3,16384
Ta 198,v3,18432
Mvaa 20,180,124
s_nop 0
Y_ 76,5120
Y_ 77,5376
Mvaa 20,184,164
s_nop 0
Ta 220,v3,20480
Ta 222,v3,22528
Mvaa 20,188,204
s_nop 0
Y_ 78,5632
Y_ 79,5888
Mvaa 24,180,128
v_cndmask_b32_e64 v10, v10, v9, s[36:37]
Mvaa 24,184,168
Mvaa 24,188,208
s_waitcnt vmcnt(20) lgkmcnt(0)
s_barrier
Mvaa 28,180,132
buffer_store_dword a255, v10, s[92:95], 0 offen offset:384 sc1
Mvaa 28,184,172
s_sub_u32 s75, s6, 3
ds_read2st64_b32 v[96:97], v5 offset0:0 offset1:1
S_ s55,s71,122880
Mvaa 28,188,212
s_and_b32 s75, s75, 7
ds_read2st64_b32 v[98:99], v5 offset0:2 offset1:3
S_ m0,s51,0
Mvaa 32,180,136
ds_read2st64_b32 v[104:105], v5 offset0:4 offset1:5
Mvaa 32,184,176
buffer_load_dword v11, v8, s[92:95], s96 offen offset:384 sc1
Mvaa 32,188,216
ds_read2st64_b32 v[106:107], v5 offset0:6 offset1:7
Mvaa 52,220,216
s_nop 0
Ta 180,%0,49152
Ta 182,%0,51200
Mvaa 52,196,176
ds_read2st64_b32 v[112:113], v5 offset0:8 offset1:9
Mvaa 52,192,136
W_ 5
Ta 184,%0,53248
Ta 186,%0,55296
Mvaa 48,220,212
L_ v7,24,s55,0
Mvaa 48,196,172
S_ s55,s71,133120
Ta 188,%0,57344
Ta 190,%0,59392
Mvaa 48,192,132
S_ m0,s51,1024
ds_read2st64_b32 v[114:115], v5 offset0:10 offset1:11
A_ v96,v16,v96
Mvaa 44,220,208
ds_read2st64_b32 v[120:121], v5 offset0:12 offset1:13
A_ v97,v17,v97
Mvaa 44,196,168
L_ v7,24,s55,0
Mvaa 44,192,128
S_ s55,s71,143360
ds_read2st64_b32 v[122:123], v5 offset0:14 offset1:15
A_ v98,v18,v98
Mvaa 40,220,204
S_ m0,s51,2048
ds_read2st64_b32 v[128:129], v5 offset0:16 offset1:17
A_ v99,v19,v99
Mvaa 40,196,164
W_ 11
ds_read2st64_b32 v[130:131], v5 offset0:18 offset1:19
A_ v104,v20,v104
Mvaa 40,192,124
L_ v7,24,s55,0
Mvaa 36,220,200
S_ s55,s71,153600
ds_read2st64_b32 v[132:133], v5 offset0:36 offset1:37
A_ v105,v21,v105
Mvaa 36,196,160
S_ m0,s51,3072
ds_read2st64_b32 v[134:135], v5 offset0:38 offset1:39
A_ v106,v22,v106
Mvaa 36,192,120
ds_read2st64_b32 v[124:125], v5 offset0:32 offset1:33
A_ v107,v23,v107
s_waitcnt vmcnt(11)
Maaa 100,70,20
L_ v7,24,s55,0
Maaa 140,68,20
W_ 7
ds_read2st64_b32 v[126:127], v5 offset0:34 offset1:35
A_ v112,v24,v112
Maaa 104,70,24
S_ s55,s71,163840
ds_read2st64_b32 v[116:117], v5 offset0:28 offset1:29
A_ v113,v25,v113
Maaa 144,68,24
S_ m0,s51,4096
ds_read2st64_b32 v[118:119], v5 offset0:30 offset1:31
A_ v114,v26,v114
Maaa 108,70,28
L_ v7,24,s55,0
Maaa 148,68,28
S_ s55,s71,174080
ds_read2st64_b32 v[108:109], v5 offset0:24 offset1:25
A_ v115,v27,v115
Maaa 112,70,32
)A"
R"A(W_ 9
ds_read2st64_b32 v[110:111], v5 offset0:26 offset1:27
A_ v120,v28,v120
Maaa 152,68,32
S_ m0,s51,5120
ds_read2st64_b32 v[100:101], v5 offset0:20 offset1:21
A_ v121,v29,v121
Maaa 116,70,36
L_ v7,24,s55,0
Maaa 156,68,36
S_ s79,s6,7
ds_read2st64_b32 v[102:103], v5 offset0:22 offset1:23
A_ v122,v30,v122
Maaa 224,70,60
s_and_b32 s79, s79, 7
A_ v123,v31,v123
Maaa 228,68,60
W_ 10
A_ v128,v32,v128
A_ v129,v33,v129
Maaa 232,70,64
s_lshl_b32 s79, s79, 7
A_ v130,v34,v130
A_ v131,v35,v131
Maaa 236,68,64
W_ 9
A_ v132,v52,v132
W_ 8
Zvav 56,180,136
S_ s71,s72,s79
Ta 192,v1,49152
Ta 194,v1,51200
Mvav 56,184,156
S_ s55,s71,491520
A_ v133,v53,v133
A_ v134,v54,v134
Mvav 56,188,176
S_ m0,s51,12288
Ta 196,v1,53248
Ta 198,v1,55296
Zvav 60,180,140
L_ v7,24,s55,0
Mvav 60,184,160
S_ s55,s71,501760
Ta 220,v1,57344
Ta 222,v1,59392
Mvav 60,188,180
S_ m0,s51,13312
A_ v135,v55,v135
Zvav 64,180,144
W_ 12
A_ v124,v48,v124
A_ v125,v49,v125
Mvav 64,184,164
L_ v7,24,s55,0
Mvav 64,188,184
S_ s55,s71,512000
A_ v126,v50,v126
A_ v127,v51,v127
Zvav 68,180,148
W_ 10
A_ v116,v44,v116
A_ v117,v45,v117
Mvav 68,184,168
S_ m0,s51,14336
A_ v118,v46,v118
A_ v119,v47,v119
Mvav 68,188,188
L_ v7,24,s55,0
Zvav 72,180,152
W_ 8
A_ v108,v40,v108
A_ v109,v41,v109
Mvav 72,184,172
S_ s55,s71,522240
A_ v110,v42,v110
A_ v111,v43,v111
Mvav 72,188,192
W_ 0
A_ v100,v36,v100
Zvav 92,220,192
S_ m0,s51,15360
Ta 180,v2,49152
Ta 182,v2,51200
Mvav 92,196,172
L_ v7,24,s55,0
Mvav 92,192,152
S_ s55,s71,532480
Ta 184,v2,53248
Ta 186,v2,55296
Zvav 88,220,188
S_ m0,s51,16384
A_ v101,v37,v101
A_ v102,v38,v102
Mvav 88,196,168
s_nop 0
Ta 188,v2,57344
Ta 190,v2,59392
Mvav 88,192,148
L_ v7,24,s55,0
Zvav 84,220,184
S_ s55,s71,542720
A_ v103,v39,v103
C_ 131,130,131
Mvav 84,196,164
S_ m0,s51,17408
C_ 130,128,129
C_ 132,132,133
Mvav 84,192,144
C_ 133,134,135
Zvav 80,220,180
buffer_store_dwordx4 v[130:133], v6, s[28:31], s73 offen offset:0 sc1
Mvav 80,196,160
s_sub_u32 s73, s73, 4096
Mvav 80,192,140
C_ 123,122,123
C_ 122,120,121
Zvav 76,220,176
C_ 124,124,125
C_ 125,126,127
Mvav 76,196,156
buffer_store_dwordx4 v[122:125], v6, s[28:31], s73 offen offset:3072 sc1
Mvav 76,192,136
W_ 0
C_ 115,114,115
C_ 114,112,113
Zvav 16,180,136
s_nop 0
Ta 192,v3,49152
Ta 194,v3,51200
Mvav 16,184,156
C_ 116,116,117
C_ 117,118,119
Mvav 16,188,176
s_nop 0
Ta 196,v3,53248
Ta 198,v3,55296
Zvav 20,180,140
buffer_store_dwordx4 v[114:117], v6, s[28:31], s73 offen offset:2048 sc1
Mvav 20,184,160
s_nop 0
Ta 220,v3,57344
Ta 222,v3,59392
Mvav 20,188,180
C_ 107,106,107
C_ 106,104,105
Zvav 24,180,144
C_ 108,108,109
C_ 109,110,111
Mvav 24,184,164
buffer_store_dwordx4 v[106:109], v6, s[28:31], s73 offen offset:1024 sc1
Mvav 24,188,184
C_ 99,98,99
C_ 98,96,97
Zvav 28,180,148
C_ 100,100,101
C_ 101,102,103
Mvav 28,184,168
Mvav 28,188,188
buffer_store_dwordx4 v[98:101], v6, s[28:31], s73 offen offset:0 sc1
Zvav 32,180,152
Mvav 32,184,172
Mvav 32,188,192
s_waitcnt vmcnt(32) lgkmcnt(0)
s_barrier
Zvav 52,220,192
s_nop 0
Ta 180,%0,36864
Ta 182,%0,38912
Mvav 52,196,172
L_ v7,24,s55,0
Mvav 52,192,152
S_ s55,s71,614400
Ta 184,%0,40960
Ta 186,%0,43008
Zvav 48,220,188
S_ m0,s51,49152
Mvav 48,196,168
s_nop 0
Ta 188,%0,45056
Ta 190,%0,47104
Mvav 48,192,148
L_ v7,24,s55,0
Zvav 44,220,184
S_ s55,s71,624640
Mvav 44,196,164
S_ m0,s51,50176
Mvav 44,192,144
Zvav 40,220,180
L_ v7,24,s55,0
Mvav 40,196,160
S_ s55,s71,634880
Mvav 40,192,140
S_ m0,s51,51200
Zvav 36,220,176
Mvav 36,196,156
L_ v7,24,s55,0
Mvav 36,192,136
W_ 0
S_ s55,s71,645120
Mvav 56,180,196
S_ m0,s51,52224
Ta 192,v1,36864
Ta 194,v1,38912
Mvav 56,184,216
Mvav 56,188,236
s_nop 0
Ta 196,v1,40960
Ta 198,v1,43008
Mvav 60,180,200
L_ v7,24,s55,0
Mvav 60,184,220
S_ s55,s71,655360
Ta 220,v1,45056
Ta 222,v1,47104
Mvav 60,188,240
S_ m0,s51,53248
Mvav 64,180,204
Mvav 64,184,224
L_ v7,24,s55,0
Mvav 64,188,244
S_ s55,s71,665600
Mvav 68,180,208
S_ m0,s51,54272
Mvav 68,184,228
Mvav 68,188,248
L_ v7,24,s55,0
Mvav 72,180,212
Mvav 72,184,232
Mvav 72,188,252
W_ 0
Mvav 92,220,252
s_nop 0
Ta 180,v2,36864
Ta 182,v2,38912
Mvav 92,196,232
Mvav 92,192,212
s_nop 0
Ta 184,v2,40960
Ta 186,v2,43008
Mvav 88,220,248
s_waitcnt vmcnt(23)
Mvav 88,196,228
s_nop 0
Ta 188,v2,45056
Ta 190,v2,47104
Mvav 88,192,208
v_cmp_eq_u32 vcc, s62, v11
Mvav 84,220,244
s_mul_i32 s76, s75, 204800
Mvav 84,196,224
s_bitcmp1_b32 vcc_lo, s75
s_cbranch_scc0 .Lspin2
.Lspun2:
Mvav 84,192,204
S_ s56,s76,s84
Mvav 80,220,240
buffer_load_dwordx4 a[20:23], v6, s[28:31], s56 offen offset:0 sc1
Mvav 80,196,220
Mvav 80,192,200
Mvav 76,220,236
Mvav 76,196,216
buffer_load_dwordx4 a[24:27], v6, s[28:31], s56 offen offset:1024 sc1
Mvav 76,192,196
W_ 0
Mvav 16,180,196
s_nop 0
Ta 192,v3,36864
Ta 194,v3,38912
Mvav 16,184,216
Mvav 16,188,236
s_nop 0
Ta 196,v3,40960
Ta 198,v3,43008
Mvav 20,180,200
buffer_load_dwordx4 a[28:31], v6, s[28:31], s56 offen offset:2048 sc1
Mvav 20,184,220
s_nop 0
Ta 220,v3,45056
Ta 222,v3,47104
Mvav 20,188,240
Mvav 24,180,204
Mvav 24,184,224
buffer_load_dwordx4 a[32:35], v6, s[28:31], s56 offen offset:3072 sc1
Mvav 24,188,244
S_ s56,s56,4096
Mvav 28,180,208
Mvav 28,184,228
Mvav 28,188,248
buffer_load_dwordx4 a[36:39], v6, s[28:31], s56 offen offset:0 sc1
Mvav 32,180,212
S_ s56,s76,s74
Mvav 32,184,232
Mvav 32,188,252
s_waitcnt vmcnt(30) lgkmcnt(0)
s_barrier
Mvav 52,220,252
S_ s55,s71,0
Ta 180,%0,24576
Ta 182,%0,26624
Mvav 52,196,232
buffer_load_dwordx4 a[60:63], v6, s[28:31], s56 offen offset:0 sc1
Mvav 52,192,212
S_ s56,s76,s82
Ta 184,%0,28672
Ta 186,%0,30720
Mvav 48,220,248
S_ m0,s51,36864
Mvav 48,196,228
s_nop 0
Ta 188,%0,32768
Ta 190,%0,34816
Mvav 48,192,208
buffer_load_dwordx4 a[64:67], v6, s[28:31], s56 offen offset:0 sc1
Mvav 44,220,244
Mvav 44,196,224
Mvav 44,192,204
Mvav 40,220,240
L_ v7,24,s55,0
Mvav 40,196,220
S_ s55,s71,10240
Mvav 40,192,200
S_ m0,s51,37888
Mvav 36,220,236
Mvav 36,196,216
L_ v7,24,s55,0
Mvav 36,192,196
W_ 0
S_ s55,s71,20480
Mvaa 56,180,0
S_ m0,s51,38912
Ta 192,v1,24576
Ta 194,v1,26624
Mvaa 56,184,40
Mvaa 56,188,80
s_nop 0
Ta 196,v1,28672
Ta 198,v1,30720
Mvaa 60,180,4
L_ v7,24,s55,0
Mvaa 60,184,44
S_ s55,s71,30720
Ta 220,v1,32768
Ta 222,v1,34816
Mvaa 60,188,84
S_ m0,s51,39936
Mvaa 64,180,8
Mvaa 64,184,48
L_ v7,24,s55,0
Mvaa 64,188,88
S_ s55,s71,40960
Mvaa 68,180,12
S_ m0,s51,40960
Mvaa 68,184,52
Mvaa 68,188,240
L_ v7,24,s55,0
Mvaa 72,180,16
S_ s55,s71,51200
Mvaa 72,184,56
S_ m0,s51,41984
Mvaa 72,188,244
W_ 0
Mvaa 92,220,244
s_nop 0
Ta 180,v2,24576
Ta 182,v2,26624
Mvaa 92,196,56
L_ v7,24,s55,0
Mvaa 92,192,16
s_nop 0
Ta 184,v2,28672
Ta 186,v2,30720
Mvaa 88,220,240
Mvaa 88,196,52
s_nop 0
Ta 188,v2,32768
Ta 190,v2,34816
Mvaa 88,192,12
Mvaa 84,220,88
Mvaa 84,196,48
Mvaa 84,192,8
Mvaa 80,220,84
Mvaa 80,196,44
Mvaa 80,192,4
Mvaa 76,220,80
Mvaa 76,196,40
Mvaa 76,192,0
W_ 0
Mvaa 16,180,0
s_nop 0
Ta 192,v3,24576
Ta 194,v3,26624
Mvaa 16,184,40
Mvaa 16,188,80
s_nop 0
Ta 196,v3,28672
Ta 198,v3,30720
Mvaa 20,180,4
Mvaa 20,184,44
s_nop 0
Ta 220,v3,32768
Ta 222,v3,34816
Mvaa 20,188,84
Mvaa 24,180,8
Mvaa 24,184,48
Mvaa 24,188,88
Mvaa 28,180,12
Mvaa 28,184,52
Mvaa 28,188,240
Mvaa 32,180,16
Mvaa 32,184,56
Mvaa 32,188,244
s_waitcnt vmcnt(30) lgkmcnt(0)
s_barrier
Mvaa 52,220,244
s_nop 0
Ta 180,%0,0
Ta 182,%0,2048
Mvaa 52,196,56
Mvaa 52,192,16
s_nop 0
Ta 184,%0,4096
Ta 186,%0,6144
Mvaa 48,220,240
Mvaa 48,196,52
s_nop 0
Ta 188,%0,8192
Ta 190,%0,10240
Mvaa 48,192,12
Mvaa 44,220,88
Mvaa 44,196,48
Mvaa 44,192,8
Mvaa 40,220,84
Mvaa 40,196,44
Mvaa 40,192,4
Mvaa 36,220,80
Mvaa 36,196,40
Mvaa 36,192,0
W_ 0
Mvaa 56,180,120
S_ s81,s6,6
Ta 192,v1,0
Ta 194,v1,2048
Mvaa 56,184,160
s_and_b32 s81, s81, 7
Mvaa 56,188,200
s_mul_i32 s81, s81, 20480
Ta 196,v1,4096
Ta 198,v1,6144
Mvaa 60,180,124
s_mov_b32 m0, s52
S_ s73,s81,s78
Mvaa 60,184,164
S_ s73,s73,4096
Ta 220,v1,8192
Ta 222,v1,10240
Mvaa 60,188,204
s_nop 0
Y_ 56,0
Y_ 57,256
Mvaa 64,180,128
s_nop 0
Y_ 58,512
Y_ 59,768
Mvaa 64,184,168
Mvaa 64,188,208
s_nop 0
Y_ 60,1024
Y_ 61,1280
Mvaa 68,180,132
s_nop 0
Y_ 62,1536
Y_ 63,1792
Mvaa 68,184,172
Mvaa 68,188,212
s_nop 0
Y_ 64,2048
Y_ 65,2304
Mvaa 72,180,136
s_nop 0
Y_ 66,2560
Y_ 67,2816
Mvaa 72,184,176
Mvaa 72,188,216
W_ 12
Y_ 68,3072
Y_ 69,3328
Mvaa 92,220,216
s_nop 0
Ta 180,v2,0
Ta 182,v2,2048
Mvaa 92,196,176
s_nop 0
Y_ 70,3584
Y_ 71,3840
Mvaa 92,192,136
s_nop 0
Ta 184,v2,4096
Ta 186,v2,6144
Mvaa 88,220,212
s_nop 0
Y_ 72,4096
Y_ 73,4352
Mvaa 88,196,172
S_ s83,s6,5
Ta 188,v2,8192
Ta 190,v2,10240
Mvaa 88,192,132
s_and_b32 s83, s83, 7
Y_ 74,4608
Y_ 75,4864
Mvaa 84,220,208
s_lshl_b32 s83, s83, 2
Y_ 92,9216
Y_ 93,9472
Mvaa 84,196,168
s_nop 0
Y_ 94,9728
Y_ 95,9984
Mvaa 84,192,128
s_nop 0
Y_ 88,8192
Y_ 89,8448
Mvaa 80,220,204
s_nop 0
Y_ 90,8704
Y_ 91,8960
Mvaa 80,196,164
v_cmp_eq_u32_e64 s[36:37], s83, v8
Mvaa 80,192,124
s_nop 0
Y_ 84,7168
Y_ 85,7424
Mvaa 76,220,200
s_nop 0
Y_ 86,7680
Y_ 87,7936
Mvaa 76,196,160
v_mov_b32 v10, 0x7fff0000
Mvaa 76,192,120
W_ 14
Y_ 80,6144
Y_ 81,6400
Mvaa 16,180,120
s_nop 0
Ta 192,v3,0
Ta 194,v3,2048
Mvaa 16,184,160
s_nop 0
Y_ 82,6656
Y_ 83,6912
Mvaa 16,188,200
s_nop 0
Ta 196,v3,4096
Ta 198,v3,6144
Mvaa 20,180,124
s_nop 0
Y_ 76,5120
Y_ 77,5376
Mvaa 20,184,164
s_nop 0
Ta 220,v3,8192
Ta 222,v3,10240
Mvaa 20,188,204
s_nop 0
Y_ 78,5632
Y_ 79,5888
Mvaa 24,180,128
v_cndmask_b32_e64 v10, v10, v9, s[36:37]
Mvaa 24,184,168
Mvaa 24,188,208
s_waitcnt vmcnt(19) lgkmcnt(0)
s_barrier
Mvaa 28,180,132
buffer_store_dword a255, v10, s[92:95], 0 offen offset:384 sc1
Mvaa 28,184,172
s_sub_u32 s75, s6, 4
ds_read2st64_b32 v[96:97], v5 offset0:0 offset1:1
S_ s55,s71,122880
Mvaa 28,188,212
s_and_b32 s75, s75, 7
ds_read2st64_b32 v[98:99], v5 offset0:2 offset1:3
S_ m0,s51,24576
Mvaa 32,180,136
ds_read2st64_b32 v[104:105], v5 offset0:4 offset1:5
Mvaa 32,184,176
buffer_load_dword v11, v8, s[92:95], s96 offen offset:384 sc1
Mvaa 32,188,216
ds_read2st64_b32 v[106:107], v5 offset0:6 offset1:7
Mvaa 52,220,216
s_nop 0
Ta 180,%0,12288
Ta 182,%0,14336
Mvaa 52,196,176
ds_read2st64_b32 v[112:113], v5 offset0:8 offset1:9
Mvaa 52,192,136
W_ 5
Ta 184,%0,16384
Ta 186,%0,18432
Mvaa 48,220,212
L_ v7,24,s55,0
Mvaa 48,196,172
S_ s55,s71,133120
Ta 188,%0,20480
Ta 190,%0,22528
Mvaa 48,192,132
S_ m0,s51,25600
ds_read2st64_b32 v[114:115], v5 offset0:10 offset1:11
A_ v96,v16,v96
Mvaa 44,220,208
ds_read2st64_b32 v[120:121], v5 offset0:12 offset1:13
A_ v97,v17,v97
Mvaa 44,196,168
L_ v7,24,s55,0
Mvaa 44,192,128
S_ s55,s71,143360
ds_read2st64_b32 v[122:123], v5 offset0:14 offset1:15
A_ v98,v18,v98
Mvaa 40,220,204
S_ m0,s51,26624
ds_read2st64_b32 v[128:129], v5 offset0:16 offset1:17
A_ v99,v19,v99
Mvaa 40,196,164
W_ 11
ds_read2st64_b32 v[130:131], v5 offset0:18 offset1:19
A_ v104,v20,v104
Mvaa 40,192,124
L_ v7,24,s55,0
Mvaa 36,220,200
S_ s55,s71,153600
ds_read2st64_b32 v[132:133], v5 offset0:36 offset1:37
A_ v105,v21,v105
Mvaa 36,196,160
S_ m0,s51,27648
ds_read2st64_b32 v[134:135], v5 offset0:38 offset1:39
A_ v106,v22,v106
Mvaa 36,192,120
ds_read2st64_b32 v[124:125], v5 offset0:32 offset1:33
A_ v107,v23,v107
s_waitcnt vmcnt(11)
Maaa 100,70,20
L_ v7,24,s55,0
Maaa 140,68,20
W_ 7
ds_read2st64_b32 v[126:127], v5 offset0:34 offset1:35
A_ v112,v24,v112
Maaa 104,70,24
S_ s55,s71,163840
ds_read2st64_b32 v[116:117], v5 offset0:28 offset1:29
A_ v113,v25,v113
Maaa 144,68,24
S_ m0,s51,28672
ds_read2st64_b32 v[118:119], v5 offset0:30 offset1:31
A_ v114,v26,v114
Maaa 108,70,28
L_ v7,24,s55,0
Maaa 148,68,28
S_ s55,s71,174080
ds_read2st64_b32 v[108:109], v5 offset0:24 offset1:25
A_ v115,v27,v115
Maaa 112,70,32
W_ 9
ds_read2st64_b32 v[110:111], v5 offset0:26 offset1:27
A_ v120,v28,v120
Maaa 152,68,32
S_ m0,s51,29696
ds_read2st64_b32 v[100:101], v5 offset0:20 offset1:21
A_ v121,v29,v121
Maaa 116,70,36
L_ v7,24,s55,0
Maaa 156,68,36
S_ s79,s6,8
ds_read2st64_b32 v[102:103], v5 offset0:22 offset1:23
A_ v122,v30,v122
Maaa 224,70,60
s_and_b32 s79, s79, 7
A_ v123,v31,v123
Maaa 228,68,60
W_ 10
A_ v128,v32,v128
A_ v129,v33,v129
Maaa 232,70,64
s_lshl_b32 s79, s79, 7
A_ v130,v34,v130
A_ v131,v35,v131
Maaa 236,68,64
W_ 9
A_ v132,v52,v132
W_ 8
Zvav 56,180,136
S_ s71,s72,s79
Ta 192,v1,12288
Ta 194,v1,14336
Mvav 56,184,156
S_ s55,s71,491520
A_ v133,v53,v133
A_ v134,v54,v134
Mvav 56,188,176
S_ m0,s51,0
Ta 196,v1,16384
Ta 198,v1,18432
Zvav 60,180,140
L_ v7,24,s55,0
Mvav 60,184,160
S_ s55,s71,501760
Ta 220,v1,20480
Ta 222,v1,22528
Mvav 60,188,180
S_ m0,s51,1024
A_ v135,v55,v135
Zvav 64,180,144
W_ 12
A_ v124,v48,v124
A_ v125,v49,v125
Mvav 64,184,164
L_ v7,24,s55,0
Mvav 64,188,184
S_ s55,s71,512000
A_ v126,v50,v126
A_ v127,v51,v127
Zvav 68,180,148
W_ 10
A_ v116,v44,v116
A_ v117,v45,v117
Mvav 68,184,168
S_ m0,s51,2048
A_ v118,v46,v118
A_ v119,v47,v119
Mvav 68,188,188
L_ v7,24,s55,0
Zvav 72,180,152
W_ 8
A_ v108,v40,v108
A_ v109,v41,v109
Mvav 72,184,172
S_ s55,s71,522240
A_ v110,v42,v110
A_ v111,v43,v111
Mvav 72,188,192
W_ 0
A_ v100,v36,v100
Zvav 92,220,192
S_ m0,s51,3072
Ta 180,v2,12288
Ta 182,v2,14336
Mvav 92,196,172
L_ v7,24,s55,0
Mvav 92,192,152
S_ s55,s71,532480
Ta 184,v2,16384
Ta 186,v2,18432
Zvav 88,220,188
S_ m0,s51,4096
A_ v101,v37,v101
A_ v102,v38,v102
Mvav 88,196,168
s_nop 0
Ta 188,v2,20480
Ta 190,v2,22528
Mvav 88,192,148
L_ v7,24,s55,0
Zvav 84,220,184
S_ s55,s71,542720
A_ v103,v39,v103
C_ 131,130,131
Mvav 84,196,164
S_ m0,s51,5120
C_ 130,128,129
C_ 132,132,133
Mvav 84,192,144
C_ 133,134,135
Zvav 80,220,180
buffer_store_dwordx4 v[130:133], v6, s[28:31], s73 offen offset:0 sc1
Mvav 80,196,160
s_sub_u32 s73, s73, 4096
Mvav 80,192,140
C_ 123,122,123
C_ 122,120,121
Zvav 76,220,176
C_ 124,124,125
C_ 125,126,127
Mvav 76,196,156
buffer_store_dwordx4 v[122:125], v6, s[28:31], s73 offen offset:3072 sc1
Mvav 76,192,136
W_ 0
C_ 115,114,115
C_ 114,112,113
Zvav 16,180,136
s_nop 0
Ta 192,v3,12288
Ta 194,v3,14336
Mvav 16,184,156
C_ 116,116,117
C_ 117,118,119
Mvav 16,188,176
s_nop 0
Ta 196,v3,16384
Ta 198,v3,18432
Zvav 20,180,140
buffer_store_dwordx4 v[114:117], v6, s[28:31], s73 offen offset:2048 sc1
Mvav 20,184,160
s_nop 0
Ta 220,v3,20480
Ta 222,v3,22528
Mvav 20,188,180
C_ 107,106,107
C_ 106,104,105
Zvav 24,180,144
C_ 108,108,109
C_ 109,110,111
Mvav 24,184,164
buffer_store_dwordx4 v[106:109], v6, s[28:31], s73 offen offset:1024 sc1
Mvav 24,188,184
C_ 99,98,99
C_ 98,96,97
Zvav 28,180,148
C_ 100,100,101
C_ 101,102,103
Mvav 28,184,168
Mvav 28,188,188
buffer_store_dwordx4 v[98:101], v6, s[28:31], s73 offen offset:0 sc1
Zvav 32,180,152
Mvav 32,184,172
Mvav 32,188,192
s_waitcnt vmcnt(31) lgkmcnt(0)
s_barrier
Zvav 52,220,192
s_nop 0
Ta 180,%0,49152
Ta 182,%0,51200
Mvav 52,196,172
L_ v7,24,s55,0
Mvav 52,192,152
S_ s55,s71,614400
Ta 184,%0,53248
Ta 186,%0,55296
Zvav 48,220,188
S_ m0,s51,12288
Mvav 48,196,168
s_nop 0
Ta 188,%0,57344
Ta 190,%0,59392
Mvav 48,192,148
L_ v7,24,s55,0
Zvav 44,220,184
S_ s55,s71,624640
Mvav 44,196,164
S_ m0,s51,13312
Mvav 44,192,144
Zvav 40,220,180
L_ v7,24,s55,0
Mvav 40,196,160
S_ s55,s71,634880
Mvav 40,192,140
S_ m0,s51,14336
Zvav 36,220,176
Mvav 36,196,156
L_ v7,24,s55,0
Mvav 36,192,136
W_ 0
S_ s55,s71,645120
Mvav 56,180,196
S_ m0,s51,15360
Ta 192,v1,49152
Ta 194,v1,51200
Mvav 56,184,216
Mvav 56,188,236
s_nop 0
Ta 196,v1,53248
Ta 198,v1,55296
Mvav 60,180,200
L_ v7,24,s55,0
Mvav 60,184,220
S_ s55,s71,655360
Ta 220,v1,57344
Ta 222,v1,59392
Mvav 60,188,240
S_ m0,s51,16384
Mvav 64,180,204
Mvav 64,184,224
L_ v7,24,s55,0
Mvav 64,188,244
S_ s55,s71,665600
Mvav 68,180,208
S_ m0,s51,17408
Mvav 68,184,228
Mvav 68,188,248
L_ v7,24,s55,0
Mvav 72,180,212
Mvav 72,184,232
Mvav 72,188,252
W_ 0
Mvav 92,220,252
s_nop 0
Ta 180,v2,49152
Ta 182,v2,51200
Mvav 92,196,232
Mvav 92,192,212
s_nop 0
Ta 184,v2,53248
Ta 186,v2,55296
Mvav 88,220,248
s_waitcnt vmcnt(23)
Mvav 88,196,228
s_nop 0
Ta 188,v2,57344
Ta 190,v2,59392
Mvav 88,192,208
v_cmp_eq_u32 vcc, s62, v11
Mvav 84,220,244
s_mul_i32 s76, s75, 204800
Mvav 84,196,224
s_bitcmp1_b32 vcc_lo, s75
s_cbranch_scc0 .Lspin3
.Lspun3:
Mvav 84,192,204
S_ s56,s76,s84
Mvav 80,220,240
buffer_load_dwordx4 a[20:23], v6, s[28:31], s56 offen offset:0 sc1
Mvav 80,196,220
Mvav 80,192,200
Mvav 76,220,236
Mvav 76,196,216
buffer_load_dwordx4 a[24:27], v6, s[28:31], s56 offen offset:1024 sc1
Mvav 76,192,196
W_ 0
Mvav 16,180,196
s_nop 0
Ta 192,v3,49152
Ta 194,v3,51200
Mvav 16,184,216
Mvav 16,188,236
s_nop 0
Ta 196,v3,53248
Ta 198,v3,55296
Mvav 20,180,200
buffer_load_dwordx4 a[28:31], v6, s[28:31], s56 offen offset:2048 sc1
Mvav 20,184,220
s_nop 0
Ta 220,v3,57344
Ta 222,v3,59392
Mvav 20,188,240
Mvav 24,180,204
Mvav 24,184,224
buffer_load_dwordx4 a[32:35], v6, s[28:31], s56 offen offset:3072 sc1
Mvav 24,188,244
S_ s56,s56,4096
Mvav 28,180,208
Mvav 28,184,228
Mvav 28,188,248
buffer_load_dwordx4 a[36:39], v6, s[28:31], s56 offen offset:0 sc1
Mvav 32,180,212
S_ s56,s76,s74
Mvav 32,184,232
Mvav 32,188,252
s_waitcnt vmcnt(30) lgkmcnt(0)
s_barrier
Mvav 52,220,252
S_ s55,s71,0
Ta 180,%0,36864
Ta 182,%0,38912
Mvav 52,196,232
buffer_load_dwordx4 a[60:63], v6, s[28:31], s56 offen offset:0 sc1
Mvav 52,192,212
S_ s56,s76,s82
Ta 184,%0,40960
Ta 186,%0,43008
Mvav 48,220,248
S_ m0,s51,49152
Mvav 48,196,228
s_nop 0
Ta 188,%0,45056
Ta 190,%0,47104
Mvav 48,192,208
buffer_load_dwordx4 a[64:67], v6, s[28:31], s56 offen offset:0 sc1
Mvav 44,220,244
s_sub_u32 s75, s6, 5
Mvav 44,196,224
s_and_b32 s75, s75, 7
Mvav 44,192,204
Mvav 40,220,240
buffer_load_dword v11, v8, s[92:95], s96 offen offset:384 sc1
Mvav 40,196,220
Mvav 40,192,200
Mvav 36,220,236
Mvav 36,196,216
L_ v7,24,s55,0
Mvav 36,192,196
W_ 0
S_ s55,s71,10240
Mvaa 56,180,0
S_ m0,s51,50176
Ta 192,v1,36864
Ta 194,v1,38912
Mvaa 56,184,40
Mvaa 56,188,80
s_nop 0
Ta 196,v1,40960
Ta 198,v1,43008
Mvaa 60,180,4
L_ v7,24,s55,0
Mvaa 60,184,44
S_ s55,s71,20480
Ta 220,v1,45056
Ta 222,v1,47104
Mvaa 60,188,84
S_ m0,s51,51200
Mvaa 64,180,8
Mvaa 64,184,48
L_ v7,24,s55,0
Mvaa 64,188,88
S_ s55,s71,30720
Mvaa 68,180,12
S_ m0,s51,52224
Mvaa 68,184,52
Mvaa 68,188,240
L_ v7,24,s55,0
Mvaa 72,180,16
S_ s55,s71,40960
Mvaa 72,184,56
S_ m0,s51,53248
Mvaa 72,188,244
W_ 0
Mvaa 92,220,244
s_nop 0
Ta 180,v2,36864
Ta 182,v2,38912
Mvaa 92,196,56
L_ v7,24,s55,0
Mvaa 92,192,16
S_ s55,s71,51200
Ta 184,v2,40960
Ta 186,v2,43008
Mvaa 88,220,240
S_ m0,s51,54272
Mvaa 88,196,52
s_nop 0
Ta 188,v2,45056
Ta 190,v2,47104
Mvaa 88,192,12
L_ v7,24,s55,0
Mvaa 84,220,88
Mvaa 84,196,48
Mvaa 84,192,8
Mvaa 80,220,84
Mvaa 80,196,44
S_ s83,s6,6
Mvaa 80,192,4
s_and_b32 s83, s83, 7
Mvaa 76,220,80
s_lshl_b32 s83, s83, 2
Mvaa 76,196,40
v_cmp_eq_u32_e64 s[36:37], s83, v8
Mvaa 76,192,0
W_ 0
v_mov_b32 v10, 0x7fff0000
Mvaa 16,180,0
s_nop 0
Ta 192,v3,36864
Ta 194,v3,38912
Mvaa 16,184,40
v_cndmask_b32_e64 v10, v10, v9, s[36:37]
Mvaa 16,188,80
s_nop 0
Ta 196,v3,40960
Ta 198,v3,43008
Mvaa 20,180,4
Mvaa 20,184,44
s_nop 0
Ta 220,v3,45056
Ta 222,v3,47104
Mvaa 20,188,84
Mvaa 24,180,8
Mvaa 24,184,48
Mvaa 24,188,88
Mvaa 28,180,12
Mvaa 28,184,52
Mvaa 28,188,240
Mvaa 32,180,16
Mvaa 32,184,56
Mvaa 32,188,244
s_waitcnt vmcnt(21) lgkmcnt(0)
s_barrier
Mvaa 52,220,244
s_nop 0
Ta 180,%0,24576
Ta 182,%0,26624
Mvaa 52,196,56
buffer_store_dword a255, v10, s[92:95], 0 offen offset:384 sc1
Mvaa 52,192,16
s_nop 0
Ta 184,%0,28672
Ta 186,%0,30720
Mvaa 48,220,240
Mvaa 48,196,52
s_nop 0
Ta 188,%0,32768
Ta 190,%0,34816
Mvaa 48,192,12
Mvaa 44,220,88
Mvaa 44,196,48
Mvaa 44,192,8
Mvaa 40,220,84
Mvaa 40,196,44
Mvaa 40,192,4
Mvaa 36,220,80
Mvaa 36,196,40
Mvaa 36,192,0
W_ 0
Mvaa 56,180,120
S_ s81,s6,7
Ta 192,v1,24576
Ta 194,v1,26624
Mvaa 56,184,160
s_and_b32 s81, s81, 7
Mvaa 56,188,200
s_mul_i32 s81, s81, 20480
Ta 196,v1,28672
Ta 198,v1,30720
Mvaa 60,180,124
s_mov_b32 m0, s52
S_ s73,s81,s78
Mvaa 60,184,164
S_ s73,s73,4096
Ta 220,v1,32768
Ta 222,v1,34816
Mvaa 60,188,204
s_nop 0
Y_ 56,0
Y_ 57,256
Mvaa 64,180,128
s_nop 0
Y_ 58,512
Y_ 59,768
Mvaa 64,184,168
Mvaa 64,188,208
s_nop 0
Y_ 60,1024
Y_ 61,1280
Mvaa 68,180,132
s_nop 0
Y_ 62,1536
Y_ 63,1792
Mvaa 68,184,172
Mvaa 68,188,212
s_nop 0
Y_ 64,2048
Y_ 65,2304
Mvaa 72,180,136
s_nop 0
Y_ 66,2560
Y_ 67,2816
Mvaa 72,184,176
Mvaa 72,188,216
W_ 12
Y_ 68,3072
Y_ 69,3328
Mvaa 92,220,216
s_nop 0
Ta 180,v2,24576
Ta 182,v2,26624
Mvaa 92,196,176
s_nop 0
Y_ 70,3584
Y_ 71,3840
Mvaa 92,192,136
s_nop 0
Ta 184,v2,28672
Ta 186,v2,30720
Mvaa 88,220,212
s_nop 0
Y_ 72,4096
Y_ 73,4352
Mvaa 88,196,172
s_nop 0
Ta 188,v2,32768
Ta 190,v2,34816
Mvaa 88,192,132
s_nop 0
Y_ 74,4608
Y_ 75,4864
Mvaa 84,220,208
s_nop 0
Y_ 92,9216
Y_ 93,9472
Mvaa 84,196,168
s_nop 0
Y_ 94,9728
Y_ 95,9984
Mvaa 84,192,128
s_nop 0
Y_ 88,8192
Y_ 89,8448
Mvaa 80,220,204
s_nop 0
Y_ 90,8704
Y_ 91,8960
Mvaa 80,196,164
Mvaa 80,192,124
s_nop 0
Y_ 84,7168
Y_ 85,7424
Mvaa 76,220,200
s_nop 0
Y_ 86,7680
Y_ 87,7936
Mvaa 76,196,160
Mvaa 76,192,120
W_ 14
Y_ 80,6144
Y_ 81,6400
Mvaa 16,180,120
s_nop 0
Ta 192,v3,24576
Ta 194,v3,26624
Mvaa 16,184,160
s_nop 0
Y_ 82,6656
Y_ 83,6912
Mvaa 16,188,200
s_nop 0
Ta 196,v3,28672
Ta 198,v3,30720
Mvaa 20,180,124
s_nop 0
Y_ 76,5120
Y_ 77,5376
Mvaa 20,184,164
s_nop 0
Ta 220,v3,32768
Ta 222,v3,34816
Mvaa 20,188,204
s_nop 0
Y_ 78,5632
Y_ 79,5888
Mvaa 24,180,128
Mvaa 24,184,168
Mvaa 24,188,208
s_waitcnt vmcnt(21) lgkmcnt(0)
s_barrier
Mvaa 28,180,132
S_ s55,s71,122880
ds_read2st64_b32 v[96:97], v5 offset0:0 offset1:1
Mvaa 28,184,172
S_ m0,s51,36864
ds_read2st64_b32 v[98:99], v5 offset0:2 offset1:3
Mvaa 28,188,212
L_ v7,24,s55,0
Mvaa 32,180,136
S_ s55,s71,133120
ds_read2st64_b32 v[104:105], v5 offset0:4 offset1:5
Mvaa 32,184,176
s_waitcnt vmcnt(8)
ds_read2st64_b32 v[106:107], v5 offset0:6 offset1:7
S_ m0,s51,37888
Mvaa 32,188,216
ds_read2st64_b32 v[112:113], v5 offset0:8 offset1:9
v_cmp_eq_u32 vcc, s62, v11
Mvaa 52,220,216
s_mul_i32 s76, s75, 204800
Ta 180,%0,0
Ta 182,%0,2048
Mvaa 52,196,176
L_ v7,24,s55,0
Mvaa 52,192,136
W_ 5
Ta 184,%0,4096
Ta 186,%0,6144
Mvaa 48,220,212
s_bitcmp1_b32 vcc_lo, s75
s_cbranch_scc0 .Lspin4
.Lspun4:
ds_read2st64_b32 v[114:115], v5 offset0:10 offset1:11
Mvaa 48,196,172
S_ s55,s71,143360
Ta 188,%0,8192
Ta 190,%0,10240
Mvaa 48,192,132
S_ m0,s51,38912
ds_read2st64_b32 v[120:121], v5 offset0:12 offset1:13
A_ v96,v16,v96
Mvaa 44,220,208
L_ v7,24,s55,0
Mvaa 44,196,168
S_ s55,s71,153600
ds_read2st64_b32 v[122:123], v5 offset0:14 offset1:15
A_ v97,v17,v97
Mvaa 44,192,128
S_ m0,s51,39936
ds_read2st64_b32 v[128:129], v5 offset0:16 offset1:17
A_ v98,v18,v98
Mvaa 40,220,204
ds_read2st64_b32 v[130:131], v5 offset0:18 offset1:19
A_ v99,v19,v99
Mvaa 40,196,164
L_ v7,24,s55,0
Mvaa 40,192,124
W_ 12
ds_read2st64_b32 v[132:133], v5 offset0:36 offset1:37
A_ v104,v20,v104
Mvaa 36,220,200
S_ s55,s71,163840
ds_read2st64_b32 v[134:135], v5 offset0:38 offset1:39
A_ v105,v21,v105
Mvaa 36,196,160
S_ m0,s51,40960
ds_read2st64_b32 v[124:125], v5 offset0:32 offset1:33
A_ v106,v22,v106
Mvaa 36,192,120
L_ v7,24,s55,0
s_waitcnt vmcnt(13)
Maaa 100,70,20
S_ s55,s71,174080
ds_read2st64_b32 v[126:127], v5 offset0:34 offset1:35
A_ v107,v23,v107
Maaa 140,68,20
W_ 10
ds_read2st64_b32 v[116:117], v5 offset0:28 offset1:29
A_ v112,v24,v112
Maaa 104,70,24
S_ m0,s51,41984
ds_read2st64_b32 v[118:119], v5 offset0:30 offset1:31
A_ v113,v25,v113
Maaa 144,68,24
L_ v7,24,s55,0
Maaa 108,70,28
ds_read2st64_b32 v[108:109], v5 offset0:24 offset1:25
A_ v114,v26,v114
Maaa 148,68,28
ds_read2st64_b32 v[110:111], v5 offset0:26 offset1:27
A_ v115,v27,v115
Maaa 112,70,32
W_ 10
ds_read2st64_b32 v[100:101], v5 offset0:20 offset1:21
A_ v120,v28,v120
Maaa 152,68,32
ds_read2st64_b32 v[102:103], v5 offset0:22 offset1:23
A_ v121,v29,v121
Maaa 116,70,36
A_ v122,v30,v122
A_ v123,v31,v123
Maaa 156,68,36
W_ 10
A_ v128,v32,v128
A_ v129,v33,v129
Maaa 224,70,60
A_ v130,v34,v130
A_ v131,v35,v131
Maaa 228,68,60
W_ 8
A_ v132,v52,v132
A_ v133,v53,v133
Maaa 232,70,64
A_ v134,v54,v134
A_ v135,v55,v135
Maaa 236,68,64
W_ 7
A_ v124,v48,v124
W_ 6
Zvav 56,180,136
s_nop 0
Ta 192,v1,0
Ta 194,v1,2048
Mvav 56,184,156
A_ v125,v49,v125
A_ v126,v50,v126
Mvav 56,188,176
S_ s56,s76,s84
Ta 196,v1,4096
Ta 198,v1,6144
Zvav 60,180,140
buffer_load_dwordx4 a[20:23], v6, s[28:31], s56 offen offset:0 sc1
Mvav 60,184,160
s_nop 0
Ta 220,v1,8192
Ta 222,v1,10240
Mvav 60,188,180
A_ v127,v51,v127
C_ 131,130,131
Zvav 64,180,144
W_ 10
A_ v116,v44,v116
A_ v117,v45,v117
Mvav 64,184,164
buffer_load_dwordx4 a[24:27], v6, s[28:31], s56 offen offset:1024 sc1
Mvav 64,188,184
A_ v118,v46,v118
A_ v119,v47,v119
Zvav 68,180,148
W_ 8
A_ v108,v40,v108
A_ v109,v41,v109
Mvav 68,184,168
A_ v110,v42,v110
A_ v111,v43,v111
Mvav 68,188,188
buffer_load_dwordx4 a[28:31], v6, s[28:31], s56 offen offset:2048 sc1
Zvav 72,180,152
W_ 6
A_ v100,v36,v100
A_ v101,v37,v101
Mvav 72,184,172
A_ v102,v38,v102
A_ v103,v39,v103
Mvav 72,188,192
W_ 0
C_ 130,128,129
C_ 132,132,133
Zvav 92,220,192
s_nop 0
Ta 180,v2,0
Ta 182,v2,2048
Mvav 92,196,172
buffer_load_dwordx4 a[32:35], v6, s[28:31], s56 offen offset:3072 sc1
Mvav 92,192,152
S_ s56,s56,4096
Ta 184,v2,4096
Ta 186,v2,6144
Zvav 88,220,188
C_ 133,134,135
Mvav 88,196,168
s_nop 0
Ta 188,v2,8192
Ta 190,v2,10240
Mvav 88,192,148
buffer_load_dwordx4 a[36:39], v6, s[28:31], s56 offen offset:0 sc1
Zvav 84,220,184
S_ s56,s76,s74
Mvav 84,196,164
Mvav 84,192,144
Zvav 80,220,180
buffer_load_dwordx4 a[60:63], v6, s[28:31], s56 offen offset:0 sc1
Mvav 80,196,160
S_ s56,s76,s82
Mvav 80,192,140
Zvav 76,220,176
Mvav 76,196,156
buffer_load_dwordx4 a[64:67], v6, s[28:31], s56 offen offset:0 sc1
Mvav 76,192,136
W_ 0
s_sub_u32 s75, s6, 6
Maav 100,180,136
s_and_b32 s75, s75, 7
Ta 192,v3,0
Ta 194,v3,2048
Maav 100,184,156
Maav 100,188,176
s_nop 0
Ta 196,v3,4096
Ta 198,v3,6144
Maav 104,180,140
buffer_load_dword v11, v8, s[92:95], s96 offen offset:384 sc1
Maav 104,184,160
s_nop 0
Ta 220,v3,8192
Ta 222,v3,10240
Maav 104,188,180
Maav 108,180,144
Maav 108,184,164
buffer_store_dwordx4 v[130:133], v6, s[28:31], s73 offen offset:0 sc1
Maav 108,188,184
s_sub_u32 s73, s73, 4096
Maav 112,180,148
C_ 123,122,123
C_ 122,120,121
Maav 112,184,168
C_ 124,124,125
C_ 125,126,127
Maav 112,188,188
buffer_store_dwordx4 v[122:125], v6, s[28:31], s73 offen offset:3072 sc1
Maav 116,180,152
C_ 115,114,115
C_ 114,112,113
Maav 116,184,172
C_ 116,116,117
C_ 117,118,119
Maav 116,188,192
s_waitcnt vmcnt(31) lgkmcnt(0)
s_barrier
Maav 156,220,192
s_nop 0
Ta 180,%0,12288
Ta 182,%0,14336
Maav 156,196,172
buffer_store_dwordx4 v[114:117], v6, s[28:31], s73 offen offset:2048 sc1
Maav 156,192,152
s_nop 0
Ta 184,%0,16384
Ta 186,%0,18432
Maav 152,220,188
C_ 107,106,107
C_ 106,104,105
Maav 152,196,168
s_nop 0
Ta 188,%0,20480
Ta 190,%0,22528
Maav 152,192,148
C_ 108,108,109
C_ 109,110,111
Maav 148,220,184
buffer_store_dwordx4 v[106:109], v6, s[28:31], s73 offen offset:1024 sc1
Maav 148,196,164
C_ 99,98,99
C_ 98,96,97
Maav 148,192,144
C_ 100,100,101
C_ 101,102,103
Maav 144,220,180
Maav 144,196,160
buffer_store_dwordx4 v[98:101], v6, s[28:31], s73 offen offset:0 sc1
Maav 144,192,140
Maav 140,220,176
Maav 140,196,156
Maav 140,192,136
W_ 0
Mvav 56,180,196
s_nop 0
Ta 192,v1,12288
Ta 194,v1,14336
Mvav 56,184,216
Mvav 56,188,236
s_nop 0
Ta 196,v1,16384
Ta 198,v1,18432
Mvav 60,180,200
Mvav 60,184,220
s_nop 0
Ta 220,v1,20480
Ta 222,v1,22528
Mvav 60,188,240
v_bfe_u32 v4, v6, 4, 4
Mvav 64,180,204
v_mul_u32_u24 v4, 2560, v4
Mvav 64,184,224
v_bfe_u32 v10, v6, 8, 1
Mvav 64,188,244
v_lshl_add_u32 v4, v10, 5, v4
Mvav 68,180,208
v_lshrrev_b32 v10, 9, v6
Mvav 68,184,228
v_lshl_add_u32 v4, v10, 4, v4
Mvav 68,188,248
Mvav 72,180,212
Mvav 72,184,232
Mvav 72,188,252
W_ 0
Mvav 92,220,252
s_nop 0
Ta 180,v2,12288
Ta 182,v2,14336
Mvav 92,196,232
Mvav 92,192,212
s_nop 0
Ta 184,v2,16384
Ta 186,v2,18432
Mvav 88,220,248
Mvav 88,196,228
s_nop 0
Ta 188,v2,20480
Ta 190,v2,22528
Mvav 88,192,208
Mvav 84,220,244
Mvav 84,196,224
Mvav 84,192,204
Mvav 80,220,240
Mvav 80,196,220
Mvav 80,192,200
Mvav 76,220,236
Mvav 76,196,216
Mvav 76,192,196
W_ 0
Maav 100,180,196
s_nop 0
Ta 192,v3,12288
Ta 194,v3,14336
Maav 100,184,216
Maav 100,188,236
s_nop 0
Ta 196,v3,16384
Ta 198,v3,18432
Maav 104,180,200
Maav 104,184,220
s_nop 0
Ta 220,v3,20480
Ta 222,v3,22528
Maav 104,188,240
Maav 108,180,204
S_ s86,s6,1
Maav 108,184,224
s_and_b32 s86, s86, 7
Maav 108,188,244
s_waitcnt vmcnt(5)
Maav 112,180,208
v_cmp_eq_u32 vcc, s62, v11
Maav 112,184,228
s_mul_i32 s76, s75, 204800
Maav 112,188,248
s_bitcmp1_b32 vcc_lo, s75
s_cbranch_scc0 .Lspin5
.Lspun5:
Maav 116,180,212
s_bitcmp1_b32 vcc_lo, s86
s_cbranch_scc0 .Lspin6
.Lspun6:
Maav 116,184,232
S_ s56,s76,s84
Maav 116,188,252
s_waitcnt vmcnt(20) lgkmcnt(0)
s_barrier
Maav 156,220,252
s_nop 0
Ta 180,%0,49152
Ta 182,%0,51200
Maav 156,196,232
buffer_load_dwordx4 v[16:19], v6, s[28:31], s56 offen offset:0 sc1
Maav 156,192,212
s_nop 0
Ta 184,%0,53248
Ta 186,%0,55296
Maav 152,220,248
Maav 152,196,228
s_nop 0
Ta 188,%0,57344
Ta 190,%0,59392
Maav 152,192,208
buffer_load_dwordx4 v[20:23], v6, s[28:31], s56 offen offset:1024 sc1
Maav 148,220,244
Maav 148,196,224
Maav 148,192,204
Maav 144,220,240
buffer_load_dwordx4 v[24:27], v6, s[28:31], s56 offen offset:2048 sc1
Maav 144,196,220
Maav 144,192,200
Maav 140,220,236
Maav 140,196,216
buffer_load_dwordx4 v[28:31], v6, s[28:31], s56 offen offset:3072 sc1
Maav 140,192,196
W_ 0
S_ s56,s56,4096
Mvaa 56,180,0
s_nop 0
Ta 192,v1,49152
Ta 194,v1,51200
Mvaa 56,184,40
R_ v164,a224
R_ v165,a225
Mvaa 56,188,80
s_nop 0
Ta 196,v1,53248
Ta 198,v1,55296
Mvaa 60,180,4
buffer_load_dwordx4 v[32:35], v6, s[28:31], s56 offen offset:0 sc1
Mvaa 60,184,44
S_ s56,s76,s74
Ta 220,v1,57344
Ta 222,v1,59392
Mvaa 60,188,84
R_ v166,a226
R_ v167,a227
Mvaa 64,180,8
R_ v168,a228
R_ v169,a229
Mvaa 64,184,48
buffer_load_dwordx4 v[36:39], v6, s[28:31], s56 offen offset:0 sc1
Mvaa 64,188,88
S_ s56,s76,s82
R_ v170,a230
R_ v171,a231
Mvaa 68,180,12
R_ v172,a232
R_ v173,a233
Mvaa 68,184,52
R_ v174,a234
R_ v175,a235
Mvaa 68,188,240
buffer_load_dwordx4 v[40:43], v6, s[28:31], s56 offen offset:0 sc1
Mvaa 72,180,16
s_mul_i32 s76, s86, 204800
R_ v176,a236
R_ v177,a237
Mvaa 72,184,56
S_ s80,s76,s84
R_ v178,a238
R_ v179,a239
Mvaa 72,188,244
W_ 0
S_ s56,s76,s74
Mvaa 92,220,244
s_nop 0
Ta 180,v2,49152
Ta 182,v2,51200
Mvaa 92,196,56
buffer_load_dwordx4 v[156:159], v6, s[28:31], s56 offen offset:0 sc1
Mvaa 92,192,16
S_ s56,s76,s82
Ta 184,v2,53248
Ta 186,v2,55296
Mvaa 88,220,240
Mvaa 88,196,52
s_nop 0
Ta 188,v2,57344
Ta 190,v2,59392
Mvaa 88,192,12
buffer_load_dwordx4 v[160:163], v6, s[28:31], s56 offen offset:0 sc1
Mvaa 84,220,88
Mvaa 84,196,48
Mvaa 84,192,8
Mvaa 80,220,84
Mvaa 80,196,44
Mvaa 80,192,4
Mvaa 76,220,80
Mvaa 76,196,40
Mvaa 76,192,0
W_ 0
Maaa 100,180,0
s_nop 0
Ta 192,v3,49152
Ta 194,v3,51200
Maaa 100,184,40
Maaa 100,188,80
s_nop 0
Ta 196,v3,53248
Ta 198,v3,55296
Maaa 104,180,4
Maaa 104,184,44
s_nop 0
Ta 220,v3,57344
Ta 222,v3,59392
Maaa 104,188,84
Maaa 108,180,8
R_ v180,a100
R_ v181,a101
Maaa 108,184,48
R_ v182,a102
R_ v183,a103
Maaa 108,188,88
Maaa 112,180,12
R_ v184,a104
R_ v185,a105
Maaa 112,184,52
R_ v186,a106
R_ v187,a107
Maaa 112,188,240
Maaa 116,180,16
R_ v188,a108
R_ v189,a109
Maaa 116,184,56
R_ v190,a110
R_ v191,a111
Maaa 116,188,244
s_waitcnt vmcnt(22) lgkmcnt(0)
s_barrier
Maaa 156,220,244
s_nop 0
Ta 180,%0,36864
Ta 182,%0,38912
Maaa 156,196,56
R_ v192,a112
R_ v193,a113
Maaa 156,192,16
s_nop 0
Ta 184,%0,40960
Ta 186,%0,43008
Maaa 152,220,240
R_ v194,a114
R_ v195,a115
Maaa 152,196,52
s_nop 0
Ta 188,%0,45056
Ta 190,%0,47104
Maaa 152,192,12
R_ v196,a116
R_ v197,a117
Maaa 148,220,88
R_ v198,a118
R_ v199,a119
Maaa 148,196,48
R_ v216,a156
R_ v217,a157
Maaa 148,192,8
R_ v218,a158
R_ v219,a159
Maaa 144,220,84
R_ v212,a152
R_ v213,a153
Maaa 144,196,44
R_ v214,a154
R_ v215,a155
Maaa 144,192,4
Maaa 140,220,80
R_ v208,a148
R_ v209,a149
Maaa 140,196,40
R_ v210,a150
R_ v211,a151
Maaa 140,192,0
s_waitcnt vmcnt(15)
Mvaa 164,70,60
R_ v204,a144
R_ v205,a145
Mvaa 168,68,60
R_ v206,a146
R_ v207,a147
Mvaa 172,70,64
Mvaa 176,68,64
R_ v200,a140
R_ v201,a141
Mvaa 180,70,20
R_ v202,a142
R_ v203,a143
Mvaa 184,70,24
Mvaa 188,70,28
Mvaa 192,70,32
Mvaa 196,70,36
Mvaa 216,68,36
Mvaa 212,68,32
Mvaa 208,68,28
Mvaa 204,68,24
Mvaa 200,68,20
W_ 0
Mvaa 56,180,120
S_ s56,s76,s84
Ta 192,v1,36864
Ta 194,v1,38912
Mvaa 56,184,160
buffer_load_dwordx4 v[136:139], v6, s[28:31], s56 offen offset:0 sc1
Mvaa 56,188,200
s_nop 0
Ta 196,v1,40960
Ta 198,v1,43008
Mvaa 60,180,124
s_mov_b32 m0, s52
Mvaa 60,184,164
s_nop 0
Ta 220,v1,45056
Ta 222,v1,47104
Mvaa 60,188,204
s_nop 0
Y_ 56,0
Y_ 57,256
Mvaa 64,180,128
s_nop 0
Y_ 58,512
Y_ 59,768
Mvaa 64,184,168
buffer_load_dwordx4 v[140:143], v6, s[28:31], s56 offen offset:1024 sc1
Mvaa 64,188,208
s_nop 0
Y_ 60,1024
Y_ 61,1280
Mvaa 68,180,132
s_nop 0
Y_ 62,1536
Y_ 63,1792
Mvaa 68,184,172
Mvaa 68,188,212
s_nop 0
Y_ 64,2048
Y_ 65,2304
Mvaa 72,180,136
s_nop 0
Y_ 66,2560
Y_ 67,2816
Mvaa 72,184,176
buffer_load_dwordx4 v[144:147], v6, s[28:31], s56 offen offset:2048 sc1
Mvaa 72,188,216
W_ 12
Y_ 68,3072
Y_ 69,3328
Mvaa 92,220,216
s_nop 0
Ta 180,v2,36864
Ta 182,v2,38912
Mvaa 92,196,176
s_nop 0
Y_ 70,3584
Y_ 71,3840
Mvaa 92,192,136
s_nop 0
Ta 184,v2,40960
Ta 186,v2,43008
Mvaa 88,220,212
s_nop 0
Y_ 72,4096
Y_ 73,4352
Mvaa 88,196,172
s_nop 0
Ta 188,v2,45056
Ta 190,v2,47104
Mvaa 88,192,132
s_nop 0
Y_ 74,4608
Y_ 75,4864
Mvaa 84,220,208
s_nop 0
Y_ 92,9216
Y_ 93,9472
Mvaa 84,196,168
s_nop 0
Y_ 94,9728
Y_ 95,9984
Mvaa 84,192,128
s_nop 0
Y_ 88,8192
Y_ 89,8448
Mvaa 80,220,204
s_nop 0
Y_ 90,8704
Y_ 91,8960
Mvaa 80,196,164
buffer_load_dwordx4 v[148:151], v6, s[28:31], s56 offen offset:3072 sc1
Mvaa 80,192,124
S_ s56,s56,4096
Y_ 84,7168
Y_ 85,7424
Mvaa 76,220,200
s_nop 0
Y_ 86,7680
Y_ 87,7936
Mvaa 76,196,160
Mvaa 76,192,120
W_ 14
Y_ 80,6144
Y_ 81,6400
Mvaa 180,180,120
s_nop 0
Ta 192,v3,36864
Ta 194,v3,38912
Mvaa 180,184,160
s_nop 0
Y_ 82,6656
Y_ 83,6912
Mvaa 180,188,200
s_nop 0
Ta 196,v3,40960
Ta 198,v3,43008
Mvaa 184,180,124
s_nop 0
Y_ 76,5120
Y_ 77,5376
Mvaa 184,184,164
s_nop 0
Ta 220,v3,45056
Ta 222,v3,47104
Mvaa 184,188,204
s_nop 0
Y_ 78,5632
Y_ 79,5888
Mvaa 188,180,128
buffer_load_dwordx4 v[152:155], v6, s[28:31], s56 offen offset:0 sc1
Mvaa 188,184,168
Mvaa 188,188,208
s_waitcnt vmcnt(63) lgkmcnt(0)
s_barrier
Mvaa 192,180,132
ds_read2st64_b32 v[96:97], v5 offset0:0 offset1:1
Mvaa 192,184,172
ds_read2st64_b32 v[98:99], v5 offset0:2 offset1:3
Mvaa 192,188,212
ds_read2st64_b32 v[104:105], v5 offset0:4 offset1:5
Mvaa 196,180,136
ds_read2st64_b32 v[106:107], v5 offset0:6 offset1:7
Mvaa 196,184,176
ds_read2st64_b32 v[112:113], v5 offset0:8 offset1:9
Mvaa 196,188,216
ds_read2st64_b32 v[114:115], v5 offset0:10 offset1:11
Mvaa 216,220,216
ds_read2st64_b32 v[120:121], v5 offset0:12 offset1:13
Mvaa 216,196,176
ds_read2st64_b32 v[122:123], v5 offset0:14 offset1:15
W_ 7
A_ v96,v96,v180
Mvaa 216,192,136
ds_read2st64_b32 v[128:129], v5 offset0:16 offset1:17
A_ v97,v97,v181
Mvaa 212,220,212
ds_read2st64_b32 v[130:131], v5 offset0:18 offset1:19
W_ 8
A_ v98,v98,v182
Mvaa 212,196,172
ds_read2st64_b32 v[132:133], v5 offset0:36 offset1:37
A_ v99,v99,v183
Mvaa 212,192,132
ds_read2st64_b32 v[134:135], v5 offset0:38 offset1:39
W_ 9
A_ v104,v104,v184
Mvaa 208,220,208
ds_read2st64_b32 v[124:125], v5 offset0:32 offset1:33
A_ v105,v105,v185
Mvaa 208,196,168
ds_read2st64_b32 v[126:127], v5 offset0:34 offset1:35
W_ 10
A_ v106,v106,v186
Mvaa 208,192,128
ds_read2st64_b32 v[116:117], v5 offset0:28 offset1:29
A_ v107,v107,v187
Mvaa 204,220,204
ds_read2st64_b32 v[118:119], v5 offset0:30 offset1:31
W_ 11
A_ v112,v112,v188
)A"
R"A(Mvaa 204,196,164
ds_read2st64_b32 v[108:109], v5 offset0:24 offset1:25
A_ v113,v113,v189
Mvaa 204,192,124
ds_read2st64_b32 v[110:111], v5 offset0:26 offset1:27
W_ 12
A_ v114,v114,v190
Mvaa 200,220,200
ds_read2st64_b32 v[100:101], v5 offset0:20 offset1:21
A_ v115,v115,v191
Mvaa 200,196,160
ds_read2st64_b32 v[102:103], v5 offset0:22 offset1:23
W_ 13
A_ v120,v120,v192
Mvaa 200,192,120
A_ v121,v121,v193
W_ 12
A_ v122,v122,v194
A_ v123,v123,v195
W_ 11
A_ v128,v128,v196
A_ v129,v129,v197
W_ 10
A_ v130,v130,v198
A_ v131,v131,v199
s_waitcnt vmcnt(5)
Mvav 164,70,36
W_ 9
A_ v132,v132,v216
A_ v133,v133,v217
Mvav 168,68,36
W_ 8
A_ v134,v134,v218
A_ v135,v135,v219
Mvav 172,70,40
W_ 7
A_ v124,v124,v212
A_ v125,v125,v213
Mvav 176,68,40
W_ 6
A_ v126,v126,v214
A_ v127,v127,v215
Mvav 164,70,156
W_ 5
A_ v116,v116,v208
A_ v117,v117,v209
Mvav 168,68,156
W_ 4
A_ v118,v118,v210
A_ v119,v119,v211
Mvav 172,70,160
W_ 3
A_ v108,v108,v204
A_ v109,v109,v205
Mvav 176,68,160
W_ 2
A_ v110,v110,v206
A_ v111,v111,v207
Mvav 96,70,16
W_ 1
A_ v100,v100,v200
A_ v101,v101,v201
Mvav 104,70,20
W_ 0
A_ v102,v102,v202
A_ v103,v103,v203
Mvav 112,70,24
C_ 220,164,165
C_ 221,166,167
Mvav 120,70,28
C_ 222,168,169
C_ 223,170,171
Mvav 128,70,32
v_permlane16_swap_b32 v220, v222
v_permlane16_swap_b32 v221, v223
Mvav 132,68,32
C_ 228,172,173
C_ 229,174,175
Mvav 124,68,28
C_ 230,176,177
C_ 231,178,179
Mvav 116,68,24
v_permlane16_swap_b32 v228, v230
v_permlane16_swap_b32 v229, v231
Mvav 108,68,20
Mvav 100,68,16
s_waitcnt vmcnt(0)
.Lsel6:
Nvav 180,70,136,96
Nvav 188,70,140,104
Nvav 196,70,144,112
Nvav 204,70,148,120
Nvav 212,70,152,128
Nvav 184,68,136,100
s_nop 3
A_ v10,v180,v188
Nvav 192,68,140,108
A_ v11,v196,v204
Nvav 200,68,144,116
A_ v10,v10,v212
v_cmp_u_f32 vcc, v10, v11
Nvav 208,68,148,124
s_cbranch_vccnz .Lretry
.Lgood:
Nvav 216,68,152,132
buffer_store_dwordx4 v[220:223], v4, s[32:35], s9 offen sc1
buffer_store_dwordx4 v[228:231], v4, s[32:35], s10 offen sc1
C_ 180,180,181
C_ 181,182,183
C_ 182,184,185
C_ 183,186,187
s_nop 0
v_permlane16_swap_b32 v180, v182
v_permlane16_swap_b32 v181, v183
buffer_store_dwordx4 v[180:183], v4, s[32:35], s8 offen sc1
C_ 188,188,189
C_ 189,190,191
C_ 190,192,193
C_ 191,194,195
S_ s8,s8,40960
v_permlane16_swap_b32 v188, v190
v_permlane16_swap_b32 v189, v191
buffer_store_dwordx4 v[188:191], v4, s[32:35], s8 offen sc1
C_ 196,196,197
C_ 197,198,199
C_ 198,200,201
C_ 199,202,203
S_ s8,s8,40960
v_permlane16_swap_b32 v196, v198
v_permlane16_swap_b32 v197, v199
buffer_store_dwordx4 v[196:199], v4, s[32:35], s8 offen sc1
C_ 204,204,205
C_ 205,206,207
C_ 206,208,209
C_ 207,210,211
S_ s8,s8,40960
v_permlane16_swap_b32 v204, v206
v_permlane16_swap_b32 v205, v207
buffer_store_dwordx4 v[204:207], v4, s[32:35], s8 offen sc1
C_ 212,212,213
C_ 213,214,215
C_ 214,216,217
C_ 215,218,219
S_ s8,s8,40960
v_permlane16_swap_b32 v212, v214
v_permlane16_swap_b32 v213, v215
buffer_store_dwordx4 v[212:215], v4, s[32:35], s8 offen sc1
s_endpgm
.Lspin0:
s_cmp_eq_u32 s57, 0
s_cbranch_scc1 .Lspun0
s_sub_u32 s57, s57, 1
s_sleep 4
buffer_load_dword v11, v8, s[92:95], s96 offen offset:384 sc1
s_waitcnt vmcnt(0)
v_cmp_eq_u32 vcc, s62, v11
s_nop 1
s_bitcmp1_b32 vcc_lo, s75
s_cbranch_scc0 .Lspin0
s_branch .Lspun0
.Lspin1:
s_cmp_eq_u32 s57, 0
s_cbranch_scc1 .Lspun1
s_sub_u32 s57, s57, 1
s_sleep 4
buffer_load_dword v11, v8, s[92:95], s96 offen offset:384 sc1
s_waitcnt vmcnt(0)
v_cmp_eq_u32 vcc, s62, v11
s_nop 1
s_bitcmp1_b32 vcc_lo, s75
s_cbranch_scc0 .Lspin1
s_branch .Lspun1
.Lspin2:
s_cmp_eq_u32 s57, 0
s_cbranch_scc1 .Lspun2
s_sub_u32 s57, s57, 1
s_sleep 4
buffer_load_dword v11, v8, s[92:95], s96 offen offset:384 sc1
s_waitcnt vmcnt(0)
v_cmp_eq_u32 vcc, s62, v11
s_nop 1
s_bitcmp1_b32 vcc_lo, s75
s_cbranch_scc0 .Lspin2
s_branch .Lspun2
.Lspin3:
s_cmp_eq_u32 s57, 0
s_cbranch_scc1 .Lspun3
s_sub_u32 s57, s57, 1
s_sleep 4
buffer_load_dword v11, v8, s[92:95], s96 offen offset:384 sc1
s_waitcnt vmcnt(0)
v_cmp_eq_u32 vcc, s62, v11
s_nop 1
s_bitcmp1_b32 vcc_lo, s75
s_cbranch_scc0 .Lspin3
s_branch .Lspun3
.Lspin4:
s_cmp_eq_u32 s57, 0
s_cbranch_scc1 .Lspun4
s_sub_u32 s57, s57, 1
s_sleep 4
buffer_load_dword v11, v8, s[92:95], s96 offen offset:384 sc1
s_waitcnt vmcnt(0)
v_cmp_eq_u32 vcc, s62, v11
s_nop 1
s_bitcmp1_b32 vcc_lo, s75
s_cbranch_scc0 .Lspin4
s_branch .Lspun4
.Lspin5:
s_cmp_eq_u32 s57, 0
s_cbranch_scc1 .Lspun5
s_sub_u32 s57, s57, 1
s_sleep 4
buffer_load_dword v11, v8, s[92:95], s96 offen offset:384 sc1
s_waitcnt vmcnt(0)
v_cmp_eq_u32 vcc, s62, v11
s_nop 1
s_bitcmp1_b32 vcc_lo, s75
s_cbranch_scc0 .Lspin5
s_branch .Lspun5
.Lspin6:
s_cmp_eq_u32 s57, 0
s_cbranch_scc1 .Lspun6
s_sub_u32 s57, s57, 1
s_sleep 4
buffer_load_dword v11, v8, s[92:95], s96 offen offset:384 sc1
s_waitcnt vmcnt(0)
v_cmp_eq_u32 vcc, s62, v11
s_nop 1
s_bitcmp1_b32 vcc_lo, s86
s_cbranch_scc0 .Lspin6
s_branch .Lspun6
.Lretry:
s_cmp_eq_u32 s57, 0
s_cbranch_scc1 .Lgood
s_sub_u32 s57, s57, 1
v_max_u32 v10, v136, v137
v_max3_u32 v10, v10, v138, v139
v_max3_u32 v10, v10, v140, v141
v_max3_u32 v10, v10, v142, v143
v_max3_u32 v10, v10, v144, v145
v_max3_u32 v10, v10, v146, v147
v_max3_u32 v10, v10, v148, v149
v_max3_u32 v10, v10, v150, v151
v_max3_u32 v10, v10, v152, v153
v_max3_u32 v10, v10, v154, v155
v_cmp_eq_u32 vcc, -1, v10
s_nop 4
s_cbranch_vccz .Lgood
s_nop 7
s_nop 7
S_ s53,s80,4096
buffer_load_dwordx4 v[136:139], v6, s[28:31], s80 offen offset:0 sc1
buffer_load_dwordx4 v[140:143], v6, s[28:31], s80 offen offset:1024 sc1
buffer_load_dwordx4 v[144:147], v6, s[28:31], s80 offen offset:2048 sc1
buffer_load_dwordx4 v[148:151], v6, s[28:31], s80 offen offset:3072 sc1
buffer_load_dwordx4 v[152:155], v6, s[28:31], s53 offen offset:0 sc1
s_waitcnt vmcnt(0)
s_branch .Lsel6
.Ldone:
s_waitcnt vmcnt(0)
.purgem S_
.purgem W_
.purgem L_
.purgem X_
.purgem R_
.purgem Ta
.purgem Da
.purgem Zvaa
.purgem Mvaa
.purgem U_
.purgem C_
.purgem Ln
.purgem Dv
.purgem Zvvv
.purgem Mvvv
.purgem B_
.purgem E_
.purgem Nvvv
.purgem Tv
.purgem Zaav
.purgem Zavv
.purgem A_
.purgem Mavv
.purgem Zvav
.purgem Mvav
.purgem Y_
.purgem Zaaa
.purgem Maaa
.purgem Maav
.purgem Nvav
)A"
    : "+v"(tid)
    : "s"(__builtin_amdgcn_kernarg_segment_ptr()), "s"(blockIdx.x)
    : "v1", "v2", "v3", "v4", "v5", "v6", "v7", "v8", "v9", "v10", "v11", "v12", "v13", "v14", "v15", "v16", "v17", "v18", "v19", "v20", "v21", "v22", "v23", "v24", "v25", "v26", "v27", "v28", "v29", "v30", "v31", "v32", "v33", "v34", "v35", "v36", "v37", "v38", "v39", "v40", "v41", "v42", "v43", "v44", "v45", "v46", "v47", "v48", "v49", "v50", "v51", "v52", "v53", "v54", "v55", "v56", "v57", "v58", "v59", "v60", "v61", "v62", "v63", "v64", "v65", "v66", "v67", "v68", "v69", "v70", "v71", "v72", "v73", "v74", "v75", "v76", "v77", "v78", "v79", "v80", "v81", "v82", "v83", "v84", "v85", "v86", "v87", "v88", "v89", "v90", "v91", "v92", "v93", "v94", "v95", "v96", "v97", "v98", "v99", "v100", "v101", "v102", "v103", "v104", "v105", "v106", "v107", "v108", "v109", "v110", "v111", "v112", "v113", "v114", "v115", "v116", "v117", "v118", "v119", "v120", "v121", "v122", "v123", "v124", "v125", "v126", "v127", "v128", "v129", "v130", "v131", "v132", "v133", "v134", "v135", "v136", "v137", "v138", "v139", "v140", "v141", "v142", "v143", "v144", "v145", "v146", "v147", "v148", "v149", "v150", "v151", "v152", "v153", "v154", "v155", "v156", "v157", "v158", "v159", "v160", "v161", "v162", "v163", "v164", "v165", "v166", "v167", "v168", "v169", "v170", "v171", "v172", "v173", "v174", "v175", "v176", "v177", "v178", "v179", "v180", "v181", "v182", "v183", "v184", "v185", "v186", "v187", "v188", "v189", "v190", "v191", "v192", "v193", "v194", "v195", "v196", "v197", "v198", "v199", "v200", "v201", "v202", "v203", "v204", "v205", "v206", "v207", "v208", "v209", "v210", "v211", "v212", "v213", "v214", "v215", "v216", "v217", "v218", "v219", "v220", "v221", "v222", "v223", "v224", "v225", "v226", "v227", "v228", "v229", "v230", "v231", "v232", "v233", "v234", "v235", "v236", "v237", "v238", "v239", "v240", "v241", "v242", "v243", "v244", "v245", "v246", "v247", "v248", "v249", "v250", "v251", "v252", "v253", "v254", "v255", "a0", "a1", "a2", "a3", "a4", "a5", "a6", "a7", "a8", "a9", "a10", "a11", "a12", "a13", "a14", "a15", "a16", "a17", "a18", "a19", "a20", "a21", "a22", "a23", "a24", "a25", "a26", "a27", "a28", "a29", "a30", "a31", "a32", "a33", "a34", "a35", "a36", "a37", "a38", "a39", "a40", "a41", "a42", "a43", "a44", "a45", "a46", "a47", "a48", "a49", "a50", "a51", "a52", "a53", "a54", "a55", "a56", "a57", "a58", "a59", "a60", "a61", "a62", "a63", "a64", "a65", "a66", "a67", "a68", "a69", "a70", "a71", "a72", "a73", "a74", "a75", "a76", "a77", "a78", "a79", "a80", "a81", "a82", "a83", "a84", "a85", "a86", "a87", "a88", "a89", "a90", "a91", "a92", "a93", "a94", "a95", "a96", "a97", "a98", "a99", "a100", "a101", "a102", "a103", "a104", "a105", "a106", "a107", "a108", "a109", "a110", "a111", "a112", "a113", "a114", "a115", "a116", "a117", "a118", "a119", "a120", "a121", "a122", "a123", "a124", "a125", "a126", "a127", "a128", "a129", "a130", "a131", "a132", "a133", "a134", "a135", "a136", "a137", "a138", "a139", "a140", "a141", "a142", "a143", "a144", "a145", "a146", "a147", "a148", "a149", "a150", "a151", "a152", "a153", "a154", "a155", "a156", "a157", "a158", "a159", "a160", "a161", "a162", "a163", "a164", "a165", "a166", "a167", "a168", "a169", "a170", "a171", "a172", "a173", "a174", "a175", "a176", "a177", "a178", "a179", "a180", "a181", "a182", "a183", "a184", "a185", "a186", "a187", "a188", "a189", "a190", "a191", "a192", "a193", "a194", "a195", "a196", "a197", "a198", "a199", "a200", "a201", "a202", "a203", "a204", "a205", "a206", "a207", "a208", "a209", "a210", "a211", "a212", "a213", "a214", "a215", "a216", "a217", "a218", "a219", "a220", "a221", "a222", "a223", "a224", "a225", "a226", "a227", "a228", "a229", "a230", "a231", "a232", "a233", "a234", "a235", "a236", "a237", "a238", "a239", "a240", "a241", "a242", "a243", "a244", "a245", "a246", "a247", "a248", "a249", "a250", "a251", "a252", "a253", "a254", "a255", "s3", "s4", "s5", "s6", "s7", "s8", "s9", "s10", "s11", "s12", "s13", "s14", "s15", "s16", "s17", "s18", "s19", "s20", "s21", "s22", "s23", "s24", "s25", "s26", "s27", "s28", "s29", "s30", "s31", "s32", "s33", "s34", "s35", "s36", "s37", "s38", "s39", "s40", "s41", "s42", "s43", "s44", "s45", "s46", "s47", "s48", "s49", "s50", "s51", "s52", "s53", "s54", "s55", "s56", "s57", "s58", "s59", "s60", "s61", "s62", "s63", "s64", "s65", "s66", "s67", "s68", "s69", "s70", "s71", "s72", "s73", "s74", "s75", "s76", "s77", "s78", "s79", "s80", "s81", "s82", "s83", "s84", "s85", "s86", "s87", "s88", "s89", "s90", "s91", "s92", "s93", "s94", "s95", "s96", "s97", "s98", "s99", "s100", "s101", "vcc", "scc", "memory");
}

extern "C" void kernel_launch(void* const* d_in, const int* in_sizes, int n_in, void* d_out, int out_size, void* d_ws, size_t ws_size, hipStream_t stream) {
  Args P;
  for (int br = 0; br < 2; ++br) {
    P.p[br][0] = d_in[5 * br]; P.p[br][1] = d_in[5 * br + 3]; P.p[br][2] = d_in[5 * br + 1]; P.p[br][3] = d_in[5 * br + 2]; P.p[br][4] = d_in[5 * br + 4];
    P.p[br][5] = d_ws; P.p[br][6] = (char*)d_out + br * 1280; P.p[br][7] = d_ws;
  }
  (void)hipFuncSetAttribute((const void*)fused_kernel, hipFuncAttributeMaxDynamicSharedMemorySize, LDS1);
  fused_kernel<<<256, 256, LDS1, stream>>>(P);
}
```

```cpp
#include <hip/hip_runtime.h>
#include <hip/hip_bf16.h>

constexpr unsigned LDS1 = 160 * 1024;

struct Args { const void* p[2][8]; };

__global__ __launch_bounds__(256, 1) void fused_kernel(Args P) {
  unsigned tid = threadIdx.x;
  asm volatile(
R"A(.macro S_ a,b,c
s_add_u32 \a, \b, \c
.endm
.macro W_ a
s_waitcnt lgkmcnt(\a)
.endm
.macro L_ x,r,s,o
buffer_load_dwordx4 \x, s[\r:\r+3], \s offen offset:\o lds
.endm
.macro X_ a,b
v_accvgpr_write_b32 \a, \b
.endm
.macro R_ a,b
v_accvgpr_read_b32 \a, \b
.endm
.macro Ta d,x,o
ds_read_b64_tr_b16 a[\d:\d+1], \x offset:\o
.endm
.macro Da d,x,o
ds_read_b128 a[\d:\d+3], \x offset:\o
.endm
.macro Zvaa d,a,b
v_mfma_f32_16x16x32_bf16 v[\d:\d+3], a[\a:\a+3], a[\b:\b+3], 0
.endm
.macro Mvaa d,a,b
v_mfma_f32_16x16x32_bf16 v[\d:\d+3], a[\a:\a+3], a[\b:\b+3], v[\d:\d+3]
.endm
.macro U_ a,b,c
v_mul_f32 \a, \b, \c
.endm
.macro C_ d,a,b
v_cvt_pk_bf16_f32 v\d, v\a, v\b
.endm
.macro Ln x,r,s,o
buffer_load_dwordx4 \x, s[\r:\r+3], \s offen offset:\o nt lds
.endm
.macro Dv d,x,o
ds_read_b128 v[\d:\d+3], \x offset:\o
.endm
.macro Zvvv d,a,b
v_mfma_f32_16x16x32_bf16 v[\d:\d+3], v[\a:\a+3], v[\b:\b+3], 0
.endm
.macro Mvvv d,a,b
v_mfma_f32_16x16x32_bf16 v[\d:\d+3], v[\a:\a+3], v[\b:\b+3], v[\d:\d+3]
.endm
.macro B_ a,b,c
v_sub_f32 \a, \b, \c
.endm
.macro E_ r
v_exp_f32 v\r, v\r
.endm
.macro Nvvv d,a,b,c
v_mfma_f32_16x16x32_bf16 v[\d:\d+3], v[\a:\a+3], v[\b:\b+3], v[\c:\c+3]
.endm
.macro Tv d,x,o
ds_read_b64_tr_b16 v[\d:\d+1], \x offset:\o
.endm
.macro Zaav d,a,b
v_mfma_f32_16x16x32_bf16 a[\d:\d+3], a[\a:\a+3], v[\b:\b+3], 0
.endm
.macro Zavv d,a,b
v_mfma_f32_16x16x32_bf16 a[\d:\d+3], v[\a:\a+3], v[\b:\b+3], 0
.endm
.macro A_ a,b,c
v_add_f32 \a, \b, \c
.endm
.macro Mavv d,a,b
v_mfma_f32_16x16x32_bf16 a[\d:\d+3], v[\a:\a+3], v[\b:\b+3], a[\d:\d+3]
.endm
.macro Zvav d,a,b
v_mfma_f32_16x16x32_bf16 v[\d:\d+3], a[\a:\a+3], v[\b:\b+3], 0
.endm
.macro Mvav d,a,b
v_mfma_f32_16x16x32_bf16 v[\d:\d+3], a[\a:\a+3], v[\b:\b+3], v[\d:\d+3]
.endm
.macro Y_ r,o
ds_write_addtid_b32 v\r offset:\o
.endm
.macro Zaaa d,a,b
v_mfma_f32_16x16x32_bf16 a[\d:\d+3], a[\a:\a+3], a[\b:\b+3], 0
.endm
.macro Maaa d,a,b
v_mfma_f32_16x16x32_bf16 a[\d:\d+3], a[\a:\a+3], a[\b:\b+3], a[\d:\d+3]
.endm
.macro Maav d,a,b
v_mfma_f32_16x16x32_bf16 a[\d:\d+3], a[\a:\a+3], v[\b:\b+3], a[\d:\d+3]
.endm
.macro Nvav d,a,b,c
v_mfma_f32_16x16x32_bf16 v[\d:\d+3], a[\a:\a+3], v[\b:\b+3], v[\c:\c+3]
.endm
s_and_b32 s3, %2, 4
s_lshl_b32 s3, s3, 4
s_load_dwordx16 s[8:23], %1, s3
s_memrealtime s[100:101]
s_memtime s[90:91]
v_readfirstlane_b32 s4, %0
v_and_b32 v16, 63, %0
v_lshlrev_b32 v10, 4, v16
s_mov_b32 s7, %2
s_lshr_b32 s4, s4, 6
.Lcode0:
s_getpc_b64 s[44:45]
s_mov_b32 s46, .Ldone-.Lcode0
s_mov_b32 s47, 0x00020000
s_lshr_b32 s66, s7, 3
s_lshl_b32 s66, s66, 2
S_ s66,s66,s4
s_lshl_b32 s66, s66, 11
v_min_u32 v40, 15, v16
v_lshlrev_b32 v40, 7, v40
buffer_load_dword v247, v40, s[44:47], s66 offen
s_and_b32 s3, s7, 1
s_lshl_b32 s3, s3, 3
s_lshr_b32 s53, s7, 5
S_ s3,s3,s53
s_and_b32 s6, s7, 2
s_lshl_b32 s6, s6, 1
s_bfe_u32 s53, s7, 0x20003
S_ s6,s6,s53
s_and_b32 s5, s7, 4
s_lshl_b32 s5, s5, 2
S_ s5,s5,s3
s_mul_i32 s50, s4, 5120
s_mul_i32 s60, s4, 51200
s_lshr_b32 s77, s4, 1
s_and_b32 s53, s4, 1
s_mul_i32 s59, s53, 393216
s_lshl_b32 s52, s53, 11
s_mul_i32 s61, s53, 6144
s_mul_i32 s51, s77, 61440
S_ s55,s51,40960
S_ s51,s55,s61
s_sub_u32 s52, s51, s52
s_lshl_b32 s86, s53, 2
s_mul_i32 s56, s53, 10240
v_lshrrev_b32 v17, 3, v16
v_and_b32 v18, 7, v16
v_lshrrev_b32 v23, 4, v16
v_and_b32 v24, 1, v23
v_lshlrev_b32 v24, 2, v24
v_xor_b32 v24, v18, v24
v_mul_u32_u24 v6, 12288, v17
v_lshl_add_u32 v6, v24, 4, v6
v_add_u32 v24, s86, v23
v_xor_b32 v24, v18, v24
v_mul_u32_u24 v4, 1280, v17
v_lshl_add_u32 v4, v24, 4, v4
v_xor_b32 v5, 64, v4
s_getreg_b32 s53, hwreg(HW_REG_XCC_ID, 0, 4)
s_bitcmp1_b32 0xaaaa, s53
s_cselect_b32 s58, 2, 0
s_cselect_b32 s89, 1, 0
s_cselect_b32 s53, 768, 0
S_ s59,s59,s53
s_lshl_b32 s54, s6, 2
S_ s54,s54,s77
s_mul_i32 s86, s54, 384
s_mul_i32 s87, s3, 204800
s_lshl_b32 s53, s3, 5
S_ s53,s53,s54
s_mul_i32 s85, s53, 196608
s_mov_b32 s30, 7853184
s_mov_b32 s31, 0x00020000
s_mov_b32 s26, 204800
s_mov_b32 s27, 0x00020000
W_ 0
s_mov_b32 s98, s100
s_mov_b32 s99, 0
s_mov_b32 s88, s90
S_ s28,s10,s86
s_addc_u32 s29, s11, 0
S_ s24,s8,s87
s_addc_u32 s25, s9, 0
s_buffer_load_dword s67, s[24:27], s60
s_mul_i32 s64, s66, 30
s_buffer_load_dword s67, s[28:31], s64
s_mov_b32 s11, 0
S_ s65,s59,0
S_ m0,s52,8192
s_mov_b64 s[92:93], s[18:19]
s_mov_b32 s53, 251658240
L_ v6,28,s65,0
S_ s65,s59,98304
S_ m0,s52,9216
S_ s92,s92,s53
s_addc_u32 s93, s93, 0
L_ v6,28,s65,0
S_ s65,s59,196608
S_ m0,s52,10240
s_mov_b32 s94, 131072
s_mov_b32 s95, 0x00020000
L_ v6,28,s65,0
S_ s65,s59,294912
S_ m0,s52,11264
s_lshl_b32 s96, s5, 3
S_ s96,s96,s6
L_ v6,28,s65,0
S_ s65,s60,0
S_ m0,s50,0
s_lshl_b32 s96, s96, 9
v_cmp_gt_u32 vcc, 8, v16
L_ v4,24,s65,0
S_ s65,s60,10240
S_ m0,s50,1024
s_lshl_b32 s54, s5, 12
v_lshl_add_u32 v24, v16, 9, s54
L_ v5,24,s65,0
S_ s65,s60,20480
S_ m0,s50,2048
v_mov_b32 v12, 0x7fff0000
v_cndmask_b32 v12, v12, v24, vcc
L_ v4,24,s65,0
S_ s65,s60,30720
S_ m0,s50,3072
s_lshl_b32 s54, s6, 2
v_add_u32 v13, s54, v12
L_ v5,24,s65,0
S_ s65,s60,40960
S_ m0,s50,4096
v_lshrrev_b32 v19, 2, v10
v_mul_u32_u24 v25, 384, v17
L_ v4,24,s65,0
s_load_dword s67, s[12:13], s85
s_load_dword s67, s[14:15], s85
S_ s64,s85,393216
s_load_dword s67, s[12:13], s64
s_load_dword s67, s[14:15], s64
s_lshl_b32 s64, s6, 2
S_ s64,s64,s4
s_mul_i32 s64, s64, 245760
s_load_dword s67, s[16:17], s64
s_cmp_eq_u32 s4, 0
s_cbranch_scc0 .Lhs0a
buffer_load_dword v20, v19, s[92:95], s96 offen offset:128 sc1
buffer_load_dword v21, v19, s[92:95], s96 offen offset:384 sc1
buffer_load_dword a252, v19, s[92:95], s96 offen offset:256 sc1
buffer_load_dword a253, v13, s[92:95], 0 offen offset:256 sc1
buffer_load_dword v22, off, s[92:95], s96 offset:0 sc1
.Lhs0a:
S_ s65,s59,0
S_ m0,s52,16256
v_xor_b32 v24, v18, v23
v_lshl_add_u32 v7, v24, 4, v25
v_lshlrev_b32 v24, 1, v23
v_xor_b32 v24, v18, v24
v_lshl_add_u32 v9, v24, 4, v25
v_and_b32 v26, 15, v16
v_lshrrev_b32 v27, 2, v26
v_and_b32 v28, 3, v16
v_lshrrev_b32 v24, 1, v26
v_xor_b32 v24, v23, v24
L_ v6,28,s65,128
S_ s65,s59,98304
S_ m0,s52,17280
v_lshlrev_b32 v24, 4, v24
v_lshl_add_u32 v24, v18, 7, v24
v_lshrrev_b32 v25, 3, v26
v_lshl_add_u32 v2, v25, 10, v24
v_add_u32 v2, s56, v2
v_mul_u32_u24 v1, 3072, v25
v_add3_u32 v1, v1, v24, s55
v_lshlrev_b32 v24, 2, v25
v_xor_b32 v24, v28, v24
v_lshlrev_b32 v24, 4, v24
L_ v6,28,s65,128
S_ s65,s59,196608
S_ m0,s52,18304
v_lshl_add_u32 v24, v27, 7, v24
v_lshl_add_u32 %0, v23, 10, v24
v_add_u32 %0, s55, %0
v_and_b32 v29, 1, v23
v_lshl_add_u32 v24, v29, 1, v25
v_lshlrev_b32 v24, 1, v24
v_lshrrev_b32 v30, 1, v28
v_xor_b32 v24, v30, v24
v_lshlrev_b32 v24, 4, v24
v_lshl_add_u32 v29, v29, 2, v27
L_ v6,28,s65,128
S_ s65,s59,294912
S_ m0,s52,19328
v_lshl_add_u32 v24, v29, 7, v24
v_and_b32 v29, 1, v28
v_lshl_add_u32 v24, v29, 3, v24
v_lshrrev_b32 v29, 1, v23
v_mul_u32_u24 v3, 3072, v29
v_add3_u32 v3, v3, v24, s55
X_ a242,%0
X_ a244,v1
X_ a243,v2
X_ a245,v3
L_ v6,28,s65,128
S_ s65,s59,0
S_ m0,s52,24320
X_ a240,v4
X_ a248,v10
X_ a241,v12
X_ a251,v13
v_xor_b32 v1, 64, %0
v_xor_b32 v3, 64, v2
v_xor_b32 v8, 64, v7
S_ s32,s12,s85
s_addc_u32 s33, s13, 0
s_mov_b32 s34, 589824
L_ v6,28,s65,256
S_ s65,s59,98304
S_ m0,s52,25344
s_mov_b32 s35, 0x00020000
S_ s36,s14,s85
s_addc_u32 s37, s15, 0
s_mov_b32 s38, 589824
s_mov_b32 s39, 0x00020000
s_lshl_b32 s87, s87, 1
S_ s20,s20,s87
s_addc_u32 s21, s21, 0
s_mov_b32 s57, 20000
s_mov_b32 s48, 1037384597
L_ v6,28,s65,256
S_ s65,s59,196608
S_ m0,s52,26368
s_mov_b32 s49, 1037384597
s_mul_i32 s53, s5, 1638400
S_ s53,s53,134217728
S_ s22,s18,s53
s_addc_u32 s23, s19, 0
s_lshl_b32 s53, s6, 3
s_lshr_b32 s54, s7, 5
S_ s53,s53,s54
s_lshl_b32 s53, s53, 2
S_ s53,s53,s4
L_ v6,28,s65,256
S_ s65,s59,294912
S_ m0,s52,27392
s_mul_i32 s87, s53, 30720
s_lshl_b32 s53, s6, 2
S_ s53,s53,s77
s_mul_i32 s72, s53, 245760
s_and_b32 s54, s4, 1
s_mul_i32 s53, s54, 61440
S_ s72,s72,s53
s_lshl_b32 s53, s77, 6
s_xor_b32 s83, s53, 64
s_xor_b32 s53, s4, 2
L_ v6,28,s65,256
S_ s65,s60,128
S_ m0,s50,20480
s_mul_i32 s75, s53, 10240
s_cmp_eq_u32 s4, 0
s_cselect_b32 s73, 0, 2147418112
s_mul_i32 s53, s4, 5120
s_mul_i32 s78, s6, 204800
S_ s78,s78,s53
s_and_b32 s84, s6, 7
s_mul_i32 s84, s84, 20480
S_ s84,s84,s53
s_mul_i32 s53, s6, 5120
L_ v4,24,s65,0
S_ s65,s60,10368
S_ m0,s50,21504
S_ s53,s53,163840
s_lshl_b32 s54, s4, 10
S_ s74,s53,s54
S_ s53,s53,4096
s_cmp_eq_u32 s4, 0
s_cselect_b32 s82, s53, 4026531840
s_and_b32 s53, s4, 1
s_mul_i32 s53, s53, 204800
s_lshl_b32 s54, s6, 7
S_ s53,s53,s54
L_ v5,24,s65,0
S_ s65,s60,20608
S_ m0,s50,22528
s_lshl_b32 s54, s77, 6
S_ s8,s53,s54
s_and_b32 s53, s6, 1
s_mul_i32 s53, s53, 204800
s_lshr_b32 s54, s6, 1
s_lshl_b32 s54, s54, 6
S_ s53,s53,s54
S_ s53,s53,1024
s_mul_i32 s54, s4, 40960
S_ s9,s53,s54
L_ v4,24,s65,0
S_ s65,s60,30848
S_ m0,s50,23552
S_ s53,s53,163840
s_cmp_eq_u32 s4, 0
s_cselect_b32 s10, s53, 4026531840
R_ v12,a248
v_lshrrev_b32 v12, 4, v12
v_min_u32 v10, 11, v12
s_mul_i32 s56, s4, 12
v_add_u32 v10, s56, v10
v_mul_u32_u24 v11, 43691, v10
v_lshrrev_b32 v11, 18, v11
L_ v5,24,s65,0
S_ s65,s60,41088
S_ m0,s50,24576
v_lshlrev_b32 v10, 7, v10
v_mul_u32_u24 v11, 11520, v11
v_add_u32 v10, v10, v11
v_min_u32 v12, 9, v12
s_and_b32 s56, s6, 3
s_lshl_b32 s56, s56, 2
S_ s56,s56,s4
s_mul_i32 s56, s56, 10
v_add_u32 v12, s56, v12
v_mul_u32_u24 v12, 1280, v12
L_ v4,24,s65,0
S_ s65,s59,786432
S_ m0,s52,0
s_lshr_b32 s56, s7, 5
s_mul_i32 s56, s56, 98304
s_and_b32 s55, s59, 4095
S_ s56,s56,s55
s_mul_i32 s55, s77, 384
s_sub_u32 s56, s56, s55
s_mov_b32 s63, 0
L_ v6,28,s65,0
S_ s65,s59,884736
S_ m0,s52,1024
s_nop 0
L_ v6,28,s65,0
S_ s65,s59,983040
S_ m0,s52,2048
s_nop 0
L_ v6,28,s65,0
S_ s65,s59,1081344
S_ m0,s52,3072
s_nop 0
L_ v6,28,s65,0
S_ s55,s56,786432
buffer_load_dword a254, v10, s[28:31], s55 offen
S_ s55,s56,1572864
buffer_load_dword a254, v10, s[28:31], s55 offen
buffer_load_dword a254, v12, s[24:27], s63 offen offset:256
S_ s55,s56,2359296
buffer_load_dword a254, v10, s[28:31], s55 offen
buffer_load_dword a254, v12, s[24:27], s63 offen offset:384
s_waitcnt vmcnt(22) lgkmcnt(0)
s_barrier
s_memtime s[90:91]
s_memrealtime s[68:69]
Ta 20,%0,8192
Ta 22,%0,8704
Ta 24,%0,12288
Ta 26,%0,12800
Da 60,v2,0
Da 100,v3,0
Da 64,v2,2048
Da 104,v3,2048
Da 68,v2,4096
Da 108,v3,4096
Da 72,v2,6144
Da 112,v3,6144
Da 76,v2,8192
Da 116,v3,8192
Ta 28,%0,8200
Ta 30,%0,8712
Ta 32,%0,12296
Ta 34,%0,12808
s_cmp_eq_u32 s4, 0
s_cbranch_scc0 .Lhs0b
v_readfirstlane_b32 s62, v22
.Lhs0l:
s_nop 3
S_ s62,s62,1
v_cmp_eq_u32 vcc, s62, v20
s_nop 1
s_or_b32 s53, vcc_lo, vcc_hi
v_cmp_eq_u32 vcc, s62, v21
s_nop 1
s_or_b32 s54, vcc_lo, vcc_hi
s_or_b32 s53, s53, s54
s_cbranch_scc1 .Lhs0l
v_mov_b32 v22, s62
s_mov_b64 exec, 1
s_nop 1
buffer_store_dword v22, off, s[92:95], s96 sc1
s_mov_b64 exec, -1
s_nop 1
.Lhs0b:
s_cmp_eq_u32 s58, 0
s_cbranch_scc1 .Lq
.Lqy:
W_ 13
Zvaa 16,20,60
s_nop 0
Ta 36,v1,8192
Ta 38,v1,8704
W_ 14
Mvaa 16,24,100
R_ v12,a248
W_ 13
Zvaa 24,20,64
v_lshrrev_b32 v12, 4, v12
W_ 12
Mvaa 24,24,104
s_nop 0
Ta 224,v1,12288
Ta 226,v1,12800
W_ 13
Zvaa 32,20,68
v_min_u32 v10, 11, v12
W_ 12
Mvaa 32,24,108
s_mul_i32 s56, s4, 12
W_ 11
Zvaa 40,20,72
v_add_u32 v10, s56, v10
W_ 10
Mvaa 40,24,112
v_mul_u32_u24 v11, 43691, v10
W_ 9
Zvaa 48,20,76
v_lshrrev_b32 v11, 18, v11
W_ 8
Mvaa 48,24,116
W_ 4
v_lshlrev_b32 v10, 7, v10
Zvaa 52,32,116
s_nop 0
Ta 20,v1,8200
Ta 22,v1,8712
Mvaa 52,28,76
v_mul_u32_u24 v11, 11520, v11
Zvaa 44,32,112
v_add_u32 v10, v10, v11
Mvaa 44,28,72
s_nop 0
Ta 24,v1,12296
Ta 26,v1,12808
Zvaa 36,32,108
v_min_u32 v12, 9, v12
Mvaa 36,28,68
s_and_b32 s56, s6, 3
Zvaa 28,32,104
s_lshl_b32 s56, s56, 2
Mvaa 28,28,64
S_ s56,s56,s4
Zvaa 20,32,100
s_waitcnt vmcnt(18) lgkmcnt(0)
s_barrier
s_mul_i32 s56, s56, 10
Mvaa 20,28,60
S_ s65,s59,786432
v_add_u32 v12, s56, v12
Zvaa 56,36,60
S_ m0,s52,8064
Ta 28,%0,16384
Ta 30,%0,16896
Mvaa 56,224,100
L_ v6,28,s65,128
Zvaa 64,36,64
S_ s65,s59,884736
v_mul_u32_u24 v12, 1280, v12
Mvaa 64,224,104
S_ m0,s52,9088
Ta 32,%0,20480
Ta 34,%0,20992
Zvaa 72,36,68
s_lshr_b32 s56, s7, 5
Mvaa 72,224,108
L_ v6,28,s65,128
Zvaa 80,36,72
S_ s65,s59,983040
s_mul_i32 s56, s56, 98304
Mvaa 80,224,112
S_ m0,s52,10112
s_and_b32 s55, s59, 4095
Zvaa 88,36,76
S_ s56,s56,s55
Mvaa 88,224,116
s_mul_i32 s55, s77, 384
Zvaa 92,24,116
s_sub_u32 s56, s56, s55
Ta 36,%0,16392
Ta 38,%0,16904
Mvaa 92,20,76
L_ v6,28,s65,128
Zvaa 84,24,112
S_ s65,s59,1081344
s_mov_b32 s63, 0
Mvaa 84,20,72
S_ m0,s52,11136
Ta 224,%0,20488
Ta 226,%0,21000
Zvaa 76,24,108
S_ s55,s56,3145728
Mvaa 76,20,68
L_ v6,28,s65,128
Zvaa 68,24,104
S_ s65,s60,256
Mvaa 68,20,64
S_ m0,s50,0
Zvaa 60,24,100
Mvaa 60,20,60
W_ 4
Zvaa 96,28,60
s_nop 0
Ta 20,v1,16384
Ta 22,v1,16896
Mvaa 96,32,100
L_ v4,24,s65,0
Zvaa 104,28,64
S_ s65,s60,10496
Mvaa 104,32,104
S_ m0,s50,1024
Ta 24,v1,20480
Ta 26,v1,20992
Zvaa 112,28,68
Mvaa 112,32,108
L_ v5,24,s65,0
Zvaa 120,28,72
S_ s65,s60,20736
Mvaa 120,32,112
S_ m0,s50,2048
Zvaa 128,28,76
Mvaa 128,32,116
W_ 4
Zvaa 132,224,116
s_nop 0
Ta 28,v1,16392
Ta 30,v1,16904
Mvaa 132,36,76
L_ v4,24,s65,0
Zvaa 124,224,112
S_ s65,s60,30976
Mvaa 124,36,72
S_ m0,s50,3072
Ta 32,v1,20488
Ta 34,v1,21000
Zvaa 116,224,108
Mvaa 116,36,68
L_ v5,24,s65,0
Zvaa 108,224,104
S_ s65,s60,41216
Mvaa 108,36,64
S_ m0,s50,4096
Zvaa 100,224,100
s_waitcnt vmcnt(17) lgkmcnt(0)
s_barrier
Mvaa 100,36,60
Da 140,v2,20480
Zvaa 136,20,60
s_nop 0
Ta 36,%0,24576
Ta 38,%0,25088
Mvaa 136,24,100
L_ v4,24,s65,0
Zvaa 144,20,64
S_ s65,s59,786432
Da 180,v3,20480
Mvaa 144,24,104
S_ m0,s52,16128
Ta 224,%0,28672
Ta 226,%0,29184
Zvaa 152,20,68
Mvaa 152,24,108
Da 144,v2,22528
Zvaa 160,20,72
L_ v6,28,s65,256
Mvaa 160,24,112
S_ s65,s59,884736
Zvaa 168,20,76
S_ m0,s52,17152
Da 184,v3,22528
Mvaa 168,24,116
Zvaa 172,32,116
s_nop 0
Ta 20,%0,24584
Ta 22,%0,25096
Mvaa 172,28,76
Da 148,v2,24576
Zvaa 164,32,112
L_ v6,28,s65,256
Mvaa 164,28,72
S_ s65,s59,983040
Ta 24,%0,28680
Ta 26,%0,29192
Zvaa 156,32,108
S_ m0,s52,18176
Da 188,v3,24576
Mvaa 156,28,68
Zvaa 148,32,104
L_ v6,28,s65,256
Mvaa 148,28,64
S_ s65,s59,1081344
Da 152,v2,26624
Zvaa 140,32,100
S_ m0,s52,19200
Mvaa 140,28,60
W_ 9
Zvaa 176,36,60
s_nop 0
Ta 28,v1,24576
Ta 30,v1,25088
Mvaa 176,224,100
Da 192,v3,26624
Zvaa 184,36,64
L_ v6,28,s65,256
Mvaa 184,224,104
s_nop 0
Ta 32,v1,28672
Ta 34,v1,29184
Zvaa 192,36,68
Da 156,v2,28672
Mvaa 192,224,108
Zvaa 200,36,72
Da 196,v3,28672
Mvaa 200,224,112
buffer_load_dword a254, v10, s[28:31], s55 offen
Zvaa 208,36,76
Mvaa 208,224,116
W_ 9
Zvaa 212,24,116
s_nop 0
Ta 36,v1,24584
Ta 38,v1,25096
Mvaa 212,20,76
buffer_load_dword a254, v12, s[24:27], s63 offen offset:512
Zvaa 204,24,112
Mvaa 204,20,72
s_nop 0
Ta 224,v1,28680
Ta 226,v1,29192
Zvaa 196,24,108
Mvaa 196,20,68
Zvaa 188,24,104
Mvaa 188,20,64
Zvaa 180,24,100
s_waitcnt vmcnt(20) lgkmcnt(0)
s_barrier
Mvaa 180,20,60
S_ s65,s59,1572864
Zvaa 216,28,60
S_ m0,s52,24576
Ta 20,%0,0
Ta 22,%0,512
Mvaa 216,32,100
L_ v6,28,s65,0
Zvaa 224,28,64
S_ s65,s59,1671168
Mvaa 224,32,104
S_ m0,s52,25600
Ta 24,%0,4096
Ta 26,%0,4608
Zvaa 232,28,68
Mvaa 232,32,108
L_ v6,28,s65,0
Zvaa 240,28,72
S_ s65,s59,1769472
Mvaa 240,32,112
S_ m0,s52,26624
Zvaa 248,28,76
Mvaa 248,32,116
Zvaa 252,224,116
s_nop 0
Ta 28,%0,8
Ta 30,%0,520
Mvaa 252,36,76
L_ v6,28,s65,0
Zvaa 244,224,112
S_ s65,s59,1867776
Mvaa 244,36,72
S_ m0,s52,27648
Ta 32,%0,4104
Ta 34,%0,4616
Zvaa 236,224,108
Mvaa 236,36,68
L_ v6,28,s65,0
Zvaa 228,224,104
S_ s65,s60,384
Mvaa 228,36,64
S_ m0,s50,20480
Zvaa 220,224,100
Mvaa 220,36,60
W_ 4
Mvaa 16,20,140
s_nop 0
Ta 36,v1,0
Ta 38,v1,512
Mvaa 16,24,180
L_ v4,24,s65,0
Mvaa 24,20,144
S_ s65,s60,10624
Mvaa 24,24,184
S_ m0,s50,21504
Ta 224,v1,4096
Ta 226,v1,4608
Mvaa 32,20,148
Mvaa 32,24,188
L_ v5,24,s65,0
Mvaa 40,20,152
S_ s65,s60,20864
Mvaa 40,24,192
S_ m0,s50,22528
Mvaa 48,20,156
Mvaa 48,24,196
W_ 4
Mvaa 52,32,196
s_nop 0
Ta 20,v1,8
Ta 22,v1,520
Mvaa 52,28,156
L_ v4,24,s65,0
Mvaa 44,32,192
S_ s65,s60,31104
Mvaa 44,28,152
S_ m0,s50,23552
Ta 24,v1,4104
Ta 26,v1,4616
Mvaa 36,32,188
Mvaa 36,28,148
L_ v5,24,s65,0
Mvaa 28,32,184
S_ s65,s60,41344
Mvaa 28,28,144
S_ m0,s50,24576
Mvaa 20,32,180
s_waitcnt vmcnt(19) lgkmcnt(0)
s_barrier
Mvaa 20,28,140
L_ v4,24,s65,0
Mvaa 56,36,140
S_ s65,s59,1572864
Ta 28,%0,8192
Ta 30,%0,8704
Mvaa 56,224,180
S_ m0,s52,-128
Mvaa 64,36,144
Mvaa 64,224,184
s_nop 0
Ta 32,%0,12288
Ta 34,%0,12800
Mvaa 72,36,148
L_ v6,28,s65,128
Mvaa 72,224,188
S_ s65,s59,1671168
Mvaa 80,36,152
S_ m0,s52,896
Mvaa 80,224,192
Mvaa 88,36,156
L_ v6,28,s65,128
Mvaa 88,224,196
S_ s65,s59,1769472
Mvaa 92,24,196
S_ m0,s52,1920
Ta 36,%0,8200
Ta 38,%0,8712
Mvaa 92,20,156
S_ s55,s56,3932160
Mvaa 84,24,192
L_ v6,28,s65,128
Mvaa 84,20,152
S_ s65,s59,1867776
Ta 224,%0,12296
Ta 226,%0,12808
Mvaa 76,24,188
S_ m0,s52,2944
Mvaa 76,20,148
Mvaa 68,24,184
L_ v6,28,s65,128
Mvaa 68,20,144
Mvaa 60,24,180
Mvaa 60,20,140
W_ 4
Mvaa 96,28,140
s_nop 0
Ta 20,v1,8192
Ta 22,v1,8704
Mvaa 96,32,180
buffer_load_dword a254, v10, s[28:31], s55 offen
Mvaa 104,28,144
Mvaa 104,32,184
s_nop 0
Ta 24,v1,12288
Ta 26,v1,12800
Mvaa 112,28,148
Mvaa 112,32,188
buffer_load_dword a254, v12, s[24:27], s63 offen offset:640
Mvaa 120,28,152
Mvaa 120,32,192
Mvaa 128,28,156
Mvaa 128,32,196
W_ 4
Mvaa 132,224,196
s_nop 0
Ta 28,v1,8200
Ta 30,v1,8712
Mvaa 132,36,156
Mvaa 124,224,192
Mvaa 124,36,152
s_nop 0
Ta 32,v1,12296
Ta 34,v1,12808
Mvaa 116,224,188
Mvaa 116,36,148
Mvaa 108,224,184
Mvaa 108,36,144
Mvaa 100,224,180
s_waitcnt vmcnt(17) lgkmcnt(0)
s_barrier
Mvaa 100,36,140
S_ s65,s59,1572864
Da 60,v2,0
Mvaa 136,20,140
S_ m0,s52,7936
Ta 36,%0,16384
Ta 38,%0,16896
Mvaa 136,24,180
L_ v6,28,s65,256
Mvaa 144,20,144
S_ s65,s59,1671168
Da 100,v3,0
Mvaa 144,24,184
S_ m0,s52,8960
Ta 224,%0,20480
Ta 226,%0,20992
Mvaa 152,20,148
Mvaa 152,24,188
Da 64,v2,2048
Mvaa 160,20,152
L_ v6,28,s65,256
Mvaa 160,24,192
S_ s65,s59,1769472
Mvaa 168,20,156
S_ m0,s52,9984
Da 104,v3,2048
Mvaa 168,24,196
Mvaa 172,32,196
s_nop 0
Ta 20,%0,16392
Ta 22,%0,16904
Mvaa 172,28,156
Da 68,v2,4096
Mvaa 164,32,192
L_ v6,28,s65,256
Mvaa 164,28,152
S_ s65,s59,1867776
Ta 24,%0,20488
Ta 26,%0,21000
Mvaa 156,32,188
S_ m0,s52,11008
Da 108,v3,4096
Mvaa 156,28,148
Mvaa 148,32,184
L_ v6,28,s65,256
Mvaa 148,28,144
Da 72,v2,6144
Mvaa 140,32,180
Mvaa 140,28,140
W_ 9
Mvaa 176,36,140
s_nop 0
Ta 28,v1,16384
Ta 30,v1,16896
Mvaa 176,224,180
Da 112,v3,6144
Mvaa 184,36,144
Mvaa 184,224,184
s_nop 0
Ta 32,v1,20480
Ta 34,v1,20992
Mvaa 192,36,148
Da 76,v2,8192
Mvaa 192,224,188
Mvaa 200,36,152
Da 116,v3,8192
Mvaa 200,224,192
Mvaa 208,36,156
Mvaa 208,224,196
W_ 9
Mvaa 212,24,196
s_nop 0
Ta 36,v1,16392
Ta 38,v1,16904
Mvaa 212,20,156
Mvaa 204,24,192
Mvaa 204,20,152
s_nop 0
Ta 224,v1,20488
Ta 226,v1,21000
Mvaa 196,24,188
Mvaa 196,20,148
Mvaa 188,24,184
Mvaa 188,20,144
Mvaa 180,24,180
s_waitcnt vmcnt(15) lgkmcnt(0)
s_barrier
Mvaa 180,20,140
S_ s65,s59,2359296
Mvaa 216,28,140
S_ m0,s52,16384
Ta 20,%0,24576
Ta 22,%0,25088
Mvaa 216,32,180
L_ v6,28,s65,0
Mvaa 224,28,144
S_ s65,s59,2457600
Mvaa 224,32,184
S_ m0,s52,17408
Ta 24,%0,28672
Ta 26,%0,29184
Mvaa 232,28,148
Mvaa 232,32,188
L_ v6,28,s65,0
Mvaa 240,28,152
S_ s65,s59,2555904
Mvaa 240,32,192
S_ m0,s52,18432
Mvaa 248,28,156
Mvaa 248,32,196
Mvaa 252,224,196
s_nop 0
Ta 28,%0,24584
Ta 30,%0,25096
Mvaa 252,36,156
L_ v6,28,s65,0
Mvaa 244,224,192
S_ s65,s59,2654208
Mvaa 244,36,152
S_ m0,s52,19456
Ta 32,%0,28680
Ta 34,%0,29192
Mvaa 236,224,188
Mvaa 236,36,148
L_ v6,28,s65,0
Mvaa 228,224,184
S_ s65,s60,512
Mvaa 228,36,144
S_ m0,s50,0
Mvaa 220,224,180
Mvaa 220,36,140
W_ 4
Mvaa 16,20,60
s_nop 0
Ta 36,v1,24576
Ta 38,v1,25088
Mvaa 16,24,100
L_ v4,24,s65,0
Mvaa 24,20,64
S_ s65,s60,10752
Mvaa 24,24,104
S_ m0,s50,1024
Ta 224,v1,28672
Ta 226,v1,29184
Mvaa 32,20,68
Mvaa 32,24,108
L_ v5,24,s65,0
Mvaa 40,20,72
S_ s65,s60,20992
Mvaa 40,24,112
S_ m0,s50,2048
Mvaa 48,20,76
Mvaa 48,24,116
W_ 4
Mvaa 52,32,116
s_nop 0
Ta 20,v1,24584
Ta 22,v1,25096
Mvaa 52,28,76
L_ v4,24,s65,0
Mvaa 44,32,112
S_ s65,s60,31232
Mvaa 44,28,72
S_ m0,s50,3072
Ta 24,v1,28680
Ta 26,v1,29192
Mvaa 36,32,108
Mvaa 36,28,68
L_ v5,24,s65,0
Mvaa 28,32,104
S_ s65,s60,41472
Mvaa 28,28,64
S_ m0,s50,4096
Mvaa 20,32,100
s_waitcnt vmcnt(14) lgkmcnt(0)
s_barrier
Mvaa 20,28,60
L_ v4,24,s65,0
Mvaa 56,36,60
S_ s65,s59,2359296
Ta 28,%0,0
Ta 30,%0,512
Mvaa 56,224,100
S_ m0,s52,24448
Mvaa 64,36,64
Mvaa 64,224,104
s_nop 0
Ta 32,%0,4096
Ta 34,%0,4608
Mvaa 72,36,68
L_ v6,28,s65,128
Mvaa 72,224,108
S_ s65,s59,2457600
Mvaa 80,36,72
S_ m0,s52,25472
Mvaa 80,224,112
Mvaa 88,36,76
L_ v6,28,s65,128
Mvaa 88,224,116
S_ s65,s59,2555904
Mvaa 92,24,116
S_ m0,s52,26496
Ta 36,%0,8
Ta 38,%0,520
Mvaa 92,20,76
S_ s55,s56,4718592
Mvaa 84,24,112
L_ v6,28,s65,128
Mvaa 84,20,72
S_ s65,s59,2654208
Ta 224,%0,4104
Ta 226,%0,4616
Mvaa 76,24,108
S_ m0,s52,27520
Mvaa 76,20,68
Mvaa 68,24,104
L_ v6,28,s65,128
Mvaa 68,20,64
Mvaa 60,24,100
Mvaa 60,20,60
W_ 4
Mvaa 96,28,60
s_nop 0
Ta 20,v1,0
Ta 22,v1,512
Mvaa 96,32,100
buffer_load_dword a254, v10, s[28:31], s55 offen
Mvaa 104,28,64
Mvaa 104,32,104
s_nop 0
Ta 24,v1,4096
Ta 26,v1,4608
Mvaa 112,28,68
Mvaa 112,32,108
buffer_load_dword a254, v12, s[24:27], s63 offen offset:768
Mvaa 120,28,72
Mvaa 120,32,112
Mvaa 128,28,76
Mvaa 128,32,116
W_ 4
Mvaa 132,224,116
s_nop 0
Ta 28,v1,8
Ta 30,v1,520
Mvaa 132,36,76
Mvaa 124,224,112
Mvaa 124,36,72
s_nop 0
Ta 32,v1,4104
Ta 34,v1,4616
Mvaa 116,224,108
Mvaa 116,36,68
Mvaa 108,224,104
Mvaa 108,36,64
Mvaa 100,224,100
s_waitcnt vmcnt(15) lgkmcnt(0)
s_barrier
Mvaa 100,36,60
S_ s65,s59,2359296
Da 140,v2,20480
Mvaa 136,20,60
S_ m0,s52,-256
Ta 36,%0,8192
Ta 38,%0,8704
Mvaa 136,24,100
L_ v6,28,s65,256
Mvaa 144,20,64
S_ s65,s59,2457600
Da 180,v3,20480
Mvaa 144,24,104
S_ m0,s52,768
Ta 224,%0,12288
Ta 226,%0,12800
Mvaa 152,20,68
Mvaa 152,24,108
Da 144,v2,22528
Mvaa 160,20,72
L_ v6,28,s65,256
Mvaa 160,24,112
S_ s65,s59,2555904
Mvaa 168,20,76
S_ m0,s52,1792
Da 184,v3,22528
Mvaa 168,24,116
Mvaa 172,32,116
s_nop 0
Ta 20,%0,8200
Ta 22,%0,8712
Mvaa 172,28,76
Da 148,v2,24576
Mvaa 164,32,112
L_ v6,28,s65,256
Mvaa 164,28,72
S_ s65,s59,2654208
Ta 24,%0,12296
Ta 26,%0,12808
Mvaa 156,32,108
S_ m0,s52,2816
Da 188,v3,24576
Mvaa 156,28,68
Mvaa 148,32,104
L_ v6,28,s65,256
Mvaa 148,28,64
Da 152,v2,26624
Mvaa 140,32,100
Mvaa 140,28,60
W_ 9
Mvaa 176,36,60
s_nop 0
Ta 28,v1,8192
Ta 30,v1,8704
Mvaa 176,224,100
Da 192,v3,26624
Mvaa 184,36,64
Mvaa 184,224,104
s_nop 0
Ta 32,v1,12288
Ta 34,v1,12800
Mvaa 192,36,68
Da 156,v2,28672
Mvaa 192,224,108
Mvaa 200,36,72
Da 196,v3,28672
Mvaa 200,224,112
Mvaa 208,36,76
Mvaa 208,224,116
W_ 9
Mvaa 212,24,116
s_nop 0
Ta 36,v1,8200
Ta 38,v1,8712
Mvaa 212,20,76
Mvaa 204,24,112
Mvaa 204,20,72
s_nop 0
Ta 224,v1,12296
Ta 226,v1,12808
Mvaa 196,24,108
Mvaa 196,20,68
Mvaa 188,24,104
Mvaa 188,20,64
Mvaa 180,24,100
s_waitcnt vmcnt(15) lgkmcnt(0)
s_barrier
Mvaa 180,20,60
S_ s65,s59,3145728
Mvaa 216,28,60
S_ m0,s52,8192
Ta 20,%0,16384
Ta 22,%0,16896
Mvaa 216,32,100
L_ v6,28,s65,0
Mvaa 224,28,64
S_ s65,s59,3244032
Mvaa 224,32,104
S_ m0,s52,9216
Ta 24,%0,20480
Ta 26,%0,20992
Mvaa 232,28,68
Mvaa 232,32,108
L_ v6,28,s65,0
Mvaa 240,28,72
S_ s65,s59,3342336
Mvaa 240,32,112
S_ m0,s52,10240
Mvaa 248,28,76
Mvaa 248,32,116
Mvaa 252,224,116
s_nop 0
Ta 28,%0,16392
Ta 30,%0,16904
Mvaa 252,36,76
L_ v6,28,s65,0
Mvaa 244,224,112
S_ s65,s59,3440640
Mvaa 244,36,72
S_ m0,s52,11264
Ta 32,%0,20488
Ta 34,%0,21000
Mvaa 236,224,108
Mvaa 236,36,68
L_ v6,28,s65,0
Mvaa 228,224,104
S_ s65,s60,640
Mvaa 228,36,64
S_ m0,s50,20480
Mvaa 220,224,100
Mvaa 220,36,60
W_ 4
Mvaa 16,20,140
s_nop 0
Ta 36,v1,16384
Ta 38,v1,16896
Mvaa 16,24,180
L_ v4,24,s65,0
Mvaa 24,20,144
S_ s65,s60,10880
Mvaa 24,24,184
S_ m0,s50,21504
Ta 224,v1,20480
Ta 226,v1,20992
Mvaa 32,20,148
Mvaa 32,24,188
L_ v5,24,s65,0
Mvaa 40,20,152
S_ s65,s60,21120
Mvaa 40,24,192
S_ m0,s50,22528
Mvaa 48,20,156
Mvaa 48,24,196
W_ 4
Mvaa 52,32,196
s_nop 0
Ta 20,v1,16392
Ta 22,v1,16904
Mvaa 52,28,156
L_ v4,24,s65,0
Mvaa 44,32,192
S_ s65,s60,31360
Mvaa 44,28,152
S_ m0,s50,23552
Ta 24,v1,20488
Ta 26,v1,21000
Mvaa 36,32,188
Mvaa 36,28,148
L_ v5,24,s65,0
Mvaa 28,32,184
S_ s65,s60,41600
Mvaa 28,28,144
S_ m0,s50,24576
Mvaa 20,32,180
s_waitcnt vmcnt(14) lgkmcnt(0)
s_barrier
Mvaa 20,28,140
L_ v4,24,s65,0
Mvaa 56,36,140
S_ s65,s59,3145728
Ta 28,%0,24576
Ta 30,%0,25088
Mvaa 56,224,180
S_ m0,s52,16256
Mvaa 64,36,144
Mvaa 64,224,184
s_nop 0
Ta 32,%0,28672
Ta 34,%0,29184
Mvaa 72,36,148
L_ v6,28,s65,128
Mvaa 72,224,188
S_ s65,s59,3244032
Mvaa 80,36,152
S_ m0,s52,17280
Mvaa 80,224,192
Mvaa 88,36,156
L_ v6,28,s65,128
Mvaa 88,224,196
S_ s65,s59,3342336
Mvaa 92,24,196
S_ m0,s52,18304
Ta 36,%0,24584
Ta 38,%0,25096
Mvaa 92,20,156
S_ s55,s56,5505024
Mvaa 84,24,192
L_ v6,28,s65,128
Mvaa 84,20,152
S_ s65,s59,3440640
Ta 224,%0,28680
Ta 226,%0,29192
Mvaa 76,24,188
S_ m0,s52,19328
Mvaa 76,20,148
Mvaa 68,24,184
L_ v6,28,s65,128
Mvaa 68,20,144
Mvaa 60,24,180
Mvaa 60,20,140
W_ 4
Mvaa 96,28,140
s_nop 0
Ta 20,v1,24576
Ta 22,v1,25088
Mvaa 96,32,180
buffer_load_dword a254, v10, s[28:31], s55 offen
Mvaa 104,28,144
Mvaa 104,32,184
s_nop 0
Ta 24,v1,28672
Ta 26,v1,29184
Mvaa 112,28,148
Mvaa 112,32,188
buffer_load_dword a254, v12, s[24:27], s63 offen offset:896
Mvaa 120,28,152
Mvaa 120,32,192
Mvaa 128,28,156
Mvaa 128,32,196
W_ 4
Mvaa 132,224,196
s_nop 0
Ta 28,v1,24584
Ta 30,v1,25096
Mvaa 132,36,156
Mvaa 124,224,192
Mvaa 124,36,152
s_nop 0
Ta 32,v1,28680
Ta 34,v1,29192
Mvaa 116,224,188
Mvaa 116,36,148
Mvaa 108,224,184
Mvaa 108,36,144
Mvaa 100,224,180
s_waitcnt vmcnt(15) lgkmcnt(0)
s_barrier
Mvaa 100,36,140
S_ s65,s59,3145728
Da 60,v2,0
Mvaa 136,20,140
S_ m0,s52,24320
Ta 36,%0,0
Ta 38,%0,512
Mvaa 136,24,180
L_ v6,28,s65,256
Mvaa 144,20,144
S_ s65,s59,3244032
Da 100,v3,0
Mvaa 144,24,184
S_ m0,s52,25344
Ta 224,%0,4096
Ta 226,%0,4608
Mvaa 152,20,148
Mvaa 152,24,188
Da 64,v2,2048
Mvaa 160,20,152
L_ v6,28,s65,256
Mvaa 160,24,192
S_ s65,s59,3342336
Mvaa 168,20,156
S_ m0,s52,26368
Da 104,v3,2048
Mvaa 168,24,196
Mvaa 172,32,196
s_nop 0
Ta 20,%0,8
Ta 22,%0,520
Mvaa 172,28,156
Da 68,v2,4096
Mvaa 164,32,192
L_ v6,28,s65,256
Mvaa 164,28,152
S_ s65,s59,3440640
Ta 24,%0,4104
Ta 26,%0,4616
Mvaa 156,32,188
S_ m0,s52,27392
Da 108,v3,4096
Mvaa 156,28,148
Mvaa 148,32,184
L_ v6,28,s65,256
Mvaa 148,28,144
Da 72,v2,6144
Mvaa 140,32,180
Mvaa 140,28,140
W_ 9
Mvaa 176,36,140
s_nop 0
Ta 28,v1,0
Ta 30,v1,512
Mvaa 176,224,180
Da 112,v3,6144
Mvaa 184,36,144
Mvaa 184,224,184
s_nop 0
Ta 32,v1,4096
Ta 34,v1,4608
Mvaa 192,36,148
Da 76,v2,8192
Mvaa 192,224,188
Mvaa 200,36,152
Da 116,v3,8192
Mvaa 200,224,192
Mvaa 208,36,156
Mvaa 208,224,196
W_ 9
Mvaa 212,24,196
s_nop 0
Ta 36,v1,8
Ta 38,v1,520
Mvaa 212,20,156
Mvaa 204,24,192
Mvaa 204,20,152
s_nop 0
Ta 224,v1,4104
Ta 226,v1,4616
Mvaa 196,24,188
Mvaa 196,20,148
Mvaa 188,24,184
Mvaa 188,20,144
Mvaa 180,24,180
s_waitcnt vmcnt(15) lgkmcnt(0)
s_barrier
Mvaa 180,20,140
S_ s65,s59,3932160
Mvaa 216,28,140
S_ m0,s52,0
Ta 20,%0,8192
Ta 22,%0,8704
Mvaa 216,32,180
L_ v6,28,s65,0
Mvaa 224,28,144
S_ s65,s59,4030464
Mvaa 224,32,184
S_ m0,s52,1024
Ta 24,%0,12288
Ta 26,%0,12800
Mvaa 232,28,148
Mvaa 232,32,188
L_ v6,28,s65,0
Mvaa 240,28,152
S_ s65,s59,4128768
Mvaa 240,32,192
S_ m0,s52,2048
Mvaa 248,28,156
Mvaa 248,32,196
Mvaa 252,224,196
s_nop 0
Ta 28,%0,8200
Ta 30,%0,8712
Mvaa 252,36,156
L_ v6,28,s65,0
Mvaa 244,224,192
S_ s65,s59,4227072
Mvaa 244,36,152
S_ m0,s52,3072
Ta 32,%0,12296
Ta 34,%0,12808
Mvaa 236,224,188
Mvaa 236,36,148
L_ v6,28,s65,0
Mvaa 228,224,184
S_ s65,s60,768
Mvaa 228,36,144
S_ m0,s50,0
Mvaa 220,224,180
Mvaa 220,36,140
W_ 4
Mvaa 16,20,60
s_nop 0
Ta 36,v1,8192
Ta 38,v1,8704
Mvaa 16,24,100
L_ v4,24,s65,0
Mvaa 24,20,64
S_ s65,s60,11008
R_ v13,a248
Mvaa 24,24,104
S_ m0,s50,1024
Ta 224,v1,12288
Ta 226,v1,12800
Mvaa 32,20,68
v_min_u32 v13, 240, v13
Mvaa 32,24,108
L_ v5,24,s65,0
Mvaa 40,20,72
S_ s65,s60,21248
v_lshlrev_b32 v13, 3, v13
Mvaa 40,24,112
S_ m0,s50,2048
Mvaa 48,20,76
Mvaa 48,24,116
W_ 4
Mvaa 52,32,116
s_nop 0
Ta 20,v1,8200
Ta 22,v1,8712
Mvaa 52,28,76
L_ v4,24,s65,0
Mvaa 44,32,112
S_ s65,s60,31488
Mvaa 44,28,72
S_ m0,s50,3072
Ta 24,v1,12296
Ta 26,v1,12808
Mvaa 36,32,108
Mvaa 36,28,68
L_ v5,24,s65,0
Mvaa 28,32,104
S_ s65,s60,41728
Mvaa 28,28,64
S_ m0,s50,4096
Mvaa 20,32,100
s_waitcnt vmcnt(14) lgkmcnt(0)
s_barrier
Mvaa 20,28,60
L_ v4,24,s65,0
Mvaa 56,36,60
S_ s65,s59,3932160
Ta 28,%0,16384
Ta 30,%0,16896
Mvaa 56,224,100
S_ m0,s52,8064
Mvaa 64,36,64
Mvaa 64,224,104
s_nop 0
Ta 32,%0,20480
Ta 34,%0,20992
Mvaa 72,36,68
L_ v6,28,s65,128
Mvaa 72,224,108
S_ s65,s59,4030464
Mvaa 80,36,72
S_ m0,s52,9088
Mvaa 80,224,112
Mvaa 88,36,76
L_ v6,28,s65,128
Mvaa 88,224,116
S_ s65,s59,4128768
Mvaa 92,24,116
S_ m0,s52,10112
Ta 36,%0,16392
Ta 38,%0,16904
Mvaa 92,20,76
S_ s55,s56,6291456
Mvaa 84,24,112
L_ v6,28,s65,128
Mvaa 84,20,72
S_ s65,s59,4227072
Ta 224,%0,20488
Ta 226,%0,21000
Mvaa 76,24,108
S_ m0,s52,11136
Mvaa 76,20,68
Mvaa 68,24,104
L_ v6,28,s65,128
Mvaa 68,20,64
Mvaa 60,24,100
Mvaa 60,20,60
W_ 4
Mvaa 96,28,60
s_nop 0
Ta 20,v1,16384
Ta 22,v1,16896
Mvaa 96,32,100
buffer_load_dword a220, v13, s[44:47], s66 offen
Mvaa 104,28,64
Mvaa 104,32,104
s_nop 0
Ta 24,v1,20480
Ta 26,v1,20992
Mvaa 112,28,68
Mvaa 112,32,108
buffer_load_dword a254, v10, s[28:31], s55 offen
Mvaa 120,28,72
Mvaa 120,32,112
Mvaa 128,28,76
Mvaa 128,32,116
W_ 4
Mvaa 132,224,116
s_nop 0
Ta 28,v1,16392
Ta 30,v1,16904
Mvaa 132,36,76
buffer_load_dword a254, v12, s[24:27], s63 offen offset:1024
Mvaa 124,224,112
Mvaa 124,36,72
s_nop 0
Ta 32,v1,20488
Ta 34,v1,21000
Mvaa 116,224,108
Mvaa 116,36,68
Mvaa 108,224,104
Mvaa 108,36,64
Mvaa 100,224,100
s_waitcnt vmcnt(16) lgkmcnt(0)
s_barrier
Mvaa 100,36,60
S_ s65,s59,3932160
Da 140,v2,20480
Mvaa 136,20,60
S_ m0,s52,16128
Ta 36,%0,24576
Ta 38,%0,25088
Mvaa 136,24,100
L_ v6,28,s65,256
Mvaa 144,20,64
S_ s65,s59,4030464
Da 180,v3,20480
Mvaa 144,24,104
S_ m0,s52,17152
Ta 224,%0,28672
Ta 226,%0,29184
Mvaa 152,20,68
Mvaa 152,24,108
Da 144,v2,22528
Mvaa 160,20,72
L_ v6,28,s65,256
Mvaa 160,24,112
S_ s65,s59,4128768
Mvaa 168,20,76
S_ m0,s52,18176
Da 184,v3,22528
Mvaa 168,24,116
Mvaa 172,32,116
s_nop 0
Ta 20,%0,24584
Ta 22,%0,25096
Mvaa 172,28,76
Da 148,v2,24576
Mvaa 164,32,112
L_ v6,28,s65,256
Mvaa 164,28,72
S_ s65,s59,4227072
Ta 24,%0,28680
Ta 26,%0,29192
Mvaa 156,32,108
S_ m0,s52,19200
Da 188,v3,24576
Mvaa 156,28,68
Mvaa 148,32,104
L_ v6,28,s65,256
Mvaa 148,28,64
Da 152,v2,26624
Mvaa 140,32,100
Mvaa 140,28,60
W_ 9
Mvaa 176,36,60
s_nop 0
Ta 28,v1,24576
Ta 30,v1,25088
Mvaa 176,224,100
Da 192,v3,26624
Mvaa 184,36,64
Mvaa 184,224,104
s_nop 0
Ta 32,v1,28672
Ta 34,v1,29184
Mvaa 192,36,68
Da 156,v2,28672
Mvaa 192,224,108
Mvaa 200,36,72
Da 196,v3,28672
Mvaa 200,224,112
Mvaa 208,36,76
Mvaa 208,224,116
W_ 9
Mvaa 212,24,116
s_nop 0
Ta 36,v1,24584
Ta 38,v1,25096
Mvaa 212,20,76
Mvaa 204,24,112
Mvaa 204,20,72
s_nop 0
Ta 224,v1,28680
Ta 226,v1,29192
Mvaa 196,24,108
Mvaa 196,20,68
Mvaa 188,24,104
Mvaa 188,20,64
Mvaa 180,24,100
s_waitcnt vmcnt(16) lgkmcnt(0)
s_barrier
Mvaa 180,20,60
S_ s65,s59,4718592
Mvaa 216,28,60
S_ m0,s52,24576
Ta 20,%0,0
Ta 22,%0,512
Mvaa 216,32,100
L_ v6,28,s65,0
Mvaa 224,28,64
S_ s65,s59,4816896
Mvaa 224,32,104
S_ m0,s52,25600
Ta 24,%0,4096
Ta 26,%0,4608
Mvaa 232,28,68
Mvaa 232,32,108
L_ v6,28,s65,0
Mvaa 240,28,72
S_ s65,s59,4915200
Mvaa 240,32,112
S_ m0,s52,26624
Mvaa 248,28,76
Mvaa 248,32,116
Mvaa 252,224,116
s_nop 0
Ta 28,%0,8
Ta 30,%0,520
Mvaa 252,36,76
L_ v6,28,s65,0
Mvaa 244,224,112
S_ s65,s59,5013504
Mvaa 244,36,72
S_ m0,s52,27648
Ta 32,%0,4104
Ta 34,%0,4616
Mvaa 236,224,108
Mvaa 236,36,68
L_ v6,28,s65,0
Mvaa 228,224,104
S_ s65,s60,896
Mvaa 228,36,64
S_ m0,s50,20480
Mvaa 220,224,100
Mvaa 220,36,60
W_ 4
Mvaa 16,20,140
s_nop 0
Ta 36,v1,0
Ta 38,v1,512
Mvaa 16,24,180
L_ v4,24,s65,0
Mvaa 24,20,144
S_ s65,s60,11136
Mvaa 24,24,184
S_ m0,s50,21504
Ta 224,v1,4096
Ta 226,v1,4608
Mvaa 32,20,148
Mvaa 32,24,188
L_ v5,24,s65,0
Mvaa 40,20,152
S_ s65,s60,21376
Mvaa 40,24,192
S_ m0,s50,22528
Mvaa 48,20,156
Mvaa 48,24,196
W_ 4
Mvaa 52,32,196
s_nop 0
Ta 20,v1,8
Ta 22,v1,520
Mvaa 52,28,156
L_ v4,24,s65,0
Mvaa 44,32,192
S_ s65,s60,31616
Mvaa 44,28,152
S_ m0,s50,23552
Ta 24,v1,4104
Ta 26,v1,4616
Mvaa 36,32,188
Mvaa 36,28,148
L_ v5,24,s65,0
Mvaa 28,32,184
S_ s65,s60,41856
Mvaa 28,28,144
S_ m0,s50,24576
Mvaa 20,32,180
s_waitcnt vmcnt(15) lgkmcnt(0)
s_barrier
Mvaa 20,28,140
L_ v4,24,s65,0
Mvaa 56,36,140
S_ s65,s59,4718592
Ta 28,%0,8192
Ta 30,%0,8704
Mvaa 56,224,180
S_ m0,s52,-128
Mvaa 64,36,144
Mvaa 64,224,184
s_nop 0
Ta 32,%0,12288
Ta 34,%0,12800
Mvaa 72,36,148
L_ v6,28,s65,128
Mvaa 72,224,188
S_ s65,s59,4816896
Mvaa 80,36,152
S_ m0,s52,896
Mvaa 80,224,192
Mvaa 88,36,156
L_ v6,28,s65,128
Mvaa 88,224,196
S_ s65,s59,4915200
Mvaa 92,24,196
S_ m0,s52,1920
Ta 36,%0,8200
Ta 38,%0,8712
Mvaa 92,20,156
S_ s55,s56,7077888
Mvaa 84,24,192
L_ v6,28,s65,128
Mvaa 84,20,152
S_ s65,s59,5013504
Ta 224,%0,12296
Ta 226,%0,12808
Mvaa 76,24,188
S_ m0,s52,2944
Mvaa 76,20,148
Mvaa 68,24,184
L_ v6,28,s65,128
Mvaa 68,20,144
Mvaa 60,24,180
Mvaa 60,20,140
W_ 4
Mvaa 96,28,140
s_nop 0
Ta 20,v1,8192
Ta 22,v1,8704
Mvaa 96,32,180
buffer_load_dword a254, v10, s[28:31], s55 offen
Mvaa 104,28,144
Mvaa 104,32,184
s_nop 0
Ta 24,v1,12288
Ta 26,v1,12800
Mvaa 112,28,148
Mvaa 112,32,188
buffer_load_dword a254, v12, s[24:27], s63 offen offset:1152
Mvaa 120,28,152
Mvaa 120,32,192
Mvaa 128,28,156
Mvaa 128,32,196
W_ 4
Mvaa 132,224,196
s_nop 0
Ta 28,v1,8200
Ta 30,v1,8712
Mvaa 132,36,156
Mvaa 124,224,192
Mvaa 124,36,152
s_nop 0
Ta 32,v1,12296
Ta 34,v1,12808
Mvaa 116,224,188
Mvaa 116,36,148
Mvaa 108,224,184
Mvaa 108,36,144
Mvaa 100,224,180
s_waitcnt vmcnt(15) lgkmcnt(0)
s_barrier
Mvaa 100,36,140
S_ s65,s59,4718592
Da 60,v2,0
Mvaa 136,20,140
S_ m0,s52,7936
Ta 36,%0,16384
Ta 38,%0,16896
Mvaa 136,24,180
L_ v6,28,s65,256
Mvaa 144,20,144
S_ s65,s59,4816896
Da 100,v3,0
Mvaa 144,24,184
S_ m0,s52,8960
Ta 224,%0,20480
Ta 226,%0,20992
Mvaa 152,20,148
Mvaa 152,24,188
Da 64,v2,2048
Mvaa 160,20,152
L_ v6,28,s65,256
Mvaa 160,24,192
S_ s65,s59,4915200
Mvaa 168,20,156
S_ m0,s52,9984
Da 104,v3,2048
Mvaa 168,24,196
Mvaa 172,32,196
s_nop 0
Ta 20,%0,16392
Ta 22,%0,16904
Mvaa 172,28,156
Da 68,v2,4096
Mvaa 164,32,192
L_ v6,28,s65,256
Mvaa 164,28,152
S_ s65,s59,5013504
Ta 24,%0,20488
Ta 26,%0,21000
Mvaa 156,32,188
S_ m0,s52,11008
Da 108,v3,4096
Mvaa 156,28,148
Mvaa 148,32,184
L_ v6,28,s65,256
Mvaa 148,28,144
Da 72,v2,6144
Mvaa 140,32,180
Mvaa 140,28,140
W_ 9
Mvaa 176,36,140
s_nop 0
Ta 28,v1,16384
Ta 30,v1,16896
Mvaa 176,224,180
Da 112,v3,6144
Mvaa 184,36,144
Mvaa 184,224,184
s_nop 0
Ta 32,v1,20480
Ta 34,v1,20992
Mvaa 192,36,148
Da 76,v2,8192
Mvaa 192,224,188
Mvaa 200,36,152
Da 116,v3,8192
)A"
R"A(Mvaa 200,224,192
Mvaa 208,36,156
Mvaa 208,224,196
W_ 9
Mvaa 212,24,196
s_nop 0
Ta 36,v1,16392
Ta 38,v1,16904
Mvaa 212,20,156
Mvaa 204,24,192
Mvaa 204,20,152
s_nop 0
Ta 224,v1,20488
Ta 226,v1,21000
Mvaa 196,24,188
Mvaa 196,20,148
Mvaa 188,24,184
Mvaa 188,20,144
Mvaa 180,24,180
s_waitcnt vmcnt(15) lgkmcnt(0)
s_barrier
Mvaa 180,20,140
S_ s65,s59,5505024
Mvaa 216,28,140
S_ m0,s52,16384
Ta 20,%0,24576
Ta 22,%0,25088
Mvaa 216,32,180
L_ v6,28,s65,0
Mvaa 224,28,144
S_ s65,s59,5603328
Mvaa 224,32,184
S_ m0,s52,17408
Ta 24,%0,28672
Ta 26,%0,29184
Mvaa 232,28,148
Mvaa 232,32,188
L_ v6,28,s65,0
Mvaa 240,28,152
S_ s65,s59,5701632
Mvaa 240,32,192
S_ m0,s52,18432
Mvaa 248,28,156
Mvaa 248,32,196
Mvaa 252,224,196
s_nop 0
Ta 28,%0,24584
Ta 30,%0,25096
Mvaa 252,36,156
L_ v6,28,s65,0
Mvaa 244,224,192
S_ s65,s59,5799936
Mvaa 244,36,152
S_ m0,s52,19456
Ta 32,%0,28680
Ta 34,%0,29192
Mvaa 236,224,188
Mvaa 236,36,148
L_ v6,28,s65,0
Mvaa 228,224,184
S_ s65,s60,1024
Mvaa 228,36,144
S_ m0,s50,0
Mvaa 220,224,180
Mvaa 220,36,140
W_ 4
Mvaa 16,20,60
s_nop 0
Ta 36,v1,24576
Ta 38,v1,25088
Mvaa 16,24,100
L_ v4,24,s65,0
Mvaa 24,20,64
S_ s65,s60,11264
Mvaa 24,24,104
S_ m0,s50,1024
Ta 224,v1,28672
Ta 226,v1,29184
Mvaa 32,20,68
Mvaa 32,24,108
L_ v5,24,s65,0
Mvaa 40,20,72
S_ s65,s60,21504
Mvaa 40,24,112
S_ m0,s50,2048
Mvaa 48,20,76
Mvaa 48,24,116
W_ 4
Mvaa 52,32,116
s_nop 0
Ta 20,v1,24584
Ta 22,v1,25096
Mvaa 52,28,76
L_ v4,24,s65,0
Mvaa 44,32,112
S_ s65,s60,31744
Mvaa 44,28,72
S_ m0,s50,3072
Ta 24,v1,28680
Ta 26,v1,29192
Mvaa 36,32,108
Mvaa 36,28,68
L_ v5,24,s65,0
Mvaa 28,32,104
S_ s65,s60,41984
Mvaa 28,28,64
S_ m0,s50,4096
Mvaa 20,32,100
s_waitcnt vmcnt(14) lgkmcnt(0)
s_barrier
Mvaa 20,28,60
L_ v4,24,s65,0
Mvaa 56,36,60
S_ s65,s59,5505024
Ta 28,%0,0
Ta 30,%0,512
Mvaa 56,224,100
S_ m0,s52,24448
Mvaa 64,36,64
Mvaa 64,224,104
s_nop 0
Ta 32,%0,4096
Ta 34,%0,4608
Mvaa 72,36,68
L_ v6,28,s65,128
Mvaa 72,224,108
S_ s65,s59,5603328
Mvaa 80,36,72
S_ m0,s52,25472
Mvaa 80,224,112
Mvaa 88,36,76
L_ v6,28,s65,128
Mvaa 88,224,116
S_ s65,s59,5701632
Mvaa 92,24,116
S_ m0,s52,26496
Ta 36,%0,8
Ta 38,%0,520
Mvaa 92,20,76
Mvaa 84,24,112
L_ v6,28,s65,128
Mvaa 84,20,72
S_ s65,s59,5799936
Ta 224,%0,4104
Ta 226,%0,4616
Mvaa 76,24,108
S_ m0,s52,27520
Mvaa 76,20,68
Mvaa 68,24,104
L_ v6,28,s65,128
Mvaa 68,20,64
Mvaa 60,24,100
Mvaa 60,20,60
W_ 4
Mvaa 96,28,60
s_nop 0
Ta 20,v1,0
Ta 22,v1,512
Mvaa 96,32,100
Mvaa 104,28,64
Mvaa 104,32,104
s_nop 0
Ta 24,v1,4096
Ta 26,v1,4608
Mvaa 112,28,68
Mvaa 112,32,108
Mvaa 120,28,72
Mvaa 120,32,112
Mvaa 128,28,76
Mvaa 128,32,116
W_ 4
Mvaa 132,224,116
s_nop 0
Ta 28,v1,8
Ta 30,v1,520
Mvaa 132,36,76
Mvaa 124,224,112
Mvaa 124,36,72
s_nop 0
Ta 32,v1,4104
Ta 34,v1,4616
Mvaa 116,224,108
Mvaa 116,36,68
Mvaa 108,224,104
Mvaa 108,36,64
Mvaa 100,224,100
s_waitcnt vmcnt(13) lgkmcnt(0)
s_barrier
Mvaa 100,36,60
S_ s65,s59,5505024
Da 140,v2,20480
Mvaa 136,20,60
S_ m0,s52,-256
Ta 36,%0,8192
Ta 38,%0,8704
Mvaa 136,24,100
L_ v6,28,s65,256
Mvaa 144,20,64
S_ s65,s59,5603328
Da 180,v3,20480
Mvaa 144,24,104
S_ m0,s52,768
Ta 224,%0,12288
Ta 226,%0,12800
Mvaa 152,20,68
Mvaa 152,24,108
Da 144,v2,22528
Mvaa 160,20,72
L_ v6,28,s65,256
Mvaa 160,24,112
S_ s65,s59,5701632
Mvaa 168,20,76
S_ m0,s52,1792
Da 184,v3,22528
Mvaa 168,24,116
Mvaa 172,32,116
s_nop 0
Ta 20,%0,8200
Ta 22,%0,8712
Mvaa 172,28,76
Da 148,v2,24576
Mvaa 164,32,112
L_ v6,28,s65,256
Mvaa 164,28,72
S_ s65,s59,5799936
Ta 24,%0,12296
Ta 26,%0,12808
Mvaa 156,32,108
S_ m0,s52,2816
Da 188,v3,24576
Mvaa 156,28,68
Mvaa 148,32,104
L_ v6,28,s65,256
Mvaa 148,28,64
Da 152,v2,26624
Mvaa 140,32,100
Mvaa 140,28,60
W_ 9
Mvaa 176,36,60
s_nop 0
Ta 28,v1,8192
Ta 30,v1,8704
Mvaa 176,224,100
Da 192,v3,26624
Mvaa 184,36,64
Mvaa 184,224,104
s_nop 0
Ta 32,v1,12288
Ta 34,v1,12800
Mvaa 192,36,68
Da 156,v2,28672
Mvaa 192,224,108
Mvaa 200,36,72
Da 196,v3,28672
Mvaa 200,224,112
Mvaa 208,36,76
Mvaa 208,224,116
W_ 9
Mvaa 212,24,116
s_nop 0
Ta 36,v1,8200
Ta 38,v1,8712
Mvaa 212,20,76
Mvaa 204,24,112
Mvaa 204,20,72
s_nop 0
Ta 224,v1,12296
Ta 226,v1,12808
Mvaa 196,24,108
Mvaa 196,20,68
Mvaa 188,24,104
Mvaa 188,20,64
Mvaa 180,24,100
s_waitcnt vmcnt(13) lgkmcnt(0)
s_barrier
Mvaa 180,20,60
S_ s65,s59,6291456
Mvaa 216,28,60
S_ m0,s52,7936
Ta 20,%0,16384
Ta 22,%0,16896
Mvaa 216,32,100
L_ v6,28,s65,256
Mvaa 224,28,64
S_ s65,s59,6389760
Mvaa 224,32,104
S_ m0,s52,8960
Ta 24,%0,20480
Ta 26,%0,20992
Mvaa 232,28,68
Mvaa 232,32,108
L_ v6,28,s65,256
Mvaa 240,28,72
S_ s65,s59,6488064
Mvaa 240,32,112
S_ m0,s52,9984
Mvaa 248,28,76
Mvaa 248,32,116
Mvaa 252,224,116
s_nop 0
Ta 28,%0,16392
Ta 30,%0,16904
Mvaa 252,36,76
L_ v6,28,s65,256
Mvaa 244,224,112
S_ s65,s59,6586368
Mvaa 244,36,72
S_ m0,s52,11008
Ta 32,%0,20488
Ta 34,%0,21000
Mvaa 236,224,108
Mvaa 236,36,68
L_ v6,28,s65,256
Mvaa 228,224,104
S_ s65,s60,1152
Mvaa 228,36,64
S_ m0,s50,20480
Mvaa 220,224,100
Mvaa 220,36,60
W_ 4
Mvaa 16,20,140
s_nop 0
Ta 36,v1,16384
Ta 38,v1,16896
Mvaa 16,24,180
L_ v4,24,s65,0
Mvaa 24,20,144
S_ s65,s60,11392
Mvaa 24,24,184
S_ m0,s50,21504
Ta 224,v1,20480
Ta 226,v1,20992
Mvaa 32,20,148
Mvaa 32,24,188
L_ v5,24,s65,0
Mvaa 40,20,152
S_ s65,s60,21632
Mvaa 40,24,192
S_ m0,s50,22528
Mvaa 48,20,156
Mvaa 48,24,196
W_ 4
Mvaa 52,32,196
s_nop 0
Ta 20,v1,16392
Ta 22,v1,16904
Mvaa 52,28,156
L_ v4,24,s65,0
Mvaa 44,32,192
S_ s65,s60,31872
Mvaa 44,28,152
S_ m0,s50,23552
Ta 24,v1,20488
Ta 26,v1,21000
Mvaa 36,32,188
Mvaa 36,28,148
L_ v5,24,s65,0
Mvaa 28,32,184
S_ s65,s60,42112
Mvaa 28,28,144
S_ m0,s50,24576
Mvaa 20,32,180
s_waitcnt vmcnt(12) lgkmcnt(0)
s_barrier
Mvaa 20,28,140
L_ v4,24,s65,0
Mvaa 56,36,140
S_ s65,s59,7077888
Ta 28,%0,24576
Ta 30,%0,25088
Mvaa 56,224,180
S_ m0,s52,16128
Mvaa 64,36,144
Mvaa 64,224,184
s_nop 0
Ta 32,%0,28672
Ta 34,%0,29184
Mvaa 72,36,148
L_ v6,28,s65,256
Mvaa 72,224,188
S_ s65,s59,7176192
Mvaa 80,36,152
S_ m0,s52,17152
Mvaa 80,224,192
Mvaa 88,36,156
L_ v6,28,s65,256
Mvaa 88,224,196
S_ s65,s59,7274496
Mvaa 92,24,196
S_ m0,s52,18176
Ta 36,%0,24584
Ta 38,%0,25096
Mvaa 92,20,156
Mvaa 84,24,192
L_ v6,28,s65,256
Mvaa 84,20,152
S_ s65,s59,7372800
Ta 224,%0,28680
Ta 226,%0,29192
Mvaa 76,24,188
S_ m0,s52,19200
Mvaa 76,20,148
Mvaa 68,24,184
L_ v6,28,s65,256
Mvaa 68,20,144
Mvaa 60,24,180
Mvaa 60,20,140
W_ 4
Mvaa 96,28,140
s_nop 0
Ta 20,v1,24576
Ta 22,v1,25088
Mvaa 96,32,180
Mvaa 104,28,144
Mvaa 104,32,184
s_nop 0
Ta 24,v1,28672
Ta 26,v1,29184
Mvaa 112,28,148
Mvaa 112,32,188
Mvaa 120,28,152
Mvaa 120,32,192
Mvaa 128,28,156
Mvaa 128,32,196
W_ 4
Mvaa 132,224,196
s_nop 0
Ta 28,v1,24584
Ta 30,v1,25096
Mvaa 132,36,156
Mvaa 124,224,192
Mvaa 124,36,152
s_nop 0
Ta 32,v1,28680
Ta 34,v1,29192
Mvaa 116,224,188
Mvaa 116,36,148
Mvaa 108,224,184
Mvaa 108,36,144
Mvaa 100,224,180
s_waitcnt vmcnt(13) lgkmcnt(0)
s_barrier
Mvaa 100,36,140
S_ s65,s59,6291456
Da 60,v2,0
Mvaa 136,20,140
S_ m0,s52,24448
Ta 36,%0,0
Ta 38,%0,512
Mvaa 136,24,180
L_ v6,28,s65,128
Mvaa 144,20,144
S_ s65,s59,6389760
Da 100,v3,0
Mvaa 144,24,184
S_ m0,s52,25472
Ta 224,%0,4096
Ta 226,%0,4608
Mvaa 152,20,148
Mvaa 152,24,188
Da 64,v2,2048
Mvaa 160,20,152
L_ v6,28,s65,128
Mvaa 160,24,192
S_ s65,s59,6488064
Mvaa 168,20,156
S_ m0,s52,26496
Da 104,v3,2048
Mvaa 168,24,196
Mvaa 172,32,196
s_nop 0
Ta 20,%0,8
Ta 22,%0,520
Mvaa 172,28,156
Da 68,v2,4096
Mvaa 164,32,192
L_ v6,28,s65,128
Mvaa 164,28,152
S_ s65,s59,6586368
Ta 24,%0,4104
Ta 26,%0,4616
Mvaa 156,32,188
S_ m0,s52,27520
Da 108,v3,4096
Mvaa 156,28,148
Mvaa 148,32,184
L_ v6,28,s65,128
Mvaa 148,28,144
Da 72,v2,6144
Mvaa 140,32,180
Mvaa 140,28,140
W_ 9
Mvaa 176,36,140
s_nop 0
Ta 28,v1,0
Ta 30,v1,512
Mvaa 176,224,180
Da 112,v3,6144
Mvaa 184,36,144
Mvaa 184,224,184
s_nop 0
Ta 32,v1,4096
Ta 34,v1,4608
Mvaa 192,36,148
Da 76,v2,8192
Mvaa 192,224,188
Mvaa 200,36,152
Da 116,v3,8192
Mvaa 200,224,192
Mvaa 208,36,156
Mvaa 208,224,196
W_ 9
Mvaa 212,24,196
s_nop 0
Ta 36,v1,8
Ta 38,v1,520
Mvaa 212,20,156
Mvaa 204,24,192
Mvaa 204,20,152
s_nop 0
Ta 224,v1,4104
Ta 226,v1,4616
Mvaa 196,24,188
Mvaa 196,20,148
Mvaa 188,24,184
Mvaa 188,20,144
Mvaa 180,24,180
s_waitcnt vmcnt(8) lgkmcnt(0)
s_barrier
Mvaa 180,20,140
S_ s65,s59,7077888
Mvaa 216,28,140
S_ m0,s52,-128
Ta 20,v1,8200
Ta 22,v1,8712
Mvaa 216,32,180
L_ v6,28,s65,128
Mvaa 224,28,144
S_ s65,s59,7176192
Mvaa 224,32,184
S_ m0,s52,896
Ta 24,v1,12296
Ta 26,v1,12808
Mvaa 232,28,148
Mvaa 232,32,188
L_ v6,28,s65,128
Mvaa 240,28,152
S_ s65,s59,7274496
Mvaa 240,32,192
S_ m0,s52,1920
Mvaa 248,28,156
Mvaa 248,32,196
Mvaa 252,224,196
s_nop 0
Ta 28,v1,8192
Ta 30,v1,8704
Mvaa 252,36,156
L_ v6,28,s65,128
Mvaa 244,224,192
S_ s65,s59,7372800
Mvaa 244,36,152
S_ m0,s52,2944
Ta 32,v1,12288
Ta 34,v1,12800
Mvaa 236,224,188
Mvaa 236,36,148
L_ v6,28,s65,128
Mvaa 228,224,184
S_ s65,s60,0
Mvaa 228,36,144
S_ m0,s50,0
Mvaa 220,224,180
Mvaa 220,36,140
W_ 4
Mvaa 220,20,60
s_nop 0
Ta 36,%0,8200
Ta 38,%0,8712
Mvaa 220,24,100
Da 140,v2,20480
Mvaa 228,20,64
L_ v4,24,s65,0
Mvaa 228,24,104
S_ s65,s60,10240
Ta 224,%0,12296
Ta 226,%0,12808
Mvaa 236,20,68
S_ m0,s50,1024
Da 180,v3,20480
Mvaa 236,24,108
Mvaa 244,20,72
Da 144,v2,22528
Mvaa 244,24,112
L_ v5,24,s65,0
Mvaa 252,20,76
S_ s65,s60,20480
Mvaa 252,24,116
W_ 7
Da 184,v3,22528
S_ m0,s50,2048
Mvaa 248,32,116
s_nop 0
Ta 20,%0,8192
Ta 22,%0,8704
Mvaa 248,28,76
L_ v4,24,s65,0
Mvaa 240,32,112
S_ s65,s60,30720
Da 148,v2,24576
Mvaa 240,28,72
S_ m0,s50,3072
Ta 24,%0,12288
Ta 26,%0,12800
Mvaa 232,32,108
Mvaa 232,28,68
Da 188,v3,24576
Mvaa 224,32,104
L_ v5,24,s65,0
Mvaa 224,28,64
S_ s65,s60,40960
Mvaa 216,32,100
s_waitcnt vmcnt(12) lgkmcnt(0)
s_barrier
Da 152,v2,26624
Mvaa 216,28,60
S_ m0,s50,4096
Mvaa 180,36,60
s_nop 0
Ta 28,v1,16392
Ta 30,v1,16904
Mvaa 180,224,100
Da 192,v3,26624
Mvaa 188,36,64
L_ v4,24,s65,0
Mvaa 188,224,104
S_ s65,s59,6291456
Ta 32,v1,20488
Ta 34,v1,21000
Mvaa 196,36,68
S_ m0,s52,8192
Da 156,v2,28672
Mvaa 196,224,108
Mvaa 204,36,72
L_ v6,28,s65,0
Mvaa 204,224,112
S_ s65,s59,6389760
Da 196,v3,28672
Mvaa 212,36,76
S_ m0,s52,9216
Mvaa 212,224,116
Mvaa 208,24,116
s_nop 0
Ta 36,v1,16384
Ta 38,v1,16896
Mvaa 208,20,76
L_ v6,28,s65,0
Mvaa 200,24,112
S_ s65,s59,6488064
Mvaa 200,20,72
S_ m0,s52,10240
Ta 224,v1,20480
Ta 226,v1,20992
Mvaa 192,24,108
Mvaa 192,20,68
L_ v6,28,s65,0
Mvaa 184,24,104
S_ s65,s59,6586368
Mvaa 184,20,64
S_ m0,s52,11264
Mvaa 176,24,100
Mvaa 176,20,60
W_ 6
Mvaa 220,28,140
s_nop 0
Ta 20,%0,16392
Ta 22,%0,16904
Mvaa 220,32,180
L_ v6,28,s65,0
Mvaa 228,28,144
Mvaa 228,32,184
s_nop 0
Ta 24,%0,20488
Ta 26,%0,21000
Mvaa 236,28,148
Mvaa 236,32,188
Mvaa 244,28,152
Mvaa 244,32,192
W_ 9
Mvaa 252,28,156
W_ 8
Mvaa 252,32,196
W_ 4
Mvaa 248,224,196
s_nop 0
Ta 28,%0,16384
Ta 30,%0,16896
Mvaa 248,36,156
Mvaa 240,224,192
Mvaa 240,36,152
s_nop 0
Ta 32,%0,20480
Ta 34,%0,20992
Mvaa 232,224,188
Mvaa 232,36,148
Mvaa 224,224,184
Mvaa 224,36,144
Mvaa 216,224,180
s_waitcnt vmcnt(13) lgkmcnt(0)
s_barrier
Mvaa 216,36,140
S_ s65,s59,7077888
Mvaa 180,20,140
S_ m0,s52,16384
Ta 36,v1,24584
Ta 38,v1,25096
Mvaa 180,24,180
L_ v6,28,s65,0
Mvaa 188,20,144
S_ s65,s59,7176192
Mvaa 188,24,184
S_ m0,s52,17408
Ta 224,v1,28680
Ta 226,v1,29192
Mvaa 196,20,148
U_ v248,s48,v248
U_ v249,s48,v249
Mvaa 196,24,188
L_ v6,28,s65,0
Mvaa 204,20,152
S_ s65,s59,7274496
U_ v250,s48,v250
U_ v251,s48,v251
Mvaa 204,24,192
S_ m0,s52,18432
U_ v252,s48,v252
U_ v253,s48,v253
Mvaa 212,20,156
U_ v254,s48,v254
U_ v255,s48,v255
Mvaa 212,24,196
C_ 255,254,255
C_ 254,252,253
Mvaa 208,32,196
s_nop 0
Ta 20,v1,24576
Ta 22,v1,25088
Mvaa 208,28,156
L_ v6,28,s65,0
Mvaa 200,32,192
S_ s65,s59,7372800
C_ 253,250,251
C_ 252,248,249
Mvaa 200,28,152
S_ m0,s52,19456
Ta 24,v1,28672
Ta 26,v1,29184
Mvaa 192,32,188
X_ a216,v252
X_ a217,v253
Mvaa 192,28,148
L_ v6,28,s65,0
Mvaa 184,32,184
X_ a218,v254
X_ a219,v255
Mvaa 184,28,144
U_ v240,s48,v240
U_ v241,s48,v241
Mvaa 176,32,180
U_ v242,s48,v242
U_ v243,s48,v243
Mvaa 176,28,140
W_ 4
U_ v244,s48,v244
U_ v245,s48,v245
Mvaa 140,36,60
s_nop 0
Ta 28,%0,24584
Ta 30,%0,25096
Mvaa 140,224,100
U_ v246,s48,v246
U_ v247,s48,v247
Mvaa 148,36,64
C_ 251,246,247
C_ 250,244,245
Mvaa 148,224,104
s_nop 0
Ta 32,%0,28680
Ta 34,%0,29192
Mvaa 156,36,68
C_ 249,242,243
C_ 248,240,241
Mvaa 156,224,108
X_ a212,v248
X_ a213,v249
Mvaa 164,36,72
X_ a214,v250
X_ a215,v251
Mvaa 164,224,112
U_ v232,s48,v232
U_ v233,s48,v233
Mvaa 172,36,76
U_ v234,s48,v234
U_ v235,s48,v235
Mvaa 172,224,116
W_ 4
U_ v236,s48,v236
U_ v237,s48,v237
Mvaa 168,24,116
s_nop 0
Ta 36,%0,24576
Ta 38,%0,25088
Mvaa 168,20,76
U_ v238,s48,v238
U_ v239,s48,v239
Mvaa 160,24,112
C_ 247,238,239
C_ 246,236,237
Mvaa 160,20,72
s_nop 0
Ta 224,%0,28672
Ta 226,%0,29184
Mvaa 152,24,108
C_ 245,234,235
C_ 244,232,233
Mvaa 152,20,68
X_ a208,v244
X_ a209,v245
Mvaa 144,24,104
X_ a210,v246
X_ a211,v247
Mvaa 144,20,64
U_ v224,s48,v224
U_ v225,s48,v225
Mvaa 136,24,100
s_waitcnt vmcnt(13) lgkmcnt(0)
s_barrier
U_ v226,s48,v226
Mvaa 136,20,60
S_ s65,s59,-768
U_ v227,s48,v227
Mvaa 100,28,60
S_ m0,s52,24320
Ta 20,v1,8
Ta 22,v1,520
Mvaa 100,32,100
L_ v6,28,s65,256
Mvaa 108,28,64
S_ s65,s59,97536
U_ v228,s48,v228
U_ v229,s48,v229
Mvaa 108,32,104
S_ m0,s52,25344
Ta 24,v1,4104
Ta 26,v1,4616
Mvaa 116,28,68
U_ v230,s48,v230
U_ v231,s48,v231
Mvaa 116,32,108
L_ v6,28,s65,256
Mvaa 124,28,72
S_ s65,s59,195840
C_ 243,230,231
C_ 242,228,229
Mvaa 124,32,112
S_ m0,s52,26368
C_ 241,226,227
C_ 240,224,225
Mvaa 132,28,76
X_ a204,v240
X_ a205,v241
Mvaa 132,32,116
X_ a206,v242
X_ a207,v243
Mvaa 128,224,116
s_nop 0
Ta 28,v1,0
Ta 30,v1,512
Mvaa 128,36,76
L_ v6,28,s65,256
Mvaa 120,224,112
S_ s65,s59,294144
U_ v216,s48,v216
U_ v217,s48,v217
Mvaa 120,36,72
S_ m0,s52,27392
Ta 32,v1,4096
Ta 34,v1,4608
Mvaa 112,224,108
U_ v218,s48,v218
U_ v219,s48,v219
Mvaa 112,36,68
L_ v6,28,s65,256
Mvaa 104,224,104
S_ s65,s60,128
U_ v220,s48,v220
U_ v221,s48,v221
Mvaa 104,36,64
S_ m0,s50,20480
U_ v222,s48,v222
U_ v223,s48,v223
Mvaa 96,224,100
C_ 239,222,223
C_ 238,220,221
Mvaa 96,36,60
W_ 4
C_ 237,218,219
C_ 236,216,217
Mvaa 140,20,140
s_nop 0
Ta 36,%0,8
Ta 38,%0,520
Mvaa 140,24,180
L_ v4,24,s65,0
Mvaa 148,20,144
S_ s65,s60,10368
X_ a200,v236
X_ a201,v237
Mvaa 148,24,184
S_ m0,s50,21504
Ta 224,%0,4104
Ta 226,%0,4616
Mvaa 156,20,148
X_ a202,v238
X_ a203,v239
Mvaa 156,24,188
L_ v5,24,s65,0
Mvaa 164,20,152
S_ s65,s60,20608
U_ v208,s48,v208
U_ v209,s48,v209
Mvaa 164,24,192
S_ m0,s50,22528
U_ v210,s48,v210
U_ v211,s48,v211
Mvaa 172,20,156
U_ v212,s48,v212
U_ v213,s48,v213
Mvaa 172,24,196
W_ 4
U_ v214,s48,v214
U_ v215,s48,v215
Mvaa 168,32,196
s_nop 0
Ta 20,%0,0
Ta 22,%0,512
Mvaa 168,28,156
L_ v4,24,s65,0
Mvaa 160,32,192
S_ s65,s60,30848
C_ 235,214,215
C_ 234,212,213
Mvaa 160,28,152
S_ m0,s50,23552
Ta 24,%0,4096
Ta 26,%0,4608
Mvaa 152,32,188
C_ 233,210,211
C_ 232,208,209
Mvaa 152,28,148
L_ v5,24,s65,0
Mvaa 144,32,184
S_ s65,s60,41088
X_ a176,v232
X_ a177,v233
Mvaa 144,28,144
S_ m0,s50,24576
X_ a178,v234
X_ a179,v235
Mvaa 136,32,180
s_waitcnt vmcnt(12) lgkmcnt(0)
s_barrier
U_ v200,s48,v200
Mvaa 136,28,140
L_ v4,24,s65,0
Mvaa 100,36,140
S_ s65,s59,785664
Ta 28,v1,8200
Ta 30,v1,8712
Mvaa 100,224,180
S_ m0,s52,0
U_ v201,s48,v201
Mvaa 108,36,144
U_ v202,s48,v202
U_ v203,s48,v203
Mvaa 108,224,184
s_nop 0
Ta 32,v1,12296
Ta 34,v1,12808
Mvaa 116,36,148
L_ v6,28,s65,0
Mvaa 116,224,188
S_ s65,s59,883968
U_ v204,s48,v204
U_ v205,s48,v205
Mvaa 124,36,152
S_ m0,s52,1024
U_ v206,s48,v206
U_ v207,s48,v207
Mvaa 124,224,192
C_ 231,206,207
C_ 230,204,205
Mvaa 132,36,156
L_ v6,28,s65,0
Mvaa 132,224,196
C_ 229,202,203
S_ s65,s59,982272
Mvaa 128,24,196
S_ m0,s52,2048
Ta 36,v1,8192
Ta 38,v1,8704
Mvaa 128,20,156
C_ 228,200,201
Mvaa 120,24,192
L_ v6,28,s65,0
Mvaa 120,20,152
S_ s65,s59,1080576
Ta 224,v1,12288
Ta 226,v1,12800
Mvaa 112,24,188
S_ m0,s52,3072
X_ a172,v228
X_ a173,v229
Mvaa 112,20,148
X_ a174,v230
X_ a175,v231
Mvaa 104,24,184
L_ v6,28,s65,0
Mvaa 104,20,144
U_ v192,s48,v192
U_ v193,s48,v193
Mvaa 96,24,180
U_ v194,s48,v194
U_ v195,s48,v195
Mvaa 96,20,140
W_ 4
U_ v196,s48,v196
U_ v197,s48,v197
Mvaa 60,28,60
s_nop 0
Ta 20,%0,8200
Ta 22,%0,8712
Mvaa 60,32,100
U_ v198,s48,v198
U_ v199,s48,v199
Mvaa 68,28,64
C_ 227,198,199
C_ 226,196,197
Mvaa 68,32,104
s_nop 0
Ta 24,%0,12296
Ta 26,%0,12808
Mvaa 76,28,68
C_ 225,194,195
C_ 224,192,193
Mvaa 76,32,108
X_ a168,v224
X_ a169,v225
Mvaa 84,28,72
X_ a170,v226
X_ a171,v227
Mvaa 84,32,112
U_ v184,s48,v184
U_ v185,s48,v185
Mvaa 92,28,76
U_ v186,s48,v186
U_ v187,s48,v187
Mvaa 92,32,116
W_ 4
U_ v188,s48,v188
U_ v189,s48,v189
Mvaa 88,224,116
s_nop 0
Ta 28,%0,8192
Ta 30,%0,8704
Mvaa 88,36,76
U_ v190,s48,v190
U_ v191,s48,v191
Mvaa 80,224,112
C_ 223,190,191
C_ 222,188,189
Mvaa 80,36,72
s_nop 0
Ta 32,%0,12288
Ta 34,%0,12800
Mvaa 72,224,108
C_ 221,186,187
C_ 220,184,185
Mvaa 72,36,68
X_ a164,v220
X_ a165,v221
Mvaa 64,224,104
X_ a166,v222
X_ a167,v223
Mvaa 64,36,64
U_ v176,s48,v176
U_ v177,s48,v177
Mvaa 56,224,100
s_waitcnt vmcnt(13) lgkmcnt(0)
s_barrier
U_ v178,s48,v178
Mvaa 56,36,60
S_ s65,s59,-768
U_ v179,s48,v179
Mvaa 20,20,60
S_ m0,s52,8192
Ta 36,v1,16392
Ta 38,v1,16904
Mvaa 20,24,100
L_ v6,28,s65,0
Mvaa 28,20,64
S_ s65,s59,97536
U_ v180,s48,v180
U_ v181,s48,v181
Mvaa 28,24,104
S_ m0,s52,9216
Ta 224,v1,20488
Ta 226,v1,21000
Mvaa 36,20,68
U_ v182,s48,v182
U_ v183,s48,v183
Mvaa 36,24,108
L_ v6,28,s65,0
Mvaa 44,20,72
S_ s65,s59,195840
C_ 219,182,183
C_ 218,180,181
Mvaa 44,24,112
S_ m0,s52,10240
C_ 217,178,179
C_ 216,176,177
Mvaa 52,20,76
X_ a160,v216
X_ a161,v217
Mvaa 52,24,116
X_ a162,v218
X_ a163,v219
Mvaa 48,32,116
s_nop 0
Ta 20,v1,16384
Ta 22,v1,16896
Mvaa 48,28,76
L_ v6,28,s65,0
Mvaa 40,32,112
S_ s65,s59,294144
U_ v168,s48,v168
U_ v169,s48,v169
Mvaa 40,28,72
S_ m0,s52,11264
Ta 24,v1,20480
Ta 26,v1,20992
Mvaa 32,32,108
U_ v170,s48,v170
U_ v171,s48,v171
Mvaa 32,28,68
L_ v6,28,s65,0
Mvaa 24,32,104
U_ v172,s48,v172
U_ v173,s48,v173
Mvaa 24,28,64
U_ v174,s48,v174
U_ v175,s48,v175
Mvaa 16,32,100
C_ 215,174,175
C_ 214,172,173
Mvaa 16,28,60
W_ 4
C_ 213,170,171
C_ 212,168,169
Mvaa 60,36,140
s_nop 0
Ta 28,%0,16392
Ta 30,%0,16904
Mvaa 60,224,180
X_ a136,v212
X_ a137,v213
Mvaa 68,36,144
X_ a138,v214
X_ a139,v215
Mvaa 68,224,184
s_nop 0
Ta 32,%0,20488
Ta 34,%0,21000
Mvaa 76,36,148
U_ v160,s48,v160
U_ v161,s48,v161
Mvaa 76,224,188
U_ v162,s48,v162
U_ v163,s48,v163
Mvaa 84,36,152
U_ v164,s48,v164
U_ v165,s48,v165
Mvaa 84,224,192
U_ v166,s48,v166
U_ v167,s48,v167
Mvaa 92,36,156
C_ 211,166,167
C_ 210,164,165
Mvaa 92,224,196
W_ 4
C_ 209,162,163
C_ 208,160,161
Mvaa 88,24,196
s_nop 0
Ta 36,%0,16384
Ta 38,%0,16896
Mvaa 88,20,156
X_ a132,v208
X_ a133,v209
Mvaa 80,24,192
X_ a134,v210
X_ a135,v211
Mvaa 80,20,152
s_nop 0
Ta 224,%0,20480
Ta 226,%0,20992
Mvaa 72,24,188
U_ v152,s48,v152
U_ v153,s48,v153
Mvaa 72,20,148
U_ v154,s48,v154
U_ v155,s48,v155
Mvaa 64,24,184
U_ v156,s48,v156
U_ v157,s48,v157
Mvaa 64,20,144
U_ v158,s48,v158
U_ v159,s48,v159
Mvaa 56,24,180
s_waitcnt vmcnt(63) lgkmcnt(0)
s_barrier
C_ 207,158,159
Mvaa 56,20,140
S_ s65,s59,-768
C_ 206,156,157
Mvaa 20,28,140
S_ m0,s52,16256
C_ 205,154,155
C_ 204,152,153
Mvaa 20,32,180
L_ v6,28,s65,128
Mvaa 28,28,144
S_ s65,s59,97536
X_ a128,v204
X_ a129,v205
Mvaa 28,32,184
S_ m0,s52,17280
X_ a130,v206
X_ a131,v207
Mvaa 36,28,148
U_ v144,s48,v144
U_ v145,s48,v145
Mvaa 36,32,188
L_ v6,28,s65,128
Mvaa 44,28,152
S_ s65,s59,195840
U_ v146,s48,v146
U_ v147,s48,v147
Mvaa 44,32,192
S_ m0,s52,18304
U_ v148,s48,v148
U_ v149,s48,v149
Mvaa 52,28,156
U_ v150,s48,v150
U_ v151,s48,v151
Mvaa 52,32,196
C_ 203,150,151
C_ 202,148,149
Mvaa 48,224,196
L_ v6,28,s65,128
Mvaa 48,36,156
S_ s65,s59,294144
C_ 201,146,147
C_ 200,144,145
Mvaa 40,224,192
S_ m0,s52,19328
X_ a124,v200
X_ a125,v201
Mvaa 40,36,152
X_ a126,v202
X_ a127,v203
Mvaa 32,224,188
L_ v6,28,s65,128
Mvaa 32,36,148
U_ v136,s48,v136
U_ v137,s48,v137
Mvaa 24,224,184
U_ v138,s48,v138
U_ v139,s48,v139
Mvaa 24,36,144
U_ v140,s48,v140
U_ v141,s48,v141
Mvaa 16,224,180
U_ v142,s48,v142
U_ v143,s48,v143
Mvaa 16,36,140
C_ 199,142,143
C_ 198,140,141
C_ 197,138,139
C_ 196,136,137
X_ a120,v196
X_ a121,v197
X_ a122,v198
X_ a123,v199
U_ v128,s48,v128
U_ v129,s48,v129
U_ v130,s48,v130
U_ v131,s48,v131
U_ v132,s48,v132
U_ v133,s48,v133
U_ v134,s48,v134
U_ v135,s48,v135
C_ 195,134,135
C_ 194,132,133
C_ 193,130,131
C_ 192,128,129
X_ a96,v192
X_ a97,v193
X_ a98,v194
X_ a99,v195
U_ v120,s48,v120
U_ v121,s48,v121
U_ v122,s48,v122
U_ v123,s48,v123
U_ v124,s48,v124
U_ v125,s48,v125
U_ v126,s48,v126
U_ v127,s48,v127
C_ 191,126,127
C_ 190,124,125
C_ 189,122,123
C_ 188,120,121
X_ a92,v188
X_ a93,v189
X_ a94,v190
X_ a95,v191
U_ v112,s48,v112
U_ v113,s48,v113
U_ v114,s48,v114
U_ v115,s48,v115
U_ v116,s48,v116
U_ v117,s48,v117
U_ v118,s48,v118
U_ v119,s48,v119
C_ 187,118,119
C_ 186,116,117
C_ 185,114,115
C_ 184,112,113
X_ a88,v184
X_ a89,v185
X_ a90,v186
X_ a91,v187
U_ v104,s48,v104
U_ v105,s48,v105
U_ v106,s48,v106
U_ v107,s48,v107
U_ v108,s48,v108
U_ v109,s48,v109
U_ v110,s48,v110
U_ v111,s48,v111
C_ 183,110,111
C_ 182,108,109
C_ 181,106,107
C_ 180,104,105
X_ a84,v180
X_ a85,v181
X_ a86,v182
X_ a87,v183
U_ v96,s48,v96
U_ v97,s48,v97
U_ v98,s48,v98
U_ v99,s48,v99
U_ v100,s48,v100
U_ v101,s48,v101
U_ v102,s48,v102
U_ v103,s48,v103
C_ 179,102,103
C_ 178,100,101
C_ 177,98,99
C_ 176,96,97
X_ a80,v176
X_ a81,v177
X_ a82,v178
X_ a83,v179
v_pk_mul_f32 v[88:89], v[88:89], s[48:49]
v_pk_mul_f32 v[90:91], v[90:91], s[48:49]
v_pk_mul_f32 v[92:93], v[92:93], s[48:49]
v_pk_mul_f32 v[94:95], v[94:95], s[48:49]
C_ 175,94,95
C_ 174,92,93
C_ 173,90,91
C_ 172,88,89
X_ a56,v172
X_ a57,v173
X_ a58,v174
X_ a59,v175
v_pk_mul_f32 v[80:81], v[80:81], s[48:49]
v_pk_mul_f32 v[82:83], v[82:83], s[48:49]
v_pk_mul_f32 v[84:85], v[84:85], s[48:49]
v_pk_mul_f32 v[86:87], v[86:87], s[48:49]
C_ 171,86,87
C_ 170,84,85
C_ 169,82,83
C_ 168,80,81
X_ a52,v168
X_ a53,v169
X_ a54,v170
X_ a55,v171
v_pk_mul_f32 v[72:73], v[72:73], s[48:49]
v_pk_mul_f32 v[74:75], v[74:75], s[48:49]
v_pk_mul_f32 v[76:77], v[76:77], s[48:49]
v_pk_mul_f32 v[78:79], v[78:79], s[48:49]
C_ 167,78,79
C_ 166,76,77
C_ 165,74,75
C_ 164,72,73
X_ a48,v164
X_ a49,v165
X_ a50,v166
X_ a51,v167
v_pk_mul_f32 v[64:65], v[64:65], s[48:49]
v_pk_mul_f32 v[66:67], v[66:67], s[48:49]
v_pk_mul_f32 v[68:69], v[68:69], s[48:49]
v_pk_mul_f32 v[70:71], v[70:71], s[48:49]
C_ 163,70,71
C_ 162,68,69
C_ 161,66,67
C_ 160,64,65
X_ a44,v160
X_ a45,v161
X_ a46,v162
X_ a47,v163
v_pk_mul_f32 v[56:57], v[56:57], s[48:49]
v_pk_mul_f32 v[58:59], v[58:59], s[48:49]
v_pk_mul_f32 v[60:61], v[60:61], s[48:49]
v_pk_mul_f32 v[62:63], v[62:63], s[48:49]
C_ 159,62,63
C_ 158,60,61
C_ 157,58,59
C_ 156,56,57
X_ a40,v156
X_ a41,v157
X_ a42,v158
X_ a43,v159
v_pk_mul_f32 v[48:49], v[48:49], s[48:49]
v_pk_mul_f32 v[50:51], v[50:51], s[48:49]
v_pk_mul_f32 v[52:53], v[52:53], s[48:49]
v_pk_mul_f32 v[54:55], v[54:55], s[48:49]
C_ 155,54,55
C_ 154,52,53
C_ 153,50,51
C_ 152,48,49
X_ a16,v152
X_ a17,v153
X_ a18,v154
X_ a19,v155
v_pk_mul_f32 v[40:41], v[40:41], s[48:49]
v_pk_mul_f32 v[42:43], v[42:43], s[48:49]
v_pk_mul_f32 v[44:45], v[44:45], s[48:49]
v_pk_mul_f32 v[46:47], v[46:47], s[48:49]
C_ 151,46,47
C_ 150,44,45
C_ 149,42,43
C_ 148,40,41
X_ a12,v148
X_ a13,v149
X_ a14,v150
X_ a15,v151
s_sub_u32 s59, s59, 768
s_waitcnt vmcnt(0) lgkmcnt(0)
s_barrier
Ta 20,%0,8192
Ta 22,%0,8704
Ta 24,%0,12288
Ta 26,%0,12800
Da 60,v2,0
Da 100,v3,0
Da 64,v2,2048
Da 104,v3,2048
Da 68,v2,4096
Da 108,v3,4096
Da 72,v2,6144
Da 112,v3,6144
Da 76,v2,8192
Da 116,v3,8192
Ta 28,%0,8200
Ta 30,%0,8712
Ta 32,%0,12296
Ta 34,%0,12808
v_pk_mul_f32 v[32:33], v[32:33], s[48:49]
v_pk_mul_f32 v[34:35], v[34:35], s[48:49]
v_pk_mul_f32 v[36:37], v[36:37], s[48:49]
v_pk_mul_f32 v[38:39], v[38:39], s[48:49]
C_ 147,38,39
C_ 146,36,37
C_ 145,34,35
C_ 144,32,33
X_ a8,v144
X_ a9,v145
X_ a10,v146
X_ a11,v147
v_pk_mul_f32 v[24:25], v[24:25], s[48:49]
v_pk_mul_f32 v[26:27], v[26:27], s[48:49]
v_pk_mul_f32 v[28:29], v[28:29], s[48:49]
v_pk_mul_f32 v[30:31], v[30:31], s[48:49]
C_ 143,30,31
C_ 142,28,29
C_ 141,26,27
C_ 140,24,25
X_ a4,v140
X_ a5,v141
X_ a6,v142
X_ a7,v143
v_pk_mul_f32 v[16:17], v[16:17], s[48:49]
v_pk_mul_f32 v[18:19], v[18:19], s[48:49]
v_pk_mul_f32 v[20:21], v[20:21], s[48:49]
v_pk_mul_f32 v[22:23], v[22:23], s[48:49]
C_ 139,22,23
C_ 138,20,21
C_ 137,18,19
C_ 136,16,17
X_ a0,v136
X_ a1,v137
X_ a2,v138
X_ a3,v139
s_sub_u32 s90, s90, s88
s_cmp_gt_u32 s90, 4000
s_cselect_b32 s11, 1, 0
s_sub_u32 s12, s68, s98
s_max_u32 s12, s12, 0
S_ s12,s12,1251
s_mov_b32 s13, 113
s_nop 1
.p2align 3
.Lq:
W_ 13
Zvaa 16,20,60
s_nop 0
Ta 36,v1,8192
Ta 38,v1,8704
W_ 14
Mvaa 16,24,100
R_ v12,a248
W_ 13
Zvaa 24,20,64
v_lshrrev_b32 v12, 4, v12
W_ 12
Mvaa 24,24,104
s_nop 0
Ta 224,v1,12288
Ta 226,v1,12800
W_ 13
Zvaa 32,20,68
v_min_u32 v10, 11, v12
W_ 12
Mvaa 32,24,108
s_mul_i32 s56, s4, 12
W_ 11
Zvaa 40,20,72
v_add_u32 v10, s56, v10
W_ 10
Mvaa 40,24,112
v_mul_u32_u24 v11, 43691, v10
W_ 9
Zvaa 48,20,76
v_lshrrev_b32 v11, 18, v11
W_ 8
Mvaa 48,24,116
W_ 4
v_lshlrev_b32 v10, 7, v10
Zvaa 52,32,116
s_nop 0
Ta 20,v1,8200
Ta 22,v1,8712
Mvaa 52,28,76
v_mul_u32_u24 v11, 11520, v11
Zvaa 44,32,112
v_add_u32 v10, v10, v11
Mvaa 44,28,72
s_nop 0
Ta 24,v1,12296
Ta 26,v1,12808
Zvaa 36,32,108
v_min_u32 v12, 9, v12
Mvaa 36,28,68
s_and_b32 s56, s6, 3
Zvaa 28,32,104
s_lshl_b32 s56, s56, 2
Mvaa 28,28,64
S_ s56,s56,s4
Zvaa 20,32,100
s_waitcnt vmcnt(18) lgkmcnt(0)
s_barrier
s_mul_i32 s56, s56, 10
Mvaa 20,28,60
S_ s65,s59,786432
v_add_u32 v12, s56, v12
Zvaa 56,36,60
S_ m0,s52,8064
Ta 28,%0,16384
Ta 30,%0,16896
Mvaa 56,224,100
L_ v6,28,s65,128
Zvaa 64,36,64
S_ s65,s59,884736
v_mul_u32_u24 v12, 1280, v12
Mvaa 64,224,104
S_ m0,s52,9088
Ta 32,%0,20480
Ta 34,%0,20992
Zvaa 72,36,68
s_lshr_b32 s56, s7, 5
Mvaa 72,224,108
L_ v6,28,s65,128
Zvaa 80,36,72
S_ s65,s59,983040
s_mul_i32 s56, s56, 98304
Mvaa 80,224,112
S_ m0,s52,10112
s_and_b32 s55, s59, 4095
Zvaa 88,36,76
S_ s56,s56,s55
Mvaa 88,224,116
s_mul_i32 s55, s77, 384
Zvaa 92,24,116
s_sub_u32 s56, s56, s55
Ta 36,%0,16392
Ta 38,%0,16904
Mvaa 92,20,76
L_ v6,28,s65,128
Zvaa 84,24,112
S_ s65,s59,1081344
s_mov_b32 s63, 0
Mvaa 84,20,72
S_ m0,s52,11136
Ta 224,%0,20488
Ta 226,%0,21000
Zvaa 76,24,108
s_bitcmp1_b32 1, s58
s_cselect_b32 s63, 0, 4026531840
Mvaa 76,20,68
L_ v6,28,s65,128
Zvaa 68,24,104
S_ s65,s60,256
S_ s56,s56,s63
Mvaa 68,20,64
S_ m0,s50,0
S_ s55,s56,3145728
Zvaa 60,24,100
Mvaa 60,20,60
W_ 4
Zvaa 96,28,60
s_nop 0
Ta 20,v1,16384
Ta 22,v1,16896
Mvaa 96,32,100
L_ v4,24,s65,0
Zvaa 104,28,64
S_ s65,s60,10496
Mvaa 104,32,104
S_ m0,s50,1024
Ta 24,v1,20480
Ta 26,v1,20992
Zvaa 112,28,68
Mvaa 112,32,108
L_ v5,24,s65,0
Zvaa 120,28,72
S_ s65,s60,20736
Mvaa 120,32,112
S_ m0,s50,2048
Zvaa 128,28,76
Mvaa 128,32,116
W_ 4
Zvaa 132,224,116
s_nop 0
Ta 28,v1,16392
Ta 30,v1,16904
Mvaa 132,36,76
L_ v4,24,s65,0
Zvaa 124,224,112
S_ s65,s60,30976
Mvaa 124,36,72
S_ m0,s50,3072
Ta 32,v1,20488
Ta 34,v1,21000
Zvaa 116,224,108
Mvaa 116,36,68
L_ v5,24,s65,0
Zvaa 108,224,104
S_ s65,s60,41216
Mvaa 108,36,64
S_ m0,s50,4096
Zvaa 100,224,100
s_waitcnt vmcnt(17) lgkmcnt(0)
s_barrier
s_cmp_eq_u32 s11, 1
s_cbranch_scc1 .Lpace1
.Lpace1_back:
Mvaa 100,36,60
Da 140,v2,20480
Zvaa 136,20,60
s_nop 0
Ta 36,%0,24576
Ta 38,%0,25088
Mvaa 136,24,100
L_ v4,24,s65,0
Zvaa 144,20,64
S_ s65,s59,786432
Da 180,v3,20480
Mvaa 144,24,104
S_ m0,s52,16128
Ta 224,%0,28672
Ta 226,%0,29184
Zvaa 152,20,68
Mvaa 152,24,108
Da 144,v2,22528
Zvaa 160,20,72
L_ v6,28,s65,256
Mvaa 160,24,112
S_ s65,s59,884736
Zvaa 168,20,76
S_ m0,s52,17152
Da 184,v3,22528
Mvaa 168,24,116
Zvaa 172,32,116
s_nop 0
Ta 20,%0,24584
Ta 22,%0,25096
Mvaa 172,28,76
Da 148,v2,24576
Zvaa 164,32,112
L_ v6,28,s65,256
Mvaa 164,28,72
S_ s65,s59,983040
Ta 24,%0,28680
Ta 26,%0,29192
Zvaa 156,32,108
S_ m0,s52,18176
Da 188,v3,24576
Mvaa 156,28,68
Zvaa 148,32,104
L_ v6,28,s65,256
Mvaa 148,28,64
S_ s65,s59,1081344
Da 152,v2,26624
Zvaa 140,32,100
S_ m0,s52,19200
Mvaa 140,28,60
W_ 9
Zvaa 176,36,60
s_nop 0
Ta 28,v1,24576
Ta 30,v1,25088
Mvaa 176,224,100
Da 192,v3,26624
Zvaa 184,36,64
L_ v6,28,s65,256
Mvaa 184,224,104
s_nop 0
Ta 32,v1,28672
Ta 34,v1,29184
Zvaa 192,36,68
Da 156,v2,28672
Mvaa 192,224,108
Zvaa 200,36,72
Da 196,v3,28672
Mvaa 200,224,112
buffer_load_dword a254, v10, s[28:31], s55 offen
Zvaa 208,36,76
Mvaa 208,224,116
W_ 9
Zvaa 212,24,116
s_nop 0
Ta 36,v1,24584
Ta 38,v1,25096
Mvaa 212,20,76
buffer_load_dword a254, v12, s[24:27], s63 offen offset:512
Zvaa 204,24,112
Mvaa 204,20,72
s_nop 0
Ta 224,v1,28680
Ta 226,v1,29192
Zvaa 196,24,108
Mvaa 196,20,68
Zvaa 188,24,104
Mvaa 188,20,64
Zvaa 180,24,100
s_waitcnt vmcnt(20) lgkmcnt(0)
s_barrier
Mvaa 180,20,60
S_ s65,s59,1572864
Zvaa 216,28,60
S_ m0,s52,24576
Ta 20,%0,0
Ta 22,%0,512
Mvaa 216,32,100
L_ v6,28,s65,0
Zvaa 224,28,64
S_ s65,s59,1671168
Mvaa 224,32,104
S_ m0,s52,25600
Ta 24,%0,4096
Ta 26,%0,4608
Zvaa 232,28,68
Mvaa 232,32,108
L_ v6,28,s65,0
Zvaa 240,28,72
S_ s65,s59,1769472
Mvaa 240,32,112
S_ m0,s52,26624
Zvaa 248,28,76
Mvaa 248,32,116
Zvaa 252,224,116
s_nop 0
Ta 28,%0,8
Ta 30,%0,520
Mvaa 252,36,76
L_ v6,28,s65,0
Zvaa 244,224,112
S_ s65,s59,1867776
Mvaa 244,36,72
S_ m0,s52,27648
Ta 32,%0,4104
Ta 34,%0,4616
Zvaa 236,224,108
Mvaa 236,36,68
L_ v6,28,s65,0
Zvaa 228,224,104
S_ s65,s60,384
Mvaa 228,36,64
S_ m0,s50,20480
Zvaa 220,224,100
Mvaa 220,36,60
W_ 4
Mvaa 16,20,140
s_nop 0
Ta 36,v1,0
Ta 38,v1,512
Mvaa 16,24,180
L_ v4,24,s65,0
Mvaa 24,20,144
S_ s65,s60,10624
Mvaa 24,24,184
S_ m0,s50,21504
Ta 224,v1,4096
Ta 226,v1,4608
Mvaa 32,20,148
Mvaa 32,24,188
L_ v5,24,s65,0
Mvaa 40,20,152
S_ s65,s60,20864
Mvaa 40,24,192
S_ m0,s50,22528
Mvaa 48,20,156
Mvaa 48,24,196
W_ 4
Mvaa 52,32,196
s_nop 0
Ta 20,v1,8
Ta 22,v1,520
Mvaa 52,28,156
L_ v4,24,s65,0
Mvaa 44,32,192
S_ s65,s60,31104
Mvaa 44,28,152
S_ m0,s50,23552
Ta 24,v1,4104
Ta 26,v1,4616
Mvaa 36,32,188
Mvaa 36,28,148
L_ v5,24,s65,0
Mvaa 28,32,184
S_ s65,s60,41344
Mvaa 28,28,144
S_ m0,s50,24576
Mvaa 20,32,180
s_waitcnt vmcnt(19) lgkmcnt(0)
s_barrier
s_cmp_eq_u32 s11, 1
s_cbranch_scc1 .Lpace3
.Lpace3_back:
Mvaa 20,28,140
L_ v4,24,s65,0
Mvaa 56,36,140
S_ s65,s59,1572864
Ta 28,%0,8192
Ta 30,%0,8704
Mvaa 56,224,180
S_ m0,s52,-128
Mvaa 64,36,144
Mvaa 64,224,184
s_nop 0
Ta 32,%0,12288
Ta 34,%0,12800
Mvaa 72,36,148
L_ v6,28,s65,128
Mvaa 72,224,188
S_ s65,s59,1671168
Mvaa 80,36,152
S_ m0,s52,896
Mvaa 80,224,192
Mvaa 88,36,156
L_ v6,28,s65,128
Mvaa 88,224,196
S_ s65,s59,1769472
Mvaa 92,24,196
S_ m0,s52,1920
Ta 36,%0,8200
Ta 38,%0,8712
Mvaa 92,20,156
S_ s55,s56,3932160
Mvaa 84,24,192
L_ v6,28,s65,128
Mvaa 84,20,152
S_ s65,s59,1867776
Ta 224,%0,12296
Ta 226,%0,12808
Mvaa 76,24,188
S_ m0,s52,2944
Mvaa 76,20,148
Mvaa 68,24,184
L_ v6,28,s65,128
Mvaa 68,20,144
Mvaa 60,24,180
Mvaa 60,20,140
W_ 4
Mvaa 96,28,140
s_nop 0
Ta 20,v1,8192
Ta 22,v1,8704
Mvaa 96,32,180
buffer_load_dword a254, v10, s[28:31], s55 offen
Mvaa 104,28,144
Mvaa 104,32,184
s_nop 0
Ta 24,v1,12288
Ta 26,v1,12800
Mvaa 112,28,148
Mvaa 112,32,188
buffer_load_dword a254, v12, s[24:27], s63 offen offset:640
Mvaa 120,28,152
Mvaa 120,32,192
Mvaa 128,28,156
Mvaa 128,32,196
W_ 4
Mvaa 132,224,196
s_nop 0
Ta 28,v1,8200
Ta 30,v1,8712
Mvaa 132,36,156
Mvaa 124,224,192
Mvaa 124,36,152
s_nop 0
Ta 32,v1,12296
Ta 34,v1,12808
Mvaa 116,224,188
Mvaa 116,36,148
Mvaa 108,224,184
Mvaa 108,36,144
Mvaa 100,224,180
s_waitcnt vmcnt(17) lgkmcnt(0)
s_barrier
Mvaa 100,36,140
S_ s65,s59,1572864
Da 60,v2,0
Mvaa 136,20,140
S_ m0,s52,7936
Ta 36,%0,16384
Ta 38,%0,16896
Mvaa 136,24,180
L_ v6,28,s65,256
Mvaa 144,20,144
S_ s65,s59,1671168
Da 100,v3,0
Mvaa 144,24,184
S_ m0,s52,8960
Ta 224,%0,20480
Ta 226,%0,20992
Mvaa 152,20,148
Mvaa 152,24,188
Da 64,v2,2048
Mvaa 160,20,152
L_ v6,28,s65,256
Mvaa 160,24,192
S_ s65,s59,1769472
Mvaa 168,20,156
S_ m0,s52,9984
Da 104,v3,2048
Mvaa 168,24,196
Mvaa 172,32,196
s_nop 0
Ta 20,%0,16392
Ta 22,%0,16904
Mvaa 172,28,156
Da 68,v2,4096
Mvaa 164,32,192
L_ v6,28,s65,256
Mvaa 164,28,152
S_ s65,s59,1867776
Ta 24,%0,20488
Ta 26,%0,21000
)A"
R"A(Mvaa 156,32,188
S_ m0,s52,11008
Da 108,v3,4096
Mvaa 156,28,148
Mvaa 148,32,184
L_ v6,28,s65,256
Mvaa 148,28,144
Da 72,v2,6144
Mvaa 140,32,180
Mvaa 140,28,140
W_ 9
Mvaa 176,36,140
s_nop 0
Ta 28,v1,16384
Ta 30,v1,16896
Mvaa 176,224,180
Da 112,v3,6144
Mvaa 184,36,144
Mvaa 184,224,184
s_nop 0
Ta 32,v1,20480
Ta 34,v1,20992
Mvaa 192,36,148
Da 76,v2,8192
Mvaa 192,224,188
Mvaa 200,36,152
Da 116,v3,8192
Mvaa 200,224,192
Mvaa 208,36,156
Mvaa 208,224,196
W_ 9
Mvaa 212,24,196
s_nop 0
Ta 36,v1,16392
Ta 38,v1,16904
Mvaa 212,20,156
Mvaa 204,24,192
Mvaa 204,20,152
s_nop 0
Ta 224,v1,20488
Ta 226,v1,21000
Mvaa 196,24,188
Mvaa 196,20,148
Mvaa 188,24,184
Mvaa 188,20,144
Mvaa 180,24,180
s_waitcnt vmcnt(15) lgkmcnt(0)
s_barrier
s_cmp_eq_u32 s11, 1
s_cbranch_scc1 .Lpace5
.Lpace5_back:
Mvaa 180,20,140
S_ s65,s59,2359296
Mvaa 216,28,140
S_ m0,s52,16384
Ta 20,%0,24576
Ta 22,%0,25088
Mvaa 216,32,180
L_ v6,28,s65,0
Mvaa 224,28,144
S_ s65,s59,2457600
Mvaa 224,32,184
S_ m0,s52,17408
Ta 24,%0,28672
Ta 26,%0,29184
Mvaa 232,28,148
Mvaa 232,32,188
L_ v6,28,s65,0
Mvaa 240,28,152
S_ s65,s59,2555904
Mvaa 240,32,192
S_ m0,s52,18432
Mvaa 248,28,156
Mvaa 248,32,196
Mvaa 252,224,196
s_nop 0
Ta 28,%0,24584
Ta 30,%0,25096
Mvaa 252,36,156
L_ v6,28,s65,0
Mvaa 244,224,192
S_ s65,s59,2654208
Mvaa 244,36,152
S_ m0,s52,19456
Ta 32,%0,28680
Ta 34,%0,29192
Mvaa 236,224,188
Mvaa 236,36,148
L_ v6,28,s65,0
Mvaa 228,224,184
S_ s65,s60,512
Mvaa 228,36,144
S_ m0,s50,0
Mvaa 220,224,180
Mvaa 220,36,140
W_ 4
Mvaa 16,20,60
s_nop 0
Ta 36,v1,24576
Ta 38,v1,25088
Mvaa 16,24,100
L_ v4,24,s65,0
Mvaa 24,20,64
S_ s65,s60,10752
Mvaa 24,24,104
S_ m0,s50,1024
Ta 224,v1,28672
Ta 226,v1,29184
Mvaa 32,20,68
Mvaa 32,24,108
L_ v5,24,s65,0
Mvaa 40,20,72
S_ s65,s60,20992
Mvaa 40,24,112
S_ m0,s50,2048
Mvaa 48,20,76
Mvaa 48,24,116
W_ 4
Mvaa 52,32,116
s_nop 0
Ta 20,v1,24584
Ta 22,v1,25096
Mvaa 52,28,76
L_ v4,24,s65,0
Mvaa 44,32,112
S_ s65,s60,31232
Mvaa 44,28,72
S_ m0,s50,3072
Ta 24,v1,28680
Ta 26,v1,29192
Mvaa 36,32,108
Mvaa 36,28,68
L_ v5,24,s65,0
Mvaa 28,32,104
S_ s65,s60,41472
Mvaa 28,28,64
S_ m0,s50,4096
Mvaa 20,32,100
s_waitcnt vmcnt(14) lgkmcnt(0)
s_barrier
Mvaa 20,28,60
L_ v4,24,s65,0
Mvaa 56,36,60
S_ s65,s59,2359296
Ta 28,%0,0
Ta 30,%0,512
Mvaa 56,224,100
S_ m0,s52,24448
Mvaa 64,36,64
Mvaa 64,224,104
s_nop 0
Ta 32,%0,4096
Ta 34,%0,4608
Mvaa 72,36,68
L_ v6,28,s65,128
Mvaa 72,224,108
S_ s65,s59,2457600
Mvaa 80,36,72
S_ m0,s52,25472
Mvaa 80,224,112
Mvaa 88,36,76
L_ v6,28,s65,128
Mvaa 88,224,116
S_ s65,s59,2555904
Mvaa 92,24,116
S_ m0,s52,26496
Ta 36,%0,8
Ta 38,%0,520
Mvaa 92,20,76
S_ s55,s56,4718592
Mvaa 84,24,112
L_ v6,28,s65,128
Mvaa 84,20,72
S_ s65,s59,2654208
Ta 224,%0,4104
Ta 226,%0,4616
Mvaa 76,24,108
S_ m0,s52,27520
Mvaa 76,20,68
Mvaa 68,24,104
L_ v6,28,s65,128
Mvaa 68,20,64
Mvaa 60,24,100
Mvaa 60,20,60
W_ 4
Mvaa 96,28,60
s_nop 0
Ta 20,v1,0
Ta 22,v1,512
Mvaa 96,32,100
buffer_load_dword a254, v10, s[28:31], s55 offen
Mvaa 104,28,64
Mvaa 104,32,104
s_nop 0
Ta 24,v1,4096
Ta 26,v1,4608
Mvaa 112,28,68
Mvaa 112,32,108
buffer_load_dword a254, v12, s[24:27], s63 offen offset:768
Mvaa 120,28,72
Mvaa 120,32,112
Mvaa 128,28,76
Mvaa 128,32,116
W_ 4
Mvaa 132,224,116
s_nop 0
Ta 28,v1,8
Ta 30,v1,520
Mvaa 132,36,76
Mvaa 124,224,112
Mvaa 124,36,72
s_nop 0
Ta 32,v1,4104
Ta 34,v1,4616
Mvaa 116,224,108
Mvaa 116,36,68
Mvaa 108,224,104
Mvaa 108,36,64
Mvaa 100,224,100
s_waitcnt vmcnt(15) lgkmcnt(0)
s_barrier
s_cmp_eq_u32 s11, 1
s_cbranch_scc1 .Lpace7
.Lpace7_back:
Mvaa 100,36,60
S_ s65,s59,2359296
Da 140,v2,20480
Mvaa 136,20,60
S_ m0,s52,-256
Ta 36,%0,8192
Ta 38,%0,8704
Mvaa 136,24,100
L_ v6,28,s65,256
Mvaa 144,20,64
S_ s65,s59,2457600
Da 180,v3,20480
Mvaa 144,24,104
S_ m0,s52,768
Ta 224,%0,12288
Ta 226,%0,12800
Mvaa 152,20,68
Mvaa 152,24,108
Da 144,v2,22528
Mvaa 160,20,72
L_ v6,28,s65,256
Mvaa 160,24,112
S_ s65,s59,2555904
Mvaa 168,20,76
S_ m0,s52,1792
Da 184,v3,22528
Mvaa 168,24,116
Mvaa 172,32,116
s_nop 0
Ta 20,%0,8200
Ta 22,%0,8712
Mvaa 172,28,76
Da 148,v2,24576
Mvaa 164,32,112
L_ v6,28,s65,256
Mvaa 164,28,72
S_ s65,s59,2654208
Ta 24,%0,12296
Ta 26,%0,12808
Mvaa 156,32,108
S_ m0,s52,2816
Da 188,v3,24576
Mvaa 156,28,68
Mvaa 148,32,104
L_ v6,28,s65,256
Mvaa 148,28,64
Da 152,v2,26624
Mvaa 140,32,100
Mvaa 140,28,60
W_ 9
Mvaa 176,36,60
s_nop 0
Ta 28,v1,8192
Ta 30,v1,8704
Mvaa 176,224,100
Da 192,v3,26624
Mvaa 184,36,64
Mvaa 184,224,104
s_nop 0
Ta 32,v1,12288
Ta 34,v1,12800
Mvaa 192,36,68
Da 156,v2,28672
Mvaa 192,224,108
Mvaa 200,36,72
Da 196,v3,28672
Mvaa 200,224,112
Mvaa 208,36,76
Mvaa 208,224,116
W_ 9
Mvaa 212,24,116
s_nop 0
Ta 36,v1,8200
Ta 38,v1,8712
Mvaa 212,20,76
Mvaa 204,24,112
Mvaa 204,20,72
s_nop 0
Ta 224,v1,12296
Ta 226,v1,12808
Mvaa 196,24,108
Mvaa 196,20,68
Mvaa 188,24,104
Mvaa 188,20,64
Mvaa 180,24,100
s_waitcnt vmcnt(15) lgkmcnt(0)
s_barrier
Mvaa 180,20,60
S_ s65,s59,3145728
Mvaa 216,28,60
S_ m0,s52,8192
Ta 20,%0,16384
Ta 22,%0,16896
Mvaa 216,32,100
L_ v6,28,s65,0
Mvaa 224,28,64
S_ s65,s59,3244032
Mvaa 224,32,104
S_ m0,s52,9216
Ta 24,%0,20480
Ta 26,%0,20992
Mvaa 232,28,68
Mvaa 232,32,108
L_ v6,28,s65,0
Mvaa 240,28,72
S_ s65,s59,3342336
Mvaa 240,32,112
S_ m0,s52,10240
Mvaa 248,28,76
Mvaa 248,32,116
Mvaa 252,224,116
s_nop 0
Ta 28,%0,16392
Ta 30,%0,16904
Mvaa 252,36,76
L_ v6,28,s65,0
Mvaa 244,224,112
S_ s65,s59,3440640
Mvaa 244,36,72
S_ m0,s52,11264
Ta 32,%0,20488
Ta 34,%0,21000
Mvaa 236,224,108
Mvaa 236,36,68
L_ v6,28,s65,0
Mvaa 228,224,104
S_ s65,s60,640
Mvaa 228,36,64
S_ m0,s50,20480
Mvaa 220,224,100
Mvaa 220,36,60
W_ 4
Mvaa 16,20,140
s_nop 0
Ta 36,v1,16384
Ta 38,v1,16896
Mvaa 16,24,180
L_ v4,24,s65,0
Mvaa 24,20,144
S_ s65,s60,10880
Mvaa 24,24,184
S_ m0,s50,21504
Ta 224,v1,20480
Ta 226,v1,20992
Mvaa 32,20,148
Mvaa 32,24,188
L_ v5,24,s65,0
Mvaa 40,20,152
S_ s65,s60,21120
Mvaa 40,24,192
S_ m0,s50,22528
Mvaa 48,20,156
Mvaa 48,24,196
W_ 4
Mvaa 52,32,196
s_nop 0
Ta 20,v1,16392
Ta 22,v1,16904
Mvaa 52,28,156
L_ v4,24,s65,0
Mvaa 44,32,192
S_ s65,s60,31360
Mvaa 44,28,152
S_ m0,s50,23552
Ta 24,v1,20488
Ta 26,v1,21000
Mvaa 36,32,188
Mvaa 36,28,148
L_ v5,24,s65,0
Mvaa 28,32,184
S_ s65,s60,41600
Mvaa 28,28,144
S_ m0,s50,24576
Mvaa 20,32,180
s_waitcnt vmcnt(14) lgkmcnt(0)
s_barrier
s_cmp_eq_u32 s11, 1
s_cbranch_scc1 .Lpace9
.Lpace9_back:
Mvaa 20,28,140
L_ v4,24,s65,0
Mvaa 56,36,140
S_ s65,s59,3145728
Ta 28,%0,24576
Ta 30,%0,25088
Mvaa 56,224,180
S_ m0,s52,16256
Mvaa 64,36,144
Mvaa 64,224,184
s_nop 0
Ta 32,%0,28672
Ta 34,%0,29184
Mvaa 72,36,148
L_ v6,28,s65,128
Mvaa 72,224,188
S_ s65,s59,3244032
Mvaa 80,36,152
S_ m0,s52,17280
Mvaa 80,224,192
Mvaa 88,36,156
L_ v6,28,s65,128
Mvaa 88,224,196
S_ s65,s59,3342336
Mvaa 92,24,196
S_ m0,s52,18304
Ta 36,%0,24584
Ta 38,%0,25096
Mvaa 92,20,156
S_ s55,s56,5505024
Mvaa 84,24,192
L_ v6,28,s65,128
Mvaa 84,20,152
S_ s65,s59,3440640
Ta 224,%0,28680
Ta 226,%0,29192
Mvaa 76,24,188
S_ m0,s52,19328
Mvaa 76,20,148
Mvaa 68,24,184
L_ v6,28,s65,128
Mvaa 68,20,144
Mvaa 60,24,180
Mvaa 60,20,140
W_ 4
Mvaa 96,28,140
s_nop 0
Ta 20,v1,24576
Ta 22,v1,25088
Mvaa 96,32,180
buffer_load_dword a254, v10, s[28:31], s55 offen
Mvaa 104,28,144
Mvaa 104,32,184
s_nop 0
Ta 24,v1,28672
Ta 26,v1,29184
Mvaa 112,28,148
Mvaa 112,32,188
buffer_load_dword a254, v12, s[24:27], s63 offen offset:896
Mvaa 120,28,152
Mvaa 120,32,192
Mvaa 128,28,156
Mvaa 128,32,196
W_ 4
Mvaa 132,224,196
s_nop 0
Ta 28,v1,24584
Ta 30,v1,25096
Mvaa 132,36,156
Mvaa 124,224,192
Mvaa 124,36,152
s_nop 0
Ta 32,v1,28680
Ta 34,v1,29192
Mvaa 116,224,188
Mvaa 116,36,148
Mvaa 108,224,184
Mvaa 108,36,144
Mvaa 100,224,180
s_waitcnt vmcnt(15) lgkmcnt(0)
s_barrier
Mvaa 100,36,140
S_ s65,s59,3145728
Da 60,v2,0
Mvaa 136,20,140
S_ m0,s52,24320
Ta 36,%0,0
Ta 38,%0,512
Mvaa 136,24,180
L_ v6,28,s65,256
Mvaa 144,20,144
S_ s65,s59,3244032
Da 100,v3,0
Mvaa 144,24,184
S_ m0,s52,25344
Ta 224,%0,4096
Ta 226,%0,4608
Mvaa 152,20,148
Mvaa 152,24,188
Da 64,v2,2048
Mvaa 160,20,152
L_ v6,28,s65,256
Mvaa 160,24,192
S_ s65,s59,3342336
Mvaa 168,20,156
S_ m0,s52,26368
Da 104,v3,2048
Mvaa 168,24,196
Mvaa 172,32,196
s_nop 0
Ta 20,%0,8
Ta 22,%0,520
Mvaa 172,28,156
Da 68,v2,4096
Mvaa 164,32,192
L_ v6,28,s65,256
Mvaa 164,28,152
S_ s65,s59,3440640
Ta 24,%0,4104
Ta 26,%0,4616
Mvaa 156,32,188
S_ m0,s52,27392
Da 108,v3,4096
Mvaa 156,28,148
Mvaa 148,32,184
L_ v6,28,s65,256
Mvaa 148,28,144
Da 72,v2,6144
Mvaa 140,32,180
Mvaa 140,28,140
W_ 9
Mvaa 176,36,140
s_nop 0
Ta 28,v1,0
Ta 30,v1,512
Mvaa 176,224,180
Da 112,v3,6144
Mvaa 184,36,144
Mvaa 184,224,184
s_nop 0
Ta 32,v1,4096
Ta 34,v1,4608
Mvaa 192,36,148
Da 76,v2,8192
Mvaa 192,224,188
Mvaa 200,36,152
Da 116,v3,8192
Mvaa 200,224,192
Mvaa 208,36,156
Mvaa 208,224,196
W_ 9
Mvaa 212,24,196
s_nop 0
Ta 36,v1,8
Ta 38,v1,520
Mvaa 212,20,156
Mvaa 204,24,192
Mvaa 204,20,152
s_nop 0
Ta 224,v1,4104
Ta 226,v1,4616
Mvaa 196,24,188
Mvaa 196,20,148
Mvaa 188,24,184
Mvaa 188,20,144
Mvaa 180,24,180
s_waitcnt vmcnt(15) lgkmcnt(0)
s_barrier
s_cmp_eq_u32 s11, 1
s_cbranch_scc1 .Lpace11
.Lpace11_back:
Mvaa 180,20,140
S_ s65,s59,3932160
Mvaa 216,28,140
S_ m0,s52,0
Ta 20,%0,8192
Ta 22,%0,8704
Mvaa 216,32,180
L_ v6,28,s65,0
Mvaa 224,28,144
S_ s65,s59,4030464
Mvaa 224,32,184
S_ m0,s52,1024
Ta 24,%0,12288
Ta 26,%0,12800
Mvaa 232,28,148
Mvaa 232,32,188
L_ v6,28,s65,0
Mvaa 240,28,152
S_ s65,s59,4128768
Mvaa 240,32,192
S_ m0,s52,2048
Mvaa 248,28,156
Mvaa 248,32,196
Mvaa 252,224,196
s_nop 0
Ta 28,%0,8200
Ta 30,%0,8712
Mvaa 252,36,156
L_ v6,28,s65,0
Mvaa 244,224,192
S_ s65,s59,4227072
Mvaa 244,36,152
S_ m0,s52,3072
Ta 32,%0,12296
Ta 34,%0,12808
Mvaa 236,224,188
Mvaa 236,36,148
L_ v6,28,s65,0
Mvaa 228,224,184
S_ s65,s60,768
Mvaa 228,36,144
S_ m0,s50,0
Mvaa 220,224,180
Mvaa 220,36,140
W_ 4
Mvaa 16,20,60
s_nop 0
Ta 36,v1,8192
Ta 38,v1,8704
Mvaa 16,24,100
L_ v4,24,s65,0
Mvaa 24,20,64
S_ s65,s60,11008
R_ v13,a248
Mvaa 24,24,104
S_ m0,s50,1024
Ta 224,v1,12288
Ta 226,v1,12800
Mvaa 32,20,68
v_min_u32 v13, 240, v13
Mvaa 32,24,108
L_ v5,24,s65,0
Mvaa 40,20,72
S_ s65,s60,21248
v_lshlrev_b32 v13, 3, v13
Mvaa 40,24,112
S_ m0,s50,2048
Mvaa 48,20,76
Mvaa 48,24,116
W_ 4
Mvaa 52,32,116
s_nop 0
Ta 20,v1,8200
Ta 22,v1,8712
Mvaa 52,28,76
L_ v4,24,s65,0
Mvaa 44,32,112
S_ s65,s60,31488
Mvaa 44,28,72
S_ m0,s50,3072
Ta 24,v1,12296
Ta 26,v1,12808
Mvaa 36,32,108
Mvaa 36,28,68
L_ v5,24,s65,0
Mvaa 28,32,104
S_ s65,s60,41728
Mvaa 28,28,64
S_ m0,s50,4096
Mvaa 20,32,100
s_waitcnt vmcnt(14) lgkmcnt(0)
s_barrier
Mvaa 20,28,60
L_ v4,24,s65,0
Mvaa 56,36,60
S_ s65,s59,3932160
Ta 28,%0,16384
Ta 30,%0,16896
Mvaa 56,224,100
S_ m0,s52,8064
Mvaa 64,36,64
Mvaa 64,224,104
s_nop 0
Ta 32,%0,20480
Ta 34,%0,20992
Mvaa 72,36,68
L_ v6,28,s65,128
Mvaa 72,224,108
S_ s65,s59,4030464
Mvaa 80,36,72
S_ m0,s52,9088
Mvaa 80,224,112
Mvaa 88,36,76
L_ v6,28,s65,128
Mvaa 88,224,116
S_ s65,s59,4128768
Mvaa 92,24,116
S_ m0,s52,10112
Ta 36,%0,16392
Ta 38,%0,16904
Mvaa 92,20,76
S_ s55,s56,6291456
Mvaa 84,24,112
L_ v6,28,s65,128
Mvaa 84,20,72
S_ s65,s59,4227072
Ta 224,%0,20488
Ta 226,%0,21000
Mvaa 76,24,108
S_ m0,s52,11136
Mvaa 76,20,68
Mvaa 68,24,104
L_ v6,28,s65,128
Mvaa 68,20,64
Mvaa 60,24,100
Mvaa 60,20,60
W_ 4
Mvaa 96,28,60
s_nop 0
Ta 20,v1,16384
Ta 22,v1,16896
Mvaa 96,32,100
buffer_load_dword a220, v13, s[44:47], s66 offen
Mvaa 104,28,64
Mvaa 104,32,104
s_nop 0
Ta 24,v1,20480
Ta 26,v1,20992
Mvaa 112,28,68
Mvaa 112,32,108
buffer_load_dword a254, v10, s[28:31], s55 offen
Mvaa 120,28,72
Mvaa 120,32,112
Mvaa 128,28,76
Mvaa 128,32,116
W_ 4
Mvaa 132,224,116
s_nop 0
Ta 28,v1,16392
Ta 30,v1,16904
Mvaa 132,36,76
buffer_load_dword a254, v12, s[24:27], s63 offen offset:1024
Mvaa 124,224,112
Mvaa 124,36,72
s_nop 0
Ta 32,v1,20488
Ta 34,v1,21000
Mvaa 116,224,108
Mvaa 116,36,68
Mvaa 108,224,104
Mvaa 108,36,64
Mvaa 100,224,100
s_waitcnt vmcnt(16) lgkmcnt(0)
s_barrier
s_cmp_eq_u32 s11, 1
s_cbranch_scc1 .Lpace13
.Lpace13_back:
Mvaa 100,36,60
S_ s65,s59,3932160
Da 140,v2,20480
Mvaa 136,20,60
S_ m0,s52,16128
Ta 36,%0,24576
Ta 38,%0,25088
Mvaa 136,24,100
L_ v6,28,s65,256
Mvaa 144,20,64
S_ s65,s59,4030464
Da 180,v3,20480
Mvaa 144,24,104
S_ m0,s52,17152
Ta 224,%0,28672
Ta 226,%0,29184
Mvaa 152,20,68
Mvaa 152,24,108
Da 144,v2,22528
Mvaa 160,20,72
L_ v6,28,s65,256
Mvaa 160,24,112
S_ s65,s59,4128768
Mvaa 168,20,76
S_ m0,s52,18176
Da 184,v3,22528
Mvaa 168,24,116
Mvaa 172,32,116
s_nop 0
Ta 20,%0,24584
Ta 22,%0,25096
Mvaa 172,28,76
Da 148,v2,24576
Mvaa 164,32,112
L_ v6,28,s65,256
Mvaa 164,28,72
S_ s65,s59,4227072
Ta 24,%0,28680
Ta 26,%0,29192
Mvaa 156,32,108
S_ m0,s52,19200
Da 188,v3,24576
Mvaa 156,28,68
Mvaa 148,32,104
L_ v6,28,s65,256
Mvaa 148,28,64
Da 152,v2,26624
Mvaa 140,32,100
Mvaa 140,28,60
W_ 9
Mvaa 176,36,60
s_nop 0
Ta 28,v1,24576
Ta 30,v1,25088
Mvaa 176,224,100
Da 192,v3,26624
Mvaa 184,36,64
Mvaa 184,224,104
s_nop 0
Ta 32,v1,28672
Ta 34,v1,29184
Mvaa 192,36,68
Da 156,v2,28672
Mvaa 192,224,108
Mvaa 200,36,72
Da 196,v3,28672
Mvaa 200,224,112
Mvaa 208,36,76
Mvaa 208,224,116
W_ 9
Mvaa 212,24,116
s_nop 0
Ta 36,v1,24584
Ta 38,v1,25096
Mvaa 212,20,76
Mvaa 204,24,112
Mvaa 204,20,72
s_nop 0
Ta 224,v1,28680
Ta 226,v1,29192
Mvaa 196,24,108
Mvaa 196,20,68
Mvaa 188,24,104
Mvaa 188,20,64
Mvaa 180,24,100
s_waitcnt vmcnt(16) lgkmcnt(0)
s_barrier
Mvaa 180,20,60
S_ s65,s59,4718592
Mvaa 216,28,60
S_ m0,s52,24576
Ta 20,%0,0
Ta 22,%0,512
Mvaa 216,32,100
L_ v6,28,s65,0
Mvaa 224,28,64
S_ s65,s59,4816896
Mvaa 224,32,104
S_ m0,s52,25600
Ta 24,%0,4096
Ta 26,%0,4608
Mvaa 232,28,68
Mvaa 232,32,108
L_ v6,28,s65,0
Mvaa 240,28,72
S_ s65,s59,4915200
Mvaa 240,32,112
S_ m0,s52,26624
Mvaa 248,28,76
Mvaa 248,32,116
Mvaa 252,224,116
s_nop 0
Ta 28,%0,8
Ta 30,%0,520
Mvaa 252,36,76
L_ v6,28,s65,0
Mvaa 244,224,112
S_ s65,s59,5013504
Mvaa 244,36,72
S_ m0,s52,27648
Ta 32,%0,4104
Ta 34,%0,4616
Mvaa 236,224,108
Mvaa 236,36,68
L_ v6,28,s65,0
Mvaa 228,224,104
S_ s65,s60,896
Mvaa 228,36,64
S_ m0,s50,20480
Mvaa 220,224,100
Mvaa 220,36,60
W_ 4
Mvaa 16,20,140
s_nop 0
Ta 36,v1,0
Ta 38,v1,512
Mvaa 16,24,180
L_ v4,24,s65,0
Mvaa 24,20,144
S_ s65,s60,11136
Mvaa 24,24,184
S_ m0,s50,21504
Ta 224,v1,4096
Ta 226,v1,4608
Mvaa 32,20,148
Mvaa 32,24,188
L_ v5,24,s65,0
Mvaa 40,20,152
S_ s65,s60,21376
Mvaa 40,24,192
S_ m0,s50,22528
Mvaa 48,20,156
Mvaa 48,24,196
W_ 4
Mvaa 52,32,196
s_nop 0
Ta 20,v1,8
Ta 22,v1,520
Mvaa 52,28,156
L_ v4,24,s65,0
Mvaa 44,32,192
S_ s65,s60,31616
Mvaa 44,28,152
S_ m0,s50,23552
Ta 24,v1,4104
Ta 26,v1,4616
Mvaa 36,32,188
Mvaa 36,28,148
L_ v5,24,s65,0
Mvaa 28,32,184
S_ s65,s60,41856
Mvaa 28,28,144
S_ m0,s50,24576
Mvaa 20,32,180
s_waitcnt vmcnt(15) lgkmcnt(0)
s_barrier
s_cmp_eq_u32 s11, 1
s_cbranch_scc1 .Lpace15
.Lpace15_back:
Mvaa 20,28,140
L_ v4,24,s65,0
Mvaa 56,36,140
S_ s65,s59,4718592
Ta 28,%0,8192
Ta 30,%0,8704
Mvaa 56,224,180
S_ m0,s52,-128
Mvaa 64,36,144
Mvaa 64,224,184
s_nop 0
Ta 32,%0,12288
Ta 34,%0,12800
Mvaa 72,36,148
L_ v6,28,s65,128
Mvaa 72,224,188
S_ s65,s59,4816896
Mvaa 80,36,152
S_ m0,s52,896
Mvaa 80,224,192
Mvaa 88,36,156
L_ v6,28,s65,128
Mvaa 88,224,196
S_ s65,s59,4915200
Mvaa 92,24,196
S_ m0,s52,1920
Ta 36,%0,8200
Ta 38,%0,8712
Mvaa 92,20,156
S_ s55,s56,7077888
Mvaa 84,24,192
L_ v6,28,s65,128
Mvaa 84,20,152
S_ s65,s59,5013504
Ta 224,%0,12296
Ta 226,%0,12808
Mvaa 76,24,188
S_ m0,s52,2944
Mvaa 76,20,148
Mvaa 68,24,184
L_ v6,28,s65,128
Mvaa 68,20,144
Mvaa 60,24,180
Mvaa 60,20,140
W_ 4
Mvaa 96,28,140
s_nop 0
Ta 20,v1,8192
Ta 22,v1,8704
Mvaa 96,32,180
buffer_load_dword a254, v10, s[28:31], s55 offen
Mvaa 104,28,144
Mvaa 104,32,184
s_nop 0
Ta 24,v1,12288
Ta 26,v1,12800
Mvaa 112,28,148
Mvaa 112,32,188
buffer_load_dword a254, v12, s[24:27], s63 offen offset:1152
Mvaa 120,28,152
Mvaa 120,32,192
Mvaa 128,28,156
Mvaa 128,32,196
W_ 4
Mvaa 132,224,196
s_nop 0
Ta 28,v1,8200
Ta 30,v1,8712
Mvaa 132,36,156
Mvaa 124,224,192
Mvaa 124,36,152
s_nop 0
Ta 32,v1,12296
Ta 34,v1,12808
Mvaa 116,224,188
Mvaa 116,36,148
Mvaa 108,224,184
Mvaa 108,36,144
Mvaa 100,224,180
s_waitcnt vmcnt(15) lgkmcnt(0)
s_barrier
Mvaa 100,36,140
S_ s65,s59,4718592
Da 60,v2,0
Mvaa 136,20,140
S_ m0,s52,7936
Ta 36,%0,16384
Ta 38,%0,16896
Mvaa 136,24,180
L_ v6,28,s65,256
Mvaa 144,20,144
S_ s65,s59,4816896
Da 100,v3,0
Mvaa 144,24,184
S_ m0,s52,8960
Ta 224,%0,20480
Ta 226,%0,20992
Mvaa 152,20,148
Mvaa 152,24,188
Da 64,v2,2048
Mvaa 160,20,152
L_ v6,28,s65,256
Mvaa 160,24,192
S_ s65,s59,4915200
Mvaa 168,20,156
S_ m0,s52,9984
Da 104,v3,2048
Mvaa 168,24,196
Mvaa 172,32,196
s_nop 0
Ta 20,%0,16392
Ta 22,%0,16904
Mvaa 172,28,156
Da 68,v2,4096
Mvaa 164,32,192
L_ v6,28,s65,256
Mvaa 164,28,152
S_ s65,s59,5013504
Ta 24,%0,20488
Ta 26,%0,21000
Mvaa 156,32,188
S_ m0,s52,11008
Da 108,v3,4096
Mvaa 156,28,148
Mvaa 148,32,184
L_ v6,28,s65,256
Mvaa 148,28,144
Da 72,v2,6144
Mvaa 140,32,180
Mvaa 140,28,140
W_ 9
Mvaa 176,36,140
s_nop 0
Ta 28,v1,16384
Ta 30,v1,16896
Mvaa 176,224,180
Da 112,v3,6144
Mvaa 184,36,144
Mvaa 184,224,184
s_nop 0
Ta 32,v1,20480
Ta 34,v1,20992
Mvaa 192,36,148
Da 76,v2,8192
Mvaa 192,224,188
Mvaa 200,36,152
Da 116,v3,8192
Mvaa 200,224,192
Mvaa 208,36,156
Mvaa 208,224,196
W_ 9
Mvaa 212,24,196
s_nop 0
Ta 36,v1,16392
Ta 38,v1,16904
Mvaa 212,20,156
Mvaa 204,24,192
Mvaa 204,20,152
s_nop 0
Ta 224,v1,20488
Ta 226,v1,21000
Mvaa 196,24,188
Mvaa 196,20,148
Mvaa 188,24,184
Mvaa 188,20,144
Mvaa 180,24,180
s_waitcnt vmcnt(15) lgkmcnt(0)
s_barrier
s_cmp_eq_u32 s11, 1
s_cbranch_scc1 .Lpace17
.Lpace17_back:
Mvaa 180,20,140
S_ s65,s59,5505024
Mvaa 216,28,140
S_ m0,s52,16384
Ta 20,%0,24576
Ta 22,%0,25088
Mvaa 216,32,180
L_ v6,28,s65,0
Mvaa 224,28,144
S_ s65,s59,5603328
Mvaa 224,32,184
S_ m0,s52,17408
Ta 24,%0,28672
Ta 26,%0,29184
Mvaa 232,28,148
Mvaa 232,32,188
L_ v6,28,s65,0
Mvaa 240,28,152
S_ s65,s59,5701632
Mvaa 240,32,192
S_ m0,s52,18432
Mvaa 248,28,156
Mvaa 248,32,196
Mvaa 252,224,196
s_nop 0
Ta 28,%0,24584
Ta 30,%0,25096
Mvaa 252,36,156
L_ v6,28,s65,0
Mvaa 244,224,192
S_ s65,s59,5799936
Mvaa 244,36,152
S_ m0,s52,19456
Ta 32,%0,28680
Ta 34,%0,29192
Mvaa 236,224,188
Mvaa 236,36,148
L_ v6,28,s65,0
Mvaa 228,224,184
S_ s65,s60,1024
Mvaa 228,36,144
S_ m0,s50,0
Mvaa 220,224,180
Mvaa 220,36,140
W_ 4
Mvaa 16,20,60
s_nop 0
Ta 36,v1,24576
Ta 38,v1,25088
Mvaa 16,24,100
L_ v4,24,s65,0
Mvaa 24,20,64
S_ s65,s60,11264
Mvaa 24,24,104
S_ m0,s50,1024
Ta 224,v1,28672
Ta 226,v1,29184
Mvaa 32,20,68
Mvaa 32,24,108
L_ v5,24,s65,0
Mvaa 40,20,72
S_ s65,s60,21504
Mvaa 40,24,112
S_ m0,s50,2048
Mvaa 48,20,76
Mvaa 48,24,116
W_ 4
Mvaa 52,32,116
s_nop 0
Ta 20,v1,24584
Ta 22,v1,25096
Mvaa 52,28,76
L_ v4,24,s65,0
Mvaa 44,32,112
S_ s65,s60,31744
Mvaa 44,28,72
S_ m0,s50,3072
Ta 24,v1,28680
Ta 26,v1,29192
Mvaa 36,32,108
Mvaa 36,28,68
L_ v5,24,s65,0
Mvaa 28,32,104
S_ s65,s60,41984
Mvaa 28,28,64
S_ m0,s50,4096
Mvaa 20,32,100
s_waitcnt vmcnt(14) lgkmcnt(0)
s_barrier
Mvaa 20,28,60
L_ v4,24,s65,0
Mvaa 56,36,60
S_ s65,s59,5505024
Ta 28,%0,0
Ta 30,%0,512
Mvaa 56,224,100
S_ m0,s52,24448
Mvaa 64,36,64
Mvaa 64,224,104
s_nop 0
Ta 32,%0,4096
Ta 34,%0,4608
Mvaa 72,36,68
L_ v6,28,s65,128
Mvaa 72,224,108
S_ s65,s59,5603328
Mvaa 80,36,72
S_ m0,s52,25472
Mvaa 80,224,112
Mvaa 88,36,76
L_ v6,28,s65,128
Mvaa 88,224,116
S_ s65,s59,5701632
Mvaa 92,24,116
S_ m0,s52,26496
Ta 36,%0,8
Ta 38,%0,520
Mvaa 92,20,76
Mvaa 84,24,112
L_ v6,28,s65,128
Mvaa 84,20,72
S_ s65,s59,5799936
Ta 224,%0,4104
Ta 226,%0,4616
Mvaa 76,24,108
S_ m0,s52,27520
Mvaa 76,20,68
Mvaa 68,24,104
L_ v6,28,s65,128
Mvaa 68,20,64
S_ s65,s61,0
Mvaa 60,24,100
S_ m0,s51,36864
Mvaa 60,20,60
W_ 4
Mvaa 96,28,60
s_nop 0
Ta 20,v1,0
Ta 22,v1,512
Mvaa 96,32,100
Ln v7,32,s65,0
Mvaa 104,28,64
Mvaa 104,32,104
s_nop 0
Ta 24,v1,4096
Ta 26,v1,4608
Mvaa 112,28,68
Mvaa 112,32,108
Ln v8,32,s65,3072
Mvaa 120,28,72
S_ m0,s51,37760
Mvaa 120,32,112
Mvaa 128,28,76
Mvaa 128,32,116
W_ 4
Mvaa 132,224,116
s_nop 0
Ta 28,v1,8
Ta 30,v1,520
Mvaa 132,36,76
Ln v7,32,s65,128
Mvaa 124,224,112
Mvaa 124,36,72
s_nop 0
Ta 32,v1,4104
Ta 34,v1,4616
Mvaa 116,224,108
Mvaa 116,36,68
Ln v8,32,s65,3200
Mvaa 108,224,104
S_ m0,s51,38656
Mvaa 108,36,64
Mvaa 100,224,100
s_waitcnt vmcnt(17) lgkmcnt(0)
s_barrier
s_cmp_eq_u32 s11, 1
s_cbranch_scc1 .Lpace19
.Lpace19_back:
Mvaa 100,36,60
Da 140,v2,20480
Mvaa 136,20,60
s_nop 0
Ta 36,%0,8192
Ta 38,%0,8704
Mvaa 136,24,100
Ln v7,32,s65,256
Mvaa 144,20,64
Da 180,v3,20480
Mvaa 144,24,104
s_nop 0
Ta 224,%0,12288
Ta 226,%0,12800
Mvaa 152,20,68
Mvaa 152,24,108
Da 144,v2,22528
Mvaa 160,20,72
Ln v8,32,s65,3328
Mvaa 160,24,112
S_ s65,s59,5505024
Mvaa 168,20,76
S_ m0,s52,-256
Da 184,v3,22528
Mvaa 168,24,116
Mvaa 172,32,116
s_nop 0
Ta 20,%0,8200
Ta 22,%0,8712
Mvaa 172,28,76
Da 148,v2,24576
Mvaa 164,32,112
L_ v6,28,s65,256
Mvaa 164,28,72
S_ s65,s59,5603328
Ta 24,%0,12296
Ta 26,%0,12808
Mvaa 156,32,108
S_ m0,s52,768
Da 188,v3,24576
Mvaa 156,28,68
Mvaa 148,32,104
L_ v6,28,s65,256
Mvaa 148,28,64
S_ s65,s59,5701632
Da 152,v2,26624
Mvaa 140,32,100
S_ m0,s52,1792
Mvaa 140,28,60
W_ 9
Mvaa 176,36,60
s_nop 0
Ta 28,v1,8192
Ta 30,v1,8704
Mvaa 176,224,100
Da 192,v3,26624
Mvaa 184,36,64
L_ v6,28,s65,256
Mvaa 184,224,104
S_ s65,s59,5799936
Ta 32,v1,12288
Ta 34,v1,12800
Mvaa 192,36,68
S_ m0,s52,2816
Da 156,v2,28672
Mvaa 192,224,108
Mvaa 200,36,72
Da 196,v3,28672
Mvaa 200,224,112
L_ v6,28,s65,256
Mvaa 208,36,76
Mvaa 208,224,116
W_ 9
Mvaa 212,24,116
s_nop 0
Ta 36,v1,8200
Ta 38,v1,8712
Mvaa 212,20,76
Mvaa 204,24,112
Mvaa 204,20,72
s_nop 0
Ta 224,v1,12296
Ta 226,v1,12808
Mvaa 196,24,108
Mvaa 196,20,68
Mvaa 188,24,104
Mvaa 188,20,64
Mvaa 180,24,100
s_waitcnt vmcnt(19) lgkmcnt(0)
s_barrier
Mvaa 180,20,60
S_ s65,s59,6291456
Mvaa 216,28,60
S_ m0,s52,7936
Ta 20,%0,16384
Ta 22,%0,16896
Mvaa 216,32,100
L_ v6,28,s65,256
Mvaa 224,28,64
S_ s65,s59,6389760
Mvaa 224,32,104
S_ m0,s52,8960
Ta 24,%0,20480
Ta 26,%0,20992
Mvaa 232,28,68
Mvaa 232,32,108
L_ v6,28,s65,256
Mvaa 240,28,72
S_ s65,s59,6488064
Mvaa 240,32,112
S_ m0,s52,9984
Mvaa 248,28,76
Mvaa 248,32,116
Mvaa 252,224,116
s_nop 0
Ta 28,%0,16392
Ta 30,%0,16904
Mvaa 252,36,76
L_ v6,28,s65,256
Mvaa 244,224,112
S_ s65,s59,6586368
Mvaa 244,36,72
S_ m0,s52,11008
Ta 32,%0,20488
Ta 34,%0,21000
Mvaa 236,224,108
Mvaa 236,36,68
L_ v6,28,s65,256
Mvaa 228,224,104
S_ s65,s60,1152
Mvaa 228,36,64
S_ m0,s50,20480
Mvaa 220,224,100
Mvaa 220,36,60
W_ 4
Mvaa 16,20,140
s_nop 0
Ta 36,v1,16384
Ta 38,v1,16896
Mvaa 16,24,180
L_ v4,24,s65,0
Mvaa 24,20,144
S_ s65,s60,11392
Mvaa 24,24,184
S_ m0,s50,21504
Ta 224,v1,20480
Ta 226,v1,20992
Mvaa 32,20,148
Mvaa 32,24,188
L_ v5,24,s65,0
Mvaa 40,20,152
S_ s65,s60,21632
Mvaa 40,24,192
S_ m0,s50,22528
Mvaa 48,20,156
Mvaa 48,24,196
W_ 4
Mvaa 52,32,196
s_nop 0
Ta 20,v1,16392
Ta 22,v1,16904
Mvaa 52,28,156
L_ v4,24,s65,0
Mvaa 44,32,192
S_ s65,s60,31872
Mvaa 44,28,152
S_ m0,s50,23552
Ta 24,v1,20488
Ta 26,v1,21000
Mvaa 36,32,188
Mvaa 36,28,148
L_ v5,24,s65,0
Mvaa 28,32,184
S_ s65,s60,42112
Mvaa 28,28,144
S_ m0,s50,24576
Mvaa 20,32,180
s_waitcnt vmcnt(18) lgkmcnt(0)
s_barrier
s_cmp_eq_u32 s11, 1
s_cbranch_scc1 .Lpace21
.Lpace21_back:
Mvaa 20,28,140
L_ v4,24,s65,0
Mvaa 56,36,140
S_ s65,s59,7077888
Ta 28,%0,24576
Ta 30,%0,25088
Mvaa 56,224,180
S_ m0,s52,16128
Mvaa 64,36,144
Mvaa 64,224,184
s_nop 0
Ta 32,%0,28672
Ta 34,%0,29184
Mvaa 72,36,148
L_ v6,28,s65,256
Mvaa 72,224,188
S_ s65,s59,7176192
Mvaa 80,36,152
S_ m0,s52,17152
Mvaa 80,224,192
Mvaa 88,36,156
L_ v6,28,s65,256
Mvaa 88,224,196
S_ s65,s59,7274496
Mvaa 92,24,196
S_ m0,s52,18176
Ta 36,%0,24584
Ta 38,%0,25096
Mvaa 92,20,156
Mvaa 84,24,192
L_ v6,28,s65,256
Mvaa 84,20,152
S_ s65,s59,7372800
Ta 224,%0,28680
Ta 226,%0,29192
Mvaa 76,24,188
S_ m0,s52,19200
Mvaa 76,20,148
Mvaa 68,24,184
L_ v6,28,s65,256
Mvaa 68,20,144
S_ s65,s61,12288
Mvaa 60,24,180
S_ m0,s51,49152
Mvaa 60,20,140
W_ 4
Mvaa 96,28,140
s_nop 0
Ta 20,v1,24576
Ta 22,v1,25088
Mvaa 96,32,180
Ln v7,32,s65,0
Mvaa 104,28,144
Mvaa 104,32,184
s_nop 0
Ta 24,v1,28672
Ta 26,v1,29184
Mvaa 112,28,148
Mvaa 112,32,188
Ln v8,32,s65,3072
Mvaa 120,28,152
S_ m0,s51,50048
Mvaa 120,32,192
Mvaa 128,28,156
Mvaa 128,32,196
W_ 4
Mvaa 132,224,196
s_nop 0
Ta 28,v1,24584
Ta 30,v1,25096
Mvaa 132,36,156
Ln v7,32,s65,128
Mvaa 124,224,192
Mvaa 124,36,152
s_nop 0
Ta 32,v1,28680
Ta 34,v1,29192
Mvaa 116,224,188
Mvaa 116,36,148
Ln v8,32,s65,3200
Mvaa 108,224,184
S_ m0,s51,50944
Mvaa 108,36,144
Mvaa 100,224,180
s_waitcnt vmcnt(17) lgkmcnt(0)
s_barrier
Mvaa 100,36,140
Da 60,v2,0
Mvaa 136,20,140
s_nop 0
Ta 36,%0,0
Ta 38,%0,512
Mvaa 136,24,180
Ln v7,32,s65,256
Mvaa 144,20,144
Da 100,v3,0
Mvaa 144,24,184
s_nop 0
Ta 224,%0,4096
Ta 226,%0,4608
Mvaa 152,20,148
Mvaa 152,24,188
Da 64,v2,2048
Mvaa 160,20,152
Ln v8,32,s65,3328
Mvaa 160,24,192
S_ s65,s59,6291456
Mvaa 168,20,156
S_ m0,s52,24448
Da 104,v3,2048
Mvaa 168,24,196
Mvaa 172,32,196
s_nop 0
Ta 20,%0,8
Ta 22,%0,520
Mvaa 172,28,156
Da 68,v2,4096
Mvaa 164,32,192
L_ v6,28,s65,128
Mvaa 164,28,152
S_ s65,s59,6389760
Ta 24,%0,4104
Ta 26,%0,4616
Mvaa 156,32,188
S_ m0,s52,25472
Da 108,v3,4096
Mvaa 156,28,148
Mvaa 148,32,184
L_ v6,28,s65,128
Mvaa 148,28,144
S_ s65,s59,6488064
Da 72,v2,6144
Mvaa 140,32,180
S_ m0,s52,26496
Mvaa 140,28,140
W_ 9
Mvaa 176,36,140
s_nop 0
Ta 28,v1,0
Ta 30,v1,512
Mvaa 176,224,180
Da 112,v3,6144
Mvaa 184,36,144
L_ v6,28,s65,128
Mvaa 184,224,184
S_ s65,s59,6586368
Ta 32,v1,4096
Ta 34,v1,4608
Mvaa 192,36,148
S_ m0,s52,27520
Da 76,v2,8192
Mvaa 192,224,188
Mvaa 200,36,152
Da 116,v3,8192
Mvaa 200,224,192
L_ v6,28,s65,128
Mvaa 208,36,156
Mvaa 208,224,196
W_ 9
Mvaa 212,24,196
s_nop 0
Ta 36,v1,8
Ta 38,v1,520
Mvaa 212,20,156
Mvaa 204,24,192
Mvaa 204,20,152
s_nop 0
Ta 224,v1,4104
Ta 226,v1,4616
Mvaa 196,24,188
Mvaa 196,20,148
Mvaa 188,24,184
Mvaa 188,20,144
Mvaa 180,24,180
s_waitcnt vmcnt(14) lgkmcnt(0)
s_barrier
s_cmp_eq_u32 s11, 1
s_cbranch_scc1 .Lpace23
.Lpace23_back:
Mvaa 180,20,140
S_ s65,s59,7077888
Mvaa 216,28,140
S_ m0,s52,-128
Ta 20,v1,8200
Ta 22,v1,8712
Mvaa 216,32,180
L_ v6,28,s65,128
Mvaa 224,28,144
S_ s65,s59,7176192
Mvaa 224,32,184
S_ m0,s52,896
Ta 24,v1,12296
Ta 26,v1,12808
Mvaa 232,28,148
Mvaa 232,32,188
L_ v6,28,s65,128
Mvaa 240,28,152
S_ s65,s59,7274496
Mvaa 240,32,192
S_ m0,s52,1920
Mvaa 248,28,156
Mvaa 248,32,196
Mvaa 252,224,196
s_nop 0
Ta 28,v1,8192
Ta 30,v1,8704
Mvaa 252,36,156
L_ v6,28,s65,128
Mvaa 244,224,192
S_ s65,s59,7372800
Mvaa 244,36,152
S_ m0,s52,2944
Ta 32,v1,12288
Ta 34,v1,12800
Mvaa 236,224,188
Mvaa 236,36,148
L_ v6,28,s65,128
Mvaa 228,224,184
Mvaa 228,36,144
Mvaa 220,224,180
Mvaa 220,36,140
W_ 4
Mvaa 220,20,60
s_nop 0
Ta 36,%0,8200
Ta 38,%0,8712
Mvaa 220,24,100
Da 140,v2,20480
Mvaa 228,20,64
Mvaa 228,24,104
s_nop 0
Ta 224,%0,12296
Ta 226,%0,12808
Mvaa 236,20,68
Da 180,v3,20480
Mvaa 236,24,108
Mvaa 244,20,72
Da 144,v2,22528
Mvaa 244,24,112
Mvaa 252,20,76
Mvaa 252,24,116
W_ 7
Da 184,v3,22528
Mvaa 248,32,116
s_nop 0
Ta 20,%0,8192
Ta 22,%0,8704
Mvaa 248,28,76
Mvaa 240,32,112
Da 148,v2,24576
Mvaa 240,28,72
s_nop 0
Ta 24,%0,12288
Ta 26,%0,12800
Mvaa 232,32,108
Mvaa 232,28,68
Da 188,v3,24576
Mvaa 224,32,104
Mvaa 224,28,64
Mvaa 216,32,100
s_waitcnt vmcnt(14) lgkmcnt(0)
s_barrier
Da 152,v2,26624
Mvaa 216,28,60
S_ s65,s59,6291456
Mvaa 180,36,60
S_ m0,s52,8192
Ta 28,v1,16392
Ta 30,v1,16904
Mvaa 180,224,100
Da 192,v3,26624
Mvaa 188,36,64
L_ v6,28,s65,0
Mvaa 188,224,104
S_ s65,s59,6389760
Ta 32,v1,20488
Ta 34,v1,21000
Mvaa 196,36,68
S_ m0,s52,9216
Da 156,v2,28672
Mvaa 196,224,108
Mvaa 204,36,72
L_ v6,28,s65,0
Mvaa 204,224,112
S_ s65,s59,6488064
Da 196,v3,28672
Mvaa 212,36,76
S_ m0,s52,10240
Mvaa 212,224,116
Mvaa 208,24,116
s_nop 0
Ta 36,v1,16384
Ta 38,v1,16896
Mvaa 208,20,76
L_ v6,28,s65,0
Mvaa 200,24,112
S_ s65,s59,6586368
Mvaa 200,20,72
S_ m0,s52,11264
Ta 224,v1,20480
Ta 226,v1,20992
Mvaa 192,24,108
Mvaa 192,20,68
L_ v6,28,s65,0
Mvaa 184,24,104
Mvaa 184,20,64
Mvaa 176,24,100
Mvaa 176,20,60
W_ 6
Mvaa 220,28,140
s_nop 0
Ta 20,%0,16392
Ta 22,%0,16904
Mvaa 220,32,180
Mvaa 228,28,144
Mvaa 228,32,184
s_nop 0
Ta 24,%0,20488
Ta 26,%0,21000
Mvaa 236,28,148
Mvaa 236,32,188
Mvaa 244,28,152
Mvaa 244,32,192
W_ 9
Mvaa 252,28,156
W_ 8
Mvaa 252,32,196
W_ 4
Mvaa 248,224,196
s_nop 0
Ta 28,%0,16384
Ta 30,%0,16896
Mvaa 248,36,156
Mvaa 240,224,192
Mvaa 240,36,152
s_nop 0
Ta 32,%0,20480
Ta 34,%0,20992
Mvaa 232,224,188
Mvaa 232,36,148
Mvaa 224,224,184
Mvaa 224,36,144
Mvaa 216,224,180
s_waitcnt vmcnt(8) lgkmcnt(0)
s_barrier
s_cmp_eq_u32 s11, 1
s_cbranch_scc1 .Lpace25
.Lpace25_back:
Mvaa 216,36,140
S_ s65,s59,7077888
Mvaa 180,20,140
S_ m0,s52,16384
Ta 36,v1,24584
Ta 38,v1,25096
Mvaa 180,24,180
L_ v6,28,s65,0
Mvaa 188,20,144
S_ s65,s59,7176192
Mvaa 188,24,184
S_ m0,s52,17408
Ta 224,v1,28680
Ta 226,v1,29192
Mvaa 196,20,148
U_ v248,s48,v248
U_ v249,s48,v249
Mvaa 196,24,188
L_ v6,28,s65,0
Mvaa 204,20,152
S_ s65,s59,7274496
U_ v250,s48,v250
U_ v251,s48,v251
Mvaa 204,24,192
S_ m0,s52,18432
U_ v252,s48,v252
U_ v253,s48,v253
Mvaa 212,20,156
U_ v254,s48,v254
U_ v255,s48,v255
Mvaa 212,24,196
C_ 255,254,255
C_ 254,252,253
Mvaa 208,32,196
s_nop 0
Ta 20,v1,24576
Ta 22,v1,25088
Mvaa 208,28,156
L_ v6,28,s65,0
Mvaa 200,32,192
S_ s65,s59,7372800
C_ 253,250,251
C_ 252,248,249
Mvaa 200,28,152
S_ m0,s52,19456
Ta 24,v1,28672
Ta 26,v1,29184
Mvaa 192,32,188
U_ v240,s48,v240
U_ v241,s48,v241
Mvaa 192,28,148
L_ v6,28,s65,0
Mvaa 184,32,184
U_ v242,s48,v242
U_ v243,s48,v243
Mvaa 184,28,144
U_ v244,s48,v244
U_ v245,s48,v245
Mvaa 176,32,180
U_ v246,s48,v246
U_ v247,s48,v247
Mvaa 176,28,140
W_ 4
C_ 251,246,247
C_ 250,244,245
Mvaa 140,36,60
s_nop 0
Ta 28,%0,24584
Ta 30,%0,25096
Mvaa 140,224,100
C_ 249,242,243
C_ 248,240,241
Mvaa 148,36,64
U_ v232,s48,v232
U_ v233,s48,v233
Mvaa 148,224,104
s_nop 0
Ta 32,%0,28680
Ta 34,%0,29192
Mvaa 156,36,68
U_ v234,s48,v234
U_ v235,s48,v235
Mvaa 156,224,108
U_ v236,s48,v236
U_ v237,s48,v237
Mvaa 164,36,72
U_ v238,s48,v238
U_ v239,s48,v239
Mvaa 164,224,112
C_ 247,238,239
C_ 246,236,237
Mvaa 172,36,76
C_ 245,234,235
C_ 244,232,233
Mvaa 172,224,116
W_ 4
U_ v224,s48,v224
U_ v225,s48,v225
Mvaa 168,24,116
s_nop 0
Ta 36,%0,24576
Ta 38,%0,25088
Mvaa 168,20,76
U_ v226,s48,v226
U_ v227,s48,v227
Mvaa 160,24,112
U_ v228,s48,v228
U_ v229,s48,v229
Mvaa 160,20,72
s_nop 0
Ta 224,%0,28672
Ta 226,%0,29184
Mvaa 152,24,108
U_ v230,s48,v230
U_ v231,s48,v231
Mvaa 152,20,68
C_ 243,230,231
C_ 242,228,229
Mvaa 144,24,104
C_ 241,226,227
C_ 240,224,225
Mvaa 144,20,64
U_ v216,s48,v216
U_ v217,s48,v217
Mvaa 136,24,100
s_waitcnt vmcnt(8) lgkmcnt(0)
s_barrier
U_ v218,s48,v218
Mvaa 136,20,60
S_ s65,s61,0
U_ v219,s48,v219
Mvaa 100,28,60
S_ m0,s51,24576
Ta 20,v1,8
Ta 22,v1,520
Mvaa 100,32,100
Ln v9,36,s65,0
Mvaa 108,28,64
U_ v220,s48,v220
U_ v221,s48,v221
Mvaa 108,32,104
s_nop 0
Ta 24,v1,4104
Ta 26,v1,4616
Mvaa 116,28,68
U_ v222,s48,v222
U_ v223,s48,v223
Mvaa 116,32,108
Ln v9,36,s65,3072
Mvaa 124,28,72
S_ m0,s51,25472
C_ 239,222,223
C_ 238,220,221
Mvaa 124,32,112
C_ 237,218,219
C_ 236,216,217
Mvaa 132,28,76
U_ v208,s48,v208
U_ v209,s48,v209
Mvaa 132,32,116
U_ v210,s48,v210
U_ v211,s48,v211
Mvaa 128,224,116
s_nop 0
Ta 28,v1,0
Ta 30,v1,512
Mvaa 128,36,76
Ln v9,36,s65,128
Mvaa 120,224,112
U_ v212,s48,v212
U_ v213,s48,v213
Mvaa 120,36,72
s_nop 0
Ta 32,v1,4096
Ta 34,v1,4608
Mvaa 112,224,108
U_ v214,s48,v214
U_ v215,s48,v215
Mvaa 112,36,68
Ln v9,36,s65,3200
Mvaa 104,224,104
S_ m0,s51,26368
C_ 235,214,215
C_ 234,212,213
Mvaa 104,36,64
C_ 233,210,211
C_ 232,208,209
Mvaa 96,224,100
U_ v200,s48,v200
U_ v201,s48,v201
Mvaa 96,36,60
)A"
R"A(W_ 4
U_ v202,s48,v202
U_ v203,s48,v203
Mvaa 140,20,140
s_nop 0
Ta 36,%0,8
Ta 38,%0,520
Mvaa 140,24,180
Ln v9,36,s65,256
Mvaa 148,20,144
U_ v204,s48,v204
U_ v205,s48,v205
Mvaa 148,24,184
s_nop 0
Ta 224,%0,4104
Ta 226,%0,4616
Mvaa 156,20,148
U_ v206,s48,v206
U_ v207,s48,v207
Mvaa 156,24,188
Ln v9,36,s65,3328
Mvaa 164,20,152
C_ 231,206,207
C_ 230,204,205
Mvaa 164,24,192
C_ 229,202,203
C_ 228,200,201
Mvaa 172,20,156
U_ v192,s48,v192
U_ v193,s48,v193
Mvaa 172,24,196
W_ 4
U_ v194,s48,v194
U_ v195,s48,v195
Mvaa 168,32,196
s_nop 0
Ta 20,%0,0
Ta 22,%0,512
Mvaa 168,28,156
U_ v196,s48,v196
U_ v197,s48,v197
Mvaa 160,32,192
U_ v198,s48,v198
U_ v199,s48,v199
Mvaa 160,28,152
s_nop 0
Ta 24,%0,4096
Ta 26,%0,4608
Mvaa 152,32,188
C_ 227,198,199
C_ 226,196,197
Mvaa 152,28,148
C_ 225,194,195
C_ 224,192,193
Mvaa 144,32,184
U_ v184,s48,v184
U_ v185,s48,v185
Mvaa 144,28,144
U_ v186,s48,v186
U_ v187,s48,v187
Mvaa 136,32,180
s_waitcnt vmcnt(10) lgkmcnt(0)
s_barrier
s_cmp_eq_u32 s11, 1
s_cbranch_scc1 .Lpace27
.Lpace27_back:
U_ v188,s48,v188
Mvaa 136,28,140
U_ v189,s48,v189
Mvaa 100,36,140
s_nop 0
Ta 28,v1,8200
Ta 30,v1,8712
Mvaa 100,224,180
U_ v190,s48,v190
U_ v191,s48,v191
Mvaa 108,36,144
C_ 223,190,191
C_ 222,188,189
Mvaa 108,224,184
s_nop 0
Ta 32,v1,12296
Ta 34,v1,12808
Mvaa 116,36,148
C_ 221,186,187
C_ 220,184,185
Mvaa 116,224,188
U_ v176,s48,v176
U_ v177,s48,v177
Mvaa 124,36,152
U_ v178,s48,v178
U_ v179,s48,v179
Mvaa 124,224,192
U_ v180,s48,v180
U_ v181,s48,v181
Mvaa 132,36,156
U_ v182,s48,v182
U_ v183,s48,v183
Mvaa 132,224,196
C_ 219,182,183
C_ 218,180,181
Mvaa 128,24,196
s_nop 0
Ta 36,v1,8192
Ta 38,v1,8704
Mvaa 128,20,156
C_ 217,178,179
C_ 216,176,177
Mvaa 120,24,192
U_ v168,s48,v168
U_ v169,s48,v169
Mvaa 120,20,152
s_nop 0
Ta 224,v1,12288
Ta 226,v1,12800
Mvaa 112,24,188
U_ v170,s48,v170
U_ v171,s48,v171
Mvaa 112,20,148
U_ v172,s48,v172
U_ v173,s48,v173
Mvaa 104,24,184
U_ v174,s48,v174
U_ v175,s48,v175
Mvaa 104,20,144
C_ 215,174,175
C_ 214,172,173
Mvaa 96,24,180
C_ 213,170,171
C_ 212,168,169
Mvaa 96,20,140
W_ 4
U_ v160,s48,v160
U_ v161,s48,v161
Mvaa 60,28,60
s_nop 0
Ta 20,%0,8200
Ta 22,%0,8712
Mvaa 60,32,100
U_ v162,s48,v162
U_ v163,s48,v163
Mvaa 68,28,64
U_ v164,s48,v164
U_ v165,s48,v165
Mvaa 68,32,104
s_nop 0
Ta 24,%0,12296
Ta 26,%0,12808
Mvaa 76,28,68
U_ v166,s48,v166
U_ v167,s48,v167
Mvaa 76,32,108
C_ 211,166,167
C_ 210,164,165
Mvaa 84,28,72
C_ 209,162,163
C_ 208,160,161
Mvaa 84,32,112
U_ v152,s48,v152
U_ v153,s48,v153
Mvaa 92,28,76
U_ v154,s48,v154
U_ v155,s48,v155
Mvaa 92,32,116
W_ 4
U_ v156,s48,v156
U_ v157,s48,v157
Mvaa 88,224,116
s_nop 0
Ta 28,%0,8192
Ta 30,%0,8704
Mvaa 88,36,76
U_ v158,s48,v158
U_ v159,s48,v159
Mvaa 80,224,112
C_ 207,158,159
C_ 206,156,157
Mvaa 80,36,72
s_nop 0
Ta 32,%0,12288
Ta 34,%0,12800
Mvaa 72,224,108
C_ 205,154,155
C_ 204,152,153
Mvaa 72,36,68
U_ v144,s48,v144
U_ v145,s48,v145
Mvaa 64,224,104
U_ v146,s48,v146
U_ v147,s48,v147
Mvaa 64,36,64
U_ v148,s48,v148
U_ v149,s48,v149
Mvaa 56,224,100
s_waitcnt vmcnt(6) lgkmcnt(0)
s_barrier
U_ v150,s48,v150
Mvaa 56,36,60
S_ s65,s61,24576
U_ v151,s48,v151
Mvaa 20,20,60
S_ m0,s51,0
Ta 36,v1,16392
Ta 38,v1,16904
Mvaa 20,24,100
Ln v7,32,s65,0
Mvaa 28,20,64
C_ 203,150,151
C_ 202,148,149
Mvaa 28,24,104
s_nop 0
Ta 224,v1,20488
Ta 226,v1,21000
Mvaa 36,20,68
C_ 201,146,147
C_ 200,144,145
Mvaa 36,24,108
Ln v8,32,s65,3072
Mvaa 44,20,72
S_ m0,s51,896
U_ v136,s48,v136
U_ v137,s48,v137
Mvaa 44,24,112
U_ v138,s48,v138
U_ v139,s48,v139
Mvaa 52,20,76
U_ v140,s48,v140
U_ v141,s48,v141
Mvaa 52,24,116
U_ v142,s48,v142
U_ v143,s48,v143
Mvaa 48,32,116
s_nop 0
Ta 20,v1,16384
Ta 22,v1,16896
Mvaa 48,28,76
Ln v7,32,s65,128
Mvaa 40,32,112
C_ 199,142,143
C_ 198,140,141
Mvaa 40,28,72
s_nop 0
Ta 24,v1,20480
Ta 26,v1,20992
Mvaa 32,32,108
C_ 197,138,139
C_ 196,136,137
Mvaa 32,28,68
Ln v8,32,s65,3200
Mvaa 24,32,104
S_ m0,s51,1792
U_ v128,s48,v128
U_ v129,s48,v129
Mvaa 24,28,64
U_ v130,s48,v130
U_ v131,s48,v131
Mvaa 16,32,100
U_ v132,s48,v132
U_ v133,s48,v133
Mvaa 16,28,60
W_ 4
U_ v134,s48,v134
U_ v135,s48,v135
Mvaa 60,36,140
s_nop 0
Ta 28,%0,16392
Ta 30,%0,16904
Mvaa 60,224,180
Ln v7,32,s65,256
Mvaa 68,36,144
C_ 195,134,135
C_ 194,132,133
Mvaa 68,224,184
s_nop 0
Ta 32,%0,20488
Ta 34,%0,21000
Mvaa 76,36,148
C_ 193,130,131
C_ 192,128,129
Mvaa 76,224,188
Ln v8,32,s65,3328
Mvaa 84,36,152
U_ v120,s48,v120
U_ v121,s48,v121
Mvaa 84,224,192
U_ v122,s48,v122
U_ v123,s48,v123
Mvaa 92,36,156
U_ v124,s48,v124
U_ v125,s48,v125
Mvaa 92,224,196
W_ 4
U_ v126,s48,v126
U_ v127,s48,v127
Mvaa 88,24,196
s_nop 0
Ta 36,%0,16384
Ta 38,%0,16896
Mvaa 88,20,156
C_ 191,126,127
C_ 190,124,125
Mvaa 80,24,192
C_ 189,122,123
C_ 188,120,121
Mvaa 80,20,152
s_nop 0
Ta 224,%0,20480
Ta 226,%0,20992
Mvaa 72,24,188
U_ v112,s48,v112
U_ v113,s48,v113
Mvaa 72,20,148
U_ v114,s48,v114
U_ v115,s48,v115
Mvaa 64,24,184
U_ v116,s48,v116
U_ v117,s48,v117
Mvaa 64,20,144
U_ v118,s48,v118
U_ v119,s48,v119
Mvaa 56,24,180
s_waitcnt vmcnt(51) lgkmcnt(0)
s_barrier
s_cmp_eq_u32 s11, 1
s_cbranch_scc1 .Lpace29
.Lpace29_back:
C_ 187,118,119
Mvaa 56,20,140
S_ s65,s61,12288
C_ 186,116,117
Mvaa 20,28,140
S_ m0,s51,12288
C_ 185,114,115
C_ 184,112,113
Mvaa 20,32,180
Ln v9,36,s65,0
Mvaa 28,28,144
U_ v104,s48,v104
U_ v105,s48,v105
Mvaa 28,32,184
U_ v106,s48,v106
U_ v107,s48,v107
Mvaa 36,28,148
U_ v108,s48,v108
U_ v109,s48,v109
Mvaa 36,32,188
Ln v9,36,s65,3072
Mvaa 44,28,152
S_ m0,s51,13184
U_ v110,s48,v110
U_ v111,s48,v111
Mvaa 44,32,192
C_ 183,110,111
C_ 182,108,109
Mvaa 52,28,156
C_ 181,106,107
C_ 180,104,105
Mvaa 52,32,196
U_ v96,s48,v96
U_ v97,s48,v97
Mvaa 48,224,196
Ln v9,36,s65,128
Mvaa 48,36,156
U_ v98,s48,v98
U_ v99,s48,v99
Mvaa 40,224,192
U_ v100,s48,v100
U_ v101,s48,v101
Mvaa 40,36,152
U_ v102,s48,v102
U_ v103,s48,v103
Mvaa 32,224,188
Ln v9,36,s65,3200
Mvaa 32,36,148
S_ m0,s51,14080
C_ 179,102,103
C_ 178,100,101
Mvaa 24,224,184
C_ 177,98,99
C_ 176,96,97
Mvaa 24,36,144
v_pk_mul_f32 v[88:89], v[88:89], s[48:49]
v_pk_mul_f32 v[90:91], v[90:91], s[48:49]
Mvaa 16,224,180
Ln v9,36,s65,256
Mvaa 16,36,140
v_pk_mul_f32 v[92:93], v[92:93], s[48:49]
v_pk_mul_f32 v[94:95], v[94:95], s[48:49]
Ln v9,36,s65,3328
C_ 175,94,95
C_ 174,92,93
C_ 173,90,91
C_ 172,88,89
v_pk_mul_f32 v[80:81], v[80:81], s[48:49]
v_pk_mul_f32 v[82:83], v[82:83], s[48:49]
v_pk_mul_f32 v[84:85], v[84:85], s[48:49]
v_pk_mul_f32 v[86:87], v[86:87], s[48:49]
C_ 171,86,87
C_ 170,84,85
C_ 169,82,83
C_ 168,80,81
v_pk_mul_f32 v[72:73], v[72:73], s[48:49]
v_pk_mul_f32 v[74:75], v[74:75], s[48:49]
v_pk_mul_f32 v[76:77], v[76:77], s[48:49]
v_pk_mul_f32 v[78:79], v[78:79], s[48:49]
C_ 167,78,79
C_ 166,76,77
C_ 165,74,75
C_ 164,72,73
v_pk_mul_f32 v[40:41], v[40:41], s[48:49]
v_pk_mul_f32 v[42:43], v[42:43], s[48:49]
v_pk_mul_f32 v[44:45], v[44:45], s[48:49]
v_pk_mul_f32 v[46:47], v[46:47], s[48:49]
C_ 151,46,47
C_ 150,44,45
C_ 149,42,43
C_ 148,40,41
v_pk_mul_f32 v[32:33], v[32:33], s[48:49]
v_pk_mul_f32 v[34:35], v[34:35], s[48:49]
v_pk_mul_f32 v[36:37], v[36:37], s[48:49]
v_pk_mul_f32 v[38:39], v[38:39], s[48:49]
C_ 147,38,39
C_ 146,36,37
C_ 145,34,35
C_ 144,32,33
v_pk_mul_f32 v[24:25], v[24:25], s[48:49]
v_pk_mul_f32 v[26:27], v[26:27], s[48:49]
v_pk_mul_f32 v[28:29], v[28:29], s[48:49]
v_pk_mul_f32 v[30:31], v[30:31], s[48:49]
C_ 143,30,31
C_ 142,28,29
C_ 141,26,27
C_ 140,24,25
R_ %0,a244
v_xor_b32 v1, 64, %0
Dv 30,%0,43008
Dv 34,v1,43008
Dv 38,%0,44032
v_pk_mul_f32 v[64:65], v[64:65], s[48:49]
v_pk_mul_f32 v[66:67], v[66:67], s[48:49]
v_pk_mul_f32 v[68:69], v[68:69], s[48:49]
v_pk_mul_f32 v[70:71], v[70:71], s[48:49]
C_ 163,70,71
C_ 162,68,69
C_ 161,66,67
C_ 160,64,65
v_pk_mul_f32 v[56:57], v[56:57], s[48:49]
v_pk_mul_f32 v[58:59], v[58:59], s[48:49]
v_pk_mul_f32 v[60:61], v[60:61], s[48:49]
v_pk_mul_f32 v[62:63], v[62:63], s[48:49]
C_ 159,62,63
C_ 158,60,61
C_ 157,58,59
C_ 156,56,57
v_pk_mul_f32 v[48:49], v[48:49], s[48:49]
v_pk_mul_f32 v[50:51], v[50:51], s[48:49]
v_pk_mul_f32 v[52:53], v[52:53], s[48:49]
v_pk_mul_f32 v[54:55], v[54:55], s[48:49]
C_ 155,54,55
C_ 154,52,53
C_ 153,50,51
C_ 152,48,49
v_pk_mul_f32 v[16:17], v[16:17], s[48:49]
v_pk_mul_f32 v[18:19], v[18:19], s[48:49]
v_pk_mul_f32 v[20:21], v[20:21], s[48:49]
v_pk_mul_f32 v[22:23], v[22:23], s[48:49]
C_ 139,22,23
C_ 138,20,21
C_ 137,18,19
C_ 136,16,17
s_cmp_eq_u32 s58, 1
s_cbranch_scc1 .Latt1
s_cmp_eq_u32 s58, 2
s_cbranch_scc1 .Latt2
.Latt0:
s_cmp_eq_u32 s4, 0
s_cbranch_scc0 .Lhs_tok0
R_ v11,a241
buffer_load_dword a255, v11, s[92:95], 0 offen offset:0 sc1
.Lhs_tok0:
S_ s59,s59,768
W_ 2
Zvvv 96,30,136
Dv 42,v1,44032
R_ v2,a245
Zvvv 100,30,140
v_xor_b32 v3, 32, v2
v_xor_b32 v4, 64, v2
Zvvv 104,30,144
v_xor_b32 v5, 96, v2
Zvvv 108,30,148
Zvvv 112,30,152
W_ 2
Mvvv 96,34,156
Dv 30,%0,45056
Mvvv 100,34,160
Mvvv 104,34,164
Mvvv 108,34,168
Mvvv 112,34,172
W_ 2
Mvvv 96,38,176
Dv 34,v1,45056
Mvvv 100,38,180
Mvvv 104,38,184
Mvvv 108,38,188
Mvvv 112,38,192
W_ 2
Mvvv 96,42,196
Dv 38,%0,36864
Mvvv 100,42,200
Mvvv 104,42,204
Mvvv 108,42,208
Mvvv 112,42,212
W_ 2
Mvvv 96,30,216
Dv 42,v1,36864
Mvvv 100,30,220
Mvvv 104,30,224
Mvvv 108,30,228
Mvvv 112,30,232
W_ 2
Mvvv 96,34,236
Dv 30,%0,37888
Mvvv 100,34,240
Mvvv 104,34,244
Mvvv 108,34,248
Mvvv 112,34,252
W_ 2
s_nop 1
v_max3_f32 v56, v96, v97, v98
v_max_f32 v56, v56, v99
Zvvv 76,38,136
Dv 34,v1,37888
v_max3_f32 v60, v100, v101, v102
Zvvv 80,38,140
v_max_f32 v60, v60, v103
Zvvv 84,38,144
v_max3_f32 v64, v104, v105, v106
v_max_f32 v64, v64, v107
Zvvv 88,38,148
v_max3_f32 v68, v108, v109, v110
v_max_f32 v68, v68, v111
Zvvv 92,38,152
W_ 2
v_max3_f32 v72, v112, v113, v114
v_max_f32 v72, v72, v115
Mvvv 76,42,156
Dv 38,%0,38912
Mvvv 80,42,160
Mvvv 84,42,164
Mvvv 88,42,168
Mvvv 92,42,172
W_ 2
Mvvv 76,30,176
Dv 42,v1,38912
Mvvv 80,30,180
Mvvv 84,30,184
Mvvv 88,30,188
Mvvv 92,30,192
s_waitcnt vmcnt(34) lgkmcnt(0)
s_barrier
Mvvv 76,34,196
S_ s65,s61,36864
Dv 30,%0,55296
Mvvv 76,38,216
S_ m0,s51,36864
Mvvv 76,42,236
Ln v7,32,s65,0
Mvvv 80,34,200
Mvvv 80,38,220
Ln v8,32,s65,3072
Mvvv 80,42,240
S_ m0,s51,37760
Mvvv 84,34,204
Ln v7,32,s65,128
Mvvv 84,38,224
v_max3_f32 v56, v56, v76, v77
v_max3_f32 v56, v56, v78, v79
Mvvv 84,42,244
Ln v8,32,s65,3200
Mvvv 88,34,208
S_ m0,s51,38656
v_max3_f32 v60, v60, v80, v81
v_max3_f32 v60, v60, v82, v83
Mvvv 88,38,228
Ln v7,32,s65,256
Mvvv 88,42,248
Mvvv 92,34,212
Ln v8,32,s65,3328
Mvvv 92,38,232
v_max3_f32 v64, v64, v84, v85
v_max3_f32 v64, v64, v86, v87
Mvvv 92,42,252
Dv 34,v1,55296
.p2align 3
v_mov_b32 v11, 0x3f803f80
v_permlane32_swap_b32 v56, v60
v_max3_f32 v68, v68, v88, v89
v_max3_f32 v68, v68, v90, v91
v_max_f32 v56, v56, v60
s_nop 1
v_max3_f32 v72, v72, v92, v93
v_max3_f32 v72, v72, v94, v95
v_mov_b32 v73, v72
v_permlane32_swap_b32 v64, v68
v_max_f32 v64, v64, v68
v_permlane32_swap_b32 v72, v73
v_max_f32 v72, v72, v73
v_mov_b32 v73, v72
v_permlane16_swap_b32 v56, v64
v_max_f32 v56, v56, v64
v_permlane16_swap_b32 v72, v73
B_ v56,0xc0e00000,v56
v_max_f32 v72, v72, v73
v_mov_b32 v64, v56
B_ v72,0xc0e00000,v72
v_mov_b32 v73, v72
v_permlane16_swap_b32 v56, v64
v_mov_b32 v60, v56
v_mov_b32 v68, v64
v_mov_b64 v[74:75], v[72:73]
v_permlane32_swap_b32 v56, v60
v_permlane32_swap_b32 v64, v68
v_mov_b32 v57, v56
v_mov_b32 v61, v60
v_mov_b32 v65, v64
v_mov_b32 v69, v68
v_pk_add_f32 v[96:97], v[96:97], v[56:57]
v_pk_add_f32 v[98:99], v[98:99], v[56:57]
v_pk_add_f32 v[76:77], v[76:77], v[56:57]
v_pk_add_f32 v[78:79], v[78:79], v[56:57]
v_mov_b64 v[58:59], v[56:57]
v_pk_add_f32 v[100:101], v[100:101], v[60:61]
v_pk_add_f32 v[102:103], v[102:103], v[60:61]
v_pk_add_f32 v[80:81], v[80:81], v[60:61]
v_pk_add_f32 v[82:83], v[82:83], v[60:61]
v_mov_b64 v[62:63], v[60:61]
v_pk_add_f32 v[104:105], v[104:105], v[64:65]
v_pk_add_f32 v[106:107], v[106:107], v[64:65]
v_pk_add_f32 v[84:85], v[84:85], v[64:65]
v_pk_add_f32 v[86:87], v[86:87], v[64:65]
v_mov_b64 v[66:67], v[64:65]
v_pk_add_f32 v[108:109], v[108:109], v[68:69]
v_pk_add_f32 v[110:111], v[110:111], v[68:69]
v_pk_add_f32 v[88:89], v[88:89], v[68:69]
v_pk_add_f32 v[90:91], v[90:91], v[68:69]
v_mov_b64 v[70:71], v[68:69]
v_pk_add_f32 v[112:113], v[112:113], v[72:73]
v_pk_add_f32 v[114:115], v[114:115], v[72:73]
v_pk_add_f32 v[92:93], v[92:93], v[72:73]
v_pk_add_f32 v[94:95], v[94:95], v[72:73]
E_ 96
E_ 97
E_ 98
E_ 99
E_ 76
E_ 77
E_ 78
E_ 79
C_ 99,98,99
C_ 98,96,97
C_ 96,76,77
C_ 97,78,79
E_ 100
E_ 101
E_ 102
E_ 103
E_ 80
E_ 81
E_ 82
E_ 83
C_ 103,102,103
C_ 102,100,101
C_ 100,80,81
C_ 101,82,83
.Lstep0_1:
W_ 1
Nvvv 116,30,136,56
s_nop 0
Dv 38,%0,56320
Dv 42,v1,56320
W_ 2
Mvvv 116,34,156
E_ 104
Nvvv 120,30,140,60
E_ 105
Mvvv 120,34,160
E_ 106
Nvvv 124,30,144,64
E_ 107
Mvvv 124,34,164
E_ 84
Nvvv 128,30,148,68
E_ 85
Mvvv 128,34,168
E_ 86
Nvvv 132,30,152,72
E_ 87
Mvvv 132,34,172
W_ 0
C_ 107,106,107
C_ 106,104,105
Mvvv 132,38,192
s_nop 0
Dv 30,%0,57344
Dv 34,v1,57344
Mvvv 132,42,212
C_ 104,84,85
C_ 105,86,87
Mvvv 128,38,188
E_ 108
Mvvv 128,42,208
E_ 109
Mvvv 124,38,184
E_ 110
Mvvv 124,42,204
E_ 111
Mvvv 120,38,180
E_ 88
Mvvv 120,42,200
E_ 89
Mvvv 116,38,176
E_ 90
Mvvv 116,42,196
W_ 0
E_ 91
Mvvv 116,30,216
s_nop 0
Dv 38,%0,49152
Dv 42,v1,49152
Mvvv 116,34,236
C_ 111,110,111
C_ 110,108,109
Mvvv 120,30,220
C_ 108,88,89
C_ 109,90,91
Mvvv 120,34,240
E_ 112
Mvvv 124,30,224
E_ 113
Mvvv 124,34,244
E_ 114
Mvvv 128,30,228
E_ 115
Mvvv 128,34,248
E_ 92
Mvvv 132,30,232
E_ 93
Mvvv 132,34,252
W_ 0
E_ 94
Nvvv 76,38,136,56
s_nop 0
Dv 30,%0,50176
Dv 34,v1,50176
Mvvv 76,42,156
E_ 95
Nvvv 80,38,140,60
C_ 115,114,115
C_ 114,112,113
Mvvv 80,42,160
C_ 112,92,93
C_ 113,94,95
Nvvv 84,38,144,64
E_ 116
Mvvv 84,42,164
E_ 117
Nvvv 88,38,148,68
E_ 118
Mvvv 88,42,168
E_ 119
Nvvv 92,38,152,72
C_ 119,118,119
C_ 118,116,117
Mvvv 92,42,172
W_ 0
E_ 120
Mvvv 92,30,192
s_nop 0
Dv 38,%0,51200
Dv 42,v1,51200
Mvvv 92,34,212
X_ a180,v11
X_ a181,v11
Mvvv 88,30,188
X_ a182,v11
X_ a183,v11
Mvvv 88,34,208
E_ 121
Mvvv 84,30,184
E_ 122
Mvvv 84,34,204
E_ 123
Mvvv 80,30,180
C_ 123,122,123
C_ 122,120,121
Mvvv 80,34,200
E_ 124
Mvvv 76,30,176
E_ 125
Mvvv 76,34,196
S_ s65,s61,24576
E_ 126
W_ 1
Mvvv 76,38,216
S_ m0,s51,49152
E_ 127
W_ 0
Mvvv 76,42,236
s_waitcnt vmcnt(18) lgkmcnt(0)
s_barrier
C_ 127,126,127
Mvvv 80,38,220
L_ v9,36,s65,0
Mvvv 80,42,240
C_ 126,124,125
Mvvv 84,38,224
L_ v9,36,s65,3072
S_ m0,s51,50048
Mvvv 84,42,244
s_nop 0
Tv 30,v2,24576
Tv 32,v2,30720
Mvvv 88,38,228
L_ v9,36,s65,128
Mvvv 88,42,248
s_nop 0
Tv 34,v3,24576
Tv 36,v3,30720
Mvvv 92,38,232
L_ v9,36,s65,3200
S_ m0,s51,50944
Mvvv 92,42,252
E_ 128
Zaav 220,180,96
L_ v9,36,s65,256
Zaav 224,180,100
E_ 129
Zaav 228,180,104
L_ v9,36,s65,3328
Zaav 232,180,108
E_ 130
Zaav 236,180,112
W_ 0
E_ 131
Zavv 0,30,96
s_nop 0
Tv 38,v4,24576
Tv 40,v4,30720
Zavv 20,34,96
C_ 131,130,131
C_ 130,128,129
Zavv 24,34,100
s_nop 0
Tv 42,v5,24576
Tv 44,v5,30720
Zavv 4,30,100
E_ 132
Zavv 8,30,104
E_ 133
Zavv 28,34,104
R_ v46,a220
R_ v47,a224
Zavv 32,34,108
R_ v48,a228
R_ v49,a232
Zavv 12,30,108
R_ v50,a236
Zavv 16,30,112
E_ 134
Zavv 36,34,112
W_ 0
E_ 135
Zavv 56,38,112
s_nop 0
Tv 30,v2,25600
Tv 32,v2,31744
Zavv 76,42,112
C_ 135,134,135
C_ 134,132,133
Zavv 72,42,108
s_nop 0
Tv 34,v3,25600
Tv 36,v3,31744
Zavv 52,38,108
E_ 76
Zavv 48,38,104
E_ 77
Zavv 68,42,104
E_ 78
Zavv 64,42,100
E_ 79
Zavv 44,38,100
C_ 116,76,77
C_ 117,78,79
Zavv 40,38,96
E_ 80
Zavv 60,42,96
W_ 0
E_ 81
Zavv 80,30,96
s_nop 0
Tv 38,v4,25600
Tv 40,v4,31744
Zavv 100,34,96
E_ 82
Zavv 104,34,100
s_nop 0
Tv 42,v5,25600
Tv 44,v5,31744
Zavv 84,30,100
E_ 83
Zavv 88,30,104
C_ 120,80,81
C_ 121,82,83
Zavv 108,34,104
E_ 84
Zavv 112,34,108
E_ 85
Zavv 92,30,108
E_ 86
Zavv 96,30,112
E_ 87
Zavv 116,34,112
W_ 0
C_ 124,84,85
C_ 125,86,87
Zavv 136,38,112
s_nop 0
Tv 30,v2,26624
Tv 32,v2,32768
Zavv 156,42,112
E_ 88
Zavv 152,42,108
s_nop 0
Tv 34,v3,26624
Tv 36,v3,32768
Zavv 132,38,108
E_ 89
Zavv 128,38,104
E_ 90
Zavv 148,42,104
E_ 91
Zavv 144,42,100
C_ 128,88,89
C_ 129,90,91
Zavv 124,38,100
E_ 92
Zavv 120,38,96
E_ 93
Zavv 140,42,96
W_ 0
E_ 94
Zavv 160,30,96
s_nop 0
Tv 38,v4,26624
Tv 40,v4,32768
Zavv 180,34,96
E_ 95
Zavv 184,34,100
s_nop 0
Tv 42,v5,26624
Tv 44,v5,32768
Zavv 164,30,100
C_ 132,92,93
C_ 133,94,95
Zavv 168,30,104
v_mov_b32 v92, 0x3f803f80
v_mov_b32 v93, 0x3f803f80
Zavv 188,34,104
S_ s65,s61,49152
v_mov_b32 v94, 0x3f803f80
v_mov_b32 v95, 0x3f803f80
Zavv 192,34,108
S_ m0,s51,24576
Zavv 172,30,108
s_waitcnt vmcnt(18) lgkmcnt(0)
s_barrier
Zavv 176,30,112
Ln v7,32,s65,0
Zavv 196,34,112
Zavv 216,38,112
Dv 30,%0,6144
Zavv 236,42,112
Dv 34,v1,6144
Zavv 232,42,108
Ln v8,32,s65,3072
S_ m0,s51,25472
Zavv 212,38,108
Zavv 208,38,104
Ln v7,32,s65,128
Zavv 228,42,104
Zavv 224,42,100
Ln v8,32,s65,3200
S_ m0,s51,26368
Zavv 204,38,100
Zavv 200,38,96
Ln v7,32,s65,256
Zavv 220,42,96
Ln v8,32,s65,3328
.Lstep0_2:
Zvvv 76,92,116
Dv 38,%0,7168
Zvvv 80,92,120
Dv 42,v1,7168
Zvvv 84,92,124
Zvvv 88,92,128
Zvvv 92,92,132
W_ 2
Nvvv 96,30,136,56
Mvvv 96,34,156
Nvvv 100,30,140,60
s_nop 1
v_max3_f32 v10, v76, v80, v84
s_nop 0
v_max3_f32 v10, v10, v88, v92
Mvvv 100,34,160
v_cmp_lt_f32 vcc, 0x42000000, v10
s_cbranch_vccnz .Ltramp0_1
Nvvv 104,30,144,64
A_ v46,v46,v76
A_ v47,v47,v80
Mvvv 104,34,164
A_ v48,v48,v84
A_ v49,v49,v88
Nvvv 108,30,148,68
A_ v50,v50,v92
Mvvv 108,34,168
Nvvv 112,30,152,72
Mvvv 112,34,172
W_ 0
Mvvv 112,38,192
Dv 30,%0,8192
Mvvv 112,42,212
Dv 34,v1,8192
Mvvv 108,38,188
Mvvv 108,42,208
Mvvv 104,38,184
Mvvv 104,42,204
Mvvv 100,38,180
Mvvv 100,42,200
Mvvv 96,38,176
Mvvv 96,42,196
W_ 0
Mvvv 96,30,216
Dv 38,%0,0
Mvvv 96,34,236
Dv 42,v1,0
Mvvv 100,30,220
Mvvv 100,34,240
Mvvv 104,30,224
Mvvv 104,34,244
Mvvv 108,30,228
Mvvv 108,34,248
s_nop 0
E_ 96
Mvvv 112,30,232
E_ 97
Mvvv 112,34,252
W_ 0
E_ 98
Nvvv 76,38,136,56
Dv 30,%0,1024
Mvvv 76,42,156
Dv 34,v1,1024
Nvvv 80,38,140,60
E_ 99
Mvvv 80,42,160
C_ 99,98,99
C_ 98,96,97
Nvvv 84,38,144,64
E_ 100
Mvvv 84,42,164
E_ 101
Nvvv 88,38,148,68
E_ 102
Mvvv 88,42,168
E_ 103
Nvvv 92,38,152,72
C_ 103,102,103
C_ 102,100,101
Mvvv 92,42,172
W_ 0
E_ 104
Mvvv 92,30,192
Dv 38,%0,2048
Mvvv 92,34,212
Dv 42,v1,2048
Mvvv 88,30,188
E_ 105
Mvvv 88,34,208
E_ 106
Mvvv 84,30,184
S_ s65,s61,36864
E_ 107
Mvvv 84,34,204
S_ m0,s51,0
C_ 107,106,107
C_ 106,104,105
Mvvv 80,30,180
s_waitcnt vmcnt(18) lgkmcnt(0)
s_barrier
Mvvv 80,34,200
L_ v9,36,s65,0
Mvvv 76,30,176
E_ 108
Mvvv 76,34,196
L_ v9,36,s65,3072
S_ m0,s51,896
Mvvv 76,38,216
Tv 30,v2,12288
Tv 32,v2,18432
Mvvv 76,42,236
L_ v9,36,s65,128
Mvvv 80,38,220
s_nop 0
Tv 34,v3,12288
Tv 36,v3,18432
Mvvv 80,42,240
L_ v9,36,s65,3200
S_ m0,s51,1792
Mvvv 84,38,224
E_ 109
Mvvv 84,42,244
L_ v9,36,s65,256
Mvvv 88,38,228
E_ 110
Mvvv 88,42,248
L_ v9,36,s65,3328
Mvvv 92,38,232
E_ 111
Mvvv 92,42,252
W_ 0
C_ 111,110,111
C_ 110,108,109
Mavv 0,30,116
s_nop 0
Tv 38,v4,12288
Tv 40,v4,18432
Mavv 20,34,116
E_ 112
Mavv 24,34,120
s_nop 0
Tv 42,v5,12288
Tv 44,v5,18432
Mavv 4,30,120
E_ 113
Mavv 8,30,124
E_ 114
Mavv 28,34,124
E_ 115
Mavv 32,34,128
C_ 115,114,115
C_ 114,112,113
Mavv 12,30,128
E_ 76
Mavv 16,30,132
E_ 77
Mavv 36,34,132
W_ 0
E_ 78
Mavv 56,38,132
s_nop 0
Tv 30,v2,13312
Tv 32,v2,19456
Mavv 76,42,132
E_ 79
Mavv 72,42,128
s_nop 0
Tv 34,v3,13312
Tv 36,v3,19456
Mavv 52,38,128
C_ 96,76,77
C_ 97,78,79
Mavv 48,38,124
E_ 80
Mavv 68,42,124
E_ 81
Mavv 64,42,120
E_ 82
Mavv 44,38,120
E_ 83
Mavv 40,38,116
C_ 100,80,81
C_ 101,82,83
Mavv 60,42,116
W_ 0
E_ 84
Mavv 80,30,116
s_nop 0
Tv 38,v4,13312
Tv 40,v4,19456
Mavv 100,34,116
E_ 85
Mavv 104,34,120
s_nop 0
Tv 42,v5,13312
Tv 44,v5,19456
Mavv 84,30,120
E_ 86
Mavv 88,30,124
E_ 87
Mavv 108,34,124
C_ 104,84,85
C_ 105,86,87
Mavv 112,34,128
E_ 88
Mavv 92,30,128
E_ 89
Mavv 96,30,132
E_ 90
Mavv 116,34,132
W_ 0
E_ 91
Mavv 136,38,132
s_nop 0
Tv 30,v2,14336
Tv 32,v2,20480
Mavv 156,42,132
C_ 108,88,89
C_ 109,90,91
Mavv 152,42,128
s_nop 0
Tv 34,v3,14336
Tv 36,v3,20480
Mavv 132,38,128
E_ 92
Mavv 128,38,124
E_ 93
Mavv 148,42,124
E_ 94
Mavv 144,42,120
E_ 95
Mavv 124,38,120
C_ 112,92,93
C_ 113,94,95
Mavv 120,38,116
v_mov_b32 v92, 0x3f803f80
v_mov_b32 v93, 0x3f803f80
Mavv 140,42,116
W_ 0
v_mov_b32 v94, 0x3f803f80
v_mov_b32 v95, 0x3f803f80
Mavv 160,30,116
s_nop 0
Tv 38,v4,14336
Tv 40,v4,20480
Mavv 180,34,116
Mavv 184,34,120
s_nop 0
Tv 42,v5,14336
Tv 44,v5,20480
Mavv 164,30,120
Mavv 168,30,124
Mavv 188,34,124
S_ s65,s61,61440
Mavv 192,34,128
S_ m0,s51,12288
Mavv 172,30,128
s_waitcnt vmcnt(18) lgkmcnt(0)
s_barrier
Mavv 176,30,132
Ln v7,32,s65,0
Mavv 196,34,132
Mavv 216,38,132
Dv 30,%0,43008
Mavv 236,42,132
Dv 34,v1,43008
Mavv 232,42,128
Ln v8,32,s65,3072
S_ m0,s51,13184
Mavv 212,38,128
Mavv 208,38,124
Ln v7,32,s65,128
Mavv 228,42,124
Mavv 224,42,120
Ln v8,32,s65,3200
S_ m0,s51,14080
Mavv 204,38,120
Mavv 200,38,116
Ln v7,32,s65,256
Mavv 220,42,116
s_cmp_eq_u32 s4, 0
s_cbranch_scc1 .Lhs_a0
.Lhs_a0_back:
.Lstep0_3:
Zvvv 76,92,96
Dv 38,%0,44032
Zvvv 80,92,100
Dv 42,v1,44032
Zvvv 84,92,104
Zvvv 88,92,108
Zvvv 92,92,112
W_ 2
Nvvv 116,30,136,56
Mvvv 116,34,156
Nvvv 120,30,140,60
s_nop 1
v_max3_f32 v10, v76, v80, v84
s_nop 0
v_max3_f32 v10, v10, v88, v92
Mvvv 120,34,160
v_cmp_lt_f32 vcc, 0x42000000, v10
s_cbranch_vccnz .Ltramp0_2
Nvvv 124,30,144,64
A_ v46,v46,v76
A_ v47,v47,v80
Mvvv 124,34,164
A_ v48,v48,v84
A_ v49,v49,v88
Nvvv 128,30,148,68
A_ v50,v50,v92
Mvvv 128,34,168
Ln v8,32,s65,3328
Nvvv 132,30,152,72
Mvvv 132,34,172
W_ 0
Mvvv 132,38,192
Dv 30,%0,45056
Mvvv 132,42,212
Dv 34,v1,45056
Mvvv 128,38,188
Mvvv 128,42,208
Mvvv 124,38,184
Mvvv 124,42,204
Mvvv 120,38,180
Mvvv 120,42,200
Mvvv 116,38,176
Mvvv 116,42,196
W_ 0
Mvvv 116,30,216
Dv 38,%0,36864
Mvvv 116,34,236
Dv 42,v1,36864
Mvvv 120,30,220
Mvvv 120,34,240
Mvvv 124,30,224
Mvvv 124,34,244
Mvvv 128,30,228
Mvvv 128,34,248
s_nop 0
E_ 116
Mvvv 132,30,232
E_ 117
Mvvv 132,34,252
W_ 0
E_ 118
Nvvv 76,38,136,56
Dv 30,%0,37888
Mvvv 76,42,156
Dv 34,v1,37888
Nvvv 80,38,140,60
E_ 119
Mvvv 80,42,160
C_ 119,118,119
C_ 118,116,117
Nvvv 84,38,144,64
E_ 120
Mvvv 84,42,164
E_ 121
Nvvv 88,38,148,68
E_ 122
Mvvv 88,42,168
E_ 123
Nvvv 92,38,152,72
C_ 123,122,123
C_ 122,120,121
Mvvv 92,42,172
W_ 0
E_ 124
Mvvv 92,30,192
Dv 38,%0,38912
Mvvv 92,34,212
Dv 42,v1,38912
Mvvv 88,30,188
E_ 125
Mvvv 88,34,208
E_ 126
Mvvv 84,30,184
S_ s65,s61,49152
E_ 127
Mvvv 84,34,204
S_ m0,s51,36864
C_ 127,126,127
C_ 126,124,125
Mvvv 80,30,180
s_waitcnt vmcnt(18) lgkmcnt(0)
s_barrier
Mvvv 80,34,200
L_ v9,36,s65,0
Mvvv 76,30,176
E_ 128
Mvvv 76,34,196
L_ v9,36,s65,3072
S_ m0,s51,37760
Mvvv 76,38,216
Tv 30,v2,49152
Tv 32,v2,55296
Mvvv 76,42,236
L_ v9,36,s65,128
Mvvv 80,38,220
s_nop 0
Tv 34,v3,49152
Tv 36,v3,55296
Mvvv 80,42,240
L_ v9,36,s65,3200
S_ m0,s51,38656
Mvvv 84,38,224
E_ 129
Mvvv 84,42,244
L_ v9,36,s65,256
Mvvv 88,38,228
E_ 130
Mvvv 88,42,248
L_ v9,36,s65,3328
Mvvv 92,38,232
E_ 131
Mvvv 92,42,252
W_ 0
C_ 131,130,131
C_ 130,128,129
Mavv 0,30,96
s_nop 0
Tv 38,v4,49152
Tv 40,v4,55296
Mavv 20,34,96
E_ 132
Mavv 24,34,100
s_nop 0
Tv 42,v5,49152
Tv 44,v5,55296
Mavv 4,30,100
E_ 133
Mavv 8,30,104
E_ 134
Mavv 28,34,104
E_ 135
Mavv 32,34,108
C_ 135,134,135
C_ 134,132,133
Mavv 12,30,108
E_ 76
Mavv 16,30,112
E_ 77
Mavv 36,34,112
W_ 0
E_ 78
Mavv 56,38,112
s_nop 0
Tv 30,v2,50176
Tv 32,v2,56320
Mavv 76,42,112
E_ 79
Mavv 72,42,108
s_nop 0
Tv 34,v3,50176
Tv 36,v3,56320
Mavv 52,38,108
C_ 116,76,77
C_ 117,78,79
Mavv 48,38,104
E_ 80
Mavv 68,42,104
E_ 81
Mavv 64,42,100
E_ 82
Mavv 44,38,100
E_ 83
Mavv 40,38,96
C_ 120,80,81
C_ 121,82,83
Mavv 60,42,96
W_ 0
E_ 84
Mavv 80,30,96
s_nop 0
Tv 38,v4,50176
Tv 40,v4,56320
Mavv 100,34,96
E_ 85
Mavv 104,34,100
s_nop 0
Tv 42,v5,50176
Tv 44,v5,56320
Mavv 84,30,100
E_ 86
Mavv 88,30,104
E_ 87
Mavv 108,34,104
C_ 124,84,85
C_ 125,86,87
Mavv 112,34,108
E_ 88
Mavv 92,30,108
E_ 89
Mavv 96,30,112
E_ 90
Mavv 116,34,112
W_ 0
E_ 91
Mavv 136,38,112
s_nop 0
Tv 30,v2,51200
Tv 32,v2,57344
Mavv 156,42,112
C_ 128,88,89
C_ 129,90,91
Mavv 152,42,108
s_nop 0
Tv 34,v3,51200
Tv 36,v3,57344
Mavv 132,38,108
E_ 92
Mavv 128,38,104
E_ 93
Mavv 148,42,104
E_ 94
Mavv 144,42,100
E_ 95
Mavv 124,38,100
C_ 132,92,93
C_ 133,94,95
Mavv 120,38,96
v_mov_b32 v92, 0x3f803f80
v_mov_b32 v93, 0x3f803f80
Mavv 140,42,96
W_ 0
v_mov_b32 v94, 0x3f803f80
v_mov_b32 v95, 0x3f803f80
Mavv 160,30,96
s_nop 0
Tv 38,v4,51200
Tv 40,v4,57344
Mavv 180,34,96
Mavv 184,34,100
s_nop 0
Tv 42,v5,51200
Tv 44,v5,57344
Mavv 164,30,100
Mavv 168,30,104
Mavv 188,34,104
S_ s65,s61,73728
Mavv 192,34,108
S_ m0,s51,49152
Mavv 172,30,108
s_waitcnt vmcnt(18) lgkmcnt(0)
s_barrier
Mavv 176,30,112
Ln v7,32,s65,0
Mavv 196,34,112
Mavv 216,38,112
Dv 30,%0,30720
Mavv 236,42,112
Dv 34,v1,30720
Mavv 232,42,108
Ln v8,32,s65,3072
S_ m0,s51,50048
Mavv 212,38,108
Mavv 208,38,104
Ln v7,32,s65,128
Mavv 228,42,104
Mavv 224,42,100
Ln v8,32,s65,3200
S_ m0,s51,50944
Mavv 204,38,100
Mavv 200,38,96
Ln v7,32,s65,256
Mavv 220,42,96
.Lstep0_4:
Zvvv 76,92,116
Dv 38,%0,31744
Zvvv 80,92,120
Dv 42,v1,31744
Zvvv 84,92,124
Zvvv 88,92,128
Zvvv 92,92,132
W_ 2
Nvvv 96,30,136,56
Mvvv 96,34,156
Nvvv 100,30,140,60
s_nop 1
v_max3_f32 v10, v76, v80, v84
s_nop 0
v_max3_f32 v10, v10, v88, v92
Mvvv 100,34,160
v_cmp_lt_f32 vcc, 0x42000000, v10
s_cbranch_vccnz .Ltramp0_3
Nvvv 104,30,144,64
A_ v46,v46,v76
A_ v47,v47,v80
Mvvv 104,34,164
A_ v48,v48,v84
A_ v49,v49,v88
Nvvv 108,30,148,68
A_ v50,v50,v92
Mvvv 108,34,168
Ln v8,32,s65,3328
Nvvv 112,30,152,72
Mvvv 112,34,172
W_ 0
Mvvv 112,38,192
Dv 30,%0,32768
Mvvv 112,42,212
Dv 34,v1,32768
Mvvv 108,38,188
Mvvv 108,42,208
Mvvv 104,38,184
Mvvv 104,42,204
Mvvv 100,38,180
Mvvv 100,42,200
Mvvv 96,38,176
Mvvv 96,42,196
W_ 0
Mvvv 96,30,216
Dv 38,%0,24576
Mvvv 96,34,236
Dv 42,v1,24576
Mvvv 100,30,220
Mvvv 100,34,240
Mvvv 104,30,224
Mvvv 104,34,244
Mvvv 108,30,228
Mvvv 108,34,248
s_nop 0
E_ 96
Mvvv 112,30,232
E_ 97
Mvvv 112,34,252
W_ 0
E_ 98
Nvvv 76,38,136,56
Dv 30,%0,25600
Mvvv 76,42,156
Dv 34,v1,25600
Nvvv 80,38,140,60
E_ 99
Mvvv 80,42,160
C_ 99,98,99
C_ 98,96,97
Nvvv 84,38,144,64
E_ 100
Mvvv 84,42,164
E_ 101
Nvvv 88,38,148,68
E_ 102
Mvvv 88,42,168
E_ 103
Nvvv 92,38,152,72
C_ 103,102,103
C_ 102,100,101
Mvvv 92,42,172
W_ 0
E_ 104
Mvvv 92,30,192
Dv 38,%0,26624
Mvvv 92,34,212
Dv 42,v1,26624
Mvvv 88,30,188
E_ 105
Mvvv 88,34,208
E_ 106
Mvvv 84,30,184
S_ s65,s61,61440
E_ 107
Mvvv 84,34,204
S_ m0,s51,24576
C_ 107,106,107
C_ 106,104,105
Mvvv 80,30,180
s_waitcnt vmcnt(18) lgkmcnt(0)
s_barrier
Mvvv 80,34,200
L_ v9,36,s65,0
Mvvv 76,30,176
E_ 108
Mvvv 76,34,196
L_ v9,36,s65,3072
S_ m0,s51,25472
Mvvv 76,38,216
Tv 30,v2,0
Tv 32,v2,6144
Mvvv 76,42,236
L_ v9,36,s65,128
Mvvv 80,38,220
s_nop 0
Tv 34,v3,0
Tv 36,v3,6144
Mvvv 80,42,240
L_ v9,36,s65,3200
S_ m0,s51,26368
Mvvv 84,38,224
E_ 109
Mvvv 84,42,244
L_ v9,36,s65,256
Mvvv 88,38,228
E_ 110
Mvvv 88,42,248
L_ v9,36,s65,3328
Mvvv 92,38,232
E_ 111
Mvvv 92,42,252
W_ 0
C_ 111,110,111
C_ 110,108,109
Mavv 0,30,116
s_nop 0
Tv 38,v4,0
Tv 40,v4,6144
Mavv 20,34,116
E_ 112
Mavv 24,34,120
s_nop 0
Tv 42,v5,0
Tv 44,v5,6144
Mavv 4,30,120
E_ 113
Mavv 8,30,124
E_ 114
Mavv 28,34,124
E_ 115
Mavv 32,34,128
C_ 115,114,115
C_ 114,112,113
Mavv 12,30,128
E_ 76
Mavv 16,30,132
E_ 77
Mavv 36,34,132
W_ 0
E_ 78
Mavv 56,38,132
s_nop 0
Tv 30,v2,1024
Tv 32,v2,7168
Mavv 76,42,132
E_ 79
Mavv 72,42,128
s_nop 0
Tv 34,v3,1024
Tv 36,v3,7168
Mavv 52,38,128
C_ 96,76,77
C_ 97,78,79
Mavv 48,38,124
E_ 80
Mavv 68,42,124
E_ 81
Mavv 64,42,120
E_ 82
Mavv 44,38,120
E_ 83
Mavv 40,38,116
C_ 100,80,81
C_ 101,82,83
Mavv 60,42,116
W_ 0
E_ 84
Mavv 80,30,116
s_nop 0
Tv 38,v4,1024
Tv 40,v4,7168
Mavv 100,34,116
E_ 85
Mavv 104,34,120
s_nop 0
Tv 42,v5,1024
Tv 44,v5,7168
Mavv 84,30,120
E_ 86
Mavv 88,30,124
E_ 87
Mavv 108,34,124
C_ 104,84,85
C_ 105,86,87
Mavv 112,34,128
E_ 88
Mavv 92,30,128
E_ 89
Mavv 96,30,132
E_ 90
Mavv 116,34,132
W_ 0
E_ 91
Mavv 136,38,132
s_nop 0
Tv 30,v2,2048
Tv 32,v2,8192
Mavv 156,42,132
C_ 108,88,89
C_ 109,90,91
Mavv 152,42,128
s_nop 0
Tv 34,v3,2048
Tv 36,v3,8192
Mavv 132,38,128
E_ 92
Mavv 128,38,124
E_ 93
Mavv 148,42,124
E_ 94
Mavv 144,42,120
E_ 95
Mavv 124,38,120
C_ 112,92,93
C_ 113,94,95
Mavv 120,38,116
v_mov_b32 v92, 0x3f803f80
v_mov_b32 v93, 0x3f803f80
Mavv 140,42,116
W_ 0
v_mov_b32 v94, 0x3f803f80
v_mov_b32 v95, 0x3f803f80
Mavv 160,30,116
s_nop 0
Tv 38,v4,2048
Tv 40,v4,8192
Mavv 180,34,116
Mavv 184,34,120
s_nop 0
Tv 42,v5,2048
Tv 44,v5,8192
Mavv 164,30,120
Mavv 168,30,124
Mavv 188,34,124
S_ s65,s61,86016
Mavv 192,34,128
S_ m0,s51,0
Mavv 172,30,128
s_waitcnt vmcnt(18) lgkmcnt(0)
s_barrier
Mavv 176,30,132
Ln v7,32,s65,0
Mavv 196,34,132
Mavv 216,38,132
Dv 30,%0,18432
Mavv 236,42,132
Dv 34,v1,18432
Mavv 232,42,128
Ln v8,32,s65,3072
S_ m0,s51,896
Mavv 212,38,128
Mavv 208,38,124
Ln v7,32,s65,128
Mavv 228,42,124
Mavv 224,42,120
Ln v8,32,s65,3200
S_ m0,s51,1792
Mavv 204,38,120
Mavv 200,38,116
Ln v7,32,s65,256
Mavv 220,42,116
.Lstep0_5:
Zvvv 76,92,96
Dv 38,%0,19456
Zvvv 80,92,100
Dv 42,v1,19456
Zvvv 84,92,104
Zvvv 88,92,108
Zvvv 92,92,112
W_ 2
Nvvv 116,30,136,56
Mvvv 116,34,156
Nvvv 120,30,140,60
s_nop 1
v_max3_f32 v10, v76, v80, v84
s_nop 0
v_max3_f32 v10, v10, v88, v92
Mvvv 120,34,160
v_cmp_lt_f32 vcc, 0x42000000, v10
s_cbranch_vccnz .Ltramp0_4
Nvvv 124,30,144,64
A_ v46,v46,v76
A_ v47,v47,v80
Mvvv 124,34,164
A_ v48,v48,v84
A_ v49,v49,v88
Nvvv 128,30,148,68
A_ v50,v50,v92
Mvvv 128,34,168
Ln v8,32,s65,3328
Nvvv 132,30,152,72
Mvvv 132,34,172
W_ 0
Mvvv 132,38,192
Dv 30,%0,20480
Mvvv 132,42,212
Dv 34,v1,20480
Mvvv 128,38,188
Mvvv 128,42,208
Mvvv 124,38,184
Mvvv 124,42,204
Mvvv 120,38,180
Mvvv 120,42,200
Mvvv 116,38,176
Mvvv 116,42,196
W_ 0
Mvvv 116,30,216
Dv 38,%0,12288
Mvvv 116,34,236
Dv 42,v1,12288
Mvvv 120,30,220
Mvvv 120,34,240
Mvvv 124,30,224
Mvvv 124,34,244
Mvvv 128,30,228
Mvvv 128,34,248
s_nop 0
E_ 116
Mvvv 132,30,232
E_ 117
Mvvv 132,34,252
W_ 0
E_ 118
Nvvv 76,38,136,56
Dv 30,%0,13312
Mvvv 76,42,156
Dv 34,v1,13312
Nvvv 80,38,140,60
E_ 119
Mvvv 80,42,160
C_ 119,118,119
C_ 118,116,117
Nvvv 84,38,144,64
E_ 120
Mvvv 84,42,164
E_ 121
Nvvv 88,38,148,68
E_ 122
Mvvv 88,42,168
E_ 123
Nvvv 92,38,152,72
C_ 123,122,123
C_ 122,120,121
Mvvv 92,42,172
W_ 0
E_ 124
Mvvv 92,30,192
Dv 38,%0,14336
Mvvv 92,34,212
Dv 42,v1,14336
Mvvv 88,30,188
E_ 125
Mvvv 88,34,208
E_ 126
Mvvv 84,30,184
S_ s65,s61,73728
E_ 127
Mvvv 84,34,204
S_ m0,s51,12288
C_ 127,126,127
C_ 126,124,125
Mvvv 80,30,180
s_waitcnt vmcnt(18) lgkmcnt(0)
s_barrier
Mvvv 80,34,200
L_ v9,36,s65,0
Mvvv 76,30,176
E_ 128
Mvvv 76,34,196
L_ v9,36,s65,3072
S_ m0,s51,13184
Mvvv 76,38,216
Tv 30,v2,36864
Tv 32,v2,43008
Mvvv 76,42,236
L_ v9,36,s65,128
Mvvv 80,38,220
s_nop 0
Tv 34,v3,36864
Tv 36,v3,43008
Mvvv 80,42,240
L_ v9,36,s65,3200
S_ m0,s51,14080
Mvvv 84,38,224
E_ 129
Mvvv 84,42,244
L_ v9,36,s65,256
Mvvv 88,38,228
E_ 130
Mvvv 88,42,248
L_ v9,36,s65,3328
Mvvv 92,38,232
E_ 131
Mvvv 92,42,252
W_ 0
C_ 131,130,131
C_ 130,128,129
Mavv 0,30,96
s_nop 0
Tv 38,v4,36864
Tv 40,v4,43008
Mavv 20,34,96
E_ 132
Mavv 24,34,100
s_nop 0
Tv 42,v5,36864
Tv 44,v5,43008
Mavv 4,30,100
E_ 133
Mavv 8,30,104
)A"
R"A(E_ 134
Mavv 28,34,104
E_ 135
Mavv 32,34,108
C_ 135,134,135
C_ 134,132,133
Mavv 12,30,108
E_ 76
Mavv 16,30,112
E_ 77
Mavv 36,34,112
W_ 0
E_ 78
Mavv 56,38,112
s_nop 0
Tv 30,v2,37888
Tv 32,v2,44032
Mavv 76,42,112
E_ 79
Mavv 72,42,108
s_nop 0
Tv 34,v3,37888
Tv 36,v3,44032
Mavv 52,38,108
C_ 116,76,77
C_ 117,78,79
Mavv 48,38,104
E_ 80
Mavv 68,42,104
E_ 81
Mavv 64,42,100
E_ 82
Mavv 44,38,100
E_ 83
Mavv 40,38,96
C_ 120,80,81
C_ 121,82,83
Mavv 60,42,96
W_ 0
E_ 84
Mavv 80,30,96
s_nop 0
Tv 38,v4,37888
Tv 40,v4,44032
Mavv 100,34,96
E_ 85
Mavv 104,34,100
s_nop 0
Tv 42,v5,37888
Tv 44,v5,44032
Mavv 84,30,100
E_ 86
Mavv 88,30,104
E_ 87
Mavv 108,34,104
C_ 124,84,85
C_ 125,86,87
Mavv 112,34,108
E_ 88
Mavv 92,30,108
E_ 89
Mavv 96,30,112
E_ 90
Mavv 116,34,112
W_ 0
E_ 91
Mavv 136,38,112
s_nop 0
Tv 30,v2,38912
Tv 32,v2,45056
Mavv 156,42,112
C_ 128,88,89
C_ 129,90,91
Mavv 152,42,108
s_nop 0
Tv 34,v3,38912
Tv 36,v3,45056
Mavv 132,38,108
E_ 92
Mavv 128,38,104
E_ 93
Mavv 148,42,104
E_ 94
Mavv 144,42,100
E_ 95
Mavv 124,38,100
C_ 132,92,93
C_ 133,94,95
Mavv 120,38,96
v_mov_b32 v92, 0x3f803f80
v_mov_b32 v93, 0x3f803f80
Mavv 140,42,96
W_ 0
v_mov_b32 v94, 0x3f803f80
v_mov_b32 v95, 0x3f803f80
Mavv 160,30,96
s_nop 0
Tv 38,v4,38912
Tv 40,v4,45056
Mavv 180,34,96
Mavv 184,34,100
s_nop 0
Tv 42,v5,38912
Tv 44,v5,45056
Mavv 164,30,100
Mavv 168,30,104
Mavv 188,34,104
S_ s65,s61,98304
Mavv 192,34,108
S_ m0,s51,36864
Mavv 172,30,108
s_waitcnt vmcnt(18) lgkmcnt(0)
s_barrier
Mavv 176,30,112
Ln v7,32,s65,0
Mavv 196,34,112
Mavv 216,38,112
Dv 30,%0,55296
Mavv 236,42,112
Dv 34,v1,55296
Mavv 232,42,108
Ln v8,32,s65,3072
S_ m0,s51,37760
Mavv 212,38,108
Mavv 208,38,104
Ln v7,32,s65,128
Mavv 228,42,104
Mavv 224,42,100
Ln v8,32,s65,3200
S_ m0,s51,38656
Mavv 204,38,100
Mavv 200,38,96
Ln v7,32,s65,256
Mavv 220,42,96
.Lstep0_6:
Zvvv 76,92,116
Dv 38,%0,56320
Zvvv 80,92,120
Dv 42,v1,56320
Zvvv 84,92,124
Zvvv 88,92,128
Zvvv 92,92,132
W_ 2
Nvvv 96,30,136,56
Mvvv 96,34,156
Nvvv 100,30,140,60
s_nop 1
v_max3_f32 v10, v76, v80, v84
s_nop 0
v_max3_f32 v10, v10, v88, v92
Mvvv 100,34,160
v_cmp_lt_f32 vcc, 0x42000000, v10
s_cbranch_vccnz .Ltramp0_5
Nvvv 104,30,144,64
A_ v46,v46,v76
A_ v47,v47,v80
Mvvv 104,34,164
A_ v48,v48,v84
A_ v49,v49,v88
Nvvv 108,30,148,68
A_ v50,v50,v92
Mvvv 108,34,168
Ln v8,32,s65,3328
Nvvv 112,30,152,72
R_ v11,a248
Mvvv 112,34,172
W_ 0
v_min_u32 v11, 240, v11
Mvvv 112,38,192
Dv 30,%0,57344
v_lshlrev_b32 v11, 3, v11
Mvvv 112,42,212
Dv 34,v1,57344
Mvvv 108,38,188
buffer_load_dword a254, v11, s[44:47], s66 offen
Mvvv 108,42,208
Mvvv 104,38,184
Mvvv 104,42,204
Mvvv 100,38,180
Mvvv 100,42,200
Mvvv 96,38,176
Mvvv 96,42,196
W_ 0
Mvvv 96,30,216
Dv 38,%0,49152
Mvvv 96,34,236
Dv 42,v1,49152
Mvvv 100,30,220
Mvvv 100,34,240
Mvvv 104,30,224
Mvvv 104,34,244
Mvvv 108,30,228
Mvvv 108,34,248
s_nop 0
E_ 96
Mvvv 112,30,232
E_ 97
Mvvv 112,34,252
W_ 0
E_ 98
Nvvv 76,38,136,56
Dv 30,%0,50176
Mvvv 76,42,156
Dv 34,v1,50176
Nvvv 80,38,140,60
E_ 99
Mvvv 80,42,160
C_ 99,98,99
C_ 98,96,97
Nvvv 84,38,144,64
E_ 100
Mvvv 84,42,164
E_ 101
Nvvv 88,38,148,68
E_ 102
Mvvv 88,42,168
E_ 103
Nvvv 92,38,152,72
C_ 103,102,103
C_ 102,100,101
Mvvv 92,42,172
W_ 0
E_ 104
Mvvv 92,30,192
Dv 38,%0,51200
Mvvv 92,34,212
Dv 42,v1,51200
Mvvv 88,30,188
E_ 105
Mvvv 88,34,208
E_ 106
Mvvv 84,30,184
S_ s65,s61,86016
E_ 107
Mvvv 84,34,204
S_ m0,s51,49152
C_ 107,106,107
C_ 106,104,105
Mvvv 80,30,180
s_waitcnt vmcnt(19) lgkmcnt(0)
s_barrier
Mvvv 80,34,200
L_ v9,36,s65,0
Mvvv 76,30,176
E_ 108
Mvvv 76,34,196
L_ v9,36,s65,3072
S_ m0,s51,50048
Mvvv 76,38,216
Tv 30,v2,24576
Tv 32,v2,30720
Mvvv 76,42,236
L_ v9,36,s65,128
Mvvv 80,38,220
s_nop 0
Tv 34,v3,24576
Tv 36,v3,30720
Mvvv 80,42,240
L_ v9,36,s65,3200
S_ m0,s51,50944
Mvvv 84,38,224
E_ 109
Mvvv 84,42,244
L_ v9,36,s65,256
Mvvv 88,38,228
E_ 110
Mvvv 88,42,248
L_ v9,36,s65,3328
Mvvv 92,38,232
E_ 111
Mvvv 92,42,252
W_ 0
C_ 111,110,111
C_ 110,108,109
Mavv 0,30,116
s_nop 0
Tv 38,v4,24576
Tv 40,v4,30720
Mavv 20,34,116
E_ 112
Mavv 24,34,120
s_nop 0
Tv 42,v5,24576
Tv 44,v5,30720
Mavv 4,30,120
E_ 113
Mavv 8,30,124
E_ 114
Mavv 28,34,124
E_ 115
Mavv 32,34,128
C_ 115,114,115
C_ 114,112,113
Mavv 12,30,128
E_ 76
Mavv 16,30,132
E_ 77
Mavv 36,34,132
W_ 0
E_ 78
Mavv 56,38,132
s_nop 0
Tv 30,v2,25600
Tv 32,v2,31744
Mavv 76,42,132
E_ 79
Mavv 72,42,128
s_nop 0
Tv 34,v3,25600
Tv 36,v3,31744
Mavv 52,38,128
C_ 96,76,77
C_ 97,78,79
Mavv 48,38,124
E_ 80
Mavv 68,42,124
E_ 81
Mavv 64,42,120
E_ 82
Mavv 44,38,120
E_ 83
Mavv 40,38,116
C_ 100,80,81
C_ 101,82,83
Mavv 60,42,116
W_ 0
E_ 84
Mavv 80,30,116
s_nop 0
Tv 38,v4,25600
Tv 40,v4,31744
Mavv 100,34,116
E_ 85
Mavv 104,34,120
s_nop 0
Tv 42,v5,25600
Tv 44,v5,31744
Mavv 84,30,120
E_ 86
Mavv 88,30,124
E_ 87
Mavv 108,34,124
C_ 104,84,85
C_ 105,86,87
Mavv 112,34,128
E_ 88
Mavv 92,30,128
E_ 89
Mavv 96,30,132
E_ 90
Mavv 116,34,132
W_ 0
E_ 91
Mavv 136,38,132
s_nop 0
Tv 30,v2,26624
Tv 32,v2,32768
Mavv 156,42,132
C_ 108,88,89
C_ 109,90,91
Mavv 152,42,128
s_nop 0
Tv 34,v3,26624
Tv 36,v3,32768
Mavv 132,38,128
E_ 92
Mavv 128,38,124
E_ 93
Mavv 148,42,124
E_ 94
Mavv 144,42,120
E_ 95
Mavv 124,38,120
C_ 112,92,93
C_ 113,94,95
Mavv 120,38,116
v_mov_b32 v92, 0x3f803f80
v_mov_b32 v93, 0x3f803f80
Mavv 140,42,116
W_ 0
v_mov_b32 v94, 0x3f803f80
v_mov_b32 v95, 0x3f803f80
Mavv 160,30,116
s_nop 0
Tv 38,v4,26624
Tv 40,v4,32768
Mavv 180,34,116
Mavv 184,34,120
s_nop 0
Tv 42,v5,26624
Tv 44,v5,32768
Mavv 164,30,120
Mavv 168,30,124
Mavv 188,34,124
S_ s65,s61,110592
Mavv 192,34,128
S_ m0,s51,24576
Mavv 172,30,128
s_waitcnt vmcnt(19) lgkmcnt(0)
s_barrier
Mavv 176,30,132
Ln v7,32,s65,0
Mavv 196,34,132
Mavv 216,38,132
Dv 30,%0,6144
Mavv 236,42,132
Dv 34,v1,6144
Mavv 232,42,128
Ln v8,32,s65,3072
S_ m0,s51,25472
Mavv 212,38,128
Mavv 208,38,124
Ln v7,32,s65,128
Mavv 228,42,124
Mavv 224,42,120
Ln v8,32,s65,3200
S_ m0,s51,26368
Mavv 204,38,120
Mavv 200,38,116
Ln v7,32,s65,256
Mavv 220,42,116
s_cmp_eq_u32 s4, 0
s_cbranch_scc1 .Lhs_c0
.Lhs_c0_back:
.Lstep0_7:
Zvvv 76,92,96
Dv 38,%0,7168
Zvvv 80,92,100
Dv 42,v1,7168
Zvvv 84,92,104
Zvvv 88,92,108
Zvvv 92,92,112
W_ 2
Nvvv 116,30,136,56
Mvvv 116,34,156
Nvvv 120,30,140,60
s_nop 1
v_max3_f32 v10, v76, v80, v84
s_nop 0
v_max3_f32 v10, v10, v88, v92
Mvvv 120,34,160
v_cmp_lt_f32 vcc, 0x42000000, v10
s_cbranch_vccnz .Ltramp0_6
Nvvv 124,30,144,64
A_ v46,v46,v76
A_ v47,v47,v80
Mvvv 124,34,164
A_ v48,v48,v84
A_ v49,v49,v88
Nvvv 128,30,148,68
A_ v50,v50,v92
Mvvv 128,34,168
Ln v8,32,s65,3328
Nvvv 132,30,152,72
Mvvv 132,34,172
W_ 0
Mvvv 132,38,192
Dv 30,%0,8192
Mvvv 132,42,212
Dv 34,v1,8192
Mvvv 128,38,188
Mvvv 128,42,208
Mvvv 124,38,184
Mvvv 124,42,204
Mvvv 120,38,180
Mvvv 120,42,200
Mvvv 116,38,176
Mvvv 116,42,196
W_ 0
Mvvv 116,30,216
Dv 38,%0,0
Mvvv 116,34,236
Dv 42,v1,0
Mvvv 120,30,220
Mvvv 120,34,240
Mvvv 124,30,224
Mvvv 124,34,244
Mvvv 128,30,228
Mvvv 128,34,248
s_nop 0
E_ 116
Mvvv 132,30,232
E_ 117
Mvvv 132,34,252
W_ 0
E_ 118
Nvvv 76,38,136,56
Dv 30,%0,1024
Mvvv 76,42,156
Dv 34,v1,1024
Nvvv 80,38,140,60
E_ 119
Mvvv 80,42,160
C_ 119,118,119
C_ 118,116,117
Nvvv 84,38,144,64
E_ 120
Mvvv 84,42,164
E_ 121
Nvvv 88,38,148,68
E_ 122
Mvvv 88,42,168
E_ 123
Nvvv 92,38,152,72
C_ 123,122,123
C_ 122,120,121
Mvvv 92,42,172
W_ 0
E_ 124
Mvvv 92,30,192
Dv 38,%0,2048
Mvvv 92,34,212
Dv 42,v1,2048
Mvvv 88,30,188
E_ 125
Mvvv 88,34,208
E_ 126
Mvvv 84,30,184
S_ s65,s61,98304
E_ 127
Mvvv 84,34,204
S_ m0,s51,0
C_ 127,126,127
C_ 126,124,125
Mvvv 80,30,180
s_waitcnt vmcnt(19) lgkmcnt(0)
s_barrier
Mvvv 80,34,200
L_ v9,36,s65,0
Mvvv 76,30,176
E_ 128
Mvvv 76,34,196
L_ v9,36,s65,3072
S_ m0,s51,896
Mvvv 76,38,216
Tv 30,v2,12288
Tv 32,v2,18432
Mvvv 76,42,236
L_ v9,36,s65,128
Mvvv 80,38,220
s_nop 0
Tv 34,v3,12288
Tv 36,v3,18432
Mvvv 80,42,240
L_ v9,36,s65,3200
S_ m0,s51,1792
Mvvv 84,38,224
E_ 129
Mvvv 84,42,244
L_ v9,36,s65,256
Mvvv 88,38,228
E_ 130
Mvvv 88,42,248
L_ v9,36,s65,3328
Mvvv 92,38,232
E_ 131
Mvvv 92,42,252
W_ 0
C_ 131,130,131
C_ 130,128,129
Mavv 0,30,96
s_nop 0
Tv 38,v4,12288
Tv 40,v4,18432
Mavv 20,34,96
E_ 132
Mavv 24,34,100
s_nop 0
Tv 42,v5,12288
Tv 44,v5,18432
Mavv 4,30,100
E_ 133
Mavv 8,30,104
E_ 134
Mavv 28,34,104
E_ 135
Mavv 32,34,108
C_ 135,134,135
C_ 134,132,133
Mavv 12,30,108
E_ 76
Mavv 16,30,112
E_ 77
Mavv 36,34,112
W_ 0
E_ 78
Mavv 56,38,112
s_nop 0
Tv 30,v2,13312
Tv 32,v2,19456
Mavv 76,42,112
E_ 79
Mavv 72,42,108
s_nop 0
Tv 34,v3,13312
Tv 36,v3,19456
Mavv 52,38,108
C_ 116,76,77
C_ 117,78,79
Mavv 48,38,104
E_ 80
Mavv 68,42,104
E_ 81
Mavv 64,42,100
E_ 82
Mavv 44,38,100
E_ 83
Mavv 40,38,96
C_ 120,80,81
C_ 121,82,83
Mavv 60,42,96
W_ 0
E_ 84
Mavv 80,30,96
s_nop 0
Tv 38,v4,13312
Tv 40,v4,19456
Mavv 100,34,96
E_ 85
Mavv 104,34,100
s_nop 0
Tv 42,v5,13312
Tv 44,v5,19456
Mavv 84,30,100
E_ 86
Mavv 88,30,104
E_ 87
Mavv 108,34,104
C_ 124,84,85
C_ 125,86,87
Mavv 112,34,108
E_ 88
Mavv 92,30,108
E_ 89
Mavv 96,30,112
E_ 90
Mavv 116,34,112
W_ 0
E_ 91
Mavv 136,38,112
s_nop 0
Tv 30,v2,14336
Tv 32,v2,20480
Mavv 156,42,112
C_ 128,88,89
C_ 129,90,91
Mavv 152,42,108
s_nop 0
Tv 34,v3,14336
Tv 36,v3,20480
Mavv 132,38,108
E_ 92
Mavv 128,38,104
E_ 93
Mavv 148,42,104
E_ 94
Mavv 144,42,100
E_ 95
Mavv 124,38,100
C_ 132,92,93
C_ 133,94,95
Mavv 120,38,96
v_mov_b32 v92, 0x3f803f80
v_mov_b32 v93, 0x3f803f80
Mavv 140,42,96
W_ 0
v_mov_b32 v94, 0x3f803f80
v_mov_b32 v95, 0x3f803f80
Mavv 160,30,96
s_nop 0
Tv 38,v4,14336
Tv 40,v4,20480
Mavv 180,34,96
Mavv 184,34,100
s_nop 0
Tv 42,v5,14336
Tv 44,v5,20480
Mavv 164,30,100
Mavv 168,30,104
Mavv 188,34,104
S_ s65,s61,122880
Mavv 192,34,108
S_ m0,s51,12288
Mavv 172,30,108
s_waitcnt vmcnt(19) lgkmcnt(0)
s_barrier
Mavv 176,30,112
Ln v7,32,s65,0
Mavv 196,34,112
Mavv 216,38,112
Dv 30,%0,43008
Mavv 236,42,112
Dv 34,v1,43008
Mavv 232,42,108
Ln v8,32,s65,3072
S_ m0,s51,13184
Mavv 212,38,108
Mavv 208,38,104
Ln v7,32,s65,128
Mavv 228,42,104
Mavv 224,42,100
Ln v8,32,s65,3200
S_ m0,s51,14080
Mavv 204,38,100
Mavv 200,38,96
Ln v7,32,s65,256
Mavv 220,42,96
.Lstep0_8:
Zvvv 76,92,116
Dv 38,%0,44032
Zvvv 80,92,120
Dv 42,v1,44032
Zvvv 84,92,124
Zvvv 88,92,128
Zvvv 92,92,132
W_ 2
Nvvv 96,30,136,56
Mvvv 96,34,156
Nvvv 100,30,140,60
s_nop 1
v_max3_f32 v10, v76, v80, v84
s_nop 0
v_max3_f32 v10, v10, v88, v92
Mvvv 100,34,160
v_cmp_lt_f32 vcc, 0x42000000, v10
s_cbranch_vccnz .Ltramp0_7
Nvvv 104,30,144,64
A_ v46,v46,v76
A_ v47,v47,v80
Mvvv 104,34,164
A_ v48,v48,v84
A_ v49,v49,v88
Nvvv 108,30,148,68
A_ v50,v50,v92
Mvvv 108,34,168
Ln v8,32,s65,3328
Nvvv 112,30,152,72
Mvvv 112,34,172
W_ 0
Mvvv 112,38,192
Dv 30,%0,45056
Mvvv 112,42,212
Dv 34,v1,45056
Mvvv 108,38,188
Mvvv 108,42,208
Mvvv 104,38,184
Mvvv 104,42,204
Mvvv 100,38,180
Mvvv 100,42,200
Mvvv 96,38,176
Mvvv 96,42,196
W_ 0
Mvvv 96,30,216
Dv 38,%0,36864
Mvvv 96,34,236
Dv 42,v1,36864
Mvvv 100,30,220
Mvvv 100,34,240
Mvvv 104,30,224
Mvvv 104,34,244
Mvvv 108,30,228
Mvvv 108,34,248
s_nop 0
E_ 96
Mvvv 112,30,232
E_ 97
Mvvv 112,34,252
W_ 0
E_ 98
Nvvv 76,38,136,56
Dv 30,%0,37888
Mvvv 76,42,156
Dv 34,v1,37888
Nvvv 80,38,140,60
E_ 99
Mvvv 80,42,160
C_ 99,98,99
C_ 98,96,97
Nvvv 84,38,144,64
E_ 100
Mvvv 84,42,164
E_ 101
Nvvv 88,38,148,68
E_ 102
Mvvv 88,42,168
E_ 103
Nvvv 92,38,152,72
C_ 103,102,103
C_ 102,100,101
Mvvv 92,42,172
W_ 0
E_ 104
Mvvv 92,30,192
Dv 38,%0,38912
Mvvv 92,34,212
Dv 42,v1,38912
Mvvv 88,30,188
E_ 105
Mvvv 88,34,208
E_ 106
Mvvv 84,30,184
S_ s65,s61,110592
E_ 107
Mvvv 84,34,204
S_ m0,s51,36864
C_ 107,106,107
C_ 106,104,105
Mvvv 80,30,180
s_waitcnt vmcnt(18) lgkmcnt(0)
s_barrier
Mvvv 80,34,200
L_ v9,36,s65,0
Mvvv 76,30,176
E_ 108
Mvvv 76,34,196
L_ v9,36,s65,3072
S_ m0,s51,37760
Mvvv 76,38,216
Tv 30,v2,49152
Tv 32,v2,55296
Mvvv 76,42,236
L_ v9,36,s65,128
Mvvv 80,38,220
s_nop 0
Tv 34,v3,49152
Tv 36,v3,55296
Mvvv 80,42,240
L_ v9,36,s65,3200
S_ m0,s51,38656
Mvvv 84,38,224
E_ 109
Mvvv 84,42,244
L_ v9,36,s65,256
Mvvv 88,38,228
E_ 110
Mvvv 88,42,248
L_ v9,36,s65,3328
Mvvv 92,38,232
E_ 111
Mvvv 92,42,252
W_ 0
C_ 111,110,111
C_ 110,108,109
Mavv 0,30,116
s_nop 0
Tv 38,v4,49152
Tv 40,v4,55296
Mavv 20,34,116
E_ 112
Mavv 24,34,120
s_nop 0
Tv 42,v5,49152
Tv 44,v5,55296
Mavv 4,30,120
E_ 113
Mavv 8,30,124
E_ 114
Mavv 28,34,124
E_ 115
Mavv 32,34,128
C_ 115,114,115
C_ 114,112,113
Mavv 12,30,128
E_ 76
Mavv 16,30,132
E_ 77
Mavv 36,34,132
W_ 0
E_ 78
Mavv 56,38,132
s_nop 0
Tv 30,v2,50176
Tv 32,v2,56320
Mavv 76,42,132
E_ 79
Mavv 72,42,128
s_nop 0
Tv 34,v3,50176
Tv 36,v3,56320
Mavv 52,38,128
C_ 96,76,77
C_ 97,78,79
Mavv 48,38,124
E_ 80
Mavv 68,42,124
E_ 81
Mavv 64,42,120
E_ 82
Mavv 44,38,120
E_ 83
Mavv 40,38,116
C_ 100,80,81
C_ 101,82,83
Mavv 60,42,116
W_ 0
E_ 84
Mavv 80,30,116
s_nop 0
Tv 38,v4,50176
Tv 40,v4,56320
Mavv 100,34,116
E_ 85
Mavv 104,34,120
s_nop 0
Tv 42,v5,50176
Tv 44,v5,56320
Mavv 84,30,120
E_ 86
Mavv 88,30,124
E_ 87
Mavv 108,34,124
C_ 104,84,85
C_ 105,86,87
Mavv 112,34,128
E_ 88
Mavv 92,30,128
E_ 89
Mavv 96,30,132
E_ 90
Mavv 116,34,132
W_ 0
E_ 91
Mavv 136,38,132
s_nop 0
Tv 30,v2,51200
Tv 32,v2,57344
Mavv 156,42,132
C_ 108,88,89
C_ 109,90,91
Mavv 152,42,128
s_nop 0
Tv 34,v3,51200
Tv 36,v3,57344
Mavv 132,38,128
E_ 92
Mavv 128,38,124
E_ 93
Mavv 148,42,124
E_ 94
Mavv 144,42,120
E_ 95
Mavv 124,38,120
C_ 112,92,93
C_ 113,94,95
Mavv 120,38,116
v_mov_b32 v92, 0x3f803f80
v_mov_b32 v93, 0x3f803f80
Mavv 140,42,116
W_ 0
v_mov_b32 v94, 0x3f803f80
v_mov_b32 v95, 0x3f803f80
Mavv 160,30,116
s_nop 0
Tv 38,v4,51200
Tv 40,v4,57344
Mavv 180,34,116
Mavv 184,34,120
s_nop 0
Tv 42,v5,51200
Tv 44,v5,57344
Mavv 164,30,120
Mavv 168,30,124
Mavv 188,34,124
S_ s65,s61,135168
Mavv 192,34,128
S_ m0,s51,49152
Mavv 172,30,128
s_waitcnt vmcnt(18) lgkmcnt(0)
s_barrier
Mavv 176,30,132
Ln v7,32,s65,0
Mavv 196,34,132
Mavv 216,38,132
Dv 30,%0,30720
Mavv 236,42,132
Dv 34,v1,30720
Mavv 232,42,128
Ln v8,32,s65,3072
S_ m0,s51,50048
Mavv 212,38,128
Mavv 208,38,124
Ln v7,32,s65,128
Mavv 228,42,124
Mavv 224,42,120
Ln v8,32,s65,3200
S_ m0,s51,50944
Mavv 204,38,120
Mavv 200,38,116
Ln v7,32,s65,256
Mavv 220,42,116
s_cmp_eq_u32 s4, 0
s_cbranch_scc1 .Lhs_d0
.Lhs_d0_back:
.Lstep0_9:
Zvvv 76,92,96
Dv 38,%0,31744
Zvvv 80,92,100
Dv 42,v1,31744
Zvvv 84,92,104
Zvvv 88,92,108
Zvvv 92,92,112
W_ 2
Nvvv 116,30,136,56
Mvvv 116,34,156
Nvvv 120,30,140,60
s_nop 1
v_max3_f32 v10, v76, v80, v84
s_nop 0
v_max3_f32 v10, v10, v88, v92
Mvvv 120,34,160
v_cmp_lt_f32 vcc, 0x42000000, v10
s_cbranch_vccnz .Ltramp0_8
Nvvv 124,30,144,64
A_ v46,v46,v76
A_ v47,v47,v80
Mvvv 124,34,164
A_ v48,v48,v84
A_ v49,v49,v88
Nvvv 128,30,148,68
A_ v50,v50,v92
Mvvv 128,34,168
Ln v8,32,s65,3328
Nvvv 132,30,152,72
Mvvv 132,34,172
W_ 0
Mvvv 132,38,192
Dv 30,%0,32768
Mvvv 132,42,212
Dv 34,v1,32768
Mvvv 128,38,188
Mvvv 128,42,208
Mvvv 124,38,184
Mvvv 124,42,204
Mvvv 120,38,180
Mvvv 120,42,200
Mvvv 116,38,176
Mvvv 116,42,196
W_ 0
Mvvv 116,30,216
Dv 38,%0,24576
Mvvv 116,34,236
Dv 42,v1,24576
Mvvv 120,30,220
Mvvv 120,34,240
Mvvv 124,30,224
Mvvv 124,34,244
Mvvv 128,30,228
Mvvv 128,34,248
s_nop 0
E_ 116
Mvvv 132,30,232
E_ 117
Mvvv 132,34,252
W_ 0
E_ 118
Nvvv 76,38,136,56
Dv 30,%0,25600
Mvvv 76,42,156
Dv 34,v1,25600
Nvvv 80,38,140,60
E_ 119
Mvvv 80,42,160
C_ 119,118,119
C_ 118,116,117
Nvvv 84,38,144,64
E_ 120
Mvvv 84,42,164
E_ 121
Nvvv 88,38,148,68
E_ 122
Mvvv 88,42,168
E_ 123
Nvvv 92,38,152,72
C_ 123,122,123
C_ 122,120,121
Mvvv 92,42,172
W_ 0
E_ 124
Mvvv 92,30,192
Dv 38,%0,26624
Mvvv 92,34,212
Dv 42,v1,26624
Mvvv 88,30,188
E_ 125
Mvvv 88,34,208
E_ 126
Mvvv 84,30,184
S_ s65,s61,122880
E_ 127
Mvvv 84,34,204
S_ m0,s51,24576
C_ 127,126,127
C_ 126,124,125
Mvvv 80,30,180
s_waitcnt vmcnt(18) lgkmcnt(0)
s_barrier
Mvvv 80,34,200
L_ v9,36,s65,0
Mvvv 76,30,176
E_ 128
Mvvv 76,34,196
L_ v9,36,s65,3072
S_ m0,s51,25472
Mvvv 76,38,216
Tv 30,v2,0
Tv 32,v2,6144
Mvvv 76,42,236
L_ v9,36,s65,128
Mvvv 80,38,220
s_nop 0
Tv 34,v3,0
Tv 36,v3,6144
Mvvv 80,42,240
L_ v9,36,s65,3200
S_ m0,s51,26368
Mvvv 84,38,224
E_ 129
Mvvv 84,42,244
L_ v9,36,s65,256
Mvvv 88,38,228
E_ 130
Mvvv 88,42,248
L_ v9,36,s65,3328
Mvvv 92,38,232
E_ 131
Mvvv 92,42,252
W_ 0
C_ 131,130,131
C_ 130,128,129
Mavv 0,30,96
s_nop 0
Tv 38,v4,0
Tv 40,v4,6144
Mavv 20,34,96
E_ 132
Mavv 24,34,100
s_nop 0
Tv 42,v5,0
Tv 44,v5,6144
Mavv 4,30,100
E_ 133
Mavv 8,30,104
E_ 134
Mavv 28,34,104
E_ 135
Mavv 32,34,108
C_ 135,134,135
C_ 134,132,133
Mavv 12,30,108
E_ 76
Mavv 16,30,112
E_ 77
Mavv 36,34,112
W_ 0
E_ 78
Mavv 56,38,112
s_nop 0
Tv 30,v2,1024
Tv 32,v2,7168
Mavv 76,42,112
E_ 79
Mavv 72,42,108
s_nop 0
Tv 34,v3,1024
Tv 36,v3,7168
Mavv 52,38,108
C_ 116,76,77
C_ 117,78,79
Mavv 48,38,104
E_ 80
Mavv 68,42,104
E_ 81
Mavv 64,42,100
E_ 82
Mavv 44,38,100
E_ 83
Mavv 40,38,96
C_ 120,80,81
C_ 121,82,83
Mavv 60,42,96
W_ 0
E_ 84
Mavv 80,30,96
s_nop 0
Tv 38,v4,1024
Tv 40,v4,7168
Mavv 100,34,96
E_ 85
Mavv 104,34,100
s_nop 0
Tv 42,v5,1024
Tv 44,v5,7168
Mavv 84,30,100
E_ 86
Mavv 88,30,104
E_ 87
Mavv 108,34,104
C_ 124,84,85
C_ 125,86,87
Mavv 112,34,108
E_ 88
Mavv 92,30,108
E_ 89
Mavv 96,30,112
E_ 90
Mavv 116,34,112
W_ 0
E_ 91
Mavv 136,38,112
s_nop 0
Tv 30,v2,2048
Tv 32,v2,8192
Mavv 156,42,112
C_ 128,88,89
C_ 129,90,91
Mavv 152,42,108
s_nop 0
Tv 34,v3,2048
Tv 36,v3,8192
Mavv 132,38,108
E_ 92
Mavv 128,38,104
E_ 93
Mavv 148,42,104
E_ 94
Mavv 144,42,100
E_ 95
Mavv 124,38,100
C_ 132,92,93
C_ 133,94,95
Mavv 120,38,96
v_mov_b32 v92, 0x3f803f80
v_mov_b32 v93, 0x3f803f80
Mavv 140,42,96
W_ 0
v_mov_b32 v94, 0x3f803f80
v_mov_b32 v95, 0x3f803f80
Mavv 160,30,96
s_nop 0
Tv 38,v4,2048
Tv 40,v4,8192
Mavv 180,34,96
Mavv 184,34,100
s_nop 0
Tv 42,v5,2048
Tv 44,v5,8192
Mavv 164,30,100
Mavv 168,30,104
Mavv 188,34,104
S_ s65,s61,147456
Mavv 192,34,108
S_ m0,s51,0
Mavv 172,30,108
s_waitcnt vmcnt(18) lgkmcnt(0)
s_barrier
Mavv 176,30,112
Ln v7,32,s65,0
Mavv 196,34,112
Mavv 216,38,112
Dv 30,%0,18432
Mavv 236,42,112
Dv 34,v1,18432
Mavv 232,42,108
Ln v8,32,s65,3072
S_ m0,s51,896
Mavv 212,38,108
Mavv 208,38,104
Ln v7,32,s65,128
Mavv 228,42,104
Mavv 224,42,100
Ln v8,32,s65,3200
S_ m0,s51,1792
Mavv 204,38,100
Mavv 200,38,96
Ln v7,32,s65,256
Mavv 220,42,96
.Lstep0_10:
Zvvv 76,92,116
Dv 38,%0,19456
Zvvv 80,92,120
Dv 42,v1,19456
Zvvv 84,92,124
Zvvv 88,92,128
Zvvv 92,92,132
W_ 2
Nvvv 96,30,136,56
Mvvv 96,34,156
Nvvv 100,30,140,60
s_nop 1
v_max3_f32 v10, v76, v80, v84
s_nop 0
v_max3_f32 v10, v10, v88, v92
Mvvv 100,34,160
v_cmp_lt_f32 vcc, 0x42000000, v10
s_cbranch_vccnz .Ltramp0_9
Nvvv 104,30,144,64
A_ v46,v46,v76
A_ v47,v47,v80
Mvvv 104,34,164
A_ v48,v48,v84
A_ v49,v49,v88
Nvvv 108,30,148,68
A_ v50,v50,v92
Mvvv 108,34,168
Ln v8,32,s65,3328
Nvvv 112,30,152,72
Mvvv 112,34,172
W_ 0
Mvvv 112,38,192
Dv 30,%0,20480
Mvvv 112,42,212
Dv 34,v1,20480
Mvvv 108,38,188
Mvvv 108,42,208
Mvvv 104,38,184
Mvvv 104,42,204
Mvvv 100,38,180
Mvvv 100,42,200
Mvvv 96,38,176
Mvvv 96,42,196
W_ 0
Mvvv 96,30,216
Dv 38,%0,12288
Mvvv 96,34,236
Dv 42,v1,12288
Mvvv 100,30,220
Mvvv 100,34,240
Mvvv 104,30,224
Mvvv 104,34,244
Mvvv 108,30,228
Mvvv 108,34,248
s_nop 0
E_ 96
Mvvv 112,30,232
E_ 97
Mvvv 112,34,252
W_ 0
E_ 98
Nvvv 76,38,136,56
Dv 30,%0,13312
Mvvv 76,42,156
Dv 34,v1,13312
Nvvv 80,38,140,60
E_ 99
Mvvv 80,42,160
C_ 99,98,99
C_ 98,96,97
Nvvv 84,38,144,64
E_ 100
Mvvv 84,42,164
E_ 101
Nvvv 88,38,148,68
E_ 102
Mvvv 88,42,168
E_ 103
Nvvv 92,38,152,72
C_ 103,102,103
C_ 102,100,101
Mvvv 92,42,172
W_ 0
E_ 104
Mvvv 92,30,192
Dv 38,%0,14336
Mvvv 92,34,212
Dv 42,v1,14336
Mvvv 88,30,188
E_ 105
Mvvv 88,34,208
E_ 106
Mvvv 84,30,184
S_ s65,s61,135168
E_ 107
Mvvv 84,34,204
S_ m0,s51,12288
C_ 107,106,107
C_ 106,104,105
Mvvv 80,30,180
s_waitcnt vmcnt(18) lgkmcnt(0)
s_barrier
Mvvv 80,34,200
L_ v9,36,s65,0
Mvvv 76,30,176
E_ 108
Mvvv 76,34,196
L_ v9,36,s65,3072
S_ m0,s51,13184
Mvvv 76,38,216
Tv 30,v2,36864
Tv 32,v2,43008
Mvvv 76,42,236
L_ v9,36,s65,128
Mvvv 80,38,220
s_nop 0
Tv 34,v3,36864
Tv 36,v3,43008
Mvvv 80,42,240
L_ v9,36,s65,3200
S_ m0,s51,14080
Mvvv 84,38,224
E_ 109
Mvvv 84,42,244
L_ v9,36,s65,256
Mvvv 88,38,228
E_ 110
Mvvv 88,42,248
L_ v9,36,s65,3328
Mvvv 92,38,232
E_ 111
Mvvv 92,42,252
W_ 0
C_ 111,110,111
C_ 110,108,109
Mavv 0,30,116
s_nop 0
Tv 38,v4,36864
Tv 40,v4,43008
Mavv 20,34,116
E_ 112
Mavv 24,34,120
s_nop 0
Tv 42,v5,36864
Tv 44,v5,43008
Mavv 4,30,120
E_ 113
Mavv 8,30,124
E_ 114
Mavv 28,34,124
E_ 115
Mavv 32,34,128
C_ 115,114,115
C_ 114,112,113
Mavv 12,30,128
E_ 76
Mavv 16,30,132
E_ 77
Mavv 36,34,132
W_ 0
E_ 78
Mavv 56,38,132
s_nop 0
Tv 30,v2,37888
Tv 32,v2,44032
Mavv 76,42,132
E_ 79
Mavv 72,42,128
s_nop 0
Tv 34,v3,37888
Tv 36,v3,44032
Mavv 52,38,128
C_ 96,76,77
C_ 97,78,79
Mavv 48,38,124
E_ 80
Mavv 68,42,124
E_ 81
Mavv 64,42,120
E_ 82
Mavv 44,38,120
E_ 83
Mavv 40,38,116
C_ 100,80,81
C_ 101,82,83
Mavv 60,42,116
W_ 0
E_ 84
Mavv 80,30,116
s_nop 0
Tv 38,v4,37888
Tv 40,v4,44032
Mavv 100,34,116
E_ 85
Mavv 104,34,120
s_nop 0
Tv 42,v5,37888
Tv 44,v5,44032
Mavv 84,30,120
E_ 86
Mavv 88,30,124
E_ 87
Mavv 108,34,124
C_ 104,84,85
C_ 105,86,87
Mavv 112,34,128
E_ 88
Mavv 92,30,128
E_ 89
Mavv 96,30,132
E_ 90
Mavv 116,34,132
W_ 0
E_ 91
Mavv 136,38,132
s_nop 0
Tv 30,v2,38912
Tv 32,v2,45056
Mavv 156,42,132
C_ 108,88,89
C_ 109,90,91
Mavv 152,42,128
s_nop 0
Tv 34,v3,38912
Tv 36,v3,45056
Mavv 132,38,128
E_ 92
Mavv 128,38,124
E_ 93
Mavv 148,42,124
E_ 94
Mavv 144,42,120
E_ 95
Mavv 124,38,120
C_ 112,92,93
C_ 113,94,95
Mavv 120,38,116
v_mov_b32 v92, 0x3f803f80
v_mov_b32 v93, 0x3f803f80
Mavv 140,42,116
W_ 0
v_mov_b32 v94, 0x3f803f80
v_mov_b32 v95, 0x3f803f80
Mavv 160,30,116
s_nop 0
Tv 38,v4,38912
Tv 40,v4,45056
Mavv 180,34,116
Mavv 184,34,120
s_nop 0
Tv 42,v5,38912
Tv 44,v5,45056
Mavv 164,30,120
Mavv 168,30,124
Mavv 188,34,124
S_ s65,s61,159744
Mavv 192,34,128
S_ m0,s51,36864
Mavv 172,30,128
s_waitcnt vmcnt(18) lgkmcnt(0)
s_barrier
Mavv 176,30,132
Ln v7,32,s65,0
Mavv 196,34,132
Mavv 216,38,132
Dv 30,%0,55296
Mavv 236,42,132
Dv 34,v1,55296
Mavv 232,42,128
Ln v8,32,s65,3072
S_ m0,s51,37760
Mavv 212,38,128
Mavv 208,38,124
Ln v7,32,s65,128
Mavv 228,42,124
Mavv 224,42,120
Ln v8,32,s65,3200
S_ m0,s51,38656
Mavv 204,38,120
Mavv 200,38,116
Ln v7,32,s65,256
Mavv 220,42,116
.Lstep0_11:
Zvvv 76,92,96
Dv 38,%0,56320
Zvvv 80,92,100
Dv 42,v1,56320
Zvvv 84,92,104
Zvvv 88,92,108
Zvvv 92,92,112
W_ 2
Nvvv 116,30,136,56
Mvvv 116,34,156
Nvvv 120,30,140,60
s_nop 1
v_max3_f32 v10, v76, v80, v84
s_nop 0
v_max3_f32 v10, v10, v88, v92
Mvvv 120,34,160
v_cmp_lt_f32 vcc, 0x42000000, v10
s_cbranch_vccnz .Ltramp0_10
Nvvv 124,30,144,64
A_ v46,v46,v76
A_ v47,v47,v80
Mvvv 124,34,164
A_ v48,v48,v84
A_ v49,v49,v88
Nvvv 128,30,148,68
A_ v50,v50,v92
Mvvv 128,34,168
Ln v8,32,s65,3328
Nvvv 132,30,152,72
Mvvv 132,34,172
W_ 0
Mvvv 132,38,192
Dv 30,%0,57344
Mvvv 132,42,212
Dv 34,v1,57344
Mvvv 128,38,188
Mvvv 128,42,208
Mvvv 124,38,184
Mvvv 124,42,204
Mvvv 120,38,180
Mvvv 120,42,200
Mvvv 116,38,176
Mvvv 116,42,196
W_ 0
Mvvv 116,30,216
Dv 38,%0,49152
Mvvv 116,34,236
Dv 42,v1,49152
Mvvv 120,30,220
Mvvv 120,34,240
Mvvv 124,30,224
Mvvv 124,34,244
Mvvv 128,30,228
Mvvv 128,34,248
s_nop 0
E_ 116
Mvvv 132,30,232
E_ 117
Mvvv 132,34,252
W_ 0
E_ 118
Nvvv 76,38,136,56
Dv 30,%0,50176
Mvvv 76,42,156
Dv 34,v1,50176
Nvvv 80,38,140,60
E_ 119
Mvvv 80,42,160
C_ 119,118,119
C_ 118,116,117
Nvvv 84,38,144,64
E_ 120
Mvvv 84,42,164
E_ 121
Nvvv 88,38,148,68
E_ 122
Mvvv 88,42,168
E_ 123
Nvvv 92,38,152,72
C_ 123,122,123
C_ 122,120,121
Mvvv 92,42,172
W_ 0
E_ 124
Mvvv 92,30,192
Dv 38,%0,51200
Mvvv 92,34,212
Dv 42,v1,51200
Mvvv 88,30,188
E_ 125
Mvvv 88,34,208
E_ 126
Mvvv 84,30,184
S_ s65,s61,147456
E_ 127
Mvvv 84,34,204
S_ m0,s51,49152
C_ 127,126,127
C_ 126,124,125
Mvvv 80,30,180
s_waitcnt vmcnt(18) lgkmcnt(0)
s_barrier
Mvvv 80,34,200
L_ v9,36,s65,0
Mvvv 76,30,176
E_ 128
Mvvv 76,34,196
L_ v9,36,s65,3072
S_ m0,s51,50048
Mvvv 76,38,216
Tv 30,v2,24576
Tv 32,v2,30720
Mvvv 76,42,236
L_ v9,36,s65,128
Mvvv 80,38,220
s_nop 0
Tv 34,v3,24576
Tv 36,v3,30720
Mvvv 80,42,240
L_ v9,36,s65,3200
S_ m0,s51,50944
Mvvv 84,38,224
E_ 129
Mvvv 84,42,244
L_ v9,36,s65,256
Mvvv 88,38,228
E_ 130
Mvvv 88,42,248
L_ v9,36,s65,3328
Mvvv 92,38,232
E_ 131
Mvvv 92,42,252
W_ 0
C_ 131,130,131
C_ 130,128,129
Mavv 0,30,96
s_nop 0
Tv 38,v4,24576
Tv 40,v4,30720
Mavv 20,34,96
E_ 132
Mavv 24,34,100
s_nop 0
Tv 42,v5,24576
Tv 44,v5,30720
Mavv 4,30,100
E_ 133
Mavv 8,30,104
E_ 134
Mavv 28,34,104
E_ 135
Mavv 32,34,108
C_ 135,134,135
C_ 134,132,133
Mavv 12,30,108
E_ 76
Mavv 16,30,112
E_ 77
Mavv 36,34,112
W_ 0
E_ 78
Mavv 56,38,112
s_nop 0
Tv 30,v2,25600
Tv 32,v2,31744
Mavv 76,42,112
E_ 79
Mavv 72,42,108
s_nop 0
Tv 34,v3,25600
Tv 36,v3,31744
Mavv 52,38,108
C_ 116,76,77
C_ 117,78,79
Mavv 48,38,104
E_ 80
Mavv 68,42,104
E_ 81
Mavv 64,42,100
E_ 82
Mavv 44,38,100
E_ 83
Mavv 40,38,96
C_ 120,80,81
C_ 121,82,83
Mavv 60,42,96
W_ 0
E_ 84
Mavv 80,30,96
s_nop 0
Tv 38,v4,25600
Tv 40,v4,31744
Mavv 100,34,96
E_ 85
Mavv 104,34,100
s_nop 0
Tv 42,v5,25600
Tv 44,v5,31744
Mavv 84,30,100
E_ 86
Mavv 88,30,104
E_ 87
Mavv 108,34,104
C_ 124,84,85
C_ 125,86,87
Mavv 112,34,108
E_ 88
Mavv 92,30,108
E_ 89
Mavv 96,30,112
E_ 90
Mavv 116,34,112
W_ 0
E_ 91
Mavv 136,38,112
s_nop 0
Tv 30,v2,26624
Tv 32,v2,32768
Mavv 156,42,112
C_ 128,88,89
C_ 129,90,91
Mavv 152,42,108
s_nop 0
Tv 34,v3,26624
Tv 36,v3,32768
Mavv 132,38,108
E_ 92
Mavv 128,38,104
E_ 93
Mavv 148,42,104
E_ 94
Mavv 144,42,100
E_ 95
Mavv 124,38,100
C_ 132,92,93
C_ 133,94,95
Mavv 120,38,96
v_mov_b32 v92, 0x3f803f80
v_mov_b32 v93, 0x3f803f80
Mavv 140,42,96
W_ 0
v_mov_b32 v94, 0x3f803f80
v_mov_b32 v95, 0x3f803f80
Mavv 160,30,96
s_nop 0
Tv 38,v4,26624
Tv 40,v4,32768
Mavv 180,34,96
Mavv 184,34,100
s_nop 0
Tv 42,v5,26624
Tv 44,v5,32768
Mavv 164,30,100
Mavv 168,30,104
Mavv 188,34,104
S_ s65,s61,172032
Mavv 192,34,108
S_ m0,s51,24576
Mavv 172,30,108
s_waitcnt vmcnt(18) lgkmcnt(0)
s_barrier
Mavv 176,30,112
Ln v7,32,s65,0
Mavv 196,34,112
Mavv 216,38,112
Dv 30,%0,6144
Mavv 236,42,112
Dv 34,v1,6144
Mavv 232,42,108
Ln v8,32,s65,3072
S_ m0,s51,25472
Mavv 212,38,108
Mavv 208,38,104
Ln v7,32,s65,128
Mavv 228,42,104
Mavv 224,42,100
Ln v8,32,s65,3200
S_ m0,s51,26368
Mavv 204,38,100
Mavv 200,38,96
Ln v7,32,s65,256
Mavv 220,42,96
.Lstep0_12:
Zvvv 76,92,116
Dv 38,%0,7168
Zvvv 80,92,120
Dv 42,v1,7168
Zvvv 84,92,124
Zvvv 88,92,128
Zvvv 92,92,132
W_ 2
Nvvv 96,30,136,56
Mvvv 96,34,156
Nvvv 100,30,140,60
s_nop 1
v_max3_f32 v10, v76, v80, v84
s_nop 0
v_max3_f32 v10, v10, v88, v92
Mvvv 100,34,160
v_cmp_lt_f32 vcc, 0x42000000, v10
s_cbranch_vccnz .Ltramp0_11
Nvvv 104,30,144,64
A_ v46,v46,v76
A_ v47,v47,v80
Mvvv 104,34,164
A_ v48,v48,v84
A_ v49,v49,v88
Nvvv 108,30,148,68
A_ v50,v50,v92
Mvvv 108,34,168
Ln v8,32,s65,3328
Nvvv 112,30,152,72
R_ v11,a248
Mvvv 112,34,172
W_ 0
v_min_u32 v11, 240, v11
Mvvv 112,38,192
Dv 30,%0,8192
v_lshlrev_b32 v11, 3, v11
Mvvv 112,42,212
Dv 34,v1,8192
Mvvv 108,38,188
buffer_load_dword a254, v11, s[44:47], s66 offen
Mvvv 108,42,208
Mvvv 104,38,184
Mvvv 104,42,204
Mvvv 100,38,180
Mvvv 100,42,200
Mvvv 96,38,176
Mvvv 96,42,196
W_ 0
Mvvv 96,30,216
Dv 38,%0,0
Mvvv 96,34,236
Dv 42,v1,0
Mvvv 100,30,220
Mvvv 100,34,240
Mvvv 104,30,224
Mvvv 104,34,244
Mvvv 108,30,228
Mvvv 108,34,248
s_nop 0
E_ 96
Mvvv 112,30,232
E_ 97
Mvvv 112,34,252
W_ 0
E_ 98
Nvvv 76,38,136,56
Dv 30,%0,1024
Mvvv 76,42,156
Dv 34,v1,1024
Nvvv 80,38,140,60
E_ 99
Mvvv 80,42,160
C_ 99,98,99
C_ 98,96,97
Nvvv 84,38,144,64
E_ 100
Mvvv 84,42,164
E_ 101
Nvvv 88,38,148,68
E_ 102
Mvvv 88,42,168
E_ 103
Nvvv 92,38,152,72
C_ 103,102,103
C_ 102,100,101
Mvvv 92,42,172
W_ 0
E_ 104
Mvvv 92,30,192
Dv 38,%0,2048
Mvvv 92,34,212
Dv 42,v1,2048
Mvvv 88,30,188
E_ 105
Mvvv 88,34,208
E_ 106
Mvvv 84,30,184
S_ s65,s61,159744
E_ 107
Mvvv 84,34,204
S_ m0,s51,0
C_ 107,106,107
C_ 106,104,105
Mvvv 80,30,180
s_waitcnt vmcnt(19) lgkmcnt(0)
s_barrier
Mvvv 80,34,200
L_ v9,36,s65,0
Mvvv 76,30,176
E_ 108
Mvvv 76,34,196
L_ v9,36,s65,3072
S_ m0,s51,896
Mvvv 76,38,216
Tv 30,v2,12288
Tv 32,v2,18432
Mvvv 76,42,236
L_ v9,36,s65,128
Mvvv 80,38,220
s_nop 0
Tv 34,v3,12288
Tv 36,v3,18432
Mvvv 80,42,240
L_ v9,36,s65,3200
S_ m0,s51,1792
Mvvv 84,38,224
E_ 109
Mvvv 84,42,244
L_ v9,36,s65,256
Mvvv 88,38,228
E_ 110
Mvvv 88,42,248
L_ v9,36,s65,3328
Mvvv 92,38,232
E_ 111
Mvvv 92,42,252
W_ 0
C_ 111,110,111
C_ 110,108,109
Mavv 0,30,116
s_nop 0
Tv 38,v4,12288
Tv 40,v4,18432
Mavv 20,34,116
E_ 112
Mavv 24,34,120
s_nop 0
Tv 42,v5,12288
Tv 44,v5,18432
Mavv 4,30,120
E_ 113
Mavv 8,30,124
E_ 114
Mavv 28,34,124
E_ 115
Mavv 32,34,128
C_ 115,114,115
)A"
R"A(C_ 114,112,113
Mavv 12,30,128
E_ 76
Mavv 16,30,132
E_ 77
Mavv 36,34,132
W_ 0
E_ 78
Mavv 56,38,132
s_nop 0
Tv 30,v2,13312
Tv 32,v2,19456
Mavv 76,42,132
E_ 79
Mavv 72,42,128
s_nop 0
Tv 34,v3,13312
Tv 36,v3,19456
Mavv 52,38,128
C_ 96,76,77
C_ 97,78,79
Mavv 48,38,124
E_ 80
Mavv 68,42,124
E_ 81
Mavv 64,42,120
E_ 82
Mavv 44,38,120
E_ 83
Mavv 40,38,116
C_ 100,80,81
C_ 101,82,83
Mavv 60,42,116
W_ 0
E_ 84
Mavv 80,30,116
s_nop 0
Tv 38,v4,13312
Tv 40,v4,19456
Mavv 100,34,116
E_ 85
Mavv 104,34,120
s_nop 0
Tv 42,v5,13312
Tv 44,v5,19456
Mavv 84,30,120
E_ 86
Mavv 88,30,124
E_ 87
Mavv 108,34,124
C_ 104,84,85
C_ 105,86,87
Mavv 112,34,128
E_ 88
Mavv 92,30,128
E_ 89
Mavv 96,30,132
E_ 90
Mavv 116,34,132
W_ 0
E_ 91
Mavv 136,38,132
s_nop 0
Tv 30,v2,14336
Tv 32,v2,20480
Mavv 156,42,132
C_ 108,88,89
C_ 109,90,91
Mavv 152,42,128
s_nop 0
Tv 34,v3,14336
Tv 36,v3,20480
Mavv 132,38,128
E_ 92
Mavv 128,38,124
E_ 93
Mavv 148,42,124
E_ 94
Mavv 144,42,120
E_ 95
Mavv 124,38,120
C_ 112,92,93
C_ 113,94,95
Mavv 120,38,116
v_mov_b32 v92, 0x3f803f80
v_mov_b32 v93, 0x3f803f80
Mavv 140,42,116
W_ 0
v_mov_b32 v94, 0x3f803f80
v_mov_b32 v95, 0x3f803f80
Mavv 160,30,116
s_nop 0
Tv 38,v4,14336
Tv 40,v4,20480
Mavv 180,34,116
Mavv 184,34,120
s_nop 0
Tv 42,v5,14336
Tv 44,v5,20480
Mavv 164,30,120
Mavv 168,30,124
Mavv 188,34,124
S_ s65,s61,184320
Mavv 192,34,128
S_ m0,s51,12288
Mavv 172,30,128
s_waitcnt vmcnt(19) lgkmcnt(0)
s_barrier
Mavv 176,30,132
Ln v7,32,s65,0
Mavv 196,34,132
Mavv 216,38,132
Dv 30,%0,43008
Mavv 236,42,132
Dv 34,v1,43008
Mavv 232,42,128
Ln v8,32,s65,3072
S_ m0,s51,13184
Mavv 212,38,128
Mavv 208,38,124
Ln v7,32,s65,128
Mavv 228,42,124
Mavv 224,42,120
Ln v8,32,s65,3200
S_ m0,s51,14080
Mavv 204,38,120
Mavv 200,38,116
Ln v7,32,s65,256
Mavv 220,42,116
.Lstep0_13:
Zvvv 76,92,96
Dv 38,%0,44032
Zvvv 80,92,100
Dv 42,v1,44032
Zvvv 84,92,104
Zvvv 88,92,108
Zvvv 92,92,112
W_ 2
Nvvv 116,30,136,56
Mvvv 116,34,156
Nvvv 120,30,140,60
s_nop 1
v_max3_f32 v10, v76, v80, v84
s_nop 0
v_max3_f32 v10, v10, v88, v92
Mvvv 120,34,160
v_cmp_lt_f32 vcc, 0x42000000, v10
s_cbranch_vccnz .Ltramp0_12
Nvvv 124,30,144,64
A_ v46,v46,v76
A_ v47,v47,v80
Mvvv 124,34,164
A_ v48,v48,v84
A_ v49,v49,v88
Nvvv 128,30,148,68
A_ v50,v50,v92
Mvvv 128,34,168
Ln v8,32,s65,3328
Nvvv 132,30,152,72
Mvvv 132,34,172
W_ 0
Mvvv 132,38,192
Dv 30,%0,45056
Mvvv 132,42,212
Dv 34,v1,45056
Mvvv 128,38,188
Mvvv 128,42,208
Mvvv 124,38,184
Mvvv 124,42,204
Mvvv 120,38,180
Mvvv 120,42,200
Mvvv 116,38,176
Mvvv 116,42,196
W_ 0
Mvvv 116,30,216
Dv 38,%0,36864
Mvvv 116,34,236
Dv 42,v1,36864
Mvvv 120,30,220
Mvvv 120,34,240
Mvvv 124,30,224
Mvvv 124,34,244
Mvvv 128,30,228
Mvvv 128,34,248
s_nop 0
E_ 116
Mvvv 132,30,232
E_ 117
Mvvv 132,34,252
W_ 0
E_ 118
Nvvv 76,38,136,56
Dv 30,%0,37888
Mvvv 76,42,156
Dv 34,v1,37888
Nvvv 80,38,140,60
E_ 119
Mvvv 80,42,160
C_ 119,118,119
C_ 118,116,117
Nvvv 84,38,144,64
E_ 120
Mvvv 84,42,164
E_ 121
Nvvv 88,38,148,68
E_ 122
Mvvv 88,42,168
E_ 123
Nvvv 92,38,152,72
C_ 123,122,123
C_ 122,120,121
Mvvv 92,42,172
W_ 0
E_ 124
Mvvv 92,30,192
Dv 38,%0,38912
Mvvv 92,34,212
Dv 42,v1,38912
Mvvv 88,30,188
E_ 125
Mvvv 88,34,208
E_ 126
Mvvv 84,30,184
S_ s65,s61,172032
E_ 127
Mvvv 84,34,204
S_ m0,s51,36864
C_ 127,126,127
C_ 126,124,125
Mvvv 80,30,180
s_waitcnt vmcnt(19) lgkmcnt(0)
s_barrier
Mvvv 80,34,200
L_ v9,36,s65,0
Mvvv 76,30,176
E_ 128
Mvvv 76,34,196
L_ v9,36,s65,3072
S_ m0,s51,37760
Mvvv 76,38,216
Tv 30,v2,49152
Tv 32,v2,55296
Mvvv 76,42,236
L_ v9,36,s65,128
Mvvv 80,38,220
s_nop 0
Tv 34,v3,49152
Tv 36,v3,55296
Mvvv 80,42,240
L_ v9,36,s65,3200
S_ m0,s51,38656
Mvvv 84,38,224
E_ 129
Mvvv 84,42,244
L_ v9,36,s65,256
Mvvv 88,38,228
E_ 130
Mvvv 88,42,248
L_ v9,36,s65,3328
Mvvv 92,38,232
E_ 131
Mvvv 92,42,252
W_ 0
C_ 131,130,131
C_ 130,128,129
Mavv 0,30,96
s_nop 0
Tv 38,v4,49152
Tv 40,v4,55296
Mavv 20,34,96
E_ 132
Mavv 24,34,100
s_nop 0
Tv 42,v5,49152
Tv 44,v5,55296
Mavv 4,30,100
E_ 133
Mavv 8,30,104
E_ 134
Mavv 28,34,104
E_ 135
Mavv 32,34,108
C_ 135,134,135
C_ 134,132,133
Mavv 12,30,108
E_ 76
Mavv 16,30,112
E_ 77
Mavv 36,34,112
W_ 0
E_ 78
Mavv 56,38,112
s_nop 0
Tv 30,v2,50176
Tv 32,v2,56320
Mavv 76,42,112
E_ 79
Mavv 72,42,108
s_nop 0
Tv 34,v3,50176
Tv 36,v3,56320
Mavv 52,38,108
C_ 116,76,77
C_ 117,78,79
Mavv 48,38,104
E_ 80
Mavv 68,42,104
E_ 81
Mavv 64,42,100
E_ 82
Mavv 44,38,100
E_ 83
Mavv 40,38,96
C_ 120,80,81
C_ 121,82,83
Mavv 60,42,96
W_ 0
E_ 84
Mavv 80,30,96
s_nop 0
Tv 38,v4,50176
Tv 40,v4,56320
Mavv 100,34,96
E_ 85
Mavv 104,34,100
s_nop 0
Tv 42,v5,50176
Tv 44,v5,56320
Mavv 84,30,100
E_ 86
Mavv 88,30,104
E_ 87
Mavv 108,34,104
C_ 124,84,85
C_ 125,86,87
Mavv 112,34,108
E_ 88
Mavv 92,30,108
E_ 89
Mavv 96,30,112
E_ 90
Mavv 116,34,112
W_ 0
E_ 91
Mavv 136,38,112
s_nop 0
Tv 30,v2,51200
Tv 32,v2,57344
Mavv 156,42,112
C_ 128,88,89
C_ 129,90,91
Mavv 152,42,108
s_nop 0
Tv 34,v3,51200
Tv 36,v3,57344
Mavv 132,38,108
E_ 92
Mavv 128,38,104
E_ 93
Mavv 148,42,104
E_ 94
Mavv 144,42,100
E_ 95
Mavv 124,38,100
C_ 132,92,93
C_ 133,94,95
Mavv 120,38,96
v_mov_b32 v92, 0x3f803f80
v_mov_b32 v93, 0x3f803f80
Mavv 140,42,96
W_ 0
v_mov_b32 v94, 0x3f803f80
v_mov_b32 v95, 0x3f803f80
Mavv 160,30,96
s_nop 0
Tv 38,v4,51200
Tv 40,v4,57344
Mavv 180,34,96
Mavv 184,34,100
s_nop 0
Tv 42,v5,51200
Tv 44,v5,57344
Mavv 164,30,100
Mavv 168,30,104
Mavv 188,34,104
S_ s65,s61,184320
Mavv 192,34,108
S_ m0,s51,49152
Mavv 172,30,108
s_waitcnt vmcnt(19) lgkmcnt(0)
s_barrier
Mavv 176,30,112
L_ v9,36,s65,0
Mavv 196,34,112
Mavv 216,38,112
Dv 30,%0,30720
Mavv 236,42,112
Dv 34,v1,30720
Mavv 232,42,108
L_ v9,36,s65,3072
S_ m0,s51,50048
Mavv 212,38,108
Mavv 208,38,104
L_ v9,36,s65,128
Mavv 228,42,104
Mavv 224,42,100
L_ v9,36,s65,3200
S_ m0,s51,50944
Mavv 204,38,100
Mavv 200,38,96
L_ v9,36,s65,256
Mavv 220,42,96
.Lstep0_14:
Zvvv 76,92,116
Dv 38,%0,31744
Zvvv 80,92,120
Dv 42,v1,31744
Zvvv 84,92,124
Zvvv 88,92,128
Zvvv 92,92,132
W_ 2
Nvvv 96,30,136,56
Mvvv 96,34,156
Nvvv 100,30,140,60
s_nop 1
v_max3_f32 v10, v76, v80, v84
s_nop 0
v_max3_f32 v10, v10, v88, v92
Mvvv 100,34,160
v_cmp_lt_f32 vcc, 0x42000000, v10
s_cbranch_vccnz .Ltramp0_13
Nvvv 104,30,144,64
A_ v46,v46,v76
A_ v47,v47,v80
Mvvv 104,34,164
A_ v48,v48,v84
A_ v49,v49,v88
Nvvv 108,30,148,68
A_ v50,v50,v92
Mvvv 108,34,168
L_ v9,36,s65,3328
Nvvv 112,30,152,72
R_ v20,a240
Mvvv 112,34,172
W_ 0
v_xor_b32 v21, 64, v20
Mvvv 112,38,192
S_ s65,s60,0
Dv 30,%0,32768
Mvvv 112,42,212
S_ m0,s50,0
Dv 34,v1,32768
Mvvv 108,38,188
L_ v20,24,s65,0
Mvvv 108,42,208
S_ s65,s60,10240
Mvvv 104,38,184
S_ m0,s50,1024
Mvvv 104,42,204
L_ v21,24,s65,0
Mvvv 100,38,180
S_ s65,s60,20480
Mvvv 100,42,200
S_ m0,s50,2048
Mvvv 96,38,176
L_ v20,24,s65,0
Mvvv 96,42,196
S_ s65,s60,30720
W_ 0
Mvvv 96,30,216
S_ m0,s50,3072
Dv 38,%0,24576
Mvvv 96,34,236
Dv 42,v1,24576
Mvvv 100,30,220
L_ v21,24,s65,0
Mvvv 100,34,240
S_ s65,s60,40960
Mvvv 104,30,224
S_ m0,s50,4096
Mvvv 104,34,244
L_ v20,24,s65,0
Mvvv 108,30,228
Mvvv 108,34,248
E_ 96
Mvvv 112,30,232
E_ 97
Mvvv 112,34,252
W_ 0
E_ 98
Nvvv 76,38,136,56
Dv 30,%0,25600
Mvvv 76,42,156
Dv 34,v1,25600
Nvvv 80,38,140,60
E_ 99
Mvvv 80,42,160
C_ 99,98,99
C_ 98,96,97
Nvvv 84,38,144,64
E_ 100
Mvvv 84,42,164
E_ 101
Nvvv 88,38,148,68
E_ 102
Mvvv 88,42,168
E_ 103
Nvvv 92,38,152,72
C_ 103,102,103
C_ 102,100,101
Mvvv 92,42,172
W_ 0
E_ 104
Mvvv 92,30,192
Dv 38,%0,26624
Mvvv 92,34,212
Dv 42,v1,26624
Mvvv 88,30,188
E_ 105
Mvvv 88,34,208
E_ 106
Mvvv 84,30,184
E_ 107
Mvvv 84,34,204
C_ 107,106,107
C_ 106,104,105
Mvvv 80,30,180
s_waitcnt vmcnt(23) lgkmcnt(0)
s_barrier
Mvvv 80,34,200
E_ 108
Mvvv 76,30,176
E_ 109
Mvvv 76,34,196
E_ 110
Mvvv 76,38,216
s_nop 0
Tv 30,v2,0
Tv 32,v2,6144
Mvvv 76,42,236
E_ 111
Mvvv 80,38,220
s_nop 0
Tv 34,v3,0
Tv 36,v3,6144
Mvvv 80,42,240
C_ 111,110,111
C_ 110,108,109
Mvvv 84,38,224
E_ 112
Mvvv 84,42,244
E_ 113
Mvvv 88,38,228
E_ 114
Mvvv 88,42,248
E_ 115
Mvvv 92,38,232
C_ 115,114,115
C_ 114,112,113
Mvvv 92,42,252
W_ 0
E_ 76
Mavv 0,30,116
s_nop 0
Tv 38,v4,0
Tv 40,v4,6144
Mavv 20,34,116
E_ 77
Mavv 24,34,120
s_nop 0
Tv 42,v5,0
Tv 44,v5,6144
Mavv 4,30,120
E_ 78
Mavv 8,30,124
E_ 79
Mavv 28,34,124
C_ 96,76,77
C_ 97,78,79
Mavv 32,34,128
E_ 80
Mavv 12,30,128
E_ 81
Mavv 16,30,132
E_ 82
Mavv 36,34,132
W_ 0
E_ 83
Mavv 56,38,132
s_nop 0
Tv 30,v2,1024
Tv 32,v2,7168
Mavv 76,42,132
C_ 100,80,81
C_ 101,82,83
Mavv 72,42,128
s_nop 0
Tv 34,v3,1024
Tv 36,v3,7168
Mavv 52,38,128
E_ 84
Mavv 48,38,124
E_ 85
Mavv 68,42,124
E_ 86
Mavv 64,42,120
E_ 87
Mavv 44,38,120
C_ 104,84,85
C_ 105,86,87
Mavv 40,38,116
E_ 88
Mavv 60,42,116
W_ 0
E_ 89
Mavv 80,30,116
s_nop 0
Tv 38,v4,1024
Tv 40,v4,7168
Mavv 100,34,116
E_ 90
Mavv 104,34,120
s_nop 0
Tv 42,v5,1024
Tv 44,v5,7168
Mavv 84,30,120
E_ 91
Mavv 88,30,124
C_ 108,88,89
C_ 109,90,91
Mavv 108,34,124
E_ 92
Mavv 112,34,128
E_ 93
Mavv 92,30,128
E_ 94
Mavv 96,30,132
E_ 95
Mavv 116,34,132
W_ 0
C_ 112,92,93
C_ 113,94,95
Mavv 136,38,132
s_nop 0
Tv 30,v2,2048
Tv 32,v2,8192
Mavv 156,42,132
v_mov_b32 v92, 0x3f803f80
v_mov_b32 v93, 0x3f803f80
Mavv 152,42,128
s_nop 0
Tv 34,v3,2048
Tv 36,v3,8192
Mavv 132,38,128
v_mov_b32 v94, 0x3f803f80
v_mov_b32 v95, 0x3f803f80
Mavv 128,38,124
Mavv 148,42,124
Mavv 144,42,120
Mavv 124,38,120
Mavv 120,38,116
Mavv 140,42,116
W_ 0
Mavv 160,30,116
s_nop 0
Tv 38,v4,2048
Tv 40,v4,8192
Mavv 180,34,116
Mavv 184,34,120
s_nop 0
Tv 42,v5,2048
Tv 44,v5,8192
Mavv 164,30,120
Mavv 168,30,124
Mavv 188,34,124
Mavv 192,34,128
Mavv 172,30,128
s_waitcnt vmcnt(17) lgkmcnt(0)
s_barrier
Mavv 176,30,132
S_ s65,s59,0
Mavv 196,34,132
S_ m0,s52,24320
Mavv 216,38,132
Dv 30,%0,18432
Mavv 236,42,132
Dv 34,v1,18432
Mavv 232,42,128
L_ v6,28,s65,256
Mavv 212,38,128
S_ s65,s59,98304
Mavv 208,38,124
S_ m0,s52,25344
Mavv 228,42,124
L_ v6,28,s65,256
Mavv 224,42,120
S_ s65,s59,196608
Mavv 204,38,120
S_ m0,s52,26368
Mavv 200,38,116
L_ v6,28,s65,256
Mavv 220,42,116
S_ s65,s59,294912
.Lstep0_15:
Zvvv 76,92,96
Dv 38,%0,19456
Zvvv 80,92,100
Dv 42,v1,19456
Zvvv 84,92,104
Zvvv 88,92,108
Zvvv 92,92,112
W_ 2
Nvvv 116,30,136,56
Mvvv 116,34,156
Nvvv 120,30,140,60
s_nop 1
v_max3_f32 v10, v76, v80, v84
s_nop 0
v_max3_f32 v10, v10, v88, v92
Mvvv 120,34,160
v_cmp_lt_f32 vcc, 0x42000000, v10
s_cbranch_vccnz .Ltramp0_14
Nvvv 124,30,144,64
S_ m0,s52,27392
A_ v46,v46,v76
A_ v47,v47,v80
Mvvv 124,34,164
A_ v48,v48,v84
A_ v49,v49,v88
Nvvv 128,30,148,68
A_ v50,v50,v92
Mvvv 128,34,168
L_ v6,28,s65,256
Nvvv 132,30,152,72
S_ s65,s59,786432
Mvvv 132,34,172
S_ m0,s52,0
W_ 0
Mvvv 132,38,192
Dv 30,%0,20480
Mvvv 132,42,212
Dv 34,v1,20480
Mvvv 128,38,188
L_ v6,28,s65,0
Mvvv 128,42,208
S_ s65,s59,884736
Mvvv 124,38,184
S_ m0,s52,1024
Mvvv 124,42,204
L_ v6,28,s65,0
Mvvv 120,38,180
S_ s65,s59,983040
Mvvv 120,42,200
S_ m0,s52,2048
Mvvv 116,38,176
L_ v6,28,s65,0
Mvvv 116,42,196
S_ s65,s59,1081344
W_ 0
Mvvv 116,30,216
S_ m0,s52,3072
Dv 38,%0,12288
Mvvv 116,34,236
Dv 42,v1,12288
Mvvv 120,30,220
L_ v6,28,s65,0
Mvvv 120,34,240
S_ s65,s60,128
Mvvv 124,30,224
S_ m0,s50,20480
Mvvv 124,34,244
L_ v20,24,s65,0
Mvvv 128,30,228
S_ s65,s60,10368
Mvvv 128,34,248
S_ m0,s50,21504
E_ 116
Mvvv 132,30,232
L_ v21,24,s65,0
Mvvv 132,34,252
S_ s65,s60,20608
E_ 117
W_ 1
Nvvv 76,38,136,56
S_ m0,s50,22528
Dv 30,%0,13312
W_ 1
Mvvv 76,42,156
Dv 34,v1,13312
Nvvv 80,38,140,60
L_ v20,24,s65,0
Mvvv 80,42,160
S_ s65,s60,30848
E_ 118
Nvvv 84,38,144,64
S_ m0,s50,23552
E_ 119
Mvvv 84,42,164
L_ v21,24,s65,0
Nvvv 88,38,148,68
S_ s65,s60,41088
C_ 119,118,119
C_ 118,116,117
Mvvv 88,42,168
S_ m0,s50,24576
E_ 120
Nvvv 92,38,152,72
L_ v20,24,s65,0
Mvvv 92,42,172
W_ 0
E_ 121
Mvvv 92,30,192
Dv 38,%0,14336
Mvvv 92,34,212
Dv 42,v1,14336
Mvvv 88,30,188
E_ 122
Mvvv 88,34,208
E_ 123
Mvvv 84,30,184
C_ 123,122,123
C_ 122,120,121
Mvvv 84,34,204
E_ 124
Mvvv 80,30,180
s_waitcnt vmcnt(24) lgkmcnt(0)
s_barrier
Mvvv 80,34,200
S_ s65,s59,0
E_ 125
Mvvv 76,30,176
S_ m0,s52,8192
E_ 126
Mvvv 76,34,196
L_ v6,28,s65,0
Mvvv 76,38,216
S_ s65,s59,98304
Tv 30,v2,36864
Tv 32,v2,43008
Mvvv 76,42,236
S_ m0,s52,9216
E_ 127
Mvvv 80,38,220
Tv 34,v3,36864
Tv 36,v3,43008
Mvvv 80,42,240
L_ v6,28,s65,0
Mvvv 84,38,224
S_ s65,s59,196608
C_ 127,126,127
C_ 126,124,125
Mvvv 84,42,244
S_ m0,s52,10240
E_ 128
Mvvv 88,38,228
L_ v6,28,s65,0
Mvvv 88,42,248
S_ s65,s59,294912
E_ 129
Mvvv 92,38,232
S_ m0,s52,11264
E_ 130
Mvvv 92,42,252
L_ v6,28,s65,0
W_ 2
Mavv 0,30,96
S_ s65,s59,0
Tv 38,v4,36864
Tv 40,v4,43008
W_ 2
Mavv 20,34,96
S_ m0,s52,16256
E_ 131
Mavv 24,34,100
Tv 42,v5,36864
Tv 44,v5,43008
Mavv 4,30,100
L_ v6,28,s65,128
Mavv 8,30,104
S_ s65,s59,98304
C_ 131,130,131
C_ 130,128,129
Mavv 28,34,104
S_ m0,s52,17280
E_ 132
Mavv 32,34,108
L_ v6,28,s65,128
Mavv 12,30,108
S_ s65,s59,196608
E_ 133
Mavv 16,30,112
S_ m0,s52,18304
E_ 134
Mavv 36,34,112
L_ v6,28,s65,128
W_ 2
Mavv 56,38,112
S_ s65,s59,294912
Tv 30,v2,37888
Tv 32,v2,44032
W_ 2
Mavv 76,42,112
S_ m0,s52,19328
E_ 135
Mavv 72,42,108
Tv 34,v3,37888
Tv 36,v3,44032
Mavv 52,38,108
L_ v6,28,s65,128
Mavv 48,38,104
C_ 135,134,135
C_ 134,132,133
Mavv 68,42,104
E_ 76
Mavv 64,42,100
E_ 77
Mavv 44,38,100
E_ 78
Mavv 40,38,96
E_ 79
Mavv 60,42,96
W_ 0
C_ 116,76,77
C_ 117,78,79
Mavv 80,30,96
s_nop 0
Tv 38,v4,37888
Tv 40,v4,44032
Mavv 100,34,96
E_ 80
Mavv 104,34,100
s_nop 0
Tv 42,v5,37888
Tv 44,v5,44032
Mavv 84,30,100
E_ 81
Mavv 88,30,104
E_ 82
Mavv 108,34,104
E_ 83
Mavv 112,34,108
C_ 120,80,81
C_ 121,82,83
Mavv 92,30,108
E_ 84
Mavv 96,30,112
E_ 85
Mavv 116,34,112
W_ 0
E_ 86
Mavv 136,38,112
s_nop 0
Tv 30,v2,38912
Tv 32,v2,45056
Mavv 156,42,112
E_ 87
Mavv 152,42,108
s_nop 0
Tv 34,v3,38912
Tv 36,v3,45056
Mavv 132,38,108
C_ 124,84,85
C_ 125,86,87
Mavv 128,38,104
E_ 88
Mavv 148,42,104
E_ 89
Mavv 144,42,100
E_ 90
Mavv 124,38,100
E_ 91
Mavv 120,38,96
C_ 128,88,89
C_ 129,90,91
Mavv 140,42,96
W_ 0
E_ 92
Mavv 160,30,96
s_nop 0
Tv 38,v4,38912
Tv 40,v4,45056
Mavv 180,34,96
E_ 93
Mavv 184,34,100
s_nop 0
Tv 42,v5,38912
Tv 44,v5,45056
Mavv 164,30,100
E_ 94
Mavv 168,30,104
E_ 95
Mavv 188,34,104
C_ 132,92,93
C_ 133,94,95
Mavv 192,34,108
v_mov_b32 v92, 0x3f803f80
v_mov_b32 v93, 0x3f803f80
Mavv 172,30,108
s_waitcnt vmcnt(26) lgkmcnt(0)
s_barrier
v_mov_b32 v94, 0x3f803f80
Mavv 176,30,112
v_mov_b32 v95, 0x3f803f80
Mavv 196,34,112
Mavv 216,38,112
s_nop 0
Tv 30,v2,49152
Tv 32,v2,55296
Zvvv 76,92,116
Zvvv 80,92,120
s_nop 0
Tv 34,v3,49152
Tv 36,v3,55296
Zvvv 84,92,124
Zvvv 88,92,128
Zvvv 92,92,132
Mavv 236,42,112
Mavv 232,42,108
Mavv 212,38,108
Mavv 208,38,104
s_nop 1
v_max3_f32 v10, v76, v80, v84
Mavv 228,42,104
v_max3_f32 v10, v10, v88, v92
v_cmp_lt_f32 vcc, 0x42000000, v10
Mavv 224,42,100
Mavv 204,38,100
Mavv 200,38,96
Mavv 220,42,96
s_cbranch_vccnz .Ltramp0_15
.Ljoin0:
W_ 2
Mavv 0,30,116
s_mov_b32 s58, 1
Tv 38,v4,49152
Tv 40,v4,55296
W_ 2
Mavv 20,34,116
S_ s61,s61,393216
R_ v12,a248
s_mov_b32 s85, 4026531840
Mavv 24,34,120
s_nop 0
Tv 42,v5,49152
Tv 44,v5,55296
Mavv 4,30,120
A_ v46,v46,v76
A_ v47,v47,v80
Mavv 8,30,124
A_ v48,v48,v84
A_ v49,v49,v88
Mavv 28,34,124
A_ v50,v50,v92
Mavv 32,34,128
v_rcp_f32 v46, v46
Mavv 12,30,128
v_rcp_f32 v47, v47
Mavv 16,30,132
v_rcp_f32 v48, v48
Mavv 36,34,132
W_ 0
v_rcp_f32 v49, v49
Mavv 56,38,132
s_nop 0
Tv 30,v2,50176
Tv 32,v2,56320
Mavv 76,42,132
v_rcp_f32 v50, v50
Mavv 72,42,128
s_nop 0
Tv 34,v3,50176
Tv 36,v3,56320
Mavv 52,38,128
R_ v96,a0
R_ v97,a1
Mavv 48,38,124
R_ v98,a2
R_ v99,a3
Mavv 68,42,124
R_ v100,a20
R_ v101,a21
Mavv 64,42,120
R_ v102,a22
R_ v103,a23
Mavv 44,38,120
U_ v96,v96,v46
U_ v97,v97,v46
Mavv 40,38,116
U_ v98,v98,v46
U_ v99,v99,v46
Mavv 60,42,116
W_ 0
U_ v100,v100,v46
U_ v101,v101,v46
Mavv 80,30,116
s_nop 0
Tv 38,v4,50176
Tv 40,v4,56320
Mavv 100,34,116
U_ v102,v102,v46
U_ v103,v103,v46
Mavv 104,34,120
s_nop 0
Tv 42,v5,50176
Tv 44,v5,56320
Mavv 84,30,120
C_ 76,96,97
C_ 77,98,99
Mavv 88,30,124
C_ 78,100,101
C_ 79,102,103
Mavv 108,34,124
X_ a0,v76
X_ a1,v77
Mavv 112,34,128
X_ a2,v78
X_ a3,v79
Mavv 92,30,128
R_ v104,a4
R_ v105,a5
Mavv 96,30,132
R_ v106,a6
R_ v107,a7
Mavv 116,34,132
W_ 0
R_ v108,a24
R_ v109,a25
Mavv 136,38,132
s_nop 0
Tv 30,v2,51200
Tv 32,v2,57344
Mavv 156,42,132
R_ v110,a26
R_ v111,a27
Mavv 152,42,128
s_nop 0
Tv 34,v3,51200
Tv 36,v3,57344
Mavv 132,38,128
U_ v104,v104,v47
U_ v105,v105,v47
Mavv 128,38,124
U_ v106,v106,v47
U_ v107,v107,v47
Mavv 148,42,124
U_ v108,v108,v47
U_ v109,v109,v47
Mavv 144,42,120
U_ v110,v110,v47
U_ v111,v111,v47
Mavv 124,38,120
C_ 80,104,105
C_ 81,106,107
Mavv 120,38,116
C_ 82,108,109
C_ 83,110,111
Mavv 140,42,116
W_ 0
X_ a4,v80
X_ a5,v81
Mavv 160,30,116
s_nop 0
Tv 38,v4,51200
Tv 40,v4,57344
Mavv 180,34,116
X_ a6,v82
X_ a7,v83
Mavv 184,34,120
s_nop 0
Tv 42,v5,51200
Tv 44,v5,57344
Mavv 164,30,120
R_ v96,a8
R_ v97,a9
Mavv 168,30,124
R_ v98,a10
R_ v99,a11
Mavv 188,34,124
R_ v100,a28
R_ v101,a29
Mavv 192,34,128
R_ v102,a30
R_ v103,a31
Mavv 172,30,128
U_ v96,v96,v48
U_ v97,v97,v48
Mavv 176,30,132
U_ v98,v98,v48
U_ v99,v99,v48
Mavv 196,34,132
W_ 0
U_ v100,v100,v48
U_ v101,v101,v48
Mavv 216,38,132
U_ v102,v102,v48
U_ v103,v103,v48
Mavv 236,42,132
C_ 84,96,97
C_ 85,98,99
Mavv 232,42,128
C_ 86,100,101
C_ 87,102,103
Mavv 212,38,128
X_ a8,v84
X_ a9,v85
Mavv 208,38,124
X_ a10,v86
X_ a11,v87
Mavv 228,42,124
Mavv 224,42,120
Mavv 204,38,120
Mavv 200,38,116
Mavv 220,42,116
.p2align 3
R_ v104,a12
R_ v105,a13
R_ v106,a14
R_ v107,a15
R_ v108,a32
R_ v109,a33
R_ v110,a34
R_ v111,a35
v_pk_mul_f32 v[104:105], v[104:105], v[48:49] op_sel:[0,1]
v_pk_mul_f32 v[106:107], v[106:107], v[48:49] op_sel:[0,1]
v_pk_mul_f32 v[108:109], v[108:109], v[48:49] op_sel:[0,1]
v_pk_mul_f32 v[110:111], v[110:111], v[48:49] op_sel:[0,1]
C_ 88,104,105
C_ 89,106,107
C_ 90,108,109
C_ 91,110,111
X_ a12,v88
X_ a13,v89
X_ a14,v90
X_ a15,v91
.p2align 3
R_ v96,a16
R_ v97,a17
R_ v98,a18
R_ v99,a19
R_ v100,a36
R_ v101,a37
R_ v102,a38
R_ v103,a39
v_pk_mul_f32 v[96:97], v[96:97], v[50:51] op_sel_hi:[1,0]
v_pk_mul_f32 v[98:99], v[98:99], v[50:51] op_sel_hi:[1,0]
v_pk_mul_f32 v[100:101], v[100:101], v[50:51] op_sel_hi:[1,0]
v_pk_mul_f32 v[102:103], v[102:103], v[50:51] op_sel_hi:[1,0]
C_ 92,96,97
C_ 93,98,99
C_ 94,100,101
C_ 95,102,103
X_ a16,v92
X_ a17,v93
X_ a18,v94
X_ a19,v95
.p2align 3
R_ v104,a40
R_ v105,a41
R_ v106,a42
R_ v107,a43
R_ v108,a60
R_ v109,a61
R_ v110,a62
R_ v111,a63
v_pk_mul_f32 v[104:105], v[104:105], v[46:47] op_sel_hi:[1,0]
v_pk_mul_f32 v[106:107], v[106:107], v[46:47] op_sel_hi:[1,0]
v_pk_mul_f32 v[108:109], v[108:109], v[46:47] op_sel_hi:[1,0]
v_pk_mul_f32 v[110:111], v[110:111], v[46:47] op_sel_hi:[1,0]
C_ 76,104,105
C_ 77,106,107
C_ 78,108,109
C_ 79,110,111
X_ a40,v76
X_ a41,v77
X_ a42,v78
X_ a43,v79
.p2align 3
R_ v96,a44
R_ v97,a45
R_ v98,a46
R_ v99,a47
R_ v100,a64
R_ v101,a65
R_ v102,a66
R_ v103,a67
v_pk_mul_f32 v[96:97], v[96:97], v[46:47] op_sel:[0,1]
v_pk_mul_f32 v[98:99], v[98:99], v[46:47] op_sel:[0,1]
v_pk_mul_f32 v[100:101], v[100:101], v[46:47] op_sel:[0,1]
v_pk_mul_f32 v[102:103], v[102:103], v[46:47] op_sel:[0,1]
C_ 80,96,97
C_ 81,98,99
C_ 82,100,101
C_ 83,102,103
X_ a44,v80
X_ a45,v81
X_ a46,v82
X_ a47,v83
.p2align 3
R_ v104,a48
R_ v105,a49
R_ v106,a50
R_ v107,a51
R_ v108,a68
R_ v109,a69
R_ v110,a70
R_ v111,a71
v_pk_mul_f32 v[104:105], v[104:105], v[48:49] op_sel_hi:[1,0]
v_pk_mul_f32 v[106:107], v[106:107], v[48:49] op_sel_hi:[1,0]
v_pk_mul_f32 v[108:109], v[108:109], v[48:49] op_sel_hi:[1,0]
v_pk_mul_f32 v[110:111], v[110:111], v[48:49] op_sel_hi:[1,0]
C_ 84,104,105
C_ 85,106,107
C_ 86,108,109
C_ 87,110,111
X_ a48,v84
X_ a49,v85
X_ a50,v86
X_ a51,v87
.p2align 3
R_ v96,a52
R_ v97,a53
R_ v98,a54
R_ v99,a55
R_ v100,a72
R_ v101,a73
R_ v102,a74
R_ v103,a75
v_pk_mul_f32 v[96:97], v[96:97], v[48:49] op_sel:[0,1]
v_pk_mul_f32 v[98:99], v[98:99], v[48:49] op_sel:[0,1]
v_pk_mul_f32 v[100:101], v[100:101], v[48:49] op_sel:[0,1]
v_pk_mul_f32 v[102:103], v[102:103], v[48:49] op_sel:[0,1]
C_ 88,96,97
C_ 89,98,99
C_ 90,100,101
C_ 91,102,103
X_ a52,v88
X_ a53,v89
X_ a54,v90
X_ a55,v91
.p2align 3
R_ v104,a56
R_ v105,a57
R_ v106,a58
R_ v107,a59
R_ v108,a76
R_ v109,a77
R_ v110,a78
R_ v111,a79
v_pk_mul_f32 v[104:105], v[104:105], v[50:51] op_sel_hi:[1,0]
v_pk_mul_f32 v[106:107], v[106:107], v[50:51] op_sel_hi:[1,0]
v_pk_mul_f32 v[108:109], v[108:109], v[50:51] op_sel_hi:[1,0]
v_pk_mul_f32 v[110:111], v[110:111], v[50:51] op_sel_hi:[1,0]
C_ 92,104,105
C_ 93,106,107
C_ 94,108,109
C_ 95,110,111
X_ a56,v92
X_ a57,v93
X_ a58,v94
X_ a59,v95
.p2align 3
R_ v96,a80
R_ v97,a81
R_ v98,a82
R_ v99,a83
R_ v100,a100
R_ v101,a101
R_ v102,a102
R_ v103,a103
v_pk_mul_f32 v[96:97], v[96:97], v[46:47] op_sel_hi:[1,0]
v_pk_mul_f32 v[98:99], v[98:99], v[46:47] op_sel_hi:[1,0]
v_pk_mul_f32 v[100:101], v[100:101], v[46:47] op_sel_hi:[1,0]
v_pk_mul_f32 v[102:103], v[102:103], v[46:47] op_sel_hi:[1,0]
C_ 76,96,97
C_ 77,98,99
C_ 78,100,101
C_ 79,102,103
X_ a80,v76
X_ a81,v77
X_ a82,v78
X_ a83,v79
.p2align 3
R_ v104,a84
R_ v105,a85
R_ v106,a86
R_ v107,a87
R_ v108,a104
R_ v109,a105
R_ v110,a106
R_ v111,a107
v_pk_mul_f32 v[104:105], v[104:105], v[46:47] op_sel:[0,1]
v_pk_mul_f32 v[106:107], v[106:107], v[46:47] op_sel:[0,1]
v_pk_mul_f32 v[108:109], v[108:109], v[46:47] op_sel:[0,1]
v_pk_mul_f32 v[110:111], v[110:111], v[46:47] op_sel:[0,1]
C_ 80,104,105
C_ 81,106,107
C_ 82,108,109
C_ 83,110,111
X_ a84,v80
X_ a85,v81
X_ a86,v82
X_ a87,v83
.p2align 3
R_ v96,a88
R_ v97,a89
R_ v98,a90
R_ v99,a91
R_ v100,a108
R_ v101,a109
R_ v102,a110
R_ v103,a111
v_pk_mul_f32 v[96:97], v[96:97], v[48:49] op_sel_hi:[1,0]
v_pk_mul_f32 v[98:99], v[98:99], v[48:49] op_sel_hi:[1,0]
v_pk_mul_f32 v[100:101], v[100:101], v[48:49] op_sel_hi:[1,0]
v_pk_mul_f32 v[102:103], v[102:103], v[48:49] op_sel_hi:[1,0]
C_ 84,96,97
C_ 85,98,99
C_ 86,100,101
C_ 87,102,103
X_ a88,v84
X_ a89,v85
X_ a90,v86
X_ a91,v87
.p2align 3
R_ v104,a92
R_ v105,a93
R_ v106,a94
R_ v107,a95
R_ v108,a112
R_ v109,a113
R_ v110,a114
R_ v111,a115
v_pk_mul_f32 v[104:105], v[104:105], v[48:49] op_sel:[0,1]
v_pk_mul_f32 v[106:107], v[106:107], v[48:49] op_sel:[0,1]
v_pk_mul_f32 v[108:109], v[108:109], v[48:49] op_sel:[0,1]
v_pk_mul_f32 v[110:111], v[110:111], v[48:49] op_sel:[0,1]
C_ 88,104,105
C_ 89,106,107
C_ 90,108,109
C_ 91,110,111
X_ a92,v88
X_ a93,v89
X_ a94,v90
X_ a95,v91
.p2align 3
R_ v96,a96
R_ v97,a97
R_ v98,a98
R_ v99,a99
R_ v100,a116
R_ v101,a117
R_ v102,a118
R_ v103,a119
v_pk_mul_f32 v[96:97], v[96:97], v[50:51] op_sel_hi:[1,0]
v_pk_mul_f32 v[98:99], v[98:99], v[50:51] op_sel_hi:[1,0]
v_pk_mul_f32 v[100:101], v[100:101], v[50:51] op_sel_hi:[1,0]
v_pk_mul_f32 v[102:103], v[102:103], v[50:51] op_sel_hi:[1,0]
C_ 92,96,97
C_ 93,98,99
C_ 94,100,101
C_ 95,102,103
X_ a96,v92
X_ a97,v93
X_ a98,v94
X_ a99,v95
.p2align 3
R_ v104,a120
R_ v105,a121
R_ v106,a122
R_ v107,a123
R_ v108,a140
R_ v109,a141
R_ v110,a142
R_ v111,a143
v_pk_mul_f32 v[104:105], v[104:105], v[46:47] op_sel_hi:[1,0]
v_pk_mul_f32 v[106:107], v[106:107], v[46:47] op_sel_hi:[1,0]
v_pk_mul_f32 v[108:109], v[108:109], v[46:47] op_sel_hi:[1,0]
v_pk_mul_f32 v[110:111], v[110:111], v[46:47] op_sel_hi:[1,0]
C_ 76,104,105
C_ 77,106,107
C_ 78,108,109
C_ 79,110,111
X_ a120,v76
X_ a121,v77
X_ a122,v78
X_ a123,v79
.p2align 3
R_ v96,a124
R_ v97,a125
R_ v98,a126
R_ v99,a127
R_ v100,a144
R_ v101,a145
R_ v102,a146
R_ v103,a147
v_pk_mul_f32 v[96:97], v[96:97], v[46:47] op_sel:[0,1]
v_pk_mul_f32 v[98:99], v[98:99], v[46:47] op_sel:[0,1]
v_pk_mul_f32 v[100:101], v[100:101], v[46:47] op_sel:[0,1]
v_pk_mul_f32 v[102:103], v[102:103], v[46:47] op_sel:[0,1]
C_ 80,96,97
C_ 81,98,99
C_ 82,100,101
C_ 83,102,103
X_ a124,v80
X_ a125,v81
X_ a126,v82
X_ a127,v83
.p2align 3
R_ v104,a128
R_ v105,a129
R_ v106,a130
R_ v107,a131
R_ v108,a148
R_ v109,a149
R_ v110,a150
R_ v111,a151
v_pk_mul_f32 v[104:105], v[104:105], v[48:49] op_sel_hi:[1,0]
v_pk_mul_f32 v[106:107], v[106:107], v[48:49] op_sel_hi:[1,0]
v_pk_mul_f32 v[108:109], v[108:109], v[48:49] op_sel_hi:[1,0]
v_pk_mul_f32 v[110:111], v[110:111], v[48:49] op_sel_hi:[1,0]
C_ 84,104,105
C_ 85,106,107
C_ 86,108,109
C_ 87,110,111
X_ a128,v84
X_ a129,v85
X_ a130,v86
X_ a131,v87
.p2align 3
R_ v96,a132
R_ v97,a133
R_ v98,a134
R_ v99,a135
R_ v100,a152
R_ v101,a153
R_ v102,a154
R_ v103,a155
v_pk_mul_f32 v[96:97], v[96:97], v[48:49] op_sel:[0,1]
v_pk_mul_f32 v[98:99], v[98:99], v[48:49] op_sel:[0,1]
v_pk_mul_f32 v[100:101], v[100:101], v[48:49] op_sel:[0,1]
v_pk_mul_f32 v[102:103], v[102:103], v[48:49] op_sel:[0,1]
C_ 88,96,97
C_ 89,98,99
C_ 90,100,101
C_ 91,102,103
X_ a132,v88
X_ a133,v89
X_ a134,v90
X_ a135,v91
.p2align 3
R_ v104,a136
R_ v105,a137
R_ v106,a138
R_ v107,a139
R_ v108,a156
R_ v109,a157
R_ v110,a158
R_ v111,a159
v_pk_mul_f32 v[104:105], v[104:105], v[50:51] op_sel_hi:[1,0]
v_pk_mul_f32 v[106:107], v[106:107], v[50:51] op_sel_hi:[1,0]
v_pk_mul_f32 v[108:109], v[108:109], v[50:51] op_sel_hi:[1,0]
v_pk_mul_f32 v[110:111], v[110:111], v[50:51] op_sel_hi:[1,0]
C_ 92,104,105
C_ 93,106,107
C_ 94,108,109
C_ 95,110,111
X_ a136,v92
X_ a137,v93
X_ a138,v94
X_ a139,v95
.p2align 3
R_ v96,a160
R_ v97,a161
R_ v98,a162
R_ v99,a163
R_ v100,a180
R_ v101,a181
R_ v102,a182
R_ v103,a183
v_pk_mul_f32 v[96:97], v[96:97], v[46:47] op_sel_hi:[1,0]
v_pk_mul_f32 v[98:99], v[98:99], v[46:47] op_sel_hi:[1,0]
v_pk_mul_f32 v[100:101], v[100:101], v[46:47] op_sel_hi:[1,0]
v_pk_mul_f32 v[102:103], v[102:103], v[46:47] op_sel_hi:[1,0]
C_ 76,96,97
C_ 77,98,99
C_ 78,100,101
C_ 79,102,103
X_ a160,v76
X_ a161,v77
X_ a162,v78
X_ a163,v79
.p2align 3
R_ v104,a164
R_ v105,a165
R_ v106,a166
R_ v107,a167
R_ v108,a184
R_ v109,a185
R_ v110,a186
R_ v111,a187
v_pk_mul_f32 v[104:105], v[104:105], v[46:47] op_sel:[0,1]
v_pk_mul_f32 v[106:107], v[106:107], v[46:47] op_sel:[0,1]
v_pk_mul_f32 v[108:109], v[108:109], v[46:47] op_sel:[0,1]
v_pk_mul_f32 v[110:111], v[110:111], v[46:47] op_sel:[0,1]
C_ 80,104,105
C_ 81,106,107
C_ 82,108,109
C_ 83,110,111
X_ a164,v80
X_ a165,v81
X_ a166,v82
X_ a167,v83
.p2align 3
R_ v96,a168
R_ v97,a169
R_ v98,a170
R_ v99,a171
R_ v100,a188
R_ v101,a189
R_ v102,a190
R_ v103,a191
v_pk_mul_f32 v[96:97], v[96:97], v[48:49] op_sel_hi:[1,0]
v_pk_mul_f32 v[98:99], v[98:99], v[48:49] op_sel_hi:[1,0]
v_pk_mul_f32 v[100:101], v[100:101], v[48:49] op_sel_hi:[1,0]
v_pk_mul_f32 v[102:103], v[102:103], v[48:49] op_sel_hi:[1,0]
C_ 84,96,97
C_ 85,98,99
C_ 86,100,101
C_ 87,102,103
X_ a168,v84
X_ a169,v85
X_ a170,v86
X_ a171,v87
.p2align 3
R_ v104,a172
R_ v105,a173
R_ v106,a174
R_ v107,a175
R_ v108,a192
R_ v109,a193
R_ v110,a194
R_ v111,a195
v_pk_mul_f32 v[104:105], v[104:105], v[48:49] op_sel:[0,1]
v_pk_mul_f32 v[106:107], v[106:107], v[48:49] op_sel:[0,1]
v_pk_mul_f32 v[108:109], v[108:109], v[48:49] op_sel:[0,1]
v_pk_mul_f32 v[110:111], v[110:111], v[48:49] op_sel:[0,1]
C_ 88,104,105
C_ 89,106,107
C_ 90,108,109
C_ 91,110,111
X_ a172,v88
X_ a173,v89
X_ a174,v90
X_ a175,v91
.p2align 3
R_ v96,a176
R_ v97,a177
R_ v98,a178
R_ v99,a179
R_ v100,a196
R_ v101,a197
R_ v102,a198
R_ v103,a199
v_pk_mul_f32 v[96:97], v[96:97], v[50:51] op_sel_hi:[1,0]
v_pk_mul_f32 v[98:99], v[98:99], v[50:51] op_sel_hi:[1,0]
v_pk_mul_f32 v[100:101], v[100:101], v[50:51] op_sel_hi:[1,0]
v_pk_mul_f32 v[102:103], v[102:103], v[50:51] op_sel_hi:[1,0]
C_ 92,96,97
C_ 93,98,99
C_ 94,100,101
C_ 95,102,103
X_ a176,v92
X_ a177,v93
X_ a178,v94
X_ a179,v95
.p2align 3
R_ v104,a200
R_ v105,a201
R_ v106,a202
R_ v107,a203
R_ v108,a220
R_ v109,a221
R_ v110,a222
R_ v111,a223
v_pk_mul_f32 v[104:105], v[104:105], v[46:47] op_sel_hi:[1,0]
v_pk_mul_f32 v[106:107], v[106:107], v[46:47] op_sel_hi:[1,0]
v_pk_mul_f32 v[108:109], v[108:109], v[46:47] op_sel_hi:[1,0]
v_pk_mul_f32 v[110:111], v[110:111], v[46:47] op_sel_hi:[1,0]
C_ 76,104,105
C_ 77,106,107
C_ 78,108,109
C_ 79,110,111
X_ a200,v76
X_ a201,v77
X_ a202,v78
X_ a203,v79
.p2align 3
R_ v96,a204
R_ v97,a205
R_ v98,a206
R_ v99,a207
R_ v100,a224
R_ v101,a225
R_ v102,a226
R_ v103,a227
v_pk_mul_f32 v[96:97], v[96:97], v[46:47] op_sel:[0,1]
v_pk_mul_f32 v[98:99], v[98:99], v[46:47] op_sel:[0,1]
v_pk_mul_f32 v[100:101], v[100:101], v[46:47] op_sel:[0,1]
v_pk_mul_f32 v[102:103], v[102:103], v[46:47] op_sel:[0,1]
C_ 80,96,97
C_ 81,98,99
C_ 82,100,101
C_ 83,102,103
X_ a204,v80
X_ a205,v81
X_ a206,v82
X_ a207,v83
v_mov_b32 v4, v20
v_mov_b32 v5, v21
R_ %0,a242
R_ v2,a243
v_xor_b32 v1, 64, %0
v_xor_b32 v3, 64, v2
s_waitcnt vmcnt(0) lgkmcnt(0)
s_barrier
Ta 20,%0,8192
Ta 22,%0,8704
Ta 24,%0,12288
Ta 26,%0,12800
Da 60,v2,0
Da 100,v3,0
Da 64,v2,2048
Da 104,v3,2048
Da 68,v2,4096
Da 108,v3,4096
Da 72,v2,6144
Da 112,v3,6144
Da 76,v2,8192
Da 116,v3,8192
Ta 28,%0,8200
Ta 30,%0,8712
Ta 32,%0,12296
Ta 34,%0,12808
.p2align 3
R_ v104,a208
R_ v105,a209
R_ v106,a210
R_ v107,a211
R_ v108,a228
R_ v109,a229
R_ v110,a230
R_ v111,a231
v_pk_mul_f32 v[104:105], v[104:105], v[48:49] op_sel_hi:[1,0]
v_pk_mul_f32 v[106:107], v[106:107], v[48:49] op_sel_hi:[1,0]
v_pk_mul_f32 v[108:109], v[108:109], v[48:49] op_sel_hi:[1,0]
v_pk_mul_f32 v[110:111], v[110:111], v[48:49] op_sel_hi:[1,0]
C_ 84,104,105
C_ 85,106,107
C_ 86,108,109
C_ 87,110,111
X_ a208,v84
X_ a209,v85
X_ a210,v86
X_ a211,v87
.p2align 3
R_ v96,a212
R_ v97,a213
R_ v98,a214
R_ v99,a215
R_ v100,a232
R_ v101,a233
R_ v102,a234
R_ v103,a235
v_pk_mul_f32 v[96:97], v[96:97], v[48:49] op_sel:[0,1]
v_pk_mul_f32 v[98:99], v[98:99], v[48:49] op_sel:[0,1]
v_pk_mul_f32 v[100:101], v[100:101], v[48:49] op_sel:[0,1]
v_pk_mul_f32 v[102:103], v[102:103], v[48:49] op_sel:[0,1]
C_ 88,96,97
C_ 89,98,99
C_ 90,100,101
C_ 91,102,103
X_ a212,v88
X_ a213,v89
X_ a214,v90
X_ a215,v91
.p2align 3
R_ v104,a216
R_ v105,a217
R_ v106,a218
R_ v107,a219
R_ v108,a236
R_ v109,a237
R_ v110,a238
R_ v111,a239
v_pk_mul_f32 v[104:105], v[104:105], v[50:51] op_sel_hi:[1,0]
v_pk_mul_f32 v[106:107], v[106:107], v[50:51] op_sel_hi:[1,0]
v_pk_mul_f32 v[108:109], v[108:109], v[50:51] op_sel_hi:[1,0]
v_pk_mul_f32 v[110:111], v[110:111], v[50:51] op_sel_hi:[1,0]
C_ 92,104,105
C_ 93,106,107
C_ 94,108,109
C_ 95,110,111
X_ a216,v92
X_ a217,v93
X_ a218,v94
X_ a219,v95
s_branch .Lq
.Ltramp0_1:
s_mov_b32 s63, 1
s_branch .Lslow1
.Ltramp0_2:
s_mov_b32 s63, 2
s_branch .Lslow0
.Ltramp0_3:
s_mov_b32 s63, 3
s_branch .Lslow1
.Ltramp0_4:
s_mov_b32 s63, 4
s_branch .Lslow0
.Ltramp0_5:
s_mov_b32 s63, 5
s_branch .Lslow1
.Ltramp0_6:
s_mov_b32 s63, 6
s_branch .Lslow0
.Ltramp0_7:
s_mov_b32 s63, 7
s_branch .Lslow1
.Ltramp0_8:
s_mov_b32 s63, 8
s_branch .Lslow0
.Ltramp0_9:
s_mov_b32 s63, 9
s_branch .Lslow1
.Ltramp0_10:
s_mov_b32 s63, 10
s_branch .Lslow0
.Ltramp0_11:
s_mov_b32 s63, 11
s_branch .Lslow1
.Ltramp0_12:
s_mov_b32 s63, 12
s_branch .Lslow0
.Ltramp0_13:
s_mov_b32 s63, 13
s_branch .Lslow1
.Ltramp0_14:
s_mov_b32 s63, 14
s_branch .Lslow0
.Ltramp0_15:
s_mov_b32 s63, 15
s_branch .Lslow1
.Lstep0_16:
Zvvv 76,92,116
Zvvv 80,92,120
Zvvv 84,92,124
Zvvv 88,92,128
Zvvv 92,92,132
s_nop 5
v_max3_f32 v10, v76, v80, v84
s_nop 0
v_max3_f32 v10, v10, v88, v92
v_cmp_lt_f32 vcc, 0x42000000, v10
s_cbranch_vccnz .Ltramp0_15
s_branch .Ljoin0
.Ltramp1_1:
s_mov_b32 s63, 1
s_branch .Lslow1
.Ltramp1_2:
s_mov_b32 s63, 2
s_branch .Lslow0
.Ltramp1_3:
s_mov_b32 s63, 3
s_branch .Lslow1
.Ltramp1_4:
s_mov_b32 s63, 4
s_branch .Lslow0
.Ltramp1_5:
s_mov_b32 s63, 5
s_branch .Lslow1
.Ltramp1_6:
s_mov_b32 s63, 6
s_branch .Lslow0
.Ltramp1_7:
s_mov_b32 s63, 7
s_branch .Lslow1
.Ltramp1_8:
s_mov_b32 s63, 8
s_branch .Lslow0
.Ltramp1_9:
s_mov_b32 s63, 9
s_branch .Lslow1
.Ltramp1_10:
s_mov_b32 s63, 10
s_branch .Lslow0
.Ltramp1_11:
s_mov_b32 s63, 11
s_branch .Lslow1
.Ltramp1_12:
s_mov_b32 s63, 12
s_branch .Lslow0
.Ltramp1_13:
s_mov_b32 s63, 13
s_branch .Lslow1
.Ltramp1_14:
s_mov_b32 s63, 14
s_branch .Lslow0
.Ltramp1_15:
s_mov_b32 s63, 15
s_branch .Lslow1
.Lstep1_16:
Zvvv 76,92,116
Zvvv 80,92,120
Zvvv 84,92,124
Zvvv 88,92,128
Zvvv 92,92,132
s_nop 5
v_max3_f32 v10, v76, v80, v84
s_nop 0
v_max3_f32 v10, v10, v88, v92
v_cmp_lt_f32 vcc, 0x42000000, v10
s_cbranch_vccnz .Ltramp1_15
s_branch .Ljoin1
.Ltramp2_1:
s_mov_b32 s63, 1
s_branch .Lslow1
.Ltramp2_2:
s_mov_b32 s63, 2
s_branch .Lslow0
.Ltramp2_3:
s_mov_b32 s63, 3
s_branch .Lslow1
.Ltramp2_4:
s_mov_b32 s63, 4
s_branch .Lslow0
.Ltramp2_5:
s_mov_b32 s63, 5
s_branch .Lslow1
.Ltramp2_6:
s_mov_b32 s63, 6
s_branch .Lslow0
.Ltramp2_7:
s_mov_b32 s63, 7
s_branch .Lslow1
.Ltramp2_8:
s_mov_b32 s63, 8
s_branch .Lslow0
.Ltramp2_9:
s_mov_b32 s63, 9
s_branch .Lslow1
.Ltramp2_10:
s_mov_b32 s63, 10
s_branch .Lslow0
.Ltramp2_11:
s_mov_b32 s63, 11
s_branch .Lslow1
.Ltramp2_12:
s_mov_b32 s63, 12
s_branch .Lslow0
.Ltramp2_13:
s_mov_b32 s63, 13
s_branch .Lslow1
.Ltramp2_14:
s_mov_b32 s63, 14
s_branch .Lslow0
.Ltramp2_15:
s_mov_b32 s63, 15
s_branch .Lslow1
.Lstep2_16:
Zvvv 76,92,116
Zvvv 80,92,120
Zvvv 84,92,124
Zvvv 88,92,128
Zvvv 92,92,132
s_nop 5
v_max3_f32 v10, v76, v80, v84
s_nop 0
v_max3_f32 v10, v10, v88, v92
v_cmp_lt_f32 vcc, 0x42000000, v10
s_cbranch_vccnz .Ltramp2_15
s_branch .Ljoin2
.Lslow0:
W_ 0
s_nop 7
s_nop 7
s_and_b32 s64, s61, 4294959104
s_mul_i32 s53, s63, 12288
S_ s64,s64,s53
R_ v11,a248
v_and_b32 v10, 0xf0, v11
v_mul_u32_u24 v10, 24, v10
v_lshrrev_b32 v11, 8, v11
v_lshl_add_u32 v10, v11, 4, v10
S_ s53,s64,6144
buffer_load_dwordx4 v[116:119], v10, s[32:35], s53 offen offset:0
S_ s53,s64,6144
buffer_load_dwordx4 v[120:123], v10, s[32:35], s53 offen offset:64
S_ s53,s64,6144
buffer_load_dwordx4 v[124:127], v10, s[32:35], s53 offen offset:128
S_ s53,s64,6144
)A"
R"A(buffer_load_dwordx4 v[128:131], v10, s[32:35], s53 offen offset:192
s_waitcnt vmcnt(0)
Nvvv 96,116,136,56
Nvvv 100,116,140,60
Nvvv 104,116,144,64
Nvvv 108,116,148,68
Nvvv 112,116,152,72
Mvvv 96,120,156
Mvvv 100,120,160
Mvvv 104,120,164
Mvvv 108,120,168
Mvvv 112,120,172
Mvvv 96,124,176
Mvvv 100,124,180
Mvvv 104,124,184
Mvvv 108,124,188
Mvvv 112,124,192
Mvvv 96,128,196
Mvvv 100,128,200
Mvvv 104,128,204
Mvvv 108,128,208
Mvvv 112,128,212
s_nop 7
S_ s53,s64,6144
buffer_load_dwordx4 v[116:119], v10, s[32:35], s53 offen offset:256
S_ s53,s64,6144
buffer_load_dwordx4 v[120:123], v10, s[32:35], s53 offen offset:320
S_ s53,s64,0
buffer_load_dwordx4 v[124:127], v10, s[32:35], s53 offen offset:0
S_ s53,s64,0
buffer_load_dwordx4 v[128:131], v10, s[32:35], s53 offen offset:64
s_waitcnt vmcnt(0)
Mvvv 96,116,216
Mvvv 100,116,220
Mvvv 104,116,224
Mvvv 108,116,228
Mvvv 112,116,232
Mvvv 96,120,236
Mvvv 100,120,240
Mvvv 104,120,244
Mvvv 108,120,248
Mvvv 112,120,252
Nvvv 76,124,136,56
Nvvv 80,124,140,60
Nvvv 84,124,144,64
Nvvv 88,124,148,68
Nvvv 92,124,152,72
Mvvv 76,128,156
Mvvv 80,128,160
Mvvv 84,128,164
Mvvv 88,128,168
Mvvv 92,128,172
s_nop 7
S_ s53,s64,0
buffer_load_dwordx4 v[116:119], v10, s[32:35], s53 offen offset:128
S_ s53,s64,0
buffer_load_dwordx4 v[120:123], v10, s[32:35], s53 offen offset:192
S_ s53,s64,0
buffer_load_dwordx4 v[124:127], v10, s[32:35], s53 offen offset:256
S_ s53,s64,0
buffer_load_dwordx4 v[128:131], v10, s[32:35], s53 offen offset:320
s_waitcnt vmcnt(0)
Mvvv 76,116,176
Mvvv 80,116,180
Mvvv 84,116,184
Mvvv 88,116,188
Mvvv 92,116,192
Mvvv 76,120,196
Mvvv 80,120,200
Mvvv 84,120,204
Mvvv 88,120,208
Mvvv 92,120,212
Mvvv 76,124,216
Mvvv 80,124,220
Mvvv 84,124,224
Mvvv 88,124,228
Mvvv 92,124,232
Mvvv 76,128,236
Mvvv 80,128,240
Mvvv 84,128,244
Mvvv 88,128,248
Mvvv 92,128,252
s_nop 7
s_nop 2
v_max3_f32 v10, v76, v77, v78
v_max3_f32 v10, v10, v79, v96
v_max3_f32 v10, v10, v97, v98
v_max_f32 v10, v10, v99
v_mov_b32 v11, v10
s_nop 1
v_permlane16_swap_b32 v10, v11
v_max_f32 v10, v10, v11
v_mov_b32 v11, v10
s_nop 1
v_permlane32_swap_b32 v10, v11
v_max_f32 v10, v10, v11
A_ v10,0x40e00000,v10
v_max_f32 v10, 0, v10
v_exp_f32 v11, -v10
s_nop 0
U_ v46,v46,v11
R_ v12,a0
U_ v12,v12,v11
X_ a0,v12
R_ v13,a1
U_ v13,v13,v11
X_ a1,v13
R_ v12,a2
U_ v12,v12,v11
X_ a2,v12
R_ v13,a3
U_ v13,v13,v11
X_ a3,v13
R_ v12,a20
U_ v12,v12,v11
X_ a20,v12
R_ v13,a21
U_ v13,v13,v11
X_ a21,v13
R_ v12,a22
U_ v12,v12,v11
X_ a22,v12
R_ v13,a23
U_ v13,v13,v11
X_ a23,v13
R_ v12,a40
U_ v12,v12,v11
X_ a40,v12
R_ v13,a41
U_ v13,v13,v11
X_ a41,v13
R_ v12,a42
U_ v12,v12,v11
X_ a42,v12
R_ v13,a43
U_ v13,v13,v11
X_ a43,v13
R_ v12,a60
U_ v12,v12,v11
X_ a60,v12
R_ v13,a61
U_ v13,v13,v11
X_ a61,v13
R_ v12,a62
U_ v12,v12,v11
X_ a62,v12
R_ v13,a63
U_ v13,v13,v11
X_ a63,v13
R_ v12,a80
U_ v12,v12,v11
X_ a80,v12
R_ v13,a81
U_ v13,v13,v11
X_ a81,v13
R_ v12,a82
U_ v12,v12,v11
X_ a82,v12
R_ v13,a83
U_ v13,v13,v11
X_ a83,v13
R_ v12,a100
U_ v12,v12,v11
X_ a100,v12
R_ v13,a101
U_ v13,v13,v11
X_ a101,v13
R_ v12,a102
U_ v12,v12,v11
X_ a102,v12
R_ v13,a103
U_ v13,v13,v11
X_ a103,v13
R_ v12,a120
U_ v12,v12,v11
X_ a120,v12
R_ v13,a121
U_ v13,v13,v11
X_ a121,v13
R_ v12,a122
U_ v12,v12,v11
X_ a122,v12
R_ v13,a123
U_ v13,v13,v11
X_ a123,v13
R_ v12,a140
U_ v12,v12,v11
X_ a140,v12
R_ v13,a141
U_ v13,v13,v11
X_ a141,v13
R_ v12,a142
U_ v12,v12,v11
X_ a142,v12
R_ v13,a143
U_ v13,v13,v11
X_ a143,v13
R_ v12,a160
U_ v12,v12,v11
X_ a160,v12
R_ v13,a161
U_ v13,v13,v11
X_ a161,v13
R_ v12,a162
U_ v12,v12,v11
X_ a162,v12
R_ v13,a163
U_ v13,v13,v11
X_ a163,v13
R_ v12,a180
U_ v12,v12,v11
X_ a180,v12
R_ v13,a181
U_ v13,v13,v11
X_ a181,v13
R_ v12,a182
U_ v12,v12,v11
X_ a182,v12
R_ v13,a183
U_ v13,v13,v11
X_ a183,v13
R_ v12,a200
U_ v12,v12,v11
X_ a200,v12
R_ v13,a201
U_ v13,v13,v11
X_ a201,v13
R_ v12,a202
U_ v12,v12,v11
X_ a202,v12
R_ v13,a203
U_ v13,v13,v11
X_ a203,v13
R_ v12,a220
U_ v12,v12,v11
X_ a220,v12
R_ v13,a221
U_ v13,v13,v11
X_ a221,v13
R_ v12,a222
U_ v12,v12,v11
X_ a222,v12
R_ v13,a223
U_ v13,v13,v11
X_ a223,v13
B_ v56,v56,v10
B_ v57,v57,v10
B_ v58,v58,v10
B_ v59,v59,v10
B_ v76,v76,v10
B_ v77,v77,v10
B_ v78,v78,v10
B_ v79,v79,v10
B_ v96,v96,v10
B_ v97,v97,v10
B_ v98,v98,v10
B_ v99,v99,v10
E_ 76
E_ 77
E_ 78
E_ 79
E_ 96
E_ 97
E_ 98
E_ 99
s_nop 0
C_ 99,98,99
C_ 98,96,97
C_ 96,76,77
C_ 97,78,79
v_max3_f32 v10, v80, v81, v82
v_max3_f32 v10, v10, v83, v100
v_max3_f32 v10, v10, v101, v102
v_max_f32 v10, v10, v103
v_mov_b32 v11, v10
s_nop 1
v_permlane16_swap_b32 v10, v11
v_max_f32 v10, v10, v11
v_mov_b32 v11, v10
s_nop 1
v_permlane32_swap_b32 v10, v11
v_max_f32 v10, v10, v11
A_ v10,0x40e00000,v10
v_max_f32 v10, 0, v10
v_exp_f32 v11, -v10
s_nop 0
U_ v47,v47,v11
R_ v12,a4
U_ v12,v12,v11
X_ a4,v12
R_ v13,a5
U_ v13,v13,v11
X_ a5,v13
R_ v12,a6
U_ v12,v12,v11
X_ a6,v12
R_ v13,a7
U_ v13,v13,v11
X_ a7,v13
R_ v12,a24
U_ v12,v12,v11
X_ a24,v12
R_ v13,a25
U_ v13,v13,v11
X_ a25,v13
R_ v12,a26
U_ v12,v12,v11
X_ a26,v12
R_ v13,a27
U_ v13,v13,v11
X_ a27,v13
R_ v12,a44
U_ v12,v12,v11
X_ a44,v12
R_ v13,a45
U_ v13,v13,v11
X_ a45,v13
R_ v12,a46
U_ v12,v12,v11
X_ a46,v12
R_ v13,a47
U_ v13,v13,v11
X_ a47,v13
R_ v12,a64
U_ v12,v12,v11
X_ a64,v12
R_ v13,a65
U_ v13,v13,v11
X_ a65,v13
R_ v12,a66
U_ v12,v12,v11
X_ a66,v12
R_ v13,a67
U_ v13,v13,v11
X_ a67,v13
R_ v12,a84
U_ v12,v12,v11
X_ a84,v12
R_ v13,a85
U_ v13,v13,v11
X_ a85,v13
R_ v12,a86
U_ v12,v12,v11
X_ a86,v12
R_ v13,a87
U_ v13,v13,v11
X_ a87,v13
R_ v12,a104
U_ v12,v12,v11
X_ a104,v12
R_ v13,a105
U_ v13,v13,v11
X_ a105,v13
R_ v12,a106
U_ v12,v12,v11
X_ a106,v12
R_ v13,a107
U_ v13,v13,v11
X_ a107,v13
R_ v12,a124
U_ v12,v12,v11
X_ a124,v12
R_ v13,a125
U_ v13,v13,v11
X_ a125,v13
R_ v12,a126
U_ v12,v12,v11
X_ a126,v12
R_ v13,a127
U_ v13,v13,v11
X_ a127,v13
R_ v12,a144
U_ v12,v12,v11
X_ a144,v12
R_ v13,a145
U_ v13,v13,v11
X_ a145,v13
R_ v12,a146
U_ v12,v12,v11
X_ a146,v12
R_ v13,a147
U_ v13,v13,v11
X_ a147,v13
R_ v12,a164
U_ v12,v12,v11
X_ a164,v12
R_ v13,a165
U_ v13,v13,v11
X_ a165,v13
R_ v12,a166
U_ v12,v12,v11
X_ a166,v12
R_ v13,a167
U_ v13,v13,v11
X_ a167,v13
R_ v12,a184
U_ v12,v12,v11
X_ a184,v12
R_ v13,a185
U_ v13,v13,v11
X_ a185,v13
R_ v12,a186
U_ v12,v12,v11
X_ a186,v12
R_ v13,a187
U_ v13,v13,v11
X_ a187,v13
R_ v12,a204
U_ v12,v12,v11
X_ a204,v12
R_ v13,a205
U_ v13,v13,v11
X_ a205,v13
R_ v12,a206
U_ v12,v12,v11
X_ a206,v12
R_ v13,a207
U_ v13,v13,v11
X_ a207,v13
R_ v12,a224
U_ v12,v12,v11
X_ a224,v12
R_ v13,a225
U_ v13,v13,v11
X_ a225,v13
R_ v12,a226
U_ v12,v12,v11
X_ a226,v12
R_ v13,a227
U_ v13,v13,v11
X_ a227,v13
B_ v60,v60,v10
B_ v61,v61,v10
B_ v62,v62,v10
B_ v63,v63,v10
B_ v80,v80,v10
B_ v81,v81,v10
B_ v82,v82,v10
B_ v83,v83,v10
B_ v100,v100,v10
B_ v101,v101,v10
B_ v102,v102,v10
B_ v103,v103,v10
E_ 80
E_ 81
E_ 82
E_ 83
E_ 100
E_ 101
E_ 102
E_ 103
s_nop 0
C_ 103,102,103
C_ 102,100,101
C_ 100,80,81
C_ 101,82,83
v_max3_f32 v10, v84, v85, v86
v_max3_f32 v10, v10, v87, v104
v_max3_f32 v10, v10, v105, v106
v_max_f32 v10, v10, v107
v_mov_b32 v11, v10
s_nop 1
v_permlane16_swap_b32 v10, v11
v_max_f32 v10, v10, v11
v_mov_b32 v11, v10
s_nop 1
v_permlane32_swap_b32 v10, v11
v_max_f32 v10, v10, v11
A_ v10,0x40e00000,v10
v_max_f32 v10, 0, v10
v_exp_f32 v11, -v10
s_nop 0
U_ v48,v48,v11
R_ v12,a8
U_ v12,v12,v11
X_ a8,v12
R_ v13,a9
U_ v13,v13,v11
X_ a9,v13
R_ v12,a10
U_ v12,v12,v11
X_ a10,v12
R_ v13,a11
U_ v13,v13,v11
X_ a11,v13
R_ v12,a28
U_ v12,v12,v11
X_ a28,v12
R_ v13,a29
U_ v13,v13,v11
X_ a29,v13
R_ v12,a30
U_ v12,v12,v11
X_ a30,v12
R_ v13,a31
U_ v13,v13,v11
X_ a31,v13
R_ v12,a48
U_ v12,v12,v11
X_ a48,v12
R_ v13,a49
U_ v13,v13,v11
X_ a49,v13
R_ v12,a50
U_ v12,v12,v11
X_ a50,v12
R_ v13,a51
U_ v13,v13,v11
X_ a51,v13
R_ v12,a68
U_ v12,v12,v11
X_ a68,v12
R_ v13,a69
U_ v13,v13,v11
X_ a69,v13
R_ v12,a70
U_ v12,v12,v11
X_ a70,v12
R_ v13,a71
U_ v13,v13,v11
X_ a71,v13
R_ v12,a88
U_ v12,v12,v11
X_ a88,v12
R_ v13,a89
U_ v13,v13,v11
X_ a89,v13
R_ v12,a90
U_ v12,v12,v11
X_ a90,v12
R_ v13,a91
U_ v13,v13,v11
X_ a91,v13
R_ v12,a108
U_ v12,v12,v11
X_ a108,v12
R_ v13,a109
U_ v13,v13,v11
X_ a109,v13
R_ v12,a110
U_ v12,v12,v11
X_ a110,v12
R_ v13,a111
U_ v13,v13,v11
X_ a111,v13
R_ v12,a128
U_ v12,v12,v11
X_ a128,v12
R_ v13,a129
U_ v13,v13,v11
X_ a129,v13
R_ v12,a130
U_ v12,v12,v11
X_ a130,v12
R_ v13,a131
U_ v13,v13,v11
X_ a131,v13
R_ v12,a148
U_ v12,v12,v11
X_ a148,v12
R_ v13,a149
U_ v13,v13,v11
X_ a149,v13
R_ v12,a150
U_ v12,v12,v11
X_ a150,v12
R_ v13,a151
U_ v13,v13,v11
X_ a151,v13
R_ v12,a168
U_ v12,v12,v11
X_ a168,v12
R_ v13,a169
U_ v13,v13,v11
X_ a169,v13
R_ v12,a170
U_ v12,v12,v11
X_ a170,v12
R_ v13,a171
U_ v13,v13,v11
X_ a171,v13
R_ v12,a188
U_ v12,v12,v11
X_ a188,v12
R_ v13,a189
U_ v13,v13,v11
X_ a189,v13
R_ v12,a190
U_ v12,v12,v11
X_ a190,v12
R_ v13,a191
U_ v13,v13,v11
X_ a191,v13
R_ v12,a208
U_ v12,v12,v11
X_ a208,v12
R_ v13,a209
U_ v13,v13,v11
X_ a209,v13
R_ v12,a210
U_ v12,v12,v11
X_ a210,v12
R_ v13,a211
U_ v13,v13,v11
X_ a211,v13
R_ v12,a228
U_ v12,v12,v11
X_ a228,v12
R_ v13,a229
U_ v13,v13,v11
X_ a229,v13
R_ v12,a230
U_ v12,v12,v11
X_ a230,v12
R_ v13,a231
U_ v13,v13,v11
X_ a231,v13
B_ v64,v64,v10
B_ v65,v65,v10
B_ v66,v66,v10
B_ v67,v67,v10
B_ v84,v84,v10
B_ v85,v85,v10
B_ v86,v86,v10
B_ v87,v87,v10
B_ v104,v104,v10
B_ v105,v105,v10
B_ v106,v106,v10
B_ v107,v107,v10
E_ 84
E_ 85
E_ 86
E_ 87
E_ 104
E_ 105
E_ 106
E_ 107
s_nop 0
C_ 107,106,107
C_ 106,104,105
C_ 104,84,85
C_ 105,86,87
v_max3_f32 v10, v88, v89, v90
v_max3_f32 v10, v10, v91, v108
v_max3_f32 v10, v10, v109, v110
v_max_f32 v10, v10, v111
v_mov_b32 v11, v10
s_nop 1
v_permlane16_swap_b32 v10, v11
v_max_f32 v10, v10, v11
v_mov_b32 v11, v10
s_nop 1
v_permlane32_swap_b32 v10, v11
v_max_f32 v10, v10, v11
A_ v10,0x40e00000,v10
v_max_f32 v10, 0, v10
v_exp_f32 v11, -v10
s_nop 0
U_ v49,v49,v11
R_ v12,a12
U_ v12,v12,v11
X_ a12,v12
R_ v13,a13
U_ v13,v13,v11
X_ a13,v13
R_ v12,a14
U_ v12,v12,v11
X_ a14,v12
R_ v13,a15
U_ v13,v13,v11
X_ a15,v13
R_ v12,a32
U_ v12,v12,v11
X_ a32,v12
R_ v13,a33
U_ v13,v13,v11
X_ a33,v13
R_ v12,a34
U_ v12,v12,v11
X_ a34,v12
R_ v13,a35
U_ v13,v13,v11
X_ a35,v13
R_ v12,a52
U_ v12,v12,v11
X_ a52,v12
R_ v13,a53
U_ v13,v13,v11
X_ a53,v13
R_ v12,a54
U_ v12,v12,v11
X_ a54,v12
R_ v13,a55
U_ v13,v13,v11
X_ a55,v13
R_ v12,a72
U_ v12,v12,v11
X_ a72,v12
R_ v13,a73
U_ v13,v13,v11
X_ a73,v13
R_ v12,a74
U_ v12,v12,v11
X_ a74,v12
R_ v13,a75
U_ v13,v13,v11
X_ a75,v13
R_ v12,a92
U_ v12,v12,v11
X_ a92,v12
R_ v13,a93
U_ v13,v13,v11
X_ a93,v13
R_ v12,a94
U_ v12,v12,v11
X_ a94,v12
R_ v13,a95
U_ v13,v13,v11
X_ a95,v13
R_ v12,a112
U_ v12,v12,v11
X_ a112,v12
R_ v13,a113
U_ v13,v13,v11
X_ a113,v13
R_ v12,a114
U_ v12,v12,v11
X_ a114,v12
R_ v13,a115
U_ v13,v13,v11
X_ a115,v13
R_ v12,a132
U_ v12,v12,v11
X_ a132,v12
R_ v13,a133
U_ v13,v13,v11
X_ a133,v13
R_ v12,a134
U_ v12,v12,v11
X_ a134,v12
R_ v13,a135
U_ v13,v13,v11
X_ a135,v13
R_ v12,a152
U_ v12,v12,v11
X_ a152,v12
R_ v13,a153
U_ v13,v13,v11
X_ a153,v13
R_ v12,a154
U_ v12,v12,v11
X_ a154,v12
R_ v13,a155
U_ v13,v13,v11
X_ a155,v13
R_ v12,a172
U_ v12,v12,v11
X_ a172,v12
R_ v13,a173
U_ v13,v13,v11
X_ a173,v13
R_ v12,a174
U_ v12,v12,v11
X_ a174,v12
R_ v13,a175
U_ v13,v13,v11
X_ a175,v13
R_ v12,a192
U_ v12,v12,v11
X_ a192,v12
R_ v13,a193
U_ v13,v13,v11
X_ a193,v13
R_ v12,a194
U_ v12,v12,v11
X_ a194,v12
R_ v13,a195
U_ v13,v13,v11
X_ a195,v13
R_ v12,a212
U_ v12,v12,v11
X_ a212,v12
R_ v13,a213
U_ v13,v13,v11
X_ a213,v13
R_ v12,a214
U_ v12,v12,v11
X_ a214,v12
R_ v13,a215
U_ v13,v13,v11
X_ a215,v13
R_ v12,a232
U_ v12,v12,v11
X_ a232,v12
R_ v13,a233
U_ v13,v13,v11
X_ a233,v13
R_ v12,a234
U_ v12,v12,v11
X_ a234,v12
R_ v13,a235
U_ v13,v13,v11
X_ a235,v13
B_ v68,v68,v10
B_ v69,v69,v10
B_ v70,v70,v10
B_ v71,v71,v10
B_ v88,v88,v10
B_ v89,v89,v10
B_ v90,v90,v10
B_ v91,v91,v10
B_ v108,v108,v10
B_ v109,v109,v10
B_ v110,v110,v10
B_ v111,v111,v10
E_ 88
E_ 89
E_ 90
E_ 91
E_ 108
E_ 109
E_ 110
E_ 111
s_nop 0
C_ 111,110,111
C_ 110,108,109
C_ 108,88,89
C_ 109,90,91
v_max3_f32 v10, v92, v93, v94
v_max3_f32 v10, v10, v95, v112
v_max3_f32 v10, v10, v113, v114
v_max_f32 v10, v10, v115
v_mov_b32 v11, v10
s_nop 1
v_permlane16_swap_b32 v10, v11
v_max_f32 v10, v10, v11
v_mov_b32 v11, v10
s_nop 1
v_permlane32_swap_b32 v10, v11
v_max_f32 v10, v10, v11
A_ v10,0x40e00000,v10
v_max_f32 v10, 0, v10
v_exp_f32 v11, -v10
s_nop 0
U_ v50,v50,v11
R_ v12,a16
U_ v12,v12,v11
X_ a16,v12
R_ v13,a17
U_ v13,v13,v11
X_ a17,v13
R_ v12,a18
U_ v12,v12,v11
X_ a18,v12
R_ v13,a19
U_ v13,v13,v11
X_ a19,v13
R_ v12,a36
U_ v12,v12,v11
X_ a36,v12
R_ v13,a37
U_ v13,v13,v11
X_ a37,v13
R_ v12,a38
U_ v12,v12,v11
X_ a38,v12
R_ v13,a39
U_ v13,v13,v11
X_ a39,v13
R_ v12,a56
U_ v12,v12,v11
X_ a56,v12
R_ v13,a57
U_ v13,v13,v11
X_ a57,v13
R_ v12,a58
U_ v12,v12,v11
X_ a58,v12
R_ v13,a59
U_ v13,v13,v11
X_ a59,v13
R_ v12,a76
U_ v12,v12,v11
X_ a76,v12
R_ v13,a77
U_ v13,v13,v11
X_ a77,v13
R_ v12,a78
U_ v12,v12,v11
X_ a78,v12
R_ v13,a79
U_ v13,v13,v11
X_ a79,v13
R_ v12,a96
U_ v12,v12,v11
X_ a96,v12
R_ v13,a97
U_ v13,v13,v11
X_ a97,v13
R_ v12,a98
U_ v12,v12,v11
X_ a98,v12
R_ v13,a99
U_ v13,v13,v11
X_ a99,v13
R_ v12,a116
U_ v12,v12,v11
X_ a116,v12
R_ v13,a117
U_ v13,v13,v11
X_ a117,v13
R_ v12,a118
U_ v12,v12,v11
X_ a118,v12
R_ v13,a119
U_ v13,v13,v11
X_ a119,v13
R_ v12,a136
U_ v12,v12,v11
X_ a136,v12
R_ v13,a137
U_ v13,v13,v11
X_ a137,v13
R_ v12,a138
U_ v12,v12,v11
X_ a138,v12
R_ v13,a139
U_ v13,v13,v11
X_ a139,v13
R_ v12,a156
U_ v12,v12,v11
X_ a156,v12
R_ v13,a157
U_ v13,v13,v11
X_ a157,v13
R_ v12,a158
U_ v12,v12,v11
X_ a158,v12
R_ v13,a159
U_ v13,v13,v11
X_ a159,v13
R_ v12,a176
U_ v12,v12,v11
X_ a176,v12
R_ v13,a177
U_ v13,v13,v11
X_ a177,v13
R_ v12,a178
U_ v12,v12,v11
X_ a178,v12
R_ v13,a179
U_ v13,v13,v11
X_ a179,v13
R_ v12,a196
U_ v12,v12,v11
X_ a196,v12
R_ v13,a197
U_ v13,v13,v11
X_ a197,v13
R_ v12,a198
U_ v12,v12,v11
X_ a198,v12
R_ v13,a199
U_ v13,v13,v11
X_ a199,v13
R_ v12,a216
U_ v12,v12,v11
X_ a216,v12
R_ v13,a217
U_ v13,v13,v11
X_ a217,v13
R_ v12,a218
U_ v12,v12,v11
X_ a218,v12
R_ v13,a219
U_ v13,v13,v11
X_ a219,v13
R_ v12,a236
U_ v12,v12,v11
X_ a236,v12
R_ v13,a237
U_ v13,v13,v11
X_ a237,v13
R_ v12,a238
U_ v12,v12,v11
X_ a238,v12
R_ v13,a239
U_ v13,v13,v11
X_ a239,v13
B_ v72,v72,v10
B_ v73,v73,v10
B_ v74,v74,v10
B_ v75,v75,v10
B_ v92,v92,v10
B_ v93,v93,v10
B_ v94,v94,v10
B_ v95,v95,v10
B_ v112,v112,v10
B_ v113,v113,v10
B_ v114,v114,v10
B_ v115,v115,v10
E_ 92
E_ 93
E_ 94
E_ 95
E_ 112
E_ 113
E_ 114
E_ 115
s_nop 0
C_ 115,114,115
C_ 114,112,113
C_ 112,92,93
C_ 113,94,95
v_mov_b32 v92, 0x3f803f80
v_mov_b32 v93, 0x3f803f80
v_mov_b32 v94, 0x3f803f80
v_mov_b32 v95, 0x3f803f80
s_nop 7
s_lshl_b32 s53, s58, 4
S_ s53,s53,s63
s_cmp_eq_u32 s53, 2
s_cbranch_scc1 .Lstep0_3
s_cmp_eq_u32 s53, 4
s_cbranch_scc1 .Lstep0_5
s_cmp_eq_u32 s53, 6
s_cbranch_scc1 .Lstep0_7
s_cmp_eq_u32 s53, 8
s_cbranch_scc1 .Lstep0_9
s_cmp_eq_u32 s53, 10
s_cbranch_scc1 .Lstep0_11
s_cmp_eq_u32 s53, 12
s_cbranch_scc1 .Lstep0_13
s_cmp_eq_u32 s53, 14
s_cbranch_scc1 .Lstep0_15
s_cmp_eq_u32 s53, 18
s_cbranch_scc1 .Lstep1_3
s_cmp_eq_u32 s53, 20
s_cbranch_scc1 .Lstep1_5
s_cmp_eq_u32 s53, 22
s_cbranch_scc1 .Lstep1_7
s_cmp_eq_u32 s53, 24
s_cbranch_scc1 .Lstep1_9
s_cmp_eq_u32 s53, 26
s_cbranch_scc1 .Lstep1_11
s_cmp_eq_u32 s53, 28
s_cbranch_scc1 .Lstep1_13
s_cmp_eq_u32 s53, 30
s_cbranch_scc1 .Lstep1_15
s_cmp_eq_u32 s53, 34
s_cbranch_scc1 .Lstep2_3
s_cmp_eq_u32 s53, 36
s_cbranch_scc1 .Lstep2_5
s_cmp_eq_u32 s53, 38
s_cbranch_scc1 .Lstep2_7
s_cmp_eq_u32 s53, 40
s_cbranch_scc1 .Lstep2_9
s_cmp_eq_u32 s53, 42
s_cbranch_scc1 .Lstep2_11
s_cmp_eq_u32 s53, 44
s_cbranch_scc1 .Lstep2_13
s_cmp_eq_u32 s53, 46
s_cbranch_scc1 .Lstep2_15
s_endpgm
.Lslow1:
W_ 0
s_nop 7
s_nop 7
s_and_b32 s64, s61, 4294959104
s_mul_i32 s53, s63, 12288
S_ s64,s64,s53
R_ v11,a248
v_and_b32 v10, 0xf0, v11
v_mul_u32_u24 v10, 24, v10
v_lshrrev_b32 v11, 8, v11
v_lshl_add_u32 v10, v11, 4, v10
S_ s53,s64,6144
buffer_load_dwordx4 v[96:99], v10, s[32:35], s53 offen offset:0
S_ s53,s64,6144
buffer_load_dwordx4 v[100:103], v10, s[32:35], s53 offen offset:64
S_ s53,s64,6144
buffer_load_dwordx4 v[104:107], v10, s[32:35], s53 offen offset:128
S_ s53,s64,6144
buffer_load_dwordx4 v[108:111], v10, s[32:35], s53 offen offset:192
s_waitcnt vmcnt(0)
Nvvv 116,96,136,56
Nvvv 120,96,140,60
Nvvv 124,96,144,64
Nvvv 128,96,148,68
Nvvv 132,96,152,72
Mvvv 116,100,156
Mvvv 120,100,160
Mvvv 124,100,164
Mvvv 128,100,168
Mvvv 132,100,172
Mvvv 116,104,176
Mvvv 120,104,180
Mvvv 124,104,184
Mvvv 128,104,188
Mvvv 132,104,192
Mvvv 116,108,196
Mvvv 120,108,200
Mvvv 124,108,204
Mvvv 128,108,208
Mvvv 132,108,212
s_nop 7
S_ s53,s64,6144
buffer_load_dwordx4 v[96:99], v10, s[32:35], s53 offen offset:256
S_ s53,s64,6144
buffer_load_dwordx4 v[100:103], v10, s[32:35], s53 offen offset:320
S_ s53,s64,0
buffer_load_dwordx4 v[104:107], v10, s[32:35], s53 offen offset:0
S_ s53,s64,0
buffer_load_dwordx4 v[108:111], v10, s[32:35], s53 offen offset:64
s_waitcnt vmcnt(0)
Mvvv 116,96,216
Mvvv 120,96,220
Mvvv 124,96,224
Mvvv 128,96,228
Mvvv 132,96,232
Mvvv 116,100,236
Mvvv 120,100,240
Mvvv 124,100,244
Mvvv 128,100,248
Mvvv 132,100,252
Nvvv 76,104,136,56
Nvvv 80,104,140,60
Nvvv 84,104,144,64
Nvvv 88,104,148,68
Nvvv 92,104,152,72
Mvvv 76,108,156
Mvvv 80,108,160
Mvvv 84,108,164
Mvvv 88,108,168
Mvvv 92,108,172
s_nop 7
S_ s53,s64,0
buffer_load_dwordx4 v[96:99], v10, s[32:35], s53 offen offset:128
S_ s53,s64,0
buffer_load_dwordx4 v[100:103], v10, s[32:35], s53 offen offset:192
S_ s53,s64,0
buffer_load_dwordx4 v[104:107], v10, s[32:35], s53 offen offset:256
S_ s53,s64,0
buffer_load_dwordx4 v[108:111], v10, s[32:35], s53 offen offset:320
s_waitcnt vmcnt(0)
Mvvv 76,96,176
Mvvv 80,96,180
Mvvv 84,96,184
Mvvv 88,96,188
Mvvv 92,96,192
Mvvv 76,100,196
Mvvv 80,100,200
Mvvv 84,100,204
Mvvv 88,100,208
Mvvv 92,100,212
Mvvv 76,104,216
Mvvv 80,104,220
Mvvv 84,104,224
Mvvv 88,104,228
Mvvv 92,104,232
Mvvv 76,108,236
Mvvv 80,108,240
Mvvv 84,108,244
Mvvv 88,108,248
Mvvv 92,108,252
s_nop 7
s_nop 2
v_max3_f32 v10, v76, v77, v78
v_max3_f32 v10, v10, v79, v116
v_max3_f32 v10, v10, v117, v118
v_max_f32 v10, v10, v119
v_mov_b32 v11, v10
s_nop 1
v_permlane16_swap_b32 v10, v11
v_max_f32 v10, v10, v11
v_mov_b32 v11, v10
s_nop 1
v_permlane32_swap_b32 v10, v11
v_max_f32 v10, v10, v11
A_ v10,0x40e00000,v10
v_max_f32 v10, 0, v10
v_exp_f32 v11, -v10
s_nop 0
U_ v46,v46,v11
R_ v12,a0
U_ v12,v12,v11
X_ a0,v12
R_ v13,a1
U_ v13,v13,v11
X_ a1,v13
R_ v12,a2
U_ v12,v12,v11
X_ a2,v12
R_ v13,a3
U_ v13,v13,v11
X_ a3,v13
R_ v12,a20
U_ v12,v12,v11
X_ a20,v12
R_ v13,a21
U_ v13,v13,v11
X_ a21,v13
R_ v12,a22
U_ v12,v12,v11
X_ a22,v12
R_ v13,a23
U_ v13,v13,v11
X_ a23,v13
R_ v12,a40
U_ v12,v12,v11
X_ a40,v12
R_ v13,a41
U_ v13,v13,v11
X_ a41,v13
R_ v12,a42
U_ v12,v12,v11
X_ a42,v12
R_ v13,a43
U_ v13,v13,v11
X_ a43,v13
R_ v12,a60
U_ v12,v12,v11
X_ a60,v12
R_ v13,a61
U_ v13,v13,v11
X_ a61,v13
R_ v12,a62
U_ v12,v12,v11
X_ a62,v12
R_ v13,a63
U_ v13,v13,v11
X_ a63,v13
R_ v12,a80
U_ v12,v12,v11
X_ a80,v12
R_ v13,a81
U_ v13,v13,v11
X_ a81,v13
R_ v12,a82
U_ v12,v12,v11
X_ a82,v12
R_ v13,a83
U_ v13,v13,v11
X_ a83,v13
R_ v12,a100
U_ v12,v12,v11
X_ a100,v12
R_ v13,a101
U_ v13,v13,v11
X_ a101,v13
R_ v12,a102
U_ v12,v12,v11
X_ a102,v12
R_ v13,a103
U_ v13,v13,v11
X_ a103,v13
R_ v12,a120
U_ v12,v12,v11
X_ a120,v12
R_ v13,a121
U_ v13,v13,v11
X_ a121,v13
R_ v12,a122
U_ v12,v12,v11
X_ a122,v12
R_ v13,a123
U_ v13,v13,v11
X_ a123,v13
R_ v12,a140
U_ v12,v12,v11
X_ a140,v12
R_ v13,a141
U_ v13,v13,v11
X_ a141,v13
R_ v12,a142
U_ v12,v12,v11
X_ a142,v12
R_ v13,a143
U_ v13,v13,v11
X_ a143,v13
R_ v12,a160
U_ v12,v12,v11
X_ a160,v12
R_ v13,a161
U_ v13,v13,v11
X_ a161,v13
R_ v12,a162
U_ v12,v12,v11
X_ a162,v12
R_ v13,a163
U_ v13,v13,v11
X_ a163,v13
R_ v12,a180
U_ v12,v12,v11
X_ a180,v12
R_ v13,a181
U_ v13,v13,v11
X_ a181,v13
R_ v12,a182
U_ v12,v12,v11
X_ a182,v12
R_ v13,a183
U_ v13,v13,v11
X_ a183,v13
R_ v12,a200
U_ v12,v12,v11
X_ a200,v12
R_ v13,a201
U_ v13,v13,v11
X_ a201,v13
R_ v12,a202
U_ v12,v12,v11
X_ a202,v12
R_ v13,a203
U_ v13,v13,v11
X_ a203,v13
R_ v12,a220
U_ v12,v12,v11
X_ a220,v12
R_ v13,a221
U_ v13,v13,v11
X_ a221,v13
R_ v12,a222
U_ v12,v12,v11
X_ a222,v12
R_ v13,a223
U_ v13,v13,v11
X_ a223,v13
B_ v56,v56,v10
B_ v57,v57,v10
B_ v58,v58,v10
B_ v59,v59,v10
B_ v76,v76,v10
B_ v77,v77,v10
B_ v78,v78,v10
B_ v79,v79,v10
B_ v116,v116,v10
B_ v117,v117,v10
B_ v118,v118,v10
B_ v119,v119,v10
E_ 76
E_ 77
E_ 78
E_ 79
E_ 116
E_ 117
E_ 118
E_ 119
s_nop 0
C_ 119,118,119
C_ 118,116,117
C_ 116,76,77
C_ 117,78,79
v_max3_f32 v10, v80, v81, v82
v_max3_f32 v10, v10, v83, v120
v_max3_f32 v10, v10, v121, v122
v_max_f32 v10, v10, v123
v_mov_b32 v11, v10
s_nop 1
v_permlane16_swap_b32 v10, v11
v_max_f32 v10, v10, v11
v_mov_b32 v11, v10
s_nop 1
v_permlane32_swap_b32 v10, v11
v_max_f32 v10, v10, v11
A_ v10,0x40e00000,v10
v_max_f32 v10, 0, v10
v_exp_f32 v11, -v10
s_nop 0
U_ v47,v47,v11
R_ v12,a4
U_ v12,v12,v11
X_ a4,v12
R_ v13,a5
U_ v13,v13,v11
X_ a5,v13
R_ v12,a6
U_ v12,v12,v11
X_ a6,v12
R_ v13,a7
U_ v13,v13,v11
X_ a7,v13
R_ v12,a24
U_ v12,v12,v11
X_ a24,v12
R_ v13,a25
U_ v13,v13,v11
X_ a25,v13
R_ v12,a26
U_ v12,v12,v11
X_ a26,v12
R_ v13,a27
U_ v13,v13,v11
X_ a27,v13
R_ v12,a44
U_ v12,v12,v11
X_ a44,v12
R_ v13,a45
U_ v13,v13,v11
X_ a45,v13
R_ v12,a46
U_ v12,v12,v11
X_ a46,v12
R_ v13,a47
U_ v13,v13,v11
X_ a47,v13
R_ v12,a64
U_ v12,v12,v11
X_ a64,v12
R_ v13,a65
U_ v13,v13,v11
X_ a65,v13
R_ v12,a66
U_ v12,v12,v11
X_ a66,v12
R_ v13,a67
U_ v13,v13,v11
X_ a67,v13
R_ v12,a84
U_ v12,v12,v11
X_ a84,v12
R_ v13,a85
U_ v13,v13,v11
X_ a85,v13
R_ v12,a86
U_ v12,v12,v11
X_ a86,v12
R_ v13,a87
U_ v13,v13,v11
X_ a87,v13
R_ v12,a104
U_ v12,v12,v11
X_ a104,v12
R_ v13,a105
U_ v13,v13,v11
X_ a105,v13
R_ v12,a106
U_ v12,v12,v11
X_ a106,v12
R_ v13,a107
U_ v13,v13,v11
X_ a107,v13
R_ v12,a124
U_ v12,v12,v11
X_ a124,v12
R_ v13,a125
U_ v13,v13,v11
X_ a125,v13
R_ v12,a126
U_ v12,v12,v11
X_ a126,v12
R_ v13,a127
U_ v13,v13,v11
X_ a127,v13
R_ v12,a144
U_ v12,v12,v11
X_ a144,v12
R_ v13,a145
U_ v13,v13,v11
X_ a145,v13
R_ v12,a146
U_ v12,v12,v11
X_ a146,v12
R_ v13,a147
U_ v13,v13,v11
X_ a147,v13
R_ v12,a164
U_ v12,v12,v11
X_ a164,v12
R_ v13,a165
U_ v13,v13,v11
X_ a165,v13
R_ v12,a166
U_ v12,v12,v11
X_ a166,v12
R_ v13,a167
U_ v13,v13,v11
X_ a167,v13
R_ v12,a184
U_ v12,v12,v11
X_ a184,v12
R_ v13,a185
U_ v13,v13,v11
X_ a185,v13
R_ v12,a186
U_ v12,v12,v11
X_ a186,v12
R_ v13,a187
U_ v13,v13,v11
X_ a187,v13
R_ v12,a204
U_ v12,v12,v11
X_ a204,v12
R_ v13,a205
U_ v13,v13,v11
X_ a205,v13
R_ v12,a206
U_ v12,v12,v11
X_ a206,v12
R_ v13,a207
U_ v13,v13,v11
X_ a207,v13
R_ v12,a224
U_ v12,v12,v11
X_ a224,v12
R_ v13,a225
U_ v13,v13,v11
X_ a225,v13
R_ v12,a226
U_ v12,v12,v11
X_ a226,v12
R_ v13,a227
U_ v13,v13,v11
X_ a227,v13
B_ v60,v60,v10
B_ v61,v61,v10
B_ v62,v62,v10
B_ v63,v63,v10
B_ v80,v80,v10
B_ v81,v81,v10
B_ v82,v82,v10
B_ v83,v83,v10
B_ v120,v120,v10
B_ v121,v121,v10
B_ v122,v122,v10
B_ v123,v123,v10
E_ 80
E_ 81
E_ 82
E_ 83
E_ 120
E_ 121
E_ 122
E_ 123
s_nop 0
C_ 123,122,123
C_ 122,120,121
C_ 120,80,81
C_ 121,82,83
v_max3_f32 v10, v84, v85, v86
v_max3_f32 v10, v10, v87, v124
v_max3_f32 v10, v10, v125, v126
v_max_f32 v10, v10, v127
v_mov_b32 v11, v10
s_nop 1
v_permlane16_swap_b32 v10, v11
v_max_f32 v10, v10, v11
v_mov_b32 v11, v10
s_nop 1
v_permlane32_swap_b32 v10, v11
v_max_f32 v10, v10, v11
A_ v10,0x40e00000,v10
v_max_f32 v10, 0, v10
v_exp_f32 v11, -v10
s_nop 0
U_ v48,v48,v11
R_ v12,a8
U_ v12,v12,v11
X_ a8,v12
R_ v13,a9
U_ v13,v13,v11
X_ a9,v13
R_ v12,a10
U_ v12,v12,v11
X_ a10,v12
R_ v13,a11
U_ v13,v13,v11
X_ a11,v13
R_ v12,a28
U_ v12,v12,v11
X_ a28,v12
R_ v13,a29
U_ v13,v13,v11
X_ a29,v13
R_ v12,a30
U_ v12,v12,v11
X_ a30,v12
R_ v13,a31
U_ v13,v13,v11
X_ a31,v13
R_ v12,a48
U_ v12,v12,v11
X_ a48,v12
R_ v13,a49
U_ v13,v13,v11
X_ a49,v13
R_ v12,a50
U_ v12,v12,v11
X_ a50,v12
R_ v13,a51
U_ v13,v13,v11
X_ a51,v13
R_ v12,a68
U_ v12,v12,v11
X_ a68,v12
R_ v13,a69
U_ v13,v13,v11
X_ a69,v13
R_ v12,a70
U_ v12,v12,v11
X_ a70,v12
R_ v13,a71
U_ v13,v13,v11
X_ a71,v13
R_ v12,a88
U_ v12,v12,v11
X_ a88,v12
R_ v13,a89
U_ v13,v13,v11
X_ a89,v13
R_ v12,a90
U_ v12,v12,v11
X_ a90,v12
R_ v13,a91
U_ v13,v13,v11
X_ a91,v13
R_ v12,a108
U_ v12,v12,v11
X_ a108,v12
R_ v13,a109
U_ v13,v13,v11
X_ a109,v13
R_ v12,a110
U_ v12,v12,v11
X_ a110,v12
R_ v13,a111
U_ v13,v13,v11
X_ a111,v13
R_ v12,a128
U_ v12,v12,v11
X_ a128,v12
R_ v13,a129
U_ v13,v13,v11
X_ a129,v13
R_ v12,a130
U_ v12,v12,v11
X_ a130,v12
R_ v13,a131
U_ v13,v13,v11
X_ a131,v13
R_ v12,a148
U_ v12,v12,v11
X_ a148,v12
R_ v13,a149
U_ v13,v13,v11
X_ a149,v13
R_ v12,a150
U_ v12,v12,v11
X_ a150,v12
R_ v13,a151
U_ v13,v13,v11
X_ a151,v13
R_ v12,a168
U_ v12,v12,v11
X_ a168,v12
R_ v13,a169
U_ v13,v13,v11
X_ a169,v13
R_ v12,a170
U_ v12,v12,v11
X_ a170,v12
R_ v13,a171
U_ v13,v13,v11
X_ a171,v13
R_ v12,a188
U_ v12,v12,v11
X_ a188,v12
R_ v13,a189
U_ v13,v13,v11
X_ a189,v13
R_ v12,a190
U_ v12,v12,v11
X_ a190,v12
R_ v13,a191
U_ v13,v13,v11
X_ a191,v13
R_ v12,a208
U_ v12,v12,v11
X_ a208,v12
R_ v13,a209
U_ v13,v13,v11
X_ a209,v13
R_ v12,a210
U_ v12,v12,v11
X_ a210,v12
R_ v13,a211
U_ v13,v13,v11
X_ a211,v13
R_ v12,a228
U_ v12,v12,v11
X_ a228,v12
R_ v13,a229
U_ v13,v13,v11
X_ a229,v13
R_ v12,a230
U_ v12,v12,v11
X_ a230,v12
R_ v13,a231
U_ v13,v13,v11
X_ a231,v13
B_ v64,v64,v10
B_ v65,v65,v10
B_ v66,v66,v10
B_ v67,v67,v10
B_ v84,v84,v10
B_ v85,v85,v10
B_ v86,v86,v10
B_ v87,v87,v10
B_ v124,v124,v10
B_ v125,v125,v10
B_ v126,v126,v10
B_ v127,v127,v10
E_ 84
E_ 85
E_ 86
E_ 87
E_ 124
E_ 125
E_ 126
E_ 127
s_nop 0
C_ 127,126,127
C_ 126,124,125
C_ 124,84,85
C_ 125,86,87
v_max3_f32 v10, v88, v89, v90
v_max3_f32 v10, v10, v91, v128
v_max3_f32 v10, v10, v129, v130
v_max_f32 v10, v10, v131
v_mov_b32 v11, v10
s_nop 1
v_permlane16_swap_b32 v10, v11
v_max_f32 v10, v10, v11
v_mov_b32 v11, v10
s_nop 1
v_permlane32_swap_b32 v10, v11
v_max_f32 v10, v10, v11
A_ v10,0x40e00000,v10
v_max_f32 v10, 0, v10
v_exp_f32 v11, -v10
s_nop 0
U_ v49,v49,v11
R_ v12,a12
U_ v12,v12,v11
X_ a12,v12
R_ v13,a13
U_ v13,v13,v11
X_ a13,v13
R_ v12,a14
U_ v12,v12,v11
X_ a14,v12
R_ v13,a15
U_ v13,v13,v11
X_ a15,v13
R_ v12,a32
U_ v12,v12,v11
X_ a32,v12
R_ v13,a33
U_ v13,v13,v11
X_ a33,v13
R_ v12,a34
U_ v12,v12,v11
X_ a34,v12
R_ v13,a35
U_ v13,v13,v11
X_ a35,v13
R_ v12,a52
U_ v12,v12,v11
X_ a52,v12
R_ v13,a53
U_ v13,v13,v11
X_ a53,v13
R_ v12,a54
U_ v12,v12,v11
X_ a54,v12
R_ v13,a55
U_ v13,v13,v11
X_ a55,v13
R_ v12,a72
U_ v12,v12,v11
X_ a72,v12
R_ v13,a73
U_ v13,v13,v11
X_ a73,v13
R_ v12,a74
U_ v12,v12,v11
X_ a74,v12
R_ v13,a75
U_ v13,v13,v11
X_ a75,v13
R_ v12,a92
U_ v12,v12,v11
X_ a92,v12
R_ v13,a93
U_ v13,v13,v11
X_ a93,v13
R_ v12,a94
U_ v12,v12,v11
X_ a94,v12
R_ v13,a95
U_ v13,v13,v11
X_ a95,v13
R_ v12,a112
U_ v12,v12,v11
X_ a112,v12
R_ v13,a113
U_ v13,v13,v11
X_ a113,v13
R_ v12,a114
U_ v12,v12,v11
X_ a114,v12
R_ v13,a115
U_ v13,v13,v11
X_ a115,v13
R_ v12,a132
U_ v12,v12,v11
X_ a132,v12
R_ v13,a133
U_ v13,v13,v11
X_ a133,v13
R_ v12,a134
U_ v12,v12,v11
X_ a134,v12
R_ v13,a135
U_ v13,v13,v11
X_ a135,v13
R_ v12,a152
U_ v12,v12,v11
X_ a152,v12
R_ v13,a153
U_ v13,v13,v11
X_ a153,v13
R_ v12,a154
U_ v12,v12,v11
X_ a154,v12
R_ v13,a155
U_ v13,v13,v11
X_ a155,v13
R_ v12,a172
U_ v12,v12,v11
X_ a172,v12
R_ v13,a173
U_ v13,v13,v11
X_ a173,v13
R_ v12,a174
U_ v12,v12,v11
X_ a174,v12
R_ v13,a175
U_ v13,v13,v11
X_ a175,v13
R_ v12,a192
U_ v12,v12,v11
X_ a192,v12
R_ v13,a193
U_ v13,v13,v11
X_ a193,v13
R_ v12,a194
U_ v12,v12,v11
X_ a194,v12
R_ v13,a195
U_ v13,v13,v11
X_ a195,v13
R_ v12,a212
U_ v12,v12,v11
X_ a212,v12
R_ v13,a213
U_ v13,v13,v11
X_ a213,v13
R_ v12,a214
U_ v12,v12,v11
X_ a214,v12
R_ v13,a215
U_ v13,v13,v11
X_ a215,v13
R_ v12,a232
U_ v12,v12,v11
X_ a232,v12
R_ v13,a233
U_ v13,v13,v11
X_ a233,v13
R_ v12,a234
U_ v12,v12,v11
X_ a234,v12
R_ v13,a235
U_ v13,v13,v11
X_ a235,v13
B_ v68,v68,v10
B_ v69,v69,v10
B_ v70,v70,v10
B_ v71,v71,v10
B_ v88,v88,v10
B_ v89,v89,v10
B_ v90,v90,v10
B_ v91,v91,v10
B_ v128,v128,v10
B_ v129,v129,v10
B_ v130,v130,v10
B_ v131,v131,v10
E_ 88
E_ 89
E_ 90
E_ 91
E_ 128
E_ 129
E_ 130
E_ 131
s_nop 0
C_ 131,130,131
C_ 130,128,129
C_ 128,88,89
C_ 129,90,91
v_max3_f32 v10, v92, v93, v94
v_max3_f32 v10, v10, v95, v132
v_max3_f32 v10, v10, v133, v134
v_max_f32 v10, v10, v135
v_mov_b32 v11, v10
s_nop 1
v_permlane16_swap_b32 v10, v11
v_max_f32 v10, v10, v11
v_mov_b32 v11, v10
s_nop 1
v_permlane32_swap_b32 v10, v11
v_max_f32 v10, v10, v11
A_ v10,0x40e00000,v10
v_max_f32 v10, 0, v10
v_exp_f32 v11, -v10
s_nop 0
U_ v50,v50,v11
R_ v12,a16
U_ v12,v12,v11
X_ a16,v12
R_ v13,a17
U_ v13,v13,v11
X_ a17,v13
R_ v12,a18
U_ v12,v12,v11
X_ a18,v12
R_ v13,a19
U_ v13,v13,v11
X_ a19,v13
R_ v12,a36
U_ v12,v12,v11
X_ a36,v12
R_ v13,a37
U_ v13,v13,v11
X_ a37,v13
R_ v12,a38
U_ v12,v12,v11
X_ a38,v12
R_ v13,a39
U_ v13,v13,v11
X_ a39,v13
R_ v12,a56
U_ v12,v12,v11
X_ a56,v12
R_ v13,a57
U_ v13,v13,v11
X_ a57,v13
R_ v12,a58
U_ v12,v12,v11
X_ a58,v12
R_ v13,a59
U_ v13,v13,v11
X_ a59,v13
R_ v12,a76
U_ v12,v12,v11
X_ a76,v12
R_ v13,a77
U_ v13,v13,v11
X_ a77,v13
R_ v12,a78
U_ v12,v12,v11
X_ a78,v12
R_ v13,a79
U_ v13,v13,v11
X_ a79,v13
R_ v12,a96
U_ v12,v12,v11
X_ a96,v12
R_ v13,a97
U_ v13,v13,v11
X_ a97,v13
R_ v12,a98
U_ v12,v12,v11
X_ a98,v12
R_ v13,a99
U_ v13,v13,v11
X_ a99,v13
R_ v12,a116
U_ v12,v12,v11
X_ a116,v12
R_ v13,a117
U_ v13,v13,v11
X_ a117,v13
R_ v12,a118
U_ v12,v12,v11
X_ a118,v12
R_ v13,a119
U_ v13,v13,v11
X_ a119,v13
)A"
R"A(R_ v12,a136
U_ v12,v12,v11
X_ a136,v12
R_ v13,a137
U_ v13,v13,v11
X_ a137,v13
R_ v12,a138
U_ v12,v12,v11
X_ a138,v12
R_ v13,a139
U_ v13,v13,v11
X_ a139,v13
R_ v12,a156
U_ v12,v12,v11
X_ a156,v12
R_ v13,a157
U_ v13,v13,v11
X_ a157,v13
R_ v12,a158
U_ v12,v12,v11
X_ a158,v12
R_ v13,a159
U_ v13,v13,v11
X_ a159,v13
R_ v12,a176
U_ v12,v12,v11
X_ a176,v12
R_ v13,a177
U_ v13,v13,v11
X_ a177,v13
R_ v12,a178
U_ v12,v12,v11
X_ a178,v12
R_ v13,a179
U_ v13,v13,v11
X_ a179,v13
R_ v12,a196
U_ v12,v12,v11
X_ a196,v12
R_ v13,a197
U_ v13,v13,v11
X_ a197,v13
R_ v12,a198
U_ v12,v12,v11
X_ a198,v12
R_ v13,a199
U_ v13,v13,v11
X_ a199,v13
R_ v12,a216
U_ v12,v12,v11
X_ a216,v12
R_ v13,a217
U_ v13,v13,v11
X_ a217,v13
R_ v12,a218
U_ v12,v12,v11
X_ a218,v12
R_ v13,a219
U_ v13,v13,v11
X_ a219,v13
R_ v12,a236
U_ v12,v12,v11
X_ a236,v12
R_ v13,a237
U_ v13,v13,v11
X_ a237,v13
R_ v12,a238
U_ v12,v12,v11
X_ a238,v12
R_ v13,a239
U_ v13,v13,v11
X_ a239,v13
B_ v72,v72,v10
B_ v73,v73,v10
B_ v74,v74,v10
B_ v75,v75,v10
B_ v92,v92,v10
B_ v93,v93,v10
B_ v94,v94,v10
B_ v95,v95,v10
B_ v132,v132,v10
B_ v133,v133,v10
B_ v134,v134,v10
B_ v135,v135,v10
E_ 92
E_ 93
E_ 94
E_ 95
E_ 132
E_ 133
E_ 134
E_ 135
s_nop 0
C_ 135,134,135
C_ 134,132,133
C_ 132,92,93
C_ 133,94,95
v_mov_b32 v92, 0x3f803f80
v_mov_b32 v93, 0x3f803f80
v_mov_b32 v94, 0x3f803f80
v_mov_b32 v95, 0x3f803f80
s_nop 7
s_lshl_b32 s53, s58, 4
S_ s53,s53,s63
s_cmp_eq_u32 s53, 1
s_cbranch_scc1 .Lstep0_2
s_cmp_eq_u32 s53, 3
s_cbranch_scc1 .Lstep0_4
s_cmp_eq_u32 s53, 5
s_cbranch_scc1 .Lstep0_6
s_cmp_eq_u32 s53, 7
s_cbranch_scc1 .Lstep0_8
s_cmp_eq_u32 s53, 9
s_cbranch_scc1 .Lstep0_10
s_cmp_eq_u32 s53, 11
s_cbranch_scc1 .Lstep0_12
s_cmp_eq_u32 s53, 13
s_cbranch_scc1 .Lstep0_14
s_cmp_eq_u32 s53, 15
s_cbranch_scc1 .Lstep0_16
s_cmp_eq_u32 s53, 17
s_cbranch_scc1 .Lstep1_2
s_cmp_eq_u32 s53, 19
s_cbranch_scc1 .Lstep1_4
s_cmp_eq_u32 s53, 21
s_cbranch_scc1 .Lstep1_6
s_cmp_eq_u32 s53, 23
s_cbranch_scc1 .Lstep1_8
s_cmp_eq_u32 s53, 25
s_cbranch_scc1 .Lstep1_10
s_cmp_eq_u32 s53, 27
s_cbranch_scc1 .Lstep1_12
s_cmp_eq_u32 s53, 29
s_cbranch_scc1 .Lstep1_14
s_cmp_eq_u32 s53, 31
s_cbranch_scc1 .Lstep1_16
s_cmp_eq_u32 s53, 33
s_cbranch_scc1 .Lstep2_2
s_cmp_eq_u32 s53, 35
s_cbranch_scc1 .Lstep2_4
s_cmp_eq_u32 s53, 37
s_cbranch_scc1 .Lstep2_6
s_cmp_eq_u32 s53, 39
s_cbranch_scc1 .Lstep2_8
s_cmp_eq_u32 s53, 41
s_cbranch_scc1 .Lstep2_10
s_cmp_eq_u32 s53, 43
s_cbranch_scc1 .Lstep2_12
s_cmp_eq_u32 s53, 45
s_cbranch_scc1 .Lstep2_14
s_cmp_eq_u32 s53, 47
s_cbranch_scc1 .Lstep2_16
s_endpgm
.Lpace1:
S_ s12,s12,s13
.Lpace1_l:
s_memrealtime s[100:101]
W_ 0
s_sub_u32 s100, s100, s98
s_cmp_ge_u32 s100, s12
s_cbranch_scc1 .Lpace1_back
s_sleep 2
s_branch .Lpace1_l
.Lpace3:
S_ s12,s12,s13
.Lpace3_l:
s_memrealtime s[100:101]
W_ 0
s_sub_u32 s100, s100, s98
s_cmp_ge_u32 s100, s12
s_cbranch_scc1 .Lpace3_back
s_sleep 2
s_branch .Lpace3_l
.Lpace5:
S_ s12,s12,s13
.Lpace5_l:
s_memrealtime s[100:101]
W_ 0
s_sub_u32 s100, s100, s98
s_cmp_ge_u32 s100, s12
s_cbranch_scc1 .Lpace5_back
s_sleep 2
s_branch .Lpace5_l
.Lpace7:
S_ s12,s12,s13
.Lpace7_l:
s_memrealtime s[100:101]
W_ 0
s_sub_u32 s100, s100, s98
s_cmp_ge_u32 s100, s12
s_cbranch_scc1 .Lpace7_back
s_sleep 2
s_branch .Lpace7_l
.Lpace9:
S_ s12,s12,s13
.Lpace9_l:
s_memrealtime s[100:101]
W_ 0
s_sub_u32 s100, s100, s98
s_cmp_ge_u32 s100, s12
s_cbranch_scc1 .Lpace9_back
s_sleep 2
s_branch .Lpace9_l
.Lpace11:
S_ s12,s12,s13
.Lpace11_l:
s_memrealtime s[100:101]
W_ 0
s_sub_u32 s100, s100, s98
s_cmp_ge_u32 s100, s12
s_cbranch_scc1 .Lpace11_back
s_sleep 2
s_branch .Lpace11_l
.Lpace13:
S_ s12,s12,s13
.Lpace13_l:
s_memrealtime s[100:101]
W_ 0
s_sub_u32 s100, s100, s98
s_cmp_ge_u32 s100, s12
s_cbranch_scc1 .Lpace13_back
s_sleep 2
s_branch .Lpace13_l
.Lpace15:
S_ s12,s12,s13
.Lpace15_l:
s_memrealtime s[100:101]
W_ 0
s_sub_u32 s100, s100, s98
s_cmp_ge_u32 s100, s12
s_cbranch_scc1 .Lpace15_back
s_sleep 2
s_branch .Lpace15_l
.Lpace17:
S_ s12,s12,s13
.Lpace17_l:
s_memrealtime s[100:101]
W_ 0
s_sub_u32 s100, s100, s98
s_cmp_ge_u32 s100, s12
s_cbranch_scc1 .Lpace17_back
s_sleep 2
s_branch .Lpace17_l
.Lpace19:
S_ s12,s12,s13
.Lpace19_l:
s_memrealtime s[100:101]
W_ 0
s_sub_u32 s100, s100, s98
s_cmp_ge_u32 s100, s12
s_cbranch_scc1 .Lpace19_back
s_sleep 2
s_branch .Lpace19_l
.Lpace21:
S_ s12,s12,s13
.Lpace21_l:
s_memrealtime s[100:101]
W_ 0
s_sub_u32 s100, s100, s98
s_cmp_ge_u32 s100, s12
s_cbranch_scc1 .Lpace21_back
s_sleep 2
s_branch .Lpace21_l
.Lpace23:
S_ s12,s12,s13
.Lpace23_l:
s_memrealtime s[100:101]
W_ 0
s_sub_u32 s100, s100, s98
s_cmp_ge_u32 s100, s12
s_cbranch_scc1 .Lpace23_back
s_sleep 2
s_branch .Lpace23_l
.Lpace25:
S_ s12,s12,s13
.Lpace25_l:
s_memrealtime s[100:101]
W_ 0
s_sub_u32 s100, s100, s98
s_cmp_ge_u32 s100, s12
s_cbranch_scc1 .Lpace25_back
s_sleep 2
s_branch .Lpace25_l
.Lpace27:
S_ s12,s12,s13
.Lpace27_l:
s_memrealtime s[100:101]
W_ 0
s_sub_u32 s100, s100, s98
s_cmp_ge_u32 s100, s12
s_cbranch_scc1 .Lpace27_back
s_sleep 2
s_branch .Lpace27_l
.Lpace29:
S_ s12,s12,s13
.Lpace29_l:
s_memrealtime s[100:101]
W_ 0
s_sub_u32 s100, s100, s98
s_cmp_ge_u32 s100, s12
s_cbranch_scc1 .Lpace29_back
s_sleep 2
s_branch .Lpace29_l
.Lhs_a0:
s_nop 4
s_waitcnt vmcnt(12)
R_ v10,a251
buffer_store_dword a255, v10, s[92:95], 0 offen offset:128 sc1
s_branch .Lhs_a0_back
.Lhs_a1:
s_nop 4
R_ v11,a248
v_lshrrev_b32 v11, 2, v11
buffer_load_dword a254, v11, s[92:95], s96 offen offset:128 sc1
s_branch .Lhs_a1_back
.Lhs_a2:
s_nop 4
s_waitcnt vmcnt(12)
R_ v10,a251
buffer_store_dword a255, v10, s[92:95], 0 offen offset:128 sc1
s_branch .Lhs_a2_back
.Lhs_b1:
s_nop 4
s_waitcnt vmcnt(12)
R_ v11,a254
v_cmp_eq_u32 vcc, s62, v11
R_ v10,a251
v_mov_b32 v11, 0x7fff0000
v_cndmask_b32 v10, v11, v10, vcc
R_ v11,a253
v_add_u32 v11, 1, v11
buffer_store_dword v11, v10, s[92:95], 0 offen offset:256 sc1
s_and_b32 s53, vcc_lo, 255
s_cmp_eq_u32 s53, 0xFF
s_cselect_b32 s97, 1, 0
s_branch .Lhs_b1_back
.Lhs_c0:
s_nop 4
R_ v11,a248
v_lshrrev_b32 v11, 2, v11
buffer_load_dword a254, v11, s[92:95], s96 offen offset:128 sc1
s_branch .Lhs_c0_back
.Lhs_c1:
s_nop 4
R_ v11,a248
v_lshrrev_b32 v11, 2, v11
buffer_load_dword a254, v11, s[92:95], s96 offen offset:256 sc1
s_branch .Lhs_c1_back
.Lhs_c2:
s_nop 4
R_ v11,a248
v_lshrrev_b32 v11, 2, v11
buffer_load_dword a254, v11, s[92:95], s96 offen offset:128 sc1
s_branch .Lhs_c2_back
.Lhs_d0:
s_nop 4
s_waitcnt vmcnt(12)
R_ v11,a254
v_cmp_eq_u32 vcc, s62, v11
R_ v10,a251
v_mov_b32 v11, 0x7fff0000
v_cndmask_b32 v10, v11, v10, vcc
R_ v11,a253
v_add_u32 v11, 1, v11
buffer_store_dword v11, v10, s[92:95], 0 offen offset:256 sc1
s_and_b32 s53, vcc_lo, 255
s_cmp_eq_u32 s53, 0xFF
s_cselect_b32 s97, 1, 0
s_branch .Lhs_d0_back
.Lhs_d2:
s_nop 4
s_waitcnt vmcnt(12)
R_ v11,a254
v_cmp_eq_u32 vcc, s62, v11
R_ v10,a251
v_mov_b32 v11, 0x7fff0000
v_cndmask_b32 v10, v11, v10, vcc
R_ v11,a253
v_add_u32 v11, 1, v11
buffer_store_dword v11, v10, s[92:95], 0 offen offset:256 sc1
s_and_b32 s53, vcc_lo, 255
s_cmp_eq_u32 s53, 0xFF
s_cselect_b32 s97, 1, 0
s_branch .Lhs_d2_back
.Lhs_d1:
s_nop 4
s_waitcnt vmcnt(12)
R_ v11,a254
R_ v10,a252
v_cmp_ne_u32 vcc, v11, v10
s_nop 1
s_and_b32 s53, vcc_lo, 255
s_cmp_eq_u32 s53, 0xFF
s_cselect_b32 s53, s97, 0
s_cmp_eq_u32 s53, 1
s_cbranch_scc1 .Lhs_ok
.Lhs_spin:
s_cmp_eq_u32 s57, 0
s_cbranch_scc1 .Lhs_ok
s_sub_u32 s57, s57, 1
s_sleep 8
R_ v10,a251
s_lshl_b32 s53, s6, 2
v_subrev_u32 v10, s53, v10
buffer_load_dword v11, v10, s[92:95], 0 offen offset:0 sc1
R_ v10,a248
v_lshrrev_b32 v10, 2, v10
buffer_load_dword v12, v10, s[92:95], s96 offen offset:128 sc1
buffer_load_dword v13, v10, s[92:95], s96 offen offset:256 sc1
s_waitcnt vmcnt(0)
X_ a255,v11
R_ v10,a251
buffer_store_dword v11, v10, s[92:95], 0 offen offset:128 sc1
s_nop 1
v_cmp_eq_u32 vcc, s62, v12
R_ v10,a251
v_mov_b32 v12, 0x7fff0000
v_cndmask_b32 v10, v12, v10, vcc
R_ v12,a253
v_add_u32 v12, 1, v12
buffer_store_dword v12, v10, s[92:95], 0 offen offset:256 sc1
s_and_b32 s53, vcc_lo, 255
s_cmp_eq_u32 s53, 0xFF
s_cselect_b32 s97, 1, 0
s_nop 1
R_ v10,a252
v_cmp_ne_u32 vcc, v13, v10
s_nop 1
s_and_b32 s53, vcc_lo, 255
s_cmp_eq_u32 s53, 0xFF
s_cselect_b32 s53, s97, 0
s_cmp_eq_u32 s53, 1
s_cbranch_scc0 .Lhs_spin
.Lhs_ok:
s_branch .Lhs_d1_back
.Latt2:
R_ v13,a248
S_ s53,s51,36864
v_add_u32 v12, s53, v13
s_mul_i32 s53, s4, 10240
S_ s53,s53,0
v_add_u32 v13, s53, v13
s_cmp_eq_u32 s4, 0
s_cbranch_scc0 .Lhs_tok2
R_ v11,a241
buffer_load_dword a255, v11, s[92:95], 0 offen offset:0 sc1
.Lhs_tok2:
W_ 2
Zvvv 96,30,136
Dv 42,v1,44032
R_ v2,a245
Zvvv 100,30,140
ds_write_b128 v13, a[0:3] offset:0
Zvvv 104,30,144
v_xor_b32 v3, 32, v2
v_xor_b32 v4, 64, v2
Zvvv 108,30,148
ds_write_b128 v13, a[4:7] offset:1024
Zvvv 112,30,152
W_ 4
v_xor_b32 v5, 96, v2
s_mov_b64 s[24:25], s[16:17]
Mvvv 96,34,156
s_mov_b32 s26, 7864320
Dv 30,%0,45056
s_mov_b64 s[28:29], s[22:23]
Mvvv 100,34,160
ds_write_b128 v13, a[8:11] offset:2048
Mvvv 104,34,164
s_mov_b32 s30, 1638400
R_ v20,a80
s_mul_i32 s52, s4, 10240
Mvvv 108,34,168
ds_write_b128 v13, a[12:15] offset:3072
Mvvv 112,34,172
W_ 6
R_ v21,a81
R_ v22,a82
Mvvv 96,38,176
Dv 34,v1,45056
R_ v23,a83
Mvvv 100,38,180
ds_write_b128 v13, a[16:19] offset:4096
Mvvv 104,38,184
R_ v24,a84
R_ v25,a85
Mvvv 108,38,188
ds_write_b128 v13, a[40:43] offset:5120
Mvvv 112,38,192
W_ 8
R_ v26,a86
R_ v27,a87
Mvvv 96,42,196
Dv 38,%0,36864
R_ v52,a88
Mvvv 100,42,200
ds_write_b128 v13, a[44:47] offset:6144
Mvvv 104,42,204
R_ v53,a89
R_ v54,a90
Mvvv 108,42,208
ds_write_b128 v13, a[48:51] offset:7168
Mvvv 112,42,212
W_ 8
R_ v55,a91
v_accvgpr_mov_b32 a240, a92
Mvvv 96,30,216
Dv 42,v1,36864
v_accvgpr_mov_b32 a241, a93
Mvvv 100,30,220
ds_write_b128 v13, a[52:55] offset:8192
Mvvv 104,30,224
v_accvgpr_mov_b32 a242, a94
v_accvgpr_mov_b32 a243, a95
Mvvv 108,30,228
ds_write_b128 v13, a[56:59] offset:9216
Mvvv 112,30,232
W_ 8
v_accvgpr_mov_b32 a244, a96
v_accvgpr_mov_b32 a245, a97
Mvvv 96,34,236
Dv 30,%0,37888
v_accvgpr_mov_b32 a246, a98
Mvvv 100,34,240
v_accvgpr_mov_b32 a247, a99
R_ v16,a120
Mvvv 104,34,244
R_ v17,a121
R_ v18,a122
Mvvv 108,34,248
R_ v19,a123
R_ v28,a124
Mvvv 112,34,252
W_ 6
R_ v29,a125
R_ v51,a126
Zvvv 76,38,136
Dv 34,v1,37888
R_ v6,a127
Zvvv 80,38,140
R_ v14,a128
R_ v15,a129
Zvvv 84,38,144
v_accvgpr_mov_b32 a249, a130
v_accvgpr_mov_b32 a250, a131
Zvvv 88,38,148
v_max3_f32 v56, v96, v97, v98
v_max_f32 v56, v56, v99
Zvvv 92,38,152
W_ 4
v_max3_f32 v60, v100, v101, v102
v_max_f32 v60, v60, v103
Mvvv 76,42,156
Dv 38,%0,38912
v_max3_f32 v64, v104, v105, v106
Mvvv 80,42,160
v_max_f32 v64, v64, v107
Mvvv 84,42,164
v_max3_f32 v68, v108, v109, v110
v_max_f32 v68, v68, v111
Mvvv 88,42,168
v_max3_f32 v72, v112, v113, v114
v_max_f32 v72, v72, v115
Mvvv 92,42,172
W_ 2
Mvvv 76,30,176
Dv 42,v1,38912
Mvvv 80,30,180
Mvvv 84,30,184
Mvvv 88,30,188
Mvvv 92,30,192
s_waitcnt vmcnt(34) lgkmcnt(0)
s_barrier
Mvvv 76,34,196
Dv 30,%0,55296
Mvvv 76,38,216
Mvvv 76,42,236
Mvvv 80,34,200
Mvvv 80,38,220
Mvvv 80,42,240
Mvvv 84,34,204
s_nop 3
v_max3_f32 v56, v56, v76, v77
v_max3_f32 v56, v56, v78, v79
Mvvv 84,38,224
Mvvv 84,42,244
Mvvv 88,34,208
v_max3_f32 v60, v60, v80, v81
v_max3_f32 v60, v60, v82, v83
Mvvv 88,38,228
Mvvv 88,42,248
Mvvv 92,34,212
s_nop 1
v_max3_f32 v64, v64, v84, v85
v_max3_f32 v64, v64, v86, v87
Mvvv 92,38,232
Mvvv 92,42,252
Dv 34,v1,55296
.p2align 3
v_mov_b32 v11, 0x3f803f80
v_permlane32_swap_b32 v56, v60
v_max3_f32 v68, v68, v88, v89
v_max3_f32 v68, v68, v90, v91
v_max_f32 v56, v56, v60
s_nop 1
v_max3_f32 v72, v72, v92, v93
v_max3_f32 v72, v72, v94, v95
v_mov_b32 v73, v72
v_permlane32_swap_b32 v64, v68
v_max_f32 v64, v64, v68
v_permlane32_swap_b32 v72, v73
v_max_f32 v72, v72, v73
v_mov_b32 v73, v72
v_permlane16_swap_b32 v56, v64
v_max_f32 v56, v56, v64
v_permlane16_swap_b32 v72, v73
B_ v56,0xc0e00000,v56
v_max_f32 v72, v72, v73
v_mov_b32 v64, v56
B_ v72,0xc0e00000,v72
v_mov_b32 v73, v72
v_permlane16_swap_b32 v56, v64
v_mov_b32 v60, v56
v_mov_b32 v68, v64
v_mov_b64 v[74:75], v[72:73]
v_permlane32_swap_b32 v56, v60
v_permlane32_swap_b32 v64, v68
v_mov_b32 v57, v56
v_mov_b32 v61, v60
v_mov_b32 v65, v64
v_mov_b32 v69, v68
v_pk_add_f32 v[96:97], v[96:97], v[56:57]
v_pk_add_f32 v[98:99], v[98:99], v[56:57]
v_pk_add_f32 v[76:77], v[76:77], v[56:57]
v_pk_add_f32 v[78:79], v[78:79], v[56:57]
v_mov_b64 v[58:59], v[56:57]
v_pk_add_f32 v[100:101], v[100:101], v[60:61]
v_pk_add_f32 v[102:103], v[102:103], v[60:61]
v_pk_add_f32 v[80:81], v[80:81], v[60:61]
v_pk_add_f32 v[82:83], v[82:83], v[60:61]
v_mov_b64 v[62:63], v[60:61]
v_pk_add_f32 v[104:105], v[104:105], v[64:65]
v_pk_add_f32 v[106:107], v[106:107], v[64:65]
v_pk_add_f32 v[84:85], v[84:85], v[64:65]
v_pk_add_f32 v[86:87], v[86:87], v[64:65]
v_mov_b64 v[66:67], v[64:65]
v_pk_add_f32 v[108:109], v[108:109], v[68:69]
v_pk_add_f32 v[110:111], v[110:111], v[68:69]
v_pk_add_f32 v[88:89], v[88:89], v[68:69]
v_pk_add_f32 v[90:91], v[90:91], v[68:69]
v_mov_b64 v[70:71], v[68:69]
v_pk_add_f32 v[112:113], v[112:113], v[72:73]
v_pk_add_f32 v[114:115], v[114:115], v[72:73]
v_pk_add_f32 v[92:93], v[92:93], v[72:73]
v_pk_add_f32 v[94:95], v[94:95], v[72:73]
E_ 96
E_ 97
E_ 98
E_ 99
E_ 76
E_ 77
E_ 78
E_ 79
C_ 99,98,99
C_ 98,96,97
C_ 96,76,77
C_ 97,78,79
E_ 100
E_ 101
E_ 102
E_ 103
E_ 80
E_ 81
E_ 82
E_ 83
C_ 103,102,103
C_ 102,100,101
C_ 100,80,81
C_ 101,82,83
.Lstep2_1:
W_ 1
Nvvv 116,30,136,56
s_nop 0
Dv 38,%0,56320
Dv 42,v1,56320
W_ 2
Mvvv 116,34,156
E_ 104
Nvvv 120,30,140,60
E_ 105
Mvvv 120,34,160
E_ 106
Nvvv 124,30,144,64
E_ 107
Mvvv 124,34,164
E_ 84
Nvvv 128,30,148,68
E_ 85
Mvvv 128,34,168
E_ 86
Nvvv 132,30,152,72
E_ 87
Mvvv 132,34,172
W_ 0
C_ 107,106,107
C_ 106,104,105
Mvvv 132,38,192
s_nop 0
Dv 30,%0,57344
Dv 34,v1,57344
Mvvv 132,42,212
C_ 104,84,85
C_ 105,86,87
Mvvv 128,38,188
E_ 108
Mvvv 128,42,208
E_ 109
Mvvv 124,38,184
E_ 110
Mvvv 124,42,204
E_ 111
Mvvv 120,38,180
E_ 88
Mvvv 120,42,200
E_ 89
Mvvv 116,38,176
E_ 90
Mvvv 116,42,196
W_ 0
E_ 91
Mvvv 116,30,216
s_nop 0
Dv 38,%0,49152
Dv 42,v1,49152
Mvvv 116,34,236
C_ 111,110,111
C_ 110,108,109
Mvvv 120,30,220
C_ 108,88,89
C_ 109,90,91
Mvvv 120,34,240
E_ 112
Mvvv 124,30,224
E_ 113
Mvvv 124,34,244
E_ 114
Mvvv 128,30,228
E_ 115
Mvvv 128,34,248
E_ 92
Mvvv 132,30,232
E_ 93
Mvvv 132,34,252
W_ 0
E_ 94
Nvvv 76,38,136,56
s_nop 0
Dv 30,%0,50176
Dv 34,v1,50176
Mvvv 76,42,156
E_ 95
Nvvv 80,38,140,60
C_ 115,114,115
C_ 114,112,113
Mvvv 80,42,160
C_ 112,92,93
C_ 113,94,95
Nvvv 84,38,144,64
E_ 116
Mvvv 84,42,164
E_ 117
Nvvv 88,38,148,68
E_ 118
Mvvv 88,42,168
E_ 119
Nvvv 92,38,152,72
C_ 119,118,119
C_ 118,116,117
Mvvv 92,42,172
W_ 0
E_ 120
Mvvv 92,30,192
s_nop 0
Dv 38,%0,51200
Dv 42,v1,51200
Mvvv 92,34,212
X_ a180,v11
X_ a181,v11
Mvvv 88,30,188
X_ a182,v11
X_ a183,v11
Mvvv 88,34,208
ds_write_b128 v12, a[132:135] offset:0
Mvvv 84,30,184
E_ 121
Mvvv 84,34,204
E_ 122
Mvvv 80,30,180
ds_write_b128 v12, a[136:139] offset:1024
Mvvv 80,34,200
E_ 123
Mvvv 76,30,176
C_ 123,122,123
C_ 122,120,121
Mvvv 76,34,196
ds_write_b128 v12, a[160:163] offset:2048
W_ 4
Mvvv 76,38,216
W_ 3
E_ 124
Mvvv 76,42,236
s_waitcnt vmcnt(12) lgkmcnt(0)
s_barrier
Mvvv 80,38,220
ds_write_b128 v12, a[164:167] offset:3072
Mvvv 80,42,240
E_ 125
Mvvv 84,38,224
E_ 126
Mvvv 84,42,244
s_nop 0
Tv 30,v2,24576
Tv 32,v2,30720
Mvvv 88,38,228
ds_write_b128 v12, a[168:171] offset:4096
Mvvv 88,42,248
s_nop 0
Tv 34,v3,24576
Tv 36,v3,30720
Mvvv 92,38,232
E_ 127
Mvvv 92,42,252
ds_write_b128 v12, a[172:175] offset:5120
Zaav 220,180,96
C_ 127,126,127
C_ 126,124,125
Zaav 224,180,100
E_ 128
Zaav 228,180,104
ds_write_b128 v12, a[176:179] offset:12288
Zaav 232,180,108
E_ 129
Zaav 236,180,112
W_ 2
E_ 130
Zavv 0,30,96
s_nop 0
Tv 38,v4,24576
Tv 40,v4,30720
Zavv 20,34,96
ds_write_b128 v12, a[200:203] offset:13312
Zavv 24,34,100
s_nop 0
Tv 42,v5,24576
Tv 44,v5,30720
Zavv 4,30,100
E_ 131
Zavv 8,30,104
ds_write_b128 v12, a[204:207] offset:14336
Zavv 28,34,104
R_ v46,a220
R_ v47,a224
Zavv 32,34,108
R_ v48,a228
R_ v49,a232
Zavv 12,30,108
R_ v50,a236
C_ 131,130,131
Zavv 16,30,112
ds_write_b128 v12, a[208:211] offset:15360
Zavv 36,34,112
W_ 2
C_ 130,128,129
Zavv 56,38,112
s_nop 0
Tv 30,v2,25600
Tv 32,v2,31744
Zavv 76,42,112
ds_write_b128 v12, a[212:215] offset:16384
Zavv 72,42,108
s_nop 0
Tv 34,v3,25600
Tv 36,v3,31744
Zavv 52,38,108
E_ 132
Zavv 48,38,104
ds_write_b128 v12, a[216:219] offset:17408
Zavv 68,42,104
E_ 133
Zavv 64,42,100
E_ 134
Zavv 44,38,100
E_ 135
Zavv 40,38,96
C_ 135,134,135
C_ 134,132,133
Zavv 60,42,96
W_ 1
E_ 76
Zavv 80,30,96
s_nop 0
Tv 38,v4,25600
Tv 40,v4,31744
Zavv 100,34,96
E_ 77
Zavv 104,34,100
s_nop 0
Tv 42,v5,25600
Tv 44,v5,31744
Zavv 84,30,100
E_ 78
Zavv 88,30,104
E_ 79
Zavv 108,34,104
C_ 116,76,77
C_ 117,78,79
Zavv 112,34,108
E_ 80
Zavv 92,30,108
E_ 81
Zavv 96,30,112
E_ 82
Zavv 116,34,112
W_ 0
E_ 83
Zavv 136,38,112
s_nop 0
Tv 30,v2,26624
Tv 32,v2,32768
Zavv 156,42,112
C_ 120,80,81
C_ 121,82,83
Zavv 152,42,108
s_nop 0
Tv 34,v3,26624
Tv 36,v3,32768
Zavv 132,38,108
E_ 84
Zavv 128,38,104
E_ 85
Zavv 148,42,104
E_ 86
Zavv 144,42,100
E_ 87
Zavv 124,38,100
C_ 124,84,85
C_ 125,86,87
Zavv 120,38,96
E_ 88
Zavv 140,42,96
W_ 0
E_ 89
Zavv 160,30,96
s_nop 0
Tv 38,v4,26624
Tv 40,v4,32768
Zavv 180,34,96
E_ 90
Zavv 184,34,100
s_nop 0
Tv 42,v5,26624
Tv 44,v5,32768
Zavv 164,30,100
E_ 91
Zavv 168,30,104
C_ 128,88,89
C_ 129,90,91
Zavv 188,34,104
S_ s65,s61,36864
E_ 92
Zavv 192,34,108
S_ m0,s51,24576
E_ 93
Zavv 172,30,108
s_waitcnt vmcnt(6) lgkmcnt(0)
s_barrier
Zavv 176,30,112
Ln v7,32,s65,0
Zavv 196,34,112
E_ 94
Zavv 216,38,112
Dv 30,%0,6144
Zavv 236,42,112
Dv 34,v1,6144
Zavv 232,42,108
Ln v8,32,s65,3072
S_ m0,s51,25472
Zavv 212,38,108
E_ 95
Zavv 208,38,104
Ln v7,32,s65,128
Zavv 228,42,104
C_ 132,92,93
C_ 133,94,95
Zavv 224,42,100
Ln v8,32,s65,3200
S_ m0,s51,26368
Zavv 204,38,100
v_mov_b32 v92, 0x3f803f80
v_mov_b32 v93, 0x3f803f80
Zavv 200,38,96
Ln v7,32,s65,256
Zavv 220,42,96
v_mov_b32 v94, 0x3f803f80
v_mov_b32 v95, 0x3f803f80
Ln v8,32,s65,3328
.Lstep2_2:
s_nop 0
Zvvv 76,92,116
Dv 38,%0,7168
Zvvv 80,92,120
Dv 42,v1,7168
Zvvv 84,92,124
Zvvv 88,92,128
Zvvv 92,92,132
W_ 2
Nvvv 96,30,136,56
Mvvv 96,34,156
Nvvv 100,30,140,60
s_nop 1
v_max3_f32 v10, v76, v80, v84
s_nop 0
v_max3_f32 v10, v10, v88, v92
Mvvv 100,34,160
v_cmp_lt_f32 vcc, 0x42000000, v10
s_cbranch_vccnz .Ltramp2_1
Nvvv 104,30,144,64
A_ v46,v46,v76
A_ v47,v47,v80
Mvvv 104,34,164
A_ v48,v48,v84
A_ v49,v49,v88
Nvvv 108,30,148,68
A_ v50,v50,v92
Mvvv 108,34,168
Nvvv 112,30,152,72
Mvvv 112,34,172
W_ 0
Mvvv 112,38,192
Dv 30,%0,8192
Mvvv 112,42,212
Dv 34,v1,8192
Mvvv 108,38,188
Mvvv 108,42,208
Mvvv 104,38,184
Mvvv 104,42,204
Mvvv 100,38,180
Mvvv 100,42,200
Mvvv 96,38,176
Mvvv 96,42,196
W_ 0
Mvvv 96,30,216
Dv 38,%0,0
Mvvv 96,34,236
Dv 42,v1,0
Mvvv 100,30,220
Mvvv 100,34,240
Mvvv 104,30,224
Mvvv 104,34,244
Mvvv 108,30,228
Mvvv 108,34,248
s_nop 0
E_ 96
Mvvv 112,30,232
E_ 97
Mvvv 112,34,252
W_ 0
E_ 98
Nvvv 76,38,136,56
Dv 30,%0,1024
Mvvv 76,42,156
Dv 34,v1,1024
Nvvv 80,38,140,60
E_ 99
Mvvv 80,42,160
C_ 99,98,99
C_ 98,96,97
Nvvv 84,38,144,64
E_ 100
Mvvv 84,42,164
E_ 101
Nvvv 88,38,148,68
E_ 102
Mvvv 88,42,168
E_ 103
Nvvv 92,38,152,72
C_ 103,102,103
C_ 102,100,101
Mvvv 92,42,172
W_ 0
E_ 104
Mvvv 92,30,192
Dv 38,%0,2048
Mvvv 92,34,212
Dv 42,v1,2048
Mvvv 88,30,188
E_ 105
Mvvv 88,34,208
E_ 106
Mvvv 84,30,184
S_ s65,s61,24576
E_ 107
Mvvv 84,34,204
S_ m0,s51,0
C_ 107,106,107
C_ 106,104,105
Mvvv 80,30,180
s_waitcnt vmcnt(6) lgkmcnt(0)
s_barrier
Mvvv 80,34,200
L_ v9,36,s65,0
Mvvv 76,30,176
E_ 108
Mvvv 76,34,196
L_ v9,36,s65,3072
S_ m0,s51,896
Mvvv 76,38,216
Tv 30,v2,12288
Tv 32,v2,18432
Mvvv 76,42,236
L_ v9,36,s65,128
Mvvv 80,38,220
s_nop 0
Tv 34,v3,12288
Tv 36,v3,18432
Mvvv 80,42,240
L_ v9,36,s65,3200
S_ m0,s51,1792
Mvvv 84,38,224
E_ 109
Mvvv 84,42,244
L_ v9,36,s65,256
Mvvv 88,38,228
E_ 110
Mvvv 88,42,248
L_ v9,36,s65,3328
Mvvv 92,38,232
E_ 111
Mvvv 92,42,252
W_ 0
C_ 111,110,111
C_ 110,108,109
Mavv 0,30,116
s_nop 0
Tv 38,v4,12288
Tv 40,v4,18432
Mavv 20,34,116
E_ 112
Mavv 24,34,120
s_nop 0
Tv 42,v5,12288
Tv 44,v5,18432
Mavv 4,30,120
E_ 113
Mavv 8,30,124
E_ 114
Mavv 28,34,124
E_ 115
Mavv 32,34,128
C_ 115,114,115
C_ 114,112,113
Mavv 12,30,128
E_ 76
Mavv 16,30,132
E_ 77
Mavv 36,34,132
W_ 0
E_ 78
Mavv 56,38,132
s_nop 0
Tv 30,v2,13312
Tv 32,v2,19456
Mavv 76,42,132
E_ 79
Mavv 72,42,128
s_nop 0
Tv 34,v3,13312
Tv 36,v3,19456
Mavv 52,38,128
C_ 96,76,77
C_ 97,78,79
Mavv 48,38,124
E_ 80
Mavv 68,42,124
E_ 81
Mavv 64,42,120
E_ 82
Mavv 44,38,120
E_ 83
Mavv 40,38,116
C_ 100,80,81
C_ 101,82,83
Mavv 60,42,116
W_ 0
E_ 84
Mavv 80,30,116
s_nop 0
Tv 38,v4,13312
Tv 40,v4,19456
Mavv 100,34,116
E_ 85
Mavv 104,34,120
s_nop 0
Tv 42,v5,13312
Tv 44,v5,19456
Mavv 84,30,120
E_ 86
Mavv 88,30,124
E_ 87
Mavv 108,34,124
C_ 104,84,85
C_ 105,86,87
Mavv 112,34,128
E_ 88
Mavv 92,30,128
E_ 89
Mavv 96,30,132
E_ 90
Mavv 116,34,132
W_ 0
E_ 91
Mavv 136,38,132
s_nop 0
Tv 30,v2,14336
Tv 32,v2,20480
Mavv 156,42,132
C_ 108,88,89
C_ 109,90,91
Mavv 152,42,128
s_nop 0
Tv 34,v3,14336
Tv 36,v3,20480
Mavv 132,38,128
E_ 92
Mavv 128,38,124
E_ 93
Mavv 148,42,124
E_ 94
Mavv 144,42,120
E_ 95
Mavv 124,38,120
C_ 112,92,93
C_ 113,94,95
Mavv 120,38,116
v_mov_b32 v92, 0x3f803f80
v_mov_b32 v93, 0x3f803f80
Mavv 140,42,116
W_ 0
v_mov_b32 v94, 0x3f803f80
v_mov_b32 v95, 0x3f803f80
Mavv 160,30,116
s_nop 0
Tv 38,v4,14336
Tv 40,v4,20480
Mavv 180,34,116
Mavv 184,34,120
s_nop 0
Tv 42,v5,14336
Tv 44,v5,20480
Mavv 164,30,120
Mavv 168,30,124
Mavv 188,34,124
S_ s65,s61,49152
Mavv 192,34,128
S_ m0,s51,12288
Mavv 172,30,128
s_waitcnt vmcnt(6) lgkmcnt(0)
s_barrier
Mavv 176,30,132
Ln v7,32,s65,0
Mavv 196,34,132
Mavv 216,38,132
Dv 30,%0,30720
Mavv 236,42,132
Dv 34,v1,30720
Mavv 232,42,128
Ln v8,32,s65,3072
S_ m0,s51,13184
Mavv 212,38,128
Mavv 208,38,124
Ln v7,32,s65,128
Mavv 228,42,124
Mavv 224,42,120
Ln v8,32,s65,3200
S_ m0,s51,14080
Mavv 204,38,120
Mavv 200,38,116
Ln v7,32,s65,256
Mavv 220,42,116
s_cmp_eq_u32 s4, 0
s_cbranch_scc1 .Lhs_a2
.Lhs_a2_back:
.Lstep2_3:
Zvvv 76,92,96
Dv 38,%0,31744
Zvvv 80,92,100
Dv 42,v1,31744
Zvvv 84,92,104
Zvvv 88,92,108
Zvvv 92,92,112
W_ 2
Nvvv 116,30,136,56
Mvvv 116,34,156
Nvvv 120,30,140,60
s_nop 1
v_max3_f32 v10, v76, v80, v84
s_nop 0
v_max3_f32 v10, v10, v88, v92
Mvvv 120,34,160
v_cmp_lt_f32 vcc, 0x42000000, v10
s_cbranch_vccnz .Ltramp2_2
Nvvv 124,30,144,64
A_ v46,v46,v76
A_ v47,v47,v80
Mvvv 124,34,164
A_ v48,v48,v84
A_ v49,v49,v88
Nvvv 128,30,148,68
A_ v50,v50,v92
Mvvv 128,34,168
Ln v8,32,s65,3328
Nvvv 132,30,152,72
Mvvv 132,34,172
W_ 0
Mvvv 132,38,192
Dv 30,%0,32768
Mvvv 132,42,212
Dv 34,v1,32768
Mvvv 128,38,188
Mvvv 128,42,208
Mvvv 124,38,184
Mvvv 124,42,204
Mvvv 120,38,180
Mvvv 120,42,200
Mvvv 116,38,176
Mvvv 116,42,196
W_ 0
Mvvv 116,30,216
Dv 38,%0,24576
Mvvv 116,34,236
Dv 42,v1,24576
Mvvv 120,30,220
Mvvv 120,34,240
Mvvv 124,30,224
Mvvv 124,34,244
Mvvv 128,30,228
Mvvv 128,34,248
s_nop 0
E_ 116
Mvvv 132,30,232
E_ 117
Mvvv 132,34,252
W_ 0
E_ 118
Nvvv 76,38,136,56
Dv 30,%0,25600
Mvvv 76,42,156
Dv 34,v1,25600
Nvvv 80,38,140,60
E_ 119
Mvvv 80,42,160
C_ 119,118,119
C_ 118,116,117
Nvvv 84,38,144,64
E_ 120
Mvvv 84,42,164
E_ 121
Nvvv 88,38,148,68
E_ 122
Mvvv 88,42,168
E_ 123
Nvvv 92,38,152,72
C_ 123,122,123
C_ 122,120,121
Mvvv 92,42,172
W_ 0
E_ 124
Mvvv 92,30,192
Dv 38,%0,26624
Mvvv 92,34,212
Dv 42,v1,26624
Mvvv 88,30,188
E_ 125
Mvvv 88,34,208
E_ 126
Mvvv 84,30,184
S_ s65,s61,36864
E_ 127
Mvvv 84,34,204
S_ m0,s51,24576
C_ 127,126,127
C_ 126,124,125
Mvvv 80,30,180
s_waitcnt vmcnt(6) lgkmcnt(0)
s_barrier
Mvvv 80,34,200
L_ v9,36,s65,0
Mvvv 76,30,176
E_ 128
Mvvv 76,34,196
L_ v9,36,s65,3072
S_ m0,s51,25472
Mvvv 76,38,216
Tv 30,v2,0
Tv 32,v2,6144
Mvvv 76,42,236
L_ v9,36,s65,128
Mvvv 80,38,220
s_nop 0
Tv 34,v3,0
Tv 36,v3,6144
Mvvv 80,42,240
L_ v9,36,s65,3200
S_ m0,s51,26368
Mvvv 84,38,224
E_ 129
Mvvv 84,42,244
L_ v9,36,s65,256
Mvvv 88,38,228
E_ 130
Mvvv 88,42,248
L_ v9,36,s65,3328
Mvvv 92,38,232
E_ 131
Mvvv 92,42,252
W_ 0
C_ 131,130,131
C_ 130,128,129
Mavv 0,30,96
s_nop 0
Tv 38,v4,0
Tv 40,v4,6144
Mavv 20,34,96
E_ 132
Mavv 24,34,100
s_nop 0
Tv 42,v5,0
Tv 44,v5,6144
Mavv 4,30,100
E_ 133
Mavv 8,30,104
E_ 134
Mavv 28,34,104
E_ 135
Mavv 32,34,108
C_ 135,134,135
C_ 134,132,133
Mavv 12,30,108
E_ 76
Mavv 16,30,112
E_ 77
Mavv 36,34,112
W_ 0
E_ 78
Mavv 56,38,112
s_nop 0
Tv 30,v2,1024
Tv 32,v2,7168
Mavv 76,42,112
E_ 79
Mavv 72,42,108
s_nop 0
Tv 34,v3,1024
Tv 36,v3,7168
Mavv 52,38,108
C_ 116,76,77
C_ 117,78,79
Mavv 48,38,104
E_ 80
Mavv 68,42,104
E_ 81
Mavv 64,42,100
E_ 82
Mavv 44,38,100
E_ 83
Mavv 40,38,96
C_ 120,80,81
C_ 121,82,83
Mavv 60,42,96
W_ 0
E_ 84
Mavv 80,30,96
s_nop 0
Tv 38,v4,1024
Tv 40,v4,7168
Mavv 100,34,96
E_ 85
Mavv 104,34,100
s_nop 0
Tv 42,v5,1024
Tv 44,v5,7168
Mavv 84,30,100
E_ 86
Mavv 88,30,104
E_ 87
Mavv 108,34,104
C_ 124,84,85
C_ 125,86,87
Mavv 112,34,108
E_ 88
Mavv 92,30,108
E_ 89
Mavv 96,30,112
E_ 90
Mavv 116,34,112
W_ 0
E_ 91
Mavv 136,38,112
s_nop 0
Tv 30,v2,2048
Tv 32,v2,8192
Mavv 156,42,112
C_ 128,88,89
C_ 129,90,91
Mavv 152,42,108
s_nop 0
Tv 34,v3,2048
Tv 36,v3,8192
Mavv 132,38,108
E_ 92
Mavv 128,38,104
E_ 93
Mavv 148,42,104
E_ 94
Mavv 144,42,100
E_ 95
Mavv 124,38,100
C_ 132,92,93
C_ 133,94,95
Mavv 120,38,96
v_mov_b32 v92, 0x3f803f80
v_mov_b32 v93, 0x3f803f80
Mavv 140,42,96
W_ 0
v_mov_b32 v94, 0x3f803f80
v_mov_b32 v95, 0x3f803f80
Mavv 160,30,96
s_nop 0
Tv 38,v4,2048
Tv 40,v4,8192
Mavv 180,34,96
Mavv 184,34,100
s_nop 0
Tv 42,v5,2048
Tv 44,v5,8192
Mavv 164,30,100
Mavv 168,30,104
Mavv 188,34,104
S_ s65,s61,61440
Mavv 192,34,108
S_ m0,s51,0
Mavv 172,30,108
s_waitcnt vmcnt(6) lgkmcnt(0)
s_barrier
Mavv 176,30,112
Ln v7,32,s65,0
Mavv 196,34,112
Mavv 216,38,112
Dv 30,%0,18432
Mavv 236,42,112
Dv 34,v1,18432
Mavv 232,42,108
Ln v8,32,s65,3072
S_ m0,s51,896
Mavv 212,38,108
Mavv 208,38,104
Ln v7,32,s65,128
Mavv 228,42,104
Mavv 224,42,100
Ln v8,32,s65,3200
S_ m0,s51,1792
Mavv 204,38,100
Mavv 200,38,96
Ln v7,32,s65,256
Mavv 220,42,96
.Lstep2_4:
Zvvv 76,92,116
Dv 38,%0,19456
Zvvv 80,92,120
Dv 42,v1,19456
Zvvv 84,92,124
Zvvv 88,92,128
Zvvv 92,92,132
W_ 2
Nvvv 96,30,136,56
Mvvv 96,34,156
Nvvv 100,30,140,60
s_nop 1
v_max3_f32 v10, v76, v80, v84
s_nop 0
v_max3_f32 v10, v10, v88, v92
Mvvv 100,34,160
v_cmp_lt_f32 vcc, 0x42000000, v10
s_cbranch_vccnz .Ltramp2_3
Nvvv 104,30,144,64
A_ v46,v46,v76
A_ v47,v47,v80
Mvvv 104,34,164
A_ v48,v48,v84
A_ v49,v49,v88
Nvvv 108,30,148,68
A_ v50,v50,v92
Mvvv 108,34,168
Ln v8,32,s65,3328
Nvvv 112,30,152,72
Mvvv 112,34,172
W_ 0
Mvvv 112,38,192
Dv 30,%0,20480
Mvvv 112,42,212
Dv 34,v1,20480
Mvvv 108,38,188
Mvvv 108,42,208
Mvvv 104,38,184
Mvvv 104,42,204
Mvvv 100,38,180
Mvvv 100,42,200
Mvvv 96,38,176
Mvvv 96,42,196
W_ 0
Mvvv 96,30,216
Dv 38,%0,12288
Mvvv 96,34,236
Dv 42,v1,12288
Mvvv 100,30,220
Mvvv 100,34,240
Mvvv 104,30,224
Mvvv 104,34,244
Mvvv 108,30,228
Mvvv 108,34,248
s_nop 0
E_ 96
Mvvv 112,30,232
E_ 97
Mvvv 112,34,252
W_ 0
E_ 98
Nvvv 76,38,136,56
Dv 30,%0,13312
Mvvv 76,42,156
Dv 34,v1,13312
Nvvv 80,38,140,60
E_ 99
Mvvv 80,42,160
C_ 99,98,99
C_ 98,96,97
Nvvv 84,38,144,64
E_ 100
Mvvv 84,42,164
E_ 101
Nvvv 88,38,148,68
E_ 102
Mvvv 88,42,168
E_ 103
Nvvv 92,38,152,72
C_ 103,102,103
C_ 102,100,101
Mvvv 92,42,172
W_ 0
E_ 104
Mvvv 92,30,192
Dv 38,%0,14336
Mvvv 92,34,212
Dv 42,v1,14336
Mvvv 88,30,188
E_ 105
Mvvv 88,34,208
E_ 106
Mvvv 84,30,184
S_ s65,s61,49152
E_ 107
Mvvv 84,34,204
S_ m0,s51,12288
C_ 107,106,107
C_ 106,104,105
Mvvv 80,30,180
s_waitcnt vmcnt(6) lgkmcnt(0)
s_barrier
Mvvv 80,34,200
L_ v9,36,s65,0
Mvvv 76,30,176
E_ 108
Mvvv 76,34,196
L_ v9,36,s65,3072
S_ m0,s51,13184
Mvvv 76,38,216
Tv 30,v2,24576
Tv 32,v2,30720
Mvvv 76,42,236
L_ v9,36,s65,128
Mvvv 80,38,220
s_nop 0
Tv 34,v3,24576
Tv 36,v3,30720
Mvvv 80,42,240
L_ v9,36,s65,3200
S_ m0,s51,14080
Mvvv 84,38,224
E_ 109
Mvvv 84,42,244
L_ v9,36,s65,256
Mvvv 88,38,228
E_ 110
Mvvv 88,42,248
L_ v9,36,s65,3328
Mvvv 92,38,232
E_ 111
Mvvv 92,42,252
W_ 0
C_ 111,110,111
C_ 110,108,109
Mavv 0,30,116
s_nop 0
Tv 38,v4,24576
Tv 40,v4,30720
Mavv 20,34,116
E_ 112
Mavv 24,34,120
s_nop 0
Tv 42,v5,24576
Tv 44,v5,30720
Mavv 4,30,120
E_ 113
Mavv 8,30,124
E_ 114
Mavv 28,34,124
E_ 115
Mavv 32,34,128
C_ 115,114,115
C_ 114,112,113
Mavv 12,30,128
E_ 76
Mavv 16,30,132
E_ 77
Mavv 36,34,132
W_ 0
E_ 78
Mavv 56,38,132
s_nop 0
Tv 30,v2,25600
Tv 32,v2,31744
Mavv 76,42,132
E_ 79
Mavv 72,42,128
s_nop 0
Tv 34,v3,25600
Tv 36,v3,31744
Mavv 52,38,128
C_ 96,76,77
C_ 97,78,79
Mavv 48,38,124
E_ 80
Mavv 68,42,124
E_ 81
Mavv 64,42,120
E_ 82
Mavv 44,38,120
E_ 83
Mavv 40,38,116
C_ 100,80,81
C_ 101,82,83
Mavv 60,42,116
W_ 0
E_ 84
Mavv 80,30,116
s_nop 0
Tv 38,v4,25600
Tv 40,v4,31744
Mavv 100,34,116
E_ 85
Mavv 104,34,120
s_nop 0
Tv 42,v5,25600
Tv 44,v5,31744
Mavv 84,30,120
E_ 86
Mavv 88,30,124
E_ 87
Mavv 108,34,124
C_ 104,84,85
C_ 105,86,87
Mavv 112,34,128
E_ 88
Mavv 92,30,128
E_ 89
Mavv 96,30,132
E_ 90
Mavv 116,34,132
W_ 0
E_ 91
Mavv 136,38,132
s_nop 0
Tv 30,v2,26624
Tv 32,v2,32768
Mavv 156,42,132
C_ 108,88,89
C_ 109,90,91
Mavv 152,42,128
s_nop 0
Tv 34,v3,26624
Tv 36,v3,32768
Mavv 132,38,128
E_ 92
Mavv 128,38,124
E_ 93
Mavv 148,42,124
E_ 94
Mavv 144,42,120
E_ 95
Mavv 124,38,120
C_ 112,92,93
C_ 113,94,95
Mavv 120,38,116
v_mov_b32 v92, 0x3f803f80
v_mov_b32 v93, 0x3f803f80
Mavv 140,42,116
W_ 0
v_mov_b32 v94, 0x3f803f80
v_mov_b32 v95, 0x3f803f80
Mavv 160,30,116
s_nop 0
Tv 38,v4,26624
Tv 40,v4,32768
Mavv 180,34,116
Mavv 184,34,120
s_nop 0
Tv 42,v5,26624
Tv 44,v5,32768
Mavv 164,30,120
Mavv 168,30,124
Mavv 188,34,124
S_ s65,s61,73728
Mavv 192,34,128
S_ m0,s51,24576
Mavv 172,30,128
s_waitcnt vmcnt(6) lgkmcnt(0)
s_barrier
Mavv 176,30,132
Ln v7,32,s65,0
Mavv 196,34,132
Mavv 216,38,132
Dv 30,%0,6144
Mavv 236,42,132
Dv 34,v1,6144
Mavv 232,42,128
Ln v8,32,s65,3072
S_ m0,s51,25472
Mavv 212,38,128
Mavv 208,38,124
Ln v7,32,s65,128
Mavv 228,42,124
Mavv 224,42,120
Ln v8,32,s65,3200
S_ m0,s51,26368
Mavv 204,38,120
Mavv 200,38,116
Ln v7,32,s65,256
Mavv 220,42,116
.Lstep2_5:
Zvvv 76,92,96
Dv 38,%0,7168
Zvvv 80,92,100
Dv 42,v1,7168
Zvvv 84,92,104
Zvvv 88,92,108
Zvvv 92,92,112
W_ 2
Nvvv 116,30,136,56
Mvvv 116,34,156
Nvvv 120,30,140,60
s_nop 1
v_max3_f32 v10, v76, v80, v84
s_nop 0
v_max3_f32 v10, v10, v88, v92
Mvvv 120,34,160
v_cmp_lt_f32 vcc, 0x42000000, v10
s_cbranch_vccnz .Ltramp2_4
Nvvv 124,30,144,64
A_ v46,v46,v76
A_ v47,v47,v80
Mvvv 124,34,164
A_ v48,v48,v84
A_ v49,v49,v88
Nvvv 128,30,148,68
A_ v50,v50,v92
Mvvv 128,34,168
Ln v8,32,s65,3328
Nvvv 132,30,152,72
Mvvv 132,34,172
W_ 0
Mvvv 132,38,192
Dv 30,%0,8192
Mvvv 132,42,212
Dv 34,v1,8192
Mvvv 128,38,188
Mvvv 128,42,208
Mvvv 124,38,184
Mvvv 124,42,204
Mvvv 120,38,180
Mvvv 120,42,200
Mvvv 116,38,176
Mvvv 116,42,196
W_ 0
Mvvv 116,30,216
Dv 38,%0,0
Mvvv 116,34,236
Dv 42,v1,0
Mvvv 120,30,220
Mvvv 120,34,240
Mvvv 124,30,224
Mvvv 124,34,244
Mvvv 128,30,228
Mvvv 128,34,248
s_nop 0
E_ 116
Mvvv 132,30,232
E_ 117
Mvvv 132,34,252
W_ 0
E_ 118
Nvvv 76,38,136,56
Dv 30,%0,1024
Mvvv 76,42,156
Dv 34,v1,1024
Nvvv 80,38,140,60
E_ 119
Mvvv 80,42,160
C_ 119,118,119
C_ 118,116,117
Nvvv 84,38,144,64
E_ 120
Mvvv 84,42,164
E_ 121
Nvvv 88,38,148,68
E_ 122
Mvvv 88,42,168
E_ 123
Nvvv 92,38,152,72
C_ 123,122,123
C_ 122,120,121
Mvvv 92,42,172
W_ 0
E_ 124
Mvvv 92,30,192
Dv 38,%0,2048
Mvvv 92,34,212
Dv 42,v1,2048
Mvvv 88,30,188
E_ 125
Mvvv 88,34,208
E_ 126
Mvvv 84,30,184
S_ s65,s61,61440
E_ 127
Mvvv 84,34,204
S_ m0,s51,0
C_ 127,126,127
C_ 126,124,125
Mvvv 80,30,180
s_waitcnt vmcnt(6) lgkmcnt(0)
s_barrier
Mvvv 80,34,200
L_ v9,36,s65,0
Mvvv 76,30,176
E_ 128
Mvvv 76,34,196
L_ v9,36,s65,3072
S_ m0,s51,896
Mvvv 76,38,216
Tv 30,v2,12288
Tv 32,v2,18432
Mvvv 76,42,236
L_ v9,36,s65,128
Mvvv 80,38,220
s_nop 0
Tv 34,v3,12288
Tv 36,v3,18432
Mvvv 80,42,240
L_ v9,36,s65,3200
S_ m0,s51,1792
Mvvv 84,38,224
E_ 129
Mvvv 84,42,244
L_ v9,36,s65,256
Mvvv 88,38,228
E_ 130
Mvvv 88,42,248
L_ v9,36,s65,3328
Mvvv 92,38,232
E_ 131
Mvvv 92,42,252
W_ 0
C_ 131,130,131
C_ 130,128,129
Mavv 0,30,96
s_nop 0
Tv 38,v4,12288
Tv 40,v4,18432
Mavv 20,34,96
E_ 132
)A"
R"A(Mavv 24,34,100
s_nop 0
Tv 42,v5,12288
Tv 44,v5,18432
Mavv 4,30,100
E_ 133
Mavv 8,30,104
E_ 134
Mavv 28,34,104
E_ 135
Mavv 32,34,108
C_ 135,134,135
C_ 134,132,133
Mavv 12,30,108
E_ 76
Mavv 16,30,112
E_ 77
Mavv 36,34,112
W_ 0
E_ 78
Mavv 56,38,112
s_nop 0
Tv 30,v2,13312
Tv 32,v2,19456
Mavv 76,42,112
E_ 79
Mavv 72,42,108
s_nop 0
Tv 34,v3,13312
Tv 36,v3,19456
Mavv 52,38,108
C_ 116,76,77
C_ 117,78,79
Mavv 48,38,104
E_ 80
Mavv 68,42,104
E_ 81
Mavv 64,42,100
E_ 82
Mavv 44,38,100
E_ 83
Mavv 40,38,96
C_ 120,80,81
C_ 121,82,83
Mavv 60,42,96
W_ 0
E_ 84
Mavv 80,30,96
s_nop 0
Tv 38,v4,13312
Tv 40,v4,19456
Mavv 100,34,96
E_ 85
Mavv 104,34,100
s_nop 0
Tv 42,v5,13312
Tv 44,v5,19456
Mavv 84,30,100
E_ 86
Mavv 88,30,104
E_ 87
Mavv 108,34,104
C_ 124,84,85
C_ 125,86,87
Mavv 112,34,108
E_ 88
Mavv 92,30,108
E_ 89
Mavv 96,30,112
E_ 90
Mavv 116,34,112
W_ 0
E_ 91
Mavv 136,38,112
s_nop 0
Tv 30,v2,14336
Tv 32,v2,20480
Mavv 156,42,112
C_ 128,88,89
C_ 129,90,91
Mavv 152,42,108
s_nop 0
Tv 34,v3,14336
Tv 36,v3,20480
Mavv 132,38,108
E_ 92
Mavv 128,38,104
E_ 93
Mavv 148,42,104
E_ 94
Mavv 144,42,100
E_ 95
Mavv 124,38,100
C_ 132,92,93
C_ 133,94,95
Mavv 120,38,96
v_mov_b32 v92, 0x3f803f80
v_mov_b32 v93, 0x3f803f80
Mavv 140,42,96
W_ 0
v_mov_b32 v94, 0x3f803f80
v_mov_b32 v95, 0x3f803f80
Mavv 160,30,96
s_nop 0
Tv 38,v4,14336
Tv 40,v4,20480
Mavv 180,34,96
Mavv 184,34,100
s_nop 0
Tv 42,v5,14336
Tv 44,v5,20480
Mavv 164,30,100
Mavv 168,30,104
Mavv 188,34,104
S_ s65,s61,86016
Mavv 192,34,108
S_ m0,s51,12288
Mavv 172,30,108
s_waitcnt vmcnt(6) lgkmcnt(0)
s_barrier
Mavv 176,30,112
Ln v7,32,s65,0
Mavv 196,34,112
Mavv 216,38,112
Dv 30,%0,30720
Mavv 236,42,112
Dv 34,v1,30720
Mavv 232,42,108
Ln v8,32,s65,3072
S_ m0,s51,13184
Mavv 212,38,108
Mavv 208,38,104
Ln v7,32,s65,128
Mavv 228,42,104
Mavv 224,42,100
Ln v8,32,s65,3200
S_ m0,s51,14080
Mavv 204,38,100
Mavv 200,38,96
Ln v7,32,s65,256
Mavv 220,42,96
.Lstep2_6:
Zvvv 76,92,116
Dv 38,%0,31744
Zvvv 80,92,120
Dv 42,v1,31744
Zvvv 84,92,124
Zvvv 88,92,128
Zvvv 92,92,132
W_ 2
Nvvv 96,30,136,56
Mvvv 96,34,156
Nvvv 100,30,140,60
s_nop 1
v_max3_f32 v10, v76, v80, v84
s_nop 0
v_max3_f32 v10, v10, v88, v92
Mvvv 100,34,160
v_cmp_lt_f32 vcc, 0x42000000, v10
s_cbranch_vccnz .Ltramp2_5
Nvvv 104,30,144,64
A_ v46,v46,v76
A_ v47,v47,v80
Mvvv 104,34,164
A_ v48,v48,v84
A_ v49,v49,v88
Nvvv 108,30,148,68
A_ v50,v50,v92
Mvvv 108,34,168
Ln v8,32,s65,3328
Nvvv 112,30,152,72
R_ v11,a248
Mvvv 112,34,172
W_ 0
v_min_u32 v11, 240, v11
Mvvv 112,38,192
Dv 30,%0,32768
v_lshlrev_b32 v11, 3, v11
Mvvv 112,42,212
Dv 34,v1,32768
Mvvv 108,38,188
buffer_load_dword a254, v11, s[44:47], s66 offen
Mvvv 108,42,208
Mvvv 104,38,184
Mvvv 104,42,204
Mvvv 100,38,180
Mvvv 100,42,200
Mvvv 96,38,176
Mvvv 96,42,196
W_ 0
Mvvv 96,30,216
Dv 38,%0,24576
Mvvv 96,34,236
Dv 42,v1,24576
Mvvv 100,30,220
Mvvv 100,34,240
Mvvv 104,30,224
Mvvv 104,34,244
Mvvv 108,30,228
Mvvv 108,34,248
s_nop 0
E_ 96
Mvvv 112,30,232
E_ 97
Mvvv 112,34,252
W_ 0
E_ 98
Nvvv 76,38,136,56
Dv 30,%0,25600
Mvvv 76,42,156
Dv 34,v1,25600
Nvvv 80,38,140,60
E_ 99
Mvvv 80,42,160
C_ 99,98,99
C_ 98,96,97
Nvvv 84,38,144,64
E_ 100
Mvvv 84,42,164
E_ 101
Nvvv 88,38,148,68
E_ 102
Mvvv 88,42,168
E_ 103
Nvvv 92,38,152,72
C_ 103,102,103
C_ 102,100,101
Mvvv 92,42,172
W_ 0
E_ 104
Mvvv 92,30,192
Dv 38,%0,26624
Mvvv 92,34,212
Dv 42,v1,26624
Mvvv 88,30,188
E_ 105
Mvvv 88,34,208
E_ 106
Mvvv 84,30,184
S_ s65,s61,73728
E_ 107
Mvvv 84,34,204
S_ m0,s51,24576
C_ 107,106,107
C_ 106,104,105
Mvvv 80,30,180
s_waitcnt vmcnt(7) lgkmcnt(0)
s_barrier
Mvvv 80,34,200
L_ v9,36,s65,0
Mvvv 76,30,176
E_ 108
Mvvv 76,34,196
L_ v9,36,s65,3072
S_ m0,s51,25472
Mvvv 76,38,216
Tv 30,v2,0
Tv 32,v2,6144
Mvvv 76,42,236
L_ v9,36,s65,128
Mvvv 80,38,220
s_nop 0
Tv 34,v3,0
Tv 36,v3,6144
Mvvv 80,42,240
L_ v9,36,s65,3200
S_ m0,s51,26368
Mvvv 84,38,224
E_ 109
Mvvv 84,42,244
L_ v9,36,s65,256
Mvvv 88,38,228
E_ 110
Mvvv 88,42,248
L_ v9,36,s65,3328
Mvvv 92,38,232
E_ 111
Mvvv 92,42,252
W_ 0
C_ 111,110,111
C_ 110,108,109
Mavv 0,30,116
s_nop 0
Tv 38,v4,0
Tv 40,v4,6144
Mavv 20,34,116
E_ 112
Mavv 24,34,120
s_nop 0
Tv 42,v5,0
Tv 44,v5,6144
Mavv 4,30,120
E_ 113
Mavv 8,30,124
E_ 114
Mavv 28,34,124
E_ 115
Mavv 32,34,128
C_ 115,114,115
C_ 114,112,113
Mavv 12,30,128
E_ 76
Mavv 16,30,132
E_ 77
Mavv 36,34,132
W_ 0
E_ 78
Mavv 56,38,132
s_nop 0
Tv 30,v2,1024
Tv 32,v2,7168
Mavv 76,42,132
E_ 79
Mavv 72,42,128
s_nop 0
Tv 34,v3,1024
Tv 36,v3,7168
Mavv 52,38,128
C_ 96,76,77
C_ 97,78,79
Mavv 48,38,124
E_ 80
Mavv 68,42,124
E_ 81
Mavv 64,42,120
E_ 82
Mavv 44,38,120
E_ 83
Mavv 40,38,116
C_ 100,80,81
C_ 101,82,83
Mavv 60,42,116
W_ 0
E_ 84
Mavv 80,30,116
s_nop 0
Tv 38,v4,1024
Tv 40,v4,7168
Mavv 100,34,116
E_ 85
Mavv 104,34,120
s_nop 0
Tv 42,v5,1024
Tv 44,v5,7168
Mavv 84,30,120
E_ 86
Mavv 88,30,124
E_ 87
Mavv 108,34,124
C_ 104,84,85
C_ 105,86,87
Mavv 112,34,128
E_ 88
Mavv 92,30,128
E_ 89
Mavv 96,30,132
E_ 90
Mavv 116,34,132
W_ 0
E_ 91
Mavv 136,38,132
s_nop 0
Tv 30,v2,2048
Tv 32,v2,8192
Mavv 156,42,132
C_ 108,88,89
C_ 109,90,91
Mavv 152,42,128
s_nop 0
Tv 34,v3,2048
Tv 36,v3,8192
Mavv 132,38,128
E_ 92
Mavv 128,38,124
E_ 93
Mavv 148,42,124
E_ 94
Mavv 144,42,120
E_ 95
Mavv 124,38,120
C_ 112,92,93
C_ 113,94,95
Mavv 120,38,116
v_mov_b32 v92, 0x3f803f80
v_mov_b32 v93, 0x3f803f80
Mavv 140,42,116
W_ 0
v_mov_b32 v94, 0x3f803f80
v_mov_b32 v95, 0x3f803f80
Mavv 160,30,116
s_nop 0
Tv 38,v4,2048
Tv 40,v4,8192
Mavv 180,34,116
Mavv 184,34,120
s_nop 0
Tv 42,v5,2048
Tv 44,v5,8192
Mavv 164,30,120
Mavv 168,30,124
Mavv 188,34,124
S_ s65,s61,98304
Mavv 192,34,128
S_ m0,s51,0
Mavv 172,30,128
s_waitcnt vmcnt(7) lgkmcnt(0)
s_barrier
Mavv 176,30,132
Ln v7,32,s65,0
Mavv 196,34,132
Mavv 216,38,132
Dv 30,%0,18432
Mavv 236,42,132
Dv 34,v1,18432
Mavv 232,42,128
Ln v8,32,s65,3072
S_ m0,s51,896
Mavv 212,38,128
Mavv 208,38,124
Ln v7,32,s65,128
Mavv 228,42,124
Mavv 224,42,120
Ln v8,32,s65,3200
S_ m0,s51,1792
Mavv 204,38,120
Mavv 200,38,116
Ln v7,32,s65,256
Mavv 220,42,116
s_cmp_eq_u32 s4, 0
s_cbranch_scc1 .Lhs_c2
.Lhs_c2_back:
.Lstep2_7:
Zvvv 76,92,96
Dv 38,%0,19456
Zvvv 80,92,100
Dv 42,v1,19456
Zvvv 84,92,104
Zvvv 88,92,108
Zvvv 92,92,112
W_ 2
Nvvv 116,30,136,56
Mvvv 116,34,156
Nvvv 120,30,140,60
s_nop 1
v_max3_f32 v10, v76, v80, v84
s_nop 0
v_max3_f32 v10, v10, v88, v92
Mvvv 120,34,160
v_cmp_lt_f32 vcc, 0x42000000, v10
s_cbranch_vccnz .Ltramp2_6
Nvvv 124,30,144,64
A_ v46,v46,v76
A_ v47,v47,v80
Mvvv 124,34,164
A_ v48,v48,v84
A_ v49,v49,v88
Nvvv 128,30,148,68
A_ v50,v50,v92
Mvvv 128,34,168
Ln v8,32,s65,3328
Nvvv 132,30,152,72
Mvvv 132,34,172
W_ 0
Mvvv 132,38,192
Dv 30,%0,20480
Mvvv 132,42,212
Dv 34,v1,20480
Mvvv 128,38,188
Mvvv 128,42,208
Mvvv 124,38,184
Mvvv 124,42,204
Mvvv 120,38,180
Mvvv 120,42,200
Mvvv 116,38,176
Mvvv 116,42,196
W_ 0
Mvvv 116,30,216
Dv 38,%0,12288
Mvvv 116,34,236
Dv 42,v1,12288
Mvvv 120,30,220
Mvvv 120,34,240
Mvvv 124,30,224
Mvvv 124,34,244
Mvvv 128,30,228
Mvvv 128,34,248
s_nop 0
E_ 116
Mvvv 132,30,232
E_ 117
Mvvv 132,34,252
W_ 0
E_ 118
Nvvv 76,38,136,56
Dv 30,%0,13312
Mvvv 76,42,156
Dv 34,v1,13312
Nvvv 80,38,140,60
E_ 119
Mvvv 80,42,160
C_ 119,118,119
C_ 118,116,117
Nvvv 84,38,144,64
E_ 120
Mvvv 84,42,164
E_ 121
Nvvv 88,38,148,68
E_ 122
Mvvv 88,42,168
E_ 123
Nvvv 92,38,152,72
C_ 123,122,123
C_ 122,120,121
Mvvv 92,42,172
W_ 0
E_ 124
Mvvv 92,30,192
Dv 38,%0,14336
Mvvv 92,34,212
Dv 42,v1,14336
Mvvv 88,30,188
E_ 125
Mvvv 88,34,208
E_ 126
Mvvv 84,30,184
S_ s65,s61,86016
E_ 127
Mvvv 84,34,204
S_ m0,s51,12288
C_ 127,126,127
C_ 126,124,125
Mvvv 80,30,180
s_waitcnt vmcnt(6) lgkmcnt(0)
s_barrier
Mvvv 80,34,200
L_ v9,36,s65,0
Mvvv 76,30,176
E_ 128
Mvvv 76,34,196
L_ v9,36,s65,3072
S_ m0,s51,13184
Mvvv 76,38,216
Tv 30,v2,24576
Tv 32,v2,30720
Mvvv 76,42,236
L_ v9,36,s65,128
Mvvv 80,38,220
s_nop 0
Tv 34,v3,24576
Tv 36,v3,30720
Mvvv 80,42,240
L_ v9,36,s65,3200
S_ m0,s51,14080
Mvvv 84,38,224
E_ 129
Mvvv 84,42,244
L_ v9,36,s65,256
Mvvv 88,38,228
E_ 130
Mvvv 88,42,248
L_ v9,36,s65,3328
Mvvv 92,38,232
E_ 131
Mvvv 92,42,252
W_ 0
C_ 131,130,131
C_ 130,128,129
Mavv 0,30,96
s_nop 0
Tv 38,v4,24576
Tv 40,v4,30720
Mavv 20,34,96
E_ 132
Mavv 24,34,100
s_nop 0
Tv 42,v5,24576
Tv 44,v5,30720
Mavv 4,30,100
E_ 133
Mavv 8,30,104
E_ 134
Mavv 28,34,104
E_ 135
Mavv 32,34,108
C_ 135,134,135
C_ 134,132,133
Mavv 12,30,108
E_ 76
Mavv 16,30,112
E_ 77
Mavv 36,34,112
W_ 0
E_ 78
Mavv 56,38,112
s_nop 0
Tv 30,v2,25600
Tv 32,v2,31744
Mavv 76,42,112
E_ 79
Mavv 72,42,108
s_nop 0
Tv 34,v3,25600
Tv 36,v3,31744
Mavv 52,38,108
C_ 116,76,77
C_ 117,78,79
Mavv 48,38,104
E_ 80
Mavv 68,42,104
E_ 81
Mavv 64,42,100
E_ 82
Mavv 44,38,100
E_ 83
Mavv 40,38,96
C_ 120,80,81
C_ 121,82,83
Mavv 60,42,96
W_ 0
E_ 84
Mavv 80,30,96
s_nop 0
Tv 38,v4,25600
Tv 40,v4,31744
Mavv 100,34,96
E_ 85
Mavv 104,34,100
s_nop 0
Tv 42,v5,25600
Tv 44,v5,31744
Mavv 84,30,100
E_ 86
Mavv 88,30,104
E_ 87
Mavv 108,34,104
C_ 124,84,85
C_ 125,86,87
Mavv 112,34,108
E_ 88
Mavv 92,30,108
E_ 89
Mavv 96,30,112
E_ 90
Mavv 116,34,112
W_ 0
E_ 91
Mavv 136,38,112
s_nop 0
Tv 30,v2,26624
Tv 32,v2,32768
Mavv 156,42,112
C_ 128,88,89
C_ 129,90,91
Mavv 152,42,108
s_nop 0
Tv 34,v3,26624
Tv 36,v3,32768
Mavv 132,38,108
E_ 92
Mavv 128,38,104
E_ 93
Mavv 148,42,104
E_ 94
Mavv 144,42,100
E_ 95
Mavv 124,38,100
C_ 132,92,93
C_ 133,94,95
Mavv 120,38,96
v_mov_b32 v92, 0x3f803f80
v_mov_b32 v93, 0x3f803f80
Mavv 140,42,96
W_ 0
v_mov_b32 v94, 0x3f803f80
v_mov_b32 v95, 0x3f803f80
Mavv 160,30,96
s_nop 0
Tv 38,v4,26624
Tv 40,v4,32768
Mavv 180,34,96
Mavv 184,34,100
s_nop 0
Tv 42,v5,26624
Tv 44,v5,32768
Mavv 164,30,100
Mavv 168,30,104
Mavv 188,34,104
S_ s65,s61,110592
Mavv 192,34,108
S_ m0,s51,24576
Mavv 172,30,108
s_waitcnt vmcnt(6) lgkmcnt(0)
s_barrier
Mavv 176,30,112
Ln v7,32,s65,0
Mavv 196,34,112
Mavv 216,38,112
Dv 30,%0,6144
Mavv 236,42,112
Dv 34,v1,6144
Mavv 232,42,108
Ln v8,32,s65,3072
S_ m0,s51,25472
Mavv 212,38,108
Mavv 208,38,104
Ln v7,32,s65,128
Mavv 228,42,104
Mavv 224,42,100
Ln v8,32,s65,3200
S_ m0,s51,26368
Mavv 204,38,100
Mavv 200,38,96
Ln v7,32,s65,256
Mavv 220,42,96
.Lstep2_8:
Zvvv 76,92,116
Dv 38,%0,7168
Zvvv 80,92,120
Dv 42,v1,7168
Zvvv 84,92,124
Zvvv 88,92,128
Zvvv 92,92,132
W_ 2
Nvvv 96,30,136,56
Mvvv 96,34,156
Nvvv 100,30,140,60
s_nop 1
v_max3_f32 v10, v76, v80, v84
s_nop 0
v_max3_f32 v10, v10, v88, v92
Mvvv 100,34,160
v_cmp_lt_f32 vcc, 0x42000000, v10
s_cbranch_vccnz .Ltramp2_7
Nvvv 104,30,144,64
A_ v46,v46,v76
A_ v47,v47,v80
Mvvv 104,34,164
A_ v48,v48,v84
A_ v49,v49,v88
Nvvv 108,30,148,68
A_ v50,v50,v92
Mvvv 108,34,168
Ln v8,32,s65,3328
Nvvv 112,30,152,72
Mvvv 112,34,172
W_ 0
Mvvv 112,38,192
Dv 30,%0,8192
Mvvv 112,42,212
Dv 34,v1,8192
Mvvv 108,38,188
Mvvv 108,42,208
Mvvv 104,38,184
Mvvv 104,42,204
Mvvv 100,38,180
Mvvv 100,42,200
Mvvv 96,38,176
Mvvv 96,42,196
W_ 0
Mvvv 96,30,216
Dv 38,%0,0
Mvvv 96,34,236
Dv 42,v1,0
Mvvv 100,30,220
Mvvv 100,34,240
Mvvv 104,30,224
Mvvv 104,34,244
Mvvv 108,30,228
Mvvv 108,34,248
s_nop 0
E_ 96
Mvvv 112,30,232
E_ 97
Mvvv 112,34,252
W_ 0
E_ 98
Nvvv 76,38,136,56
Dv 30,%0,1024
Mvvv 76,42,156
Dv 34,v1,1024
Nvvv 80,38,140,60
E_ 99
Mvvv 80,42,160
C_ 99,98,99
C_ 98,96,97
Nvvv 84,38,144,64
E_ 100
Mvvv 84,42,164
E_ 101
Nvvv 88,38,148,68
E_ 102
Mvvv 88,42,168
E_ 103
Nvvv 92,38,152,72
C_ 103,102,103
C_ 102,100,101
Mvvv 92,42,172
W_ 0
E_ 104
Mvvv 92,30,192
Dv 38,%0,2048
Mvvv 92,34,212
Dv 42,v1,2048
Mvvv 88,30,188
E_ 105
Mvvv 88,34,208
E_ 106
Mvvv 84,30,184
S_ s65,s61,98304
E_ 107
Mvvv 84,34,204
S_ m0,s51,0
C_ 107,106,107
C_ 106,104,105
Mvvv 80,30,180
s_waitcnt vmcnt(6) lgkmcnt(0)
s_barrier
Mvvv 80,34,200
L_ v9,36,s65,0
Mvvv 76,30,176
E_ 108
Mvvv 76,34,196
L_ v9,36,s65,3072
S_ m0,s51,896
Mvvv 76,38,216
Tv 30,v2,12288
Tv 32,v2,18432
Mvvv 76,42,236
L_ v9,36,s65,128
Mvvv 80,38,220
s_nop 0
Tv 34,v3,12288
Tv 36,v3,18432
Mvvv 80,42,240
L_ v9,36,s65,3200
S_ m0,s51,1792
Mvvv 84,38,224
E_ 109
Mvvv 84,42,244
L_ v9,36,s65,256
Mvvv 88,38,228
E_ 110
Mvvv 88,42,248
L_ v9,36,s65,3328
Mvvv 92,38,232
E_ 111
Mvvv 92,42,252
W_ 0
C_ 111,110,111
C_ 110,108,109
Mavv 0,30,116
s_nop 0
Tv 38,v4,12288
Tv 40,v4,18432
Mavv 20,34,116
E_ 112
Mavv 24,34,120
s_nop 0
Tv 42,v5,12288
Tv 44,v5,18432
Mavv 4,30,120
E_ 113
Mavv 8,30,124
E_ 114
Mavv 28,34,124
E_ 115
Mavv 32,34,128
C_ 115,114,115
C_ 114,112,113
Mavv 12,30,128
E_ 76
Mavv 16,30,132
E_ 77
Mavv 36,34,132
W_ 0
E_ 78
Mavv 56,38,132
s_nop 0
Tv 30,v2,13312
Tv 32,v2,19456
Mavv 76,42,132
E_ 79
Mavv 72,42,128
s_nop 0
Tv 34,v3,13312
Tv 36,v3,19456
Mavv 52,38,128
C_ 96,76,77
C_ 97,78,79
Mavv 48,38,124
E_ 80
Mavv 68,42,124
E_ 81
Mavv 64,42,120
E_ 82
Mavv 44,38,120
E_ 83
Mavv 40,38,116
C_ 100,80,81
C_ 101,82,83
Mavv 60,42,116
W_ 0
E_ 84
Mavv 80,30,116
s_nop 0
Tv 38,v4,13312
Tv 40,v4,19456
Mavv 100,34,116
E_ 85
Mavv 104,34,120
s_nop 0
Tv 42,v5,13312
Tv 44,v5,19456
Mavv 84,30,120
E_ 86
Mavv 88,30,124
E_ 87
Mavv 108,34,124
C_ 104,84,85
C_ 105,86,87
Mavv 112,34,128
E_ 88
Mavv 92,30,128
E_ 89
Mavv 96,30,132
E_ 90
Mavv 116,34,132
W_ 0
E_ 91
Mavv 136,38,132
s_nop 0
Tv 30,v2,14336
Tv 32,v2,20480
Mavv 156,42,132
C_ 108,88,89
C_ 109,90,91
Mavv 152,42,128
s_nop 0
Tv 34,v3,14336
Tv 36,v3,20480
Mavv 132,38,128
E_ 92
Mavv 128,38,124
E_ 93
Mavv 148,42,124
E_ 94
Mavv 144,42,120
E_ 95
Mavv 124,38,120
C_ 112,92,93
C_ 113,94,95
Mavv 120,38,116
v_mov_b32 v92, 0x3f803f80
v_mov_b32 v93, 0x3f803f80
Mavv 140,42,116
W_ 0
v_mov_b32 v94, 0x3f803f80
v_mov_b32 v95, 0x3f803f80
Mavv 160,30,116
s_nop 0
Tv 38,v4,14336
Tv 40,v4,20480
Mavv 180,34,116
Mavv 184,34,120
s_nop 0
Tv 42,v5,14336
Tv 44,v5,20480
Mavv 164,30,120
Mavv 168,30,124
Mavv 188,34,124
S_ s65,s61,122880
Mavv 192,34,128
S_ m0,s51,12288
Mavv 172,30,128
s_waitcnt vmcnt(6) lgkmcnt(0)
s_barrier
Mavv 176,30,132
Ln v7,32,s65,0
Mavv 196,34,132
Mavv 216,38,132
Dv 30,%0,30720
Mavv 236,42,132
Dv 34,v1,30720
Mavv 232,42,128
Ln v8,32,s65,3072
S_ m0,s51,13184
Mavv 212,38,128
Mavv 208,38,124
Ln v7,32,s65,128
Mavv 228,42,124
Mavv 224,42,120
Ln v8,32,s65,3200
S_ m0,s51,14080
Mavv 204,38,120
Mavv 200,38,116
Ln v7,32,s65,256
Mavv 220,42,116
s_cmp_eq_u32 s4, 0
s_cbranch_scc1 .Lhs_d2
.Lhs_d2_back:
.Lstep2_9:
Zvvv 76,92,96
Dv 38,%0,31744
Zvvv 80,92,100
Dv 42,v1,31744
Zvvv 84,92,104
Zvvv 88,92,108
Zvvv 92,92,112
W_ 2
Nvvv 116,30,136,56
Mvvv 116,34,156
Nvvv 120,30,140,60
s_nop 1
v_max3_f32 v10, v76, v80, v84
s_nop 0
v_max3_f32 v10, v10, v88, v92
Mvvv 120,34,160
v_cmp_lt_f32 vcc, 0x42000000, v10
s_cbranch_vccnz .Ltramp2_8
Nvvv 124,30,144,64
A_ v46,v46,v76
A_ v47,v47,v80
Mvvv 124,34,164
A_ v48,v48,v84
A_ v49,v49,v88
Nvvv 128,30,148,68
A_ v50,v50,v92
Mvvv 128,34,168
Ln v8,32,s65,3328
Nvvv 132,30,152,72
Mvvv 132,34,172
W_ 0
Mvvv 132,38,192
Dv 30,%0,32768
Mvvv 132,42,212
Dv 34,v1,32768
Mvvv 128,38,188
Mvvv 128,42,208
Mvvv 124,38,184
Mvvv 124,42,204
Mvvv 120,38,180
Mvvv 120,42,200
Mvvv 116,38,176
Mvvv 116,42,196
W_ 0
Mvvv 116,30,216
Dv 38,%0,24576
Mvvv 116,34,236
Dv 42,v1,24576
Mvvv 120,30,220
Mvvv 120,34,240
Mvvv 124,30,224
Mvvv 124,34,244
Mvvv 128,30,228
Mvvv 128,34,248
s_nop 0
E_ 116
Mvvv 132,30,232
E_ 117
Mvvv 132,34,252
W_ 0
E_ 118
Nvvv 76,38,136,56
Dv 30,%0,25600
Mvvv 76,42,156
Dv 34,v1,25600
Nvvv 80,38,140,60
E_ 119
Mvvv 80,42,160
C_ 119,118,119
C_ 118,116,117
Nvvv 84,38,144,64
E_ 120
Mvvv 84,42,164
E_ 121
Nvvv 88,38,148,68
E_ 122
Mvvv 88,42,168
E_ 123
Nvvv 92,38,152,72
C_ 123,122,123
C_ 122,120,121
Mvvv 92,42,172
W_ 0
E_ 124
Mvvv 92,30,192
Dv 38,%0,26624
Mvvv 92,34,212
Dv 42,v1,26624
Mvvv 88,30,188
E_ 125
Mvvv 88,34,208
E_ 126
Mvvv 84,30,184
S_ s65,s61,110592
E_ 127
Mvvv 84,34,204
S_ m0,s51,24576
C_ 127,126,127
C_ 126,124,125
Mvvv 80,30,180
s_waitcnt vmcnt(6) lgkmcnt(0)
s_barrier
Mvvv 80,34,200
Ln v9,36,s65,0
Mvvv 76,30,176
E_ 128
Mvvv 76,34,196
Ln v9,36,s65,3072
S_ m0,s51,25472
Mvvv 76,38,216
Tv 30,v2,0
Tv 32,v2,6144
Mvvv 76,42,236
Ln v9,36,s65,128
Mvvv 80,38,220
s_nop 0
Tv 34,v3,0
Tv 36,v3,6144
Mvvv 80,42,240
Ln v9,36,s65,3200
S_ m0,s51,26368
Mvvv 84,38,224
E_ 129
Mvvv 84,42,244
Ln v9,36,s65,256
Mvvv 88,38,228
E_ 130
Mvvv 88,42,248
Ln v9,36,s65,3328
Mvvv 92,38,232
E_ 131
Mvvv 92,42,252
W_ 0
C_ 131,130,131
C_ 130,128,129
Mavv 0,30,96
s_nop 0
Tv 38,v4,0
Tv 40,v4,6144
Mavv 20,34,96
E_ 132
Mavv 24,34,100
s_nop 0
Tv 42,v5,0
Tv 44,v5,6144
Mavv 4,30,100
E_ 133
Mavv 8,30,104
E_ 134
Mavv 28,34,104
E_ 135
Mavv 32,34,108
C_ 135,134,135
C_ 134,132,133
Mavv 12,30,108
E_ 76
Mavv 16,30,112
E_ 77
Mavv 36,34,112
W_ 0
E_ 78
Mavv 56,38,112
s_nop 0
Tv 30,v2,1024
Tv 32,v2,7168
Mavv 76,42,112
E_ 79
Mavv 72,42,108
s_nop 0
Tv 34,v3,1024
Tv 36,v3,7168
Mavv 52,38,108
C_ 116,76,77
C_ 117,78,79
Mavv 48,38,104
E_ 80
Mavv 68,42,104
E_ 81
Mavv 64,42,100
E_ 82
Mavv 44,38,100
E_ 83
Mavv 40,38,96
C_ 120,80,81
C_ 121,82,83
Mavv 60,42,96
W_ 0
E_ 84
Mavv 80,30,96
s_nop 0
Tv 38,v4,1024
Tv 40,v4,7168
Mavv 100,34,96
E_ 85
Mavv 104,34,100
s_nop 0
Tv 42,v5,1024
Tv 44,v5,7168
Mavv 84,30,100
E_ 86
Mavv 88,30,104
E_ 87
Mavv 108,34,104
C_ 124,84,85
C_ 125,86,87
Mavv 112,34,108
E_ 88
Mavv 92,30,108
E_ 89
Mavv 96,30,112
E_ 90
Mavv 116,34,112
W_ 0
E_ 91
Mavv 136,38,112
s_nop 0
Tv 30,v2,2048
Tv 32,v2,8192
Mavv 156,42,112
C_ 128,88,89
C_ 129,90,91
Mavv 152,42,108
s_nop 0
Tv 34,v3,2048
Tv 36,v3,8192
Mavv 132,38,108
E_ 92
Mavv 128,38,104
E_ 93
Mavv 148,42,104
E_ 94
Mavv 144,42,100
E_ 95
Mavv 124,38,100
C_ 132,92,93
C_ 133,94,95
Mavv 120,38,96
v_mov_b32 v92, 0x3f803f80
v_mov_b32 v93, 0x3f803f80
Mavv 140,42,96
W_ 0
v_mov_b32 v94, 0x3f803f80
v_mov_b32 v95, 0x3f803f80
Mavv 160,30,96
s_nop 0
Tv 38,v4,2048
Tv 40,v4,8192
Mavv 180,34,96
Mavv 184,34,100
s_nop 0
Tv 42,v5,2048
Tv 44,v5,8192
Mavv 164,30,100
Mavv 168,30,104
Mavv 188,34,104
S_ s65,s61,135168
Mavv 192,34,108
S_ m0,s51,0
Mavv 172,30,108
s_waitcnt vmcnt(6) lgkmcnt(0)
s_barrier
Mavv 176,30,112
Ln v7,32,s65,0
Mavv 196,34,112
Mavv 216,38,112
Dv 30,%0,18432
Mavv 236,42,112
Dv 34,v1,18432
Mavv 232,42,108
Ln v8,32,s65,3072
S_ m0,s51,896
Mavv 212,38,108
Mavv 208,38,104
Ln v7,32,s65,128
Mavv 228,42,104
Mavv 224,42,100
Ln v8,32,s65,3200
S_ m0,s51,1792
Mavv 204,38,100
Mavv 200,38,96
Ln v7,32,s65,256
Mavv 220,42,96
.Lstep2_10:
Zvvv 76,92,116
Dv 38,%0,19456
Zvvv 80,92,120
Dv 42,v1,19456
Zvvv 84,92,124
Zvvv 88,92,128
Zvvv 92,92,132
W_ 2
Nvvv 96,30,136,56
Mvvv 96,34,156
Nvvv 100,30,140,60
s_nop 1
v_max3_f32 v10, v76, v80, v84
s_nop 0
v_max3_f32 v10, v10, v88, v92
Mvvv 100,34,160
v_cmp_lt_f32 vcc, 0x42000000, v10
s_cbranch_vccnz .Ltramp2_9
Nvvv 104,30,144,64
A_ v46,v46,v76
A_ v47,v47,v80
Mvvv 104,34,164
A_ v48,v48,v84
A_ v49,v49,v88
Nvvv 108,30,148,68
A_ v50,v50,v92
Mvvv 108,34,168
Ln v8,32,s65,3328
Nvvv 112,30,152,72
Mvvv 112,34,172
W_ 0
Mvvv 112,38,192
Dv 30,%0,20480
Mvvv 112,42,212
Dv 34,v1,20480
Mvvv 108,38,188
Mvvv 108,42,208
Mvvv 104,38,184
Mvvv 104,42,204
Mvvv 100,38,180
Mvvv 100,42,200
Mvvv 96,38,176
Mvvv 96,42,196
W_ 0
Mvvv 96,30,216
Dv 38,%0,12288
Mvvv 96,34,236
Dv 42,v1,12288
Mvvv 100,30,220
Mvvv 100,34,240
Mvvv 104,30,224
Mvvv 104,34,244
Mvvv 108,30,228
Mvvv 108,34,248
s_nop 0
E_ 96
Mvvv 112,30,232
E_ 97
Mvvv 112,34,252
W_ 0
E_ 98
Nvvv 76,38,136,56
Dv 30,%0,13312
Mvvv 76,42,156
Dv 34,v1,13312
Nvvv 80,38,140,60
E_ 99
Mvvv 80,42,160
C_ 99,98,99
C_ 98,96,97
Nvvv 84,38,144,64
E_ 100
Mvvv 84,42,164
E_ 101
Nvvv 88,38,148,68
E_ 102
Mvvv 88,42,168
E_ 103
Nvvv 92,38,152,72
C_ 103,102,103
C_ 102,100,101
Mvvv 92,42,172
W_ 0
E_ 104
Mvvv 92,30,192
Dv 38,%0,14336
Mvvv 92,34,212
Dv 42,v1,14336
Mvvv 88,30,188
E_ 105
Mvvv 88,34,208
E_ 106
Mvvv 84,30,184
S_ s65,s61,122880
E_ 107
Mvvv 84,34,204
S_ m0,s51,12288
C_ 107,106,107
C_ 106,104,105
Mvvv 80,30,180
s_waitcnt vmcnt(6) lgkmcnt(0)
s_barrier
Mvvv 80,34,200
Ln v9,36,s65,0
Mvvv 76,30,176
E_ 108
Mvvv 76,34,196
Ln v9,36,s65,3072
S_ m0,s51,13184
Mvvv 76,38,216
Tv 30,v2,24576
Tv 32,v2,30720
Mvvv 76,42,236
Ln v9,36,s65,128
Mvvv 80,38,220
s_nop 0
Tv 34,v3,24576
Tv 36,v3,30720
Mvvv 80,42,240
Ln v9,36,s65,3200
S_ m0,s51,14080
Mvvv 84,38,224
E_ 109
Mvvv 84,42,244
Ln v9,36,s65,256
Mvvv 88,38,228
E_ 110
Mvvv 88,42,248
Ln v9,36,s65,3328
Mvvv 92,38,232
E_ 111
Mvvv 92,42,252
W_ 0
C_ 111,110,111
C_ 110,108,109
Mavv 0,30,116
s_nop 0
Tv 38,v4,24576
Tv 40,v4,30720
Mavv 20,34,116
E_ 112
Mavv 24,34,120
s_nop 0
Tv 42,v5,24576
Tv 44,v5,30720
Mavv 4,30,120
E_ 113
Mavv 8,30,124
E_ 114
Mavv 28,34,124
E_ 115
Mavv 32,34,128
C_ 115,114,115
C_ 114,112,113
Mavv 12,30,128
E_ 76
Mavv 16,30,132
E_ 77
Mavv 36,34,132
W_ 0
E_ 78
Mavv 56,38,132
s_nop 0
Tv 30,v2,25600
Tv 32,v2,31744
Mavv 76,42,132
E_ 79
Mavv 72,42,128
s_nop 0
Tv 34,v3,25600
Tv 36,v3,31744
Mavv 52,38,128
C_ 96,76,77
C_ 97,78,79
Mavv 48,38,124
E_ 80
Mavv 68,42,124
E_ 81
Mavv 64,42,120
E_ 82
Mavv 44,38,120
E_ 83
Mavv 40,38,116
C_ 100,80,81
C_ 101,82,83
Mavv 60,42,116
W_ 0
E_ 84
Mavv 80,30,116
s_nop 0
Tv 38,v4,25600
Tv 40,v4,31744
Mavv 100,34,116
E_ 85
Mavv 104,34,120
s_nop 0
Tv 42,v5,25600
Tv 44,v5,31744
Mavv 84,30,120
E_ 86
Mavv 88,30,124
E_ 87
Mavv 108,34,124
C_ 104,84,85
C_ 105,86,87
Mavv 112,34,128
E_ 88
Mavv 92,30,128
E_ 89
Mavv 96,30,132
E_ 90
Mavv 116,34,132
W_ 0
E_ 91
Mavv 136,38,132
s_nop 0
Tv 30,v2,26624
Tv 32,v2,32768
Mavv 156,42,132
C_ 108,88,89
C_ 109,90,91
Mavv 152,42,128
s_nop 0
Tv 34,v3,26624
Tv 36,v3,32768
Mavv 132,38,128
E_ 92
Mavv 128,38,124
E_ 93
Mavv 148,42,124
E_ 94
Mavv 144,42,120
E_ 95
Mavv 124,38,120
C_ 112,92,93
C_ 113,94,95
Mavv 120,38,116
v_mov_b32 v92, 0x3f803f80
v_mov_b32 v93, 0x3f803f80
Mavv 140,42,116
W_ 0
v_mov_b32 v94, 0x3f803f80
v_mov_b32 v95, 0x3f803f80
Mavv 160,30,116
s_nop 0
Tv 38,v4,26624
Tv 40,v4,32768
Mavv 180,34,116
Mavv 184,34,120
s_nop 0
Tv 42,v5,26624
Tv 44,v5,32768
Mavv 164,30,120
Mavv 168,30,124
Mavv 188,34,124
S_ s65,s61,147456
Mavv 192,34,128
S_ m0,s51,24576
Mavv 172,30,128
s_waitcnt vmcnt(6) lgkmcnt(0)
s_barrier
Mavv 176,30,132
Ln v7,32,s65,0
Mavv 196,34,132
Mavv 216,38,132
Dv 30,%0,6144
Mavv 236,42,132
Dv 34,v1,6144
Mavv 232,42,128
Ln v8,32,s65,3072
S_ m0,s51,25472
Mavv 212,38,128
Mavv 208,38,124
Ln v7,32,s65,128
Mavv 228,42,124
Mavv 224,42,120
Ln v8,32,s65,3200
S_ m0,s51,26368
Mavv 204,38,120
Mavv 200,38,116
Ln v7,32,s65,256
Mavv 220,42,116
.Lstep2_11:
Zvvv 76,92,96
Dv 38,%0,7168
Zvvv 80,92,100
Dv 42,v1,7168
Zvvv 84,92,104
Zvvv 88,92,108
Zvvv 92,92,112
W_ 2
Nvvv 116,30,136,56
Mvvv 116,34,156
Nvvv 120,30,140,60
s_nop 1
v_max3_f32 v10, v76, v80, v84
s_nop 0
v_max3_f32 v10, v10, v88, v92
Mvvv 120,34,160
v_cmp_lt_f32 vcc, 0x42000000, v10
s_cbranch_vccnz .Ltramp2_10
Nvvv 124,30,144,64
A_ v46,v46,v76
A_ v47,v47,v80
Mvvv 124,34,164
A_ v48,v48,v84
A_ v49,v49,v88
Nvvv 128,30,148,68
A_ v50,v50,v92
Mvvv 128,34,168
Ln v8,32,s65,3328
Nvvv 132,30,152,72
Mvvv 132,34,172
W_ 0
Mvvv 132,38,192
Dv 30,%0,8192
Mvvv 132,42,212
Dv 34,v1,8192
Mvvv 128,38,188
Mvvv 128,42,208
Mvvv 124,38,184
Mvvv 124,42,204
Mvvv 120,38,180
Mvvv 120,42,200
Mvvv 116,38,176
Mvvv 116,42,196
W_ 0
Mvvv 116,30,216
Dv 38,%0,0
Mvvv 116,34,236
Dv 42,v1,0
Mvvv 120,30,220
Mvvv 120,34,240
Mvvv 124,30,224
Mvvv 124,34,244
Mvvv 128,30,228
Mvvv 128,34,248
s_nop 0
E_ 116
Mvvv 132,30,232
E_ 117
Mvvv 132,34,252
W_ 0
E_ 118
Nvvv 76,38,136,56
Dv 30,%0,1024
Mvvv 76,42,156
Dv 34,v1,1024
Nvvv 80,38,140,60
E_ 119
Mvvv 80,42,160
C_ 119,118,119
C_ 118,116,117
Nvvv 84,38,144,64
E_ 120
Mvvv 84,42,164
E_ 121
Nvvv 88,38,148,68
E_ 122
Mvvv 88,42,168
E_ 123
Nvvv 92,38,152,72
C_ 123,122,123
C_ 122,120,121
Mvvv 92,42,172
W_ 0
E_ 124
Mvvv 92,30,192
Dv 38,%0,2048
Mvvv 92,34,212
Dv 42,v1,2048
Mvvv 88,30,188
E_ 125
Mvvv 88,34,208
E_ 126
Mvvv 84,30,184
S_ s65,s61,135168
E_ 127
Mvvv 84,34,204
S_ m0,s51,0
C_ 127,126,127
C_ 126,124,125
Mvvv 80,30,180
s_waitcnt vmcnt(6) lgkmcnt(0)
s_barrier
Mvvv 80,34,200
Ln v9,36,s65,0
Mvvv 76,30,176
E_ 128
Mvvv 76,34,196
Ln v9,36,s65,3072
S_ m0,s51,896
Mvvv 76,38,216
Tv 30,v2,12288
Tv 32,v2,18432
Mvvv 76,42,236
Ln v9,36,s65,128
Mvvv 80,38,220
s_nop 0
Tv 34,v3,12288
Tv 36,v3,18432
Mvvv 80,42,240
Ln v9,36,s65,3200
S_ m0,s51,1792
Mvvv 84,38,224
E_ 129
Mvvv 84,42,244
Ln v9,36,s65,256
Mvvv 88,38,228
E_ 130
Mvvv 88,42,248
Ln v9,36,s65,3328
Mvvv 92,38,232
E_ 131
Mvvv 92,42,252
W_ 0
C_ 131,130,131
C_ 130,128,129
Mavv 0,30,96
s_nop 0
Tv 38,v4,12288
Tv 40,v4,18432
Mavv 20,34,96
E_ 132
Mavv 24,34,100
s_nop 0
Tv 42,v5,12288
Tv 44,v5,18432
Mavv 4,30,100
E_ 133
Mavv 8,30,104
E_ 134
Mavv 28,34,104
E_ 135
Mavv 32,34,108
C_ 135,134,135
C_ 134,132,133
Mavv 12,30,108
E_ 76
Mavv 16,30,112
E_ 77
Mavv 36,34,112
W_ 0
E_ 78
Mavv 56,38,112
s_nop 0
Tv 30,v2,13312
Tv 32,v2,19456
Mavv 76,42,112
E_ 79
Mavv 72,42,108
s_nop 0
Tv 34,v3,13312
Tv 36,v3,19456
Mavv 52,38,108
C_ 116,76,77
C_ 117,78,79
Mavv 48,38,104
E_ 80
Mavv 68,42,104
E_ 81
Mavv 64,42,100
E_ 82
Mavv 44,38,100
E_ 83
Mavv 40,38,96
C_ 120,80,81
C_ 121,82,83
Mavv 60,42,96
W_ 0
E_ 84
Mavv 80,30,96
s_nop 0
Tv 38,v4,13312
Tv 40,v4,19456
Mavv 100,34,96
E_ 85
Mavv 104,34,100
s_nop 0
Tv 42,v5,13312
Tv 44,v5,19456
Mavv 84,30,100
E_ 86
Mavv 88,30,104
E_ 87
Mavv 108,34,104
C_ 124,84,85
C_ 125,86,87
Mavv 112,34,108
E_ 88
Mavv 92,30,108
E_ 89
Mavv 96,30,112
E_ 90
Mavv 116,34,112
W_ 0
E_ 91
Mavv 136,38,112
s_nop 0
Tv 30,v2,14336
Tv 32,v2,20480
Mavv 156,42,112
C_ 128,88,89
C_ 129,90,91
Mavv 152,42,108
s_nop 0
Tv 34,v3,14336
Tv 36,v3,20480
Mavv 132,38,108
E_ 92
Mavv 128,38,104
E_ 93
Mavv 148,42,104
E_ 94
Mavv 144,42,100
E_ 95
Mavv 124,38,100
C_ 132,92,93
C_ 133,94,95
Mavv 120,38,96
v_mov_b32 v92, 0x3f803f80
v_mov_b32 v93, 0x3f803f80
Mavv 140,42,96
W_ 0
v_mov_b32 v94, 0x3f803f80
v_mov_b32 v95, 0x3f803f80
Mavv 160,30,96
s_nop 0
Tv 38,v4,14336
Tv 40,v4,20480
Mavv 180,34,96
Mavv 184,34,100
s_nop 0
Tv 42,v5,14336
Tv 44,v5,20480
Mavv 164,30,100
Mavv 168,30,104
Mavv 188,34,104
S_ s65,s61,159744
Mavv 192,34,108
S_ m0,s51,12288
Mavv 172,30,108
s_waitcnt vmcnt(6) lgkmcnt(0)
s_barrier
Mavv 176,30,112
Ln v7,32,s65,0
Mavv 196,34,112
Mavv 216,38,112
Dv 30,%0,30720
Mavv 236,42,112
Dv 34,v1,30720
Mavv 232,42,108
Ln v8,32,s65,3072
S_ m0,s51,13184
Mavv 212,38,108
Mavv 208,38,104
Ln v7,32,s65,128
Mavv 228,42,104
Mavv 224,42,100
Ln v8,32,s65,3200
S_ m0,s51,14080
Mavv 204,38,100
Mavv 200,38,96
Ln v7,32,s65,256
Mavv 220,42,96
.Lstep2_12:
Zvvv 76,92,116
Dv 38,%0,31744
Zvvv 80,92,120
Dv 42,v1,31744
Zvvv 84,92,124
Zvvv 88,92,128
Zvvv 92,92,132
W_ 2
Nvvv 96,30,136,56
Mvvv 96,34,156
Nvvv 100,30,140,60
s_nop 1
v_max3_f32 v10, v76, v80, v84
s_nop 0
v_max3_f32 v10, v10, v88, v92
Mvvv 100,34,160
v_cmp_lt_f32 vcc, 0x42000000, v10
s_cbranch_vccnz .Ltramp2_11
Nvvv 104,30,144,64
A_ v46,v46,v76
A_ v47,v47,v80
Mvvv 104,34,164
A_ v48,v48,v84
A_ v49,v49,v88
Nvvv 108,30,148,68
A_ v50,v50,v92
Mvvv 108,34,168
Ln v8,32,s65,3328
Nvvv 112,30,152,72
R_ v11,a248
Mvvv 112,34,172
W_ 0
v_min_u32 v11, 240, v11
Mvvv 112,38,192
Dv 30,%0,32768
v_lshlrev_b32 v11, 3, v11
Mvvv 112,42,212
Dv 34,v1,32768
Mvvv 108,38,188
buffer_load_dword a254, v11, s[44:47], s66 offen
Mvvv 108,42,208
Mvvv 104,38,184
Mvvv 104,42,204
Mvvv 100,38,180
Mvvv 100,42,200
Mvvv 96,38,176
Mvvv 96,42,196
W_ 0
Mvvv 96,30,216
Dv 38,%0,24576
Mvvv 96,34,236
Dv 42,v1,24576
Mvvv 100,30,220
Mvvv 100,34,240
Mvvv 104,30,224
Mvvv 104,34,244
Mvvv 108,30,228
Mvvv 108,34,248
s_nop 0
E_ 96
Mvvv 112,30,232
E_ 97
Mvvv 112,34,252
W_ 0
E_ 98
Nvvv 76,38,136,56
Dv 30,%0,25600
Mvvv 76,42,156
Dv 34,v1,25600
Nvvv 80,38,140,60
E_ 99
Mvvv 80,42,160
C_ 99,98,99
C_ 98,96,97
Nvvv 84,38,144,64
E_ 100
Mvvv 84,42,164
E_ 101
Nvvv 88,38,148,68
E_ 102
Mvvv 88,42,168
E_ 103
Nvvv 92,38,152,72
C_ 103,102,103
C_ 102,100,101
Mvvv 92,42,172
W_ 0
E_ 104
Mvvv 92,30,192
Dv 38,%0,26624
Mvvv 92,34,212
Dv 42,v1,26624
Mvvv 88,30,188
E_ 105
Mvvv 88,34,208
E_ 106
Mvvv 84,30,184
S_ s65,s61,147456
E_ 107
Mvvv 84,34,204
S_ m0,s51,24576
C_ 107,106,107
C_ 106,104,105
Mvvv 80,30,180
s_waitcnt vmcnt(7) lgkmcnt(0)
s_barrier
Mvvv 80,34,200
Ln v9,36,s65,0
Mvvv 76,30,176
E_ 108
Mvvv 76,34,196
Ln v9,36,s65,3072
S_ m0,s51,25472
Mvvv 76,38,216
Tv 30,v2,0
Tv 32,v2,6144
Mvvv 76,42,236
Ln v9,36,s65,128
Mvvv 80,38,220
s_nop 0
Tv 34,v3,0
Tv 36,v3,6144
Mvvv 80,42,240
Ln v9,36,s65,3200
S_ m0,s51,26368
Mvvv 84,38,224
E_ 109
Mvvv 84,42,244
Ln v9,36,s65,256
Mvvv 88,38,228
E_ 110
Mvvv 88,42,248
Ln v9,36,s65,3328
Mvvv 92,38,232
E_ 111
Mvvv 92,42,252
W_ 0
C_ 111,110,111
C_ 110,108,109
Mavv 0,30,116
s_nop 0
Tv 38,v4,0
Tv 40,v4,6144
Mavv 20,34,116
E_ 112
Mavv 24,34,120
s_nop 0
Tv 42,v5,0
Tv 44,v5,6144
Mavv 4,30,120
)A"
R"A(E_ 113
Mavv 8,30,124
E_ 114
Mavv 28,34,124
E_ 115
Mavv 32,34,128
C_ 115,114,115
C_ 114,112,113
Mavv 12,30,128
E_ 76
Mavv 16,30,132
E_ 77
Mavv 36,34,132
W_ 0
E_ 78
Mavv 56,38,132
s_nop 0
Tv 30,v2,1024
Tv 32,v2,7168
Mavv 76,42,132
E_ 79
Mavv 72,42,128
s_nop 0
Tv 34,v3,1024
Tv 36,v3,7168
Mavv 52,38,128
C_ 96,76,77
C_ 97,78,79
Mavv 48,38,124
E_ 80
Mavv 68,42,124
E_ 81
Mavv 64,42,120
E_ 82
Mavv 44,38,120
E_ 83
Mavv 40,38,116
C_ 100,80,81
C_ 101,82,83
Mavv 60,42,116
W_ 0
E_ 84
Mavv 80,30,116
s_nop 0
Tv 38,v4,1024
Tv 40,v4,7168
Mavv 100,34,116
E_ 85
Mavv 104,34,120
s_nop 0
Tv 42,v5,1024
Tv 44,v5,7168
Mavv 84,30,120
E_ 86
Mavv 88,30,124
E_ 87
Mavv 108,34,124
C_ 104,84,85
C_ 105,86,87
Mavv 112,34,128
E_ 88
Mavv 92,30,128
E_ 89
Mavv 96,30,132
E_ 90
Mavv 116,34,132
W_ 0
E_ 91
Mavv 136,38,132
s_nop 0
Tv 30,v2,2048
Tv 32,v2,8192
Mavv 156,42,132
C_ 108,88,89
C_ 109,90,91
Mavv 152,42,128
s_nop 0
Tv 34,v3,2048
Tv 36,v3,8192
Mavv 132,38,128
E_ 92
Mavv 128,38,124
E_ 93
Mavv 148,42,124
E_ 94
Mavv 144,42,120
E_ 95
Mavv 124,38,120
C_ 112,92,93
C_ 113,94,95
Mavv 120,38,116
v_mov_b32 v92, 0x3f803f80
v_mov_b32 v93, 0x3f803f80
Mavv 140,42,116
W_ 0
v_mov_b32 v94, 0x3f803f80
v_mov_b32 v95, 0x3f803f80
Mavv 160,30,116
s_nop 0
Tv 38,v4,2048
Tv 40,v4,8192
Mavv 180,34,116
Mavv 184,34,120
s_nop 0
Tv 42,v5,2048
Tv 44,v5,8192
Mavv 164,30,120
Mavv 168,30,124
Mavv 188,34,124
S_ s65,s61,172032
Mavv 192,34,128
S_ m0,s51,0
Mavv 172,30,128
s_waitcnt vmcnt(7) lgkmcnt(0)
s_barrier
Mavv 176,30,132
Ln v7,32,s65,0
Mavv 196,34,132
Mavv 216,38,132
Dv 30,%0,18432
Mavv 236,42,132
Dv 34,v1,18432
Mavv 232,42,128
Ln v8,32,s65,3072
S_ m0,s51,896
Mavv 212,38,128
Mavv 208,38,124
Ln v7,32,s65,128
Mavv 228,42,124
Mavv 224,42,120
Ln v8,32,s65,3200
S_ m0,s51,1792
Mavv 204,38,120
Mavv 200,38,116
Ln v7,32,s65,256
Mavv 220,42,116
.Lstep2_13:
Zvvv 76,92,96
Dv 38,%0,19456
Zvvv 80,92,100
Dv 42,v1,19456
Zvvv 84,92,104
Zvvv 88,92,108
Zvvv 92,92,112
W_ 2
Nvvv 116,30,136,56
Mvvv 116,34,156
Nvvv 120,30,140,60
s_nop 1
v_max3_f32 v10, v76, v80, v84
s_nop 0
v_max3_f32 v10, v10, v88, v92
Mvvv 120,34,160
v_cmp_lt_f32 vcc, 0x42000000, v10
s_cbranch_vccnz .Ltramp2_12
Nvvv 124,30,144,64
A_ v46,v46,v76
A_ v47,v47,v80
Mvvv 124,34,164
A_ v48,v48,v84
A_ v49,v49,v88
Nvvv 128,30,148,68
A_ v50,v50,v92
Mvvv 128,34,168
Ln v8,32,s65,3328
Nvvv 132,30,152,72
Mvvv 132,34,172
W_ 0
Mvvv 132,38,192
Dv 30,%0,20480
Mvvv 132,42,212
Dv 34,v1,20480
Mvvv 128,38,188
Mvvv 128,42,208
Mvvv 124,38,184
Mvvv 124,42,204
Mvvv 120,38,180
Mvvv 120,42,200
Mvvv 116,38,176
Mvvv 116,42,196
W_ 0
Mvvv 116,30,216
Dv 38,%0,12288
Mvvv 116,34,236
Dv 42,v1,12288
Mvvv 120,30,220
Mvvv 120,34,240
Mvvv 124,30,224
Mvvv 124,34,244
Mvvv 128,30,228
Mvvv 128,34,248
s_nop 0
E_ 116
Mvvv 132,30,232
E_ 117
Mvvv 132,34,252
W_ 0
E_ 118
Nvvv 76,38,136,56
Dv 30,%0,13312
Mvvv 76,42,156
Dv 34,v1,13312
Nvvv 80,38,140,60
E_ 119
Mvvv 80,42,160
C_ 119,118,119
C_ 118,116,117
Nvvv 84,38,144,64
E_ 120
Mvvv 84,42,164
E_ 121
Nvvv 88,38,148,68
E_ 122
Mvvv 88,42,168
E_ 123
Nvvv 92,38,152,72
C_ 123,122,123
C_ 122,120,121
Mvvv 92,42,172
W_ 0
E_ 124
Mvvv 92,30,192
Dv 38,%0,14336
Mvvv 92,34,212
Dv 42,v1,14336
Mvvv 88,30,188
E_ 125
Mvvv 88,34,208
E_ 126
Mvvv 84,30,184
S_ s65,s61,159744
E_ 127
Mvvv 84,34,204
S_ m0,s51,12288
C_ 127,126,127
C_ 126,124,125
Mvvv 80,30,180
s_waitcnt vmcnt(6) lgkmcnt(0)
s_barrier
Mvvv 80,34,200
Ln v9,36,s65,0
Mvvv 76,30,176
E_ 128
Mvvv 76,34,196
Ln v9,36,s65,3072
S_ m0,s51,13184
Mvvv 76,38,216
Tv 30,v2,24576
Tv 32,v2,30720
Mvvv 76,42,236
Ln v9,36,s65,128
Mvvv 80,38,220
s_nop 0
Tv 34,v3,24576
Tv 36,v3,30720
Mvvv 80,42,240
Ln v9,36,s65,3200
S_ m0,s51,14080
Mvvv 84,38,224
E_ 129
Mvvv 84,42,244
Ln v9,36,s65,256
Mvvv 88,38,228
E_ 130
Mvvv 88,42,248
Ln v9,36,s65,3328
Mvvv 92,38,232
E_ 131
Mvvv 92,42,252
W_ 0
C_ 131,130,131
C_ 130,128,129
Mavv 0,30,96
s_nop 0
Tv 38,v4,24576
Tv 40,v4,30720
Mavv 20,34,96
E_ 132
Mavv 24,34,100
s_nop 0
Tv 42,v5,24576
Tv 44,v5,30720
Mavv 4,30,100
E_ 133
Mavv 8,30,104
E_ 134
Mavv 28,34,104
E_ 135
Mavv 32,34,108
C_ 135,134,135
C_ 134,132,133
Mavv 12,30,108
E_ 76
Mavv 16,30,112
E_ 77
Mavv 36,34,112
W_ 0
E_ 78
Mavv 56,38,112
s_nop 0
Tv 30,v2,25600
Tv 32,v2,31744
Mavv 76,42,112
E_ 79
Mavv 72,42,108
s_nop 0
Tv 34,v3,25600
Tv 36,v3,31744
Mavv 52,38,108
C_ 116,76,77
C_ 117,78,79
Mavv 48,38,104
E_ 80
Mavv 68,42,104
E_ 81
Mavv 64,42,100
E_ 82
Mavv 44,38,100
E_ 83
Mavv 40,38,96
C_ 120,80,81
C_ 121,82,83
Mavv 60,42,96
W_ 0
E_ 84
Mavv 80,30,96
s_nop 0
Tv 38,v4,25600
Tv 40,v4,31744
Mavv 100,34,96
E_ 85
Mavv 104,34,100
s_nop 0
Tv 42,v5,25600
Tv 44,v5,31744
Mavv 84,30,100
E_ 86
Mavv 88,30,104
E_ 87
Mavv 108,34,104
C_ 124,84,85
C_ 125,86,87
Mavv 112,34,108
E_ 88
Mavv 92,30,108
E_ 89
Mavv 96,30,112
E_ 90
Mavv 116,34,112
W_ 0
E_ 91
Mavv 136,38,112
s_nop 0
Tv 30,v2,26624
Tv 32,v2,32768
Mavv 156,42,112
C_ 128,88,89
C_ 129,90,91
Mavv 152,42,108
s_nop 0
Tv 34,v3,26624
Tv 36,v3,32768
Mavv 132,38,108
E_ 92
Mavv 128,38,104
E_ 93
Mavv 148,42,104
E_ 94
Mavv 144,42,100
E_ 95
Mavv 124,38,100
C_ 132,92,93
C_ 133,94,95
Mavv 120,38,96
v_mov_b32 v92, 0x3f803f80
v_mov_b32 v93, 0x3f803f80
Mavv 140,42,96
W_ 0
v_mov_b32 v94, 0x3f803f80
v_mov_b32 v95, 0x3f803f80
Mavv 160,30,96
s_nop 0
Tv 38,v4,26624
Tv 40,v4,32768
Mavv 180,34,96
Mavv 184,34,100
s_nop 0
Tv 42,v5,26624
Tv 44,v5,32768
Mavv 164,30,100
Mavv 168,30,104
Mavv 188,34,104
S_ s65,s61,184320
Mavv 192,34,108
S_ m0,s51,24576
Mavv 172,30,108
s_waitcnt vmcnt(6) lgkmcnt(0)
s_barrier
Mavv 176,30,112
Ln v7,32,s65,0
Mavv 196,34,112
Mavv 216,38,112
Dv 30,%0,6144
Mavv 236,42,112
Dv 34,v1,6144
Mavv 232,42,108
Ln v8,32,s65,3072
S_ m0,s51,25472
Mavv 212,38,108
Mavv 208,38,104
Ln v7,32,s65,128
Mavv 228,42,104
Mavv 224,42,100
Ln v8,32,s65,3200
S_ m0,s51,26368
Mavv 204,38,100
Mavv 200,38,96
Ln v7,32,s65,256
Mavv 220,42,96
.Lstep2_14:
Zvvv 76,92,116
Dv 38,%0,7168
Zvvv 80,92,120
Dv 42,v1,7168
Zvvv 84,92,124
Zvvv 88,92,128
Zvvv 92,92,132
W_ 2
Nvvv 96,30,136,56
Mvvv 96,34,156
Nvvv 100,30,140,60
s_nop 1
v_max3_f32 v10, v76, v80, v84
s_nop 0
v_max3_f32 v10, v10, v88, v92
Mvvv 100,34,160
v_cmp_lt_f32 vcc, 0x42000000, v10
s_cbranch_vccnz .Ltramp2_13
Nvvv 104,30,144,64
A_ v46,v46,v76
A_ v47,v47,v80
Mvvv 104,34,164
A_ v48,v48,v84
A_ v49,v49,v88
Nvvv 108,30,148,68
A_ v50,v50,v92
Mvvv 108,34,168
Ln v8,32,s65,3328
Nvvv 112,30,152,72
Mvvv 112,34,172
W_ 0
Mvvv 112,38,192
Dv 30,%0,8192
Mvvv 112,42,212
Dv 34,v1,8192
Mvvv 108,38,188
Mvvv 108,42,208
Mvvv 104,38,184
Mvvv 104,42,204
Mvvv 100,38,180
Mvvv 100,42,200
Mvvv 96,38,176
Mvvv 96,42,196
W_ 0
Mvvv 96,30,216
Dv 38,%0,0
Mvvv 96,34,236
Dv 42,v1,0
Mvvv 100,30,220
Mvvv 100,34,240
Mvvv 104,30,224
Mvvv 104,34,244
Mvvv 108,30,228
Mvvv 108,34,248
s_nop 0
E_ 96
Mvvv 112,30,232
E_ 97
Mvvv 112,34,252
W_ 0
E_ 98
Nvvv 76,38,136,56
Dv 30,%0,1024
Mvvv 76,42,156
Dv 34,v1,1024
Nvvv 80,38,140,60
E_ 99
Mvvv 80,42,160
C_ 99,98,99
C_ 98,96,97
Nvvv 84,38,144,64
E_ 100
Mvvv 84,42,164
E_ 101
Nvvv 88,38,148,68
E_ 102
Mvvv 88,42,168
E_ 103
Nvvv 92,38,152,72
C_ 103,102,103
C_ 102,100,101
Mvvv 92,42,172
W_ 0
E_ 104
Mvvv 92,30,192
Dv 38,%0,2048
Mvvv 92,34,212
Dv 42,v1,2048
Mvvv 88,30,188
E_ 105
Mvvv 88,34,208
E_ 106
Mvvv 84,30,184
S_ s65,s61,172032
E_ 107
Mvvv 84,34,204
S_ m0,s51,0
C_ 107,106,107
C_ 106,104,105
Mvvv 80,30,180
s_waitcnt vmcnt(6) lgkmcnt(0)
s_barrier
Mvvv 80,34,200
Ln v9,36,s65,0
Mvvv 76,30,176
E_ 108
Mvvv 76,34,196
Ln v9,36,s65,3072
S_ m0,s51,896
Mvvv 76,38,216
Tv 30,v2,12288
Tv 32,v2,18432
Mvvv 76,42,236
Ln v9,36,s65,128
Mvvv 80,38,220
s_nop 0
Tv 34,v3,12288
Tv 36,v3,18432
Mvvv 80,42,240
Ln v9,36,s65,3200
S_ m0,s51,1792
Mvvv 84,38,224
E_ 109
Mvvv 84,42,244
Ln v9,36,s65,256
Mvvv 88,38,228
E_ 110
Mvvv 88,42,248
Ln v9,36,s65,3328
Mvvv 92,38,232
E_ 111
Mvvv 92,42,252
W_ 0
C_ 111,110,111
C_ 110,108,109
Mavv 0,30,116
s_nop 0
Tv 38,v4,12288
Tv 40,v4,18432
Mavv 20,34,116
E_ 112
Mavv 24,34,120
s_nop 0
Tv 42,v5,12288
Tv 44,v5,18432
Mavv 4,30,120
E_ 113
Mavv 8,30,124
E_ 114
Mavv 28,34,124
E_ 115
Mavv 32,34,128
C_ 115,114,115
C_ 114,112,113
Mavv 12,30,128
E_ 76
Mavv 16,30,132
E_ 77
Mavv 36,34,132
W_ 0
E_ 78
Mavv 56,38,132
s_nop 0
Tv 30,v2,13312
Tv 32,v2,19456
Mavv 76,42,132
E_ 79
Mavv 72,42,128
s_nop 0
Tv 34,v3,13312
Tv 36,v3,19456
Mavv 52,38,128
C_ 96,76,77
C_ 97,78,79
Mavv 48,38,124
E_ 80
Mavv 68,42,124
E_ 81
Mavv 64,42,120
E_ 82
Mavv 44,38,120
E_ 83
Mavv 40,38,116
C_ 100,80,81
C_ 101,82,83
Mavv 60,42,116
W_ 0
E_ 84
Mavv 80,30,116
s_nop 0
Tv 38,v4,13312
Tv 40,v4,19456
Mavv 100,34,116
E_ 85
Mavv 104,34,120
s_nop 0
Tv 42,v5,13312
Tv 44,v5,19456
Mavv 84,30,120
E_ 86
Mavv 88,30,124
E_ 87
Mavv 108,34,124
C_ 104,84,85
C_ 105,86,87
Mavv 112,34,128
E_ 88
Mavv 92,30,128
E_ 89
Mavv 96,30,132
E_ 90
Mavv 116,34,132
W_ 0
E_ 91
Mavv 136,38,132
s_nop 0
Tv 30,v2,14336
Tv 32,v2,20480
Mavv 156,42,132
C_ 108,88,89
C_ 109,90,91
Mavv 152,42,128
s_nop 0
Tv 34,v3,14336
Tv 36,v3,20480
Mavv 132,38,128
E_ 92
Mavv 128,38,124
E_ 93
Mavv 148,42,124
E_ 94
Mavv 144,42,120
E_ 95
Mavv 124,38,120
C_ 112,92,93
C_ 113,94,95
Mavv 120,38,116
v_mov_b32 v92, 0x3f803f80
v_mov_b32 v93, 0x3f803f80
Mavv 140,42,116
W_ 0
v_mov_b32 v94, 0x3f803f80
v_mov_b32 v95, 0x3f803f80
Mavv 160,30,116
s_nop 0
Tv 38,v4,14336
Tv 40,v4,20480
Mavv 180,34,116
Mavv 184,34,120
s_nop 0
Tv 42,v5,14336
Tv 44,v5,20480
Mavv 164,30,120
Mavv 168,30,124
Mavv 188,34,124
S_ s65,s61,184320
Mavv 192,34,128
S_ m0,s51,12288
Mavv 172,30,128
s_waitcnt vmcnt(6) lgkmcnt(0)
s_barrier
Mavv 176,30,132
Ln v9,36,s65,0
Mavv 196,34,132
Mavv 216,38,132
Dv 30,%0,30720
Mavv 236,42,132
Dv 34,v1,30720
Mavv 232,42,128
Ln v9,36,s65,3072
S_ m0,s51,13184
Mavv 212,38,128
Mavv 208,38,124
Ln v9,36,s65,128
Mavv 228,42,124
Mavv 224,42,120
Ln v9,36,s65,3200
S_ m0,s51,14080
Mavv 204,38,120
Mavv 200,38,116
Ln v9,36,s65,256
Mavv 220,42,116
.Lstep2_15:
Zvvv 76,92,96
Dv 38,%0,31744
Zvvv 80,92,100
Dv 42,v1,31744
Zvvv 84,92,104
Zvvv 88,92,108
Zvvv 92,92,112
W_ 2
Nvvv 116,30,136,56
Mvvv 116,34,156
Nvvv 120,30,140,60
s_nop 1
v_max3_f32 v10, v76, v80, v84
s_nop 0
v_max3_f32 v10, v10, v88, v92
Mvvv 120,34,160
v_cmp_lt_f32 vcc, 0x42000000, v10
s_cbranch_vccnz .Ltramp2_14
Nvvv 124,30,144,64
A_ v46,v46,v76
A_ v47,v47,v80
Mvvv 124,34,164
A_ v48,v48,v84
A_ v49,v49,v88
Nvvv 128,30,148,68
A_ v50,v50,v92
Mvvv 128,34,168
Ln v9,36,s65,3328
Nvvv 132,30,152,72
Mvvv 132,34,172
W_ 0
Mvvv 132,38,192
Dv 30,%0,32768
Mvvv 132,42,212
Dv 34,v1,32768
Mvvv 128,38,188
Mvvv 128,42,208
Mvvv 124,38,184
Mvvv 124,42,204
Mvvv 120,38,180
Mvvv 120,42,200
Mvvv 116,38,176
Mvvv 116,42,196
W_ 0
Mvvv 116,30,216
Dv 38,%0,24576
Mvvv 116,34,236
Dv 42,v1,24576
Mvvv 120,30,220
Mvvv 120,34,240
Mvvv 124,30,224
Mvvv 124,34,244
Mvvv 128,30,228
Mvvv 128,34,248
s_nop 0
E_ 116
Mvvv 132,30,232
E_ 117
Mvvv 132,34,252
W_ 0
E_ 118
Nvvv 76,38,136,56
Dv 30,%0,25600
Mvvv 76,42,156
Dv 34,v1,25600
Nvvv 80,38,140,60
E_ 119
Mvvv 80,42,160
C_ 119,118,119
C_ 118,116,117
Nvvv 84,38,144,64
E_ 120
Mvvv 84,42,164
E_ 121
Nvvv 88,38,148,68
E_ 122
Mvvv 88,42,168
E_ 123
Nvvv 92,38,152,72
C_ 123,122,123
C_ 122,120,121
Mvvv 92,42,172
W_ 0
E_ 124
Mvvv 92,30,192
Dv 38,%0,26624
Mvvv 92,34,212
Dv 42,v1,26624
Mvvv 88,30,188
E_ 125
Mvvv 88,34,208
E_ 126
Mvvv 84,30,184
S_ s65,s61,393216
E_ 127
Mvvv 84,34,204
S_ m0,s51,24576
C_ 127,126,127
C_ 126,124,125
Mvvv 80,30,180
s_waitcnt vmcnt(6) lgkmcnt(0)
s_barrier
Mvvv 80,34,200
L_ v9,36,s65,0
Mvvv 76,30,176
E_ 128
Mvvv 76,34,196
L_ v9,36,s65,3072
S_ m0,s51,25472
Mvvv 76,38,216
Tv 30,v2,0
Tv 32,v2,6144
Mvvv 76,42,236
L_ v9,36,s65,128
Mvvv 80,38,220
s_nop 0
Tv 34,v3,0
Tv 36,v3,6144
Mvvv 80,42,240
L_ v9,36,s65,3200
S_ m0,s51,26368
Mvvv 84,38,224
E_ 129
Mvvv 84,42,244
L_ v9,36,s65,256
Mvvv 88,38,228
E_ 130
Mvvv 88,42,248
L_ v9,36,s65,3328
Mvvv 92,38,232
E_ 131
Mvvv 92,42,252
W_ 0
C_ 131,130,131
C_ 130,128,129
Mavv 0,30,96
s_nop 0
Tv 38,v4,0
Tv 40,v4,6144
Mavv 20,34,96
E_ 132
Mavv 24,34,100
s_nop 0
Tv 42,v5,0
Tv 44,v5,6144
Mavv 4,30,100
E_ 133
Mavv 8,30,104
E_ 134
Mavv 28,34,104
E_ 135
Mavv 32,34,108
C_ 135,134,135
C_ 134,132,133
Mavv 12,30,108
E_ 76
Mavv 16,30,112
E_ 77
Mavv 36,34,112
W_ 0
E_ 78
Mavv 56,38,112
s_nop 0
Tv 30,v2,1024
Tv 32,v2,7168
Mavv 76,42,112
E_ 79
Mavv 72,42,108
s_nop 0
Tv 34,v3,1024
Tv 36,v3,7168
Mavv 52,38,108
C_ 116,76,77
C_ 117,78,79
Mavv 48,38,104
E_ 80
Mavv 68,42,104
E_ 81
Mavv 64,42,100
E_ 82
Mavv 44,38,100
E_ 83
Mavv 40,38,96
C_ 120,80,81
C_ 121,82,83
Mavv 60,42,96
W_ 0
E_ 84
Mavv 80,30,96
s_nop 0
Tv 38,v4,1024
Tv 40,v4,7168
Mavv 100,34,96
E_ 85
Mavv 104,34,100
s_nop 0
Tv 42,v5,1024
Tv 44,v5,7168
Mavv 84,30,100
E_ 86
Mavv 88,30,104
E_ 87
Mavv 108,34,104
C_ 124,84,85
C_ 125,86,87
Mavv 112,34,108
E_ 88
Mavv 92,30,108
E_ 89
Mavv 96,30,112
E_ 90
Mavv 116,34,112
W_ 0
E_ 91
Mavv 136,38,112
s_nop 0
Tv 30,v2,2048
Tv 32,v2,8192
Mavv 156,42,112
C_ 128,88,89
C_ 129,90,91
Mavv 152,42,108
s_nop 0
Tv 34,v3,2048
Tv 36,v3,8192
Mavv 132,38,108
E_ 92
Mavv 128,38,104
E_ 93
Mavv 148,42,104
E_ 94
Mavv 144,42,100
E_ 95
Mavv 124,38,100
C_ 132,92,93
C_ 133,94,95
Mavv 120,38,96
v_mov_b32 v92, 0x3f803f80
v_mov_b32 v93, 0x3f803f80
Mavv 140,42,96
W_ 0
v_mov_b32 v94, 0x3f803f80
v_mov_b32 v95, 0x3f803f80
Mavv 160,30,96
s_nop 0
Tv 38,v4,2048
Tv 40,v4,8192
Mavv 180,34,96
Mavv 184,34,100
s_nop 0
Tv 42,v5,2048
Tv 44,v5,8192
Mavv 164,30,100
Mavv 168,30,104
Mavv 188,34,104
S_ s65,s61,393216
Mavv 192,34,108
S_ m0,s51,0
Mavv 172,30,108
s_waitcnt vmcnt(6) lgkmcnt(0)
s_barrier
Mavv 176,30,112
Ln v7,32,s65,0
Mavv 196,34,112
Mavv 216,38,112
s_nop 0
Tv 30,v2,12288
Tv 32,v2,18432
Zvvv 76,92,116
Ln v8,32,s65,3072
S_ m0,s51,896
Zvvv 80,92,120
s_nop 0
Tv 34,v3,12288
Tv 36,v3,18432
Zvvv 84,92,124
Ln v7,32,s65,128
Zvvv 88,92,128
Zvvv 92,92,132
Ln v8,32,s65,3200
S_ m0,s51,1792
Mavv 236,42,112
Mavv 232,42,108
Ln v7,32,s65,256
Mavv 212,38,108
Mavv 208,38,104
v_max3_f32 v10, v76, v80, v84
Mavv 228,42,104
v_max3_f32 v10, v10, v88, v92
v_cmp_lt_f32 vcc, 0x42000000, v10
Mavv 224,42,100
Ln v8,32,s65,3328
Mavv 204,38,100
Mavv 200,38,96
Mavv 220,42,96
s_cbranch_vccnz .Ltramp2_15
.Ljoin2:
W_ 2
Mavv 0,30,116
s_mov_b32 s58, 1
Tv 38,v4,12288
Tv 40,v4,18432
W_ 2
Mavv 20,34,116
R_ v11,a248
R_ v13,a248
Mavv 24,34,120
S_ s53,s51,36864
Tv 42,v5,12288
Tv 44,v5,18432
Mavv 4,30,120
v_add_u32 v12, s53, v13
s_mul_i32 s53, s4, 10240
Mavv 8,30,124
v_add_u32 v13, s53, v13
A_ v46,v46,v76
Mavv 28,34,124
A_ v47,v47,v80
Mavv 32,34,128
A_ v48,v48,v84
A_ v49,v49,v88
Mavv 12,30,128
A_ v50,v50,v92
Mavv 16,30,132
v_rcp_f32 v46, v46
Mavv 36,34,132
W_ 0
v_rcp_f32 v47, v47
Mavv 56,38,132
s_nop 0
Tv 30,v2,13312
Tv 32,v2,19456
Mavv 76,42,132
v_rcp_f32 v48, v48
Mavv 72,42,128
s_nop 0
Tv 34,v3,13312
Tv 36,v3,19456
Mavv 52,38,128
v_rcp_f32 v49, v49
Mavv 48,38,124
v_rcp_f32 v50, v50
Mavv 68,42,124
s_mov_b32 s56, 12288
v_mov_b32 v176, v20
v_mov_b32 v177, v21
Mavv 64,42,120
Dv 136,v13,0
v_mov_b32 v178, v22
Mavv 44,38,120
Dv 140,v13,1024
v_mov_b32 v179, v23
Mavv 40,38,116
Dv 144,v13,2048
v_mov_b32 v180, v24
Mavv 60,42,116
W_ 3
Dv 148,v13,3072
v_mov_b32 v181, v25
Mavv 80,30,116
s_nop 0
Tv 38,v4,13312
Tv 40,v4,19456
Mavv 100,34,116
Dv 152,v13,4096
v_mov_b32 v182, v26
Mavv 104,34,120
s_nop 0
Tv 42,v5,13312
Tv 44,v5,19456
Mavv 84,30,120
Dv 156,v13,5120
v_mov_b32 v183, v27
Mavv 88,30,124
Dv 160,v13,6144
v_mov_b32 v184, v52
Mavv 108,34,124
Dv 164,v13,7168
v_mov_b32 v185, v53
Mavv 112,34,128
Dv 168,v13,8192
v_mov_b32 v186, v54
Mavv 92,30,128
Dv 172,v13,9216
v_mov_b32 v187, v55
Mavv 96,30,132
Dv 208,v12,0
R_ v188,a240
Mavv 116,34,132
W_ 6
Dv 212,v12,1024
R_ v189,a241
Mavv 136,38,132
s_nop 0
Tv 30,v2,14336
Tv 32,v2,20480
Mavv 156,42,132
Dv 216,v12,2048
R_ v190,a242
Mavv 152,42,128
s_nop 0
Tv 34,v3,14336
Tv 36,v3,20480
Mavv 132,38,128
Dv 220,v12,3072
R_ v191,a243
Mavv 128,38,124
Dv 224,v12,4096
R_ v192,a244
Mavv 148,42,124
Dv 228,v12,5120
R_ v193,a245
Mavv 144,42,120
Dv 232,v12,12288
R_ v194,a246
Mavv 124,38,120
Dv 236,v12,13312
R_ v195,a247
Mavv 120,38,116
Dv 240,v12,14336
v_mov_b32 v196, v16
Mavv 140,42,116
W_ 6
Dv 244,v12,15360
v_mov_b32 v197, v17
Mavv 160,30,116
s_nop 0
Tv 38,v4,14336
Tv 40,v4,20480
Mavv 180,34,116
Dv 248,v12,16384
v_mov_b32 v198, v18
Mavv 184,34,120
s_nop 0
Tv 42,v5,14336
Tv 44,v5,20480
Mavv 164,30,120
Dv 252,v12,17408
v_mov_b32 v199, v19
Mavv 168,30,124
v_mov_b32 v200, v28
v_mov_b32 v201, v29
Mavv 188,34,124
v_mov_b32 v202, v51
v_mov_b32 v203, v6
Mavv 192,34,128
v_mov_b32 v204, v14
v_mov_b32 v205, v15
Mavv 172,30,128
S_ s65,s61,405504
R_ v206,a249
R_ v207,a250
Mavv 176,30,132
S_ m0,s51,12288
R_ v96,a0
R_ v97,a1
Mavv 196,34,132
s_waitcnt vmcnt(63) lgkmcnt(0)
s_barrier
R_ v98,a2
Mavv 216,38,132
Ln v7,32,s65,0
Mavv 236,42,132
R_ v99,a3
Mavv 232,42,128
Ln v8,32,s65,3072
S_ m0,s51,13184
Mavv 212,38,128
R_ v100,a20
R_ v101,a21
Mavv 208,38,124
Ln v7,32,s65,128
Mavv 228,42,124
R_ v102,a22
R_ v103,a23
Mavv 224,42,120
Ln v8,32,s65,3200
S_ m0,s51,14080
Mavv 204,38,120
U_ v96,v96,v46
U_ v97,v97,v46
Mavv 200,38,116
Ln v7,32,s65,256
Mavv 220,42,116
U_ v98,v98,v46
U_ v99,v99,v46
U_ v100,v100,v46
U_ v101,v101,v46
U_ v102,v102,v46
U_ v103,v103,v46
C_ 76,96,97
C_ 77,98,99
C_ 78,100,101
C_ 79,102,103
ds_write_b128 v13, v[76:79] offset:0
Ln v8,32,s65,3328
S_ s61,s61,393216
.p2align 3
R_ v104,a4
R_ v105,a5
R_ v106,a6
R_ v107,a7
R_ v108,a24
R_ v109,a25
R_ v110,a26
R_ v111,a27
v_pk_mul_f32 v[104:105], v[104:105], v[46:47] op_sel:[0,1]
v_pk_mul_f32 v[106:107], v[106:107], v[46:47] op_sel:[0,1]
v_pk_mul_f32 v[108:109], v[108:109], v[46:47] op_sel:[0,1]
v_pk_mul_f32 v[110:111], v[110:111], v[46:47] op_sel:[0,1]
C_ 80,104,105
C_ 81,106,107
C_ 82,108,109
C_ 83,110,111
ds_write_b128 v13, v[80:83] offset:1024
.p2align 3
R_ v96,a8
R_ v97,a9
R_ v98,a10
R_ v99,a11
R_ v100,a28
R_ v101,a29
R_ v102,a30
R_ v103,a31
v_pk_mul_f32 v[96:97], v[96:97], v[48:49] op_sel_hi:[1,0]
v_pk_mul_f32 v[98:99], v[98:99], v[48:49] op_sel_hi:[1,0]
v_pk_mul_f32 v[100:101], v[100:101], v[48:49] op_sel_hi:[1,0]
v_pk_mul_f32 v[102:103], v[102:103], v[48:49] op_sel_hi:[1,0]
C_ 84,96,97
C_ 85,98,99
C_ 86,100,101
C_ 87,102,103
ds_write_b128 v13, v[84:87] offset:2048
.p2align 3
R_ v104,a12
R_ v105,a13
R_ v106,a14
R_ v107,a15
R_ v108,a32
R_ v109,a33
R_ v110,a34
R_ v111,a35
v_pk_mul_f32 v[104:105], v[104:105], v[48:49] op_sel:[0,1]
v_pk_mul_f32 v[106:107], v[106:107], v[48:49] op_sel:[0,1]
v_pk_mul_f32 v[108:109], v[108:109], v[48:49] op_sel:[0,1]
v_pk_mul_f32 v[110:111], v[110:111], v[48:49] op_sel:[0,1]
C_ 88,104,105
C_ 89,106,107
C_ 90,108,109
C_ 91,110,111
ds_write_b128 v13, v[88:91] offset:3072
.p2align 3
R_ v96,a16
R_ v97,a17
R_ v98,a18
R_ v99,a19
R_ v100,a36
R_ v101,a37
R_ v102,a38
R_ v103,a39
v_pk_mul_f32 v[96:97], v[96:97], v[50:51] op_sel_hi:[1,0]
v_pk_mul_f32 v[98:99], v[98:99], v[50:51] op_sel_hi:[1,0]
v_pk_mul_f32 v[100:101], v[100:101], v[50:51] op_sel_hi:[1,0]
v_pk_mul_f32 v[102:103], v[102:103], v[50:51] op_sel_hi:[1,0]
C_ 92,96,97
C_ 93,98,99
C_ 94,100,101
C_ 95,102,103
ds_write_b128 v13, v[92:95] offset:4096
.p2align 3
R_ v104,a40
R_ v105,a41
R_ v106,a42
R_ v107,a43
R_ v108,a60
R_ v109,a61
R_ v110,a62
R_ v111,a63
v_pk_mul_f32 v[104:105], v[104:105], v[46:47] op_sel_hi:[1,0]
v_pk_mul_f32 v[106:107], v[106:107], v[46:47] op_sel_hi:[1,0]
v_pk_mul_f32 v[108:109], v[108:109], v[46:47] op_sel_hi:[1,0]
v_pk_mul_f32 v[110:111], v[110:111], v[46:47] op_sel_hi:[1,0]
C_ 76,104,105
C_ 77,106,107
C_ 78,108,109
C_ 79,110,111
ds_write_b128 v13, v[76:79] offset:5120
.p2align 3
R_ v96,a44
R_ v97,a45
R_ v98,a46
R_ v99,a47
R_ v100,a64
R_ v101,a65
R_ v102,a66
R_ v103,a67
v_pk_mul_f32 v[96:97], v[96:97], v[46:47] op_sel:[0,1]
v_pk_mul_f32 v[98:99], v[98:99], v[46:47] op_sel:[0,1]
v_pk_mul_f32 v[100:101], v[100:101], v[46:47] op_sel:[0,1]
v_pk_mul_f32 v[102:103], v[102:103], v[46:47] op_sel:[0,1]
C_ 80,96,97
C_ 81,98,99
C_ 82,100,101
C_ 83,102,103
ds_write_b128 v13, v[80:83] offset:6144
.p2align 3
R_ v104,a48
R_ v105,a49
R_ v106,a50
R_ v107,a51
R_ v108,a68
R_ v109,a69
R_ v110,a70
R_ v111,a71
v_pk_mul_f32 v[104:105], v[104:105], v[48:49] op_sel_hi:[1,0]
v_pk_mul_f32 v[106:107], v[106:107], v[48:49] op_sel_hi:[1,0]
v_pk_mul_f32 v[108:109], v[108:109], v[48:49] op_sel_hi:[1,0]
v_pk_mul_f32 v[110:111], v[110:111], v[48:49] op_sel_hi:[1,0]
C_ 84,104,105
C_ 85,106,107
C_ 86,108,109
C_ 87,110,111
ds_write_b128 v13, v[84:87] offset:7168
.p2align 3
R_ v96,a52
R_ v97,a53
R_ v98,a54
R_ v99,a55
R_ v100,a72
R_ v101,a73
R_ v102,a74
R_ v103,a75
v_pk_mul_f32 v[96:97], v[96:97], v[48:49] op_sel:[0,1]
v_pk_mul_f32 v[98:99], v[98:99], v[48:49] op_sel:[0,1]
v_pk_mul_f32 v[100:101], v[100:101], v[48:49] op_sel:[0,1]
v_pk_mul_f32 v[102:103], v[102:103], v[48:49] op_sel:[0,1]
C_ 88,96,97
C_ 89,98,99
C_ 90,100,101
C_ 91,102,103
ds_write_b128 v13, v[88:91] offset:8192
.p2align 3
R_ v104,a56
R_ v105,a57
R_ v106,a58
R_ v107,a59
R_ v108,a76
R_ v109,a77
R_ v110,a78
R_ v111,a79
v_pk_mul_f32 v[104:105], v[104:105], v[50:51] op_sel_hi:[1,0]
v_pk_mul_f32 v[106:107], v[106:107], v[50:51] op_sel_hi:[1,0]
v_pk_mul_f32 v[108:109], v[108:109], v[50:51] op_sel_hi:[1,0]
v_pk_mul_f32 v[110:111], v[110:111], v[50:51] op_sel_hi:[1,0]
C_ 92,104,105
C_ 93,106,107
C_ 94,108,109
C_ 95,110,111
ds_write_b128 v13, v[92:95] offset:9216
.p2align 3
R_ v104,a92
R_ v105,a93
R_ v106,a94
R_ v107,a95
R_ v108,a112
R_ v109,a113
R_ v110,a114
R_ v111,a115
v_pk_mul_f32 v[104:105], v[104:105], v[48:49] op_sel:[0,1]
v_pk_mul_f32 v[106:107], v[106:107], v[48:49] op_sel:[0,1]
v_pk_mul_f32 v[108:109], v[108:109], v[48:49] op_sel:[0,1]
v_pk_mul_f32 v[110:111], v[110:111], v[48:49] op_sel:[0,1]
C_ 88,104,105
C_ 89,106,107
C_ 90,108,109
C_ 91,110,111
X_ a240,v88
X_ a241,v89
X_ a242,v90
X_ a243,v91
.p2align 3
R_ v96,a96
R_ v97,a97
R_ v98,a98
R_ v99,a99
R_ v100,a116
R_ v101,a117
R_ v102,a118
R_ v103,a119
v_pk_mul_f32 v[96:97], v[96:97], v[50:51] op_sel_hi:[1,0]
v_pk_mul_f32 v[98:99], v[98:99], v[50:51] op_sel_hi:[1,0]
v_pk_mul_f32 v[100:101], v[100:101], v[50:51] op_sel_hi:[1,0]
v_pk_mul_f32 v[102:103], v[102:103], v[50:51] op_sel_hi:[1,0]
C_ 92,96,97
C_ 93,98,99
C_ 94,100,101
C_ 95,102,103
X_ a244,v92
X_ a245,v93
X_ a246,v94
X_ a247,v95
.p2align 3
R_ v96,a124
R_ v97,a125
R_ v98,a126
R_ v99,a127
R_ v100,a144
R_ v101,a145
R_ v102,a146
R_ v103,a147
v_pk_mul_f32 v[96:97], v[96:97], v[46:47] op_sel:[0,1]
v_pk_mul_f32 v[98:99], v[98:99], v[46:47] op_sel:[0,1]
v_pk_mul_f32 v[100:101], v[100:101], v[46:47] op_sel:[0,1]
v_pk_mul_f32 v[102:103], v[102:103], v[46:47] op_sel:[0,1]
C_ 80,96,97
C_ 81,98,99
C_ 82,100,101
C_ 83,102,103
v_mov_b32 v28, v80
v_mov_b32 v29, v81
v_mov_b32 v51, v82
v_mov_b32 v6, v83
.p2align 3
R_ v104,a128
R_ v105,a129
R_ v106,a130
R_ v107,a131
R_ v108,a148
R_ v109,a149
R_ v110,a150
R_ v111,a151
v_pk_mul_f32 v[104:105], v[104:105], v[48:49] op_sel_hi:[1,0]
v_pk_mul_f32 v[106:107], v[106:107], v[48:49] op_sel_hi:[1,0]
v_pk_mul_f32 v[108:109], v[108:109], v[48:49] op_sel_hi:[1,0]
v_pk_mul_f32 v[110:111], v[110:111], v[48:49] op_sel_hi:[1,0]
C_ 84,104,105
C_ 85,106,107
C_ 86,108,109
C_ 87,110,111
v_mov_b32 v14, v84
v_mov_b32 v15, v85
X_ a249,v86
X_ a250,v87
.p2align 3
R_ v96,a132
R_ v97,a133
R_ v98,a134
R_ v99,a135
R_ v100,a152
R_ v101,a153
R_ v102,a154
R_ v103,a155
v_pk_mul_f32 v[96:97], v[96:97], v[48:49] op_sel:[0,1]
v_pk_mul_f32 v[98:99], v[98:99], v[48:49] op_sel:[0,1]
v_pk_mul_f32 v[100:101], v[100:101], v[48:49] op_sel:[0,1]
v_pk_mul_f32 v[102:103], v[102:103], v[48:49] op_sel:[0,1]
C_ 88,96,97
C_ 89,98,99
C_ 90,100,101
C_ 91,102,103
ds_write_b128 v12, v[88:91] offset:0
.p2align 3
R_ v104,a136
R_ v105,a137
R_ v106,a138
R_ v107,a139
R_ v108,a156
R_ v109,a157
R_ v110,a158
R_ v111,a159
v_pk_mul_f32 v[104:105], v[104:105], v[50:51] op_sel_hi:[1,0]
v_pk_mul_f32 v[106:107], v[106:107], v[50:51] op_sel_hi:[1,0]
v_pk_mul_f32 v[108:109], v[108:109], v[50:51] op_sel_hi:[1,0]
v_pk_mul_f32 v[110:111], v[110:111], v[50:51] op_sel_hi:[1,0]
C_ 92,104,105
C_ 93,106,107
C_ 94,108,109
C_ 95,110,111
ds_write_b128 v12, v[92:95] offset:1024
.p2align 3
R_ v96,a160
R_ v97,a161
R_ v98,a162
R_ v99,a163
R_ v100,a180
R_ v101,a181
R_ v102,a182
R_ v103,a183
v_pk_mul_f32 v[96:97], v[96:97], v[46:47] op_sel_hi:[1,0]
v_pk_mul_f32 v[98:99], v[98:99], v[46:47] op_sel_hi:[1,0]
v_pk_mul_f32 v[100:101], v[100:101], v[46:47] op_sel_hi:[1,0]
v_pk_mul_f32 v[102:103], v[102:103], v[46:47] op_sel_hi:[1,0]
C_ 76,96,97
C_ 77,98,99
C_ 78,100,101
C_ 79,102,103
ds_write_b128 v12, v[76:79] offset:2048
.p2align 3
R_ v104,a164
R_ v105,a165
R_ v106,a166
R_ v107,a167
R_ v108,a184
R_ v109,a185
R_ v110,a186
R_ v111,a187
v_pk_mul_f32 v[104:105], v[104:105], v[46:47] op_sel:[0,1]
v_pk_mul_f32 v[106:107], v[106:107], v[46:47] op_sel:[0,1]
v_pk_mul_f32 v[108:109], v[108:109], v[46:47] op_sel:[0,1]
v_pk_mul_f32 v[110:111], v[110:111], v[46:47] op_sel:[0,1]
C_ 80,104,105
C_ 81,106,107
C_ 82,108,109
C_ 83,110,111
ds_write_b128 v12, v[80:83] offset:3072
.p2align 3
R_ v96,a168
R_ v97,a169
R_ v98,a170
R_ v99,a171
R_ v100,a188
R_ v101,a189
R_ v102,a190
R_ v103,a191
v_pk_mul_f32 v[96:97], v[96:97], v[48:49] op_sel_hi:[1,0]
v_pk_mul_f32 v[98:99], v[98:99], v[48:49] op_sel_hi:[1,0]
v_pk_mul_f32 v[100:101], v[100:101], v[48:49] op_sel_hi:[1,0]
v_pk_mul_f32 v[102:103], v[102:103], v[48:49] op_sel_hi:[1,0]
C_ 84,96,97
C_ 85,98,99
C_ 86,100,101
C_ 87,102,103
ds_write_b128 v12, v[84:87] offset:4096
.p2align 3
R_ v104,a172
R_ v105,a173
R_ v106,a174
R_ v107,a175
R_ v108,a192
R_ v109,a193
R_ v110,a194
R_ v111,a195
v_pk_mul_f32 v[104:105], v[104:105], v[48:49] op_sel:[0,1]
v_pk_mul_f32 v[106:107], v[106:107], v[48:49] op_sel:[0,1]
v_pk_mul_f32 v[108:109], v[108:109], v[48:49] op_sel:[0,1]
v_pk_mul_f32 v[110:111], v[110:111], v[48:49] op_sel:[0,1]
C_ 88,104,105
C_ 89,106,107
C_ 90,108,109
C_ 91,110,111
ds_write_b128 v12, v[88:91] offset:5120
.p2align 3
R_ v96,a176
R_ v97,a177
R_ v98,a178
R_ v99,a179
R_ v100,a196
R_ v101,a197
R_ v102,a198
R_ v103,a199
v_pk_mul_f32 v[96:97], v[96:97], v[50:51] op_sel_hi:[1,0]
v_pk_mul_f32 v[98:99], v[98:99], v[50:51] op_sel_hi:[1,0]
v_pk_mul_f32 v[100:101], v[100:101], v[50:51] op_sel_hi:[1,0]
v_pk_mul_f32 v[102:103], v[102:103], v[50:51] op_sel_hi:[1,0]
C_ 92,96,97
C_ 93,98,99
C_ 94,100,101
C_ 95,102,103
ds_write_b128 v12, v[92:95] offset:12288
.p2align 3
R_ v104,a200
R_ v105,a201
R_ v106,a202
R_ v107,a203
R_ v108,a220
R_ v109,a221
R_ v110,a222
R_ v111,a223
v_pk_mul_f32 v[104:105], v[104:105], v[46:47] op_sel_hi:[1,0]
v_pk_mul_f32 v[106:107], v[106:107], v[46:47] op_sel_hi:[1,0]
v_pk_mul_f32 v[108:109], v[108:109], v[46:47] op_sel_hi:[1,0]
v_pk_mul_f32 v[110:111], v[110:111], v[46:47] op_sel_hi:[1,0]
C_ 76,104,105
C_ 77,106,107
C_ 78,108,109
C_ 79,110,111
ds_write_b128 v12, v[76:79] offset:13312
.p2align 3
R_ v96,a204
R_ v97,a205
R_ v98,a206
R_ v99,a207
R_ v100,a224
R_ v101,a225
R_ v102,a226
R_ v103,a227
v_pk_mul_f32 v[96:97], v[96:97], v[46:47] op_sel:[0,1]
v_pk_mul_f32 v[98:99], v[98:99], v[46:47] op_sel:[0,1]
v_pk_mul_f32 v[100:101], v[100:101], v[46:47] op_sel:[0,1]
v_pk_mul_f32 v[102:103], v[102:103], v[46:47] op_sel:[0,1]
C_ 80,96,97
C_ 81,98,99
C_ 82,100,101
C_ 83,102,103
ds_write_b128 v12, v[80:83] offset:14336
.p2align 3
R_ v104,a208
R_ v105,a209
R_ v106,a210
R_ v107,a211
R_ v108,a228
R_ v109,a229
R_ v110,a230
R_ v111,a231
v_pk_mul_f32 v[104:105], v[104:105], v[48:49] op_sel_hi:[1,0]
v_pk_mul_f32 v[106:107], v[106:107], v[48:49] op_sel_hi:[1,0]
v_pk_mul_f32 v[108:109], v[108:109], v[48:49] op_sel_hi:[1,0]
v_pk_mul_f32 v[110:111], v[110:111], v[48:49] op_sel_hi:[1,0]
C_ 84,104,105
C_ 85,106,107
C_ 86,108,109
C_ 87,110,111
ds_write_b128 v12, v[84:87] offset:15360
s_waitcnt vmcnt(6) lgkmcnt(0)
s_barrier
Dv 30,%0,6144
Dv 34,v1,6144
Dv 38,%0,7168
.p2align 3
R_ v96,a212
R_ v97,a213
R_ v98,a214
R_ v99,a215
R_ v100,a232
R_ v101,a233
R_ v102,a234
R_ v103,a235
v_pk_mul_f32 v[96:97], v[96:97], v[48:49] op_sel:[0,1]
v_pk_mul_f32 v[98:99], v[98:99], v[48:49] op_sel:[0,1]
v_pk_mul_f32 v[100:101], v[100:101], v[48:49] op_sel:[0,1]
v_pk_mul_f32 v[102:103], v[102:103], v[48:49] op_sel:[0,1]
C_ 88,96,97
C_ 89,98,99
C_ 90,100,101
C_ 91,102,103
ds_write_b128 v12, v[88:91] offset:16384
.p2align 3
R_ v104,a216
R_ v105,a217
R_ v106,a218
R_ v107,a219
R_ v108,a236
R_ v109,a237
R_ v110,a238
R_ v111,a239
v_pk_mul_f32 v[104:105], v[104:105], v[50:51] op_sel_hi:[1,0]
v_pk_mul_f32 v[106:107], v[106:107], v[50:51] op_sel_hi:[1,0]
v_pk_mul_f32 v[108:109], v[108:109], v[50:51] op_sel_hi:[1,0]
v_pk_mul_f32 v[110:111], v[110:111], v[50:51] op_sel_hi:[1,0]
C_ 92,104,105
C_ 93,106,107
C_ 94,108,109
C_ 95,110,111
ds_write_b128 v12, v[92:95] offset:17408
.Latt3:
W_ 4
Zvvv 96,30,136
Dv 42,v1,7168
R_ v116,a80
Zvvv 100,30,140
R_ v117,a81
R_ v118,a82
Zvvv 104,30,144
R_ v119,a83
R_ v120,a100
Zvvv 108,30,148
R_ v121,a101
R_ v122,a102
Zvvv 112,30,152
W_ 4
R_ v123,a103
U_ v116,v116,v46
Mvvv 96,34,156
Dv 30,%0,8192
U_ v117,v117,v46
Mvvv 100,34,160
U_ v118,v118,v46
U_ v119,v119,v46
Mvvv 104,34,164
U_ v120,v120,v46
U_ v121,v121,v46
Mvvv 108,34,168
U_ v122,v122,v46
U_ v123,v123,v46
Mvvv 112,34,172
W_ 4
C_ 20,116,117
C_ 21,118,119
Mvvv 96,38,176
Dv 34,v1,8192
C_ 22,120,121
Mvvv 100,38,180
C_ 23,122,123
R_ v124,a84
Mvvv 104,38,184
R_ v125,a85
R_ v126,a86
Mvvv 108,38,188
R_ v127,a87
R_ v128,a104
Mvvv 112,38,192
W_ 2
R_ v129,a105
R_ v130,a106
Mvvv 96,42,196
Dv 38,%0,0
R_ v131,a107
Mvvv 100,42,200
U_ v124,v124,v47
U_ v125,v125,v47
Mvvv 104,42,204
U_ v126,v126,v47
U_ v127,v127,v47
Mvvv 108,42,208
U_ v128,v128,v47
U_ v129,v129,v47
Mvvv 112,42,212
W_ 2
U_ v130,v130,v47
U_ v131,v131,v47
Mvvv 96,30,216
Dv 42,v1,0
C_ 24,124,125
Mvvv 100,30,220
C_ 25,126,127
C_ 26,128,129
Mvvv 104,30,224
C_ 27,130,131
R_ v116,a88
Mvvv 108,30,228
R_ v117,a89
R_ v118,a90
Mvvv 112,30,232
W_ 2
R_ v119,a91
R_ v120,a108
Mvvv 96,34,236
Dv 30,%0,1024
R_ v121,a109
Mvvv 100,34,240
R_ v122,a110
R_ v123,a111
Mvvv 104,34,244
U_ v116,v116,v48
U_ v117,v117,v48
Mvvv 108,34,248
U_ v118,v118,v48
U_ v119,v119,v48
Mvvv 112,34,252
W_ 2
U_ v120,v120,v48
U_ v121,v121,v48
Zvvv 76,38,136
Dv 34,v1,1024
U_ v122,v122,v48
Zvvv 80,38,140
U_ v123,v123,v48
C_ 52,116,117
Zvvv 84,38,144
C_ 53,118,119
C_ 54,120,121
Zvvv 88,38,148
C_ 55,122,123
R_ v124,a120
Zvvv 92,38,152
W_ 2
R_ v125,a121
R_ v126,a122
Mvvv 76,42,156
Dv 38,%0,2048
R_ v127,a123
Mvvv 80,42,160
R_ v128,a140
R_ v129,a141
Mvvv 84,42,164
R_ v130,a142
R_ v131,a143
Mvvv 88,42,168
U_ v124,v124,v46
U_ v125,v125,v46
Mvvv 92,42,172
W_ 2
U_ v126,v126,v46
U_ v127,v127,v46
Mvvv 76,30,176
Dv 42,v1,2048
U_ v128,v128,v46
Mvvv 80,30,180
U_ v129,v129,v46
U_ v130,v130,v46
Mvvv 84,30,184
U_ v131,v131,v46
C_ 16,124,125
Mvvv 88,30,188
C_ 17,126,127
C_ 18,128,129
Mvvv 92,30,192
s_waitcnt vmcnt(0) lgkmcnt(0)
s_barrier
Mvvv 76,34,196
S_ s65,s61,24576
Dv 30,%0,18432
C_ 19,130,131
Mvvv 76,38,216
S_ m0,s51,0
v_max3_f32 v56, v96, v97, v98
v_max_f32 v56, v56, v99
Mvvv 76,42,236
Ln v7,32,s65,0
Mvvv 80,34,200
v_max3_f32 v60, v100, v101, v102
v_max_f32 v60, v60, v103
Mvvv 80,38,220
Ln v8,32,s65,3072
Mvvv 80,42,240
S_ m0,s51,896
v_max3_f32 v64, v104, v105, v106
v_max_f32 v64, v64, v107
Mvvv 84,34,204
Ln v7,32,s65,128
Mvvv 84,38,224
v_max3_f32 v68, v108, v109, v110
v_max_f32 v68, v68, v111
Mvvv 84,42,244
Ln v8,32,s65,3200
Mvvv 88,34,208
S_ m0,s51,1792
v_max3_f32 v72, v112, v113, v114
v_max_f32 v72, v72, v115
Mvvv 88,38,228
Ln v7,32,s65,256
Mvvv 88,42,248
v_max3_f32 v56, v56, v76, v77
v_max3_f32 v56, v56, v78, v79
Mvvv 92,34,212
Ln v8,32,s65,3328
Mvvv 92,38,232
v_max3_f32 v60, v60, v80, v81
v_max3_f32 v60, v60, v82, v83
Mvvv 92,42,252
v_max3_f32 v64, v64, v84, v85
v_max3_f32 v64, v64, v86, v87
Dv 34,v1,18432
.p2align 3
v_mov_b32 v11, 0x3f803f80
v_permlane32_swap_b32 v56, v60
v_max3_f32 v68, v68, v88, v89
v_max3_f32 v68, v68, v90, v91
v_max_f32 v56, v56, v60
v_max3_f32 v72, v72, v92, v93
v_max3_f32 v72, v72, v94, v95
v_mov_b32 v73, v72
v_permlane32_swap_b32 v64, v68
v_max_f32 v64, v64, v68
v_permlane32_swap_b32 v72, v73
v_max_f32 v72, v72, v73
v_mov_b32 v73, v72
v_permlane16_swap_b32 v56, v64
v_max_f32 v56, v56, v64
v_permlane16_swap_b32 v72, v73
B_ v56,0xc0e00000,v56
v_max_f32 v72, v72, v73
v_mov_b32 v64, v56
B_ v72,0xc0e00000,v72
v_mov_b32 v73, v72
v_permlane16_swap_b32 v56, v64
v_mov_b32 v60, v56
v_mov_b32 v68, v64
v_mov_b64 v[74:75], v[72:73]
v_permlane32_swap_b32 v56, v60
v_permlane32_swap_b32 v64, v68
v_mov_b32 v57, v56
v_mov_b32 v61, v60
v_mov_b32 v65, v64
v_mov_b32 v69, v68
v_pk_add_f32 v[96:97], v[96:97], v[56:57]
v_pk_add_f32 v[98:99], v[98:99], v[56:57]
v_pk_add_f32 v[76:77], v[76:77], v[56:57]
v_pk_add_f32 v[78:79], v[78:79], v[56:57]
v_mov_b64 v[58:59], v[56:57]
v_pk_add_f32 v[100:101], v[100:101], v[60:61]
v_pk_add_f32 v[102:103], v[102:103], v[60:61]
v_pk_add_f32 v[80:81], v[80:81], v[60:61]
v_pk_add_f32 v[82:83], v[82:83], v[60:61]
v_mov_b64 v[62:63], v[60:61]
v_pk_add_f32 v[104:105], v[104:105], v[64:65]
v_pk_add_f32 v[106:107], v[106:107], v[64:65]
v_pk_add_f32 v[84:85], v[84:85], v[64:65]
v_pk_add_f32 v[86:87], v[86:87], v[64:65]
v_mov_b64 v[66:67], v[64:65]
v_pk_add_f32 v[108:109], v[108:109], v[68:69]
v_pk_add_f32 v[110:111], v[110:111], v[68:69]
v_pk_add_f32 v[88:89], v[88:89], v[68:69]
v_pk_add_f32 v[90:91], v[90:91], v[68:69]
v_mov_b64 v[70:71], v[68:69]
v_pk_add_f32 v[112:113], v[112:113], v[72:73]
v_pk_add_f32 v[114:115], v[114:115], v[72:73]
v_pk_add_f32 v[92:93], v[92:93], v[72:73]
v_pk_add_f32 v[94:95], v[94:95], v[72:73]
E_ 96
E_ 97
E_ 98
E_ 99
E_ 76
E_ 77
E_ 78
E_ 79
C_ 99,98,99
C_ 98,96,97
C_ 96,76,77
C_ 97,78,79
E_ 100
E_ 101
E_ 102
E_ 103
E_ 80
E_ 81
E_ 82
E_ 83
C_ 103,102,103
C_ 102,100,101
C_ 100,80,81
C_ 101,82,83
.Lstep3_1:
W_ 1
Nvvv 116,30,136,56
s_nop 0
Dv 38,%0,19456
Dv 42,v1,19456
W_ 2
Mvvv 116,34,156
E_ 104
Nvvv 120,30,140,60
E_ 105
Mvvv 120,34,160
E_ 106
Nvvv 124,30,144,64
E_ 107
Mvvv 124,34,164
E_ 84
Nvvv 128,30,148,68
E_ 85
Mvvv 128,34,168
E_ 86
Nvvv 132,30,152,72
E_ 87
Mvvv 132,34,172
W_ 0
C_ 107,106,107
C_ 106,104,105
Mvvv 132,38,192
s_nop 0
Dv 30,%0,20480
Dv 34,v1,20480
Mvvv 132,42,212
C_ 104,84,85
C_ 105,86,87
Mvvv 128,38,188
E_ 108
Mvvv 128,42,208
E_ 109
Mvvv 124,38,184
E_ 110
Mvvv 124,42,204
E_ 111
Mvvv 120,38,180
E_ 88
Mvvv 120,42,200
E_ 89
Mvvv 116,38,176
E_ 90
Mvvv 116,42,196
W_ 0
E_ 91
Mvvv 116,30,216
s_nop 0
Dv 38,%0,12288
Dv 42,v1,12288
Mvvv 116,34,236
C_ 111,110,111
C_ 110,108,109
Mvvv 120,30,220
C_ 108,88,89
C_ 109,90,91
Mvvv 120,34,240
E_ 112
Mvvv 124,30,224
)A"
R"A(E_ 113
Mvvv 124,34,244
E_ 114
Mvvv 128,30,228
E_ 115
Mvvv 128,34,248
E_ 92
Mvvv 132,30,232
E_ 93
Mvvv 132,34,252
W_ 0
E_ 94
Nvvv 76,38,136,56
s_nop 0
Dv 30,%0,13312
Dv 34,v1,13312
Mvvv 76,42,156
E_ 95
Nvvv 80,38,140,60
C_ 115,114,115
C_ 114,112,113
Mvvv 80,42,160
C_ 112,92,93
C_ 113,94,95
Nvvv 84,38,144,64
E_ 116
Mvvv 84,42,164
E_ 117
Nvvv 88,38,148,68
E_ 118
Mvvv 88,42,168
E_ 119
Nvvv 92,38,152,72
C_ 119,118,119
C_ 118,116,117
Mvvv 92,42,172
W_ 0
E_ 120
Mvvv 92,30,192
s_nop 0
Dv 38,%0,14336
Dv 42,v1,14336
Mvvv 92,34,212
X_ a180,v11
X_ a181,v11
Mvvv 88,30,188
X_ a182,v11
X_ a183,v11
Mvvv 88,34,208
E_ 121
Mvvv 84,30,184
E_ 122
Mvvv 84,34,204
E_ 123
Mvvv 80,30,180
C_ 123,122,123
C_ 122,120,121
Mvvv 80,34,200
E_ 124
Mvvv 76,30,176
E_ 125
Mvvv 76,34,196
S_ s65,s61,12288
E_ 126
W_ 1
Mvvv 76,38,216
S_ m0,s51,12288
E_ 127
W_ 0
Mvvv 76,42,236
s_waitcnt vmcnt(18) lgkmcnt(0)
s_barrier
C_ 127,126,127
Mvvv 80,38,220
L_ v9,36,s65,0
Mvvv 80,42,240
C_ 126,124,125
Mvvv 84,38,224
L_ v9,36,s65,3072
S_ m0,s51,13184
Mvvv 84,42,244
s_nop 0
Tv 30,v2,24576
Tv 32,v2,30720
Mvvv 88,38,228
L_ v9,36,s65,128
Mvvv 88,42,248
s_nop 0
Tv 34,v3,24576
Tv 36,v3,30720
Mvvv 92,38,232
L_ v9,36,s65,3200
S_ m0,s51,14080
Mvvv 92,42,252
E_ 128
Zaav 220,180,96
L_ v9,36,s65,256
Zaav 224,180,100
E_ 129
Zaav 228,180,104
L_ v9,36,s65,3328
Zaav 232,180,108
E_ 130
Zaav 236,180,112
W_ 0
E_ 131
Zavv 0,30,96
s_nop 0
Tv 38,v4,24576
Tv 40,v4,30720
Zavv 20,34,96
C_ 131,130,131
C_ 130,128,129
Zavv 24,34,100
s_nop 0
Tv 42,v5,24576
Tv 44,v5,30720
Zavv 4,30,100
E_ 132
Zavv 8,30,104
E_ 133
Zavv 28,34,104
R_ v46,a220
R_ v47,a224
Zavv 32,34,108
R_ v48,a228
R_ v49,a232
Zavv 12,30,108
R_ v50,a236
Zavv 16,30,112
E_ 134
Zavv 36,34,112
W_ 0
E_ 135
Zavv 56,38,112
s_nop 0
Tv 30,v2,25600
Tv 32,v2,31744
Zavv 76,42,112
C_ 135,134,135
C_ 134,132,133
Zavv 72,42,108
s_nop 0
Tv 34,v3,25600
Tv 36,v3,31744
Zavv 52,38,108
E_ 76
Zavv 48,38,104
E_ 77
Zavv 68,42,104
E_ 78
Zavv 64,42,100
E_ 79
Zavv 44,38,100
C_ 116,76,77
C_ 117,78,79
Zavv 40,38,96
E_ 80
Zavv 60,42,96
W_ 0
E_ 81
Zavv 80,30,96
s_nop 0
Tv 38,v4,25600
Tv 40,v4,31744
Zavv 100,34,96
E_ 82
Zavv 104,34,100
s_nop 0
Tv 42,v5,25600
Tv 44,v5,31744
Zavv 84,30,100
E_ 83
Zavv 88,30,104
C_ 120,80,81
C_ 121,82,83
Zavv 108,34,104
E_ 84
Zavv 112,34,108
E_ 85
Zavv 92,30,108
E_ 86
Zavv 96,30,112
E_ 87
Zavv 116,34,112
W_ 0
C_ 124,84,85
C_ 125,86,87
Zavv 136,38,112
s_nop 0
Tv 30,v2,26624
Tv 32,v2,32768
Zavv 156,42,112
E_ 88
Zavv 152,42,108
s_nop 0
Tv 34,v3,26624
Tv 36,v3,32768
Zavv 132,38,108
E_ 89
Zavv 128,38,104
E_ 90
Zavv 148,42,104
E_ 91
Zavv 144,42,100
C_ 128,88,89
C_ 129,90,91
Zavv 124,38,100
E_ 92
Zavv 120,38,96
E_ 93
Zavv 140,42,96
W_ 0
E_ 94
Zavv 160,30,96
s_nop 0
Tv 38,v4,26624
Tv 40,v4,32768
Zavv 180,34,96
E_ 95
Zavv 184,34,100
s_nop 0
Tv 42,v5,26624
Tv 44,v5,32768
Zavv 164,30,100
C_ 132,92,93
C_ 133,94,95
Zavv 168,30,104
v_mov_b32 v92, 0x3f803f80
v_mov_b32 v93, 0x3f803f80
Zavv 188,34,104
S_ s65,s61,36864
v_mov_b32 v94, 0x3f803f80
v_mov_b32 v95, 0x3f803f80
Zavv 192,34,108
S_ m0,s51,24576
Zavv 172,30,108
s_waitcnt vmcnt(6) lgkmcnt(0)
s_barrier
Zavv 176,30,112
Ln v7,32,s65,0
Zavv 196,34,112
Zavv 216,38,112
Dv 30,%0,6144
Zavv 236,42,112
Dv 34,v1,6144
Zavv 232,42,108
Ln v8,32,s65,3072
S_ m0,s51,25472
Zavv 212,38,108
Zavv 208,38,104
Ln v7,32,s65,128
Zavv 228,42,104
Zavv 224,42,100
Ln v8,32,s65,3200
S_ m0,s51,26368
Zavv 204,38,100
Zavv 200,38,96
Ln v7,32,s65,256
Zavv 220,42,96
Ln v8,32,s65,3328
W_ 0
s_nop 7
s_branch .Lstep1_2
.Latt1:
R_ v13,a248
S_ s53,s51,36864
v_add_u32 v12, s53, v13
s_mul_i32 s53, s4, 10240
S_ s53,s53,0
v_add_u32 v13, s53, v13
W_ 2
Zvvv 96,30,136
Dv 42,v1,44032
R_ v2,a245
Zvvv 100,30,140
ds_write_b128 v13, a[0:3] offset:0
Zvvv 104,30,144
v_xor_b32 v3, 32, v2
v_xor_b32 v4, 64, v2
Zvvv 108,30,148
ds_write_b128 v13, a[4:7] offset:1024
Zvvv 112,30,152
W_ 4
v_xor_b32 v5, 96, v2
s_mov_b64 s[24:25], s[16:17]
Mvvv 96,34,156
s_mov_b32 s26, 7864320
Dv 30,%0,45056
s_mov_b64 s[28:29], s[22:23]
Mvvv 100,34,160
ds_write_b128 v13, a[8:11] offset:2048
Mvvv 104,34,164
s_mov_b32 s30, 1638400
R_ v20,a80
s_mul_i32 s52, s4, 10240
Mvvv 108,34,168
ds_write_b128 v13, a[12:15] offset:3072
Mvvv 112,34,172
W_ 6
R_ v21,a81
R_ v22,a82
Mvvv 96,38,176
Dv 34,v1,45056
R_ v23,a83
Mvvv 100,38,180
ds_write_b128 v13, a[16:19] offset:4096
Mvvv 104,38,184
R_ v24,a84
R_ v25,a85
Mvvv 108,38,188
ds_write_b128 v13, a[40:43] offset:5120
Mvvv 112,38,192
W_ 8
R_ v26,a86
R_ v27,a87
Mvvv 96,42,196
Dv 38,%0,36864
R_ v52,a88
Mvvv 100,42,200
ds_write_b128 v13, a[44:47] offset:6144
Mvvv 104,42,204
R_ v53,a89
R_ v54,a90
Mvvv 108,42,208
ds_write_b128 v13, a[48:51] offset:7168
Mvvv 112,42,212
W_ 8
R_ v55,a91
v_accvgpr_mov_b32 a240, a92
Mvvv 96,30,216
Dv 42,v1,36864
v_accvgpr_mov_b32 a241, a93
Mvvv 100,30,220
ds_write_b128 v13, a[52:55] offset:8192
Mvvv 104,30,224
v_accvgpr_mov_b32 a242, a94
v_accvgpr_mov_b32 a243, a95
Mvvv 108,30,228
ds_write_b128 v13, a[56:59] offset:9216
Mvvv 112,30,232
W_ 8
v_accvgpr_mov_b32 a244, a96
v_accvgpr_mov_b32 a245, a97
Mvvv 96,34,236
Dv 30,%0,37888
v_accvgpr_mov_b32 a246, a98
Mvvv 100,34,240
v_accvgpr_mov_b32 a247, a99
R_ v16,a120
Mvvv 104,34,244
R_ v17,a121
R_ v18,a122
Mvvv 108,34,248
R_ v19,a123
R_ v28,a124
Mvvv 112,34,252
W_ 6
R_ v29,a125
R_ v51,a126
Zvvv 76,38,136
Dv 34,v1,37888
R_ v6,a127
Zvvv 80,38,140
R_ v14,a128
R_ v15,a129
Zvvv 84,38,144
v_accvgpr_mov_b32 a249, a130
v_accvgpr_mov_b32 a250, a131
Zvvv 88,38,148
v_max3_f32 v56, v96, v97, v98
v_max_f32 v56, v56, v99
Zvvv 92,38,152
W_ 4
v_max3_f32 v60, v100, v101, v102
v_max_f32 v60, v60, v103
Mvvv 76,42,156
Dv 38,%0,38912
v_max3_f32 v64, v104, v105, v106
Mvvv 80,42,160
v_max_f32 v64, v64, v107
Mvvv 84,42,164
v_max3_f32 v68, v108, v109, v110
v_max_f32 v68, v68, v111
Mvvv 88,42,168
v_max3_f32 v72, v112, v113, v114
v_max_f32 v72, v72, v115
Mvvv 92,42,172
W_ 2
Mvvv 76,30,176
Dv 42,v1,38912
Mvvv 80,30,180
Mvvv 84,30,184
Mvvv 88,30,188
Mvvv 92,30,192
s_waitcnt vmcnt(34) lgkmcnt(0)
s_barrier
Mvvv 76,34,196
Dv 30,%0,55296
Mvvv 76,38,216
Mvvv 76,42,236
Mvvv 80,34,200
Mvvv 80,38,220
Mvvv 80,42,240
Mvvv 84,34,204
s_nop 3
v_max3_f32 v56, v56, v76, v77
v_max3_f32 v56, v56, v78, v79
Mvvv 84,38,224
Mvvv 84,42,244
Mvvv 88,34,208
v_max3_f32 v60, v60, v80, v81
v_max3_f32 v60, v60, v82, v83
Mvvv 88,38,228
Mvvv 88,42,248
Mvvv 92,34,212
s_nop 1
v_max3_f32 v64, v64, v84, v85
v_max3_f32 v64, v64, v86, v87
Mvvv 92,38,232
Mvvv 92,42,252
Dv 34,v1,55296
.p2align 3
v_mov_b32 v11, 0x3f803f80
v_permlane32_swap_b32 v56, v60
v_max3_f32 v68, v68, v88, v89
v_max3_f32 v68, v68, v90, v91
v_max_f32 v56, v56, v60
s_nop 1
v_max3_f32 v72, v72, v92, v93
v_max3_f32 v72, v72, v94, v95
v_mov_b32 v73, v72
v_permlane32_swap_b32 v64, v68
v_max_f32 v64, v64, v68
v_permlane32_swap_b32 v72, v73
v_max_f32 v72, v72, v73
v_mov_b32 v73, v72
v_permlane16_swap_b32 v56, v64
v_max_f32 v56, v56, v64
v_permlane16_swap_b32 v72, v73
B_ v56,0xc0e00000,v56
v_max_f32 v72, v72, v73
v_mov_b32 v64, v56
B_ v72,0xc0e00000,v72
v_mov_b32 v73, v72
v_permlane16_swap_b32 v56, v64
v_mov_b32 v60, v56
v_mov_b32 v68, v64
v_mov_b64 v[74:75], v[72:73]
v_permlane32_swap_b32 v56, v60
v_permlane32_swap_b32 v64, v68
v_mov_b32 v57, v56
v_mov_b32 v61, v60
v_mov_b32 v65, v64
v_mov_b32 v69, v68
v_pk_add_f32 v[96:97], v[96:97], v[56:57]
v_pk_add_f32 v[98:99], v[98:99], v[56:57]
v_pk_add_f32 v[76:77], v[76:77], v[56:57]
v_pk_add_f32 v[78:79], v[78:79], v[56:57]
v_mov_b64 v[58:59], v[56:57]
v_pk_add_f32 v[100:101], v[100:101], v[60:61]
v_pk_add_f32 v[102:103], v[102:103], v[60:61]
v_pk_add_f32 v[80:81], v[80:81], v[60:61]
v_pk_add_f32 v[82:83], v[82:83], v[60:61]
v_mov_b64 v[62:63], v[60:61]
v_pk_add_f32 v[104:105], v[104:105], v[64:65]
v_pk_add_f32 v[106:107], v[106:107], v[64:65]
v_pk_add_f32 v[84:85], v[84:85], v[64:65]
v_pk_add_f32 v[86:87], v[86:87], v[64:65]
v_mov_b64 v[66:67], v[64:65]
v_pk_add_f32 v[108:109], v[108:109], v[68:69]
v_pk_add_f32 v[110:111], v[110:111], v[68:69]
v_pk_add_f32 v[88:89], v[88:89], v[68:69]
v_pk_add_f32 v[90:91], v[90:91], v[68:69]
v_mov_b64 v[70:71], v[68:69]
v_pk_add_f32 v[112:113], v[112:113], v[72:73]
v_pk_add_f32 v[114:115], v[114:115], v[72:73]
v_pk_add_f32 v[92:93], v[92:93], v[72:73]
v_pk_add_f32 v[94:95], v[94:95], v[72:73]
E_ 96
E_ 97
E_ 98
E_ 99
E_ 76
E_ 77
E_ 78
E_ 79
C_ 99,98,99
C_ 98,96,97
C_ 96,76,77
C_ 97,78,79
E_ 100
E_ 101
E_ 102
E_ 103
E_ 80
E_ 81
E_ 82
E_ 83
C_ 103,102,103
C_ 102,100,101
C_ 100,80,81
C_ 101,82,83
.Lstep1_1:
W_ 1
Nvvv 116,30,136,56
s_nop 0
Dv 38,%0,56320
Dv 42,v1,56320
W_ 2
Mvvv 116,34,156
E_ 104
Nvvv 120,30,140,60
E_ 105
Mvvv 120,34,160
E_ 106
Nvvv 124,30,144,64
E_ 107
Mvvv 124,34,164
E_ 84
Nvvv 128,30,148,68
E_ 85
Mvvv 128,34,168
E_ 86
Nvvv 132,30,152,72
E_ 87
Mvvv 132,34,172
W_ 0
C_ 107,106,107
C_ 106,104,105
Mvvv 132,38,192
s_nop 0
Dv 30,%0,57344
Dv 34,v1,57344
Mvvv 132,42,212
C_ 104,84,85
C_ 105,86,87
Mvvv 128,38,188
E_ 108
Mvvv 128,42,208
E_ 109
Mvvv 124,38,184
E_ 110
Mvvv 124,42,204
E_ 111
Mvvv 120,38,180
E_ 88
Mvvv 120,42,200
E_ 89
Mvvv 116,38,176
E_ 90
Mvvv 116,42,196
W_ 0
E_ 91
Mvvv 116,30,216
s_nop 0
Dv 38,%0,49152
Dv 42,v1,49152
Mvvv 116,34,236
C_ 111,110,111
C_ 110,108,109
Mvvv 120,30,220
C_ 108,88,89
C_ 109,90,91
Mvvv 120,34,240
E_ 112
Mvvv 124,30,224
E_ 113
Mvvv 124,34,244
E_ 114
Mvvv 128,30,228
E_ 115
Mvvv 128,34,248
E_ 92
Mvvv 132,30,232
E_ 93
Mvvv 132,34,252
W_ 0
E_ 94
Nvvv 76,38,136,56
s_nop 0
Dv 30,%0,50176
Dv 34,v1,50176
Mvvv 76,42,156
E_ 95
Nvvv 80,38,140,60
C_ 115,114,115
C_ 114,112,113
Mvvv 80,42,160
C_ 112,92,93
C_ 113,94,95
Nvvv 84,38,144,64
E_ 116
Mvvv 84,42,164
E_ 117
Nvvv 88,38,148,68
E_ 118
Mvvv 88,42,168
E_ 119
Nvvv 92,38,152,72
C_ 119,118,119
C_ 118,116,117
Mvvv 92,42,172
W_ 0
E_ 120
Mvvv 92,30,192
s_nop 0
Dv 38,%0,51200
Dv 42,v1,51200
Mvvv 92,34,212
X_ a180,v11
X_ a181,v11
Mvvv 88,30,188
X_ a182,v11
X_ a183,v11
Mvvv 88,34,208
ds_write_b128 v12, a[132:135] offset:0
Mvvv 84,30,184
E_ 121
Mvvv 84,34,204
E_ 122
Mvvv 80,30,180
ds_write_b128 v12, a[136:139] offset:1024
Mvvv 80,34,200
E_ 123
Mvvv 76,30,176
C_ 123,122,123
C_ 122,120,121
Mvvv 76,34,196
ds_write_b128 v12, a[160:163] offset:2048
W_ 4
Mvvv 76,38,216
W_ 3
E_ 124
Mvvv 76,42,236
s_waitcnt vmcnt(12) lgkmcnt(0)
s_barrier
Mvvv 80,38,220
ds_write_b128 v12, a[164:167] offset:3072
Mvvv 80,42,240
E_ 125
Mvvv 84,38,224
E_ 126
Mvvv 84,42,244
s_nop 0
Tv 30,v2,24576
Tv 32,v2,30720
Mvvv 88,38,228
ds_write_b128 v12, a[168:171] offset:4096
Mvvv 88,42,248
s_nop 0
Tv 34,v3,24576
Tv 36,v3,30720
Mvvv 92,38,232
E_ 127
Mvvv 92,42,252
ds_write_b128 v12, a[172:175] offset:5120
Zaav 220,180,96
C_ 127,126,127
C_ 126,124,125
Zaav 224,180,100
E_ 128
Zaav 228,180,104
ds_write_b128 v12, a[176:179] offset:12288
Zaav 232,180,108
E_ 129
Zaav 236,180,112
W_ 2
E_ 130
Zavv 0,30,96
s_nop 0
Tv 38,v4,24576
Tv 40,v4,30720
Zavv 20,34,96
ds_write_b128 v12, a[200:203] offset:13312
Zavv 24,34,100
s_nop 0
Tv 42,v5,24576
Tv 44,v5,30720
Zavv 4,30,100
E_ 131
Zavv 8,30,104
ds_write_b128 v12, a[204:207] offset:14336
Zavv 28,34,104
R_ v46,a220
R_ v47,a224
Zavv 32,34,108
R_ v48,a228
R_ v49,a232
Zavv 12,30,108
R_ v50,a236
C_ 131,130,131
Zavv 16,30,112
ds_write_b128 v12, a[208:211] offset:15360
Zavv 36,34,112
W_ 2
C_ 130,128,129
Zavv 56,38,112
s_nop 0
Tv 30,v2,25600
Tv 32,v2,31744
Zavv 76,42,112
ds_write_b128 v12, a[212:215] offset:16384
Zavv 72,42,108
s_nop 0
Tv 34,v3,25600
Tv 36,v3,31744
Zavv 52,38,108
E_ 132
Zavv 48,38,104
ds_write_b128 v12, a[216:219] offset:17408
Zavv 68,42,104
E_ 133
Zavv 64,42,100
E_ 134
Zavv 44,38,100
E_ 135
Zavv 40,38,96
C_ 135,134,135
C_ 134,132,133
Zavv 60,42,96
W_ 1
E_ 76
Zavv 80,30,96
s_nop 0
Tv 38,v4,25600
Tv 40,v4,31744
Zavv 100,34,96
E_ 77
Zavv 104,34,100
s_nop 0
Tv 42,v5,25600
Tv 44,v5,31744
Zavv 84,30,100
E_ 78
Zavv 88,30,104
E_ 79
Zavv 108,34,104
C_ 116,76,77
C_ 117,78,79
Zavv 112,34,108
E_ 80
Zavv 92,30,108
E_ 81
Zavv 96,30,112
E_ 82
Zavv 116,34,112
W_ 0
E_ 83
Zavv 136,38,112
s_nop 0
Tv 30,v2,26624
Tv 32,v2,32768
Zavv 156,42,112
C_ 120,80,81
C_ 121,82,83
Zavv 152,42,108
s_nop 0
Tv 34,v3,26624
Tv 36,v3,32768
Zavv 132,38,108
E_ 84
Zavv 128,38,104
E_ 85
Zavv 148,42,104
E_ 86
Zavv 144,42,100
E_ 87
Zavv 124,38,100
C_ 124,84,85
C_ 125,86,87
Zavv 120,38,96
E_ 88
Zavv 140,42,96
W_ 0
E_ 89
Zavv 160,30,96
s_nop 0
Tv 38,v4,26624
Tv 40,v4,32768
Zavv 180,34,96
E_ 90
Zavv 184,34,100
s_nop 0
Tv 42,v5,26624
Tv 44,v5,32768
Zavv 164,30,100
E_ 91
Zavv 168,30,104
C_ 128,88,89
C_ 129,90,91
Zavv 188,34,104
S_ s65,s61,36864
E_ 92
Zavv 192,34,108
S_ m0,s51,24576
E_ 93
Zavv 172,30,108
s_waitcnt vmcnt(6) lgkmcnt(0)
s_barrier
Zavv 176,30,112
Ln v7,32,s65,0
Zavv 196,34,112
E_ 94
Zavv 216,38,112
Dv 30,%0,6144
Zavv 236,42,112
Dv 34,v1,6144
Zavv 232,42,108
Ln v8,32,s65,3072
S_ m0,s51,25472
Zavv 212,38,108
E_ 95
Zavv 208,38,104
Ln v7,32,s65,128
Zavv 228,42,104
C_ 132,92,93
C_ 133,94,95
Zavv 224,42,100
Ln v8,32,s65,3200
S_ m0,s51,26368
Zavv 204,38,100
v_mov_b32 v92, 0x3f803f80
v_mov_b32 v93, 0x3f803f80
Zavv 200,38,96
Ln v7,32,s65,256
Zavv 220,42,96
v_mov_b32 v94, 0x3f803f80
v_mov_b32 v95, 0x3f803f80
Ln v8,32,s65,3328
.Lstep1_2:
s_nop 0
Zvvv 76,92,116
Dv 38,%0,7168
Zvvv 80,92,120
Dv 42,v1,7168
Zvvv 84,92,124
Zvvv 88,92,128
Zvvv 92,92,132
W_ 2
Nvvv 96,30,136,56
Mvvv 96,34,156
Nvvv 100,30,140,60
s_nop 1
v_max3_f32 v10, v76, v80, v84
s_nop 0
v_max3_f32 v10, v10, v88, v92
Mvvv 100,34,160
v_cmp_lt_f32 vcc, 0x42000000, v10
s_cbranch_vccnz .Ltramp1_1
Nvvv 104,30,144,64
A_ v46,v46,v76
A_ v47,v47,v80
Mvvv 104,34,164
A_ v48,v48,v84
A_ v49,v49,v88
Nvvv 108,30,148,68
A_ v50,v50,v92
Mvvv 108,34,168
Nvvv 112,30,152,72
Mvvv 112,34,172
W_ 0
Mvvv 112,38,192
Dv 30,%0,8192
Mvvv 112,42,212
Dv 34,v1,8192
Mvvv 108,38,188
Mvvv 108,42,208
Mvvv 104,38,184
Mvvv 104,42,204
Mvvv 100,38,180
Mvvv 100,42,200
Mvvv 96,38,176
Mvvv 96,42,196
W_ 0
Mvvv 96,30,216
Dv 38,%0,0
Mvvv 96,34,236
Dv 42,v1,0
Mvvv 100,30,220
Mvvv 100,34,240
Mvvv 104,30,224
Mvvv 104,34,244
Mvvv 108,30,228
Mvvv 108,34,248
s_nop 0
E_ 96
Mvvv 112,30,232
E_ 97
Mvvv 112,34,252
W_ 0
E_ 98
Nvvv 76,38,136,56
Dv 30,%0,1024
Mvvv 76,42,156
Dv 34,v1,1024
Nvvv 80,38,140,60
E_ 99
Mvvv 80,42,160
C_ 99,98,99
C_ 98,96,97
Nvvv 84,38,144,64
E_ 100
Mvvv 84,42,164
E_ 101
Nvvv 88,38,148,68
E_ 102
Mvvv 88,42,168
E_ 103
Nvvv 92,38,152,72
C_ 103,102,103
C_ 102,100,101
Mvvv 92,42,172
W_ 0
E_ 104
Mvvv 92,30,192
Dv 38,%0,2048
Mvvv 92,34,212
Dv 42,v1,2048
Mvvv 88,30,188
E_ 105
Mvvv 88,34,208
E_ 106
Mvvv 84,30,184
S_ s65,s61,24576
E_ 107
Mvvv 84,34,204
S_ m0,s51,0
C_ 107,106,107
C_ 106,104,105
Mvvv 80,30,180
s_waitcnt vmcnt(6) lgkmcnt(0)
s_barrier
Mvvv 80,34,200
L_ v9,36,s65,0
Mvvv 76,30,176
E_ 108
Mvvv 76,34,196
L_ v9,36,s65,3072
S_ m0,s51,896
Mvvv 76,38,216
Tv 30,v2,12288
Tv 32,v2,18432
Mvvv 76,42,236
L_ v9,36,s65,128
Mvvv 80,38,220
s_nop 0
Tv 34,v3,12288
Tv 36,v3,18432
Mvvv 80,42,240
L_ v9,36,s65,3200
S_ m0,s51,1792
Mvvv 84,38,224
E_ 109
Mvvv 84,42,244
L_ v9,36,s65,256
Mvvv 88,38,228
E_ 110
Mvvv 88,42,248
L_ v9,36,s65,3328
Mvvv 92,38,232
E_ 111
Mvvv 92,42,252
W_ 0
C_ 111,110,111
C_ 110,108,109
Mavv 0,30,116
s_nop 0
Tv 38,v4,12288
Tv 40,v4,18432
Mavv 20,34,116
E_ 112
Mavv 24,34,120
s_nop 0
Tv 42,v5,12288
Tv 44,v5,18432
Mavv 4,30,120
E_ 113
Mavv 8,30,124
E_ 114
Mavv 28,34,124
E_ 115
Mavv 32,34,128
C_ 115,114,115
C_ 114,112,113
Mavv 12,30,128
E_ 76
Mavv 16,30,132
E_ 77
Mavv 36,34,132
W_ 0
E_ 78
Mavv 56,38,132
s_nop 0
Tv 30,v2,13312
Tv 32,v2,19456
Mavv 76,42,132
E_ 79
Mavv 72,42,128
s_nop 0
Tv 34,v3,13312
Tv 36,v3,19456
Mavv 52,38,128
C_ 96,76,77
C_ 97,78,79
Mavv 48,38,124
E_ 80
Mavv 68,42,124
E_ 81
Mavv 64,42,120
E_ 82
Mavv 44,38,120
E_ 83
Mavv 40,38,116
C_ 100,80,81
C_ 101,82,83
Mavv 60,42,116
W_ 0
E_ 84
Mavv 80,30,116
s_nop 0
Tv 38,v4,13312
Tv 40,v4,19456
Mavv 100,34,116
E_ 85
Mavv 104,34,120
s_nop 0
Tv 42,v5,13312
Tv 44,v5,19456
Mavv 84,30,120
E_ 86
Mavv 88,30,124
E_ 87
Mavv 108,34,124
C_ 104,84,85
C_ 105,86,87
Mavv 112,34,128
E_ 88
Mavv 92,30,128
E_ 89
Mavv 96,30,132
E_ 90
Mavv 116,34,132
W_ 0
E_ 91
Mavv 136,38,132
s_nop 0
Tv 30,v2,14336
Tv 32,v2,20480
Mavv 156,42,132
C_ 108,88,89
C_ 109,90,91
Mavv 152,42,128
s_nop 0
Tv 34,v3,14336
Tv 36,v3,20480
Mavv 132,38,128
E_ 92
Mavv 128,38,124
E_ 93
Mavv 148,42,124
E_ 94
Mavv 144,42,120
E_ 95
Mavv 124,38,120
C_ 112,92,93
C_ 113,94,95
Mavv 120,38,116
v_mov_b32 v92, 0x3f803f80
v_mov_b32 v93, 0x3f803f80
Mavv 140,42,116
W_ 0
v_mov_b32 v94, 0x3f803f80
v_mov_b32 v95, 0x3f803f80
Mavv 160,30,116
s_nop 0
Tv 38,v4,14336
Tv 40,v4,20480
Mavv 180,34,116
Mavv 184,34,120
s_nop 0
Tv 42,v5,14336
Tv 44,v5,20480
Mavv 164,30,120
Mavv 168,30,124
Mavv 188,34,124
S_ s65,s61,49152
Mavv 192,34,128
S_ m0,s51,12288
Mavv 172,30,128
s_waitcnt vmcnt(6) lgkmcnt(0)
s_barrier
Mavv 176,30,132
Ln v7,32,s65,0
Mavv 196,34,132
Mavv 216,38,132
Dv 30,%0,30720
Mavv 236,42,132
Dv 34,v1,30720
Mavv 232,42,128
Ln v8,32,s65,3072
S_ m0,s51,13184
Mavv 212,38,128
Mavv 208,38,124
Ln v7,32,s65,128
Mavv 228,42,124
Mavv 224,42,120
Ln v8,32,s65,3200
S_ m0,s51,14080
Mavv 204,38,120
Mavv 200,38,116
Ln v7,32,s65,256
Mavv 220,42,116
s_cmp_eq_u32 s4, 0
s_cbranch_scc1 .Lhs_a1
.Lhs_a1_back:
.Lstep1_3:
Zvvv 76,92,96
Dv 38,%0,31744
Zvvv 80,92,100
Dv 42,v1,31744
Zvvv 84,92,104
Zvvv 88,92,108
Zvvv 92,92,112
W_ 2
Nvvv 116,30,136,56
Mvvv 116,34,156
Nvvv 120,30,140,60
s_nop 1
v_max3_f32 v10, v76, v80, v84
s_nop 0
v_max3_f32 v10, v10, v88, v92
Mvvv 120,34,160
v_cmp_lt_f32 vcc, 0x42000000, v10
s_cbranch_vccnz .Ltramp1_2
Nvvv 124,30,144,64
A_ v46,v46,v76
A_ v47,v47,v80
Mvvv 124,34,164
A_ v48,v48,v84
A_ v49,v49,v88
Nvvv 128,30,148,68
A_ v50,v50,v92
Mvvv 128,34,168
Ln v8,32,s65,3328
Nvvv 132,30,152,72
Mvvv 132,34,172
W_ 0
Mvvv 132,38,192
Dv 30,%0,32768
Mvvv 132,42,212
Dv 34,v1,32768
Mvvv 128,38,188
Mvvv 128,42,208
Mvvv 124,38,184
Mvvv 124,42,204
Mvvv 120,38,180
Mvvv 120,42,200
Mvvv 116,38,176
Mvvv 116,42,196
W_ 0
Mvvv 116,30,216
Dv 38,%0,24576
Mvvv 116,34,236
Dv 42,v1,24576
Mvvv 120,30,220
Mvvv 120,34,240
Mvvv 124,30,224
Mvvv 124,34,244
Mvvv 128,30,228
Mvvv 128,34,248
s_nop 0
E_ 116
Mvvv 132,30,232
E_ 117
Mvvv 132,34,252
W_ 0
E_ 118
Nvvv 76,38,136,56
Dv 30,%0,25600
Mvvv 76,42,156
Dv 34,v1,25600
Nvvv 80,38,140,60
E_ 119
Mvvv 80,42,160
C_ 119,118,119
C_ 118,116,117
Nvvv 84,38,144,64
E_ 120
Mvvv 84,42,164
E_ 121
Nvvv 88,38,148,68
E_ 122
Mvvv 88,42,168
E_ 123
Nvvv 92,38,152,72
C_ 123,122,123
C_ 122,120,121
Mvvv 92,42,172
W_ 0
E_ 124
Mvvv 92,30,192
Dv 38,%0,26624
Mvvv 92,34,212
Dv 42,v1,26624
Mvvv 88,30,188
E_ 125
Mvvv 88,34,208
E_ 126
Mvvv 84,30,184
S_ s65,s61,36864
E_ 127
Mvvv 84,34,204
S_ m0,s51,24576
C_ 127,126,127
C_ 126,124,125
Mvvv 80,30,180
s_waitcnt vmcnt(6) lgkmcnt(0)
s_barrier
Mvvv 80,34,200
L_ v9,36,s65,0
Mvvv 76,30,176
E_ 128
Mvvv 76,34,196
L_ v9,36,s65,3072
S_ m0,s51,25472
Mvvv 76,38,216
Tv 30,v2,0
Tv 32,v2,6144
Mvvv 76,42,236
L_ v9,36,s65,128
Mvvv 80,38,220
s_nop 0
Tv 34,v3,0
Tv 36,v3,6144
Mvvv 80,42,240
L_ v9,36,s65,3200
S_ m0,s51,26368
Mvvv 84,38,224
E_ 129
Mvvv 84,42,244
L_ v9,36,s65,256
Mvvv 88,38,228
E_ 130
Mvvv 88,42,248
L_ v9,36,s65,3328
Mvvv 92,38,232
E_ 131
Mvvv 92,42,252
W_ 0
C_ 131,130,131
C_ 130,128,129
Mavv 0,30,96
s_nop 0
Tv 38,v4,0
Tv 40,v4,6144
Mavv 20,34,96
E_ 132
Mavv 24,34,100
s_nop 0
Tv 42,v5,0
Tv 44,v5,6144
Mavv 4,30,100
E_ 133
Mavv 8,30,104
E_ 134
Mavv 28,34,104
E_ 135
Mavv 32,34,108
C_ 135,134,135
C_ 134,132,133
Mavv 12,30,108
E_ 76
Mavv 16,30,112
E_ 77
Mavv 36,34,112
W_ 0
E_ 78
Mavv 56,38,112
s_nop 0
Tv 30,v2,1024
Tv 32,v2,7168
Mavv 76,42,112
E_ 79
Mavv 72,42,108
s_nop 0
Tv 34,v3,1024
Tv 36,v3,7168
Mavv 52,38,108
C_ 116,76,77
C_ 117,78,79
Mavv 48,38,104
E_ 80
Mavv 68,42,104
E_ 81
Mavv 64,42,100
E_ 82
Mavv 44,38,100
E_ 83
Mavv 40,38,96
C_ 120,80,81
C_ 121,82,83
Mavv 60,42,96
W_ 0
E_ 84
Mavv 80,30,96
s_nop 0
Tv 38,v4,1024
Tv 40,v4,7168
Mavv 100,34,96
E_ 85
Mavv 104,34,100
s_nop 0
Tv 42,v5,1024
Tv 44,v5,7168
Mavv 84,30,100
E_ 86
Mavv 88,30,104
E_ 87
Mavv 108,34,104
C_ 124,84,85
C_ 125,86,87
Mavv 112,34,108
E_ 88
Mavv 92,30,108
E_ 89
Mavv 96,30,112
E_ 90
Mavv 116,34,112
W_ 0
E_ 91
Mavv 136,38,112
s_nop 0
Tv 30,v2,2048
Tv 32,v2,8192
Mavv 156,42,112
C_ 128,88,89
C_ 129,90,91
Mavv 152,42,108
s_nop 0
Tv 34,v3,2048
Tv 36,v3,8192
Mavv 132,38,108
E_ 92
Mavv 128,38,104
E_ 93
Mavv 148,42,104
E_ 94
Mavv 144,42,100
E_ 95
Mavv 124,38,100
C_ 132,92,93
C_ 133,94,95
Mavv 120,38,96
v_mov_b32 v92, 0x3f803f80
v_mov_b32 v93, 0x3f803f80
Mavv 140,42,96
W_ 0
v_mov_b32 v94, 0x3f803f80
v_mov_b32 v95, 0x3f803f80
Mavv 160,30,96
s_nop 0
Tv 38,v4,2048
Tv 40,v4,8192
Mavv 180,34,96
Mavv 184,34,100
s_nop 0
Tv 42,v5,2048
Tv 44,v5,8192
Mavv 164,30,100
Mavv 168,30,104
Mavv 188,34,104
S_ s65,s61,61440
Mavv 192,34,108
S_ m0,s51,0
Mavv 172,30,108
s_waitcnt vmcnt(6) lgkmcnt(0)
s_barrier
Mavv 176,30,112
Ln v7,32,s65,0
Mavv 196,34,112
Mavv 216,38,112
Dv 30,%0,18432
Mavv 236,42,112
Dv 34,v1,18432
Mavv 232,42,108
Ln v8,32,s65,3072
S_ m0,s51,896
Mavv 212,38,108
Mavv 208,38,104
Ln v7,32,s65,128
Mavv 228,42,104
Mavv 224,42,100
Ln v8,32,s65,3200
S_ m0,s51,1792
Mavv 204,38,100
Mavv 200,38,96
Ln v7,32,s65,256
Mavv 220,42,96
.Lstep1_4:
Zvvv 76,92,116
Dv 38,%0,19456
Zvvv 80,92,120
Dv 42,v1,19456
Zvvv 84,92,124
Zvvv 88,92,128
Zvvv 92,92,132
W_ 2
Nvvv 96,30,136,56
Mvvv 96,34,156
Nvvv 100,30,140,60
s_nop 1
v_max3_f32 v10, v76, v80, v84
s_nop 0
v_max3_f32 v10, v10, v88, v92
Mvvv 100,34,160
v_cmp_lt_f32 vcc, 0x42000000, v10
s_cbranch_vccnz .Ltramp1_3
Nvvv 104,30,144,64
A_ v46,v46,v76
A_ v47,v47,v80
Mvvv 104,34,164
A_ v48,v48,v84
A_ v49,v49,v88
Nvvv 108,30,148,68
A_ v50,v50,v92
Mvvv 108,34,168
Ln v8,32,s65,3328
Nvvv 112,30,152,72
Mvvv 112,34,172
W_ 0
Mvvv 112,38,192
Dv 30,%0,20480
Mvvv 112,42,212
Dv 34,v1,20480
Mvvv 108,38,188
Mvvv 108,42,208
Mvvv 104,38,184
Mvvv 104,42,204
Mvvv 100,38,180
Mvvv 100,42,200
Mvvv 96,38,176
Mvvv 96,42,196
W_ 0
Mvvv 96,30,216
Dv 38,%0,12288
Mvvv 96,34,236
Dv 42,v1,12288
Mvvv 100,30,220
Mvvv 100,34,240
Mvvv 104,30,224
Mvvv 104,34,244
Mvvv 108,30,228
Mvvv 108,34,248
s_nop 0
E_ 96
Mvvv 112,30,232
E_ 97
Mvvv 112,34,252
W_ 0
E_ 98
Nvvv 76,38,136,56
Dv 30,%0,13312
Mvvv 76,42,156
Dv 34,v1,13312
Nvvv 80,38,140,60
E_ 99
Mvvv 80,42,160
C_ 99,98,99
C_ 98,96,97
Nvvv 84,38,144,64
E_ 100
Mvvv 84,42,164
E_ 101
Nvvv 88,38,148,68
E_ 102
Mvvv 88,42,168
E_ 103
Nvvv 92,38,152,72
C_ 103,102,103
C_ 102,100,101
Mvvv 92,42,172
W_ 0
E_ 104
Mvvv 92,30,192
Dv 38,%0,14336
Mvvv 92,34,212
Dv 42,v1,14336
Mvvv 88,30,188
E_ 105
Mvvv 88,34,208
E_ 106
Mvvv 84,30,184
S_ s65,s61,49152
E_ 107
Mvvv 84,34,204
S_ m0,s51,12288
C_ 107,106,107
C_ 106,104,105
Mvvv 80,30,180
s_waitcnt vmcnt(6) lgkmcnt(0)
s_barrier
Mvvv 80,34,200
L_ v9,36,s65,0
Mvvv 76,30,176
E_ 108
Mvvv 76,34,196
L_ v9,36,s65,3072
S_ m0,s51,13184
Mvvv 76,38,216
Tv 30,v2,24576
Tv 32,v2,30720
Mvvv 76,42,236
L_ v9,36,s65,128
Mvvv 80,38,220
s_nop 0
Tv 34,v3,24576
Tv 36,v3,30720
Mvvv 80,42,240
L_ v9,36,s65,3200
S_ m0,s51,14080
Mvvv 84,38,224
E_ 109
Mvvv 84,42,244
L_ v9,36,s65,256
Mvvv 88,38,228
E_ 110
Mvvv 88,42,248
L_ v9,36,s65,3328
Mvvv 92,38,232
E_ 111
Mvvv 92,42,252
W_ 0
C_ 111,110,111
C_ 110,108,109
Mavv 0,30,116
s_nop 0
Tv 38,v4,24576
Tv 40,v4,30720
Mavv 20,34,116
E_ 112
Mavv 24,34,120
s_nop 0
Tv 42,v5,24576
Tv 44,v5,30720
Mavv 4,30,120
E_ 113
Mavv 8,30,124
E_ 114
Mavv 28,34,124
E_ 115
Mavv 32,34,128
C_ 115,114,115
C_ 114,112,113
Mavv 12,30,128
E_ 76
Mavv 16,30,132
E_ 77
Mavv 36,34,132
W_ 0
E_ 78
Mavv 56,38,132
s_nop 0
Tv 30,v2,25600
Tv 32,v2,31744
Mavv 76,42,132
E_ 79
Mavv 72,42,128
s_nop 0
Tv 34,v3,25600
Tv 36,v3,31744
Mavv 52,38,128
C_ 96,76,77
C_ 97,78,79
Mavv 48,38,124
E_ 80
Mavv 68,42,124
E_ 81
Mavv 64,42,120
E_ 82
Mavv 44,38,120
E_ 83
Mavv 40,38,116
C_ 100,80,81
C_ 101,82,83
Mavv 60,42,116
W_ 0
E_ 84
Mavv 80,30,116
s_nop 0
Tv 38,v4,25600
Tv 40,v4,31744
Mavv 100,34,116
E_ 85
Mavv 104,34,120
s_nop 0
Tv 42,v5,25600
Tv 44,v5,31744
Mavv 84,30,120
E_ 86
Mavv 88,30,124
E_ 87
Mavv 108,34,124
C_ 104,84,85
C_ 105,86,87
Mavv 112,34,128
E_ 88
Mavv 92,30,128
E_ 89
Mavv 96,30,132
E_ 90
Mavv 116,34,132
W_ 0
E_ 91
Mavv 136,38,132
s_nop 0
Tv 30,v2,26624
Tv 32,v2,32768
Mavv 156,42,132
C_ 108,88,89
C_ 109,90,91
Mavv 152,42,128
s_nop 0
Tv 34,v3,26624
Tv 36,v3,32768
Mavv 132,38,128
E_ 92
Mavv 128,38,124
E_ 93
Mavv 148,42,124
E_ 94
Mavv 144,42,120
E_ 95
Mavv 124,38,120
C_ 112,92,93
C_ 113,94,95
Mavv 120,38,116
v_mov_b32 v92, 0x3f803f80
v_mov_b32 v93, 0x3f803f80
Mavv 140,42,116
W_ 0
v_mov_b32 v94, 0x3f803f80
v_mov_b32 v95, 0x3f803f80
Mavv 160,30,116
s_nop 0
Tv 38,v4,26624
Tv 40,v4,32768
Mavv 180,34,116
Mavv 184,34,120
s_nop 0
Tv 42,v5,26624
Tv 44,v5,32768
Mavv 164,30,120
Mavv 168,30,124
Mavv 188,34,124
S_ s65,s61,73728
Mavv 192,34,128
S_ m0,s51,24576
Mavv 172,30,128
s_waitcnt vmcnt(6) lgkmcnt(0)
s_barrier
Mavv 176,30,132
Ln v7,32,s65,0
Mavv 196,34,132
Mavv 216,38,132
Dv 30,%0,6144
Mavv 236,42,132
Dv 34,v1,6144
Mavv 232,42,128
Ln v8,32,s65,3072
S_ m0,s51,25472
Mavv 212,38,128
Mavv 208,38,124
Ln v7,32,s65,128
Mavv 228,42,124
Mavv 224,42,120
Ln v8,32,s65,3200
S_ m0,s51,26368
Mavv 204,38,120
Mavv 200,38,116
Ln v7,32,s65,256
Mavv 220,42,116
s_cmp_eq_u32 s4, 0
s_cbranch_scc1 .Lhs_b1
.Lhs_b1_back:
.Lstep1_5:
Zvvv 76,92,96
Dv 38,%0,7168
Zvvv 80,92,100
Dv 42,v1,7168
Zvvv 84,92,104
Zvvv 88,92,108
Zvvv 92,92,112
W_ 2
Nvvv 116,30,136,56
Mvvv 116,34,156
Nvvv 120,30,140,60
s_nop 1
v_max3_f32 v10, v76, v80, v84
s_nop 0
v_max3_f32 v10, v10, v88, v92
Mvvv 120,34,160
v_cmp_lt_f32 vcc, 0x42000000, v10
s_cbranch_vccnz .Ltramp1_4
Nvvv 124,30,144,64
A_ v46,v46,v76
A_ v47,v47,v80
Mvvv 124,34,164
A_ v48,v48,v84
A_ v49,v49,v88
Nvvv 128,30,148,68
A_ v50,v50,v92
Mvvv 128,34,168
Ln v8,32,s65,3328
Nvvv 132,30,152,72
Mvvv 132,34,172
W_ 0
Mvvv 132,38,192
Dv 30,%0,8192
Mvvv 132,42,212
Dv 34,v1,8192
Mvvv 128,38,188
Mvvv 128,42,208
Mvvv 124,38,184
Mvvv 124,42,204
Mvvv 120,38,180
Mvvv 120,42,200
Mvvv 116,38,176
Mvvv 116,42,196
W_ 0
Mvvv 116,30,216
Dv 38,%0,0
Mvvv 116,34,236
Dv 42,v1,0
Mvvv 120,30,220
Mvvv 120,34,240
Mvvv 124,30,224
Mvvv 124,34,244
Mvvv 128,30,228
Mvvv 128,34,248
s_nop 0
E_ 116
Mvvv 132,30,232
E_ 117
Mvvv 132,34,252
W_ 0
E_ 118
Nvvv 76,38,136,56
Dv 30,%0,1024
Mvvv 76,42,156
Dv 34,v1,1024
Nvvv 80,38,140,60
E_ 119
Mvvv 80,42,160
C_ 119,118,119
C_ 118,116,117
Nvvv 84,38,144,64
E_ 120
Mvvv 84,42,164
E_ 121
Nvvv 88,38,148,68
E_ 122
Mvvv 88,42,168
E_ 123
Nvvv 92,38,152,72
C_ 123,122,123
C_ 122,120,121
Mvvv 92,42,172
W_ 0
E_ 124
Mvvv 92,30,192
Dv 38,%0,2048
Mvvv 92,34,212
Dv 42,v1,2048
Mvvv 88,30,188
E_ 125
Mvvv 88,34,208
E_ 126
Mvvv 84,30,184
S_ s65,s61,61440
E_ 127
Mvvv 84,34,204
S_ m0,s51,0
C_ 127,126,127
C_ 126,124,125
Mvvv 80,30,180
s_waitcnt vmcnt(6) lgkmcnt(0)
s_barrier
Mvvv 80,34,200
L_ v9,36,s65,0
Mvvv 76,30,176
E_ 128
Mvvv 76,34,196
L_ v9,36,s65,3072
S_ m0,s51,896
Mvvv 76,38,216
Tv 30,v2,12288
Tv 32,v2,18432
Mvvv 76,42,236
L_ v9,36,s65,128
Mvvv 80,38,220
s_nop 0
Tv 34,v3,12288
Tv 36,v3,18432
Mvvv 80,42,240
L_ v9,36,s65,3200
S_ m0,s51,1792
Mvvv 84,38,224
E_ 129
Mvvv 84,42,244
L_ v9,36,s65,256
Mvvv 88,38,228
E_ 130
Mvvv 88,42,248
L_ v9,36,s65,3328
Mvvv 92,38,232
E_ 131
Mvvv 92,42,252
W_ 0
C_ 131,130,131
C_ 130,128,129
Mavv 0,30,96
s_nop 0
Tv 38,v4,12288
Tv 40,v4,18432
Mavv 20,34,96
E_ 132
Mavv 24,34,100
s_nop 0
Tv 42,v5,12288
Tv 44,v5,18432
Mavv 4,30,100
E_ 133
Mavv 8,30,104
E_ 134
Mavv 28,34,104
E_ 135
Mavv 32,34,108
C_ 135,134,135
C_ 134,132,133
Mavv 12,30,108
E_ 76
Mavv 16,30,112
E_ 77
Mavv 36,34,112
W_ 0
E_ 78
Mavv 56,38,112
s_nop 0
Tv 30,v2,13312
Tv 32,v2,19456
Mavv 76,42,112
E_ 79
Mavv 72,42,108
s_nop 0
Tv 34,v3,13312
Tv 36,v3,19456
Mavv 52,38,108
C_ 116,76,77
C_ 117,78,79
Mavv 48,38,104
E_ 80
Mavv 68,42,104
E_ 81
Mavv 64,42,100
E_ 82
Mavv 44,38,100
E_ 83
Mavv 40,38,96
C_ 120,80,81
C_ 121,82,83
Mavv 60,42,96
W_ 0
E_ 84
Mavv 80,30,96
s_nop 0
Tv 38,v4,13312
Tv 40,v4,19456
Mavv 100,34,96
E_ 85
Mavv 104,34,100
s_nop 0
Tv 42,v5,13312
Tv 44,v5,19456
Mavv 84,30,100
E_ 86
Mavv 88,30,104
E_ 87
Mavv 108,34,104
C_ 124,84,85
C_ 125,86,87
Mavv 112,34,108
E_ 88
Mavv 92,30,108
E_ 89
Mavv 96,30,112
E_ 90
Mavv 116,34,112
W_ 0
E_ 91
Mavv 136,38,112
s_nop 0
Tv 30,v2,14336
Tv 32,v2,20480
Mavv 156,42,112
C_ 128,88,89
C_ 129,90,91
Mavv 152,42,108
s_nop 0
Tv 34,v3,14336
Tv 36,v3,20480
Mavv 132,38,108
E_ 92
Mavv 128,38,104
E_ 93
Mavv 148,42,104
E_ 94
Mavv 144,42,100
E_ 95
Mavv 124,38,100
C_ 132,92,93
C_ 133,94,95
Mavv 120,38,96
v_mov_b32 v92, 0x3f803f80
v_mov_b32 v93, 0x3f803f80
Mavv 140,42,96
W_ 0
v_mov_b32 v94, 0x3f803f80
v_mov_b32 v95, 0x3f803f80
Mavv 160,30,96
s_nop 0
Tv 38,v4,14336
Tv 40,v4,20480
Mavv 180,34,96
Mavv 184,34,100
s_nop 0
Tv 42,v5,14336
Tv 44,v5,20480
Mavv 164,30,100
Mavv 168,30,104
Mavv 188,34,104
S_ s65,s61,86016
Mavv 192,34,108
S_ m0,s51,12288
Mavv 172,30,108
s_waitcnt vmcnt(6) lgkmcnt(0)
s_barrier
Mavv 176,30,112
Ln v7,32,s65,0
Mavv 196,34,112
Mavv 216,38,112
Dv 30,%0,30720
Mavv 236,42,112
Dv 34,v1,30720
Mavv 232,42,108
Ln v8,32,s65,3072
S_ m0,s51,13184
Mavv 212,38,108
Mavv 208,38,104
Ln v7,32,s65,128
Mavv 228,42,104
Mavv 224,42,100
Ln v8,32,s65,3200
S_ m0,s51,14080
Mavv 204,38,100
Mavv 200,38,96
Ln v7,32,s65,256
Mavv 220,42,96
.Lstep1_6:
Zvvv 76,92,116
Dv 38,%0,31744
Zvvv 80,92,120
Dv 42,v1,31744
Zvvv 84,92,124
Zvvv 88,92,128
Zvvv 92,92,132
W_ 2
Nvvv 96,30,136,56
Mvvv 96,34,156
Nvvv 100,30,140,60
s_nop 1
v_max3_f32 v10, v76, v80, v84
s_nop 0
v_max3_f32 v10, v10, v88, v92
Mvvv 100,34,160
v_cmp_lt_f32 vcc, 0x42000000, v10
s_cbranch_vccnz .Ltramp1_5
Nvvv 104,30,144,64
A_ v46,v46,v76
A_ v47,v47,v80
Mvvv 104,34,164
A_ v48,v48,v84
A_ v49,v49,v88
Nvvv 108,30,148,68
A_ v50,v50,v92
Mvvv 108,34,168
Ln v8,32,s65,3328
Nvvv 112,30,152,72
R_ v11,a248
Mvvv 112,34,172
)A"
R"A(W_ 0
v_min_u32 v11, 240, v11
Mvvv 112,38,192
Dv 30,%0,32768
v_lshlrev_b32 v11, 3, v11
Mvvv 112,42,212
Dv 34,v1,32768
Mvvv 108,38,188
buffer_load_dword a254, v11, s[44:47], s66 offen
Mvvv 108,42,208
Mvvv 104,38,184
Mvvv 104,42,204
Mvvv 100,38,180
Mvvv 100,42,200
Mvvv 96,38,176
Mvvv 96,42,196
W_ 0
Mvvv 96,30,216
Dv 38,%0,24576
Mvvv 96,34,236
Dv 42,v1,24576
Mvvv 100,30,220
Mvvv 100,34,240
Mvvv 104,30,224
Mvvv 104,34,244
Mvvv 108,30,228
Mvvv 108,34,248
s_nop 0
E_ 96
Mvvv 112,30,232
E_ 97
Mvvv 112,34,252
W_ 0
E_ 98
Nvvv 76,38,136,56
Dv 30,%0,25600
Mvvv 76,42,156
Dv 34,v1,25600
Nvvv 80,38,140,60
E_ 99
Mvvv 80,42,160
C_ 99,98,99
C_ 98,96,97
Nvvv 84,38,144,64
E_ 100
Mvvv 84,42,164
E_ 101
Nvvv 88,38,148,68
E_ 102
Mvvv 88,42,168
E_ 103
Nvvv 92,38,152,72
C_ 103,102,103
C_ 102,100,101
Mvvv 92,42,172
W_ 0
E_ 104
Mvvv 92,30,192
Dv 38,%0,26624
Mvvv 92,34,212
Dv 42,v1,26624
Mvvv 88,30,188
E_ 105
Mvvv 88,34,208
E_ 106
Mvvv 84,30,184
S_ s65,s61,73728
E_ 107
Mvvv 84,34,204
S_ m0,s51,24576
C_ 107,106,107
C_ 106,104,105
Mvvv 80,30,180
s_waitcnt vmcnt(7) lgkmcnt(0)
s_barrier
Mvvv 80,34,200
L_ v9,36,s65,0
Mvvv 76,30,176
E_ 108
Mvvv 76,34,196
L_ v9,36,s65,3072
S_ m0,s51,25472
Mvvv 76,38,216
Tv 30,v2,0
Tv 32,v2,6144
Mvvv 76,42,236
L_ v9,36,s65,128
Mvvv 80,38,220
s_nop 0
Tv 34,v3,0
Tv 36,v3,6144
Mvvv 80,42,240
L_ v9,36,s65,3200
S_ m0,s51,26368
Mvvv 84,38,224
E_ 109
Mvvv 84,42,244
L_ v9,36,s65,256
Mvvv 88,38,228
E_ 110
Mvvv 88,42,248
L_ v9,36,s65,3328
Mvvv 92,38,232
E_ 111
Mvvv 92,42,252
W_ 0
C_ 111,110,111
C_ 110,108,109
Mavv 0,30,116
s_nop 0
Tv 38,v4,0
Tv 40,v4,6144
Mavv 20,34,116
E_ 112
Mavv 24,34,120
s_nop 0
Tv 42,v5,0
Tv 44,v5,6144
Mavv 4,30,120
E_ 113
Mavv 8,30,124
E_ 114
Mavv 28,34,124
E_ 115
Mavv 32,34,128
C_ 115,114,115
C_ 114,112,113
Mavv 12,30,128
E_ 76
Mavv 16,30,132
E_ 77
Mavv 36,34,132
W_ 0
E_ 78
Mavv 56,38,132
s_nop 0
Tv 30,v2,1024
Tv 32,v2,7168
Mavv 76,42,132
E_ 79
Mavv 72,42,128
s_nop 0
Tv 34,v3,1024
Tv 36,v3,7168
Mavv 52,38,128
C_ 96,76,77
C_ 97,78,79
Mavv 48,38,124
E_ 80
Mavv 68,42,124
E_ 81
Mavv 64,42,120
E_ 82
Mavv 44,38,120
E_ 83
Mavv 40,38,116
C_ 100,80,81
C_ 101,82,83
Mavv 60,42,116
W_ 0
E_ 84
Mavv 80,30,116
s_nop 0
Tv 38,v4,1024
Tv 40,v4,7168
Mavv 100,34,116
E_ 85
Mavv 104,34,120
s_nop 0
Tv 42,v5,1024
Tv 44,v5,7168
Mavv 84,30,120
E_ 86
Mavv 88,30,124
E_ 87
Mavv 108,34,124
C_ 104,84,85
C_ 105,86,87
Mavv 112,34,128
E_ 88
Mavv 92,30,128
E_ 89
Mavv 96,30,132
E_ 90
Mavv 116,34,132
W_ 0
E_ 91
Mavv 136,38,132
s_nop 0
Tv 30,v2,2048
Tv 32,v2,8192
Mavv 156,42,132
C_ 108,88,89
C_ 109,90,91
Mavv 152,42,128
s_nop 0
Tv 34,v3,2048
Tv 36,v3,8192
Mavv 132,38,128
E_ 92
Mavv 128,38,124
E_ 93
Mavv 148,42,124
E_ 94
Mavv 144,42,120
E_ 95
Mavv 124,38,120
C_ 112,92,93
C_ 113,94,95
Mavv 120,38,116
v_mov_b32 v92, 0x3f803f80
v_mov_b32 v93, 0x3f803f80
Mavv 140,42,116
W_ 0
v_mov_b32 v94, 0x3f803f80
v_mov_b32 v95, 0x3f803f80
Mavv 160,30,116
s_nop 0
Tv 38,v4,2048
Tv 40,v4,8192
Mavv 180,34,116
Mavv 184,34,120
s_nop 0
Tv 42,v5,2048
Tv 44,v5,8192
Mavv 164,30,120
Mavv 168,30,124
Mavv 188,34,124
S_ s65,s61,98304
Mavv 192,34,128
S_ m0,s51,0
Mavv 172,30,128
s_waitcnt vmcnt(7) lgkmcnt(0)
s_barrier
Mavv 176,30,132
Ln v7,32,s65,0
Mavv 196,34,132
Mavv 216,38,132
Dv 30,%0,18432
Mavv 236,42,132
Dv 34,v1,18432
Mavv 232,42,128
Ln v8,32,s65,3072
S_ m0,s51,896
Mavv 212,38,128
Mavv 208,38,124
Ln v7,32,s65,128
Mavv 228,42,124
Mavv 224,42,120
Ln v8,32,s65,3200
S_ m0,s51,1792
Mavv 204,38,120
Mavv 200,38,116
Ln v7,32,s65,256
Mavv 220,42,116
s_cmp_eq_u32 s4, 0
s_cbranch_scc1 .Lhs_c1
.Lhs_c1_back:
.Lstep1_7:
Zvvv 76,92,96
Dv 38,%0,19456
Zvvv 80,92,100
Dv 42,v1,19456
Zvvv 84,92,104
Zvvv 88,92,108
Zvvv 92,92,112
W_ 2
Nvvv 116,30,136,56
Mvvv 116,34,156
Nvvv 120,30,140,60
s_nop 1
v_max3_f32 v10, v76, v80, v84
s_nop 0
v_max3_f32 v10, v10, v88, v92
Mvvv 120,34,160
v_cmp_lt_f32 vcc, 0x42000000, v10
s_cbranch_vccnz .Ltramp1_6
Nvvv 124,30,144,64
A_ v46,v46,v76
A_ v47,v47,v80
Mvvv 124,34,164
A_ v48,v48,v84
A_ v49,v49,v88
Nvvv 128,30,148,68
A_ v50,v50,v92
Mvvv 128,34,168
Ln v8,32,s65,3328
Nvvv 132,30,152,72
Mvvv 132,34,172
W_ 0
Mvvv 132,38,192
Dv 30,%0,20480
Mvvv 132,42,212
Dv 34,v1,20480
Mvvv 128,38,188
Mvvv 128,42,208
Mvvv 124,38,184
Mvvv 124,42,204
Mvvv 120,38,180
Mvvv 120,42,200
Mvvv 116,38,176
Mvvv 116,42,196
W_ 0
Mvvv 116,30,216
Dv 38,%0,12288
Mvvv 116,34,236
Dv 42,v1,12288
Mvvv 120,30,220
Mvvv 120,34,240
Mvvv 124,30,224
Mvvv 124,34,244
Mvvv 128,30,228
Mvvv 128,34,248
s_nop 0
E_ 116
Mvvv 132,30,232
E_ 117
Mvvv 132,34,252
W_ 0
E_ 118
Nvvv 76,38,136,56
Dv 30,%0,13312
Mvvv 76,42,156
Dv 34,v1,13312
Nvvv 80,38,140,60
E_ 119
Mvvv 80,42,160
C_ 119,118,119
C_ 118,116,117
Nvvv 84,38,144,64
E_ 120
Mvvv 84,42,164
E_ 121
Nvvv 88,38,148,68
E_ 122
Mvvv 88,42,168
E_ 123
Nvvv 92,38,152,72
C_ 123,122,123
C_ 122,120,121
Mvvv 92,42,172
W_ 0
E_ 124
Mvvv 92,30,192
Dv 38,%0,14336
Mvvv 92,34,212
Dv 42,v1,14336
Mvvv 88,30,188
E_ 125
Mvvv 88,34,208
E_ 126
Mvvv 84,30,184
S_ s65,s61,86016
E_ 127
Mvvv 84,34,204
S_ m0,s51,12288
C_ 127,126,127
C_ 126,124,125
Mvvv 80,30,180
s_waitcnt vmcnt(6) lgkmcnt(0)
s_barrier
Mvvv 80,34,200
L_ v9,36,s65,0
Mvvv 76,30,176
E_ 128
Mvvv 76,34,196
L_ v9,36,s65,3072
S_ m0,s51,13184
Mvvv 76,38,216
Tv 30,v2,24576
Tv 32,v2,30720
Mvvv 76,42,236
L_ v9,36,s65,128
Mvvv 80,38,220
s_nop 0
Tv 34,v3,24576
Tv 36,v3,30720
Mvvv 80,42,240
L_ v9,36,s65,3200
S_ m0,s51,14080
Mvvv 84,38,224
E_ 129
Mvvv 84,42,244
L_ v9,36,s65,256
Mvvv 88,38,228
E_ 130
Mvvv 88,42,248
L_ v9,36,s65,3328
Mvvv 92,38,232
E_ 131
Mvvv 92,42,252
W_ 0
C_ 131,130,131
C_ 130,128,129
Mavv 0,30,96
s_nop 0
Tv 38,v4,24576
Tv 40,v4,30720
Mavv 20,34,96
E_ 132
Mavv 24,34,100
s_nop 0
Tv 42,v5,24576
Tv 44,v5,30720
Mavv 4,30,100
E_ 133
Mavv 8,30,104
E_ 134
Mavv 28,34,104
E_ 135
Mavv 32,34,108
C_ 135,134,135
C_ 134,132,133
Mavv 12,30,108
E_ 76
Mavv 16,30,112
E_ 77
Mavv 36,34,112
W_ 0
E_ 78
Mavv 56,38,112
s_nop 0
Tv 30,v2,25600
Tv 32,v2,31744
Mavv 76,42,112
E_ 79
Mavv 72,42,108
s_nop 0
Tv 34,v3,25600
Tv 36,v3,31744
Mavv 52,38,108
C_ 116,76,77
C_ 117,78,79
Mavv 48,38,104
E_ 80
Mavv 68,42,104
E_ 81
Mavv 64,42,100
E_ 82
Mavv 44,38,100
E_ 83
Mavv 40,38,96
C_ 120,80,81
C_ 121,82,83
Mavv 60,42,96
W_ 0
E_ 84
Mavv 80,30,96
s_nop 0
Tv 38,v4,25600
Tv 40,v4,31744
Mavv 100,34,96
E_ 85
Mavv 104,34,100
s_nop 0
Tv 42,v5,25600
Tv 44,v5,31744
Mavv 84,30,100
E_ 86
Mavv 88,30,104
E_ 87
Mavv 108,34,104
C_ 124,84,85
C_ 125,86,87
Mavv 112,34,108
E_ 88
Mavv 92,30,108
E_ 89
Mavv 96,30,112
E_ 90
Mavv 116,34,112
W_ 0
E_ 91
Mavv 136,38,112
s_nop 0
Tv 30,v2,26624
Tv 32,v2,32768
Mavv 156,42,112
C_ 128,88,89
C_ 129,90,91
Mavv 152,42,108
s_nop 0
Tv 34,v3,26624
Tv 36,v3,32768
Mavv 132,38,108
E_ 92
Mavv 128,38,104
E_ 93
Mavv 148,42,104
E_ 94
Mavv 144,42,100
E_ 95
Mavv 124,38,100
C_ 132,92,93
C_ 133,94,95
Mavv 120,38,96
v_mov_b32 v92, 0x3f803f80
v_mov_b32 v93, 0x3f803f80
Mavv 140,42,96
W_ 0
v_mov_b32 v94, 0x3f803f80
v_mov_b32 v95, 0x3f803f80
Mavv 160,30,96
s_nop 0
Tv 38,v4,26624
Tv 40,v4,32768
Mavv 180,34,96
Mavv 184,34,100
s_nop 0
Tv 42,v5,26624
Tv 44,v5,32768
Mavv 164,30,100
Mavv 168,30,104
Mavv 188,34,104
S_ s65,s61,110592
Mavv 192,34,108
S_ m0,s51,24576
Mavv 172,30,108
s_waitcnt vmcnt(6) lgkmcnt(0)
s_barrier
Mavv 176,30,112
Ln v7,32,s65,0
Mavv 196,34,112
Mavv 216,38,112
Dv 30,%0,6144
Mavv 236,42,112
Dv 34,v1,6144
Mavv 232,42,108
Ln v8,32,s65,3072
S_ m0,s51,25472
Mavv 212,38,108
Mavv 208,38,104
Ln v7,32,s65,128
Mavv 228,42,104
Mavv 224,42,100
Ln v8,32,s65,3200
S_ m0,s51,26368
Mavv 204,38,100
Mavv 200,38,96
Ln v7,32,s65,256
Mavv 220,42,96
.Lstep1_8:
Zvvv 76,92,116
Dv 38,%0,7168
Zvvv 80,92,120
Dv 42,v1,7168
Zvvv 84,92,124
Zvvv 88,92,128
Zvvv 92,92,132
W_ 2
Nvvv 96,30,136,56
Mvvv 96,34,156
Nvvv 100,30,140,60
s_nop 1
v_max3_f32 v10, v76, v80, v84
s_nop 0
v_max3_f32 v10, v10, v88, v92
Mvvv 100,34,160
v_cmp_lt_f32 vcc, 0x42000000, v10
s_cbranch_vccnz .Ltramp1_7
Nvvv 104,30,144,64
A_ v46,v46,v76
A_ v47,v47,v80
Mvvv 104,34,164
A_ v48,v48,v84
A_ v49,v49,v88
Nvvv 108,30,148,68
A_ v50,v50,v92
Mvvv 108,34,168
Ln v8,32,s65,3328
Nvvv 112,30,152,72
Mvvv 112,34,172
W_ 0
Mvvv 112,38,192
Dv 30,%0,8192
Mvvv 112,42,212
Dv 34,v1,8192
Mvvv 108,38,188
Mvvv 108,42,208
Mvvv 104,38,184
Mvvv 104,42,204
Mvvv 100,38,180
Mvvv 100,42,200
Mvvv 96,38,176
Mvvv 96,42,196
W_ 0
Mvvv 96,30,216
Dv 38,%0,0
Mvvv 96,34,236
Dv 42,v1,0
Mvvv 100,30,220
Mvvv 100,34,240
Mvvv 104,30,224
Mvvv 104,34,244
Mvvv 108,30,228
Mvvv 108,34,248
s_nop 0
E_ 96
Mvvv 112,30,232
E_ 97
Mvvv 112,34,252
W_ 0
E_ 98
Nvvv 76,38,136,56
Dv 30,%0,1024
Mvvv 76,42,156
Dv 34,v1,1024
Nvvv 80,38,140,60
E_ 99
Mvvv 80,42,160
C_ 99,98,99
C_ 98,96,97
Nvvv 84,38,144,64
E_ 100
Mvvv 84,42,164
E_ 101
Nvvv 88,38,148,68
E_ 102
Mvvv 88,42,168
E_ 103
Nvvv 92,38,152,72
C_ 103,102,103
C_ 102,100,101
Mvvv 92,42,172
W_ 0
E_ 104
Mvvv 92,30,192
Dv 38,%0,2048
Mvvv 92,34,212
Dv 42,v1,2048
Mvvv 88,30,188
E_ 105
Mvvv 88,34,208
E_ 106
Mvvv 84,30,184
S_ s65,s61,98304
E_ 107
Mvvv 84,34,204
S_ m0,s51,0
C_ 107,106,107
C_ 106,104,105
Mvvv 80,30,180
s_waitcnt vmcnt(6) lgkmcnt(0)
s_barrier
Mvvv 80,34,200
L_ v9,36,s65,0
Mvvv 76,30,176
E_ 108
Mvvv 76,34,196
L_ v9,36,s65,3072
S_ m0,s51,896
Mvvv 76,38,216
Tv 30,v2,12288
Tv 32,v2,18432
Mvvv 76,42,236
L_ v9,36,s65,128
Mvvv 80,38,220
s_nop 0
Tv 34,v3,12288
Tv 36,v3,18432
Mvvv 80,42,240
L_ v9,36,s65,3200
S_ m0,s51,1792
Mvvv 84,38,224
E_ 109
Mvvv 84,42,244
L_ v9,36,s65,256
Mvvv 88,38,228
E_ 110
Mvvv 88,42,248
L_ v9,36,s65,3328
Mvvv 92,38,232
E_ 111
Mvvv 92,42,252
W_ 0
C_ 111,110,111
C_ 110,108,109
Mavv 0,30,116
s_nop 0
Tv 38,v4,12288
Tv 40,v4,18432
Mavv 20,34,116
E_ 112
Mavv 24,34,120
s_nop 0
Tv 42,v5,12288
Tv 44,v5,18432
Mavv 4,30,120
E_ 113
Mavv 8,30,124
E_ 114
Mavv 28,34,124
E_ 115
Mavv 32,34,128
C_ 115,114,115
C_ 114,112,113
Mavv 12,30,128
E_ 76
Mavv 16,30,132
E_ 77
Mavv 36,34,132
W_ 0
E_ 78
Mavv 56,38,132
s_nop 0
Tv 30,v2,13312
Tv 32,v2,19456
Mavv 76,42,132
E_ 79
Mavv 72,42,128
s_nop 0
Tv 34,v3,13312
Tv 36,v3,19456
Mavv 52,38,128
C_ 96,76,77
C_ 97,78,79
Mavv 48,38,124
E_ 80
Mavv 68,42,124
E_ 81
Mavv 64,42,120
E_ 82
Mavv 44,38,120
E_ 83
Mavv 40,38,116
C_ 100,80,81
C_ 101,82,83
Mavv 60,42,116
W_ 0
E_ 84
Mavv 80,30,116
s_nop 0
Tv 38,v4,13312
Tv 40,v4,19456
Mavv 100,34,116
E_ 85
Mavv 104,34,120
s_nop 0
Tv 42,v5,13312
Tv 44,v5,19456
Mavv 84,30,120
E_ 86
Mavv 88,30,124
E_ 87
Mavv 108,34,124
C_ 104,84,85
C_ 105,86,87
Mavv 112,34,128
E_ 88
Mavv 92,30,128
E_ 89
Mavv 96,30,132
E_ 90
Mavv 116,34,132
W_ 0
E_ 91
Mavv 136,38,132
s_nop 0
Tv 30,v2,14336
Tv 32,v2,20480
Mavv 156,42,132
C_ 108,88,89
C_ 109,90,91
Mavv 152,42,128
s_nop 0
Tv 34,v3,14336
Tv 36,v3,20480
Mavv 132,38,128
E_ 92
Mavv 128,38,124
E_ 93
Mavv 148,42,124
E_ 94
Mavv 144,42,120
E_ 95
Mavv 124,38,120
C_ 112,92,93
C_ 113,94,95
Mavv 120,38,116
v_mov_b32 v92, 0x3f803f80
v_mov_b32 v93, 0x3f803f80
Mavv 140,42,116
W_ 0
v_mov_b32 v94, 0x3f803f80
v_mov_b32 v95, 0x3f803f80
Mavv 160,30,116
s_nop 0
Tv 38,v4,14336
Tv 40,v4,20480
Mavv 180,34,116
Mavv 184,34,120
s_nop 0
Tv 42,v5,14336
Tv 44,v5,20480
Mavv 164,30,120
Mavv 168,30,124
Mavv 188,34,124
S_ s65,s61,122880
Mavv 192,34,128
S_ m0,s51,12288
Mavv 172,30,128
s_waitcnt vmcnt(6) lgkmcnt(0)
s_barrier
Mavv 176,30,132
Ln v7,32,s65,0
Mavv 196,34,132
Mavv 216,38,132
Dv 30,%0,30720
Mavv 236,42,132
Dv 34,v1,30720
Mavv 232,42,128
Ln v8,32,s65,3072
S_ m0,s51,13184
Mavv 212,38,128
Mavv 208,38,124
Ln v7,32,s65,128
Mavv 228,42,124
Mavv 224,42,120
Ln v8,32,s65,3200
S_ m0,s51,14080
Mavv 204,38,120
Mavv 200,38,116
Ln v7,32,s65,256
Mavv 220,42,116
s_cmp_eq_u32 s4, 0
s_cbranch_scc1 .Lhs_d1
.Lhs_d1_back:
.Lstep1_9:
Zvvv 76,92,96
Dv 38,%0,31744
Zvvv 80,92,100
Dv 42,v1,31744
Zvvv 84,92,104
Zvvv 88,92,108
Zvvv 92,92,112
W_ 2
Nvvv 116,30,136,56
Mvvv 116,34,156
Nvvv 120,30,140,60
s_nop 1
v_max3_f32 v10, v76, v80, v84
s_nop 0
v_max3_f32 v10, v10, v88, v92
Mvvv 120,34,160
v_cmp_lt_f32 vcc, 0x42000000, v10
s_cbranch_vccnz .Ltramp1_8
Nvvv 124,30,144,64
A_ v46,v46,v76
A_ v47,v47,v80
Mvvv 124,34,164
A_ v48,v48,v84
A_ v49,v49,v88
Nvvv 128,30,148,68
A_ v50,v50,v92
Mvvv 128,34,168
Ln v8,32,s65,3328
Nvvv 132,30,152,72
Mvvv 132,34,172
W_ 0
Mvvv 132,38,192
Dv 30,%0,32768
Mvvv 132,42,212
Dv 34,v1,32768
Mvvv 128,38,188
Mvvv 128,42,208
Mvvv 124,38,184
Mvvv 124,42,204
Mvvv 120,38,180
Mvvv 120,42,200
Mvvv 116,38,176
Mvvv 116,42,196
W_ 0
Mvvv 116,30,216
Dv 38,%0,24576
Mvvv 116,34,236
Dv 42,v1,24576
Mvvv 120,30,220
Mvvv 120,34,240
Mvvv 124,30,224
Mvvv 124,34,244
Mvvv 128,30,228
Mvvv 128,34,248
s_nop 0
E_ 116
Mvvv 132,30,232
E_ 117
Mvvv 132,34,252
W_ 0
E_ 118
Nvvv 76,38,136,56
Dv 30,%0,25600
Mvvv 76,42,156
Dv 34,v1,25600
Nvvv 80,38,140,60
E_ 119
Mvvv 80,42,160
C_ 119,118,119
C_ 118,116,117
Nvvv 84,38,144,64
E_ 120
Mvvv 84,42,164
E_ 121
Nvvv 88,38,148,68
E_ 122
Mvvv 88,42,168
E_ 123
Nvvv 92,38,152,72
C_ 123,122,123
C_ 122,120,121
Mvvv 92,42,172
W_ 0
E_ 124
Mvvv 92,30,192
Dv 38,%0,26624
Mvvv 92,34,212
Dv 42,v1,26624
Mvvv 88,30,188
E_ 125
Mvvv 88,34,208
E_ 126
Mvvv 84,30,184
S_ s65,s61,110592
E_ 127
Mvvv 84,34,204
S_ m0,s51,24576
C_ 127,126,127
C_ 126,124,125
Mvvv 80,30,180
s_waitcnt vmcnt(6) lgkmcnt(0)
s_barrier
Mvvv 80,34,200
L_ v9,36,s65,0
Mvvv 76,30,176
E_ 128
Mvvv 76,34,196
L_ v9,36,s65,3072
S_ m0,s51,25472
Mvvv 76,38,216
Tv 30,v2,0
Tv 32,v2,6144
Mvvv 76,42,236
L_ v9,36,s65,128
Mvvv 80,38,220
s_nop 0
Tv 34,v3,0
Tv 36,v3,6144
Mvvv 80,42,240
L_ v9,36,s65,3200
S_ m0,s51,26368
Mvvv 84,38,224
E_ 129
Mvvv 84,42,244
L_ v9,36,s65,256
Mvvv 88,38,228
E_ 130
Mvvv 88,42,248
L_ v9,36,s65,3328
Mvvv 92,38,232
E_ 131
Mvvv 92,42,252
W_ 0
C_ 131,130,131
C_ 130,128,129
Mavv 0,30,96
s_nop 0
Tv 38,v4,0
Tv 40,v4,6144
Mavv 20,34,96
E_ 132
Mavv 24,34,100
s_nop 0
Tv 42,v5,0
Tv 44,v5,6144
Mavv 4,30,100
E_ 133
Mavv 8,30,104
E_ 134
Mavv 28,34,104
E_ 135
Mavv 32,34,108
C_ 135,134,135
C_ 134,132,133
Mavv 12,30,108
E_ 76
Mavv 16,30,112
E_ 77
Mavv 36,34,112
W_ 0
E_ 78
Mavv 56,38,112
s_nop 0
Tv 30,v2,1024
Tv 32,v2,7168
Mavv 76,42,112
E_ 79
Mavv 72,42,108
s_nop 0
Tv 34,v3,1024
Tv 36,v3,7168
Mavv 52,38,108
C_ 116,76,77
C_ 117,78,79
Mavv 48,38,104
E_ 80
Mavv 68,42,104
E_ 81
Mavv 64,42,100
E_ 82
Mavv 44,38,100
E_ 83
Mavv 40,38,96
C_ 120,80,81
C_ 121,82,83
Mavv 60,42,96
W_ 0
E_ 84
Mavv 80,30,96
s_nop 0
Tv 38,v4,1024
Tv 40,v4,7168
Mavv 100,34,96
E_ 85
Mavv 104,34,100
s_nop 0
Tv 42,v5,1024
Tv 44,v5,7168
Mavv 84,30,100
E_ 86
Mavv 88,30,104
E_ 87
Mavv 108,34,104
C_ 124,84,85
C_ 125,86,87
Mavv 112,34,108
E_ 88
Mavv 92,30,108
E_ 89
Mavv 96,30,112
E_ 90
Mavv 116,34,112
W_ 0
E_ 91
Mavv 136,38,112
s_nop 0
Tv 30,v2,2048
Tv 32,v2,8192
Mavv 156,42,112
C_ 128,88,89
C_ 129,90,91
Mavv 152,42,108
s_nop 0
Tv 34,v3,2048
Tv 36,v3,8192
Mavv 132,38,108
E_ 92
Mavv 128,38,104
E_ 93
Mavv 148,42,104
E_ 94
Mavv 144,42,100
E_ 95
Mavv 124,38,100
C_ 132,92,93
C_ 133,94,95
Mavv 120,38,96
v_mov_b32 v92, 0x3f803f80
v_mov_b32 v93, 0x3f803f80
Mavv 140,42,96
W_ 0
v_mov_b32 v94, 0x3f803f80
v_mov_b32 v95, 0x3f803f80
Mavv 160,30,96
s_nop 0
Tv 38,v4,2048
Tv 40,v4,8192
Mavv 180,34,96
Mavv 184,34,100
s_nop 0
Tv 42,v5,2048
Tv 44,v5,8192
Mavv 164,30,100
Mavv 168,30,104
Mavv 188,34,104
S_ s65,s61,135168
Mavv 192,34,108
S_ m0,s51,0
Mavv 172,30,108
s_waitcnt vmcnt(6) lgkmcnt(0)
s_barrier
Mavv 176,30,112
Ln v7,32,s65,0
Mavv 196,34,112
Mavv 216,38,112
Dv 30,%0,18432
Mavv 236,42,112
Dv 34,v1,18432
Mavv 232,42,108
Ln v8,32,s65,3072
S_ m0,s51,896
Mavv 212,38,108
Mavv 208,38,104
Ln v7,32,s65,128
Mavv 228,42,104
Mavv 224,42,100
Ln v8,32,s65,3200
S_ m0,s51,1792
Mavv 204,38,100
Mavv 200,38,96
Ln v7,32,s65,256
Mavv 220,42,96
.Lstep1_10:
Zvvv 76,92,116
Dv 38,%0,19456
Zvvv 80,92,120
Dv 42,v1,19456
Zvvv 84,92,124
Zvvv 88,92,128
Zvvv 92,92,132
W_ 2
Nvvv 96,30,136,56
Mvvv 96,34,156
Nvvv 100,30,140,60
s_nop 1
v_max3_f32 v10, v76, v80, v84
s_nop 0
v_max3_f32 v10, v10, v88, v92
Mvvv 100,34,160
v_cmp_lt_f32 vcc, 0x42000000, v10
s_cbranch_vccnz .Ltramp1_9
Nvvv 104,30,144,64
A_ v46,v46,v76
A_ v47,v47,v80
Mvvv 104,34,164
A_ v48,v48,v84
A_ v49,v49,v88
Nvvv 108,30,148,68
A_ v50,v50,v92
Mvvv 108,34,168
Ln v8,32,s65,3328
Nvvv 112,30,152,72
Mvvv 112,34,172
W_ 0
Mvvv 112,38,192
Dv 30,%0,20480
Mvvv 112,42,212
Dv 34,v1,20480
Mvvv 108,38,188
Mvvv 108,42,208
Mvvv 104,38,184
Mvvv 104,42,204
Mvvv 100,38,180
Mvvv 100,42,200
Mvvv 96,38,176
Mvvv 96,42,196
W_ 0
Mvvv 96,30,216
Dv 38,%0,12288
Mvvv 96,34,236
Dv 42,v1,12288
Mvvv 100,30,220
Mvvv 100,34,240
Mvvv 104,30,224
Mvvv 104,34,244
Mvvv 108,30,228
Mvvv 108,34,248
s_nop 0
E_ 96
Mvvv 112,30,232
E_ 97
Mvvv 112,34,252
W_ 0
E_ 98
Nvvv 76,38,136,56
Dv 30,%0,13312
Mvvv 76,42,156
Dv 34,v1,13312
Nvvv 80,38,140,60
E_ 99
Mvvv 80,42,160
C_ 99,98,99
C_ 98,96,97
Nvvv 84,38,144,64
E_ 100
Mvvv 84,42,164
E_ 101
Nvvv 88,38,148,68
E_ 102
Mvvv 88,42,168
E_ 103
Nvvv 92,38,152,72
C_ 103,102,103
C_ 102,100,101
Mvvv 92,42,172
W_ 0
E_ 104
Mvvv 92,30,192
Dv 38,%0,14336
Mvvv 92,34,212
Dv 42,v1,14336
Mvvv 88,30,188
E_ 105
Mvvv 88,34,208
E_ 106
Mvvv 84,30,184
S_ s65,s61,122880
E_ 107
Mvvv 84,34,204
S_ m0,s51,12288
C_ 107,106,107
C_ 106,104,105
Mvvv 80,30,180
s_waitcnt vmcnt(6) lgkmcnt(0)
s_barrier
Mvvv 80,34,200
L_ v9,36,s65,0
Mvvv 76,30,176
E_ 108
Mvvv 76,34,196
L_ v9,36,s65,3072
S_ m0,s51,13184
Mvvv 76,38,216
Tv 30,v2,24576
Tv 32,v2,30720
Mvvv 76,42,236
L_ v9,36,s65,128
Mvvv 80,38,220
s_nop 0
Tv 34,v3,24576
Tv 36,v3,30720
Mvvv 80,42,240
L_ v9,36,s65,3200
S_ m0,s51,14080
Mvvv 84,38,224
E_ 109
Mvvv 84,42,244
L_ v9,36,s65,256
Mvvv 88,38,228
E_ 110
Mvvv 88,42,248
L_ v9,36,s65,3328
Mvvv 92,38,232
E_ 111
Mvvv 92,42,252
W_ 0
C_ 111,110,111
C_ 110,108,109
Mavv 0,30,116
s_nop 0
Tv 38,v4,24576
Tv 40,v4,30720
Mavv 20,34,116
E_ 112
Mavv 24,34,120
s_nop 0
Tv 42,v5,24576
Tv 44,v5,30720
Mavv 4,30,120
E_ 113
Mavv 8,30,124
E_ 114
Mavv 28,34,124
E_ 115
Mavv 32,34,128
C_ 115,114,115
C_ 114,112,113
Mavv 12,30,128
E_ 76
Mavv 16,30,132
E_ 77
Mavv 36,34,132
W_ 0
E_ 78
Mavv 56,38,132
s_nop 0
Tv 30,v2,25600
Tv 32,v2,31744
Mavv 76,42,132
E_ 79
Mavv 72,42,128
s_nop 0
Tv 34,v3,25600
Tv 36,v3,31744
Mavv 52,38,128
C_ 96,76,77
C_ 97,78,79
Mavv 48,38,124
E_ 80
Mavv 68,42,124
E_ 81
Mavv 64,42,120
E_ 82
Mavv 44,38,120
E_ 83
Mavv 40,38,116
C_ 100,80,81
C_ 101,82,83
Mavv 60,42,116
W_ 0
E_ 84
Mavv 80,30,116
s_nop 0
Tv 38,v4,25600
Tv 40,v4,31744
Mavv 100,34,116
E_ 85
Mavv 104,34,120
s_nop 0
Tv 42,v5,25600
Tv 44,v5,31744
Mavv 84,30,120
E_ 86
Mavv 88,30,124
E_ 87
Mavv 108,34,124
C_ 104,84,85
C_ 105,86,87
Mavv 112,34,128
E_ 88
Mavv 92,30,128
E_ 89
Mavv 96,30,132
E_ 90
Mavv 116,34,132
W_ 0
E_ 91
Mavv 136,38,132
s_nop 0
Tv 30,v2,26624
Tv 32,v2,32768
Mavv 156,42,132
C_ 108,88,89
C_ 109,90,91
Mavv 152,42,128
s_nop 0
Tv 34,v3,26624
Tv 36,v3,32768
Mavv 132,38,128
E_ 92
Mavv 128,38,124
E_ 93
Mavv 148,42,124
E_ 94
Mavv 144,42,120
E_ 95
Mavv 124,38,120
C_ 112,92,93
C_ 113,94,95
Mavv 120,38,116
v_mov_b32 v92, 0x3f803f80
v_mov_b32 v93, 0x3f803f80
Mavv 140,42,116
W_ 0
v_mov_b32 v94, 0x3f803f80
v_mov_b32 v95, 0x3f803f80
Mavv 160,30,116
s_nop 0
Tv 38,v4,26624
Tv 40,v4,32768
Mavv 180,34,116
Mavv 184,34,120
s_nop 0
Tv 42,v5,26624
Tv 44,v5,32768
Mavv 164,30,120
Mavv 168,30,124
Mavv 188,34,124
S_ s65,s61,147456
Mavv 192,34,128
S_ m0,s51,24576
Mavv 172,30,128
s_waitcnt vmcnt(6) lgkmcnt(0)
s_barrier
Mavv 176,30,132
Ln v7,32,s65,0
Mavv 196,34,132
Mavv 216,38,132
Dv 30,%0,6144
Mavv 236,42,132
Dv 34,v1,6144
Mavv 232,42,128
Ln v8,32,s65,3072
S_ m0,s51,25472
Mavv 212,38,128
Mavv 208,38,124
Ln v7,32,s65,128
Mavv 228,42,124
Mavv 224,42,120
Ln v8,32,s65,3200
S_ m0,s51,26368
Mavv 204,38,120
Mavv 200,38,116
Ln v7,32,s65,256
Mavv 220,42,116
.Lstep1_11:
Zvvv 76,92,96
Dv 38,%0,7168
Zvvv 80,92,100
Dv 42,v1,7168
Zvvv 84,92,104
Zvvv 88,92,108
Zvvv 92,92,112
W_ 2
Nvvv 116,30,136,56
Mvvv 116,34,156
Nvvv 120,30,140,60
s_nop 1
v_max3_f32 v10, v76, v80, v84
s_nop 0
v_max3_f32 v10, v10, v88, v92
Mvvv 120,34,160
v_cmp_lt_f32 vcc, 0x42000000, v10
s_cbranch_vccnz .Ltramp1_10
Nvvv 124,30,144,64
A_ v46,v46,v76
A_ v47,v47,v80
Mvvv 124,34,164
A_ v48,v48,v84
A_ v49,v49,v88
Nvvv 128,30,148,68
A_ v50,v50,v92
Mvvv 128,34,168
Ln v8,32,s65,3328
Nvvv 132,30,152,72
Mvvv 132,34,172
W_ 0
Mvvv 132,38,192
Dv 30,%0,8192
Mvvv 132,42,212
Dv 34,v1,8192
Mvvv 128,38,188
Mvvv 128,42,208
Mvvv 124,38,184
Mvvv 124,42,204
Mvvv 120,38,180
Mvvv 120,42,200
Mvvv 116,38,176
Mvvv 116,42,196
W_ 0
Mvvv 116,30,216
Dv 38,%0,0
Mvvv 116,34,236
Dv 42,v1,0
Mvvv 120,30,220
Mvvv 120,34,240
Mvvv 124,30,224
Mvvv 124,34,244
Mvvv 128,30,228
Mvvv 128,34,248
s_nop 0
E_ 116
Mvvv 132,30,232
E_ 117
Mvvv 132,34,252
W_ 0
E_ 118
Nvvv 76,38,136,56
Dv 30,%0,1024
Mvvv 76,42,156
Dv 34,v1,1024
Nvvv 80,38,140,60
E_ 119
Mvvv 80,42,160
C_ 119,118,119
C_ 118,116,117
Nvvv 84,38,144,64
E_ 120
Mvvv 84,42,164
E_ 121
Nvvv 88,38,148,68
E_ 122
Mvvv 88,42,168
E_ 123
Nvvv 92,38,152,72
C_ 123,122,123
C_ 122,120,121
Mvvv 92,42,172
W_ 0
E_ 124
Mvvv 92,30,192
Dv 38,%0,2048
Mvvv 92,34,212
Dv 42,v1,2048
Mvvv 88,30,188
E_ 125
Mvvv 88,34,208
E_ 126
Mvvv 84,30,184
S_ s65,s61,135168
E_ 127
Mvvv 84,34,204
S_ m0,s51,0
C_ 127,126,127
C_ 126,124,125
Mvvv 80,30,180
s_waitcnt vmcnt(6) lgkmcnt(0)
s_barrier
Mvvv 80,34,200
L_ v9,36,s65,0
Mvvv 76,30,176
E_ 128
Mvvv 76,34,196
L_ v9,36,s65,3072
S_ m0,s51,896
Mvvv 76,38,216
Tv 30,v2,12288
Tv 32,v2,18432
Mvvv 76,42,236
L_ v9,36,s65,128
Mvvv 80,38,220
s_nop 0
Tv 34,v3,12288
Tv 36,v3,18432
Mvvv 80,42,240
L_ v9,36,s65,3200
S_ m0,s51,1792
Mvvv 84,38,224
E_ 129
Mvvv 84,42,244
L_ v9,36,s65,256
Mvvv 88,38,228
E_ 130
Mvvv 88,42,248
L_ v9,36,s65,3328
Mvvv 92,38,232
E_ 131
Mvvv 92,42,252
W_ 0
C_ 131,130,131
C_ 130,128,129
Mavv 0,30,96
s_nop 0
Tv 38,v4,12288
Tv 40,v4,18432
Mavv 20,34,96
E_ 132
Mavv 24,34,100
s_nop 0
Tv 42,v5,12288
Tv 44,v5,18432
Mavv 4,30,100
E_ 133
Mavv 8,30,104
E_ 134
Mavv 28,34,104
E_ 135
Mavv 32,34,108
C_ 135,134,135
C_ 134,132,133
Mavv 12,30,108
E_ 76
Mavv 16,30,112
E_ 77
Mavv 36,34,112
W_ 0
E_ 78
Mavv 56,38,112
s_nop 0
Tv 30,v2,13312
Tv 32,v2,19456
Mavv 76,42,112
E_ 79
Mavv 72,42,108
s_nop 0
Tv 34,v3,13312
Tv 36,v3,19456
Mavv 52,38,108
C_ 116,76,77
C_ 117,78,79
Mavv 48,38,104
E_ 80
Mavv 68,42,104
E_ 81
Mavv 64,42,100
E_ 82
Mavv 44,38,100
E_ 83
Mavv 40,38,96
C_ 120,80,81
C_ 121,82,83
Mavv 60,42,96
W_ 0
E_ 84
Mavv 80,30,96
s_nop 0
Tv 38,v4,13312
Tv 40,v4,19456
Mavv 100,34,96
E_ 85
Mavv 104,34,100
s_nop 0
Tv 42,v5,13312
Tv 44,v5,19456
Mavv 84,30,100
E_ 86
Mavv 88,30,104
E_ 87
Mavv 108,34,104
C_ 124,84,85
C_ 125,86,87
Mavv 112,34,108
E_ 88
Mavv 92,30,108
E_ 89
Mavv 96,30,112
E_ 90
Mavv 116,34,112
W_ 0
E_ 91
Mavv 136,38,112
s_nop 0
Tv 30,v2,14336
Tv 32,v2,20480
Mavv 156,42,112
C_ 128,88,89
C_ 129,90,91
Mavv 152,42,108
s_nop 0
Tv 34,v3,14336
Tv 36,v3,20480
Mavv 132,38,108
E_ 92
Mavv 128,38,104
E_ 93
Mavv 148,42,104
E_ 94
Mavv 144,42,100
E_ 95
Mavv 124,38,100
C_ 132,92,93
C_ 133,94,95
Mavv 120,38,96
v_mov_b32 v92, 0x3f803f80
v_mov_b32 v93, 0x3f803f80
Mavv 140,42,96
W_ 0
v_mov_b32 v94, 0x3f803f80
v_mov_b32 v95, 0x3f803f80
Mavv 160,30,96
s_nop 0
Tv 38,v4,14336
Tv 40,v4,20480
Mavv 180,34,96
Mavv 184,34,100
s_nop 0
Tv 42,v5,14336
Tv 44,v5,20480
Mavv 164,30,100
Mavv 168,30,104
Mavv 188,34,104
S_ s65,s61,159744
Mavv 192,34,108
S_ m0,s51,12288
Mavv 172,30,108
s_waitcnt vmcnt(6) lgkmcnt(0)
s_barrier
Mavv 176,30,112
Ln v7,32,s65,0
Mavv 196,34,112
Mavv 216,38,112
Dv 30,%0,30720
Mavv 236,42,112
Dv 34,v1,30720
Mavv 232,42,108
Ln v8,32,s65,3072
S_ m0,s51,13184
Mavv 212,38,108
Mavv 208,38,104
Ln v7,32,s65,128
Mavv 228,42,104
Mavv 224,42,100
Ln v8,32,s65,3200
S_ m0,s51,14080
Mavv 204,38,100
Mavv 200,38,96
Ln v7,32,s65,256
Mavv 220,42,96
.Lstep1_12:
Zvvv 76,92,116
Dv 38,%0,31744
Zvvv 80,92,120
Dv 42,v1,31744
Zvvv 84,92,124
Zvvv 88,92,128
Zvvv 92,92,132
W_ 2
Nvvv 96,30,136,56
Mvvv 96,34,156
Nvvv 100,30,140,60
s_nop 1
v_max3_f32 v10, v76, v80, v84
s_nop 0
v_max3_f32 v10, v10, v88, v92
Mvvv 100,34,160
v_cmp_lt_f32 vcc, 0x42000000, v10
s_cbranch_vccnz .Ltramp1_11
Nvvv 104,30,144,64
A_ v46,v46,v76
A_ v47,v47,v80
Mvvv 104,34,164
A_ v48,v48,v84
A_ v49,v49,v88
Nvvv 108,30,148,68
A_ v50,v50,v92
Mvvv 108,34,168
Ln v8,32,s65,3328
Nvvv 112,30,152,72
R_ v11,a248
Mvvv 112,34,172
W_ 0
v_min_u32 v11, 240, v11
Mvvv 112,38,192
Dv 30,%0,32768
v_lshlrev_b32 v11, 3, v11
Mvvv 112,42,212
Dv 34,v1,32768
Mvvv 108,38,188
buffer_load_dword a254, v11, s[44:47], s66 offen
Mvvv 108,42,208
Mvvv 104,38,184
Mvvv 104,42,204
Mvvv 100,38,180
Mvvv 100,42,200
Mvvv 96,38,176
Mvvv 96,42,196
W_ 0
Mvvv 96,30,216
Dv 38,%0,24576
Mvvv 96,34,236
Dv 42,v1,24576
Mvvv 100,30,220
Mvvv 100,34,240
Mvvv 104,30,224
Mvvv 104,34,244
Mvvv 108,30,228
Mvvv 108,34,248
s_nop 0
E_ 96
Mvvv 112,30,232
E_ 97
Mvvv 112,34,252
W_ 0
E_ 98
Nvvv 76,38,136,56
Dv 30,%0,25600
Mvvv 76,42,156
Dv 34,v1,25600
Nvvv 80,38,140,60
E_ 99
Mvvv 80,42,160
C_ 99,98,99
C_ 98,96,97
Nvvv 84,38,144,64
E_ 100
Mvvv 84,42,164
E_ 101
Nvvv 88,38,148,68
E_ 102
Mvvv 88,42,168
E_ 103
Nvvv 92,38,152,72
C_ 103,102,103
C_ 102,100,101
Mvvv 92,42,172
W_ 0
E_ 104
Mvvv 92,30,192
Dv 38,%0,26624
Mvvv 92,34,212
Dv 42,v1,26624
Mvvv 88,30,188
E_ 105
Mvvv 88,34,208
E_ 106
Mvvv 84,30,184
S_ s65,s61,147456
E_ 107
Mvvv 84,34,204
S_ m0,s51,24576
C_ 107,106,107
C_ 106,104,105
Mvvv 80,30,180
s_waitcnt vmcnt(7) lgkmcnt(0)
s_barrier
Mvvv 80,34,200
Ln v9,36,s65,0
Mvvv 76,30,176
E_ 108
Mvvv 76,34,196
Ln v9,36,s65,3072
S_ m0,s51,25472
Mvvv 76,38,216
Tv 30,v2,0
Tv 32,v2,6144
Mvvv 76,42,236
Ln v9,36,s65,128
Mvvv 80,38,220
s_nop 0
Tv 34,v3,0
Tv 36,v3,6144
Mvvv 80,42,240
Ln v9,36,s65,3200
S_ m0,s51,26368
Mvvv 84,38,224
E_ 109
Mvvv 84,42,244
Ln v9,36,s65,256
Mvvv 88,38,228
E_ 110
Mvvv 88,42,248
Ln v9,36,s65,3328
Mvvv 92,38,232
E_ 111
Mvvv 92,42,252
W_ 0
C_ 111,110,111
C_ 110,108,109
Mavv 0,30,116
s_nop 0
Tv 38,v4,0
Tv 40,v4,6144
Mavv 20,34,116
E_ 112
Mavv 24,34,120
s_nop 0
Tv 42,v5,0
Tv 44,v5,6144
Mavv 4,30,120
E_ 113
Mavv 8,30,124
E_ 114
Mavv 28,34,124
E_ 115
Mavv 32,34,128
C_ 115,114,115
C_ 114,112,113
Mavv 12,30,128
E_ 76
Mavv 16,30,132
E_ 77
Mavv 36,34,132
W_ 0
E_ 78
Mavv 56,38,132
s_nop 0
Tv 30,v2,1024
Tv 32,v2,7168
Mavv 76,42,132
E_ 79
Mavv 72,42,128
s_nop 0
Tv 34,v3,1024
Tv 36,v3,7168
Mavv 52,38,128
C_ 96,76,77
C_ 97,78,79
Mavv 48,38,124
E_ 80
Mavv 68,42,124
E_ 81
Mavv 64,42,120
E_ 82
Mavv 44,38,120
E_ 83
Mavv 40,38,116
C_ 100,80,81
C_ 101,82,83
Mavv 60,42,116
W_ 0
E_ 84
Mavv 80,30,116
s_nop 0
Tv 38,v4,1024
Tv 40,v4,7168
Mavv 100,34,116
E_ 85
Mavv 104,34,120
s_nop 0
Tv 42,v5,1024
Tv 44,v5,7168
Mavv 84,30,120
E_ 86
Mavv 88,30,124
E_ 87
Mavv 108,34,124
C_ 104,84,85
C_ 105,86,87
Mavv 112,34,128
E_ 88
Mavv 92,30,128
E_ 89
Mavv 96,30,132
E_ 90
Mavv 116,34,132
W_ 0
E_ 91
Mavv 136,38,132
s_nop 0
Tv 30,v2,2048
Tv 32,v2,8192
Mavv 156,42,132
C_ 108,88,89
C_ 109,90,91
Mavv 152,42,128
s_nop 0
Tv 34,v3,2048
Tv 36,v3,8192
Mavv 132,38,128
E_ 92
Mavv 128,38,124
E_ 93
Mavv 148,42,124
E_ 94
Mavv 144,42,120
E_ 95
Mavv 124,38,120
C_ 112,92,93
C_ 113,94,95
Mavv 120,38,116
v_mov_b32 v92, 0x3f803f80
v_mov_b32 v93, 0x3f803f80
Mavv 140,42,116
W_ 0
v_mov_b32 v94, 0x3f803f80
v_mov_b32 v95, 0x3f803f80
Mavv 160,30,116
s_nop 0
Tv 38,v4,2048
Tv 40,v4,8192
Mavv 180,34,116
Mavv 184,34,120
s_nop 0
Tv 42,v5,2048
Tv 44,v5,8192
Mavv 164,30,120
Mavv 168,30,124
Mavv 188,34,124
S_ s65,s61,172032
Mavv 192,34,128
S_ m0,s51,0
Mavv 172,30,128
s_waitcnt vmcnt(7) lgkmcnt(0)
s_barrier
Mavv 176,30,132
Ln v7,32,s65,0
Mavv 196,34,132
Mavv 216,38,132
Dv 30,%0,18432
Mavv 236,42,132
Dv 34,v1,18432
Mavv 232,42,128
Ln v8,32,s65,3072
S_ m0,s51,896
Mavv 212,38,128
Mavv 208,38,124
Ln v7,32,s65,128
Mavv 228,42,124
Mavv 224,42,120
Ln v8,32,s65,3200
S_ m0,s51,1792
Mavv 204,38,120
Mavv 200,38,116
Ln v7,32,s65,256
Mavv 220,42,116
.Lstep1_13:
Zvvv 76,92,96
Dv 38,%0,19456
Zvvv 80,92,100
Dv 42,v1,19456
Zvvv 84,92,104
Zvvv 88,92,108
Zvvv 92,92,112
W_ 2
Nvvv 116,30,136,56
Mvvv 116,34,156
Nvvv 120,30,140,60
s_nop 1
v_max3_f32 v10, v76, v80, v84
s_nop 0
v_max3_f32 v10, v10, v88, v92
Mvvv 120,34,160
v_cmp_lt_f32 vcc, 0x42000000, v10
s_cbranch_vccnz .Ltramp1_12
Nvvv 124,30,144,64
A_ v46,v46,v76
A_ v47,v47,v80
Mvvv 124,34,164
A_ v48,v48,v84
A_ v49,v49,v88
Nvvv 128,30,148,68
A_ v50,v50,v92
Mvvv 128,34,168
Ln v8,32,s65,3328
Nvvv 132,30,152,72
Mvvv 132,34,172
W_ 0
Mvvv 132,38,192
Dv 30,%0,20480
Mvvv 132,42,212
Dv 34,v1,20480
Mvvv 128,38,188
)A"
R"A(Mvvv 128,42,208
Mvvv 124,38,184
Mvvv 124,42,204
Mvvv 120,38,180
Mvvv 120,42,200
Mvvv 116,38,176
Mvvv 116,42,196
W_ 0
Mvvv 116,30,216
Dv 38,%0,12288
Mvvv 116,34,236
Dv 42,v1,12288
Mvvv 120,30,220
Mvvv 120,34,240
Mvvv 124,30,224
Mvvv 124,34,244
Mvvv 128,30,228
Mvvv 128,34,248
s_nop 0
E_ 116
Mvvv 132,30,232
E_ 117
Mvvv 132,34,252
W_ 0
E_ 118
Nvvv 76,38,136,56
Dv 30,%0,13312
Mvvv 76,42,156
Dv 34,v1,13312
Nvvv 80,38,140,60
E_ 119
Mvvv 80,42,160
C_ 119,118,119
C_ 118,116,117
Nvvv 84,38,144,64
E_ 120
Mvvv 84,42,164
E_ 121
Nvvv 88,38,148,68
E_ 122
Mvvv 88,42,168
E_ 123
Nvvv 92,38,152,72
C_ 123,122,123
C_ 122,120,121
Mvvv 92,42,172
W_ 0
E_ 124
Mvvv 92,30,192
Dv 38,%0,14336
Mvvv 92,34,212
Dv 42,v1,14336
Mvvv 88,30,188
E_ 125
Mvvv 88,34,208
E_ 126
Mvvv 84,30,184
S_ s65,s61,159744
E_ 127
Mvvv 84,34,204
S_ m0,s51,12288
C_ 127,126,127
C_ 126,124,125
Mvvv 80,30,180
s_waitcnt vmcnt(6) lgkmcnt(0)
s_barrier
Mvvv 80,34,200
Ln v9,36,s65,0
Mvvv 76,30,176
E_ 128
Mvvv 76,34,196
Ln v9,36,s65,3072
S_ m0,s51,13184
Mvvv 76,38,216
Tv 30,v2,24576
Tv 32,v2,30720
Mvvv 76,42,236
Ln v9,36,s65,128
Mvvv 80,38,220
s_nop 0
Tv 34,v3,24576
Tv 36,v3,30720
Mvvv 80,42,240
Ln v9,36,s65,3200
S_ m0,s51,14080
Mvvv 84,38,224
E_ 129
Mvvv 84,42,244
Ln v9,36,s65,256
Mvvv 88,38,228
E_ 130
Mvvv 88,42,248
Ln v9,36,s65,3328
Mvvv 92,38,232
E_ 131
Mvvv 92,42,252
W_ 0
C_ 131,130,131
C_ 130,128,129
Mavv 0,30,96
s_nop 0
Tv 38,v4,24576
Tv 40,v4,30720
Mavv 20,34,96
E_ 132
Mavv 24,34,100
s_nop 0
Tv 42,v5,24576
Tv 44,v5,30720
Mavv 4,30,100
E_ 133
Mavv 8,30,104
E_ 134
Mavv 28,34,104
E_ 135
Mavv 32,34,108
C_ 135,134,135
C_ 134,132,133
Mavv 12,30,108
E_ 76
Mavv 16,30,112
E_ 77
Mavv 36,34,112
W_ 0
E_ 78
Mavv 56,38,112
s_nop 0
Tv 30,v2,25600
Tv 32,v2,31744
Mavv 76,42,112
E_ 79
Mavv 72,42,108
s_nop 0
Tv 34,v3,25600
Tv 36,v3,31744
Mavv 52,38,108
C_ 116,76,77
C_ 117,78,79
Mavv 48,38,104
E_ 80
Mavv 68,42,104
E_ 81
Mavv 64,42,100
E_ 82
Mavv 44,38,100
E_ 83
Mavv 40,38,96
C_ 120,80,81
C_ 121,82,83
Mavv 60,42,96
W_ 0
E_ 84
Mavv 80,30,96
s_nop 0
Tv 38,v4,25600
Tv 40,v4,31744
Mavv 100,34,96
E_ 85
Mavv 104,34,100
s_nop 0
Tv 42,v5,25600
Tv 44,v5,31744
Mavv 84,30,100
E_ 86
Mavv 88,30,104
E_ 87
Mavv 108,34,104
C_ 124,84,85
C_ 125,86,87
Mavv 112,34,108
E_ 88
Mavv 92,30,108
E_ 89
Mavv 96,30,112
E_ 90
Mavv 116,34,112
W_ 0
E_ 91
Mavv 136,38,112
s_nop 0
Tv 30,v2,26624
Tv 32,v2,32768
Mavv 156,42,112
C_ 128,88,89
C_ 129,90,91
Mavv 152,42,108
s_nop 0
Tv 34,v3,26624
Tv 36,v3,32768
Mavv 132,38,108
E_ 92
Mavv 128,38,104
E_ 93
Mavv 148,42,104
E_ 94
Mavv 144,42,100
E_ 95
Mavv 124,38,100
C_ 132,92,93
C_ 133,94,95
Mavv 120,38,96
v_mov_b32 v92, 0x3f803f80
v_mov_b32 v93, 0x3f803f80
Mavv 140,42,96
W_ 0
v_mov_b32 v94, 0x3f803f80
v_mov_b32 v95, 0x3f803f80
Mavv 160,30,96
s_nop 0
Tv 38,v4,26624
Tv 40,v4,32768
Mavv 180,34,96
Mavv 184,34,100
s_nop 0
Tv 42,v5,26624
Tv 44,v5,32768
Mavv 164,30,100
Mavv 168,30,104
Mavv 188,34,104
S_ s65,s61,184320
Mavv 192,34,108
S_ m0,s51,24576
Mavv 172,30,108
s_waitcnt vmcnt(6) lgkmcnt(0)
s_barrier
Mavv 176,30,112
Ln v7,32,s65,0
Mavv 196,34,112
Mavv 216,38,112
Dv 30,%0,6144
Mavv 236,42,112
Dv 34,v1,6144
Mavv 232,42,108
Ln v8,32,s65,3072
S_ m0,s51,25472
Mavv 212,38,108
Mavv 208,38,104
Ln v7,32,s65,128
Mavv 228,42,104
Mavv 224,42,100
Ln v8,32,s65,3200
S_ m0,s51,26368
Mavv 204,38,100
Mavv 200,38,96
Ln v7,32,s65,256
Mavv 220,42,96
.Lstep1_14:
Zvvv 76,92,116
Dv 38,%0,7168
Zvvv 80,92,120
Dv 42,v1,7168
Zvvv 84,92,124
Zvvv 88,92,128
Zvvv 92,92,132
W_ 2
Nvvv 96,30,136,56
Mvvv 96,34,156
Nvvv 100,30,140,60
s_nop 1
v_max3_f32 v10, v76, v80, v84
s_nop 0
v_max3_f32 v10, v10, v88, v92
Mvvv 100,34,160
v_cmp_lt_f32 vcc, 0x42000000, v10
s_cbranch_vccnz .Ltramp1_13
Nvvv 104,30,144,64
A_ v46,v46,v76
A_ v47,v47,v80
Mvvv 104,34,164
A_ v48,v48,v84
A_ v49,v49,v88
Nvvv 108,30,148,68
A_ v50,v50,v92
Mvvv 108,34,168
Ln v8,32,s65,3328
Nvvv 112,30,152,72
Mvvv 112,34,172
W_ 0
Mvvv 112,38,192
Dv 30,%0,8192
Mvvv 112,42,212
Dv 34,v1,8192
Mvvv 108,38,188
Mvvv 108,42,208
Mvvv 104,38,184
Mvvv 104,42,204
Mvvv 100,38,180
Mvvv 100,42,200
Mvvv 96,38,176
Mvvv 96,42,196
W_ 0
Mvvv 96,30,216
Dv 38,%0,0
Mvvv 96,34,236
Dv 42,v1,0
Mvvv 100,30,220
Mvvv 100,34,240
Mvvv 104,30,224
Mvvv 104,34,244
Mvvv 108,30,228
Mvvv 108,34,248
s_nop 0
E_ 96
Mvvv 112,30,232
E_ 97
Mvvv 112,34,252
W_ 0
E_ 98
Nvvv 76,38,136,56
Dv 30,%0,1024
Mvvv 76,42,156
Dv 34,v1,1024
Nvvv 80,38,140,60
E_ 99
Mvvv 80,42,160
C_ 99,98,99
C_ 98,96,97
Nvvv 84,38,144,64
E_ 100
Mvvv 84,42,164
E_ 101
Nvvv 88,38,148,68
E_ 102
Mvvv 88,42,168
E_ 103
Nvvv 92,38,152,72
C_ 103,102,103
C_ 102,100,101
Mvvv 92,42,172
W_ 0
E_ 104
Mvvv 92,30,192
Dv 38,%0,2048
Mvvv 92,34,212
Dv 42,v1,2048
Mvvv 88,30,188
E_ 105
Mvvv 88,34,208
E_ 106
Mvvv 84,30,184
S_ s65,s61,172032
E_ 107
Mvvv 84,34,204
S_ m0,s51,0
C_ 107,106,107
C_ 106,104,105
Mvvv 80,30,180
s_waitcnt vmcnt(6) lgkmcnt(0)
s_barrier
Mvvv 80,34,200
Ln v9,36,s65,0
Mvvv 76,30,176
E_ 108
Mvvv 76,34,196
Ln v9,36,s65,3072
S_ m0,s51,896
Mvvv 76,38,216
Tv 30,v2,12288
Tv 32,v2,18432
Mvvv 76,42,236
Ln v9,36,s65,128
Mvvv 80,38,220
s_nop 0
Tv 34,v3,12288
Tv 36,v3,18432
Mvvv 80,42,240
Ln v9,36,s65,3200
S_ m0,s51,1792
Mvvv 84,38,224
E_ 109
Mvvv 84,42,244
Ln v9,36,s65,256
Mvvv 88,38,228
E_ 110
Mvvv 88,42,248
Ln v9,36,s65,3328
Mvvv 92,38,232
E_ 111
Mvvv 92,42,252
W_ 0
C_ 111,110,111
C_ 110,108,109
Mavv 0,30,116
s_nop 0
Tv 38,v4,12288
Tv 40,v4,18432
Mavv 20,34,116
E_ 112
Mavv 24,34,120
s_nop 0
Tv 42,v5,12288
Tv 44,v5,18432
Mavv 4,30,120
E_ 113
Mavv 8,30,124
E_ 114
Mavv 28,34,124
E_ 115
Mavv 32,34,128
C_ 115,114,115
C_ 114,112,113
Mavv 12,30,128
E_ 76
Mavv 16,30,132
E_ 77
Mavv 36,34,132
W_ 0
E_ 78
Mavv 56,38,132
s_nop 0
Tv 30,v2,13312
Tv 32,v2,19456
Mavv 76,42,132
E_ 79
Mavv 72,42,128
s_nop 0
Tv 34,v3,13312
Tv 36,v3,19456
Mavv 52,38,128
C_ 96,76,77
C_ 97,78,79
Mavv 48,38,124
E_ 80
Mavv 68,42,124
E_ 81
Mavv 64,42,120
E_ 82
Mavv 44,38,120
E_ 83
Mavv 40,38,116
C_ 100,80,81
C_ 101,82,83
Mavv 60,42,116
W_ 0
E_ 84
Mavv 80,30,116
s_nop 0
Tv 38,v4,13312
Tv 40,v4,19456
Mavv 100,34,116
E_ 85
Mavv 104,34,120
s_nop 0
Tv 42,v5,13312
Tv 44,v5,19456
Mavv 84,30,120
E_ 86
Mavv 88,30,124
E_ 87
Mavv 108,34,124
C_ 104,84,85
C_ 105,86,87
Mavv 112,34,128
E_ 88
Mavv 92,30,128
E_ 89
Mavv 96,30,132
E_ 90
Mavv 116,34,132
W_ 0
E_ 91
Mavv 136,38,132
s_nop 0
Tv 30,v2,14336
Tv 32,v2,20480
Mavv 156,42,132
C_ 108,88,89
C_ 109,90,91
Mavv 152,42,128
s_nop 0
Tv 34,v3,14336
Tv 36,v3,20480
Mavv 132,38,128
E_ 92
Mavv 128,38,124
E_ 93
Mavv 148,42,124
E_ 94
Mavv 144,42,120
E_ 95
Mavv 124,38,120
C_ 112,92,93
C_ 113,94,95
Mavv 120,38,116
v_mov_b32 v92, 0x3f803f80
v_mov_b32 v93, 0x3f803f80
Mavv 140,42,116
W_ 0
v_mov_b32 v94, 0x3f803f80
v_mov_b32 v95, 0x3f803f80
Mavv 160,30,116
s_nop 0
Tv 38,v4,14336
Tv 40,v4,20480
Mavv 180,34,116
Mavv 184,34,120
s_nop 0
Tv 42,v5,14336
Tv 44,v5,20480
Mavv 164,30,120
Mavv 168,30,124
Mavv 188,34,124
S_ s65,s61,184320
Mavv 192,34,128
S_ m0,s51,12288
Mavv 172,30,128
s_waitcnt vmcnt(6) lgkmcnt(0)
s_barrier
Mavv 176,30,132
Ln v9,36,s65,0
Mavv 196,34,132
Mavv 216,38,132
Dv 30,%0,30720
Mavv 236,42,132
Dv 34,v1,30720
Mavv 232,42,128
Ln v9,36,s65,3072
S_ m0,s51,13184
Mavv 212,38,128
Mavv 208,38,124
Ln v9,36,s65,128
Mavv 228,42,124
Mavv 224,42,120
Ln v9,36,s65,3200
S_ m0,s51,14080
Mavv 204,38,120
Mavv 200,38,116
Ln v9,36,s65,256
Mavv 220,42,116
.Lstep1_15:
Zvvv 76,92,96
Dv 38,%0,31744
Zvvv 80,92,100
Dv 42,v1,31744
Zvvv 84,92,104
Zvvv 88,92,108
Zvvv 92,92,112
W_ 2
Nvvv 116,30,136,56
Mvvv 116,34,156
Nvvv 120,30,140,60
s_nop 1
v_max3_f32 v10, v76, v80, v84
s_nop 0
v_max3_f32 v10, v10, v88, v92
Mvvv 120,34,160
v_cmp_lt_f32 vcc, 0x42000000, v10
s_cbranch_vccnz .Ltramp1_14
Nvvv 124,30,144,64
A_ v46,v46,v76
A_ v47,v47,v80
Mvvv 124,34,164
A_ v48,v48,v84
A_ v49,v49,v88
Nvvv 128,30,148,68
A_ v50,v50,v92
Mvvv 128,34,168
Ln v9,36,s65,3328
Nvvv 132,30,152,72
Mvvv 132,34,172
W_ 0
Mvvv 132,38,192
Dv 30,%0,32768
Mvvv 132,42,212
Dv 34,v1,32768
Mvvv 128,38,188
Mvvv 128,42,208
Mvvv 124,38,184
Mvvv 124,42,204
Mvvv 120,38,180
Mvvv 120,42,200
Mvvv 116,38,176
Mvvv 116,42,196
W_ 0
Mvvv 116,30,216
Dv 38,%0,24576
Mvvv 116,34,236
Dv 42,v1,24576
Mvvv 120,30,220
Mvvv 120,34,240
Mvvv 124,30,224
Mvvv 124,34,244
Mvvv 128,30,228
Mvvv 128,34,248
s_nop 0
E_ 116
Mvvv 132,30,232
E_ 117
Mvvv 132,34,252
W_ 0
E_ 118
Nvvv 76,38,136,56
Dv 30,%0,25600
Mvvv 76,42,156
Dv 34,v1,25600
Nvvv 80,38,140,60
E_ 119
Mvvv 80,42,160
C_ 119,118,119
C_ 118,116,117
Nvvv 84,38,144,64
E_ 120
Mvvv 84,42,164
E_ 121
Nvvv 88,38,148,68
E_ 122
Mvvv 88,42,168
E_ 123
Nvvv 92,38,152,72
C_ 123,122,123
C_ 122,120,121
Mvvv 92,42,172
W_ 0
E_ 124
Mvvv 92,30,192
Dv 38,%0,26624
Mvvv 92,34,212
Dv 42,v1,26624
Mvvv 88,30,188
E_ 125
Mvvv 88,34,208
E_ 126
Mvvv 84,30,184
E_ 127
Mvvv 84,34,204
C_ 127,126,127
C_ 126,124,125
Mvvv 80,30,180
s_waitcnt vmcnt(6) lgkmcnt(0)
s_barrier
Mvvv 80,34,200
R_ v7,a248
Mvvv 76,30,176
v_lshrrev_b32 v7, 7, v7
Mvvv 76,34,196
v_mul_u32_u24 v7, 896, v7
Mvvv 76,38,216
s_nop 0
Tv 30,v2,0
Tv 32,v2,6144
Mvvv 76,42,236
v_add_u32 v7, v7, v9
Mvvv 80,38,220
S_ s71,s72,1024
Tv 34,v3,0
Tv 36,v3,6144
Mvvv 80,42,240
S_ s55,s71,491520
E_ 128
Mvvv 84,38,224
S_ m0,s51,24576
E_ 129
Mvvv 84,42,244
L_ v7,24,s55,0
Mvvv 88,38,228
S_ s55,s71,501760
E_ 130
Mvvv 88,42,248
S_ m0,s51,25600
E_ 131
Mvvv 92,38,232
L_ v7,24,s55,0
Mvvv 92,42,252
S_ s55,s71,512000
C_ 131,130,131
C_ 130,128,129
W_ 2
Mavv 0,30,96
S_ m0,s51,26624
Tv 38,v4,0
Tv 40,v4,6144
W_ 2
Mavv 20,34,96
L_ v7,24,s55,0
Mavv 24,34,100
S_ s55,s71,522240
Tv 42,v5,0
Tv 44,v5,6144
Mavv 4,30,100
S_ m0,s51,27648
E_ 132
Mavv 8,30,104
L_ v7,24,s55,0
Mavv 28,34,104
S_ s55,s71,532480
E_ 133
Mavv 32,34,108
S_ m0,s51,28672
E_ 134
Mavv 12,30,108
L_ v7,24,s55,0
Mavv 16,30,112
S_ s55,s71,542720
E_ 135
Mavv 36,34,112
S_ m0,s51,29696
C_ 135,134,135
C_ 134,132,133
W_ 2
Mavv 56,38,112
Tv 30,v2,1024
Tv 32,v2,7168
W_ 2
Mavv 76,42,112
L_ v7,24,s55,0
Mavv 72,42,108
Tv 34,v3,1024
Tv 36,v3,7168
Mavv 52,38,108
E_ 76
Mavv 48,38,104
E_ 77
Mavv 68,42,104
E_ 78
Mavv 64,42,100
E_ 79
Mavv 44,38,100
C_ 116,76,77
C_ 117,78,79
Mavv 40,38,96
E_ 80
Mavv 60,42,96
W_ 0
E_ 81
Mavv 80,30,96
s_nop 0
Tv 38,v4,1024
Tv 40,v4,7168
Mavv 100,34,96
E_ 82
Mavv 104,34,100
s_nop 0
Tv 42,v5,1024
Tv 44,v5,7168
Mavv 84,30,100
E_ 83
Mavv 88,30,104
C_ 120,80,81
C_ 121,82,83
Mavv 108,34,104
E_ 84
Mavv 112,34,108
E_ 85
Mavv 92,30,108
E_ 86
Mavv 96,30,112
E_ 87
Mavv 116,34,112
W_ 0
C_ 124,84,85
C_ 125,86,87
Mavv 136,38,112
s_nop 0
Tv 30,v2,2048
Tv 32,v2,8192
Mavv 156,42,112
E_ 88
Mavv 152,42,108
s_nop 0
Tv 34,v3,2048
Tv 36,v3,8192
Mavv 132,38,108
E_ 89
Mavv 128,38,104
E_ 90
Mavv 148,42,104
E_ 91
Mavv 144,42,100
C_ 128,88,89
C_ 129,90,91
Mavv 124,38,100
E_ 92
Mavv 120,38,96
E_ 93
Mavv 140,42,96
W_ 0
E_ 94
Mavv 160,30,96
s_nop 0
Tv 38,v4,2048
Tv 40,v4,8192
Mavv 180,34,96
E_ 95
Mavv 184,34,100
s_nop 0
Tv 42,v5,2048
Tv 44,v5,8192
Mavv 164,30,100
C_ 132,92,93
C_ 133,94,95
Mavv 168,30,104
v_mov_b32 v92, 0x3f803f80
v_mov_b32 v93, 0x3f803f80
Mavv 188,34,104
v_mov_b32 v94, 0x3f803f80
v_mov_b32 v95, 0x3f803f80
Mavv 192,34,108
R_ v11,a248
Mavv 172,30,108
s_waitcnt vmcnt(6) lgkmcnt(0)
s_barrier
v_mul_u32_u24 v11, 80, v11
Mavv 176,30,112
S_ s55,s71,614400
v_min_u32 v11, 29440, v11
Mavv 196,34,112
S_ m0,s51,0
S_ s85,s87,1024
Mavv 216,38,112
s_nop 0
Tv 30,v2,12288
Tv 32,v2,18432
Zvvv 76,92,116
L_ v7,24,s55,0
Zvvv 80,92,120
S_ s55,s71,624640
Tv 34,v3,12288
Tv 36,v3,18432
Zvvv 84,92,124
buffer_load_dword a254, v11, s[24:27], s85 offen
Zvvv 88,92,128
S_ m0,s51,1024
S_ s85,s87,1152
Zvvv 92,92,132
L_ v7,24,s55,0
Mavv 236,42,112
S_ s55,s71,634880
Mavv 232,42,108
buffer_load_dword a254, v11, s[24:27], s85 offen
Mavv 212,38,108
S_ m0,s51,2048
S_ s85,s6,1
Mavv 208,38,104
s_and_b32 s85, s85, 7
v_max3_f32 v10, v76, v80, v84
Mavv 228,42,104
s_lshl_b32 s85, s85, 7
v_max3_f32 v10, v10, v88, v92
v_cmp_lt_f32 vcc, 0x42000000, v10
Mavv 224,42,100
L_ v7,24,s55,0
Mavv 204,38,100
S_ s55,s71,645120
S_ s85,s85,s87
Mavv 200,38,96
buffer_load_dword a254, v11, s[24:27], s85 offen
Mavv 220,42,96
S_ m0,s51,3072
s_cbranch_vccnz .Ltramp1_15
.Ljoin1:
W_ 2
Mavv 0,30,116
s_nop 0
Tv 38,v4,12288
Tv 40,v4,18432
W_ 2
Mavv 20,34,116
L_ v7,24,s55,0
Mavv 24,34,120
S_ s55,s71,655360
Tv 42,v5,12288
Tv 44,v5,18432
Mavv 4,30,120
S_ m0,s51,4096
R_ v13,a248
S_ s53,s51,36864
Mavv 8,30,124
L_ v7,24,s55,0
Mavv 28,34,124
S_ s55,s71,665600
v_add_u32 v12, s53, v13
s_mul_i32 s53, s4, 10240
Mavv 32,34,128
S_ m0,s51,5120
v_add_u32 v13, s53, v13
R_ v10,a248
Mavv 12,30,128
L_ v7,24,s55,0
Mavv 16,30,132
A_ v46,v46,v76
A_ v47,v47,v80
Mavv 36,34,132
buffer_load_dword v11, off, s[92:95], s96 offset:0 sc1
W_ 2
Mavv 56,38,132
W_ 0
Tv 30,v2,13312
Tv 32,v2,19456
Mavv 76,42,132
A_ v48,v48,v84
A_ v49,v49,v88
Mavv 72,42,128
s_nop 0
Tv 34,v3,13312
Tv 36,v3,19456
Mavv 52,38,128
A_ v50,v50,v92
Mavv 48,38,124
v_rcp_f32 v46, v46
Mavv 68,42,124
v_rcp_f32 v47, v47
Mavv 64,42,120
v_rcp_f32 v48, v48
Mavv 44,38,120
v_rcp_f32 v49, v49
Mavv 40,38,116
v_rcp_f32 v50, v50
Mavv 60,42,116
W_ 0
R_ v96,a0
R_ v97,a1
Mavv 80,30,116
s_nop 0
Tv 38,v4,13312
Tv 40,v4,19456
Mavv 100,34,116
R_ v98,a2
R_ v99,a3
Mavv 104,34,120
s_nop 0
Tv 42,v5,13312
Tv 44,v5,19456
Mavv 84,30,120
R_ v100,a20
R_ v101,a21
Mavv 88,30,124
R_ v102,a22
R_ v103,a23
Mavv 108,34,124
U_ v96,v96,v46
U_ v97,v97,v46
Mavv 112,34,128
U_ v98,v98,v46
U_ v99,v99,v46
Mavv 92,30,128
U_ v100,v100,v46
U_ v101,v101,v46
Mavv 96,30,132
U_ v102,v102,v46
U_ v103,v103,v46
Mavv 116,34,132
W_ 0
C_ 136,96,97
C_ 137,98,99
Mavv 136,38,132
s_nop 0
Tv 30,v2,14336
Tv 32,v2,20480
Mavv 156,42,132
C_ 138,100,101
C_ 139,102,103
Mavv 152,42,128
s_nop 0
Tv 34,v3,14336
Tv 36,v3,20480
Mavv 132,38,128
Da 0,v13,0
Mavv 128,38,124
R_ v104,a4
R_ v105,a5
Mavv 148,42,124
R_ v106,a6
R_ v107,a7
Mavv 144,42,120
R_ v108,a24
R_ v109,a25
Mavv 124,38,120
R_ v110,a26
R_ v111,a27
Mavv 120,38,116
U_ v104,v104,v47
U_ v105,v105,v47
Mavv 140,42,116
W_ 1
U_ v106,v106,v47
U_ v107,v107,v47
Mavv 160,30,116
s_nop 0
Tv 38,v4,14336
Tv 40,v4,20480
Mavv 180,34,116
U_ v108,v108,v47
U_ v109,v109,v47
Mavv 184,34,120
s_nop 0
Tv 42,v5,14336
Tv 44,v5,20480
Mavv 164,30,120
U_ v110,v110,v47
U_ v111,v111,v47
Mavv 168,30,124
C_ 140,104,105
C_ 141,106,107
Mavv 188,34,124
C_ 142,108,109
C_ 143,110,111
Mavv 192,34,128
Da 4,v13,1024
Mavv 172,30,128
S_ s55,s71,0
R_ v96,a8
R_ v97,a9
Mavv 176,30,132
S_ m0,s51,12288
R_ v98,a10
R_ v99,a11
Mavv 196,34,132
s_waitcnt vmcnt(63) lgkmcnt(0)
s_barrier
R_ v100,a28
Mavv 216,38,132
L_ v7,24,s55,0
S_ s55,s71,10240
S_ m0,s51,13312
Mavv 236,42,132
R_ v101,a29
Mavv 232,42,128
L_ v7,24,s55,0
S_ s55,s71,20480
S_ m0,s51,14336
Mavv 212,38,128
R_ v102,a30
R_ v103,a31
Mavv 208,38,124
L_ v7,24,s55,0
S_ s55,s71,30720
S_ m0,s51,15360
Mavv 228,42,124
U_ v96,v96,v48
U_ v97,v97,v48
Mavv 224,42,120
L_ v7,24,s55,0
S_ s55,s71,40960
S_ m0,s51,16384
Mavv 204,38,120
U_ v98,v98,v48
U_ v99,v99,v48
Mavv 200,38,116
L_ v7,24,s55,0
S_ s55,s71,51200
S_ m0,s51,17408
Mavv 220,42,116
U_ v100,v100,v48
U_ v101,v101,v48
U_ v102,v102,v48
U_ v103,v103,v48
C_ 144,96,97
C_ 145,98,99
C_ 146,100,101
C_ 147,102,103
Da 8,v13,2048
L_ v7,24,s55,0
.p2align 3
R_ v104,a12
R_ v105,a13
R_ v106,a14
R_ v107,a15
R_ v108,a32
R_ v109,a33
R_ v110,a34
R_ v111,a35
v_pk_mul_f32 v[104:105], v[104:105], v[48:49] op_sel:[0,1]
v_pk_mul_f32 v[106:107], v[106:107], v[48:49] op_sel:[0,1]
v_pk_mul_f32 v[108:109], v[108:109], v[48:49] op_sel:[0,1]
v_pk_mul_f32 v[110:111], v[110:111], v[48:49] op_sel:[0,1]
C_ 148,104,105
C_ 149,106,107
C_ 150,108,109
C_ 151,110,111
Da 12,v13,3072
.p2align 3
R_ v96,a16
R_ v97,a17
R_ v98,a18
R_ v99,a19
R_ v100,a36
R_ v101,a37
R_ v102,a38
R_ v103,a39
v_pk_mul_f32 v[96:97], v[96:97], v[50:51] op_sel_hi:[1,0]
v_pk_mul_f32 v[98:99], v[98:99], v[50:51] op_sel_hi:[1,0]
v_pk_mul_f32 v[100:101], v[100:101], v[50:51] op_sel_hi:[1,0]
v_pk_mul_f32 v[102:103], v[102:103], v[50:51] op_sel_hi:[1,0]
C_ 152,96,97
C_ 153,98,99
C_ 154,100,101
C_ 155,102,103
Da 16,v13,4096
.p2align 3
R_ v104,a40
R_ v105,a41
R_ v106,a42
R_ v107,a43
R_ v108,a60
R_ v109,a61
R_ v110,a62
R_ v111,a63
v_pk_mul_f32 v[104:105], v[104:105], v[46:47] op_sel_hi:[1,0]
v_pk_mul_f32 v[106:107], v[106:107], v[46:47] op_sel_hi:[1,0]
v_pk_mul_f32 v[108:109], v[108:109], v[46:47] op_sel_hi:[1,0]
v_pk_mul_f32 v[110:111], v[110:111], v[46:47] op_sel_hi:[1,0]
C_ 156,104,105
C_ 157,106,107
C_ 158,108,109
C_ 159,110,111
Da 40,v13,5120
.p2align 3
R_ v96,a44
R_ v97,a45
R_ v98,a46
R_ v99,a47
R_ v100,a64
R_ v101,a65
R_ v102,a66
R_ v103,a67
v_pk_mul_f32 v[96:97], v[96:97], v[46:47] op_sel:[0,1]
v_pk_mul_f32 v[98:99], v[98:99], v[46:47] op_sel:[0,1]
v_pk_mul_f32 v[100:101], v[100:101], v[46:47] op_sel:[0,1]
v_pk_mul_f32 v[102:103], v[102:103], v[46:47] op_sel:[0,1]
C_ 160,96,97
C_ 161,98,99
C_ 162,100,101
C_ 163,102,103
Da 44,v13,6144
.p2align 3
R_ v104,a48
R_ v105,a49
R_ v106,a50
R_ v107,a51
R_ v108,a68
R_ v109,a69
R_ v110,a70
R_ v111,a71
v_pk_mul_f32 v[104:105], v[104:105], v[48:49] op_sel_hi:[1,0]
v_pk_mul_f32 v[106:107], v[106:107], v[48:49] op_sel_hi:[1,0]
v_pk_mul_f32 v[108:109], v[108:109], v[48:49] op_sel_hi:[1,0]
v_pk_mul_f32 v[110:111], v[110:111], v[48:49] op_sel_hi:[1,0]
C_ 164,104,105
C_ 165,106,107
C_ 166,108,109
C_ 167,110,111
Da 48,v13,7168
.p2align 3
R_ v96,a52
R_ v97,a53
R_ v98,a54
R_ v99,a55
R_ v100,a72
R_ v101,a73
R_ v102,a74
R_ v103,a75
v_pk_mul_f32 v[96:97], v[96:97], v[48:49] op_sel:[0,1]
v_pk_mul_f32 v[98:99], v[98:99], v[48:49] op_sel:[0,1]
v_pk_mul_f32 v[100:101], v[100:101], v[48:49] op_sel:[0,1]
v_pk_mul_f32 v[102:103], v[102:103], v[48:49] op_sel:[0,1]
C_ 168,96,97
C_ 169,98,99
C_ 170,100,101
C_ 171,102,103
Da 52,v13,8192
.p2align 3
R_ v104,a56
R_ v105,a57
R_ v106,a58
R_ v107,a59
R_ v108,a76
R_ v109,a77
R_ v110,a78
R_ v111,a79
v_pk_mul_f32 v[104:105], v[104:105], v[50:51] op_sel_hi:[1,0]
v_pk_mul_f32 v[106:107], v[106:107], v[50:51] op_sel_hi:[1,0]
v_pk_mul_f32 v[108:109], v[108:109], v[50:51] op_sel_hi:[1,0]
v_pk_mul_f32 v[110:111], v[110:111], v[50:51] op_sel_hi:[1,0]
C_ 172,104,105
C_ 173,106,107
C_ 174,108,109
C_ 175,110,111
Da 56,v13,9216
.p2align 3
R_ v96,a80
R_ v97,a81
R_ v98,a82
R_ v99,a83
R_ v100,a100
R_ v101,a101
R_ v102,a102
R_ v103,a103
v_pk_mul_f32 v[96:97], v[96:97], v[46:47] op_sel_hi:[1,0]
v_pk_mul_f32 v[98:99], v[98:99], v[46:47] op_sel_hi:[1,0]
v_pk_mul_f32 v[100:101], v[100:101], v[46:47] op_sel_hi:[1,0]
v_pk_mul_f32 v[102:103], v[102:103], v[46:47] op_sel_hi:[1,0]
C_ 176,96,97
C_ 177,98,99
C_ 178,100,101
C_ 179,102,103
X_ a80,v20
X_ a81,v21
X_ a82,v22
X_ a83,v23
.p2align 3
R_ v104,a84
R_ v105,a85
R_ v106,a86
R_ v107,a87
R_ v108,a104
R_ v109,a105
R_ v110,a106
R_ v111,a107
v_pk_mul_f32 v[104:105], v[104:105], v[46:47] op_sel:[0,1]
v_pk_mul_f32 v[106:107], v[106:107], v[46:47] op_sel:[0,1]
v_pk_mul_f32 v[108:109], v[108:109], v[46:47] op_sel:[0,1]
v_pk_mul_f32 v[110:111], v[110:111], v[46:47] op_sel:[0,1]
C_ 180,104,105
C_ 181,106,107
C_ 182,108,109
C_ 183,110,111
X_ a84,v24
X_ a85,v25
X_ a86,v26
X_ a87,v27
.p2align 3
R_ v96,a88
R_ v97,a89
R_ v98,a90
R_ v99,a91
R_ v100,a108
R_ v101,a109
R_ v102,a110
R_ v103,a111
v_pk_mul_f32 v[96:97], v[96:97], v[48:49] op_sel_hi:[1,0]
v_pk_mul_f32 v[98:99], v[98:99], v[48:49] op_sel_hi:[1,0]
v_pk_mul_f32 v[100:101], v[100:101], v[48:49] op_sel_hi:[1,0]
v_pk_mul_f32 v[102:103], v[102:103], v[48:49] op_sel_hi:[1,0]
C_ 184,96,97
C_ 185,98,99
C_ 186,100,101
C_ 187,102,103
X_ a88,v52
X_ a89,v53
X_ a90,v54
X_ a91,v55
.p2align 3
R_ v104,a92
R_ v105,a93
R_ v106,a94
R_ v107,a95
R_ v108,a112
R_ v109,a113
R_ v110,a114
R_ v111,a115
v_pk_mul_f32 v[104:105], v[104:105], v[48:49] op_sel:[0,1]
v_pk_mul_f32 v[106:107], v[106:107], v[48:49] op_sel:[0,1]
v_pk_mul_f32 v[108:109], v[108:109], v[48:49] op_sel:[0,1]
v_pk_mul_f32 v[110:111], v[110:111], v[48:49] op_sel:[0,1]
C_ 188,104,105
C_ 189,106,107
C_ 190,108,109
C_ 191,110,111
.p2align 3
R_ v96,a96
R_ v97,a97
R_ v98,a98
R_ v99,a99
R_ v100,a116
R_ v101,a117
R_ v102,a118
R_ v103,a119
v_pk_mul_f32 v[96:97], v[96:97], v[50:51] op_sel_hi:[1,0]
v_pk_mul_f32 v[98:99], v[98:99], v[50:51] op_sel_hi:[1,0]
v_pk_mul_f32 v[100:101], v[100:101], v[50:51] op_sel_hi:[1,0]
v_pk_mul_f32 v[102:103], v[102:103], v[50:51] op_sel_hi:[1,0]
C_ 192,96,97
C_ 193,98,99
C_ 194,100,101
C_ 195,102,103
.p2align 3
R_ v104,a120
R_ v105,a121
R_ v106,a122
R_ v107,a123
R_ v108,a140
R_ v109,a141
R_ v110,a142
R_ v111,a143
v_pk_mul_f32 v[104:105], v[104:105], v[46:47] op_sel_hi:[1,0]
v_pk_mul_f32 v[106:107], v[106:107], v[46:47] op_sel_hi:[1,0]
v_pk_mul_f32 v[108:109], v[108:109], v[46:47] op_sel_hi:[1,0]
v_pk_mul_f32 v[110:111], v[110:111], v[46:47] op_sel_hi:[1,0]
C_ 196,104,105
C_ 197,106,107
C_ 198,108,109
C_ 199,110,111
X_ a120,v16
X_ a121,v17
X_ a122,v18
X_ a123,v19
.p2align 3
R_ v96,a124
R_ v97,a125
R_ v98,a126
R_ v99,a127
R_ v100,a144
R_ v101,a145
R_ v102,a146
R_ v103,a147
v_pk_mul_f32 v[96:97], v[96:97], v[46:47] op_sel:[0,1]
v_pk_mul_f32 v[98:99], v[98:99], v[46:47] op_sel:[0,1]
v_pk_mul_f32 v[100:101], v[100:101], v[46:47] op_sel:[0,1]
v_pk_mul_f32 v[102:103], v[102:103], v[46:47] op_sel:[0,1]
C_ 200,96,97
C_ 201,98,99
C_ 202,100,101
C_ 203,102,103
X_ a124,v28
X_ a125,v29
X_ a126,v51
X_ a127,v6
.p2align 3
R_ v104,a128
R_ v105,a129
R_ v106,a130
R_ v107,a131
R_ v108,a148
R_ v109,a149
R_ v110,a150
R_ v111,a151
v_pk_mul_f32 v[104:105], v[104:105], v[48:49] op_sel_hi:[1,0]
v_pk_mul_f32 v[106:107], v[106:107], v[48:49] op_sel_hi:[1,0]
v_pk_mul_f32 v[108:109], v[108:109], v[48:49] op_sel_hi:[1,0]
v_pk_mul_f32 v[110:111], v[110:111], v[48:49] op_sel_hi:[1,0]
C_ 204,104,105
C_ 205,106,107
C_ 206,108,109
C_ 207,110,111
X_ a128,v14
X_ a129,v15
v_accvgpr_mov_b32 a130, a249
v_accvgpr_mov_b32 a131, a250
.p2align 3
R_ v96,a132
R_ v97,a133
R_ v98,a134
R_ v99,a135
R_ v100,a152
R_ v101,a153
R_ v102,a154
R_ v103,a155
v_pk_mul_f32 v[96:97], v[96:97], v[48:49] op_sel:[0,1]
v_pk_mul_f32 v[98:99], v[98:99], v[48:49] op_sel:[0,1]
v_pk_mul_f32 v[100:101], v[100:101], v[48:49] op_sel:[0,1]
v_pk_mul_f32 v[102:103], v[102:103], v[48:49] op_sel:[0,1]
C_ 208,96,97
C_ 209,98,99
C_ 210,100,101
C_ 211,102,103
Da 132,v12,0
.p2align 3
R_ v104,a136
R_ v105,a137
R_ v106,a138
R_ v107,a139
R_ v108,a156
R_ v109,a157
R_ v110,a158
R_ v111,a159
v_pk_mul_f32 v[104:105], v[104:105], v[50:51] op_sel_hi:[1,0]
v_pk_mul_f32 v[106:107], v[106:107], v[50:51] op_sel_hi:[1,0]
v_pk_mul_f32 v[108:109], v[108:109], v[50:51] op_sel_hi:[1,0]
v_pk_mul_f32 v[110:111], v[110:111], v[50:51] op_sel_hi:[1,0]
C_ 212,104,105
C_ 213,106,107
C_ 214,108,109
C_ 215,110,111
Da 136,v12,1024
.p2align 3
R_ v96,a160
R_ v97,a161
R_ v98,a162
R_ v99,a163
R_ v100,a180
R_ v101,a181
R_ v102,a182
R_ v103,a183
v_pk_mul_f32 v[96:97], v[96:97], v[46:47] op_sel_hi:[1,0]
v_pk_mul_f32 v[98:99], v[98:99], v[46:47] op_sel_hi:[1,0]
v_pk_mul_f32 v[100:101], v[100:101], v[46:47] op_sel_hi:[1,0]
v_pk_mul_f32 v[102:103], v[102:103], v[46:47] op_sel_hi:[1,0]
C_ 216,96,97
C_ 217,98,99
C_ 218,100,101
C_ 219,102,103
Da 160,v12,2048
.p2align 3
R_ v104,a164
R_ v105,a165
R_ v106,a166
R_ v107,a167
R_ v108,a184
R_ v109,a185
R_ v110,a186
R_ v111,a187
v_pk_mul_f32 v[104:105], v[104:105], v[46:47] op_sel:[0,1]
v_pk_mul_f32 v[106:107], v[106:107], v[46:47] op_sel:[0,1]
v_pk_mul_f32 v[108:109], v[108:109], v[46:47] op_sel:[0,1]
v_pk_mul_f32 v[110:111], v[110:111], v[46:47] op_sel:[0,1]
C_ 220,104,105
C_ 221,106,107
C_ 222,108,109
C_ 223,110,111
Da 164,v12,3072
.p2align 3
R_ v96,a168
R_ v97,a169
R_ v98,a170
R_ v99,a171
R_ v100,a188
R_ v101,a189
R_ v102,a190
R_ v103,a191
v_pk_mul_f32 v[96:97], v[96:97], v[48:49] op_sel_hi:[1,0]
v_pk_mul_f32 v[98:99], v[98:99], v[48:49] op_sel_hi:[1,0]
v_pk_mul_f32 v[100:101], v[100:101], v[48:49] op_sel_hi:[1,0]
v_pk_mul_f32 v[102:103], v[102:103], v[48:49] op_sel_hi:[1,0]
C_ 224,96,97
C_ 225,98,99
C_ 226,100,101
C_ 227,102,103
Da 168,v12,4096
.p2align 3
R_ v104,a172
R_ v105,a173
R_ v106,a174
R_ v107,a175
R_ v108,a192
R_ v109,a193
R_ v110,a194
R_ v111,a195
v_pk_mul_f32 v[104:105], v[104:105], v[48:49] op_sel:[0,1]
v_pk_mul_f32 v[106:107], v[106:107], v[48:49] op_sel:[0,1]
v_pk_mul_f32 v[108:109], v[108:109], v[48:49] op_sel:[0,1]
v_pk_mul_f32 v[110:111], v[110:111], v[48:49] op_sel:[0,1]
C_ 228,104,105
C_ 229,106,107
C_ 230,108,109
C_ 231,110,111
Da 172,v12,5120
.p2align 3
R_ v96,a176
R_ v97,a177
R_ v98,a178
R_ v99,a179
R_ v100,a196
R_ v101,a197
R_ v102,a198
R_ v103,a199
v_pk_mul_f32 v[96:97], v[96:97], v[50:51] op_sel_hi:[1,0]
v_pk_mul_f32 v[98:99], v[98:99], v[50:51] op_sel_hi:[1,0]
v_pk_mul_f32 v[100:101], v[100:101], v[50:51] op_sel_hi:[1,0]
v_pk_mul_f32 v[102:103], v[102:103], v[50:51] op_sel_hi:[1,0]
C_ 232,96,97
C_ 233,98,99
C_ 234,100,101
C_ 235,102,103
Da 176,v12,12288
.p2align 3
R_ v104,a200
R_ v105,a201
R_ v106,a202
R_ v107,a203
R_ v108,a220
R_ v109,a221
R_ v110,a222
R_ v111,a223
v_pk_mul_f32 v[104:105], v[104:105], v[46:47] op_sel_hi:[1,0]
v_pk_mul_f32 v[106:107], v[106:107], v[46:47] op_sel_hi:[1,0]
v_pk_mul_f32 v[108:109], v[108:109], v[46:47] op_sel_hi:[1,0]
v_pk_mul_f32 v[110:111], v[110:111], v[46:47] op_sel_hi:[1,0]
C_ 236,104,105
C_ 237,106,107
C_ 238,108,109
C_ 239,110,111
Da 200,v12,13312
.p2align 3
R_ v96,a204
R_ v97,a205
R_ v98,a206
R_ v99,a207
R_ v100,a224
R_ v101,a225
R_ v102,a226
R_ v103,a227
v_pk_mul_f32 v[96:97], v[96:97], v[46:47] op_sel:[0,1]
v_pk_mul_f32 v[98:99], v[98:99], v[46:47] op_sel:[0,1]
v_pk_mul_f32 v[100:101], v[100:101], v[46:47] op_sel:[0,1]
v_pk_mul_f32 v[102:103], v[102:103], v[46:47] op_sel:[0,1]
C_ 240,96,97
C_ 241,98,99
C_ 242,100,101
C_ 243,102,103
Da 204,v12,14336
v_mov_b32 v6, v10
v_lshrrev_b32 v10, 9, v6
v_lshlrev_b32 v10, 11, v10
v_sub_u32 %0, v2, v10
v_xor_b32 %0, s83, %0
s_waitcnt vmcnt(16) lgkmcnt(0)
s_barrier
Ta 180,%0,24576
Ta 182,%0,26624
Ta 184,%0,28672
Ta 186,%0,30720
Ta 188,%0,32768
Ta 190,%0,34816
.p2align 3
R_ v104,a208
R_ v105,a209
R_ v106,a210
R_ v107,a211
R_ v108,a228
R_ v109,a229
R_ v110,a230
R_ v111,a231
v_pk_mul_f32 v[104:105], v[104:105], v[48:49] op_sel_hi:[1,0]
v_pk_mul_f32 v[106:107], v[106:107], v[48:49] op_sel_hi:[1,0]
v_pk_mul_f32 v[108:109], v[108:109], v[48:49] op_sel_hi:[1,0]
v_pk_mul_f32 v[110:111], v[110:111], v[48:49] op_sel_hi:[1,0]
C_ 244,104,105
C_ 245,106,107
C_ 246,108,109
C_ 247,110,111
Da 208,v12,15360
.p2align 3
R_ v96,a212
R_ v97,a213
R_ v98,a214
R_ v99,a215
R_ v100,a232
R_ v101,a233
R_ v102,a234
R_ v103,a235
v_pk_mul_f32 v[96:97], v[96:97], v[48:49] op_sel:[0,1]
v_pk_mul_f32 v[98:99], v[98:99], v[48:49] op_sel:[0,1]
v_pk_mul_f32 v[100:101], v[100:101], v[48:49] op_sel:[0,1]
v_pk_mul_f32 v[102:103], v[102:103], v[48:49] op_sel:[0,1]
C_ 248,96,97
C_ 249,98,99
C_ 250,100,101
C_ 251,102,103
Da 212,v12,16384
.p2align 3
R_ v104,a216
R_ v105,a217
R_ v106,a218
R_ v107,a219
R_ v108,a236
R_ v109,a237
R_ v110,a238
R_ v111,a239
v_pk_mul_f32 v[104:105], v[104:105], v[50:51] op_sel_hi:[1,0]
v_pk_mul_f32 v[106:107], v[106:107], v[50:51] op_sel_hi:[1,0]
v_pk_mul_f32 v[108:109], v[108:109], v[50:51] op_sel_hi:[1,0]
v_pk_mul_f32 v[110:111], v[110:111], v[50:51] op_sel_hi:[1,0]
C_ 252,104,105
C_ 253,106,107
C_ 254,108,109
C_ 255,110,111
Da 216,v12,17408
v_xor_b32 v1, 32, %0
W_ 7
Zvav 56,180,136
s_mul_i32 s76, s6, 204800
Ta 192,v1,24576
Ta 194,v1,26624
W_ 7
Mvav 56,184,156
v_xor_b32 v2, 64, %0
v_xor_b32 v3, 96, %0
W_ 5
Mvav 56,188,176
s_nop 0
Ta 196,v1,28672
Ta 198,v1,30720
Zvav 60,180,140
W_ 4
R_ v4,a248
v_mul_u32_u24 v4, 80, v4
Mvav 60,184,160
S_ s55,s71,122880
Ta 220,v1,32768
Ta 222,v1,34816
Mvav 60,188,180
S_ m0,s51,49152
v_min_u32 v4, 29440, v4
s_waitcnt vmcnt(6)
Zvav 64,180,144
v_readfirstlane_b32 s62, v11
v_lshrrev_b32 v8, 2, v6
Mvav 64,184,164
v_add_u32 v5, s75, v8
R_ v9,a251
Mvav 64,188,184
v_or_b32 v9, s73, v9
v_lshrrev_b32 v10, 8, v6
Zvav 68,180,148
v_bfe_u32 v12, v6, 6, 2
v_cmp_eq_u32 vcc, v10, v12
Mvav 68,184,168
v_and_b32 v10, 16, v6
v_mov_b32 v12, 0x3f80
Mvav 68,188,188
v_lshlrev_b32 v12, v10, v12
v_cndmask_b32 v12, 0, v12, vcc
Zvav 72,180,152
v_and_b32 v10, 32, v6
v_cmp_eq_u32 vcc, 0, v10
Mvav 72,184,172
X_ a68,0
X_ a69,0
Mvav 72,188,192
W_ 0
X_ a72,0
X_ a73,0
Zvav 92,220,192
s_nop 0
Ta 180,v2,24576
Ta 182,v2,26624
Mvav 92,196,172
v_cndmask_b32 v10, 0, v12, vcc
v_sub_u32 v12, v12, v10
Mvav 92,192,152
s_nop 0
Ta 184,v2,28672
Ta 186,v2,30720
Zvav 88,220,188
X_ a70,v10
X_ a71,v12
Mvav 88,196,168
s_mov_b64 s[32:33], s[20:21]
Ta 188,v2,32768
Ta 190,v2,34816
Mvav 88,192,148
s_mov_b32 s34, 409600
S_ s85,s6,2
Zvav 84,220,184
L_ v7,24,s55,0
Mvav 84,196,164
S_ s55,s71,133120
s_and_b32 s85, s85, 7
Mvav 84,192,144
S_ m0,s51,50176
s_lshl_b32 s85, s85, 7
Zvav 80,220,180
S_ s85,s85,s87
Mvav 80,196,160
L_ v7,24,s55,0
Mvav 80,192,140
S_ s55,s71,143360
Zvav 76,220,176
S_ m0,s51,51200
Mvav 76,196,156
Mvav 76,192,136
W_ 0
Zvav 16,180,136
s_nop 0
Ta 192,v3,24576
Ta 194,v3,26624
Mvav 16,184,156
L_ v7,24,s55,0
Mvav 16,188,176
S_ s55,s71,153600
Ta 196,v3,28672
Ta 198,v3,30720
Zvav 20,180,140
S_ m0,s51,52224
Mvav 20,184,160
s_nop 0
Ta 220,v3,32768
Ta 222,v3,34816
Mvav 20,188,180
L_ v7,24,s55,0
Zvav 24,180,144
S_ s55,s71,163840
Mvav 24,184,164
S_ m0,s51,53248
Mvav 24,188,184
Zvav 28,180,148
L_ v7,24,s55,0
Mvav 28,184,168
S_ s55,s71,174080
Mvav 28,188,188
S_ m0,s51,54272
Zvav 32,180,152
Mvav 32,184,172
L_ v7,24,s55,0
Mvav 32,188,192
s_waitcnt vmcnt(13) lgkmcnt(0)
s_barrier
S_ s71,s72,1152
Zvav 52,220,192
S_ s55,s71,491520
Ta 180,%0,0
Ta 182,%0,2048
Mvav 52,196,172
S_ m0,s51,36864
Mvav 52,192,152
s_nop 0
Ta 184,%0,4096
Ta 186,%0,6144
Zvav 48,220,188
L_ v7,24,s55,0
Mvav 48,196,168
S_ s55,s71,501760
Ta 188,%0,8192
Ta 190,%0,10240
Mvav 48,192,148
S_ m0,s51,37888
Zvav 44,220,184
Mvav 44,196,164
L_ v7,24,s55,0
Mvav 44,192,144
S_ s55,s71,512000
Zvav 40,220,180
S_ m0,s51,38912
Mvav 40,196,160
Mvav 40,192,140
L_ v7,24,s55,0
Zvav 36,220,176
S_ s55,s71,522240
Mvav 36,196,156
S_ m0,s51,39936
Mvav 36,192,136
W_ 0
Mvav 56,180,196
s_nop 0
Ta 192,v1,0
Ta 194,v1,2048
Mvav 56,184,216
L_ v7,24,s55,0
Mvav 56,188,236
S_ s55,s71,532480
Ta 196,v1,4096
Ta 198,v1,6144
Mvav 60,180,200
S_ m0,s51,40960
Mvav 60,184,220
s_nop 0
Ta 220,v1,8192
Ta 222,v1,10240
Mvav 60,188,240
L_ v7,24,s55,0
Mvav 64,180,204
S_ s55,s71,542720
Mvav 64,184,224
S_ m0,s51,41984
Mvav 64,188,244
Mvav 68,180,208
L_ v7,24,s55,0
Mvav 68,184,228
S_ s55,s71,614400
Mvav 68,188,248
S_ m0,s51,24576
Mvav 72,180,212
Mvav 72,184,232
L_ v7,24,s55,0
Mvav 72,188,252
W_ 0
S_ s55,s71,624640
Mvav 92,220,252
S_ m0,s51,25600
Ta 180,v2,0
Ta 182,v2,2048
Mvav 92,196,232
Mvav 92,192,212
s_nop 0
Ta 184,v2,4096
Ta 186,v2,6144
Mvav 88,220,248
L_ v7,24,s55,0
Mvav 88,196,228
S_ s55,s71,634880
Ta 188,v2,8192
Ta 190,v2,10240
Mvav 88,192,208
S_ m0,s51,26624
Mvav 84,220,244
Mvav 84,196,224
L_ v7,24,s55,0
Mvav 84,192,204
S_ s55,s71,645120
Mvav 80,220,240
S_ m0,s51,27648
Mvav 80,196,220
Mvav 80,192,200
L_ v7,24,s55,0
Mvav 76,220,236
S_ s55,s71,655360
Mvav 76,196,216
S_ m0,s51,28672
Mvav 76,192,196
W_ 0
Mvav 16,180,196
s_nop 0
Ta 192,v3,0
Ta 194,v3,2048
Mvav 16,184,216
L_ v7,24,s55,0
Mvav 16,188,236
S_ s55,s71,665600
Ta 196,v3,4096
Ta 198,v3,6144
Mvav 20,180,200
S_ m0,s51,29696
Mvav 20,184,220
s_nop 0
Ta 220,v3,8192
Ta 222,v3,10240
Mvav 20,188,240
L_ v7,24,s55,0
Mvav 24,180,204
Mvav 24,184,224
Mvav 24,188,244
Mvav 28,180,208
buffer_load_dword a74, v4, s[24:27], s85 offen
Mvav 28,184,228
Mvav 28,188,248
Mvav 32,180,212
Mvav 32,184,232
Mvav 32,188,252
s_waitcnt vmcnt(19) lgkmcnt(0)
s_barrier
Mvav 52,220,252
S_ s55,s71,0
Ta 180,%0,12288
Ta 182,%0,14336
Mvav 52,196,232
S_ m0,s51,0
Mvav 52,192,212
s_nop 0
Ta 184,%0,16384
Ta 186,%0,18432
Mvav 48,220,248
L_ v7,24,s55,0
Mvav 48,196,228
S_ s55,s71,10240
Ta 188,%0,20480
Ta 190,%0,22528
Mvav 48,192,208
S_ m0,s51,1024
Mvav 44,220,244
Mvav 44,196,224
L_ v7,24,s55,0
Mvav 44,192,204
S_ s55,s71,20480
Mvav 40,220,240
S_ m0,s51,2048
Mvav 40,196,220
Mvav 40,192,200
L_ v7,24,s55,0
Mvav 36,220,236
S_ s55,s71,30720
Mvav 36,196,216
S_ m0,s51,3072
Mvav 36,192,196
W_ 0
Mvaa 56,180,0
s_nop 0
Ta 192,v1,12288
Ta 194,v1,14336
Mvaa 56,184,40
L_ v7,24,s55,0
Mvaa 56,188,80
S_ s55,s71,40960
Ta 196,v1,16384
Ta 198,v1,18432
Mvaa 60,180,4
S_ m0,s51,4096
Mvaa 60,184,44
s_nop 0
Ta 220,v1,20480
Ta 222,v1,22528
Mvaa 60,188,84
L_ v7,24,s55,0
Mvaa 64,180,8
S_ s55,s71,51200
Mvaa 64,184,48
S_ m0,s51,5120
Mvaa 64,188,88
Mvaa 68,180,12
L_ v7,24,s55,0
Mvaa 68,184,52
Mvaa 68,188,240
Mvaa 72,180,16
Mvaa 72,184,56
Mvaa 72,188,244
W_ 0
Mvaa 92,220,244
s_nop 0
Ta 180,v2,12288
Ta 182,v2,14336
Mvaa 92,196,56
Mvaa 92,192,16
s_nop 0
Ta 184,v2,16384
Ta 186,v2,18432
Mvaa 88,220,240
Mvaa 88,196,52
s_nop 0
Ta 188,v2,20480
Ta 190,v2,22528
Mvaa 88,192,12
Mvaa 84,220,88
Mvaa 84,196,48
Mvaa 84,192,8
Mvaa 80,220,84
Mvaa 80,196,44
Mvaa 80,192,4
Mvaa 76,220,80
Mvaa 76,196,40
Mvaa 76,192,0
W_ 0
Mvaa 16,180,0
s_nop 0
Ta 192,v3,12288
Ta 194,v3,14336
Mvaa 16,184,40
Mvaa 16,188,80
s_nop 0
Ta 196,v3,16384
Ta 198,v3,18432
Mvaa 20,180,4
Mvaa 20,184,44
s_nop 0
Ta 220,v3,20480
Ta 222,v3,22528
Mvaa 20,188,84
Mvaa 24,180,8
Mvaa 24,184,48
Mvaa 24,188,88
Mvaa 28,180,12
Mvaa 28,184,52
Mvaa 28,188,240
Mvaa 32,180,16
Mvaa 32,184,56
Mvaa 32,188,244
s_waitcnt vmcnt(19) lgkmcnt(0)
s_barrier
Mvaa 52,220,244
s_nop 0
Ta 180,%0,49152
Ta 182,%0,51200
Mvaa 52,196,56
Mvaa 52,192,16
s_nop 0
Ta 184,%0,53248
Ta 186,%0,55296
Mvaa 48,220,240
Mvaa 48,196,52
s_nop 0
Ta 188,%0,57344
Ta 190,%0,59392
Mvaa 48,192,12
Mvaa 44,220,88
Mvaa 44,196,48
Mvaa 44,192,8
Mvaa 40,220,84
Mvaa 40,196,44
Mvaa 40,192,4
Mvaa 36,220,80
Mvaa 36,196,40
Mvaa 36,192,0
W_ 0
Mvaa 56,180,120
S_ s73,s78,163840
Ta 192,v1,49152
Ta 194,v1,51200
Mvaa 56,184,160
S_ s73,s73,4096
Mvaa 56,188,200
s_nop 0
Ta 196,v1,53248
Ta 198,v1,55296
Mvaa 60,180,124
s_mov_b32 m0, s52
Mvaa 60,184,164
s_nop 0
Ta 220,v1,57344
Ta 222,v1,59392
Mvaa 60,188,204
s_nop 0
Y_ 56,0
Y_ 57,256
Mvaa 64,180,128
s_nop 0
Y_ 58,512
Y_ 59,768
Mvaa 64,184,168
Mvaa 64,188,208
s_nop 0
Y_ 60,1024
Y_ 61,1280
)A"
R"A(Mvaa 68,180,132
s_nop 0
Y_ 62,1536
Y_ 63,1792
Mvaa 68,184,172
Mvaa 68,188,212
s_nop 0
Y_ 64,2048
Y_ 65,2304
Mvaa 72,180,136
s_nop 0
Y_ 66,2560
Y_ 67,2816
Mvaa 72,184,176
Mvaa 72,188,216
W_ 12
Y_ 68,3072
Y_ 69,3328
Mvaa 92,220,216
s_nop 0
Ta 180,v2,49152
Ta 182,v2,51200
Mvaa 92,196,176
s_nop 0
Y_ 70,3584
Y_ 71,3840
Mvaa 92,192,136
s_nop 0
Ta 184,v2,53248
Ta 186,v2,55296
Mvaa 88,220,212
s_nop 0
Y_ 72,4096
Y_ 73,4352
Mvaa 88,196,172
s_nop 0
Ta 188,v2,57344
Ta 190,v2,59392
Mvaa 88,192,132
s_nop 0
Y_ 74,4608
Y_ 75,4864
Mvaa 84,220,208
s_nop 0
Y_ 92,9216
Y_ 93,9472
Mvaa 84,196,168
s_nop 0
Y_ 94,9728
Y_ 95,9984
Mvaa 84,192,128
s_nop 0
Y_ 88,8192
Y_ 89,8448
Mvaa 80,220,204
s_nop 0
Y_ 90,8704
Y_ 91,8960
Mvaa 80,196,164
Mvaa 80,192,124
s_nop 0
Y_ 84,7168
Y_ 85,7424
Mvaa 76,220,200
s_nop 0
Y_ 86,7680
Y_ 87,7936
Mvaa 76,196,160
Mvaa 76,192,120
W_ 14
Y_ 80,6144
Y_ 81,6400
Mvaa 16,180,120
s_nop 0
Ta 192,v3,49152
Ta 194,v3,51200
Mvaa 16,184,160
s_nop 0
Y_ 82,6656
Y_ 83,6912
Mvaa 16,188,200
s_nop 0
Ta 196,v3,53248
Ta 198,v3,55296
Mvaa 20,180,124
s_nop 0
Y_ 76,5120
Y_ 77,5376
Mvaa 20,184,164
s_nop 0
Ta 220,v3,57344
Ta 222,v3,59392
Mvaa 20,188,204
s_nop 0
Y_ 78,5632
Y_ 79,5888
Mvaa 24,180,128
Mvaa 24,184,168
Mvaa 24,188,208
s_waitcnt vmcnt(13) lgkmcnt(0)
s_barrier
Mvaa 28,180,132
S_ s55,s71,122880
ds_read2st64_b32 v[96:97], v5 offset0:0 offset1:1
Mvaa 28,184,172
S_ m0,s51,12288
ds_read2st64_b32 v[98:99], v5 offset0:2 offset1:3
Mvaa 28,188,212
L_ v7,24,s55,0
Mvaa 32,180,136
S_ s55,s71,133120
ds_read2st64_b32 v[104:105], v5 offset0:4 offset1:5
Mvaa 32,184,176
S_ m0,s51,13312
ds_read2st64_b32 v[106:107], v5 offset0:6 offset1:7
Mvaa 32,188,216
ds_read2st64_b32 v[112:113], v5 offset0:8 offset1:9
Mvaa 52,220,216
s_nop 0
Ta 180,%0,36864
Ta 182,%0,38912
Mvaa 52,196,176
L_ v7,24,s55,0
Mvaa 52,192,136
W_ 5
Ta 184,%0,40960
Ta 186,%0,43008
Mvaa 48,220,212
S_ s55,s71,143360
ds_read2st64_b32 v[114:115], v5 offset0:10 offset1:11
A_ v96,v16,v96
Mvaa 48,196,172
S_ m0,s51,14336
Ta 188,%0,45056
Ta 190,%0,47104
Mvaa 48,192,132
L_ v7,24,s55,0
Mvaa 44,220,208
S_ s55,s71,153600
ds_read2st64_b32 v[120:121], v5 offset0:12 offset1:13
A_ v97,v17,v97
Mvaa 44,196,168
S_ m0,s51,15360
ds_read2st64_b32 v[122:123], v5 offset0:14 offset1:15
A_ v98,v18,v98
Mvaa 44,192,128
ds_read2st64_b32 v[128:129], v5 offset0:16 offset1:17
A_ v99,v19,v99
Mvaa 40,220,204
L_ v7,24,s55,0
Mvaa 40,196,164
W_ 11
ds_read2st64_b32 v[130:131], v5 offset0:18 offset1:19
A_ v104,v20,v104
Mvaa 40,192,124
S_ s55,s71,163840
ds_read2st64_b32 v[132:133], v5 offset0:36 offset1:37
A_ v105,v21,v105
Mvaa 36,220,200
S_ m0,s51,16384
ds_read2st64_b32 v[134:135], v5 offset0:38 offset1:39
A_ v106,v22,v106
Mvaa 36,196,160
L_ v7,24,s55,0
Mvaa 36,192,120
W_ 6
ds_read2st64_b32 v[124:125], v5 offset0:32 offset1:33
A_ v107,v23,v107
Zvav 56,180,136
Ta 192,v1,36864
Ta 194,v1,38912
Mvav 56,184,156
S_ s55,s71,174080
ds_read2st64_b32 v[126:127], v5 offset0:34 offset1:35
A_ v112,v24,v112
Mvav 56,188,176
S_ m0,s51,17408
Ta 196,v1,40960
Ta 198,v1,43008
Zvav 60,180,140
L_ v7,24,s55,0
Mvav 60,184,160
S_ s79,s6,1
Ta 220,v1,45056
Ta 222,v1,47104
Mvav 60,188,180
s_and_b32 s79, s79, 7
ds_read2st64_b32 v[116:117], v5 offset0:28 offset1:29
A_ v113,v25,v113
Zvav 64,180,144
s_lshl_b32 s79, s79, 7
ds_read2st64_b32 v[118:119], v5 offset0:30 offset1:31
A_ v114,v26,v114
Mvav 64,184,164
S_ s71,s72,s79
ds_read2st64_b32 v[108:109], v5 offset0:24 offset1:25
A_ v115,v27,v115
Mvav 64,188,184
W_ 14
ds_read2st64_b32 v[110:111], v5 offset0:26 offset1:27
A_ v120,v28,v120
Zvav 68,180,148
S_ s55,s71,491520
ds_read2st64_b32 v[100:101], v5 offset0:20 offset1:21
A_ v121,v29,v121
Mvav 68,184,168
S_ m0,s51,49152
ds_read2st64_b32 v[102:103], v5 offset0:22 offset1:23
A_ v122,v30,v122
Mvav 68,188,188
L_ v7,24,s55,0
Zvav 72,180,152
S_ s55,s71,501760
A_ v123,v31,v123
R_ v12,a248
Mvav 72,184,172
W_ 14
A_ v128,v32,v128
A_ v129,v33,v129
Mvav 72,188,192
W_ 6
A_ v130,v34,v130
A_ v131,v35,v131
Zvav 92,220,192
Ta 180,v2,36864
Ta 182,v2,38912
Mvav 92,196,172
S_ m0,s51,50176
A_ v132,v52,v132
A_ v133,v53,v133
Mvav 92,192,152
s_nop 0
Ta 184,v2,40960
Ta 186,v2,43008
Zvav 88,220,188
L_ v7,24,s55,0
Mvav 88,196,168
S_ s55,s71,512000
Ta 188,v2,45056
Ta 190,v2,47104
Mvav 88,192,148
S_ m0,s51,51200
A_ v134,v54,v134
A_ v135,v55,v135
Zvav 84,220,184
A_ v124,v48,v124
A_ v125,v49,v125
Mvav 84,196,164
L_ v7,24,s55,0
Mvav 84,192,144
S_ s55,s71,522240
A_ v126,v50,v126
A_ v127,v51,v127
Zvav 80,220,180
W_ 10
A_ v116,v44,v116
A_ v117,v45,v117
Mvav 80,196,160
S_ m0,s51,52224
A_ v118,v46,v118
A_ v119,v47,v119
Mvav 80,192,140
L_ v7,24,s55,0
Zvav 76,220,176
W_ 8
A_ v108,v40,v108
A_ v109,v41,v109
Mvav 76,196,156
S_ s55,s71,532480
A_ v110,v42,v110
A_ v111,v43,v111
Mvav 76,192,136
W_ 0
A_ v100,v36,v100
Zvav 16,180,136
S_ m0,s51,53248
Ta 192,v3,36864
Ta 194,v3,38912
Mvav 16,184,156
L_ v7,24,s55,0
Mvav 16,188,176
S_ s55,s71,542720
Ta 196,v3,40960
Ta 198,v3,43008
Zvav 20,180,140
S_ m0,s51,54272
A_ v101,v37,v101
A_ v102,v38,v102
Mvav 20,184,160
S_ s85,s6,3
Ta 220,v3,45056
Ta 222,v3,47104
Mvav 20,188,180
L_ v7,24,s55,0
Zvav 24,180,144
s_and_b32 s85, s85, 7
A_ v103,v39,v103
C_ 131,130,131
Mvav 24,184,164
s_lshl_b32 s85, s85, 7
C_ 130,128,129
C_ 132,132,133
Mvav 24,188,184
S_ s85,s85,s87
C_ 133,134,135
v_min_u32 v12, 240, v12
Zvav 28,180,148
buffer_store_dwordx4 v[130:133], v6, s[28:31], s73 offen offset:0 sc1
Mvav 28,184,168
s_sub_u32 s73, s73, 4096
v_lshlrev_b32 v12, 3, v12
Mvav 28,188,188
C_ 123,122,123
C_ 122,120,121
Zvav 32,180,152
C_ 124,124,125
C_ 125,126,127
Mvav 32,184,172
buffer_store_dwordx4 v[122:125], v6, s[28:31], s73 offen offset:3072 sc1
Mvav 32,188,192
s_waitcnt vmcnt(21) lgkmcnt(0)
s_barrier
C_ 115,114,115
Zvav 52,220,192
S_ s55,s71,614400
Ta 180,%0,24576
Ta 182,%0,26624
Mvav 52,196,172
S_ m0,s51,36864
C_ 114,112,113
C_ 116,116,117
Mvav 52,192,152
s_nop 0
Ta 184,%0,28672
Ta 186,%0,30720
Zvav 48,220,188
L_ v7,24,s55,0
Mvav 48,196,168
S_ s55,s71,624640
Ta 188,%0,32768
Ta 190,%0,34816
Mvav 48,192,148
S_ m0,s51,37888
C_ 117,118,119
Zvav 44,220,184
Mvav 44,196,164
buffer_store_dwordx4 v[114:117], v6, s[28:31], s73 offen offset:2048 sc1
Mvav 44,192,144
C_ 107,106,107
C_ 106,104,105
Zvav 40,220,180
C_ 108,108,109
C_ 109,110,111
Mvav 40,196,160
Mvav 40,192,140
buffer_store_dwordx4 v[106:109], v6, s[28:31], s73 offen offset:1024 sc1
Zvav 36,220,176
C_ 99,98,99
C_ 98,96,97
Mvav 36,196,156
C_ 100,100,101
C_ 101,102,103
Mvav 36,192,136
W_ 0
Mvav 56,180,196
s_nop 0
Ta 192,v1,24576
Ta 194,v1,26624
Mvav 56,184,216
buffer_store_dwordx4 v[98:101], v6, s[28:31], s73 offen offset:0 sc1
Mvav 56,188,236
s_nop 0
Ta 196,v1,28672
Ta 198,v1,30720
Mvav 60,180,200
Mvav 60,184,220
s_nop 0
Ta 220,v1,32768
Ta 222,v1,34816
Mvav 60,188,240
L_ v7,24,s55,0
Mvav 64,180,204
S_ s55,s71,634880
Mvav 64,184,224
S_ m0,s51,38912
Mvav 64,188,244
Mvav 68,180,208
L_ v7,24,s55,0
Mvav 68,184,228
S_ s55,s71,645120
Mvav 68,188,248
S_ m0,s51,39936
Mvav 72,180,212
Mvav 72,184,232
L_ v7,24,s55,0
Mvav 72,188,252
W_ 0
S_ s55,s71,655360
Mvav 92,220,252
S_ m0,s51,40960
Ta 180,v2,24576
Ta 182,v2,26624
Mvav 92,196,232
Mvav 92,192,212
s_nop 0
Ta 184,v2,28672
Ta 186,v2,30720
Mvav 88,220,248
L_ v7,24,s55,0
Mvav 88,196,228
S_ s55,s71,665600
Ta 188,v2,32768
Ta 190,v2,34816
Mvav 88,192,208
S_ m0,s51,41984
Mvav 84,220,244
Mvav 84,196,224
L_ v7,24,s55,0
Mvav 84,192,204
Mvav 80,220,240
Mvav 80,196,220
Mvav 80,192,200
buffer_load_dword a74, v12, s[44:47], s66 offen
Mvav 76,220,236
Mvav 76,196,216
Mvav 76,192,196
W_ 0
Mvav 16,180,196
s_nop 0
Ta 192,v3,24576
Ta 194,v3,26624
Mvav 16,184,216
buffer_load_dword a74, v4, s[24:27], s85 offen
Mvav 16,188,236
s_nop 0
Ta 196,v3,28672
Ta 198,v3,30720
Mvav 20,180,200
Mvav 20,184,220
s_nop 0
Ta 220,v3,32768
Ta 222,v3,34816
Mvav 20,188,240
Mvav 24,180,204
Mvav 24,184,224
Mvav 24,188,244
Mvav 28,180,208
Mvav 28,184,228
Mvav 28,188,248
Mvav 32,180,212
Mvav 32,184,232
Mvav 32,188,252
s_waitcnt vmcnt(25) lgkmcnt(0)
s_barrier
Mvav 52,220,252
S_ s55,s71,0
Ta 180,%0,0
Ta 182,%0,2048
Mvav 52,196,232
S_ m0,s51,24576
Mvav 52,192,212
s_nop 0
Ta 184,%0,4096
Ta 186,%0,6144
Mvav 48,220,248
L_ v7,24,s55,0
Mvav 48,196,228
S_ s55,s71,10240
Ta 188,%0,8192
Ta 190,%0,10240
Mvav 48,192,208
S_ m0,s51,25600
Mvav 44,220,244
Mvav 44,196,224
L_ v7,24,s55,0
Mvav 44,192,204
S_ s55,s71,20480
Mvav 40,220,240
S_ m0,s51,26624
Mvav 40,196,220
Mvav 40,192,200
L_ v7,24,s55,0
Mvav 36,220,236
S_ s55,s71,30720
Mvav 36,196,216
S_ m0,s51,27648
Mvav 36,192,196
W_ 0
Mvaa 56,180,0
s_nop 0
Ta 192,v1,0
Ta 194,v1,2048
Mvaa 56,184,40
L_ v7,24,s55,0
Mvaa 56,188,80
S_ s55,s71,40960
Ta 196,v1,4096
Ta 198,v1,6144
Mvaa 60,180,4
S_ m0,s51,28672
Mvaa 60,184,44
s_nop 0
Ta 220,v1,8192
Ta 222,v1,10240
Mvaa 60,188,84
L_ v7,24,s55,0
Mvaa 64,180,8
S_ s55,s71,51200
Mvaa 64,184,48
S_ m0,s51,29696
Mvaa 64,188,88
Mvaa 68,180,12
L_ v7,24,s55,0
Mvaa 68,184,52
Mvaa 68,188,240
Mvaa 72,180,16
Mvaa 72,184,56
Mvaa 72,188,244
W_ 0
Mvaa 92,220,244
s_nop 0
Ta 180,v2,0
Ta 182,v2,2048
Mvaa 92,196,56
Mvaa 92,192,16
s_nop 0
Ta 184,v2,4096
Ta 186,v2,6144
Mvaa 88,220,240
Mvaa 88,196,52
s_nop 0
Ta 188,v2,8192
Ta 190,v2,10240
Mvaa 88,192,12
Mvaa 84,220,88
Mvaa 84,196,48
Mvaa 84,192,8
Mvaa 80,220,84
Mvaa 80,196,44
Mvaa 80,192,4
Mvaa 76,220,80
Mvaa 76,196,40
Mvaa 76,192,0
W_ 0
Mvaa 16,180,0
s_nop 0
Ta 192,v3,0
Ta 194,v3,2048
Mvaa 16,184,40
Mvaa 16,188,80
s_nop 0
Ta 196,v3,4096
Ta 198,v3,6144
Mvaa 20,180,4
Mvaa 20,184,44
s_nop 0
Ta 220,v3,8192
Ta 222,v3,10240
Mvaa 20,188,84
Mvaa 24,180,8
Mvaa 24,184,48
Mvaa 24,188,88
Mvaa 28,180,12
Mvaa 28,184,52
Mvaa 28,188,240
Mvaa 32,180,16
Mvaa 32,184,56
Mvaa 32,188,244
s_waitcnt vmcnt(25) lgkmcnt(0)
s_barrier
Mvaa 52,220,244
s_nop 0
Ta 180,%0,12288
Ta 182,%0,14336
Mvaa 52,196,56
Mvaa 52,192,16
s_nop 0
Ta 184,%0,16384
Ta 186,%0,18432
Mvaa 48,220,240
Mvaa 48,196,52
s_nop 0
Ta 188,%0,20480
Ta 190,%0,22528
Mvaa 48,192,12
Mvaa 44,220,88
Mvaa 44,196,48
Mvaa 44,192,8
Mvaa 40,220,84
Mvaa 40,196,44
Mvaa 40,192,4
Mvaa 36,220,80
Mvaa 36,196,40
Mvaa 36,192,0
W_ 0
Mvaa 56,180,120
S_ s73,s78,184320
Ta 192,v1,12288
Ta 194,v1,14336
Mvaa 56,184,160
S_ s73,s73,4096
Mvaa 56,188,200
s_nop 0
Ta 196,v1,16384
Ta 198,v1,18432
Mvaa 60,180,124
s_mov_b32 m0, s52
Mvaa 60,184,164
s_nop 0
Ta 220,v1,20480
Ta 222,v1,22528
Mvaa 60,188,204
s_nop 0
Y_ 56,0
Y_ 57,256
Mvaa 64,180,128
s_nop 0
Y_ 58,512
Y_ 59,768
Mvaa 64,184,168
Mvaa 64,188,208
s_nop 0
Y_ 60,1024
Y_ 61,1280
Mvaa 68,180,132
s_nop 0
Y_ 62,1536
Y_ 63,1792
Mvaa 68,184,172
Mvaa 68,188,212
s_nop 0
Y_ 64,2048
Y_ 65,2304
Mvaa 72,180,136
s_nop 0
Y_ 66,2560
Y_ 67,2816
Mvaa 72,184,176
Mvaa 72,188,216
W_ 12
Y_ 68,3072
Y_ 69,3328
Mvaa 92,220,216
s_nop 0
Ta 180,v2,12288
Ta 182,v2,14336
Mvaa 92,196,176
s_nop 0
Y_ 70,3584
Y_ 71,3840
Mvaa 92,192,136
s_nop 0
Ta 184,v2,16384
Ta 186,v2,18432
Mvaa 88,220,212
s_nop 0
Y_ 72,4096
Y_ 73,4352
Mvaa 88,196,172
s_nop 0
Ta 188,v2,20480
Ta 190,v2,22528
Mvaa 88,192,132
s_nop 0
Y_ 74,4608
Y_ 75,4864
Mvaa 84,220,208
s_nop 0
Y_ 92,9216
Y_ 93,9472
Mvaa 84,196,168
s_nop 0
Y_ 94,9728
Y_ 95,9984
Mvaa 84,192,128
s_nop 0
Y_ 88,8192
Y_ 89,8448
Mvaa 80,220,204
s_nop 0
Y_ 90,8704
Y_ 91,8960
Mvaa 80,196,164
Mvaa 80,192,124
s_nop 0
Y_ 84,7168
Y_ 85,7424
Mvaa 76,220,200
s_nop 0
Y_ 86,7680
Y_ 87,7936
Mvaa 76,196,160
Mvaa 76,192,120
W_ 14
Y_ 80,6144
Y_ 81,6400
Mvaa 16,180,120
s_nop 0
Ta 192,v3,12288
Ta 194,v3,14336
Mvaa 16,184,160
s_nop 0
Y_ 82,6656
Y_ 83,6912
Mvaa 16,188,200
s_nop 0
Ta 196,v3,16384
Ta 198,v3,18432
Mvaa 20,180,124
s_nop 0
Y_ 76,5120
Y_ 77,5376
Mvaa 20,184,164
s_nop 0
Ta 220,v3,20480
Ta 222,v3,22528
Mvaa 20,188,204
s_nop 0
Y_ 78,5632
Y_ 79,5888
Mvaa 24,180,128
Mvaa 24,184,168
Mvaa 24,188,208
s_waitcnt vmcnt(19) lgkmcnt(0)
s_barrier
Mvaa 28,180,132
S_ s55,s71,122880
ds_read2st64_b32 v[96:97], v5 offset0:0 offset1:1
Mvaa 28,184,172
S_ m0,s51,0
ds_read2st64_b32 v[98:99], v5 offset0:2 offset1:3
Mvaa 28,188,212
L_ v7,24,s55,0
Mvaa 32,180,136
S_ s55,s71,133120
ds_read2st64_b32 v[104:105], v5 offset0:4 offset1:5
Mvaa 32,184,176
S_ m0,s51,1024
ds_read2st64_b32 v[106:107], v5 offset0:6 offset1:7
Mvaa 32,188,216
ds_read2st64_b32 v[112:113], v5 offset0:8 offset1:9
Mvaa 52,220,216
s_nop 0
Ta 180,%0,49152
Ta 182,%0,51200
Mvaa 52,196,176
L_ v7,24,s55,0
Mvaa 52,192,136
W_ 5
Ta 184,%0,53248
Ta 186,%0,55296
Mvaa 48,220,212
S_ s55,s71,143360
ds_read2st64_b32 v[114:115], v5 offset0:10 offset1:11
A_ v96,v16,v96
Mvaa 48,196,172
S_ m0,s51,2048
Ta 188,%0,57344
Ta 190,%0,59392
Mvaa 48,192,132
L_ v7,24,s55,0
Mvaa 44,220,208
S_ s55,s71,153600
ds_read2st64_b32 v[120:121], v5 offset0:12 offset1:13
A_ v97,v17,v97
Mvaa 44,196,168
S_ m0,s51,3072
ds_read2st64_b32 v[122:123], v5 offset0:14 offset1:15
A_ v98,v18,v98
Mvaa 44,192,128
ds_read2st64_b32 v[128:129], v5 offset0:16 offset1:17
A_ v99,v19,v99
Mvaa 40,220,204
L_ v7,24,s55,0
Mvaa 40,196,164
W_ 11
ds_read2st64_b32 v[130:131], v5 offset0:18 offset1:19
A_ v104,v20,v104
Mvaa 40,192,124
S_ s55,s71,163840
ds_read2st64_b32 v[132:133], v5 offset0:36 offset1:37
A_ v105,v21,v105
Mvaa 36,220,200
S_ m0,s51,4096
ds_read2st64_b32 v[134:135], v5 offset0:38 offset1:39
A_ v106,v22,v106
Mvaa 36,196,160
L_ v7,24,s55,0
Mvaa 36,192,120
W_ 6
ds_read2st64_b32 v[124:125], v5 offset0:32 offset1:33
A_ v107,v23,v107
Zvav 56,180,136
Ta 192,v1,49152
Ta 194,v1,51200
Mvav 56,184,156
S_ s55,s71,174080
ds_read2st64_b32 v[126:127], v5 offset0:34 offset1:35
A_ v112,v24,v112
Mvav 56,188,176
S_ m0,s51,5120
Ta 196,v1,53248
Ta 198,v1,55296
Zvav 60,180,140
L_ v7,24,s55,0
Mvav 60,184,160
S_ s79,s6,2
Ta 220,v1,57344
Ta 222,v1,59392
Mvav 60,188,180
s_and_b32 s79, s79, 7
ds_read2st64_b32 v[116:117], v5 offset0:28 offset1:29
A_ v113,v25,v113
Zvav 64,180,144
s_lshl_b32 s79, s79, 7
ds_read2st64_b32 v[118:119], v5 offset0:30 offset1:31
A_ v114,v26,v114
Mvav 64,184,164
S_ s71,s72,s79
ds_read2st64_b32 v[108:109], v5 offset0:24 offset1:25
A_ v115,v27,v115
Mvav 64,188,184
W_ 14
ds_read2st64_b32 v[110:111], v5 offset0:26 offset1:27
A_ v120,v28,v120
Zvav 68,180,148
S_ s55,s71,491520
ds_read2st64_b32 v[100:101], v5 offset0:20 offset1:21
A_ v121,v29,v121
Mvav 68,184,168
S_ m0,s51,12288
ds_read2st64_b32 v[102:103], v5 offset0:22 offset1:23
A_ v122,v30,v122
Mvav 68,188,188
L_ v7,24,s55,0
Zvav 72,180,152
S_ s55,s71,501760
A_ v123,v31,v123
Mvav 72,184,172
W_ 14
A_ v128,v32,v128
A_ v129,v33,v129
Mvav 72,188,192
W_ 6
A_ v130,v34,v130
A_ v131,v35,v131
Zvav 92,220,192
Ta 180,v2,49152
Ta 182,v2,51200
Mvav 92,196,172
S_ m0,s51,13312
A_ v132,v52,v132
A_ v133,v53,v133
Mvav 92,192,152
s_nop 0
Ta 184,v2,53248
Ta 186,v2,55296
Zvav 88,220,188
L_ v7,24,s55,0
Mvav 88,196,168
S_ s55,s71,512000
Ta 188,v2,57344
Ta 190,v2,59392
Mvav 88,192,148
S_ m0,s51,14336
A_ v134,v54,v134
A_ v135,v55,v135
Zvav 84,220,184
A_ v124,v48,v124
A_ v125,v49,v125
Mvav 84,196,164
L_ v7,24,s55,0
Mvav 84,192,144
S_ s55,s71,522240
A_ v126,v50,v126
A_ v127,v51,v127
Zvav 80,220,180
W_ 10
A_ v116,v44,v116
A_ v117,v45,v117
Mvav 80,196,160
S_ m0,s51,15360
A_ v118,v46,v118
A_ v119,v47,v119
Mvav 80,192,140
L_ v7,24,s55,0
Zvav 76,220,176
W_ 8
A_ v108,v40,v108
A_ v109,v41,v109
Mvav 76,196,156
S_ s55,s71,532480
A_ v110,v42,v110
A_ v111,v43,v111
Mvav 76,192,136
W_ 0
A_ v100,v36,v100
Zvav 16,180,136
S_ m0,s51,16384
Ta 192,v3,49152
Ta 194,v3,51200
Mvav 16,184,156
L_ v7,24,s55,0
Mvav 16,188,176
S_ s55,s71,542720
Ta 196,v3,53248
Ta 198,v3,55296
Zvav 20,180,140
S_ m0,s51,17408
A_ v101,v37,v101
A_ v102,v38,v102
Mvav 20,184,160
S_ s85,s6,4
Ta 220,v3,57344
Ta 222,v3,59392
Mvav 20,188,180
L_ v7,24,s55,0
Zvav 24,180,144
s_and_b32 s85, s85, 7
A_ v103,v39,v103
C_ 131,130,131
Mvav 24,184,164
s_lshl_b32 s85, s85, 7
C_ 130,128,129
C_ 132,132,133
Mvav 24,188,184
S_ s85,s85,s87
C_ 133,134,135
Zvav 28,180,148
buffer_store_dwordx4 v[130:133], v6, s[28:31], s73 offen offset:0 sc1
Mvav 28,184,168
s_sub_u32 s73, s73, 4096
Mvav 28,188,188
C_ 123,122,123
C_ 122,120,121
Zvav 32,180,152
C_ 124,124,125
C_ 125,126,127
Mvav 32,184,172
buffer_store_dwordx4 v[122:125], v6, s[28:31], s73 offen offset:3072 sc1
Mvav 32,188,192
s_waitcnt vmcnt(22) lgkmcnt(0)
s_barrier
C_ 115,114,115
Zvav 52,220,192
S_ s55,s71,614400
Ta 180,%0,36864
Ta 182,%0,38912
Mvav 52,196,172
S_ m0,s51,49152
C_ 114,112,113
C_ 116,116,117
Mvav 52,192,152
s_nop 0
Ta 184,%0,40960
Ta 186,%0,43008
Zvav 48,220,188
L_ v7,24,s55,0
Mvav 48,196,168
S_ s55,s71,624640
Ta 188,%0,45056
Ta 190,%0,47104
Mvav 48,192,148
S_ m0,s51,50176
C_ 117,118,119
Zvav 44,220,184
Mvav 44,196,164
buffer_store_dwordx4 v[114:117], v6, s[28:31], s73 offen offset:2048 sc1
Mvav 44,192,144
C_ 107,106,107
C_ 106,104,105
Zvav 40,220,180
C_ 108,108,109
C_ 109,110,111
Mvav 40,196,160
Mvav 40,192,140
buffer_store_dwordx4 v[106:109], v6, s[28:31], s73 offen offset:1024 sc1
Zvav 36,220,176
C_ 99,98,99
C_ 98,96,97
Mvav 36,196,156
C_ 100,100,101
C_ 101,102,103
Mvav 36,192,136
W_ 0
Mvav 56,180,196
s_nop 0
Ta 192,v1,36864
Ta 194,v1,38912
Mvav 56,184,216
buffer_store_dwordx4 v[98:101], v6, s[28:31], s73 offen offset:0 sc1
Mvav 56,188,236
s_nop 0
Ta 196,v1,40960
Ta 198,v1,43008
Mvav 60,180,200
X_ a20,-1
X_ a21,-1
Mvav 60,184,220
s_nop 0
Ta 220,v1,45056
Ta 222,v1,47104
Mvav 60,188,240
L_ v7,24,s55,0
Mvav 64,180,204
S_ s55,s71,634880
X_ a22,-1
X_ a23,-1
Mvav 64,184,224
S_ s81,s6,7
S_ m0,s51,51200
Mvav 64,188,244
s_and_b32 s81, s81, 7
Mvav 68,180,208
L_ v7,24,s55,0
Mvav 68,184,228
s_mul_i32 s81, s81, 20480
S_ s55,s71,645120
Mvav 68,188,248
S_ s81,s81,s78
S_ m0,s51,52224
Mvav 72,180,212
Mvav 72,184,232
buffer_store_dwordx4 a[20:23], v6, s[28:31], s81 offen offset:0 sc1
Mvav 72,188,252
W_ 0
Mvav 92,220,252
s_nop 0
Ta 180,v2,36864
Ta 182,v2,38912
Mvav 92,196,232
Mvav 92,192,212
s_nop 0
Ta 184,v2,40960
Ta 186,v2,43008
Mvav 88,220,248
buffer_store_dwordx4 a[20:23], v6, s[28:31], s81 offen offset:1024 sc1
Mvav 88,196,228
s_nop 0
Ta 188,v2,45056
Ta 190,v2,47104
Mvav 88,192,208
Mvav 84,220,244
Mvav 84,196,224
buffer_store_dwordx4 a[20:23], v6, s[28:31], s81 offen offset:2048 sc1
Mvav 84,192,204
Mvav 80,220,240
Mvav 80,196,220
Mvav 80,192,200
buffer_store_dwordx4 a[20:23], v6, s[28:31], s81 offen offset:3072 sc1
Mvav 76,220,236
S_ s81,s81,4096
Mvav 76,196,216
Mvav 76,192,196
W_ 0
Mvav 16,180,196
s_nop 0
Ta 192,v3,36864
Ta 194,v3,38912
Mvav 16,184,216
buffer_store_dwordx4 a[20:23], v6, s[28:31], s81 offen offset:0 sc1
Mvav 16,188,236
s_nop 0
Ta 196,v3,40960
Ta 198,v3,43008
Mvav 20,180,200
Mvav 20,184,220
s_nop 0
Ta 220,v3,45056
Ta 222,v3,47104
Mvav 20,188,240
L_ v7,24,s55,0
Mvav 24,180,204
S_ s55,s71,655360
Mvav 24,184,224
S_ m0,s51,53248
Mvav 24,188,244
Mvav 28,180,208
L_ v7,24,s55,0
Mvav 28,184,228
S_ s55,s71,665600
Mvav 28,188,248
S_ m0,s51,54272
Mvav 32,180,212
Mvav 32,184,232
L_ v7,24,s55,0
Mvav 32,188,252
s_waitcnt vmcnt(28) lgkmcnt(0)
s_barrier
Mvav 52,220,252
S_ s55,s71,0
Ta 180,%0,24576
Ta 182,%0,26624
Mvav 52,196,232
S_ m0,s51,36864
Mvav 52,192,212
s_nop 0
Ta 184,%0,28672
Ta 186,%0,30720
Mvav 48,220,248
L_ v7,24,s55,0
Mvav 48,196,228
S_ s55,s71,10240
Ta 188,%0,32768
Ta 190,%0,34816
Mvav 48,192,208
S_ m0,s51,37888
Mvav 44,220,244
Mvav 44,196,224
L_ v7,24,s55,0
Mvav 44,192,204
S_ s55,s71,20480
Mvav 40,220,240
S_ m0,s51,38912
Mvav 40,196,220
Mvav 40,192,200
L_ v7,24,s55,0
Mvav 36,220,236
S_ s55,s71,30720
Mvav 36,196,216
S_ m0,s51,39936
Mvav 36,192,196
W_ 0
Mvaa 56,180,0
s_nop 0
Ta 192,v1,24576
Ta 194,v1,26624
Mvaa 56,184,40
L_ v7,24,s55,0
Mvaa 56,188,80
S_ s55,s71,40960
Ta 196,v1,28672
Ta 198,v1,30720
Mvaa 60,180,4
S_ m0,s51,40960
Mvaa 60,184,44
s_nop 0
Ta 220,v1,32768
Ta 222,v1,34816
Mvaa 60,188,84
L_ v7,24,s55,0
Mvaa 64,180,8
S_ s55,s71,51200
Mvaa 64,184,48
S_ m0,s51,41984
Mvaa 64,188,88
Mvaa 68,180,12
L_ v7,24,s55,0
Mvaa 68,184,52
Mvaa 68,188,240
Mvaa 72,180,16
Mvaa 72,184,56
buffer_load_dword a74, v4, s[24:27], s85 offen
Mvaa 72,188,244
W_ 0
Mvaa 92,220,244
s_nop 0
Ta 180,v2,24576
Ta 182,v2,26624
Mvaa 92,196,56
Mvaa 92,192,16
s_nop 0
Ta 184,v2,28672
Ta 186,v2,30720
Mvaa 88,220,240
Mvaa 88,196,52
s_nop 0
Ta 188,v2,32768
Ta 190,v2,34816
Mvaa 88,192,12
Mvaa 84,220,88
Mvaa 84,196,48
Mvaa 84,192,8
Mvaa 80,220,84
Mvaa 80,196,44
Mvaa 80,192,4
Mvaa 76,220,80
Mvaa 76,196,40
Mvaa 76,192,0
W_ 0
Mvaa 16,180,0
s_nop 0
Ta 192,v3,24576
Ta 194,v3,26624
Mvaa 16,184,40
Mvaa 16,188,80
s_nop 0
Ta 196,v3,28672
Ta 198,v3,30720
Mvaa 20,180,4
Mvaa 20,184,44
s_nop 0
Ta 220,v3,32768
Ta 222,v3,34816
Mvaa 20,188,84
Mvaa 24,180,8
Mvaa 24,184,48
Mvaa 24,188,88
Mvaa 28,180,12
Mvaa 28,184,52
Mvaa 28,188,240
Mvaa 32,180,16
Mvaa 32,184,56
Mvaa 32,188,244
s_waitcnt vmcnt(29) lgkmcnt(0)
s_barrier
Mvaa 52,220,244
s_nop 0
Ta 180,%0,0
Ta 182,%0,2048
Mvaa 52,196,56
Mvaa 52,192,16
s_nop 0
Ta 184,%0,4096
Ta 186,%0,6144
Mvaa 48,220,240
Mvaa 48,196,52
s_nop 0
Ta 188,%0,8192
Ta 190,%0,10240
Mvaa 48,192,12
Mvaa 44,220,88
Mvaa 44,196,48
Mvaa 44,192,8
Mvaa 40,220,84
Mvaa 40,196,44
Mvaa 40,192,4
Mvaa 36,220,80
Mvaa 36,196,40
Mvaa 36,192,0
W_ 0
Mvaa 56,180,120
S_ s81,s6,1
Ta 192,v1,0
Ta 194,v1,2048
Mvaa 56,184,160
s_and_b32 s81, s81, 7
Mvaa 56,188,200
s_mul_i32 s81, s81, 20480
Ta 196,v1,4096
Ta 198,v1,6144
Mvaa 60,180,124
s_mov_b32 m0, s52
S_ s73,s81,s78
Mvaa 60,184,164
S_ s73,s73,4096
Ta 220,v1,8192
Ta 222,v1,10240
Mvaa 60,188,204
s_nop 0
Y_ 56,0
Y_ 57,256
Mvaa 64,180,128
s_nop 0
Y_ 58,512
Y_ 59,768
Mvaa 64,184,168
Mvaa 64,188,208
s_nop 0
Y_ 60,1024
Y_ 61,1280
Mvaa 68,180,132
s_nop 0
Y_ 62,1536
Y_ 63,1792
Mvaa 68,184,172
Mvaa 68,188,212
s_nop 0
Y_ 64,2048
Y_ 65,2304
Mvaa 72,180,136
s_nop 0
Y_ 66,2560
Y_ 67,2816
Mvaa 72,184,176
Mvaa 72,188,216
W_ 12
Y_ 68,3072
Y_ 69,3328
Mvaa 92,220,216
s_nop 0
Ta 180,v2,0
Ta 182,v2,2048
Mvaa 92,196,176
s_nop 0
Y_ 70,3584
Y_ 71,3840
Mvaa 92,192,136
s_nop 0
Ta 184,v2,4096
Ta 186,v2,6144
Mvaa 88,220,212
s_nop 0
Y_ 72,4096
Y_ 73,4352
Mvaa 88,196,172
s_nop 0
Ta 188,v2,8192
Ta 190,v2,10240
Mvaa 88,192,132
s_nop 0
Y_ 74,4608
Y_ 75,4864
Mvaa 84,220,208
s_nop 0
Y_ 92,9216
Y_ 93,9472
Mvaa 84,196,168
s_nop 0
Y_ 94,9728
Y_ 95,9984
Mvaa 84,192,128
s_nop 0
Y_ 88,8192
Y_ 89,8448
Mvaa 80,220,204
s_nop 0
Y_ 90,8704
Y_ 91,8960
Mvaa 80,196,164
Mvaa 80,192,124
s_nop 0
Y_ 84,7168
Y_ 85,7424
Mvaa 76,220,200
s_nop 0
Y_ 86,7680
Y_ 87,7936
Mvaa 76,196,160
Mvaa 76,192,120
W_ 14
Y_ 80,6144
Y_ 81,6400
Mvaa 16,180,120
s_nop 0
Ta 192,v3,0
Ta 194,v3,2048
Mvaa 16,184,160
s_nop 0
Y_ 82,6656
Y_ 83,6912
Mvaa 16,188,200
s_nop 0
Ta 196,v3,4096
Ta 198,v3,6144
Mvaa 20,180,124
s_nop 0
Y_ 76,5120
Y_ 77,5376
Mvaa 20,184,164
s_nop 0
Ta 220,v3,8192
Ta 222,v3,10240
Mvaa 20,188,204
s_nop 0
Y_ 78,5632
Y_ 79,5888
Mvaa 24,180,128
Mvaa 24,184,168
Mvaa 24,188,208
s_waitcnt vmcnt(23) lgkmcnt(0)
s_barrier
Mvaa 28,180,132
S_ s55,s71,122880
ds_read2st64_b32 v[96:97], v5 offset0:0 offset1:1
Mvaa 28,184,172
S_ m0,s51,24576
ds_read2st64_b32 v[98:99], v5 offset0:2 offset1:3
Mvaa 28,188,212
L_ v7,24,s55,0
Mvaa 32,180,136
S_ s55,s71,133120
ds_read2st64_b32 v[104:105], v5 offset0:4 offset1:5
Mvaa 32,184,176
S_ m0,s51,25600
ds_read2st64_b32 v[106:107], v5 offset0:6 offset1:7
Mvaa 32,188,216
ds_read2st64_b32 v[112:113], v5 offset0:8 offset1:9
Mvaa 52,220,216
s_nop 0
Ta 180,%0,12288
Ta 182,%0,14336
Mvaa 52,196,176
L_ v7,24,s55,0
Mvaa 52,192,136
W_ 5
Ta 184,%0,16384
Ta 186,%0,18432
Mvaa 48,220,212
S_ s55,s71,143360
ds_read2st64_b32 v[114:115], v5 offset0:10 offset1:11
A_ v96,v16,v96
Mvaa 48,196,172
S_ m0,s51,26624
Ta 188,%0,20480
Ta 190,%0,22528
Mvaa 48,192,132
L_ v7,24,s55,0
Mvaa 44,220,208
S_ s55,s71,153600
ds_read2st64_b32 v[120:121], v5 offset0:12 offset1:13
A_ v97,v17,v97
Mvaa 44,196,168
S_ m0,s51,27648
ds_read2st64_b32 v[122:123], v5 offset0:14 offset1:15
A_ v98,v18,v98
Mvaa 44,192,128
ds_read2st64_b32 v[128:129], v5 offset0:16 offset1:17
A_ v99,v19,v99
Mvaa 40,220,204
L_ v7,24,s55,0
Mvaa 40,196,164
W_ 11
ds_read2st64_b32 v[130:131], v5 offset0:18 offset1:19
A_ v104,v20,v104
Mvaa 40,192,124
S_ s55,s71,163840
ds_read2st64_b32 v[132:133], v5 offset0:36 offset1:37
A_ v105,v21,v105
Mvaa 36,220,200
S_ m0,s51,28672
ds_read2st64_b32 v[134:135], v5 offset0:38 offset1:39
A_ v106,v22,v106
Mvaa 36,196,160
L_ v7,24,s55,0
Mvaa 36,192,120
W_ 6
ds_read2st64_b32 v[124:125], v5 offset0:32 offset1:33
A_ v107,v23,v107
Zvav 56,180,136
Ta 192,v1,12288
Ta 194,v1,14336
Mvav 56,184,156
S_ s55,s71,174080
ds_read2st64_b32 v[126:127], v5 offset0:34 offset1:35
A_ v112,v24,v112
Mvav 56,188,176
S_ m0,s51,29696
Ta 196,v1,16384
Ta 198,v1,18432
Zvav 60,180,140
L_ v7,24,s55,0
Mvav 60,184,160
S_ s79,s6,3
Ta 220,v1,20480
Ta 222,v1,22528
Mvav 60,188,180
s_and_b32 s79, s79, 7
ds_read2st64_b32 v[116:117], v5 offset0:28 offset1:29
A_ v113,v25,v113
Zvav 64,180,144
s_lshl_b32 s79, s79, 7
ds_read2st64_b32 v[118:119], v5 offset0:30 offset1:31
A_ v114,v26,v114
Mvav 64,184,164
S_ s71,s72,s79
ds_read2st64_b32 v[108:109], v5 offset0:24 offset1:25
A_ v115,v27,v115
Mvav 64,188,184
W_ 14
ds_read2st64_b32 v[110:111], v5 offset0:26 offset1:27
A_ v120,v28,v120
Zvav 68,180,148
S_ s55,s71,491520
ds_read2st64_b32 v[100:101], v5 offset0:20 offset1:21
A_ v121,v29,v121
Mvav 68,184,168
S_ m0,s51,0
ds_read2st64_b32 v[102:103], v5 offset0:22 offset1:23
A_ v122,v30,v122
Mvav 68,188,188
L_ v7,24,s55,0
Zvav 72,180,152
S_ s55,s71,501760
A_ v123,v31,v123
Mvav 72,184,172
W_ 14
A_ v128,v32,v128
A_ v129,v33,v129
Mvav 72,188,192
W_ 6
A_ v130,v34,v130
A_ v131,v35,v131
Zvav 92,220,192
Ta 180,v2,12288
Ta 182,v2,14336
Mvav 92,196,172
S_ m0,s51,1024
A_ v132,v52,v132
A_ v133,v53,v133
Mvav 92,192,152
s_nop 0
Ta 184,v2,16384
Ta 186,v2,18432
Zvav 88,220,188
L_ v7,24,s55,0
Mvav 88,196,168
S_ s55,s71,512000
Ta 188,v2,20480
Ta 190,v2,22528
Mvav 88,192,148
S_ m0,s51,2048
A_ v134,v54,v134
A_ v135,v55,v135
Zvav 84,220,184
A_ v124,v48,v124
A_ v125,v49,v125
Mvav 84,196,164
L_ v7,24,s55,0
Mvav 84,192,144
S_ s55,s71,522240
A_ v126,v50,v126
A_ v127,v51,v127
Zvav 80,220,180
W_ 10
A_ v116,v44,v116
A_ v117,v45,v117
Mvav 80,196,160
S_ m0,s51,3072
A_ v118,v46,v118
A_ v119,v47,v119
Mvav 80,192,140
L_ v7,24,s55,0
Zvav 76,220,176
W_ 8
A_ v108,v40,v108
A_ v109,v41,v109
Mvav 76,196,156
S_ s55,s71,532480
A_ v110,v42,v110
A_ v111,v43,v111
Mvav 76,192,136
W_ 0
A_ v100,v36,v100
Zvav 16,180,136
S_ m0,s51,4096
Ta 192,v3,12288
Ta 194,v3,14336
Mvav 16,184,156
L_ v7,24,s55,0
Mvav 16,188,176
S_ s55,s71,542720
Ta 196,v3,16384
Ta 198,v3,18432
Zvav 20,180,140
S_ m0,s51,5120
A_ v101,v37,v101
A_ v102,v38,v102
Mvav 20,184,160
S_ s85,s6,5
Ta 220,v3,20480
Ta 222,v3,22528
Mvav 20,188,180
L_ v7,24,s55,0
Zvav 24,180,144
s_and_b32 s85, s85, 7
A_ v103,v39,v103
C_ 131,130,131
Mvav 24,184,164
s_lshl_b32 s85, s85, 7
C_ 130,128,129
C_ 132,132,133
Mvav 24,188,184
S_ s85,s85,s87
C_ 133,134,135
Zvav 28,180,148
buffer_store_dwordx4 v[130:133], v6, s[28:31], s73 offen offset:0 sc1
Mvav 28,184,168
s_sub_u32 s73, s73, 4096
Mvav 28,188,188
C_ 123,122,123
C_ 122,120,121
Zvav 32,180,152
C_ 124,124,125
C_ 125,126,127
Mvav 32,184,172
buffer_store_dwordx4 v[122:125], v6, s[28:31], s73 offen offset:3072 sc1
Mvav 32,188,192
s_waitcnt vmcnt(21) lgkmcnt(0)
s_barrier
C_ 115,114,115
Zvav 52,220,192
S_ s56,s76,s74
Ta 180,%0,49152
Ta 182,%0,51200
Mvav 52,196,172
S_ s55,s71,614400
C_ 114,112,113
C_ 116,116,117
Mvav 52,192,152
S_ m0,s51,12288
Ta 184,%0,53248
Ta 186,%0,55296
Zvav 48,220,188
buffer_load_dwordx4 a[60:63], v6, s[28:31], s56 offen offset:0 sc1
Mvav 48,196,168
S_ s56,s76,s82
Ta 188,%0,57344
Ta 190,%0,59392
Mvav 48,192,148
C_ 117,118,119
Zvav 44,220,184
Mvav 44,196,164
buffer_load_dwordx4 a[64:67], v6, s[28:31], s56 offen offset:0 sc1
Mvav 44,192,144
Zvav 40,220,180
Mvav 40,196,160
Mvav 40,192,140
buffer_store_dwordx4 v[114:117], v6, s[28:31], s73 offen offset:2048 sc1
Zvav 36,220,176
C_ 107,106,107
C_ 106,104,105
Mvav 36,196,156
C_ 108,108,109
C_ 109,110,111
Mvav 36,192,136
W_ 0
Mvav 56,180,196
s_nop 0
Ta 192,v1,49152
Ta 194,v1,51200
Mvav 56,184,216
buffer_store_dwordx4 v[106:109], v6, s[28:31], s73 offen offset:1024 sc1
Mvav 56,188,236
s_nop 0
Ta 196,v1,53248
Ta 198,v1,55296
Mvav 60,180,200
C_ 99,98,99
C_ 98,96,97
Mvav 60,184,220
s_nop 0
Ta 220,v1,57344
Ta 222,v1,59392
Mvav 60,188,240
L_ v7,24,s55,0
Mvav 64,180,204
S_ s55,s71,624640
C_ 100,100,101
C_ 101,102,103
Mvav 64,184,224
S_ m0,s51,13312
Mvav 64,188,244
Mvav 68,180,208
buffer_store_dwordx4 v[98:101], v6, s[28:31], s73 offen offset:0 sc1
Mvav 68,184,228
Mvav 68,188,248
Mvav 72,180,212
Mvav 72,184,232
L_ v7,24,s55,0
Mvav 72,188,252
W_ 0
S_ s55,s71,634880
Mvav 92,220,252
S_ m0,s51,14336
Ta 180,v2,49152
Ta 182,v2,51200
Mvav 92,196,232
Mvav 92,192,212
s_nop 0
Ta 184,v2,53248
Ta 186,v2,55296
Mvav 88,220,248
L_ v7,24,s55,0
Mvav 88,196,228
S_ s55,s71,645120
Ta 188,v2,57344
Ta 190,v2,59392
Mvav 88,192,208
S_ m0,s51,15360
Mvav 84,220,244
Mvav 84,196,224
L_ v7,24,s55,0
Mvav 84,192,204
S_ s55,s71,655360
Mvav 80,220,240
S_ m0,s51,16384
Mvav 80,196,220
S_ s83,s6,7
Mvav 80,192,200
L_ v7,24,s55,0
Mvav 76,220,236
s_and_b32 s83, s83, 7
S_ s55,s71,665600
Mvav 76,196,216
s_lshl_b32 s83, s83, 2
S_ m0,s51,17408
Mvav 76,192,196
W_ 0
v_cmp_eq_u32_e64 s[36:37], s83, v8
Mvav 16,180,196
s_nop 0
Ta 192,v3,49152
Ta 194,v3,51200
Mvav 16,184,216
L_ v7,24,s55,0
Mvav 16,188,236
s_nop 0
Ta 196,v3,53248
Ta 198,v3,55296
Mvav 20,180,200
v_mov_b32 v10, 0x7fff0000
Mvav 20,184,220
s_nop 0
Ta 220,v3,57344
Ta 222,v3,59392
Mvav 20,188,240
buffer_load_dword a74, v4, s[24:27], s85 offen
Mvav 24,180,204
v_cndmask_b32_e64 v10, v10, v9, s[36:37]
Mvav 24,184,224
Mvav 24,188,244
Mvav 28,180,208
Mvav 28,184,228
Mvav 28,188,248
Mvav 32,180,212
Mvav 32,184,232
Mvav 32,188,252
s_waitcnt vmcnt(27) lgkmcnt(0)
s_barrier
Mvav 52,220,252
S_ s55,s71,0
Ta 180,%0,36864
Ta 182,%0,38912
Mvav 52,196,232
buffer_store_dword a255, v10, s[92:95], 0 offen offset:384 sc1
Mvav 52,192,212
S_ m0,s51,49152
Ta 184,%0,40960
Ta 186,%0,43008
Mvav 48,220,248
Mvav 48,196,228
s_nop 0
Ta 188,%0,45056
Ta 190,%0,47104
Mvav 48,192,208
L_ v7,24,s55,0
Mvav 44,220,244
S_ s55,s71,10240
Mvav 44,196,224
S_ m0,s51,50176
Mvav 44,192,204
Mvav 40,220,240
L_ v7,24,s55,0
Mvav 40,196,220
S_ s55,s71,20480
Mvav 40,192,200
S_ m0,s51,51200
Mvav 36,220,236
Mvav 36,196,216
L_ v7,24,s55,0
Mvav 36,192,196
W_ 0
S_ s55,s71,30720
Mvaa 56,180,0
S_ m0,s51,52224
Ta 192,v1,36864
Ta 194,v1,38912
Mvaa 56,184,40
Mvaa 56,188,80
s_nop 0
Ta 196,v1,40960
Ta 198,v1,43008
Mvaa 60,180,4
L_ v7,24,s55,0
Mvaa 60,184,44
S_ s55,s71,40960
Ta 220,v1,45056
Ta 222,v1,47104
Mvaa 60,188,84
S_ m0,s51,53248
Mvaa 64,180,8
Mvaa 64,184,48
L_ v7,24,s55,0
Mvaa 64,188,88
S_ s55,s71,51200
Mvaa 68,180,12
S_ m0,s51,54272
Mvaa 68,184,52
Mvaa 68,188,240
L_ v7,24,s55,0
Mvaa 72,180,16
Mvaa 72,184,56
Mvaa 72,188,244
W_ 0
Mvaa 92,220,244
s_nop 0
Ta 180,v2,36864
Ta 182,v2,38912
Mvaa 92,196,56
Mvaa 92,192,16
s_nop 0
Ta 184,v2,40960
Ta 186,v2,43008
Mvaa 88,220,240
Mvaa 88,196,52
s_nop 0
Ta 188,v2,45056
Ta 190,v2,47104
Mvaa 88,192,12
Mvaa 84,220,88
Mvaa 84,196,48
Mvaa 84,192,8
Mvaa 80,220,84
Mvaa 80,196,44
Mvaa 80,192,4
Mvaa 76,220,80
Mvaa 76,196,40
Mvaa 76,192,0
W_ 0
Mvaa 16,180,0
s_nop 0
Ta 192,v3,36864
Ta 194,v3,38912
Mvaa 16,184,40
Mvaa 16,188,80
s_nop 0
Ta 196,v3,40960
Ta 198,v3,43008
Mvaa 20,180,4
Mvaa 20,184,44
s_nop 0
Ta 220,v3,45056
Ta 222,v3,47104
Mvaa 20,188,84
Mvaa 24,180,8
Mvaa 24,184,48
Mvaa 24,188,88
Mvaa 28,180,12
Mvaa 28,184,52
Mvaa 28,188,240
Mvaa 32,180,16
Mvaa 32,184,56
Mvaa 32,188,244
s_waitcnt vmcnt(27) lgkmcnt(0)
s_barrier
Mvaa 52,220,244
s_nop 0
Ta 180,%0,24576
Ta 182,%0,26624
Mvaa 52,196,56
Mvaa 52,192,16
s_nop 0
Ta 184,%0,28672
Ta 186,%0,30720
Mvaa 48,220,240
Mvaa 48,196,52
s_nop 0
Ta 188,%0,32768
Ta 190,%0,34816
Mvaa 48,192,12
Mvaa 44,220,88
Mvaa 44,196,48
Mvaa 44,192,8
Mvaa 40,220,84
Mvaa 40,196,44
Mvaa 40,192,4
Mvaa 36,220,80
Mvaa 36,196,40
Mvaa 36,192,0
W_ 0
Mvaa 56,180,120
S_ s81,s6,2
Ta 192,v1,24576
Ta 194,v1,26624
Mvaa 56,184,160
s_and_b32 s81, s81, 7
Mvaa 56,188,200
s_mul_i32 s81, s81, 20480
Ta 196,v1,28672
Ta 198,v1,30720
Mvaa 60,180,124
s_mov_b32 m0, s52
S_ s73,s81,s78
Mvaa 60,184,164
S_ s73,s73,4096
Ta 220,v1,32768
Ta 222,v1,34816
Mvaa 60,188,204
s_nop 0
Y_ 56,0
Y_ 57,256
Mvaa 64,180,128
s_nop 0
Y_ 58,512
Y_ 59,768
Mvaa 64,184,168
Mvaa 64,188,208
s_nop 0
Y_ 60,1024
Y_ 61,1280
Mvaa 68,180,132
s_nop 0
Y_ 62,1536
Y_ 63,1792
Mvaa 68,184,172
Mvaa 68,188,212
s_nop 0
Y_ 64,2048
Y_ 65,2304
Mvaa 72,180,136
s_nop 0
Y_ 66,2560
Y_ 67,2816
Mvaa 72,184,176
Mvaa 72,188,216
W_ 12
Y_ 68,3072
Y_ 69,3328
Mvaa 92,220,216
s_nop 0
Ta 180,v2,24576
Ta 182,v2,26624
Mvaa 92,196,176
s_nop 0
Y_ 70,3584
Y_ 71,3840
Mvaa 92,192,136
s_nop 0
Ta 184,v2,28672
Ta 186,v2,30720
Mvaa 88,220,212
s_nop 0
Y_ 72,4096
Y_ 73,4352
Mvaa 88,196,172
S_ s83,s6,1
Ta 188,v2,32768
Ta 190,v2,34816
Mvaa 88,192,132
s_and_b32 s83, s83, 7
Y_ 74,4608
Y_ 75,4864
Mvaa 84,220,208
s_lshl_b32 s83, s83, 2
Y_ 92,9216
Y_ 93,9472
Mvaa 84,196,168
s_nop 0
Y_ 94,9728
Y_ 95,9984
Mvaa 84,192,128
s_nop 0
Y_ 88,8192
Y_ 89,8448
Mvaa 80,220,204
s_nop 0
Y_ 90,8704
Y_ 91,8960
Mvaa 80,196,164
v_cmp_eq_u32_e64 s[36:37], s83, v8
Mvaa 80,192,124
s_nop 0
Y_ 84,7168
Y_ 85,7424
Mvaa 76,220,200
s_nop 0
Y_ 86,7680
Y_ 87,7936
Mvaa 76,196,160
v_mov_b32 v10, 0x7fff0000
Mvaa 76,192,120
W_ 14
Y_ 80,6144
Y_ 81,6400
Mvaa 16,180,120
s_nop 0
Ta 192,v3,24576
Ta 194,v3,26624
Mvaa 16,184,160
s_nop 0
Y_ 82,6656
Y_ 83,6912
Mvaa 16,188,200
s_nop 0
Ta 196,v3,28672
Ta 198,v3,30720
Mvaa 20,180,124
s_nop 0
Y_ 76,5120
Y_ 77,5376
Mvaa 20,184,164
s_nop 0
Ta 220,v3,32768
Ta 222,v3,34816
Mvaa 20,188,204
s_nop 0
Y_ 78,5632
Y_ 79,5888
Mvaa 24,180,128
v_cndmask_b32_e64 v10, v10, v9, s[36:37]
Mvaa 24,184,168
Mvaa 24,188,208
s_waitcnt vmcnt(13) lgkmcnt(0)
s_barrier
Mvaa 28,180,132
buffer_store_dword a255, v10, s[92:95], 0 offen offset:384 sc1
Mvaa 28,184,172
S_ s55,s71,122880
ds_read2st64_b32 v[96:97], v5 offset0:0 offset1:1
)A"
R"A(Mvaa 28,188,212
S_ m0,s51,36864
ds_read2st64_b32 v[98:99], v5 offset0:2 offset1:3
Mvaa 32,180,136
ds_read2st64_b32 v[104:105], v5 offset0:4 offset1:5
Mvaa 32,184,176
L_ v7,24,s55,0
Mvaa 32,188,216
S_ s55,s71,133120
ds_read2st64_b32 v[106:107], v5 offset0:6 offset1:7
Mvaa 52,220,216
S_ m0,s51,37888
Ta 180,%0,0
Ta 182,%0,2048
Mvaa 52,196,176
ds_read2st64_b32 v[112:113], v5 offset0:8 offset1:9
Mvaa 52,192,136
W_ 5
Ta 184,%0,4096
Ta 186,%0,6144
Mvaa 48,220,212
L_ v7,24,s55,0
Mvaa 48,196,172
S_ s55,s71,143360
Ta 188,%0,8192
Ta 190,%0,10240
Mvaa 48,192,132
S_ m0,s51,38912
ds_read2st64_b32 v[114:115], v5 offset0:10 offset1:11
A_ v96,v16,v96
Mvaa 44,220,208
ds_read2st64_b32 v[120:121], v5 offset0:12 offset1:13
A_ v97,v17,v97
Mvaa 44,196,168
L_ v7,24,s55,0
Mvaa 44,192,128
S_ s55,s71,153600
ds_read2st64_b32 v[122:123], v5 offset0:14 offset1:15
A_ v98,v18,v98
Mvaa 40,220,204
S_ m0,s51,39936
ds_read2st64_b32 v[128:129], v5 offset0:16 offset1:17
A_ v99,v19,v99
Mvaa 40,196,164
W_ 11
ds_read2st64_b32 v[130:131], v5 offset0:18 offset1:19
A_ v104,v20,v104
Mvaa 40,192,124
L_ v7,24,s55,0
Mvaa 36,220,200
S_ s55,s71,163840
ds_read2st64_b32 v[132:133], v5 offset0:36 offset1:37
A_ v105,v21,v105
Mvaa 36,196,160
S_ m0,s51,40960
ds_read2st64_b32 v[134:135], v5 offset0:38 offset1:39
A_ v106,v22,v106
Mvaa 36,192,120
ds_read2st64_b32 v[124:125], v5 offset0:32 offset1:33
A_ v107,v23,v107
s_waitcnt vmcnt(22)
Zaaa 224,70,60
L_ v7,24,s55,0
Zaaa 228,68,60
W_ 7
ds_read2st64_b32 v[126:127], v5 offset0:34 offset1:35
A_ v112,v24,v112
Zaaa 232,70,64
S_ s55,s71,174080
ds_read2st64_b32 v[116:117], v5 offset0:28 offset1:29
A_ v113,v25,v113
Zaaa 236,68,64
ds_read2st64_b32 v[118:119], v5 offset0:30 offset1:31
A_ v114,v26,v114
Zvav 56,180,136
S_ m0,s51,41984
Ta 192,v1,0
Ta 194,v1,2048
Mvav 56,184,156
L_ v7,24,s55,0
Mvav 56,188,176
S_ s79,s6,4
Ta 196,v1,4096
Ta 198,v1,6144
Zvav 60,180,140
s_and_b32 s79, s79, 7
ds_read2st64_b32 v[108:109], v5 offset0:24 offset1:25
A_ v115,v27,v115
Mvav 60,184,160
W_ 13
Ta 220,v1,8192
Ta 222,v1,10240
Mvav 60,188,180
s_lshl_b32 s79, s79, 7
ds_read2st64_b32 v[110:111], v5 offset0:26 offset1:27
A_ v120,v28,v120
Zvav 64,180,144
S_ s71,s72,s79
ds_read2st64_b32 v[100:101], v5 offset0:20 offset1:21
A_ v121,v29,v121
Mvav 64,184,164
S_ s55,s71,491520
ds_read2st64_b32 v[102:103], v5 offset0:22 offset1:23
A_ v122,v30,v122
Mvav 64,188,184
S_ m0,s51,24576
A_ v123,v31,v123
R_ v12,a248
Zvav 68,180,148
L_ v7,24,s55,0
Mvav 68,184,168
W_ 14
A_ v128,v32,v128
A_ v129,v33,v129
Mvav 68,188,188
S_ s55,s71,501760
A_ v130,v34,v130
A_ v131,v35,v131
Zvav 72,180,152
A_ v132,v52,v132
A_ v133,v53,v133
Mvav 72,184,172
S_ m0,s51,25600
A_ v134,v54,v134
A_ v135,v55,v135
Mvav 72,188,192
W_ 3
A_ v124,v48,v124
Zvav 92,220,192
s_nop 0
Ta 180,v2,0
Ta 182,v2,2048
Mvav 92,196,172
L_ v7,24,s55,0
Mvav 92,192,152
S_ s55,s71,512000
Ta 184,v2,4096
Ta 186,v2,6144
Zvav 88,220,188
S_ m0,s51,26624
A_ v125,v49,v125
A_ v126,v50,v126
Mvav 88,196,168
s_nop 0
Ta 188,v2,8192
Ta 190,v2,10240
Mvav 88,192,148
L_ v7,24,s55,0
Zvav 84,220,184
S_ s55,s71,522240
A_ v127,v51,v127
C_ 131,130,131
Mvav 84,196,164
A_ v116,v44,v116
A_ v117,v45,v117
Mvav 84,192,144
S_ m0,s51,27648
A_ v118,v46,v118
A_ v119,v47,v119
Zvav 80,220,180
L_ v7,24,s55,0
Mvav 80,196,160
W_ 8
A_ v108,v40,v108
A_ v109,v41,v109
Mvav 80,192,140
S_ s55,s71,532480
A_ v110,v42,v110
A_ v111,v43,v111
Zvav 76,220,176
W_ 6
A_ v100,v36,v100
A_ v101,v37,v101
Mvav 76,196,156
S_ m0,s51,28672
A_ v102,v38,v102
A_ v103,v39,v103
Mvav 76,192,136
W_ 0
C_ 130,128,129
C_ 132,132,133
Zvav 16,180,136
S_ s85,s6,6
Ta 192,v3,0
Ta 194,v3,2048
Mvav 16,184,156
L_ v7,24,s55,0
Mvav 16,188,176
S_ s55,s71,542720
Ta 196,v3,4096
Ta 198,v3,6144
Zvav 20,180,140
S_ m0,s51,29696
C_ 133,134,135
v_min_u32 v12, 240, v12
Mvav 20,184,160
s_and_b32 s85, s85, 7
Ta 220,v3,8192
Ta 222,v3,10240
Mvav 20,188,180
buffer_store_dwordx4 v[130:133], v6, s[28:31], s73 offen offset:0 sc1
Zvav 24,180,144
s_sub_u32 s73, s73, 4096
v_lshlrev_b32 v12, 3, v12
s_lshl_b32 s85, s85, 7
Mvav 24,184,164
S_ s85,s85,s87
C_ 123,122,123
C_ 122,120,121
Mvav 24,188,184
C_ 124,124,125
C_ 125,126,127
Zvav 28,180,148
buffer_store_dwordx4 v[122:125], v6, s[28:31], s73 offen offset:3072 sc1
Mvav 28,184,168
C_ 115,114,115
C_ 114,112,113
Mvav 28,188,188
C_ 116,116,117
C_ 117,118,119
Zvav 32,180,152
Mvav 32,184,172
buffer_store_dwordx4 v[114:117], v6, s[28:31], s73 offen offset:2048 sc1
Mvav 32,188,192
s_waitcnt vmcnt(23) lgkmcnt(0)
s_barrier
C_ 107,106,107
Zvav 52,220,192
s_nop 0
Ta 180,%0,12288
Ta 182,%0,14336
Mvav 52,196,172
C_ 106,104,105
C_ 108,108,109
Mvav 52,192,152
s_nop 0
Ta 184,%0,16384
Ta 186,%0,18432
Zvav 48,220,188
L_ v7,24,s55,0
Mvav 48,196,168
S_ s55,s71,614400
Ta 188,%0,20480
Ta 190,%0,22528
Mvav 48,192,148
S_ m0,s51,0
C_ 109,110,111
Zvav 44,220,184
Mvav 44,196,164
buffer_store_dwordx4 v[106:109], v6, s[28:31], s73 offen offset:1024 sc1
Mvav 44,192,144
C_ 99,98,99
C_ 98,96,97
Zvav 40,220,180
C_ 100,100,101
C_ 101,102,103
Mvav 40,196,160
Mvav 40,192,140
buffer_store_dwordx4 v[98:101], v6, s[28:31], s73 offen offset:0 sc1
Zvav 36,220,176
Mvav 36,196,156
Mvav 36,192,136
W_ 0
Mvav 56,180,196
s_nop 0
Ta 192,v1,12288
Ta 194,v1,14336
Mvav 56,184,216
L_ v7,24,s55,0
Mvav 56,188,236
S_ s55,s71,624640
Ta 196,v1,16384
Ta 198,v1,18432
Mvav 60,180,200
S_ m0,s51,1024
Mvav 60,184,220
s_nop 0
Ta 220,v1,20480
Ta 222,v1,22528
Mvav 60,188,240
L_ v7,24,s55,0
Mvav 64,180,204
S_ s55,s71,634880
Mvav 64,184,224
S_ m0,s51,2048
Mvav 64,188,244
Mvav 68,180,208
L_ v7,24,s55,0
Mvav 68,184,228
S_ s55,s71,645120
Mvav 68,188,248
S_ m0,s51,3072
Mvav 72,180,212
Mvav 72,184,232
L_ v7,24,s55,0
Mvav 72,188,252
W_ 0
S_ s55,s71,655360
Mvav 92,220,252
S_ m0,s51,4096
Ta 180,v2,12288
Ta 182,v2,14336
Mvav 92,196,232
Mvav 92,192,212
s_nop 0
Ta 184,v2,16384
Ta 186,v2,18432
Mvav 88,220,248
L_ v7,24,s55,0
Mvav 88,196,228
S_ s55,s71,665600
Ta 188,v2,20480
Ta 190,v2,22528
Mvav 88,192,208
S_ m0,s51,5120
Mvav 84,220,244
Mvav 84,196,224
L_ v7,24,s55,0
Mvav 84,192,204
Mvav 80,220,240
Mvav 80,196,220
Mvav 80,192,200
buffer_load_dword a74, v12, s[44:47], s66 offen
Mvav 76,220,236
Mvav 76,196,216
Mvav 76,192,196
W_ 0
Mvav 16,180,196
s_nop 0
Ta 192,v3,12288
Ta 194,v3,14336
Mvav 16,184,216
buffer_load_dword a74, v4, s[24:27], s85 offen
Mvav 16,188,236
s_nop 0
Ta 196,v3,16384
Ta 198,v3,18432
Mvav 20,180,200
Mvav 20,184,220
s_nop 0
Ta 220,v3,20480
Ta 222,v3,22528
Mvav 20,188,240
Mvav 24,180,204
Mvav 24,184,224
Mvav 24,188,244
Mvav 28,180,208
Mvav 28,184,228
Mvav 28,188,248
Mvav 32,180,212
Mvav 32,184,232
Mvav 32,188,252
s_waitcnt vmcnt(26) lgkmcnt(0)
s_barrier
Mvav 52,220,252
S_ s55,s71,0
Ta 180,%0,49152
Ta 182,%0,51200
Mvav 52,196,232
S_ m0,s51,12288
Mvav 52,192,212
s_nop 0
Ta 184,%0,53248
Ta 186,%0,55296
Mvav 48,220,248
L_ v7,24,s55,0
Mvav 48,196,228
S_ s55,s71,10240
Ta 188,%0,57344
Ta 190,%0,59392
Mvav 48,192,208
S_ m0,s51,13312
Mvav 44,220,244
Mvav 44,196,224
L_ v7,24,s55,0
Mvav 44,192,204
S_ s55,s71,20480
Mvav 40,220,240
S_ m0,s51,14336
Mvav 40,196,220
Mvav 40,192,200
L_ v7,24,s55,0
Mvav 36,220,236
S_ s55,s71,30720
Mvav 36,196,216
S_ m0,s51,15360
Mvav 36,192,196
W_ 0
Mvaa 56,180,0
s_nop 0
Ta 192,v1,49152
Ta 194,v1,51200
Mvaa 56,184,40
L_ v7,24,s55,0
Mvaa 56,188,80
S_ s55,s71,40960
Ta 196,v1,53248
Ta 198,v1,55296
Mvaa 60,180,4
S_ m0,s51,16384
Mvaa 60,184,44
s_nop 0
Ta 220,v1,57344
Ta 222,v1,59392
Mvaa 60,188,84
L_ v7,24,s55,0
Mvaa 64,180,8
S_ s55,s71,51200
Mvaa 64,184,48
S_ m0,s51,17408
Mvaa 64,188,88
Mvaa 68,180,12
L_ v7,24,s55,0
Mvaa 68,184,52
Mvaa 68,188,240
Mvaa 72,180,16
Mvaa 72,184,56
Mvaa 72,188,244
W_ 0
Mvaa 92,220,244
s_nop 0
Ta 180,v2,49152
Ta 182,v2,51200
Mvaa 92,196,56
Mvaa 92,192,16
s_nop 0
Ta 184,v2,53248
Ta 186,v2,55296
Mvaa 88,220,240
Mvaa 88,196,52
s_nop 0
Ta 188,v2,57344
Ta 190,v2,59392
Mvaa 88,192,12
Mvaa 84,220,88
Mvaa 84,196,48
Mvaa 84,192,8
Mvaa 80,220,84
Mvaa 80,196,44
Mvaa 80,192,4
Mvaa 76,220,80
Mvaa 76,196,40
Mvaa 76,192,0
W_ 0
Mvaa 16,180,0
s_nop 0
Ta 192,v3,49152
Ta 194,v3,51200
Mvaa 16,184,40
Mvaa 16,188,80
s_nop 0
Ta 196,v3,53248
Ta 198,v3,55296
Mvaa 20,180,4
Mvaa 20,184,44
s_nop 0
Ta 220,v3,57344
Ta 222,v3,59392
Mvaa 20,188,84
Mvaa 24,180,8
Mvaa 24,184,48
Mvaa 24,188,88
Mvaa 28,180,12
Mvaa 28,184,52
Mvaa 28,188,240
Mvaa 32,180,16
Mvaa 32,184,56
Mvaa 32,188,244
s_waitcnt vmcnt(25) lgkmcnt(0)
s_barrier
Mvaa 52,220,244
s_nop 0
Ta 180,%0,36864
Ta 182,%0,38912
Mvaa 52,196,56
Mvaa 52,192,16
s_nop 0
Ta 184,%0,40960
Ta 186,%0,43008
Mvaa 48,220,240
Mvaa 48,196,52
s_nop 0
Ta 188,%0,45056
Ta 190,%0,47104
Mvaa 48,192,12
Mvaa 44,220,88
Mvaa 44,196,48
Mvaa 44,192,8
Mvaa 40,220,84
Mvaa 40,196,44
Mvaa 40,192,4
Mvaa 36,220,80
Mvaa 36,196,40
Mvaa 36,192,0
W_ 0
Mvaa 56,180,120
S_ s81,s6,3
Ta 192,v1,36864
Ta 194,v1,38912
Mvaa 56,184,160
s_and_b32 s81, s81, 7
Mvaa 56,188,200
s_mul_i32 s81, s81, 20480
Ta 196,v1,40960
Ta 198,v1,43008
Mvaa 60,180,124
s_mov_b32 m0, s52
S_ s73,s81,s78
Mvaa 60,184,164
S_ s73,s73,4096
Ta 220,v1,45056
Ta 222,v1,47104
Mvaa 60,188,204
s_nop 0
Y_ 56,0
Y_ 57,256
Mvaa 64,180,128
s_nop 0
Y_ 58,512
Y_ 59,768
Mvaa 64,184,168
Mvaa 64,188,208
s_nop 0
Y_ 60,1024
Y_ 61,1280
Mvaa 68,180,132
s_nop 0
Y_ 62,1536
Y_ 63,1792
Mvaa 68,184,172
Mvaa 68,188,212
s_nop 0
Y_ 64,2048
Y_ 65,2304
Mvaa 72,180,136
s_nop 0
Y_ 66,2560
Y_ 67,2816
Mvaa 72,184,176
Mvaa 72,188,216
W_ 12
Y_ 68,3072
Y_ 69,3328
Mvaa 92,220,216
s_nop 0
Ta 180,v2,36864
Ta 182,v2,38912
Mvaa 92,196,176
s_nop 0
Y_ 70,3584
Y_ 71,3840
Mvaa 92,192,136
s_nop 0
Ta 184,v2,40960
Ta 186,v2,43008
Mvaa 88,220,212
s_nop 0
Y_ 72,4096
Y_ 73,4352
Mvaa 88,196,172
S_ s83,s6,2
Ta 188,v2,45056
Ta 190,v2,47104
Mvaa 88,192,132
s_and_b32 s83, s83, 7
Y_ 74,4608
Y_ 75,4864
Mvaa 84,220,208
s_lshl_b32 s83, s83, 2
Y_ 92,9216
Y_ 93,9472
Mvaa 84,196,168
s_nop 0
Y_ 94,9728
Y_ 95,9984
Mvaa 84,192,128
s_nop 0
Y_ 88,8192
Y_ 89,8448
Mvaa 80,220,204
s_nop 0
Y_ 90,8704
Y_ 91,8960
Mvaa 80,196,164
v_cmp_eq_u32_e64 s[36:37], s83, v8
Mvaa 80,192,124
s_nop 0
Y_ 84,7168
Y_ 85,7424
Mvaa 76,220,200
s_nop 0
Y_ 86,7680
Y_ 87,7936
Mvaa 76,196,160
v_mov_b32 v10, 0x7fff0000
Mvaa 76,192,120
W_ 14
Y_ 80,6144
Y_ 81,6400
Mvaa 16,180,120
s_nop 0
Ta 192,v3,36864
Ta 194,v3,38912
Mvaa 16,184,160
s_nop 0
Y_ 82,6656
Y_ 83,6912
Mvaa 16,188,200
s_nop 0
Ta 196,v3,40960
Ta 198,v3,43008
Mvaa 20,180,124
s_nop 0
Y_ 76,5120
Y_ 77,5376
Mvaa 20,184,164
s_nop 0
Ta 220,v3,45056
Ta 222,v3,47104
Mvaa 20,188,204
s_nop 0
Y_ 78,5632
Y_ 79,5888
Mvaa 24,180,128
v_cndmask_b32_e64 v10, v10, v9, s[36:37]
Mvaa 24,184,168
Mvaa 24,188,208
s_waitcnt vmcnt(14) lgkmcnt(0)
s_barrier
Mvaa 28,180,132
buffer_store_dword a255, v10, s[92:95], 0 offen offset:384 sc1
Mvaa 28,184,172
s_sub_u32 s75, s6, 1
ds_read2st64_b32 v[96:97], v5 offset0:0 offset1:1
S_ s55,s71,122880
Mvaa 28,188,212
s_and_b32 s75, s75, 7
ds_read2st64_b32 v[98:99], v5 offset0:2 offset1:3
S_ m0,s51,49152
Mvaa 32,180,136
ds_read2st64_b32 v[104:105], v5 offset0:4 offset1:5
Mvaa 32,184,176
buffer_load_dword v11, v8, s[92:95], s96 offen offset:384 sc1
Mvaa 32,188,216
ds_read2st64_b32 v[106:107], v5 offset0:6 offset1:7
Mvaa 52,220,216
s_nop 0
Ta 180,%0,24576
Ta 182,%0,26624
Mvaa 52,196,176
ds_read2st64_b32 v[112:113], v5 offset0:8 offset1:9
Mvaa 52,192,136
W_ 5
Ta 184,%0,28672
Ta 186,%0,30720
Mvaa 48,220,212
L_ v7,24,s55,0
Mvaa 48,196,172
S_ s55,s71,133120
Ta 188,%0,32768
Ta 190,%0,34816
Mvaa 48,192,132
S_ m0,s51,50176
ds_read2st64_b32 v[114:115], v5 offset0:10 offset1:11
A_ v96,v16,v96
Mvaa 44,220,208
ds_read2st64_b32 v[120:121], v5 offset0:12 offset1:13
A_ v97,v17,v97
Mvaa 44,196,168
L_ v7,24,s55,0
Mvaa 44,192,128
S_ s55,s71,143360
ds_read2st64_b32 v[122:123], v5 offset0:14 offset1:15
A_ v98,v18,v98
Mvaa 40,220,204
S_ m0,s51,51200
ds_read2st64_b32 v[128:129], v5 offset0:16 offset1:17
A_ v99,v19,v99
Mvaa 40,196,164
W_ 11
ds_read2st64_b32 v[130:131], v5 offset0:18 offset1:19
A_ v104,v20,v104
Mvaa 40,192,124
L_ v7,24,s55,0
Mvaa 36,220,200
S_ s55,s71,153600
ds_read2st64_b32 v[132:133], v5 offset0:36 offset1:37
A_ v105,v21,v105
Mvaa 36,196,160
S_ m0,s51,52224
ds_read2st64_b32 v[134:135], v5 offset0:38 offset1:39
A_ v106,v22,v106
Mvaa 36,192,120
W_ 7
ds_read2st64_b32 v[124:125], v5 offset0:32 offset1:33
A_ v107,v23,v107
Zvav 56,180,136
W_ 7
Ta 192,v1,24576
Ta 194,v1,26624
Mvav 56,184,156
L_ v7,24,s55,0
Mvav 56,188,176
S_ s55,s71,163840
Ta 196,v1,28672
Ta 198,v1,30720
Zvav 60,180,140
S_ m0,s51,53248
ds_read2st64_b32 v[126:127], v5 offset0:34 offset1:35
A_ v112,v24,v112
Mvav 60,184,160
s_nop 0
Ta 220,v1,32768
Ta 222,v1,34816
Mvav 60,188,180
L_ v7,24,s55,0
Zvav 64,180,144
S_ s55,s71,174080
ds_read2st64_b32 v[116:117], v5 offset0:28 offset1:29
A_ v113,v25,v113
Mvav 64,184,164
S_ m0,s51,54272
ds_read2st64_b32 v[118:119], v5 offset0:30 offset1:31
A_ v114,v26,v114
Mvav 64,188,184
ds_read2st64_b32 v[108:109], v5 offset0:24 offset1:25
A_ v115,v27,v115
Zvav 68,180,148
L_ v7,24,s55,0
Mvav 68,184,168
W_ 14
ds_read2st64_b32 v[110:111], v5 offset0:26 offset1:27
A_ v120,v28,v120
Mvav 68,188,188
S_ s79,s6,5
ds_read2st64_b32 v[100:101], v5 offset0:20 offset1:21
A_ v121,v29,v121
Zvav 72,180,152
s_and_b32 s79, s79, 7
ds_read2st64_b32 v[102:103], v5 offset0:22 offset1:23
A_ v122,v30,v122
Mvav 72,184,172
s_lshl_b32 s79, s79, 7
A_ v123,v31,v123
Mvav 72,188,192
W_ 6
A_ v128,v32,v128
Zvav 92,220,192
S_ s71,s72,s79
Ta 180,v2,24576
Ta 182,v2,26624
Mvav 92,196,172
S_ s55,s71,491520
A_ v129,v33,v129
A_ v130,v34,v130
Mvav 92,192,152
S_ m0,s51,36864
Ta 184,v2,28672
Ta 186,v2,30720
Zvav 88,220,188
L_ v7,24,s55,0
Mvav 88,196,168
S_ s55,s71,501760
Ta 188,v2,32768
Ta 190,v2,34816
Mvav 88,192,148
S_ m0,s51,37888
A_ v131,v35,v131
S_ s85,s6,7
Zvav 84,220,184
A_ v132,v52,v132
A_ v133,v53,v133
Mvav 84,196,164
L_ v7,24,s55,0
Mvav 84,192,144
S_ s55,s71,512000
A_ v134,v54,v134
A_ v135,v55,v135
Zvav 80,220,180
A_ v124,v48,v124
A_ v125,v49,v125
Mvav 80,196,160
S_ m0,s51,38912
A_ v126,v50,v126
A_ v127,v51,v127
Mvav 80,192,140
L_ v7,24,s55,0
Zvav 76,220,176
W_ 10
A_ v116,v44,v116
A_ v117,v45,v117
Mvav 76,196,156
S_ s55,s71,522240
A_ v118,v46,v118
A_ v119,v47,v119
Mvav 76,192,136
W_ 0
A_ v108,v40,v108
Zvav 16,180,136
S_ m0,s51,39936
Ta 192,v3,24576
Ta 194,v3,26624
Mvav 16,184,156
L_ v7,24,s55,0
Mvav 16,188,176
S_ s55,s71,532480
Ta 196,v3,28672
Ta 198,v3,30720
Zvav 20,180,140
S_ m0,s51,40960
A_ v109,v41,v109
A_ v110,v42,v110
Mvav 20,184,160
s_and_b32 s85, s85, 7
Ta 220,v3,32768
Ta 222,v3,34816
Mvav 20,188,180
L_ v7,24,s55,0
Zvav 24,180,144
S_ s55,s71,542720
A_ v111,v43,v111
C_ 131,130,131
Mvav 24,184,164
A_ v100,v36,v100
A_ v101,v37,v101
Mvav 24,188,184
S_ m0,s51,41984
A_ v102,v38,v102
A_ v103,v39,v103
Zvav 28,180,148
L_ v7,24,s55,0
Mvav 28,184,168
s_lshl_b32 s85, s85, 7
C_ 130,128,129
C_ 132,132,133
Mvav 28,188,188
S_ s85,s85,s87
C_ 133,134,135
Zvav 32,180,152
Mvav 32,184,172
buffer_store_dwordx4 v[130:133], v6, s[28:31], s73 offen offset:0 sc1
Mvav 32,188,192
s_waitcnt vmcnt(23) lgkmcnt(0)
s_barrier
s_sub_u32 s73, s73, 4096
Zvav 52,220,192
S_ s55,s71,614400
Ta 180,%0,0
Ta 182,%0,2048
Mvav 52,196,172
S_ m0,s51,24576
C_ 123,122,123
C_ 122,120,121
Mvav 52,192,152
s_nop 0
Ta 184,%0,4096
Ta 186,%0,6144
Zvav 48,220,188
L_ v7,24,s55,0
Mvav 48,196,168
S_ s55,s71,624640
Ta 188,%0,8192
Ta 190,%0,10240
Mvav 48,192,148
S_ m0,s51,25600
C_ 124,124,125
C_ 125,126,127
Zvav 44,220,184
Mvav 44,196,164
buffer_store_dwordx4 v[122:125], v6, s[28:31], s73 offen offset:3072 sc1
Mvav 44,192,144
C_ 115,114,115
C_ 114,112,113
Zvav 40,220,180
C_ 116,116,117
C_ 117,118,119
Mvav 40,196,160
Mvav 40,192,140
buffer_store_dwordx4 v[114:117], v6, s[28:31], s73 offen offset:2048 sc1
Zvav 36,220,176
C_ 107,106,107
C_ 106,104,105
Mvav 36,196,156
C_ 108,108,109
C_ 109,110,111
Mvav 36,192,136
W_ 0
Mvav 56,180,196
s_nop 0
Ta 192,v1,0
Ta 194,v1,2048
Mvav 56,184,216
buffer_store_dwordx4 v[106:109], v6, s[28:31], s73 offen offset:1024 sc1
Mvav 56,188,236
s_nop 0
Ta 196,v1,4096
Ta 198,v1,6144
Mvav 60,180,200
C_ 99,98,99
C_ 98,96,97
Mvav 60,184,220
s_nop 0
Ta 220,v1,8192
Ta 222,v1,10240
Mvav 60,188,240
L_ v7,24,s55,0
Mvav 64,180,204
S_ s55,s71,634880
C_ 100,100,101
C_ 101,102,103
Mvav 64,184,224
S_ m0,s51,26624
Mvav 64,188,244
Mvav 68,180,208
buffer_store_dwordx4 v[98:101], v6, s[28:31], s73 offen offset:0 sc1
Mvav 68,184,228
Mvav 68,188,248
Mvav 72,180,212
Mvav 72,184,232
L_ v7,24,s55,0
Mvav 72,188,252
W_ 0
S_ s55,s71,645120
Mvav 92,220,252
S_ m0,s51,27648
Ta 180,v2,0
Ta 182,v2,2048
Mvav 92,196,232
Mvav 92,192,212
s_nop 0
Ta 184,v2,4096
Ta 186,v2,6144
Mvav 88,220,248
L_ v7,24,s55,0
Mvav 88,196,228
s_waitcnt vmcnt(21)
Ta 188,v2,8192
Ta 190,v2,10240
Mvav 88,192,208
S_ s55,s71,655360
v_cmp_eq_u32 vcc, s62, v11
Mvav 84,220,244
s_mul_i32 s76, s75, 204800
S_ m0,s51,28672
Mvav 84,196,224
L_ v7,24,s55,0
Mvav 84,192,204
s_bitcmp1_b32 vcc_lo, s75
s_cbranch_scc0 .Lspin0
.Lspun0:
S_ s55,s71,665600
Mvav 80,220,240
S_ s56,s76,s84
S_ m0,s51,29696
Mvav 80,196,220
Mvav 80,192,200
buffer_load_dwordx4 a[20:23], v6, s[28:31], s56 offen offset:0 sc1
Mvav 76,220,236
Mvav 76,196,216
Mvav 76,192,196
W_ 0
Mvav 16,180,196
s_nop 0
Ta 192,v3,0
Ta 194,v3,2048
Mvav 16,184,216
buffer_load_dwordx4 a[24:27], v6, s[28:31], s56 offen offset:1024 sc1
Mvav 16,188,236
s_nop 0
Ta 196,v3,4096
Ta 198,v3,6144
Mvav 20,180,200
Mvav 20,184,220
s_nop 0
Ta 220,v3,8192
Ta 222,v3,10240
Mvav 20,188,240
buffer_load_dwordx4 a[28:31], v6, s[28:31], s56 offen offset:2048 sc1
Mvav 24,180,204
Mvav 24,184,224
Mvav 24,188,244
Mvav 28,180,208
buffer_load_dwordx4 a[32:35], v6, s[28:31], s56 offen offset:3072 sc1
Mvav 28,184,228
S_ s56,s56,4096
Mvav 28,188,248
Mvav 32,180,212
Mvav 32,184,232
buffer_load_dwordx4 a[36:39], v6, s[28:31], s56 offen offset:0 sc1
Mvav 32,188,252
s_waitcnt vmcnt(29) lgkmcnt(0)
s_barrier
S_ s56,s76,s74
Mvav 52,220,252
s_nop 0
Ta 180,%0,12288
Ta 182,%0,14336
Mvav 52,196,232
Mvav 52,192,212
s_nop 0
Ta 184,%0,16384
Ta 186,%0,18432
Mvav 48,220,248
buffer_load_dwordx4 a[60:63], v6, s[28:31], s56 offen offset:0 sc1
Mvav 48,196,228
S_ s56,s76,s82
Ta 188,%0,20480
Ta 190,%0,22528
Mvav 48,192,208
Mvav 44,220,244
Mvav 44,196,224
buffer_load_dwordx4 a[64:67], v6, s[28:31], s56 offen offset:0 sc1
Mvav 44,192,204
Mvav 40,220,240
Mvav 40,196,220
Mvav 40,192,200
L_ v7,24,s55,0
Mvav 36,220,236
S_ s55,s71,0
Mvav 36,196,216
S_ m0,s51,0
Mvav 36,192,196
W_ 0
Mvaa 56,180,0
s_nop 0
Ta 192,v1,12288
Ta 194,v1,14336
Mvaa 56,184,40
L_ v7,24,s55,0
Mvaa 56,188,80
S_ s55,s71,10240
Ta 196,v1,16384
Ta 198,v1,18432
Mvaa 60,180,4
S_ m0,s51,1024
Mvaa 60,184,44
s_nop 0
Ta 220,v1,20480
Ta 222,v1,22528
Mvaa 60,188,84
L_ v7,24,s55,0
Mvaa 64,180,8
S_ s55,s71,20480
Mvaa 64,184,48
S_ m0,s51,2048
Mvaa 64,188,88
Mvaa 68,180,12
L_ v7,24,s55,0
Mvaa 68,184,52
S_ s55,s71,30720
Mvaa 68,188,240
S_ m0,s51,3072
Mvaa 72,180,16
Mvaa 72,184,56
L_ v7,24,s55,0
Mvaa 72,188,244
W_ 0
S_ s55,s71,40960
Mvaa 92,220,244
S_ m0,s51,4096
Ta 180,v2,12288
Ta 182,v2,14336
Mvaa 92,196,56
Mvaa 92,192,16
s_nop 0
Ta 184,v2,16384
Ta 186,v2,18432
Mvaa 88,220,240
L_ v7,24,s55,0
Mvaa 88,196,52
S_ s55,s71,51200
Ta 188,v2,20480
Ta 190,v2,22528
Mvaa 88,192,12
S_ m0,s51,5120
Mvaa 84,220,88
Mvaa 84,196,48
L_ v7,24,s55,0
Mvaa 84,192,8
Mvaa 80,220,84
Mvaa 80,196,44
Mvaa 80,192,4
buffer_load_dword a74, v4, s[24:27], s85 offen
Mvaa 76,220,80
Mvaa 76,196,40
Mvaa 76,192,0
W_ 0
Mvaa 16,180,0
s_nop 0
Ta 192,v3,12288
Ta 194,v3,14336
Mvaa 16,184,40
Mvaa 16,188,80
s_nop 0
Ta 196,v3,16384
Ta 198,v3,18432
Mvaa 20,180,4
Mvaa 20,184,44
s_nop 0
Ta 220,v3,20480
Ta 222,v3,22528
Mvaa 20,188,84
Mvaa 24,180,8
Mvaa 24,184,48
Mvaa 24,188,88
Mvaa 28,180,12
Mvaa 28,184,52
Mvaa 28,188,240
Mvaa 32,180,16
Mvaa 32,184,56
Mvaa 32,188,244
s_waitcnt vmcnt(31) lgkmcnt(0)
s_barrier
Mvaa 52,220,244
s_nop 0
Ta 180,%0,49152
Ta 182,%0,51200
Mvaa 52,196,56
Mvaa 52,192,16
s_nop 0
Ta 184,%0,53248
Ta 186,%0,55296
Mvaa 48,220,240
Mvaa 48,196,52
s_nop 0
Ta 188,%0,57344
Ta 190,%0,59392
Mvaa 48,192,12
Mvaa 44,220,88
Mvaa 44,196,48
Mvaa 44,192,8
Mvaa 40,220,84
Mvaa 40,196,44
Mvaa 40,192,4
Mvaa 36,220,80
Mvaa 36,196,40
Mvaa 36,192,0
W_ 0
Mvaa 56,180,120
S_ s81,s6,4
Ta 192,v1,49152
Ta 194,v1,51200
Mvaa 56,184,160
s_and_b32 s81, s81, 7
Mvaa 56,188,200
s_mul_i32 s81, s81, 20480
Ta 196,v1,53248
Ta 198,v1,55296
Mvaa 60,180,124
s_mov_b32 m0, s52
S_ s73,s81,s78
Mvaa 60,184,164
S_ s73,s73,4096
Ta 220,v1,57344
Ta 222,v1,59392
Mvaa 60,188,204
s_nop 0
Y_ 56,0
Y_ 57,256
Mvaa 64,180,128
s_nop 0
Y_ 58,512
Y_ 59,768
Mvaa 64,184,168
Mvaa 64,188,208
s_nop 0
Y_ 60,1024
Y_ 61,1280
Mvaa 68,180,132
s_nop 0
Y_ 62,1536
Y_ 63,1792
Mvaa 68,184,172
Mvaa 68,188,212
s_nop 0
Y_ 64,2048
Y_ 65,2304
Mvaa 72,180,136
s_nop 0
Y_ 66,2560
Y_ 67,2816
Mvaa 72,184,176
Mvaa 72,188,216
W_ 12
Y_ 68,3072
Y_ 69,3328
Mvaa 92,220,216
s_nop 0
Ta 180,v2,49152
Ta 182,v2,51200
Mvaa 92,196,176
s_nop 0
Y_ 70,3584
Y_ 71,3840
Mvaa 92,192,136
s_nop 0
Ta 184,v2,53248
Ta 186,v2,55296
Mvaa 88,220,212
s_nop 0
Y_ 72,4096
Y_ 73,4352
Mvaa 88,196,172
S_ s83,s6,3
Ta 188,v2,57344
Ta 190,v2,59392
Mvaa 88,192,132
s_and_b32 s83, s83, 7
Y_ 74,4608
Y_ 75,4864
Mvaa 84,220,208
s_lshl_b32 s83, s83, 2
Y_ 92,9216
Y_ 93,9472
Mvaa 84,196,168
s_nop 0
Y_ 94,9728
Y_ 95,9984
Mvaa 84,192,128
s_nop 0
Y_ 88,8192
Y_ 89,8448
Mvaa 80,220,204
s_nop 0
Y_ 90,8704
Y_ 91,8960
Mvaa 80,196,164
v_cmp_eq_u32_e64 s[36:37], s83, v8
Mvaa 80,192,124
s_nop 0
Y_ 84,7168
Y_ 85,7424
Mvaa 76,220,200
s_nop 0
Y_ 86,7680
Y_ 87,7936
Mvaa 76,196,160
v_mov_b32 v10, 0x7fff0000
Mvaa 76,192,120
W_ 14
Y_ 80,6144
Y_ 81,6400
Mvaa 16,180,120
s_nop 0
Ta 192,v3,49152
Ta 194,v3,51200
Mvaa 16,184,160
s_nop 0
Y_ 82,6656
Y_ 83,6912
Mvaa 16,188,200
s_nop 0
Ta 196,v3,53248
Ta 198,v3,55296
Mvaa 20,180,124
s_nop 0
Y_ 76,5120
Y_ 77,5376
Mvaa 20,184,164
s_nop 0
Ta 220,v3,57344
Ta 222,v3,59392
Mvaa 20,188,204
s_nop 0
Y_ 78,5632
Y_ 79,5888
Mvaa 24,180,128
v_cndmask_b32_e64 v10, v10, v9, s[36:37]
Mvaa 24,184,168
Mvaa 24,188,208
s_waitcnt vmcnt(18) lgkmcnt(0)
s_barrier
Mvaa 28,180,132
buffer_store_dword a255, v10, s[92:95], 0 offen offset:384 sc1
Mvaa 28,184,172
s_sub_u32 s75, s6, 2
ds_read2st64_b32 v[96:97], v5 offset0:0 offset1:1
S_ s55,s71,122880
Mvaa 28,188,212
s_and_b32 s75, s75, 7
ds_read2st64_b32 v[98:99], v5 offset0:2 offset1:3
S_ m0,s51,12288
Mvaa 32,180,136
ds_read2st64_b32 v[104:105], v5 offset0:4 offset1:5
Mvaa 32,184,176
buffer_load_dword v11, v8, s[92:95], s96 offen offset:384 sc1
Mvaa 32,188,216
ds_read2st64_b32 v[106:107], v5 offset0:6 offset1:7
Mvaa 52,220,216
s_nop 0
Ta 180,%0,36864
Ta 182,%0,38912
Mvaa 52,196,176
ds_read2st64_b32 v[112:113], v5 offset0:8 offset1:9
Mvaa 52,192,136
W_ 5
Ta 184,%0,40960
Ta 186,%0,43008
Mvaa 48,220,212
L_ v7,24,s55,0
Mvaa 48,196,172
S_ s55,s71,133120
Ta 188,%0,45056
Ta 190,%0,47104
Mvaa 48,192,132
S_ m0,s51,13312
ds_read2st64_b32 v[114:115], v5 offset0:10 offset1:11
A_ v96,v16,v96
Mvaa 44,220,208
ds_read2st64_b32 v[120:121], v5 offset0:12 offset1:13
A_ v97,v17,v97
Mvaa 44,196,168
L_ v7,24,s55,0
Mvaa 44,192,128
S_ s55,s71,143360
ds_read2st64_b32 v[122:123], v5 offset0:14 offset1:15
A_ v98,v18,v98
Mvaa 40,220,204
S_ m0,s51,14336
ds_read2st64_b32 v[128:129], v5 offset0:16 offset1:17
A_ v99,v19,v99
Mvaa 40,196,164
W_ 11
ds_read2st64_b32 v[130:131], v5 offset0:18 offset1:19
A_ v104,v20,v104
Mvaa 40,192,124
L_ v7,24,s55,0
Mvaa 36,220,200
S_ s55,s71,153600
ds_read2st64_b32 v[132:133], v5 offset0:36 offset1:37
A_ v105,v21,v105
Mvaa 36,196,160
S_ m0,s51,15360
ds_read2st64_b32 v[134:135], v5 offset0:38 offset1:39
A_ v106,v22,v106
Mvaa 36,192,120
ds_read2st64_b32 v[124:125], v5 offset0:32 offset1:33
A_ v107,v23,v107
s_waitcnt vmcnt(13)
Zaaa 100,70,20
L_ v7,24,s55,0
Zaaa 140,68,20
W_ 7
ds_read2st64_b32 v[126:127], v5 offset0:34 offset1:35
A_ v112,v24,v112
Zaaa 104,70,24
S_ s55,s71,163840
ds_read2st64_b32 v[116:117], v5 offset0:28 offset1:29
A_ v113,v25,v113
Zaaa 144,68,24
S_ m0,s51,16384
ds_read2st64_b32 v[118:119], v5 offset0:30 offset1:31
A_ v114,v26,v114
Zaaa 108,70,28
L_ v7,24,s55,0
Zaaa 148,68,28
S_ s55,s71,174080
ds_read2st64_b32 v[108:109], v5 offset0:24 offset1:25
A_ v115,v27,v115
Zaaa 112,70,32
W_ 9
ds_read2st64_b32 v[110:111], v5 offset0:26 offset1:27
A_ v120,v28,v120
Zaaa 152,68,32
S_ m0,s51,17408
ds_read2st64_b32 v[100:101], v5 offset0:20 offset1:21
A_ v121,v29,v121
Zaaa 116,70,36
L_ v7,24,s55,0
Zaaa 156,68,36
S_ s79,s6,6
ds_read2st64_b32 v[102:103], v5 offset0:22 offset1:23
A_ v122,v30,v122
Maaa 224,70,60
s_and_b32 s79, s79, 7
A_ v123,v31,v123
Maaa 228,68,60
W_ 10
A_ v128,v32,v128
A_ v129,v33,v129
Maaa 232,70,64
s_lshl_b32 s79, s79, 7
A_ v130,v34,v130
A_ v131,v35,v131
Maaa 236,68,64
W_ 9
A_ v132,v52,v132
W_ 8
Zvav 56,180,136
S_ s71,s72,s79
Ta 192,v1,36864
Ta 194,v1,38912
Mvav 56,184,156
S_ s55,s71,491520
A_ v133,v53,v133
A_ v134,v54,v134
Mvav 56,188,176
S_ m0,s51,49152
Ta 196,v1,40960
Ta 198,v1,43008
Zvav 60,180,140
L_ v7,24,s55,0
Mvav 60,184,160
S_ s55,s71,501760
Ta 220,v1,45056
Ta 222,v1,47104
Mvav 60,188,180
S_ m0,s51,50176
A_ v135,v55,v135
Zvav 64,180,144
W_ 12
A_ v124,v48,v124
A_ v125,v49,v125
Mvav 64,184,164
L_ v7,24,s55,0
Mvav 64,188,184
S_ s55,s71,512000
A_ v126,v50,v126
A_ v127,v51,v127
Zvav 68,180,148
W_ 10
A_ v116,v44,v116
A_ v117,v45,v117
Mvav 68,184,168
S_ m0,s51,51200
A_ v118,v46,v118
A_ v119,v47,v119
Mvav 68,188,188
L_ v7,24,s55,0
Zvav 72,180,152
W_ 8
A_ v108,v40,v108
A_ v109,v41,v109
Mvav 72,184,172
S_ s55,s71,522240
A_ v110,v42,v110
A_ v111,v43,v111
Mvav 72,188,192
W_ 0
A_ v100,v36,v100
Zvav 92,220,192
S_ m0,s51,52224
Ta 180,v2,36864
Ta 182,v2,38912
Mvav 92,196,172
L_ v7,24,s55,0
Mvav 92,192,152
S_ s55,s71,532480
Ta 184,v2,40960
Ta 186,v2,43008
Zvav 88,220,188
S_ m0,s51,53248
A_ v101,v37,v101
A_ v102,v38,v102
Mvav 88,196,168
s_nop 0
Ta 188,v2,45056
Ta 190,v2,47104
Mvav 88,192,148
L_ v7,24,s55,0
Zvav 84,220,184
S_ s55,s71,542720
A_ v103,v39,v103
C_ 131,130,131
Mvav 84,196,164
S_ m0,s51,54272
C_ 130,128,129
C_ 132,132,133
Mvav 84,192,144
S_ s85,s6,8
C_ 133,134,135
Zvav 80,220,180
buffer_store_dwordx4 v[130:133], v6, s[28:31], s73 offen offset:0 sc1
Mvav 80,196,160
s_sub_u32 s73, s73, 4096
s_and_b32 s85, s85, 7
Mvav 80,192,140
s_lshl_b32 s85, s85, 7
C_ 123,122,123
C_ 122,120,121
Zvav 76,220,176
S_ s85,s85,s87
C_ 124,124,125
C_ 125,126,127
Mvav 76,196,156
buffer_store_dwordx4 v[122:125], v6, s[28:31], s73 offen offset:3072 sc1
Mvav 76,192,136
W_ 0
C_ 115,114,115
C_ 114,112,113
Zvav 16,180,136
s_nop 0
Ta 192,v3,36864
Ta 194,v3,38912
Mvav 16,184,156
C_ 116,116,117
C_ 117,118,119
Mvav 16,188,176
s_nop 0
Ta 196,v3,40960
Ta 198,v3,43008
Zvav 20,180,140
buffer_store_dwordx4 v[114:117], v6, s[28:31], s73 offen offset:2048 sc1
Mvav 20,184,160
s_nop 0
Ta 220,v3,45056
Ta 222,v3,47104
Mvav 20,188,180
C_ 107,106,107
C_ 106,104,105
Zvav 24,180,144
C_ 108,108,109
C_ 109,110,111
Mvav 24,184,164
buffer_store_dwordx4 v[106:109], v6, s[28:31], s73 offen offset:1024 sc1
Mvav 24,188,184
C_ 99,98,99
C_ 98,96,97
Zvav 28,180,148
C_ 100,100,101
C_ 101,102,103
Mvav 28,184,168
Mvav 28,188,188
buffer_store_dwordx4 v[98:101], v6, s[28:31], s73 offen offset:0 sc1
Zvav 32,180,152
Mvav 32,184,172
Mvav 32,188,192
s_waitcnt vmcnt(25) lgkmcnt(0)
s_barrier
Zvav 52,220,192
s_nop 0
Ta 180,%0,24576
Ta 182,%0,26624
Mvav 52,196,172
L_ v7,24,s55,0
Mvav 52,192,152
S_ s55,s71,614400
Ta 184,%0,28672
Ta 186,%0,30720
Zvav 48,220,188
S_ m0,s51,36864
Mvav 48,196,168
s_nop 0
Ta 188,%0,32768
Ta 190,%0,34816
Mvav 48,192,148
L_ v7,24,s55,0
Zvav 44,220,184
S_ s55,s71,624640
Mvav 44,196,164
S_ m0,s51,37888
Mvav 44,192,144
Zvav 40,220,180
L_ v7,24,s55,0
Mvav 40,196,160
S_ s55,s71,634880
Mvav 40,192,140
S_ m0,s51,38912
Zvav 36,220,176
Mvav 36,196,156
L_ v7,24,s55,0
Mvav 36,192,136
W_ 0
S_ s55,s71,645120
Mvav 56,180,196
S_ m0,s51,39936
Ta 192,v1,24576
Ta 194,v1,26624
Mvav 56,184,216
Mvav 56,188,236
s_nop 0
Ta 196,v1,28672
Ta 198,v1,30720
Mvav 60,180,200
L_ v7,24,s55,0
Mvav 60,184,220
S_ s55,s71,655360
Ta 220,v1,32768
Ta 222,v1,34816
Mvav 60,188,240
S_ m0,s51,40960
Mvav 64,180,204
Mvav 64,184,224
L_ v7,24,s55,0
Mvav 64,188,244
S_ s55,s71,665600
Mvav 68,180,208
S_ m0,s51,41984
Mvav 68,184,228
Mvav 68,188,248
L_ v7,24,s55,0
Mvav 72,180,212
Mvav 72,184,232
Mvav 72,188,252
W_ 0
Mvav 92,220,252
s_nop 0
Ta 180,v2,24576
Ta 182,v2,26624
Mvav 92,196,232
buffer_load_dword a74, v4, s[24:27], s85 offen
Mvav 92,192,212
s_nop 0
Ta 184,v2,28672
Ta 186,v2,30720
Mvav 88,220,248
s_waitcnt vmcnt(24)
Mvav 88,196,228
s_nop 0
Ta 188,v2,32768
Ta 190,v2,34816
Mvav 88,192,208
v_cmp_eq_u32 vcc, s62, v11
Mvav 84,220,244
s_mul_i32 s76, s75, 204800
Mvav 84,196,224
s_bitcmp1_b32 vcc_lo, s75
s_cbranch_scc0 .Lspin1
.Lspun1:
Mvav 84,192,204
S_ s56,s76,s84
Mvav 80,220,240
buffer_load_dwordx4 a[20:23], v6, s[28:31], s56 offen offset:0 sc1
Mvav 80,196,220
Mvav 80,192,200
Mvav 76,220,236
Mvav 76,196,216
buffer_load_dwordx4 a[24:27], v6, s[28:31], s56 offen offset:1024 sc1
Mvav 76,192,196
W_ 0
Mvav 16,180,196
s_nop 0
Ta 192,v3,24576
Ta 194,v3,26624
Mvav 16,184,216
Mvav 16,188,236
s_nop 0
Ta 196,v3,28672
Ta 198,v3,30720
Mvav 20,180,200
buffer_load_dwordx4 a[28:31], v6, s[28:31], s56 offen offset:2048 sc1
Mvav 20,184,220
s_nop 0
Ta 220,v3,32768
Ta 222,v3,34816
Mvav 20,188,240
Mvav 24,180,204
Mvav 24,184,224
buffer_load_dwordx4 a[32:35], v6, s[28:31], s56 offen offset:3072 sc1
Mvav 24,188,244
S_ s56,s56,4096
Mvav 28,180,208
Mvav 28,184,228
Mvav 28,188,248
buffer_load_dwordx4 a[36:39], v6, s[28:31], s56 offen offset:0 sc1
Mvav 32,180,212
S_ s56,s76,s74
Mvav 32,184,232
Mvav 32,188,252
s_waitcnt vmcnt(32) lgkmcnt(0)
s_barrier
Mvav 52,220,252
S_ s55,s71,0
Ta 180,%0,0
Ta 182,%0,2048
Mvav 52,196,232
buffer_load_dwordx4 a[60:63], v6, s[28:31], s56 offen offset:0 sc1
Mvav 52,192,212
S_ s56,s76,s82
Ta 184,%0,4096
Ta 186,%0,6144
Mvav 48,220,248
S_ m0,s51,24576
Mvav 48,196,228
s_nop 0
Ta 188,%0,8192
Ta 190,%0,10240
Mvav 48,192,208
buffer_load_dwordx4 a[64:67], v6, s[28:31], s56 offen offset:0 sc1
Mvav 44,220,244
Mvav 44,196,224
Mvav 44,192,204
Mvav 40,220,240
L_ v7,24,s55,0
Mvav 40,196,220
S_ s55,s71,10240
Mvav 40,192,200
S_ m0,s51,25600
Mvav 36,220,236
Mvav 36,196,216
L_ v7,24,s55,0
Mvav 36,192,196
W_ 0
S_ s55,s71,20480
Mvaa 56,180,0
S_ m0,s51,26624
Ta 192,v1,0
Ta 194,v1,2048
Mvaa 56,184,40
Mvaa 56,188,80
s_nop 0
Ta 196,v1,4096
Ta 198,v1,6144
Mvaa 60,180,4
L_ v7,24,s55,0
Mvaa 60,184,44
S_ s55,s71,30720
Ta 220,v1,8192
Ta 222,v1,10240
Mvaa 60,188,84
S_ m0,s51,27648
Mvaa 64,180,8
Mvaa 64,184,48
L_ v7,24,s55,0
Mvaa 64,188,88
S_ s55,s71,40960
Mvaa 68,180,12
S_ m0,s51,28672
Mvaa 68,184,52
Mvaa 68,188,240
L_ v7,24,s55,0
Mvaa 72,180,16
S_ s55,s71,51200
Mvaa 72,184,56
S_ m0,s51,29696
Mvaa 72,188,244
W_ 0
Mvaa 92,220,244
s_nop 0
Ta 180,v2,0
Ta 182,v2,2048
Mvaa 92,196,56
L_ v7,24,s55,0
Mvaa 92,192,16
s_nop 0
Ta 184,v2,4096
Ta 186,v2,6144
Mvaa 88,220,240
Mvaa 88,196,52
s_nop 0
Ta 188,v2,8192
Ta 190,v2,10240
Mvaa 88,192,12
Mvaa 84,220,88
Mvaa 84,196,48
Mvaa 84,192,8
Mvaa 80,220,84
Mvaa 80,196,44
Mvaa 80,192,4
Mvaa 76,220,80
Mvaa 76,196,40
Mvaa 76,192,0
W_ 0
Mvaa 16,180,0
s_nop 0
Ta 192,v3,0
Ta 194,v3,2048
Mvaa 16,184,40
Mvaa 16,188,80
s_nop 0
Ta 196,v3,4096
Ta 198,v3,6144
Mvaa 20,180,4
Mvaa 20,184,44
s_nop 0
Ta 220,v3,8192
Ta 222,v3,10240
Mvaa 20,188,84
Mvaa 24,180,8
Mvaa 24,184,48
Mvaa 24,188,88
Mvaa 28,180,12
Mvaa 28,184,52
Mvaa 28,188,240
Mvaa 32,180,16
Mvaa 32,184,56
Mvaa 32,188,244
s_waitcnt vmcnt(31) lgkmcnt(0)
s_barrier
Mvaa 52,220,244
s_nop 0
Ta 180,%0,12288
Ta 182,%0,14336
Mvaa 52,196,56
Mvaa 52,192,16
s_nop 0
Ta 184,%0,16384
Ta 186,%0,18432
Mvaa 48,220,240
Mvaa 48,196,52
s_nop 0
Ta 188,%0,20480
Ta 190,%0,22528
Mvaa 48,192,12
Mvaa 44,220,88
Mvaa 44,196,48
Mvaa 44,192,8
Mvaa 40,220,84
Mvaa 40,196,44
Mvaa 40,192,4
Mvaa 36,220,80
Mvaa 36,196,40
Mvaa 36,192,0
W_ 0
Mvaa 56,180,120
S_ s81,s6,5
Ta 192,v1,12288
Ta 194,v1,14336
Mvaa 56,184,160
s_and_b32 s81, s81, 7
Mvaa 56,188,200
s_mul_i32 s81, s81, 20480
Ta 196,v1,16384
Ta 198,v1,18432
Mvaa 60,180,124
s_mov_b32 m0, s52
S_ s73,s81,s78
Mvaa 60,184,164
S_ s73,s73,4096
Ta 220,v1,20480
Ta 222,v1,22528
Mvaa 60,188,204
s_nop 0
Y_ 56,0
Y_ 57,256
Mvaa 64,180,128
s_nop 0
Y_ 58,512
Y_ 59,768
Mvaa 64,184,168
Mvaa 64,188,208
s_nop 0
Y_ 60,1024
Y_ 61,1280
Mvaa 68,180,132
s_nop 0
Y_ 62,1536
Y_ 63,1792
Mvaa 68,184,172
Mvaa 68,188,212
s_nop 0
Y_ 64,2048
Y_ 65,2304
Mvaa 72,180,136
s_nop 0
Y_ 66,2560
Y_ 67,2816
Mvaa 72,184,176
Mvaa 72,188,216
W_ 12
Y_ 68,3072
Y_ 69,3328
Mvaa 92,220,216
s_nop 0
Ta 180,v2,12288
Ta 182,v2,14336
Mvaa 92,196,176
s_nop 0
Y_ 70,3584
Y_ 71,3840
Mvaa 92,192,136
s_nop 0
Ta 184,v2,16384
Ta 186,v2,18432
Mvaa 88,220,212
s_nop 0
Y_ 72,4096
Y_ 73,4352
Mvaa 88,196,172
S_ s83,s6,4
Ta 188,v2,20480
Ta 190,v2,22528
Mvaa 88,192,132
s_and_b32 s83, s83, 7
Y_ 74,4608
Y_ 75,4864
Mvaa 84,220,208
s_lshl_b32 s83, s83, 2
Y_ 92,9216
Y_ 93,9472
Mvaa 84,196,168
s_nop 0
Y_ 94,9728
Y_ 95,9984
Mvaa 84,192,128
s_nop 0
Y_ 88,8192
Y_ 89,8448
Mvaa 80,220,204
s_nop 0
Y_ 90,8704
Y_ 91,8960
Mvaa 80,196,164
v_cmp_eq_u32_e64 s[36:37], s83, v8
Mvaa 80,192,124
s_nop 0
Y_ 84,7168
Y_ 85,7424
Mvaa 76,220,200
s_nop 0
Y_ 86,7680
Y_ 87,7936
Mvaa 76,196,160
v_mov_b32 v10, 0x7fff0000
Mvaa 76,192,120
W_ 14
Y_ 80,6144
Y_ 81,6400
Mvaa 16,180,120
s_nop 0
Ta 192,v3,12288
Ta 194,v3,14336
Mvaa 16,184,160
s_nop 0
Y_ 82,6656
Y_ 83,6912
Mvaa 16,188,200
s_nop 0
Ta 196,v3,16384
Ta 198,v3,18432
Mvaa 20,180,124
s_nop 0
Y_ 76,5120
Y_ 77,5376
Mvaa 20,184,164
s_nop 0
Ta 220,v3,20480
Ta 222,v3,22528
Mvaa 20,188,204
s_nop 0
Y_ 78,5632
Y_ 79,5888
Mvaa 24,180,128
v_cndmask_b32_e64 v10, v10, v9, s[36:37]
Mvaa 24,184,168
Mvaa 24,188,208
s_waitcnt vmcnt(20) lgkmcnt(0)
s_barrier
Mvaa 28,180,132
buffer_store_dword a255, v10, s[92:95], 0 offen offset:384 sc1
Mvaa 28,184,172
s_sub_u32 s75, s6, 3
ds_read2st64_b32 v[96:97], v5 offset0:0 offset1:1
S_ s55,s71,122880
Mvaa 28,188,212
s_and_b32 s75, s75, 7
ds_read2st64_b32 v[98:99], v5 offset0:2 offset1:3
S_ m0,s51,0
Mvaa 32,180,136
ds_read2st64_b32 v[104:105], v5 offset0:4 offset1:5
Mvaa 32,184,176
buffer_load_dword v11, v8, s[92:95], s96 offen offset:384 sc1
Mvaa 32,188,216
ds_read2st64_b32 v[106:107], v5 offset0:6 offset1:7
Mvaa 52,220,216
s_nop 0
Ta 180,%0,49152
Ta 182,%0,51200
Mvaa 52,196,176
ds_read2st64_b32 v[112:113], v5 offset0:8 offset1:9
Mvaa 52,192,136
W_ 5
Ta 184,%0,53248
Ta 186,%0,55296
Mvaa 48,220,212
L_ v7,24,s55,0
Mvaa 48,196,172
S_ s55,s71,133120
Ta 188,%0,57344
Ta 190,%0,59392
Mvaa 48,192,132
S_ m0,s51,1024
ds_read2st64_b32 v[114:115], v5 offset0:10 offset1:11
A_ v96,v16,v96
Mvaa 44,220,208
ds_read2st64_b32 v[120:121], v5 offset0:12 offset1:13
A_ v97,v17,v97
Mvaa 44,196,168
L_ v7,24,s55,0
Mvaa 44,192,128
S_ s55,s71,143360
ds_read2st64_b32 v[122:123], v5 offset0:14 offset1:15
A_ v98,v18,v98
Mvaa 40,220,204
S_ m0,s51,2048
ds_read2st64_b32 v[128:129], v5 offset0:16 offset1:17
A_ v99,v19,v99
Mvaa 40,196,164
W_ 11
ds_read2st64_b32 v[130:131], v5 offset0:18 offset1:19
A_ v104,v20,v104
Mvaa 40,192,124
L_ v7,24,s55,0
Mvaa 36,220,200
S_ s55,s71,153600
ds_read2st64_b32 v[132:133], v5 offset0:36 offset1:37
A_ v105,v21,v105
Mvaa 36,196,160
S_ m0,s51,3072
ds_read2st64_b32 v[134:135], v5 offset0:38 offset1:39
)A"
R"A(A_ v106,v22,v106
Mvaa 36,192,120
ds_read2st64_b32 v[124:125], v5 offset0:32 offset1:33
A_ v107,v23,v107
s_waitcnt vmcnt(11)
Maaa 100,70,20
L_ v7,24,s55,0
Maaa 140,68,20
W_ 7
ds_read2st64_b32 v[126:127], v5 offset0:34 offset1:35
A_ v112,v24,v112
Maaa 104,70,24
S_ s55,s71,163840
ds_read2st64_b32 v[116:117], v5 offset0:28 offset1:29
A_ v113,v25,v113
Maaa 144,68,24
S_ m0,s51,4096
ds_read2st64_b32 v[118:119], v5 offset0:30 offset1:31
A_ v114,v26,v114
Maaa 108,70,28
L_ v7,24,s55,0
Maaa 148,68,28
S_ s55,s71,174080
ds_read2st64_b32 v[108:109], v5 offset0:24 offset1:25
A_ v115,v27,v115
Maaa 112,70,32
W_ 9
ds_read2st64_b32 v[110:111], v5 offset0:26 offset1:27
A_ v120,v28,v120
Maaa 152,68,32
S_ m0,s51,5120
ds_read2st64_b32 v[100:101], v5 offset0:20 offset1:21
A_ v121,v29,v121
Maaa 116,70,36
L_ v7,24,s55,0
Maaa 156,68,36
S_ s79,s6,7
ds_read2st64_b32 v[102:103], v5 offset0:22 offset1:23
A_ v122,v30,v122
Maaa 224,70,60
s_and_b32 s79, s79, 7
A_ v123,v31,v123
Maaa 228,68,60
W_ 10
A_ v128,v32,v128
A_ v129,v33,v129
Maaa 232,70,64
s_lshl_b32 s79, s79, 7
A_ v130,v34,v130
A_ v131,v35,v131
Maaa 236,68,64
W_ 9
A_ v132,v52,v132
W_ 8
Zvav 56,180,136
S_ s71,s72,s79
Ta 192,v1,49152
Ta 194,v1,51200
Mvav 56,184,156
S_ s55,s71,491520
A_ v133,v53,v133
A_ v134,v54,v134
Mvav 56,188,176
S_ m0,s51,12288
Ta 196,v1,53248
Ta 198,v1,55296
Zvav 60,180,140
L_ v7,24,s55,0
Mvav 60,184,160
S_ s55,s71,501760
Ta 220,v1,57344
Ta 222,v1,59392
Mvav 60,188,180
S_ m0,s51,13312
A_ v135,v55,v135
Zvav 64,180,144
W_ 12
A_ v124,v48,v124
A_ v125,v49,v125
Mvav 64,184,164
L_ v7,24,s55,0
Mvav 64,188,184
S_ s55,s71,512000
A_ v126,v50,v126
A_ v127,v51,v127
Zvav 68,180,148
W_ 10
A_ v116,v44,v116
A_ v117,v45,v117
Mvav 68,184,168
S_ m0,s51,14336
A_ v118,v46,v118
A_ v119,v47,v119
Mvav 68,188,188
L_ v7,24,s55,0
Zvav 72,180,152
W_ 8
A_ v108,v40,v108
A_ v109,v41,v109
Mvav 72,184,172
S_ s55,s71,522240
A_ v110,v42,v110
A_ v111,v43,v111
Mvav 72,188,192
W_ 0
A_ v100,v36,v100
Zvav 92,220,192
S_ m0,s51,15360
Ta 180,v2,49152
Ta 182,v2,51200
Mvav 92,196,172
L_ v7,24,s55,0
Mvav 92,192,152
S_ s55,s71,532480
Ta 184,v2,53248
Ta 186,v2,55296
Zvav 88,220,188
S_ m0,s51,16384
A_ v101,v37,v101
A_ v102,v38,v102
Mvav 88,196,168
s_nop 0
Ta 188,v2,57344
Ta 190,v2,59392
Mvav 88,192,148
L_ v7,24,s55,0
Zvav 84,220,184
S_ s55,s71,542720
A_ v103,v39,v103
C_ 131,130,131
Mvav 84,196,164
S_ m0,s51,17408
C_ 130,128,129
C_ 132,132,133
Mvav 84,192,144
C_ 133,134,135
Zvav 80,220,180
buffer_store_dwordx4 v[130:133], v6, s[28:31], s73 offen offset:0 sc1
Mvav 80,196,160
s_sub_u32 s73, s73, 4096
Mvav 80,192,140
C_ 123,122,123
C_ 122,120,121
Zvav 76,220,176
C_ 124,124,125
C_ 125,126,127
Mvav 76,196,156
buffer_store_dwordx4 v[122:125], v6, s[28:31], s73 offen offset:3072 sc1
Mvav 76,192,136
W_ 0
C_ 115,114,115
C_ 114,112,113
Zvav 16,180,136
s_nop 0
Ta 192,v3,49152
Ta 194,v3,51200
Mvav 16,184,156
C_ 116,116,117
C_ 117,118,119
Mvav 16,188,176
s_nop 0
Ta 196,v3,53248
Ta 198,v3,55296
Zvav 20,180,140
buffer_store_dwordx4 v[114:117], v6, s[28:31], s73 offen offset:2048 sc1
Mvav 20,184,160
s_nop 0
Ta 220,v3,57344
Ta 222,v3,59392
Mvav 20,188,180
C_ 107,106,107
C_ 106,104,105
Zvav 24,180,144
C_ 108,108,109
C_ 109,110,111
Mvav 24,184,164
buffer_store_dwordx4 v[106:109], v6, s[28:31], s73 offen offset:1024 sc1
Mvav 24,188,184
C_ 99,98,99
C_ 98,96,97
Zvav 28,180,148
C_ 100,100,101
C_ 101,102,103
Mvav 28,184,168
Mvav 28,188,188
buffer_store_dwordx4 v[98:101], v6, s[28:31], s73 offen offset:0 sc1
Zvav 32,180,152
Mvav 32,184,172
Mvav 32,188,192
s_waitcnt vmcnt(32) lgkmcnt(0)
s_barrier
Zvav 52,220,192
s_nop 0
Ta 180,%0,36864
Ta 182,%0,38912
Mvav 52,196,172
L_ v7,24,s55,0
Mvav 52,192,152
S_ s55,s71,614400
Ta 184,%0,40960
Ta 186,%0,43008
Zvav 48,220,188
S_ m0,s51,49152
Mvav 48,196,168
s_nop 0
Ta 188,%0,45056
Ta 190,%0,47104
Mvav 48,192,148
L_ v7,24,s55,0
Zvav 44,220,184
S_ s55,s71,624640
Mvav 44,196,164
S_ m0,s51,50176
Mvav 44,192,144
Zvav 40,220,180
L_ v7,24,s55,0
Mvav 40,196,160
S_ s55,s71,634880
Mvav 40,192,140
S_ m0,s51,51200
Zvav 36,220,176
Mvav 36,196,156
L_ v7,24,s55,0
Mvav 36,192,136
W_ 0
S_ s55,s71,645120
Mvav 56,180,196
S_ m0,s51,52224
Ta 192,v1,36864
Ta 194,v1,38912
Mvav 56,184,216
Mvav 56,188,236
s_nop 0
Ta 196,v1,40960
Ta 198,v1,43008
Mvav 60,180,200
L_ v7,24,s55,0
Mvav 60,184,220
S_ s55,s71,655360
Ta 220,v1,45056
Ta 222,v1,47104
Mvav 60,188,240
S_ m0,s51,53248
Mvav 64,180,204
Mvav 64,184,224
L_ v7,24,s55,0
Mvav 64,188,244
S_ s55,s71,665600
Mvav 68,180,208
S_ m0,s51,54272
Mvav 68,184,228
Mvav 68,188,248
L_ v7,24,s55,0
Mvav 72,180,212
Mvav 72,184,232
Mvav 72,188,252
W_ 0
Mvav 92,220,252
s_nop 0
Ta 180,v2,36864
Ta 182,v2,38912
Mvav 92,196,232
Mvav 92,192,212
s_nop 0
Ta 184,v2,40960
Ta 186,v2,43008
Mvav 88,220,248
s_waitcnt vmcnt(23)
Mvav 88,196,228
s_nop 0
Ta 188,v2,45056
Ta 190,v2,47104
Mvav 88,192,208
v_cmp_eq_u32 vcc, s62, v11
Mvav 84,220,244
s_mul_i32 s76, s75, 204800
Mvav 84,196,224
s_bitcmp1_b32 vcc_lo, s75
s_cbranch_scc0 .Lspin2
.Lspun2:
Mvav 84,192,204
S_ s56,s76,s84
Mvav 80,220,240
buffer_load_dwordx4 a[20:23], v6, s[28:31], s56 offen offset:0 sc1
Mvav 80,196,220
Mvav 80,192,200
Mvav 76,220,236
Mvav 76,196,216
buffer_load_dwordx4 a[24:27], v6, s[28:31], s56 offen offset:1024 sc1
Mvav 76,192,196
W_ 0
Mvav 16,180,196
s_nop 0
Ta 192,v3,36864
Ta 194,v3,38912
Mvav 16,184,216
Mvav 16,188,236
s_nop 0
Ta 196,v3,40960
Ta 198,v3,43008
Mvav 20,180,200
buffer_load_dwordx4 a[28:31], v6, s[28:31], s56 offen offset:2048 sc1
Mvav 20,184,220
s_nop 0
Ta 220,v3,45056
Ta 222,v3,47104
Mvav 20,188,240
Mvav 24,180,204
Mvav 24,184,224
buffer_load_dwordx4 a[32:35], v6, s[28:31], s56 offen offset:3072 sc1
Mvav 24,188,244
S_ s56,s56,4096
Mvav 28,180,208
Mvav 28,184,228
Mvav 28,188,248
buffer_load_dwordx4 a[36:39], v6, s[28:31], s56 offen offset:0 sc1
Mvav 32,180,212
S_ s56,s76,s74
Mvav 32,184,232
Mvav 32,188,252
s_waitcnt vmcnt(30) lgkmcnt(0)
s_barrier
Mvav 52,220,252
S_ s55,s71,0
Ta 180,%0,24576
Ta 182,%0,26624
Mvav 52,196,232
buffer_load_dwordx4 a[60:63], v6, s[28:31], s56 offen offset:0 sc1
Mvav 52,192,212
S_ s56,s76,s82
Ta 184,%0,28672
Ta 186,%0,30720
Mvav 48,220,248
S_ m0,s51,36864
Mvav 48,196,228
s_nop 0
Ta 188,%0,32768
Ta 190,%0,34816
Mvav 48,192,208
buffer_load_dwordx4 a[64:67], v6, s[28:31], s56 offen offset:0 sc1
Mvav 44,220,244
Mvav 44,196,224
Mvav 44,192,204
Mvav 40,220,240
L_ v7,24,s55,0
Mvav 40,196,220
S_ s55,s71,10240
Mvav 40,192,200
S_ m0,s51,37888
Mvav 36,220,236
Mvav 36,196,216
L_ v7,24,s55,0
Mvav 36,192,196
W_ 0
S_ s55,s71,20480
Mvaa 56,180,0
S_ m0,s51,38912
Ta 192,v1,24576
Ta 194,v1,26624
Mvaa 56,184,40
Mvaa 56,188,80
s_nop 0
Ta 196,v1,28672
Ta 198,v1,30720
Mvaa 60,180,4
L_ v7,24,s55,0
Mvaa 60,184,44
S_ s55,s71,30720
Ta 220,v1,32768
Ta 222,v1,34816
Mvaa 60,188,84
S_ m0,s51,39936
Mvaa 64,180,8
Mvaa 64,184,48
L_ v7,24,s55,0
Mvaa 64,188,88
S_ s55,s71,40960
Mvaa 68,180,12
S_ m0,s51,40960
Mvaa 68,184,52
Mvaa 68,188,240
L_ v7,24,s55,0
Mvaa 72,180,16
S_ s55,s71,51200
Mvaa 72,184,56
S_ m0,s51,41984
Mvaa 72,188,244
W_ 0
Mvaa 92,220,244
s_nop 0
Ta 180,v2,24576
Ta 182,v2,26624
Mvaa 92,196,56
L_ v7,24,s55,0
Mvaa 92,192,16
s_nop 0
Ta 184,v2,28672
Ta 186,v2,30720
Mvaa 88,220,240
Mvaa 88,196,52
s_nop 0
Ta 188,v2,32768
Ta 190,v2,34816
Mvaa 88,192,12
Mvaa 84,220,88
Mvaa 84,196,48
Mvaa 84,192,8
Mvaa 80,220,84
Mvaa 80,196,44
Mvaa 80,192,4
Mvaa 76,220,80
Mvaa 76,196,40
Mvaa 76,192,0
W_ 0
Mvaa 16,180,0
s_nop 0
Ta 192,v3,24576
Ta 194,v3,26624
Mvaa 16,184,40
Mvaa 16,188,80
s_nop 0
Ta 196,v3,28672
Ta 198,v3,30720
Mvaa 20,180,4
Mvaa 20,184,44
s_nop 0
Ta 220,v3,32768
Ta 222,v3,34816
Mvaa 20,188,84
Mvaa 24,180,8
Mvaa 24,184,48
Mvaa 24,188,88
Mvaa 28,180,12
Mvaa 28,184,52
Mvaa 28,188,240
Mvaa 32,180,16
Mvaa 32,184,56
Mvaa 32,188,244
s_waitcnt vmcnt(30) lgkmcnt(0)
s_barrier
Mvaa 52,220,244
s_nop 0
Ta 180,%0,0
Ta 182,%0,2048
Mvaa 52,196,56
Mvaa 52,192,16
s_nop 0
Ta 184,%0,4096
Ta 186,%0,6144
Mvaa 48,220,240
Mvaa 48,196,52
s_nop 0
Ta 188,%0,8192
Ta 190,%0,10240
Mvaa 48,192,12
Mvaa 44,220,88
Mvaa 44,196,48
Mvaa 44,192,8
Mvaa 40,220,84
Mvaa 40,196,44
Mvaa 40,192,4
Mvaa 36,220,80
Mvaa 36,196,40
Mvaa 36,192,0
W_ 0
Mvaa 56,180,120
S_ s81,s6,6
Ta 192,v1,0
Ta 194,v1,2048
Mvaa 56,184,160
s_and_b32 s81, s81, 7
Mvaa 56,188,200
s_mul_i32 s81, s81, 20480
Ta 196,v1,4096
Ta 198,v1,6144
Mvaa 60,180,124
s_mov_b32 m0, s52
S_ s73,s81,s78
Mvaa 60,184,164
S_ s73,s73,4096
Ta 220,v1,8192
Ta 222,v1,10240
Mvaa 60,188,204
s_nop 0
Y_ 56,0
Y_ 57,256
Mvaa 64,180,128
s_nop 0
Y_ 58,512
Y_ 59,768
Mvaa 64,184,168
Mvaa 64,188,208
s_nop 0
Y_ 60,1024
Y_ 61,1280
Mvaa 68,180,132
s_nop 0
Y_ 62,1536
Y_ 63,1792
Mvaa 68,184,172
Mvaa 68,188,212
s_nop 0
Y_ 64,2048
Y_ 65,2304
Mvaa 72,180,136
s_nop 0
Y_ 66,2560
Y_ 67,2816
Mvaa 72,184,176
Mvaa 72,188,216
W_ 12
Y_ 68,3072
Y_ 69,3328
Mvaa 92,220,216
s_nop 0
Ta 180,v2,0
Ta 182,v2,2048
Mvaa 92,196,176
s_nop 0
Y_ 70,3584
Y_ 71,3840
Mvaa 92,192,136
s_nop 0
Ta 184,v2,4096
Ta 186,v2,6144
Mvaa 88,220,212
s_nop 0
Y_ 72,4096
Y_ 73,4352
Mvaa 88,196,172
S_ s83,s6,5
Ta 188,v2,8192
Ta 190,v2,10240
Mvaa 88,192,132
s_and_b32 s83, s83, 7
Y_ 74,4608
Y_ 75,4864
Mvaa 84,220,208
s_lshl_b32 s83, s83, 2
Y_ 92,9216
Y_ 93,9472
Mvaa 84,196,168
s_nop 0
Y_ 94,9728
Y_ 95,9984
Mvaa 84,192,128
s_nop 0
Y_ 88,8192
Y_ 89,8448
Mvaa 80,220,204
s_nop 0
Y_ 90,8704
Y_ 91,8960
Mvaa 80,196,164
v_cmp_eq_u32_e64 s[36:37], s83, v8
Mvaa 80,192,124
s_nop 0
Y_ 84,7168
Y_ 85,7424
Mvaa 76,220,200
s_nop 0
Y_ 86,7680
Y_ 87,7936
Mvaa 76,196,160
v_mov_b32 v10, 0x7fff0000
Mvaa 76,192,120
W_ 14
Y_ 80,6144
Y_ 81,6400
Mvaa 16,180,120
s_nop 0
Ta 192,v3,0
Ta 194,v3,2048
Mvaa 16,184,160
s_nop 0
Y_ 82,6656
Y_ 83,6912
Mvaa 16,188,200
s_nop 0
Ta 196,v3,4096
Ta 198,v3,6144
Mvaa 20,180,124
s_nop 0
Y_ 76,5120
Y_ 77,5376
Mvaa 20,184,164
s_nop 0
Ta 220,v3,8192
Ta 222,v3,10240
Mvaa 20,188,204
s_nop 0
Y_ 78,5632
Y_ 79,5888
Mvaa 24,180,128
v_cndmask_b32_e64 v10, v10, v9, s[36:37]
Mvaa 24,184,168
Mvaa 24,188,208
s_waitcnt vmcnt(19) lgkmcnt(0)
s_barrier
Mvaa 28,180,132
buffer_store_dword a255, v10, s[92:95], 0 offen offset:384 sc1
Mvaa 28,184,172
s_sub_u32 s75, s6, 4
ds_read2st64_b32 v[96:97], v5 offset0:0 offset1:1
S_ s55,s71,122880
Mvaa 28,188,212
s_and_b32 s75, s75, 7
ds_read2st64_b32 v[98:99], v5 offset0:2 offset1:3
S_ m0,s51,24576
Mvaa 32,180,136
ds_read2st64_b32 v[104:105], v5 offset0:4 offset1:5
Mvaa 32,184,176
buffer_load_dword v11, v8, s[92:95], s96 offen offset:384 sc1
Mvaa 32,188,216
ds_read2st64_b32 v[106:107], v5 offset0:6 offset1:7
Mvaa 52,220,216
s_nop 0
Ta 180,%0,12288
Ta 182,%0,14336
Mvaa 52,196,176
ds_read2st64_b32 v[112:113], v5 offset0:8 offset1:9
Mvaa 52,192,136
W_ 5
Ta 184,%0,16384
Ta 186,%0,18432
Mvaa 48,220,212
L_ v7,24,s55,0
Mvaa 48,196,172
S_ s55,s71,133120
Ta 188,%0,20480
Ta 190,%0,22528
Mvaa 48,192,132
S_ m0,s51,25600
ds_read2st64_b32 v[114:115], v5 offset0:10 offset1:11
A_ v96,v16,v96
Mvaa 44,220,208
ds_read2st64_b32 v[120:121], v5 offset0:12 offset1:13
A_ v97,v17,v97
Mvaa 44,196,168
L_ v7,24,s55,0
Mvaa 44,192,128
S_ s55,s71,143360
ds_read2st64_b32 v[122:123], v5 offset0:14 offset1:15
A_ v98,v18,v98
Mvaa 40,220,204
S_ m0,s51,26624
ds_read2st64_b32 v[128:129], v5 offset0:16 offset1:17
A_ v99,v19,v99
Mvaa 40,196,164
W_ 11
ds_read2st64_b32 v[130:131], v5 offset0:18 offset1:19
A_ v104,v20,v104
Mvaa 40,192,124
L_ v7,24,s55,0
Mvaa 36,220,200
S_ s55,s71,153600
ds_read2st64_b32 v[132:133], v5 offset0:36 offset1:37
A_ v105,v21,v105
Mvaa 36,196,160
S_ m0,s51,27648
ds_read2st64_b32 v[134:135], v5 offset0:38 offset1:39
A_ v106,v22,v106
Mvaa 36,192,120
ds_read2st64_b32 v[124:125], v5 offset0:32 offset1:33
A_ v107,v23,v107
s_waitcnt vmcnt(11)
Maaa 100,70,20
L_ v7,24,s55,0
Maaa 140,68,20
W_ 7
ds_read2st64_b32 v[126:127], v5 offset0:34 offset1:35
A_ v112,v24,v112
Maaa 104,70,24
S_ s55,s71,163840
ds_read2st64_b32 v[116:117], v5 offset0:28 offset1:29
A_ v113,v25,v113
Maaa 144,68,24
S_ m0,s51,28672
ds_read2st64_b32 v[118:119], v5 offset0:30 offset1:31
A_ v114,v26,v114
Maaa 108,70,28
L_ v7,24,s55,0
Maaa 148,68,28
S_ s55,s71,174080
ds_read2st64_b32 v[108:109], v5 offset0:24 offset1:25
A_ v115,v27,v115
Maaa 112,70,32
W_ 9
ds_read2st64_b32 v[110:111], v5 offset0:26 offset1:27
A_ v120,v28,v120
Maaa 152,68,32
S_ m0,s51,29696
ds_read2st64_b32 v[100:101], v5 offset0:20 offset1:21
A_ v121,v29,v121
Maaa 116,70,36
L_ v7,24,s55,0
Maaa 156,68,36
S_ s79,s6,8
ds_read2st64_b32 v[102:103], v5 offset0:22 offset1:23
A_ v122,v30,v122
Maaa 224,70,60
s_and_b32 s79, s79, 7
A_ v123,v31,v123
Maaa 228,68,60
W_ 10
A_ v128,v32,v128
A_ v129,v33,v129
Maaa 232,70,64
s_lshl_b32 s79, s79, 7
A_ v130,v34,v130
A_ v131,v35,v131
Maaa 236,68,64
W_ 9
A_ v132,v52,v132
W_ 8
Zvav 56,180,136
S_ s71,s72,s79
Ta 192,v1,12288
Ta 194,v1,14336
Mvav 56,184,156
S_ s55,s71,491520
A_ v133,v53,v133
A_ v134,v54,v134
Mvav 56,188,176
S_ m0,s51,0
Ta 196,v1,16384
Ta 198,v1,18432
Zvav 60,180,140
L_ v7,24,s55,0
Mvav 60,184,160
S_ s55,s71,501760
Ta 220,v1,20480
Ta 222,v1,22528
Mvav 60,188,180
S_ m0,s51,1024
A_ v135,v55,v135
Zvav 64,180,144
W_ 12
A_ v124,v48,v124
A_ v125,v49,v125
Mvav 64,184,164
L_ v7,24,s55,0
Mvav 64,188,184
S_ s55,s71,512000
A_ v126,v50,v126
A_ v127,v51,v127
Zvav 68,180,148
W_ 10
A_ v116,v44,v116
A_ v117,v45,v117
Mvav 68,184,168
S_ m0,s51,2048
A_ v118,v46,v118
A_ v119,v47,v119
Mvav 68,188,188
L_ v7,24,s55,0
Zvav 72,180,152
W_ 8
A_ v108,v40,v108
A_ v109,v41,v109
Mvav 72,184,172
S_ s55,s71,522240
A_ v110,v42,v110
A_ v111,v43,v111
Mvav 72,188,192
W_ 0
A_ v100,v36,v100
Zvav 92,220,192
S_ m0,s51,3072
Ta 180,v2,12288
Ta 182,v2,14336
Mvav 92,196,172
L_ v7,24,s55,0
Mvav 92,192,152
S_ s55,s71,532480
Ta 184,v2,16384
Ta 186,v2,18432
Zvav 88,220,188
S_ m0,s51,4096
A_ v101,v37,v101
A_ v102,v38,v102
Mvav 88,196,168
s_nop 0
Ta 188,v2,20480
Ta 190,v2,22528
Mvav 88,192,148
L_ v7,24,s55,0
Zvav 84,220,184
S_ s55,s71,542720
A_ v103,v39,v103
C_ 131,130,131
Mvav 84,196,164
S_ m0,s51,5120
C_ 130,128,129
C_ 132,132,133
Mvav 84,192,144
C_ 133,134,135
Zvav 80,220,180
buffer_store_dwordx4 v[130:133], v6, s[28:31], s73 offen offset:0 sc1
Mvav 80,196,160
s_sub_u32 s73, s73, 4096
Mvav 80,192,140
C_ 123,122,123
C_ 122,120,121
Zvav 76,220,176
C_ 124,124,125
C_ 125,126,127
Mvav 76,196,156
buffer_store_dwordx4 v[122:125], v6, s[28:31], s73 offen offset:3072 sc1
Mvav 76,192,136
W_ 0
C_ 115,114,115
C_ 114,112,113
Zvav 16,180,136
s_nop 0
Ta 192,v3,12288
Ta 194,v3,14336
Mvav 16,184,156
C_ 116,116,117
C_ 117,118,119
Mvav 16,188,176
s_nop 0
Ta 196,v3,16384
Ta 198,v3,18432
Zvav 20,180,140
buffer_store_dwordx4 v[114:117], v6, s[28:31], s73 offen offset:2048 sc1
Mvav 20,184,160
s_nop 0
Ta 220,v3,20480
Ta 222,v3,22528
Mvav 20,188,180
C_ 107,106,107
C_ 106,104,105
Zvav 24,180,144
C_ 108,108,109
C_ 109,110,111
Mvav 24,184,164
buffer_store_dwordx4 v[106:109], v6, s[28:31], s73 offen offset:1024 sc1
Mvav 24,188,184
C_ 99,98,99
C_ 98,96,97
Zvav 28,180,148
C_ 100,100,101
C_ 101,102,103
Mvav 28,184,168
Mvav 28,188,188
buffer_store_dwordx4 v[98:101], v6, s[28:31], s73 offen offset:0 sc1
Zvav 32,180,152
Mvav 32,184,172
Mvav 32,188,192
s_waitcnt vmcnt(31) lgkmcnt(0)
s_barrier
Zvav 52,220,192
s_nop 0
Ta 180,%0,49152
Ta 182,%0,51200
Mvav 52,196,172
L_ v7,24,s55,0
Mvav 52,192,152
S_ s55,s71,614400
Ta 184,%0,53248
Ta 186,%0,55296
Zvav 48,220,188
S_ m0,s51,12288
Mvav 48,196,168
s_nop 0
Ta 188,%0,57344
Ta 190,%0,59392
Mvav 48,192,148
L_ v7,24,s55,0
Zvav 44,220,184
S_ s55,s71,624640
Mvav 44,196,164
S_ m0,s51,13312
Mvav 44,192,144
Zvav 40,220,180
L_ v7,24,s55,0
Mvav 40,196,160
S_ s55,s71,634880
Mvav 40,192,140
S_ m0,s51,14336
Zvav 36,220,176
Mvav 36,196,156
L_ v7,24,s55,0
Mvav 36,192,136
W_ 0
S_ s55,s71,645120
Mvav 56,180,196
S_ m0,s51,15360
Ta 192,v1,49152
Ta 194,v1,51200
Mvav 56,184,216
Mvav 56,188,236
s_nop 0
Ta 196,v1,53248
Ta 198,v1,55296
Mvav 60,180,200
L_ v7,24,s55,0
Mvav 60,184,220
S_ s55,s71,655360
Ta 220,v1,57344
Ta 222,v1,59392
Mvav 60,188,240
S_ m0,s51,16384
Mvav 64,180,204
Mvav 64,184,224
L_ v7,24,s55,0
Mvav 64,188,244
S_ s55,s71,665600
Mvav 68,180,208
S_ m0,s51,17408
Mvav 68,184,228
Mvav 68,188,248
L_ v7,24,s55,0
Mvav 72,180,212
Mvav 72,184,232
Mvav 72,188,252
W_ 0
Mvav 92,220,252
s_nop 0
Ta 180,v2,49152
Ta 182,v2,51200
Mvav 92,196,232
Mvav 92,192,212
s_nop 0
Ta 184,v2,53248
Ta 186,v2,55296
Mvav 88,220,248
s_waitcnt vmcnt(23)
Mvav 88,196,228
s_nop 0
Ta 188,v2,57344
Ta 190,v2,59392
Mvav 88,192,208
v_cmp_eq_u32 vcc, s62, v11
Mvav 84,220,244
s_mul_i32 s76, s75, 204800
Mvav 84,196,224
s_bitcmp1_b32 vcc_lo, s75
s_cbranch_scc0 .Lspin3
.Lspun3:
Mvav 84,192,204
S_ s56,s76,s84
Mvav 80,220,240
buffer_load_dwordx4 a[20:23], v6, s[28:31], s56 offen offset:0 sc1
Mvav 80,196,220
Mvav 80,192,200
Mvav 76,220,236
Mvav 76,196,216
buffer_load_dwordx4 a[24:27], v6, s[28:31], s56 offen offset:1024 sc1
Mvav 76,192,196
W_ 0
Mvav 16,180,196
s_nop 0
Ta 192,v3,49152
Ta 194,v3,51200
Mvav 16,184,216
Mvav 16,188,236
s_nop 0
Ta 196,v3,53248
Ta 198,v3,55296
Mvav 20,180,200
buffer_load_dwordx4 a[28:31], v6, s[28:31], s56 offen offset:2048 sc1
Mvav 20,184,220
s_nop 0
Ta 220,v3,57344
Ta 222,v3,59392
Mvav 20,188,240
Mvav 24,180,204
Mvav 24,184,224
buffer_load_dwordx4 a[32:35], v6, s[28:31], s56 offen offset:3072 sc1
Mvav 24,188,244
S_ s56,s56,4096
Mvav 28,180,208
Mvav 28,184,228
Mvav 28,188,248
buffer_load_dwordx4 a[36:39], v6, s[28:31], s56 offen offset:0 sc1
Mvav 32,180,212
S_ s56,s76,s74
Mvav 32,184,232
Mvav 32,188,252
s_waitcnt vmcnt(30) lgkmcnt(0)
s_barrier
Mvav 52,220,252
S_ s55,s71,0
Ta 180,%0,36864
Ta 182,%0,38912
Mvav 52,196,232
buffer_load_dwordx4 a[60:63], v6, s[28:31], s56 offen offset:0 sc1
Mvav 52,192,212
S_ s56,s76,s82
Ta 184,%0,40960
Ta 186,%0,43008
Mvav 48,220,248
S_ m0,s51,49152
Mvav 48,196,228
s_nop 0
Ta 188,%0,45056
Ta 190,%0,47104
Mvav 48,192,208
buffer_load_dwordx4 a[64:67], v6, s[28:31], s56 offen offset:0 sc1
Mvav 44,220,244
s_sub_u32 s75, s6, 5
Mvav 44,196,224
s_and_b32 s75, s75, 7
Mvav 44,192,204
Mvav 40,220,240
buffer_load_dword v11, v8, s[92:95], s96 offen offset:384 sc1
Mvav 40,196,220
Mvav 40,192,200
Mvav 36,220,236
Mvav 36,196,216
L_ v7,24,s55,0
Mvav 36,192,196
W_ 0
S_ s55,s71,10240
Mvaa 56,180,0
S_ m0,s51,50176
Ta 192,v1,36864
Ta 194,v1,38912
Mvaa 56,184,40
Mvaa 56,188,80
s_nop 0
Ta 196,v1,40960
Ta 198,v1,43008
Mvaa 60,180,4
L_ v7,24,s55,0
Mvaa 60,184,44
S_ s55,s71,20480
Ta 220,v1,45056
Ta 222,v1,47104
Mvaa 60,188,84
S_ m0,s51,51200
Mvaa 64,180,8
Mvaa 64,184,48
L_ v7,24,s55,0
Mvaa 64,188,88
S_ s55,s71,30720
Mvaa 68,180,12
S_ m0,s51,52224
Mvaa 68,184,52
Mvaa 68,188,240
L_ v7,24,s55,0
Mvaa 72,180,16
S_ s55,s71,40960
Mvaa 72,184,56
S_ m0,s51,53248
Mvaa 72,188,244
W_ 0
Mvaa 92,220,244
s_nop 0
Ta 180,v2,36864
Ta 182,v2,38912
Mvaa 92,196,56
L_ v7,24,s55,0
Mvaa 92,192,16
S_ s55,s71,51200
Ta 184,v2,40960
Ta 186,v2,43008
Mvaa 88,220,240
S_ m0,s51,54272
Mvaa 88,196,52
s_nop 0
Ta 188,v2,45056
Ta 190,v2,47104
Mvaa 88,192,12
L_ v7,24,s55,0
Mvaa 84,220,88
Mvaa 84,196,48
Mvaa 84,192,8
Mvaa 80,220,84
Mvaa 80,196,44
S_ s83,s6,6
Mvaa 80,192,4
s_and_b32 s83, s83, 7
Mvaa 76,220,80
s_lshl_b32 s83, s83, 2
Mvaa 76,196,40
v_cmp_eq_u32_e64 s[36:37], s83, v8
Mvaa 76,192,0
W_ 0
v_mov_b32 v10, 0x7fff0000
Mvaa 16,180,0
s_nop 0
Ta 192,v3,36864
Ta 194,v3,38912
Mvaa 16,184,40
v_cndmask_b32_e64 v10, v10, v9, s[36:37]
Mvaa 16,188,80
s_nop 0
Ta 196,v3,40960
Ta 198,v3,43008
Mvaa 20,180,4
Mvaa 20,184,44
s_nop 0
Ta 220,v3,45056
Ta 222,v3,47104
Mvaa 20,188,84
Mvaa 24,180,8
Mvaa 24,184,48
Mvaa 24,188,88
Mvaa 28,180,12
Mvaa 28,184,52
Mvaa 28,188,240
Mvaa 32,180,16
Mvaa 32,184,56
Mvaa 32,188,244
s_waitcnt vmcnt(21) lgkmcnt(0)
s_barrier
Mvaa 52,220,244
s_nop 0
Ta 180,%0,24576
Ta 182,%0,26624
Mvaa 52,196,56
buffer_store_dword a255, v10, s[92:95], 0 offen offset:384 sc1
Mvaa 52,192,16
s_nop 0
Ta 184,%0,28672
Ta 186,%0,30720
Mvaa 48,220,240
Mvaa 48,196,52
s_nop 0
Ta 188,%0,32768
Ta 190,%0,34816
Mvaa 48,192,12
Mvaa 44,220,88
Mvaa 44,196,48
Mvaa 44,192,8
Mvaa 40,220,84
Mvaa 40,196,44
Mvaa 40,192,4
Mvaa 36,220,80
Mvaa 36,196,40
Mvaa 36,192,0
W_ 0
Mvaa 56,180,120
S_ s81,s6,7
Ta 192,v1,24576
Ta 194,v1,26624
Mvaa 56,184,160
s_and_b32 s81, s81, 7
Mvaa 56,188,200
s_mul_i32 s81, s81, 20480
Ta 196,v1,28672
Ta 198,v1,30720
Mvaa 60,180,124
s_mov_b32 m0, s52
S_ s73,s81,s78
Mvaa 60,184,164
S_ s73,s73,4096
Ta 220,v1,32768
Ta 222,v1,34816
Mvaa 60,188,204
s_nop 0
Y_ 56,0
Y_ 57,256
Mvaa 64,180,128
s_nop 0
Y_ 58,512
Y_ 59,768
Mvaa 64,184,168
Mvaa 64,188,208
s_nop 0
Y_ 60,1024
Y_ 61,1280
Mvaa 68,180,132
s_nop 0
Y_ 62,1536
Y_ 63,1792
Mvaa 68,184,172
Mvaa 68,188,212
s_nop 0
Y_ 64,2048
Y_ 65,2304
Mvaa 72,180,136
s_nop 0
Y_ 66,2560
Y_ 67,2816
Mvaa 72,184,176
Mvaa 72,188,216
W_ 12
Y_ 68,3072
Y_ 69,3328
Mvaa 92,220,216
s_nop 0
Ta 180,v2,24576
Ta 182,v2,26624
Mvaa 92,196,176
s_nop 0
Y_ 70,3584
Y_ 71,3840
Mvaa 92,192,136
s_nop 0
Ta 184,v2,28672
Ta 186,v2,30720
Mvaa 88,220,212
s_nop 0
Y_ 72,4096
Y_ 73,4352
Mvaa 88,196,172
s_nop 0
Ta 188,v2,32768
Ta 190,v2,34816
Mvaa 88,192,132
s_nop 0
Y_ 74,4608
Y_ 75,4864
Mvaa 84,220,208
s_nop 0
Y_ 92,9216
Y_ 93,9472
Mvaa 84,196,168
s_nop 0
Y_ 94,9728
Y_ 95,9984
Mvaa 84,192,128
s_nop 0
Y_ 88,8192
Y_ 89,8448
Mvaa 80,220,204
s_nop 0
Y_ 90,8704
Y_ 91,8960
Mvaa 80,196,164
Mvaa 80,192,124
s_nop 0
Y_ 84,7168
Y_ 85,7424
Mvaa 76,220,200
s_nop 0
Y_ 86,7680
Y_ 87,7936
Mvaa 76,196,160
Mvaa 76,192,120
W_ 14
Y_ 80,6144
Y_ 81,6400
Mvaa 16,180,120
s_nop 0
Ta 192,v3,24576
Ta 194,v3,26624
Mvaa 16,184,160
s_nop 0
Y_ 82,6656
Y_ 83,6912
Mvaa 16,188,200
s_nop 0
Ta 196,v3,28672
Ta 198,v3,30720
Mvaa 20,180,124
s_nop 0
Y_ 76,5120
Y_ 77,5376
Mvaa 20,184,164
s_nop 0
Ta 220,v3,32768
Ta 222,v3,34816
Mvaa 20,188,204
s_nop 0
Y_ 78,5632
Y_ 79,5888
Mvaa 24,180,128
Mvaa 24,184,168
Mvaa 24,188,208
s_waitcnt vmcnt(21) lgkmcnt(0)
s_barrier
Mvaa 28,180,132
S_ s55,s71,122880
ds_read2st64_b32 v[96:97], v5 offset0:0 offset1:1
Mvaa 28,184,172
S_ m0,s51,36864
ds_read2st64_b32 v[98:99], v5 offset0:2 offset1:3
Mvaa 28,188,212
L_ v7,24,s55,0
Mvaa 32,180,136
S_ s55,s71,133120
ds_read2st64_b32 v[104:105], v5 offset0:4 offset1:5
Mvaa 32,184,176
s_waitcnt vmcnt(8)
ds_read2st64_b32 v[106:107], v5 offset0:6 offset1:7
S_ m0,s51,37888
Mvaa 32,188,216
ds_read2st64_b32 v[112:113], v5 offset0:8 offset1:9
v_cmp_eq_u32 vcc, s62, v11
Mvaa 52,220,216
s_mul_i32 s76, s75, 204800
Ta 180,%0,0
Ta 182,%0,2048
Mvaa 52,196,176
L_ v7,24,s55,0
Mvaa 52,192,136
W_ 5
Ta 184,%0,4096
Ta 186,%0,6144
Mvaa 48,220,212
s_bitcmp1_b32 vcc_lo, s75
s_cbranch_scc0 .Lspin4
.Lspun4:
ds_read2st64_b32 v[114:115], v5 offset0:10 offset1:11
Mvaa 48,196,172
S_ s55,s71,143360
Ta 188,%0,8192
Ta 190,%0,10240
Mvaa 48,192,132
S_ m0,s51,38912
ds_read2st64_b32 v[120:121], v5 offset0:12 offset1:13
A_ v96,v16,v96
Mvaa 44,220,208
L_ v7,24,s55,0
Mvaa 44,196,168
S_ s55,s71,153600
ds_read2st64_b32 v[122:123], v5 offset0:14 offset1:15
A_ v97,v17,v97
Mvaa 44,192,128
S_ m0,s51,39936
ds_read2st64_b32 v[128:129], v5 offset0:16 offset1:17
A_ v98,v18,v98
Mvaa 40,220,204
ds_read2st64_b32 v[130:131], v5 offset0:18 offset1:19
A_ v99,v19,v99
Mvaa 40,196,164
L_ v7,24,s55,0
Mvaa 40,192,124
W_ 12
ds_read2st64_b32 v[132:133], v5 offset0:36 offset1:37
A_ v104,v20,v104
Mvaa 36,220,200
S_ s55,s71,163840
ds_read2st64_b32 v[134:135], v5 offset0:38 offset1:39
A_ v105,v21,v105
Mvaa 36,196,160
S_ m0,s51,40960
ds_read2st64_b32 v[124:125], v5 offset0:32 offset1:33
A_ v106,v22,v106
Mvaa 36,192,120
L_ v7,24,s55,0
s_waitcnt vmcnt(13)
Maaa 100,70,20
S_ s55,s71,174080
ds_read2st64_b32 v[126:127], v5 offset0:34 offset1:35
A_ v107,v23,v107
Maaa 140,68,20
W_ 10
ds_read2st64_b32 v[116:117], v5 offset0:28 offset1:29
A_ v112,v24,v112
Maaa 104,70,24
S_ m0,s51,41984
ds_read2st64_b32 v[118:119], v5 offset0:30 offset1:31
A_ v113,v25,v113
Maaa 144,68,24
L_ v7,24,s55,0
Maaa 108,70,28
ds_read2st64_b32 v[108:109], v5 offset0:24 offset1:25
A_ v114,v26,v114
Maaa 148,68,28
ds_read2st64_b32 v[110:111], v5 offset0:26 offset1:27
A_ v115,v27,v115
Maaa 112,70,32
W_ 10
ds_read2st64_b32 v[100:101], v5 offset0:20 offset1:21
A_ v120,v28,v120
Maaa 152,68,32
ds_read2st64_b32 v[102:103], v5 offset0:22 offset1:23
A_ v121,v29,v121
Maaa 116,70,36
A_ v122,v30,v122
A_ v123,v31,v123
Maaa 156,68,36
W_ 10
A_ v128,v32,v128
A_ v129,v33,v129
Maaa 224,70,60
A_ v130,v34,v130
A_ v131,v35,v131
Maaa 228,68,60
W_ 8
A_ v132,v52,v132
A_ v133,v53,v133
Maaa 232,70,64
A_ v134,v54,v134
A_ v135,v55,v135
Maaa 236,68,64
W_ 7
A_ v124,v48,v124
W_ 6
Zvav 56,180,136
s_nop 0
Ta 192,v1,0
Ta 194,v1,2048
Mvav 56,184,156
A_ v125,v49,v125
A_ v126,v50,v126
Mvav 56,188,176
S_ s56,s76,s84
Ta 196,v1,4096
Ta 198,v1,6144
Zvav 60,180,140
buffer_load_dwordx4 a[20:23], v6, s[28:31], s56 offen offset:0 sc1
Mvav 60,184,160
s_nop 0
Ta 220,v1,8192
Ta 222,v1,10240
Mvav 60,188,180
A_ v127,v51,v127
C_ 131,130,131
Zvav 64,180,144
W_ 10
A_ v116,v44,v116
A_ v117,v45,v117
Mvav 64,184,164
buffer_load_dwordx4 a[24:27], v6, s[28:31], s56 offen offset:1024 sc1
Mvav 64,188,184
A_ v118,v46,v118
A_ v119,v47,v119
Zvav 68,180,148
W_ 8
A_ v108,v40,v108
A_ v109,v41,v109
Mvav 68,184,168
A_ v110,v42,v110
A_ v111,v43,v111
Mvav 68,188,188
buffer_load_dwordx4 a[28:31], v6, s[28:31], s56 offen offset:2048 sc1
Zvav 72,180,152
W_ 6
A_ v100,v36,v100
A_ v101,v37,v101
Mvav 72,184,172
A_ v102,v38,v102
A_ v103,v39,v103
Mvav 72,188,192
W_ 0
C_ 130,128,129
C_ 132,132,133
Zvav 92,220,192
s_nop 0
Ta 180,v2,0
Ta 182,v2,2048
Mvav 92,196,172
buffer_load_dwordx4 a[32:35], v6, s[28:31], s56 offen offset:3072 sc1
Mvav 92,192,152
S_ s56,s56,4096
Ta 184,v2,4096
Ta 186,v2,6144
Zvav 88,220,188
C_ 133,134,135
Mvav 88,196,168
s_nop 0
Ta 188,v2,8192
Ta 190,v2,10240
Mvav 88,192,148
buffer_load_dwordx4 a[36:39], v6, s[28:31], s56 offen offset:0 sc1
Zvav 84,220,184
S_ s56,s76,s74
Mvav 84,196,164
Mvav 84,192,144
Zvav 80,220,180
buffer_load_dwordx4 a[60:63], v6, s[28:31], s56 offen offset:0 sc1
Mvav 80,196,160
S_ s56,s76,s82
Mvav 80,192,140
Zvav 76,220,176
Mvav 76,196,156
buffer_load_dwordx4 a[64:67], v6, s[28:31], s56 offen offset:0 sc1
Mvav 76,192,136
W_ 0
s_sub_u32 s75, s6, 6
Maav 100,180,136
s_and_b32 s75, s75, 7
Ta 192,v3,0
Ta 194,v3,2048
Maav 100,184,156
Maav 100,188,176
s_nop 0
Ta 196,v3,4096
Ta 198,v3,6144
Maav 104,180,140
buffer_load_dword v11, v8, s[92:95], s96 offen offset:384 sc1
Maav 104,184,160
s_nop 0
Ta 220,v3,8192
Ta 222,v3,10240
Maav 104,188,180
Maav 108,180,144
Maav 108,184,164
buffer_store_dwordx4 v[130:133], v6, s[28:31], s73 offen offset:0 sc1
Maav 108,188,184
s_sub_u32 s73, s73, 4096
Maav 112,180,148
C_ 123,122,123
C_ 122,120,121
Maav 112,184,168
C_ 124,124,125
C_ 125,126,127
Maav 112,188,188
buffer_store_dwordx4 v[122:125], v6, s[28:31], s73 offen offset:3072 sc1
Maav 116,180,152
C_ 115,114,115
C_ 114,112,113
Maav 116,184,172
C_ 116,116,117
C_ 117,118,119
Maav 116,188,192
s_waitcnt vmcnt(31) lgkmcnt(0)
s_barrier
Maav 156,220,192
s_nop 0
Ta 180,%0,12288
Ta 182,%0,14336
Maav 156,196,172
buffer_store_dwordx4 v[114:117], v6, s[28:31], s73 offen offset:2048 sc1
Maav 156,192,152
s_nop 0
Ta 184,%0,16384
Ta 186,%0,18432
Maav 152,220,188
C_ 107,106,107
C_ 106,104,105
Maav 152,196,168
s_nop 0
Ta 188,%0,20480
Ta 190,%0,22528
Maav 152,192,148
C_ 108,108,109
C_ 109,110,111
Maav 148,220,184
buffer_store_dwordx4 v[106:109], v6, s[28:31], s73 offen offset:1024 sc1
Maav 148,196,164
C_ 99,98,99
C_ 98,96,97
Maav 148,192,144
C_ 100,100,101
C_ 101,102,103
Maav 144,220,180
Maav 144,196,160
buffer_store_dwordx4 v[98:101], v6, s[28:31], s73 offen offset:0 sc1
Maav 144,192,140
Maav 140,220,176
Maav 140,196,156
Maav 140,192,136
W_ 0
Mvav 56,180,196
s_nop 0
Ta 192,v1,12288
Ta 194,v1,14336
Mvav 56,184,216
Mvav 56,188,236
s_nop 0
Ta 196,v1,16384
Ta 198,v1,18432
Mvav 60,180,200
Mvav 60,184,220
s_nop 0
Ta 220,v1,20480
Ta 222,v1,22528
Mvav 60,188,240
v_bfe_u32 v4, v6, 4, 4
Mvav 64,180,204
v_mul_u32_u24 v4, 2560, v4
Mvav 64,184,224
v_bfe_u32 v10, v6, 8, 1
Mvav 64,188,244
v_lshl_add_u32 v4, v10, 5, v4
Mvav 68,180,208
v_lshrrev_b32 v10, 9, v6
Mvav 68,184,228
v_lshl_add_u32 v4, v10, 4, v4
Mvav 68,188,248
Mvav 72,180,212
Mvav 72,184,232
Mvav 72,188,252
W_ 0
Mvav 92,220,252
s_nop 0
Ta 180,v2,12288
Ta 182,v2,14336
Mvav 92,196,232
Mvav 92,192,212
s_nop 0
Ta 184,v2,16384
Ta 186,v2,18432
Mvav 88,220,248
Mvav 88,196,228
s_nop 0
Ta 188,v2,20480
Ta 190,v2,22528
Mvav 88,192,208
Mvav 84,220,244
Mvav 84,196,224
Mvav 84,192,204
Mvav 80,220,240
Mvav 80,196,220
Mvav 80,192,200
Mvav 76,220,236
Mvav 76,196,216
Mvav 76,192,196
W_ 0
Maav 100,180,196
s_nop 0
Ta 192,v3,12288
Ta 194,v3,14336
Maav 100,184,216
Maav 100,188,236
s_nop 0
Ta 196,v3,16384
Ta 198,v3,18432
Maav 104,180,200
Maav 104,184,220
s_nop 0
Ta 220,v3,20480
Ta 222,v3,22528
Maav 104,188,240
Maav 108,180,204
S_ s86,s6,1
Maav 108,184,224
s_and_b32 s86, s86, 7
Maav 108,188,244
s_waitcnt vmcnt(5)
Maav 112,180,208
v_cmp_eq_u32 vcc, s62, v11
Maav 112,184,228
s_mul_i32 s76, s75, 204800
Maav 112,188,248
s_bitcmp1_b32 vcc_lo, s75
s_cbranch_scc0 .Lspin5
.Lspun5:
Maav 116,180,212
s_bitcmp1_b32 vcc_lo, s86
s_cbranch_scc0 .Lspin6
.Lspun6:
Maav 116,184,232
S_ s56,s76,s84
Maav 116,188,252
s_waitcnt vmcnt(20) lgkmcnt(0)
s_barrier
Maav 156,220,252
s_nop 0
Ta 180,%0,49152
Ta 182,%0,51200
Maav 156,196,232
buffer_load_dwordx4 v[16:19], v6, s[28:31], s56 offen offset:0 sc1
Maav 156,192,212
s_nop 0
Ta 184,%0,53248
Ta 186,%0,55296
Maav 152,220,248
Maav 152,196,228
s_nop 0
Ta 188,%0,57344
Ta 190,%0,59392
Maav 152,192,208
buffer_load_dwordx4 v[20:23], v6, s[28:31], s56 offen offset:1024 sc1
Maav 148,220,244
Maav 148,196,224
Maav 148,192,204
Maav 144,220,240
buffer_load_dwordx4 v[24:27], v6, s[28:31], s56 offen offset:2048 sc1
Maav 144,196,220
Maav 144,192,200
Maav 140,220,236
Maav 140,196,216
buffer_load_dwordx4 v[28:31], v6, s[28:31], s56 offen offset:3072 sc1
Maav 140,192,196
W_ 0
S_ s56,s56,4096
Mvaa 56,180,0
s_nop 0
Ta 192,v1,49152
Ta 194,v1,51200
Mvaa 56,184,40
R_ v164,a224
R_ v165,a225
Mvaa 56,188,80
s_nop 0
Ta 196,v1,53248
Ta 198,v1,55296
Mvaa 60,180,4
buffer_load_dwordx4 v[32:35], v6, s[28:31], s56 offen offset:0 sc1
Mvaa 60,184,44
S_ s56,s76,s74
Ta 220,v1,57344
Ta 222,v1,59392
Mvaa 60,188,84
R_ v166,a226
R_ v167,a227
Mvaa 64,180,8
R_ v168,a228
R_ v169,a229
Mvaa 64,184,48
buffer_load_dwordx4 v[36:39], v6, s[28:31], s56 offen offset:0 sc1
Mvaa 64,188,88
S_ s56,s76,s82
R_ v170,a230
R_ v171,a231
Mvaa 68,180,12
R_ v172,a232
R_ v173,a233
Mvaa 68,184,52
R_ v174,a234
R_ v175,a235
Mvaa 68,188,240
buffer_load_dwordx4 v[40:43], v6, s[28:31], s56 offen offset:0 sc1
Mvaa 72,180,16
s_mul_i32 s76, s86, 204800
R_ v176,a236
R_ v177,a237
Mvaa 72,184,56
S_ s80,s76,s84
R_ v178,a238
R_ v179,a239
Mvaa 72,188,244
W_ 0
S_ s56,s76,s74
Mvaa 92,220,244
s_nop 0
Ta 180,v2,49152
Ta 182,v2,51200
Mvaa 92,196,56
buffer_load_dwordx4 v[156:159], v6, s[28:31], s56 offen offset:0 sc1
Mvaa 92,192,16
S_ s56,s76,s82
Ta 184,v2,53248
Ta 186,v2,55296
Mvaa 88,220,240
Mvaa 88,196,52
s_nop 0
Ta 188,v2,57344
Ta 190,v2,59392
Mvaa 88,192,12
buffer_load_dwordx4 v[160:163], v6, s[28:31], s56 offen offset:0 sc1
Mvaa 84,220,88
Mvaa 84,196,48
Mvaa 84,192,8
Mvaa 80,220,84
Mvaa 80,196,44
Mvaa 80,192,4
Mvaa 76,220,80
Mvaa 76,196,40
Mvaa 76,192,0
W_ 0
Maaa 100,180,0
s_nop 0
Ta 192,v3,49152
Ta 194,v3,51200
Maaa 100,184,40
Maaa 100,188,80
s_nop 0
Ta 196,v3,53248
Ta 198,v3,55296
Maaa 104,180,4
Maaa 104,184,44
s_nop 0
Ta 220,v3,57344
Ta 222,v3,59392
Maaa 104,188,84
Maaa 108,180,8
R_ v180,a100
R_ v181,a101
Maaa 108,184,48
R_ v182,a102
R_ v183,a103
Maaa 108,188,88
Maaa 112,180,12
R_ v184,a104
R_ v185,a105
Maaa 112,184,52
R_ v186,a106
R_ v187,a107
Maaa 112,188,240
Maaa 116,180,16
R_ v188,a108
R_ v189,a109
Maaa 116,184,56
R_ v190,a110
R_ v191,a111
Maaa 116,188,244
s_waitcnt vmcnt(22) lgkmcnt(0)
s_barrier
Maaa 156,220,244
s_nop 0
Ta 180,%0,36864
Ta 182,%0,38912
Maaa 156,196,56
R_ v192,a112
R_ v193,a113
Maaa 156,192,16
s_nop 0
Ta 184,%0,40960
Ta 186,%0,43008
Maaa 152,220,240
R_ v194,a114
R_ v195,a115
Maaa 152,196,52
s_nop 0
Ta 188,%0,45056
Ta 190,%0,47104
Maaa 152,192,12
R_ v196,a116
R_ v197,a117
Maaa 148,220,88
R_ v198,a118
R_ v199,a119
Maaa 148,196,48
R_ v216,a156
R_ v217,a157
Maaa 148,192,8
R_ v218,a158
R_ v219,a159
Maaa 144,220,84
R_ v212,a152
R_ v213,a153
Maaa 144,196,44
R_ v214,a154
R_ v215,a155
Maaa 144,192,4
Maaa 140,220,80
R_ v208,a148
R_ v209,a149
Maaa 140,196,40
R_ v210,a150
R_ v211,a151
Maaa 140,192,0
s_waitcnt vmcnt(15)
Mvaa 164,70,60
R_ v204,a144
R_ v205,a145
Mvaa 168,68,60
R_ v206,a146
R_ v207,a147
Mvaa 172,70,64
Mvaa 176,68,64
R_ v200,a140
R_ v201,a141
Mvaa 180,70,20
R_ v202,a142
R_ v203,a143
Mvaa 184,70,24
Mvaa 188,70,28
Mvaa 192,70,32
Mvaa 196,70,36
Mvaa 216,68,36
Mvaa 212,68,32
Mvaa 208,68,28
Mvaa 204,68,24
Mvaa 200,68,20
W_ 0
Mvaa 56,180,120
S_ s56,s76,s84
Ta 192,v1,36864
Ta 194,v1,38912
Mvaa 56,184,160
buffer_load_dwordx4 v[136:139], v6, s[28:31], s56 offen offset:0 sc1
Mvaa 56,188,200
s_nop 0
Ta 196,v1,40960
Ta 198,v1,43008
Mvaa 60,180,124
s_mov_b32 m0, s52
Mvaa 60,184,164
s_nop 0
Ta 220,v1,45056
Ta 222,v1,47104
Mvaa 60,188,204
s_nop 0
Y_ 56,0
Y_ 57,256
Mvaa 64,180,128
s_nop 0
Y_ 58,512
Y_ 59,768
Mvaa 64,184,168
buffer_load_dwordx4 v[140:143], v6, s[28:31], s56 offen offset:1024 sc1
Mvaa 64,188,208
s_nop 0
Y_ 60,1024
Y_ 61,1280
Mvaa 68,180,132
s_nop 0
Y_ 62,1536
Y_ 63,1792
Mvaa 68,184,172
Mvaa 68,188,212
s_nop 0
Y_ 64,2048
Y_ 65,2304
Mvaa 72,180,136
s_nop 0
Y_ 66,2560
Y_ 67,2816
Mvaa 72,184,176
buffer_load_dwordx4 v[144:147], v6, s[28:31], s56 offen offset:2048 sc1
Mvaa 72,188,216
W_ 12
Y_ 68,3072
Y_ 69,3328
Mvaa 92,220,216
s_nop 0
Ta 180,v2,36864
Ta 182,v2,38912
Mvaa 92,196,176
s_nop 0
Y_ 70,3584
Y_ 71,3840
Mvaa 92,192,136
s_nop 0
Ta 184,v2,40960
Ta 186,v2,43008
Mvaa 88,220,212
s_nop 0
Y_ 72,4096
Y_ 73,4352
Mvaa 88,196,172
s_nop 0
Ta 188,v2,45056
Ta 190,v2,47104
Mvaa 88,192,132
s_nop 0
Y_ 74,4608
Y_ 75,4864
Mvaa 84,220,208
s_nop 0
Y_ 92,9216
Y_ 93,9472
Mvaa 84,196,168
s_nop 0
Y_ 94,9728
Y_ 95,9984
Mvaa 84,192,128
s_nop 0
Y_ 88,8192
Y_ 89,8448
Mvaa 80,220,204
s_nop 0
Y_ 90,8704
Y_ 91,8960
Mvaa 80,196,164
buffer_load_dwordx4 v[148:151], v6, s[28:31], s56 offen offset:3072 sc1
Mvaa 80,192,124
S_ s56,s56,4096
Y_ 84,7168
Y_ 85,7424
Mvaa 76,220,200
s_nop 0
Y_ 86,7680
Y_ 87,7936
Mvaa 76,196,160
Mvaa 76,192,120
W_ 14
Y_ 80,6144
Y_ 81,6400
Mvaa 180,180,120
s_nop 0
Ta 192,v3,36864
Ta 194,v3,38912
Mvaa 180,184,160
s_nop 0
Y_ 82,6656
Y_ 83,6912
Mvaa 180,188,200
s_nop 0
Ta 196,v3,40960
Ta 198,v3,43008
Mvaa 184,180,124
s_nop 0
Y_ 76,5120
Y_ 77,5376
Mvaa 184,184,164
s_nop 0
Ta 220,v3,45056
Ta 222,v3,47104
Mvaa 184,188,204
s_nop 0
Y_ 78,5632
Y_ 79,5888
Mvaa 188,180,128
buffer_load_dwordx4 v[152:155], v6, s[28:31], s56 offen offset:0 sc1
Mvaa 188,184,168
Mvaa 188,188,208
s_waitcnt vmcnt(63) lgkmcnt(0)
s_barrier
Mvaa 192,180,132
ds_read2st64_b32 v[96:97], v5 offset0:0 offset1:1
Mvaa 192,184,172
ds_read2st64_b32 v[98:99], v5 offset0:2 offset1:3
Mvaa 192,188,212
ds_read2st64_b32 v[104:105], v5 offset0:4 offset1:5
Mvaa 196,180,136
ds_read2st64_b32 v[106:107], v5 offset0:6 offset1:7
Mvaa 196,184,176
ds_read2st64_b32 v[112:113], v5 offset0:8 offset1:9
Mvaa 196,188,216
ds_read2st64_b32 v[114:115], v5 offset0:10 offset1:11
Mvaa 216,220,216
ds_read2st64_b32 v[120:121], v5 offset0:12 offset1:13
Mvaa 216,196,176
ds_read2st64_b32 v[122:123], v5 offset0:14 offset1:15
W_ 7
A_ v96,v96,v180
Mvaa 216,192,136
ds_read2st64_b32 v[128:129], v5 offset0:16 offset1:17
)A"
R"A(A_ v97,v97,v181
Mvaa 212,220,212
ds_read2st64_b32 v[130:131], v5 offset0:18 offset1:19
W_ 8
A_ v98,v98,v182
Mvaa 212,196,172
ds_read2st64_b32 v[132:133], v5 offset0:36 offset1:37
A_ v99,v99,v183
Mvaa 212,192,132
ds_read2st64_b32 v[134:135], v5 offset0:38 offset1:39
W_ 9
A_ v104,v104,v184
Mvaa 208,220,208
ds_read2st64_b32 v[124:125], v5 offset0:32 offset1:33
A_ v105,v105,v185
Mvaa 208,196,168
ds_read2st64_b32 v[126:127], v5 offset0:34 offset1:35
W_ 10
A_ v106,v106,v186
Mvaa 208,192,128
ds_read2st64_b32 v[116:117], v5 offset0:28 offset1:29
A_ v107,v107,v187
Mvaa 204,220,204
ds_read2st64_b32 v[118:119], v5 offset0:30 offset1:31
W_ 11
A_ v112,v112,v188
Mvaa 204,196,164
ds_read2st64_b32 v[108:109], v5 offset0:24 offset1:25
A_ v113,v113,v189
Mvaa 204,192,124
ds_read2st64_b32 v[110:111], v5 offset0:26 offset1:27
W_ 12
A_ v114,v114,v190
Mvaa 200,220,200
ds_read2st64_b32 v[100:101], v5 offset0:20 offset1:21
A_ v115,v115,v191
Mvaa 200,196,160
ds_read2st64_b32 v[102:103], v5 offset0:22 offset1:23
W_ 13
A_ v120,v120,v192
Mvaa 200,192,120
A_ v121,v121,v193
W_ 12
A_ v122,v122,v194
A_ v123,v123,v195
W_ 11
A_ v128,v128,v196
A_ v129,v129,v197
W_ 10
A_ v130,v130,v198
A_ v131,v131,v199
s_waitcnt vmcnt(5)
Mvav 164,70,36
W_ 9
A_ v132,v132,v216
A_ v133,v133,v217
Mvav 168,68,36
W_ 8
A_ v134,v134,v218
A_ v135,v135,v219
Mvav 172,70,40
W_ 7
A_ v124,v124,v212
A_ v125,v125,v213
Mvav 176,68,40
W_ 6
A_ v126,v126,v214
A_ v127,v127,v215
Mvav 164,70,156
W_ 5
A_ v116,v116,v208
A_ v117,v117,v209
Mvav 168,68,156
W_ 4
A_ v118,v118,v210
A_ v119,v119,v211
Mvav 172,70,160
W_ 3
A_ v108,v108,v204
A_ v109,v109,v205
Mvav 176,68,160
W_ 2
A_ v110,v110,v206
A_ v111,v111,v207
Mvav 96,70,16
W_ 1
A_ v100,v100,v200
A_ v101,v101,v201
Mvav 104,70,20
W_ 0
A_ v102,v102,v202
A_ v103,v103,v203
Mvav 112,70,24
C_ 220,164,165
C_ 221,166,167
Mvav 120,70,28
C_ 222,168,169
C_ 223,170,171
Mvav 128,70,32
v_permlane16_swap_b32 v220, v222
v_permlane16_swap_b32 v221, v223
Mvav 132,68,32
C_ 228,172,173
C_ 229,174,175
Mvav 124,68,28
C_ 230,176,177
C_ 231,178,179
Mvav 116,68,24
v_permlane16_swap_b32 v228, v230
v_permlane16_swap_b32 v229, v231
Mvav 108,68,20
Mvav 100,68,16
s_waitcnt vmcnt(0)
.Lsel6:
Nvav 180,70,136,96
Nvav 188,70,140,104
Nvav 196,70,144,112
Nvav 204,70,148,120
Nvav 212,70,152,128
Nvav 184,68,136,100
s_nop 3
A_ v10,v180,v188
Nvav 192,68,140,108
A_ v11,v196,v204
Nvav 200,68,144,116
A_ v10,v10,v212
v_cmp_u_f32 vcc, v10, v11
Nvav 208,68,148,124
s_cbranch_vccnz .Lretry
.Lgood:
Nvav 216,68,152,132
buffer_store_dwordx4 v[220:223], v4, s[32:35], s9 offen sc1
buffer_store_dwordx4 v[228:231], v4, s[32:35], s10 offen sc1
C_ 180,180,181
C_ 181,182,183
C_ 182,184,185
C_ 183,186,187
s_nop 0
v_permlane16_swap_b32 v180, v182
v_permlane16_swap_b32 v181, v183
buffer_store_dwordx4 v[180:183], v4, s[32:35], s8 offen sc1
C_ 188,188,189
C_ 189,190,191
C_ 190,192,193
C_ 191,194,195
S_ s8,s8,40960
v_permlane16_swap_b32 v188, v190
v_permlane16_swap_b32 v189, v191
buffer_store_dwordx4 v[188:191], v4, s[32:35], s8 offen sc1
C_ 196,196,197
C_ 197,198,199
C_ 198,200,201
C_ 199,202,203
S_ s8,s8,40960
v_permlane16_swap_b32 v196, v198
v_permlane16_swap_b32 v197, v199
buffer_store_dwordx4 v[196:199], v4, s[32:35], s8 offen sc1
C_ 204,204,205
C_ 205,206,207
C_ 206,208,209
C_ 207,210,211
S_ s8,s8,40960
v_permlane16_swap_b32 v204, v206
v_permlane16_swap_b32 v205, v207
buffer_store_dwordx4 v[204:207], v4, s[32:35], s8 offen sc1
C_ 212,212,213
C_ 213,214,215
C_ 214,216,217
C_ 215,218,219
S_ s8,s8,40960
v_permlane16_swap_b32 v212, v214
v_permlane16_swap_b32 v213, v215
buffer_store_dwordx4 v[212:215], v4, s[32:35], s8 offen sc1
s_endpgm
.Lspin0:
s_cmp_eq_u32 s57, 0
s_cbranch_scc1 .Lspun0
s_sub_u32 s57, s57, 1
s_sleep 4
buffer_load_dword v11, v8, s[92:95], s96 offen offset:384 sc1
s_waitcnt vmcnt(0)
v_cmp_eq_u32 vcc, s62, v11
s_nop 1
s_bitcmp1_b32 vcc_lo, s75
s_cbranch_scc0 .Lspin0
s_branch .Lspun0
.Lspin1:
s_cmp_eq_u32 s57, 0
s_cbranch_scc1 .Lspun1
s_sub_u32 s57, s57, 1
s_sleep 4
buffer_load_dword v11, v8, s[92:95], s96 offen offset:384 sc1
s_waitcnt vmcnt(0)
v_cmp_eq_u32 vcc, s62, v11
s_nop 1
s_bitcmp1_b32 vcc_lo, s75
s_cbranch_scc0 .Lspin1
s_branch .Lspun1
.Lspin2:
s_cmp_eq_u32 s57, 0
s_cbranch_scc1 .Lspun2
s_sub_u32 s57, s57, 1
s_sleep 4
buffer_load_dword v11, v8, s[92:95], s96 offen offset:384 sc1
s_waitcnt vmcnt(0)
v_cmp_eq_u32 vcc, s62, v11
s_nop 1
s_bitcmp1_b32 vcc_lo, s75
s_cbranch_scc0 .Lspin2
s_branch .Lspun2
.Lspin3:
s_cmp_eq_u32 s57, 0
s_cbranch_scc1 .Lspun3
s_sub_u32 s57, s57, 1
s_sleep 4
buffer_load_dword v11, v8, s[92:95], s96 offen offset:384 sc1
s_waitcnt vmcnt(0)
v_cmp_eq_u32 vcc, s62, v11
s_nop 1
s_bitcmp1_b32 vcc_lo, s75
s_cbranch_scc0 .Lspin3
s_branch .Lspun3
.Lspin4:
s_cmp_eq_u32 s57, 0
s_cbranch_scc1 .Lspun4
s_sub_u32 s57, s57, 1
s_sleep 4
buffer_load_dword v11, v8, s[92:95], s96 offen offset:384 sc1
s_waitcnt vmcnt(0)
v_cmp_eq_u32 vcc, s62, v11
s_nop 1
s_bitcmp1_b32 vcc_lo, s75
s_cbranch_scc0 .Lspin4
s_branch .Lspun4
.Lspin5:
s_cmp_eq_u32 s57, 0
s_cbranch_scc1 .Lspun5
s_sub_u32 s57, s57, 1
s_sleep 4
buffer_load_dword v11, v8, s[92:95], s96 offen offset:384 sc1
s_waitcnt vmcnt(0)
v_cmp_eq_u32 vcc, s62, v11
s_nop 1
s_bitcmp1_b32 vcc_lo, s75
s_cbranch_scc0 .Lspin5
s_branch .Lspun5
.Lspin6:
s_cmp_eq_u32 s57, 0
s_cbranch_scc1 .Lspun6
s_sub_u32 s57, s57, 1
s_sleep 4
buffer_load_dword v11, v8, s[92:95], s96 offen offset:384 sc1
s_waitcnt vmcnt(0)
v_cmp_eq_u32 vcc, s62, v11
s_nop 1
s_bitcmp1_b32 vcc_lo, s86
s_cbranch_scc0 .Lspin6
s_branch .Lspun6
.Lretry:
s_cmp_eq_u32 s57, 0
s_cbranch_scc1 .Lgood
s_sub_u32 s57, s57, 1
v_max_u32 v10, v136, v137
v_max3_u32 v10, v10, v138, v139
v_max3_u32 v10, v10, v140, v141
v_max3_u32 v10, v10, v142, v143
v_max3_u32 v10, v10, v144, v145
v_max3_u32 v10, v10, v146, v147
v_max3_u32 v10, v10, v148, v149
v_max3_u32 v10, v10, v150, v151
v_max3_u32 v10, v10, v152, v153
v_max3_u32 v10, v10, v154, v155
v_cmp_eq_u32 vcc, -1, v10
s_nop 4
s_cbranch_vccz .Lgood
s_nop 7
s_nop 7
S_ s53,s80,4096
buffer_load_dwordx4 v[136:139], v6, s[28:31], s80 offen offset:0 sc1
buffer_load_dwordx4 v[140:143], v6, s[28:31], s80 offen offset:1024 sc1
buffer_load_dwordx4 v[144:147], v6, s[28:31], s80 offen offset:2048 sc1
buffer_load_dwordx4 v[148:151], v6, s[28:31], s80 offen offset:3072 sc1
buffer_load_dwordx4 v[152:155], v6, s[28:31], s53 offen offset:0 sc1
s_waitcnt vmcnt(0)
s_branch .Lsel6
.Ldone:
s_waitcnt vmcnt(0)
.purgem S_
.purgem W_
.purgem L_
.purgem X_
.purgem R_
.purgem Ta
.purgem Da
.purgem Zvaa
.purgem Mvaa
.purgem U_
.purgem C_
.purgem Ln
.purgem Dv
.purgem Zvvv
.purgem Mvvv
.purgem B_
.purgem E_
.purgem Nvvv
.purgem Tv
.purgem Zaav
.purgem Zavv
.purgem A_
.purgem Mavv
.purgem Zvav
.purgem Mvav
.purgem Y_
.purgem Zaaa
.purgem Maaa
.purgem Maav
.purgem Nvav
)A"
    : "+v"(tid)
    : "s"(__builtin_amdgcn_kernarg_segment_ptr()), "s"(blockIdx.x)
    : "v1", "v2", "v3", "v4", "v5", "v6", "v7", "v8", "v9", "v10", "v11", "v12", "v13", "v14", "v15", "v16", "v17", "v18", "v19", "v20", "v21", "v22", "v23", "v24", "v25", "v26", "v27", "v28", "v29", "v30", "v31", "v32", "v33", "v34", "v35", "v36", "v37", "v38", "v39", "v40", "v41", "v42", "v43", "v44", "v45", "v46", "v47", "v48", "v49", "v50", "v51", "v52", "v53", "v54", "v55", "v56", "v57", "v58", "v59", "v60", "v61", "v62", "v63", "v64", "v65", "v66", "v67", "v68", "v69", "v70", "v71", "v72", "v73", "v74", "v75", "v76", "v77", "v78", "v79", "v80", "v81", "v82", "v83", "v84", "v85", "v86", "v87", "v88", "v89", "v90", "v91", "v92", "v93", "v94", "v95", "v96", "v97", "v98", "v99", "v100", "v101", "v102", "v103", "v104", "v105", "v106", "v107", "v108", "v109", "v110", "v111", "v112", "v113", "v114", "v115", "v116", "v117", "v118", "v119", "v120", "v121", "v122", "v123", "v124", "v125", "v126", "v127", "v128", "v129", "v130", "v131", "v132", "v133", "v134", "v135", "v136", "v137", "v138", "v139", "v140", "v141", "v142", "v143", "v144", "v145", "v146", "v147", "v148", "v149", "v150", "v151", "v152", "v153", "v154", "v155", "v156", "v157", "v158", "v159", "v160", "v161", "v162", "v163", "v164", "v165", "v166", "v167", "v168", "v169", "v170", "v171", "v172", "v173", "v174", "v175", "v176", "v177", "v178", "v179", "v180", "v181", "v182", "v183", "v184", "v185", "v186", "v187", "v188", "v189", "v190", "v191", "v192", "v193", "v194", "v195", "v196", "v197", "v198", "v199", "v200", "v201", "v202", "v203", "v204", "v205", "v206", "v207", "v208", "v209", "v210", "v211", "v212", "v213", "v214", "v215", "v216", "v217", "v218", "v219", "v220", "v221", "v222", "v223", "v224", "v225", "v226", "v227", "v228", "v229", "v230", "v231", "v232", "v233", "v234", "v235", "v236", "v237", "v238", "v239", "v240", "v241", "v242", "v243", "v244", "v245", "v246", "v247", "v248", "v249", "v250", "v251", "v252", "v253", "v254", "v255", "a0", "a1", "a2", "a3", "a4", "a5", "a6", "a7", "a8", "a9", "a10", "a11", "a12", "a13", "a14", "a15", "a16", "a17", "a18", "a19", "a20", "a21", "a22", "a23", "a24", "a25", "a26", "a27", "a28", "a29", "a30", "a31", "a32", "a33", "a34", "a35", "a36", "a37", "a38", "a39", "a40", "a41", "a42", "a43", "a44", "a45", "a46", "a47", "a48", "a49", "a50", "a51", "a52", "a53", "a54", "a55", "a56", "a57", "a58", "a59", "a60", "a61", "a62", "a63", "a64", "a65", "a66", "a67", "a68", "a69", "a70", "a71", "a72", "a73", "a74", "a75", "a76", "a77", "a78", "a79", "a80", "a81", "a82", "a83", "a84", "a85", "a86", "a87", "a88", "a89", "a90", "a91", "a92", "a93", "a94", "a95", "a96", "a97", "a98", "a99", "a100", "a101", "a102", "a103", "a104", "a105", "a106", "a107", "a108", "a109", "a110", "a111", "a112", "a113", "a114", "a115", "a116", "a117", "a118", "a119", "a120", "a121", "a122", "a123", "a124", "a125", "a126", "a127", "a128", "a129", "a130", "a131", "a132", "a133", "a134", "a135", "a136", "a137", "a138", "a139", "a140", "a141", "a142", "a143", "a144", "a145", "a146", "a147", "a148", "a149", "a150", "a151", "a152", "a153", "a154", "a155", "a156", "a157", "a158", "a159", "a160", "a161", "a162", "a163", "a164", "a165", "a166", "a167", "a168", "a169", "a170", "a171", "a172", "a173", "a174", "a175", "a176", "a177", "a178", "a179", "a180", "a181", "a182", "a183", "a184", "a185", "a186", "a187", "a188", "a189", "a190", "a191", "a192", "a193", "a194", "a195", "a196", "a197", "a198", "a199", "a200", "a201", "a202", "a203", "a204", "a205", "a206", "a207", "a208", "a209", "a210", "a211", "a212", "a213", "a214", "a215", "a216", "a217", "a218", "a219", "a220", "a221", "a222", "a223", "a224", "a225", "a226", "a227", "a228", "a229", "a230", "a231", "a232", "a233", "a234", "a235", "a236", "a237", "a238", "a239", "a240", "a241", "a242", "a243", "a244", "a245", "a246", "a247", "a248", "a249", "a250", "a251", "a252", "a253", "a254", "a255", "s3", "s4", "s5", "s6", "s7", "s8", "s9", "s10", "s11", "s12", "s13", "s14", "s15", "s16", "s17", "s18", "s19", "s20", "s21", "s22", "s23", "s24", "s25", "s26", "s27", "s28", "s29", "s30", "s31", "s32", "s33", "s34", "s35", "s36", "s37", "s38", "s39", "s40", "s41", "s42", "s43", "s44", "s45", "s46", "s47", "s48", "s49", "s50", "s51", "s52", "s53", "s54", "s55", "s56", "s57", "s58", "s59", "s60", "s61", "s62", "s63", "s64", "s65", "s66", "s67", "s68", "s69", "s70", "s71", "s72", "s73", "s74", "s75", "s76", "s77", "s78", "s79", "s80", "s81", "s82", "s83", "s84", "s85", "s86", "s87", "s88", "s89", "s90", "s91", "s92", "s93", "s94", "s95", "s96", "s97", "s98", "s99", "s100", "s101", "vcc", "scc", "memory");
}

extern "C" void kernel_launch(void* const* d_in, const int* in_sizes, int n_in, void* d_out, int out_size, void* d_ws, size_t ws_size, hipStream_t stream) {
  Args P;
  for (int br = 0; br < 2; ++br) {
    P.p[br][0] = d_in[5 * br]; P.p[br][1] = d_in[5 * br + 3]; P.p[br][2] = d_in[5 * br + 1]; P.p[br][3] = d_in[5 * br + 2]; P.p[br][4] = d_in[5 * br + 4];
    P.p[br][5] = d_ws; P.p[br][6] = (char*)d_out + br * 1280; P.p[br][7] = d_ws;
  }
  (void)hipFuncSetAttribute((const void*)fused_kernel, hipFuncAttributeMaxDynamicSharedMemorySize, LDS1);
  fused_kernel<<<256, 256, LDS1, stream>>>(P);
}
```

```cpp
#include <hip/hip_runtime.h>
#include <hip/hip_bf16.h>

constexpr unsigned LDS1 = 160 * 1024;

struct Args { const void* p[2][8]; };

__global__ __launch_bounds__(256, 1) void fused_kernel(Args P) {
  unsigned tid = threadIdx.x;
  asm volatile(
R"A(.macro S_ a,b,c
s_add_u32 \a, \b, \c
.endm
.macro W_ a
s_waitcnt lgkmcnt(\a)
.endm
.macro L_ x,r,s,o
buffer_load_dwordx4 \x, s[\r:\r+3], \s offen offset:\o lds
.endm
.macro X_ a,b
v_accvgpr_write_b32 \a, \b
.endm
.macro R_ a,b
v_accvgpr_read_b32 \a, \b
.endm
.macro Ta d,x,o
ds_read_b64_tr_b16 a[\d:\d+1], \x offset:\o
.endm
.macro Da d,x,o
ds_read_b128 a[\d:\d+3], \x offset:\o
.endm
.macro Zvaa d,a,b
v_mfma_f32_16x16x32_bf16 v[\d:\d+3], a[\a:\a+3], a[\b:\b+3], 0
.endm
.macro Mvaa d,a,b
v_mfma_f32_16x16x32_bf16 v[\d:\d+3], a[\a:\a+3], a[\b:\b+3], v[\d:\d+3]
.endm
.macro U_ a,b,c
v_mul_f32 \a, \b, \c
.endm
.macro C_ d,a,b
v_cvt_pk_bf16_f32 v\d, v\a, v\b
.endm
.macro Ln x,r,s,o
buffer_load_dwordx4 \x, s[\r:\r+3], \s offen offset:\o nt lds
.endm
.macro Dv d,x,o
ds_read_b128 v[\d:\d+3], \x offset:\o
.endm
.macro Zvvv d,a,b
v_mfma_f32_16x16x32_bf16 v[\d:\d+3], v[\a:\a+3], v[\b:\b+3], 0
.endm
.macro Mvvv d,a,b
v_mfma_f32_16x16x32_bf16 v[\d:\d+3], v[\a:\a+3], v[\b:\b+3], v[\d:\d+3]
.endm
.macro B_ a,b,c
v_sub_f32 \a, \b, \c
.endm
.macro E_ r
v_exp_f32 v\r, v\r
.endm
.macro Nvvv d,a,b,c
v_mfma_f32_16x16x32_bf16 v[\d:\d+3], v[\a:\a+3], v[\b:\b+3], v[\c:\c+3]
.endm
.macro Tv d,x,o
ds_read_b64_tr_b16 v[\d:\d+1], \x offset:\o
.endm
.macro Zaav d,a,b
v_mfma_f32_16x16x32_bf16 a[\d:\d+3], a[\a:\a+3], v[\b:\b+3], 0
.endm
.macro Zavv d,a,b
v_mfma_f32_16x16x32_bf16 a[\d:\d+3], v[\a:\a+3], v[\b:\b+3], 0
.endm
.macro A_ a,b,c
v_add_f32 \a, \b, \c
.endm
.macro Mavv d,a,b
v_mfma_f32_16x16x32_bf16 a[\d:\d+3], v[\a:\a+3], v[\b:\b+3], a[\d:\d+3]
.endm
.macro Zvav d,a,b
v_mfma_f32_16x16x32_bf16 v[\d:\d+3], a[\a:\a+3], v[\b:\b+3], 0
.endm
.macro Mvav d,a,b
v_mfma_f32_16x16x32_bf16 v[\d:\d+3], a[\a:\a+3], v[\b:\b+3], v[\d:\d+3]
.endm
.macro Y_ r,o
ds_write_addtid_b32 v\r offset:\o
.endm
.macro Zaaa d,a,b
v_mfma_f32_16x16x32_bf16 a[\d:\d+3], a[\a:\a+3], a[\b:\b+3], 0
.endm
.macro Maaa d,a,b
v_mfma_f32_16x16x32_bf16 a[\d:\d+3], a[\a:\a+3], a[\b:\b+3], a[\d:\d+3]
.endm
.macro Maav d,a,b
v_mfma_f32_16x16x32_bf16 a[\d:\d+3], a[\a:\a+3], v[\b:\b+3], a[\d:\d+3]
.endm
.macro Nvav d,a,b,c
v_mfma_f32_16x16x32_bf16 v[\d:\d+3], a[\a:\a+3], v[\b:\b+3], v[\c:\c+3]
.endm
s_and_b32 s3, %2, 4
s_lshl_b32 s3, s3, 4
s_load_dwordx16 s[8:23], %1, s3
s_memrealtime s[100:101]
s_memtime s[90:91]
v_readfirstlane_b32 s4, %0
v_and_b32 v16, 63, %0
v_lshlrev_b32 v10, 4, v16
s_mov_b32 s7, %2
s_lshr_b32 s4, s4, 6
.Lcode0:
s_getpc_b64 s[44:45]
s_mov_b32 s46, .Ldone-.Lcode0
s_mov_b32 s47, 0x00020000
s_lshr_b32 s66, s7, 3
s_lshl_b32 s66, s66, 2
S_ s66,s66,s4
s_lshl_b32 s66, s66, 11
v_min_u32 v40, 15, v16
v_lshlrev_b32 v40, 7, v40
buffer_load_dword v247, v40, s[44:47], s66 offen
s_and_b32 s3, s7, 1
s_lshl_b32 s3, s3, 3
s_lshr_b32 s53, s7, 5
S_ s3,s3,s53
s_and_b32 s6, s7, 2
s_lshl_b32 s6, s6, 1
s_bfe_u32 s53, s7, 0x20003
S_ s6,s6,s53
s_and_b32 s5, s7, 4
s_lshl_b32 s5, s5, 2
S_ s5,s5,s3
s_mul_i32 s50, s4, 5120
s_mul_i32 s60, s4, 51200
s_lshr_b32 s77, s4, 1
s_and_b32 s53, s4, 1
s_mul_i32 s59, s53, 393216
s_lshl_b32 s52, s53, 11
s_mul_i32 s61, s53, 6144
s_mul_i32 s51, s77, 61440
S_ s55,s51,40960
S_ s51,s55,s61
s_sub_u32 s52, s51, s52
s_lshl_b32 s86, s53, 2
s_mul_i32 s56, s53, 10240
v_lshrrev_b32 v17, 3, v16
v_and_b32 v18, 7, v16
v_lshrrev_b32 v23, 4, v16
v_and_b32 v24, 1, v23
v_lshlrev_b32 v24, 2, v24
v_xor_b32 v24, v18, v24
v_mul_u32_u24 v6, 12288, v17
v_lshl_add_u32 v6, v24, 4, v6
v_add_u32 v24, s86, v23
v_xor_b32 v24, v18, v24
v_mul_u32_u24 v4, 1280, v17
v_lshl_add_u32 v4, v24, 4, v4
v_xor_b32 v5, 64, v4
s_getreg_b32 s53, hwreg(HW_REG_XCC_ID, 0, 4)
s_bitcmp1_b32 0xaaaa, s53
s_cselect_b32 s58, 2, 0
s_cselect_b32 s89, 1, 0
s_cselect_b32 s53, 768, 0
S_ s59,s59,s53
s_lshl_b32 s54, s6, 2
S_ s54,s54,s77
s_mul_i32 s86, s54, 384
s_mul_i32 s87, s3, 204800
s_lshl_b32 s53, s3, 5
S_ s53,s53,s54
s_mul_i32 s85, s53, 196608
s_mov_b32 s30, 7853184
s_mov_b32 s31, 0x00020000
s_mov_b32 s26, 204800
s_mov_b32 s27, 0x00020000
W_ 0
s_mov_b32 s98, s100
s_mov_b32 s99, 0
s_mov_b32 s88, s90
S_ s28,s10,s86
s_addc_u32 s29, s11, 0
S_ s24,s8,s87
s_addc_u32 s25, s9, 0
s_buffer_load_dword s67, s[24:27], s60
s_mul_i32 s64, s66, 30
s_buffer_load_dword s67, s[28:31], s64
s_mov_b32 s11, 0
S_ s65,s59,0
S_ m0,s52,8192
s_mov_b64 s[92:93], s[18:19]
s_mov_b32 s53, 251658240
L_ v6,28,s65,0
S_ s65,s59,98304
S_ m0,s52,9216
S_ s92,s92,s53
s_addc_u32 s93, s93, 0
L_ v6,28,s65,0
S_ s65,s59,196608
S_ m0,s52,10240
s_mov_b32 s94, 131072
s_mov_b32 s95, 0x00020000
L_ v6,28,s65,0
S_ s65,s59,294912
S_ m0,s52,11264
s_lshl_b32 s96, s5, 3
S_ s96,s96,s6
L_ v6,28,s65,0
S_ s65,s60,0
S_ m0,s50,0
s_lshl_b32 s96, s96, 9
v_cmp_gt_u32 vcc, 8, v16
L_ v4,24,s65,0
S_ s65,s60,10240
S_ m0,s50,1024
s_lshl_b32 s54, s5, 12
v_lshl_add_u32 v24, v16, 9, s54
L_ v5,24,s65,0
S_ s65,s60,20480
S_ m0,s50,2048
v_mov_b32 v12, 0x7fff0000
v_cndmask_b32 v12, v12, v24, vcc
L_ v4,24,s65,0
S_ s65,s60,30720
S_ m0,s50,3072
s_lshl_b32 s54, s6, 2
v_add_u32 v13, s54, v12
L_ v5,24,s65,0
S_ s65,s60,40960
S_ m0,s50,4096
v_lshrrev_b32 v19, 2, v10
v_mul_u32_u24 v25, 384, v17
L_ v4,24,s65,0
s_load_dword s67, s[12:13], s85
s_load_dword s67, s[14:15], s85
S_ s64,s85,393216
s_load_dword s67, s[12:13], s64
s_load_dword s67, s[14:15], s64
s_lshl_b32 s64, s6, 2
S_ s64,s64,s4
s_mul_i32 s64, s64, 245760
s_load_dword s67, s[16:17], s64
s_cmp_eq_u32 s4, 0
s_cbranch_scc0 .Lhs0a
buffer_load_dword v20, v19, s[92:95], s96 offen offset:128 sc1
buffer_load_dword v21, v19, s[92:95], s96 offen offset:384 sc1
buffer_load_dword a252, v19, s[92:95], s96 offen offset:256 sc1
buffer_load_dword a253, v13, s[92:95], 0 offen offset:256 sc1
buffer_load_dword v22, off, s[92:95], s96 offset:0 sc1
.Lhs0a:
S_ s65,s59,0
S_ m0,s52,16256
v_xor_b32 v24, v18, v23
v_lshl_add_u32 v7, v24, 4, v25
v_lshlrev_b32 v24, 1, v23
v_xor_b32 v24, v18, v24
v_lshl_add_u32 v9, v24, 4, v25
v_and_b32 v26, 15, v16
v_lshrrev_b32 v27, 2, v26
v_and_b32 v28, 3, v16
v_lshrrev_b32 v24, 1, v26
v_xor_b32 v24, v23, v24
L_ v6,28,s65,128
S_ s65,s59,98304
S_ m0,s52,17280
v_lshlrev_b32 v24, 4, v24
v_lshl_add_u32 v24, v18, 7, v24
v_lshrrev_b32 v25, 3, v26
v_lshl_add_u32 v2, v25, 10, v24
v_add_u32 v2, s56, v2
v_mul_u32_u24 v1, 3072, v25
v_add3_u32 v1, v1, v24, s55
v_lshlrev_b32 v24, 2, v25
v_xor_b32 v24, v28, v24
v_lshlrev_b32 v24, 4, v24
L_ v6,28,s65,128
S_ s65,s59,196608
S_ m0,s52,18304
v_lshl_add_u32 v24, v27, 7, v24
v_lshl_add_u32 %0, v23, 10, v24
v_add_u32 %0, s55, %0
v_and_b32 v29, 1, v23
v_lshl_add_u32 v24, v29, 1, v25
v_lshlrev_b32 v24, 1, v24
v_lshrrev_b32 v30, 1, v28
v_xor_b32 v24, v30, v24
v_lshlrev_b32 v24, 4, v24
v_lshl_add_u32 v29, v29, 2, v27
L_ v6,28,s65,128
S_ s65,s59,294912
S_ m0,s52,19328
v_lshl_add_u32 v24, v29, 7, v24
v_and_b32 v29, 1, v28
v_lshl_add_u32 v24, v29, 3, v24
v_lshrrev_b32 v29, 1, v23
v_mul_u32_u24 v3, 3072, v29
v_add3_u32 v3, v3, v24, s55
X_ a242,%0
X_ a244,v1
X_ a243,v2
X_ a245,v3
L_ v6,28,s65,128
S_ s65,s59,0
S_ m0,s52,24320
X_ a240,v4
X_ a248,v10
X_ a241,v12
X_ a251,v13
v_xor_b32 v1, 64, %0
v_xor_b32 v3, 64, v2
v_xor_b32 v8, 64, v7
S_ s32,s12,s85
s_addc_u32 s33, s13, 0
s_mov_b32 s34, 589824
L_ v6,28,s65,256
S_ s65,s59,98304
S_ m0,s52,25344
s_mov_b32 s35, 0x00020000
S_ s36,s14,s85
s_addc_u32 s37, s15, 0
s_mov_b32 s38, 589824
s_mov_b32 s39, 0x00020000
s_lshl_b32 s87, s87, 1
S_ s20,s20,s87
s_addc_u32 s21, s21, 0
s_mov_b32 s57, 20000
s_mov_b32 s48, 1037384597
L_ v6,28,s65,256
S_ s65,s59,196608
S_ m0,s52,26368
s_mov_b32 s49, 1037384597
s_mul_i32 s53, s5, 1638400
S_ s53,s53,134217728
S_ s22,s18,s53
s_addc_u32 s23, s19, 0
s_lshl_b32 s53, s6, 3
s_lshr_b32 s54, s7, 5
S_ s53,s53,s54
s_lshl_b32 s53, s53, 2
S_ s53,s53,s4
L_ v6,28,s65,256
S_ s65,s59,294912
S_ m0,s52,27392
s_mul_i32 s87, s53, 30720
s_lshl_b32 s53, s6, 2
S_ s53,s53,s77
s_mul_i32 s72, s53, 245760
s_and_b32 s54, s4, 1
s_mul_i32 s53, s54, 61440
S_ s72,s72,s53
s_lshl_b32 s53, s77, 6
s_xor_b32 s83, s53, 64
s_xor_b32 s53, s4, 2
L_ v6,28,s65,256
S_ s65,s60,128
S_ m0,s50,20480
s_mul_i32 s75, s53, 10240
s_cmp_eq_u32 s4, 0
s_cselect_b32 s73, 0, 2147418112
s_mul_i32 s53, s4, 5120
s_mul_i32 s78, s6, 204800
S_ s78,s78,s53
s_and_b32 s84, s6, 7
s_mul_i32 s84, s84, 20480
S_ s84,s84,s53
s_mul_i32 s53, s6, 5120
L_ v4,24,s65,0
S_ s65,s60,10368
S_ m0,s50,21504
S_ s53,s53,163840
s_lshl_b32 s54, s4, 10
S_ s74,s53,s54
S_ s53,s53,4096
s_cmp_eq_u32 s4, 0
s_cselect_b32 s82, s53, 4026531840
s_and_b32 s53, s4, 1
s_mul_i32 s53, s53, 204800
s_lshl_b32 s54, s6, 7
S_ s53,s53,s54
L_ v5,24,s65,0
S_ s65,s60,20608
S_ m0,s50,22528
s_lshl_b32 s54, s77, 6
S_ s8,s53,s54
s_and_b32 s53, s6, 1
s_mul_i32 s53, s53, 204800
s_lshr_b32 s54, s6, 1
s_lshl_b32 s54, s54, 6
S_ s53,s53,s54
S_ s53,s53,1024
s_mul_i32 s54, s4, 40960
S_ s9,s53,s54
L_ v4,24,s65,0
S_ s65,s60,30848
S_ m0,s50,23552
S_ s53,s53,163840
s_cmp_eq_u32 s4, 0
s_cselect_b32 s10, s53, 4026531840
R_ v12,a248
v_lshrrev_b32 v12, 4, v12
v_min_u32 v10, 11, v12
s_mul_i32 s56, s4, 12
v_add_u32 v10, s56, v10
v_mul_u32_u24 v11, 43691, v10
v_lshrrev_b32 v11, 18, v11
L_ v5,24,s65,0
S_ s65,s60,41088
S_ m0,s50,24576
v_lshlrev_b32 v10, 7, v10
v_mul_u32_u24 v11, 11520, v11
v_add_u32 v10, v10, v11
v_min_u32 v12, 9, v12
s_and_b32 s56, s6, 3
s_lshl_b32 s56, s56, 2
S_ s56,s56,s4
s_mul_i32 s56, s56, 10
v_add_u32 v12, s56, v12
v_mul_u32_u24 v12, 1280, v12
L_ v4,24,s65,0
S_ s65,s59,786432
S_ m0,s52,0
s_lshr_b32 s56, s7, 5
s_mul_i32 s56, s56, 98304
s_and_b32 s55, s59, 4095
S_ s56,s56,s55
s_mul_i32 s55, s77, 384
s_sub_u32 s56, s56, s55
s_mov_b32 s63, 0
L_ v6,28,s65,0
S_ s65,s59,884736
S_ m0,s52,1024
s_nop 0
L_ v6,28,s65,0
S_ s65,s59,983040
S_ m0,s52,2048
s_nop 0
L_ v6,28,s65,0
S_ s65,s59,1081344
S_ m0,s52,3072
s_nop 0
L_ v6,28,s65,0
S_ s55,s56,786432
buffer_load_dword a254, v10, s[28:31], s55 offen
S_ s55,s56,1572864
buffer_load_dword a254, v10, s[28:31], s55 offen
buffer_load_dword a254, v12, s[24:27], s63 offen offset:256
S_ s55,s56,2359296
buffer_load_dword a254, v10, s[28:31], s55 offen
buffer_load_dword a254, v12, s[24:27], s63 offen offset:384
s_waitcnt vmcnt(22) lgkmcnt(0)
s_barrier
s_memtime s[90:91]
s_memrealtime s[68:69]
Ta 20,%0,8192
Ta 22,%0,8704
Ta 24,%0,12288
Ta 26,%0,12800
Da 60,v2,0
Da 100,v3,0
Da 64,v2,2048
Da 104,v3,2048
Da 68,v2,4096
Da 108,v3,4096
Da 72,v2,6144
Da 112,v3,6144
Da 76,v2,8192
Da 116,v3,8192
Ta 28,%0,8200
Ta 30,%0,8712
Ta 32,%0,12296
Ta 34,%0,12808
s_cmp_eq_u32 s4, 0
s_cbranch_scc0 .Lhs0b
v_readfirstlane_b32 s62, v22
.Lhs0l:
s_nop 3
S_ s62,s62,1
v_cmp_eq_u32 vcc, s62, v20
s_nop 1
s_or_b32 s53, vcc_lo, vcc_hi
v_cmp_eq_u32 vcc, s62, v21
s_nop 1
s_or_b32 s54, vcc_lo, vcc_hi
s_or_b32 s53, s53, s54
s_cbranch_scc1 .Lhs0l
v_mov_b32 v22, s62
s_mov_b64 exec, 1
s_nop 1
buffer_store_dword v22, off, s[92:95], s96 sc1
s_mov_b64 exec, -1
s_nop 1
.Lhs0b:
s_cmp_eq_u32 s58, 0
s_cbranch_scc1 .Lq
.Lqy:
W_ 13
Zvaa 16,20,60
s_nop 0
Ta 36,v1,8192
Ta 38,v1,8704
W_ 14
Mvaa 16,24,100
R_ v12,a248
W_ 13
Zvaa 24,20,64
v_lshrrev_b32 v12, 4, v12
W_ 12
Mvaa 24,24,104
s_nop 0
Ta 224,v1,12288
Ta 226,v1,12800
W_ 13
Zvaa 32,20,68
v_min_u32 v10, 11, v12
W_ 12
Mvaa 32,24,108
s_mul_i32 s56, s4, 12
W_ 11
Zvaa 40,20,72
v_add_u32 v10, s56, v10
W_ 10
Mvaa 40,24,112
v_mul_u32_u24 v11, 43691, v10
W_ 9
Zvaa 48,20,76
v_lshrrev_b32 v11, 18, v11
W_ 8
Mvaa 48,24,116
W_ 4
v_lshlrev_b32 v10, 7, v10
Zvaa 52,32,116
s_nop 0
Ta 20,v1,8200
Ta 22,v1,8712
Mvaa 52,28,76
v_mul_u32_u24 v11, 11520, v11
Zvaa 44,32,112
v_add_u32 v10, v10, v11
Mvaa 44,28,72
s_nop 0
Ta 24,v1,12296
Ta 26,v1,12808
Zvaa 36,32,108
v_min_u32 v12, 9, v12
Mvaa 36,28,68
s_and_b32 s56, s6, 3
Zvaa 28,32,104
s_lshl_b32 s56, s56, 2
Mvaa 28,28,64
S_ s56,s56,s4
Zvaa 20,32,100
s_waitcnt vmcnt(18) lgkmcnt(0)
s_barrier
s_mul_i32 s56, s56, 10
Mvaa 20,28,60
S_ s65,s59,786432
v_add_u32 v12, s56, v12
Zvaa 56,36,60
S_ m0,s52,8064
Ta 28,%0,16384
Ta 30,%0,16896
Mvaa 56,224,100
L_ v6,28,s65,128
Zvaa 64,36,64
S_ s65,s59,884736
v_mul_u32_u24 v12, 1280, v12
Mvaa 64,224,104
S_ m0,s52,9088
Ta 32,%0,20480
Ta 34,%0,20992
Zvaa 72,36,68
s_lshr_b32 s56, s7, 5
Mvaa 72,224,108
L_ v6,28,s65,128
Zvaa 80,36,72
S_ s65,s59,983040
s_mul_i32 s56, s56, 98304
Mvaa 80,224,112
S_ m0,s52,10112
s_and_b32 s55, s59, 4095
Zvaa 88,36,76
S_ s56,s56,s55
Mvaa 88,224,116
s_mul_i32 s55, s77, 384
Zvaa 92,24,116
s_sub_u32 s56, s56, s55
Ta 36,%0,16392
Ta 38,%0,16904
Mvaa 92,20,76
L_ v6,28,s65,128
Zvaa 84,24,112
S_ s65,s59,1081344
s_mov_b32 s63, 0
Mvaa 84,20,72
S_ m0,s52,11136
Ta 224,%0,20488
Ta 226,%0,21000
Zvaa 76,24,108
S_ s55,s56,3145728
Mvaa 76,20,68
L_ v6,28,s65,128
Zvaa 68,24,104
S_ s65,s60,256
Mvaa 68,20,64
S_ m0,s50,0
Zvaa 60,24,100
Mvaa 60,20,60
W_ 4
Zvaa 96,28,60
s_nop 0
Ta 20,v1,16384
Ta 22,v1,16896
Mvaa 96,32,100
L_ v4,24,s65,0
Zvaa 104,28,64
S_ s65,s60,10496
Mvaa 104,32,104
S_ m0,s50,1024
Ta 24,v1,20480
Ta 26,v1,20992
Zvaa 112,28,68
Mvaa 112,32,108
L_ v5,24,s65,0
Zvaa 120,28,72
S_ s65,s60,20736
Mvaa 120,32,112
S_ m0,s50,2048
Zvaa 128,28,76
Mvaa 128,32,116
W_ 4
Zvaa 132,224,116
s_nop 0
Ta 28,v1,16392
Ta 30,v1,16904
Mvaa 132,36,76
L_ v4,24,s65,0
Zvaa 124,224,112
S_ s65,s60,30976
Mvaa 124,36,72
S_ m0,s50,3072
Ta 32,v1,20488
Ta 34,v1,21000
Zvaa 116,224,108
Mvaa 116,36,68
L_ v5,24,s65,0
Zvaa 108,224,104
S_ s65,s60,41216
Mvaa 108,36,64
S_ m0,s50,4096
Zvaa 100,224,100
s_waitcnt vmcnt(17) lgkmcnt(0)
s_barrier
Mvaa 100,36,60
Da 140,v2,20480
Zvaa 136,20,60
s_nop 0
Ta 36,%0,24576
Ta 38,%0,25088
Mvaa 136,24,100
L_ v4,24,s65,0
Zvaa 144,20,64
S_ s65,s59,786432
Da 180,v3,20480
Mvaa 144,24,104
S_ m0,s52,16128
Ta 224,%0,28672
Ta 226,%0,29184
Zvaa 152,20,68
Mvaa 152,24,108
Da 144,v2,22528
Zvaa 160,20,72
L_ v6,28,s65,256
Mvaa 160,24,112
S_ s65,s59,884736
Zvaa 168,20,76
S_ m0,s52,17152
Da 184,v3,22528
Mvaa 168,24,116
Zvaa 172,32,116
s_nop 0
Ta 20,%0,24584
Ta 22,%0,25096
Mvaa 172,28,76
Da 148,v2,24576
Zvaa 164,32,112
L_ v6,28,s65,256
Mvaa 164,28,72
S_ s65,s59,983040
Ta 24,%0,28680
Ta 26,%0,29192
Zvaa 156,32,108
S_ m0,s52,18176
Da 188,v3,24576
Mvaa 156,28,68
Zvaa 148,32,104
L_ v6,28,s65,256
Mvaa 148,28,64
S_ s65,s59,1081344
Da 152,v2,26624
Zvaa 140,32,100
S_ m0,s52,19200
Mvaa 140,28,60
W_ 9
Zvaa 176,36,60
s_nop 0
Ta 28,v1,24576
Ta 30,v1,25088
Mvaa 176,224,100
Da 192,v3,26624
Zvaa 184,36,64
L_ v6,28,s65,256
Mvaa 184,224,104
s_nop 0
Ta 32,v1,28672
Ta 34,v1,29184
Zvaa 192,36,68
Da 156,v2,28672
Mvaa 192,224,108
Zvaa 200,36,72
Da 196,v3,28672
Mvaa 200,224,112
buffer_load_dword a254, v10, s[28:31], s55 offen
Zvaa 208,36,76
Mvaa 208,224,116
W_ 9
Zvaa 212,24,116
s_nop 0
Ta 36,v1,24584
Ta 38,v1,25096
Mvaa 212,20,76
buffer_load_dword a254, v12, s[24:27], s63 offen offset:512
Zvaa 204,24,112
Mvaa 204,20,72
s_nop 0
Ta 224,v1,28680
Ta 226,v1,29192
Zvaa 196,24,108
Mvaa 196,20,68
Zvaa 188,24,104
Mvaa 188,20,64
Zvaa 180,24,100
s_waitcnt vmcnt(20) lgkmcnt(0)
s_barrier
Mvaa 180,20,60
S_ s65,s59,1572864
Zvaa 216,28,60
S_ m0,s52,24576
Ta 20,%0,0
Ta 22,%0,512
Mvaa 216,32,100
L_ v6,28,s65,0
Zvaa 224,28,64
S_ s65,s59,1671168
Mvaa 224,32,104
S_ m0,s52,25600
Ta 24,%0,4096
Ta 26,%0,4608
Zvaa 232,28,68
Mvaa 232,32,108
L_ v6,28,s65,0
Zvaa 240,28,72
S_ s65,s59,1769472
Mvaa 240,32,112
S_ m0,s52,26624
Zvaa 248,28,76
Mvaa 248,32,116
Zvaa 252,224,116
s_nop 0
Ta 28,%0,8
Ta 30,%0,520
Mvaa 252,36,76
L_ v6,28,s65,0
Zvaa 244,224,112
S_ s65,s59,1867776
Mvaa 244,36,72
S_ m0,s52,27648
Ta 32,%0,4104
Ta 34,%0,4616
Zvaa 236,224,108
Mvaa 236,36,68
L_ v6,28,s65,0
Zvaa 228,224,104
S_ s65,s60,384
Mvaa 228,36,64
S_ m0,s50,20480
Zvaa 220,224,100
Mvaa 220,36,60
W_ 4
Mvaa 16,20,140
s_nop 0
Ta 36,v1,0
Ta 38,v1,512
Mvaa 16,24,180
L_ v4,24,s65,0
Mvaa 24,20,144
S_ s65,s60,10624
Mvaa 24,24,184
S_ m0,s50,21504
Ta 224,v1,4096
Ta 226,v1,4608
Mvaa 32,20,148
Mvaa 32,24,188
L_ v5,24,s65,0
Mvaa 40,20,152
S_ s65,s60,20864
Mvaa 40,24,192
S_ m0,s50,22528
Mvaa 48,20,156
Mvaa 48,24,196
W_ 4
Mvaa 52,32,196
s_nop 0
Ta 20,v1,8
Ta 22,v1,520
Mvaa 52,28,156
L_ v4,24,s65,0
Mvaa 44,32,192
S_ s65,s60,31104
Mvaa 44,28,152
S_ m0,s50,23552
Ta 24,v1,4104
Ta 26,v1,4616
Mvaa 36,32,188
Mvaa 36,28,148
L_ v5,24,s65,0
Mvaa 28,32,184
S_ s65,s60,41344
Mvaa 28,28,144
S_ m0,s50,24576
Mvaa 20,32,180
s_waitcnt vmcnt(19) lgkmcnt(0)
s_barrier
Mvaa 20,28,140
L_ v4,24,s65,0
Mvaa 56,36,140
S_ s65,s59,1572864
Ta 28,%0,8192
Ta 30,%0,8704
Mvaa 56,224,180
S_ m0,s52,-128
Mvaa 64,36,144
Mvaa 64,224,184
s_nop 0
Ta 32,%0,12288
Ta 34,%0,12800
Mvaa 72,36,148
L_ v6,28,s65,128
Mvaa 72,224,188
S_ s65,s59,1671168
Mvaa 80,36,152
S_ m0,s52,896
Mvaa 80,224,192
Mvaa 88,36,156
L_ v6,28,s65,128
Mvaa 88,224,196
S_ s65,s59,1769472
Mvaa 92,24,196
S_ m0,s52,1920
Ta 36,%0,8200
Ta 38,%0,8712
Mvaa 92,20,156
S_ s55,s56,3932160
Mvaa 84,24,192
L_ v6,28,s65,128
Mvaa 84,20,152
S_ s65,s59,1867776
Ta 224,%0,12296
Ta 226,%0,12808
Mvaa 76,24,188
S_ m0,s52,2944
Mvaa 76,20,148
Mvaa 68,24,184
L_ v6,28,s65,128
Mvaa 68,20,144
Mvaa 60,24,180
Mvaa 60,20,140
W_ 4
Mvaa 96,28,140
s_nop 0
Ta 20,v1,8192
Ta 22,v1,8704
Mvaa 96,32,180
buffer_load_dword a254, v10, s[28:31], s55 offen
Mvaa 104,28,144
Mvaa 104,32,184
s_nop 0
Ta 24,v1,12288
Ta 26,v1,12800
Mvaa 112,28,148
Mvaa 112,32,188
buffer_load_dword a254, v12, s[24:27], s63 offen offset:640
Mvaa 120,28,152
Mvaa 120,32,192
Mvaa 128,28,156
Mvaa 128,32,196
W_ 4
Mvaa 132,224,196
s_nop 0
Ta 28,v1,8200
Ta 30,v1,8712
Mvaa 132,36,156
Mvaa 124,224,192
Mvaa 124,36,152
s_nop 0
Ta 32,v1,12296
Ta 34,v1,12808
Mvaa 116,224,188
Mvaa 116,36,148
Mvaa 108,224,184
Mvaa 108,36,144
Mvaa 100,224,180
s_waitcnt vmcnt(17) lgkmcnt(0)
s_barrier
Mvaa 100,36,140
S_ s65,s59,1572864
Da 60,v2,0
Mvaa 136,20,140
S_ m0,s52,7936
Ta 36,%0,16384
Ta 38,%0,16896
Mvaa 136,24,180
L_ v6,28,s65,256
Mvaa 144,20,144
S_ s65,s59,1671168
Da 100,v3,0
Mvaa 144,24,184
S_ m0,s52,8960
Ta 224,%0,20480
Ta 226,%0,20992
Mvaa 152,20,148
Mvaa 152,24,188
Da 64,v2,2048
Mvaa 160,20,152
L_ v6,28,s65,256
Mvaa 160,24,192
S_ s65,s59,1769472
Mvaa 168,20,156
S_ m0,s52,9984
Da 104,v3,2048
Mvaa 168,24,196
Mvaa 172,32,196
s_nop 0
Ta 20,%0,16392
Ta 22,%0,16904
Mvaa 172,28,156
Da 68,v2,4096
Mvaa 164,32,192
L_ v6,28,s65,256
Mvaa 164,28,152
S_ s65,s59,1867776
Ta 24,%0,20488
Ta 26,%0,21000
Mvaa 156,32,188
S_ m0,s52,11008
Da 108,v3,4096
Mvaa 156,28,148
Mvaa 148,32,184
L_ v6,28,s65,256
Mvaa 148,28,144
Da 72,v2,6144
Mvaa 140,32,180
Mvaa 140,28,140
W_ 9
Mvaa 176,36,140
s_nop 0
Ta 28,v1,16384
Ta 30,v1,16896
Mvaa 176,224,180
Da 112,v3,6144
Mvaa 184,36,144
Mvaa 184,224,184
s_nop 0
Ta 32,v1,20480
Ta 34,v1,20992
Mvaa 192,36,148
Da 76,v2,8192
Mvaa 192,224,188
Mvaa 200,36,152
Da 116,v3,8192
Mvaa 200,224,192
Mvaa 208,36,156
Mvaa 208,224,196
W_ 9
Mvaa 212,24,196
s_nop 0
Ta 36,v1,16392
Ta 38,v1,16904
Mvaa 212,20,156
Mvaa 204,24,192
Mvaa 204,20,152
s_nop 0
Ta 224,v1,20488
Ta 226,v1,21000
Mvaa 196,24,188
Mvaa 196,20,148
Mvaa 188,24,184
Mvaa 188,20,144
Mvaa 180,24,180
s_waitcnt vmcnt(15) lgkmcnt(0)
s_barrier
Mvaa 180,20,140
S_ s65,s59,2359296
Mvaa 216,28,140
S_ m0,s52,16384
Ta 20,%0,24576
Ta 22,%0,25088
Mvaa 216,32,180
L_ v6,28,s65,0
Mvaa 224,28,144
S_ s65,s59,2457600
Mvaa 224,32,184
S_ m0,s52,17408
Ta 24,%0,28672
Ta 26,%0,29184
Mvaa 232,28,148
Mvaa 232,32,188
L_ v6,28,s65,0
Mvaa 240,28,152
S_ s65,s59,2555904
Mvaa 240,32,192
S_ m0,s52,18432
Mvaa 248,28,156
Mvaa 248,32,196
Mvaa 252,224,196
s_nop 0
Ta 28,%0,24584
Ta 30,%0,25096
Mvaa 252,36,156
L_ v6,28,s65,0
Mvaa 244,224,192
S_ s65,s59,2654208
Mvaa 244,36,152
S_ m0,s52,19456
Ta 32,%0,28680
Ta 34,%0,29192
Mvaa 236,224,188
Mvaa 236,36,148
L_ v6,28,s65,0
Mvaa 228,224,184
S_ s65,s60,512
Mvaa 228,36,144
S_ m0,s50,0
Mvaa 220,224,180
Mvaa 220,36,140
W_ 4
Mvaa 16,20,60
s_nop 0
Ta 36,v1,24576
Ta 38,v1,25088
Mvaa 16,24,100
L_ v4,24,s65,0
Mvaa 24,20,64
S_ s65,s60,10752
Mvaa 24,24,104
S_ m0,s50,1024
Ta 224,v1,28672
Ta 226,v1,29184
Mvaa 32,20,68
Mvaa 32,24,108
L_ v5,24,s65,0
Mvaa 40,20,72
S_ s65,s60,20992
Mvaa 40,24,112
S_ m0,s50,2048
Mvaa 48,20,76
Mvaa 48,24,116
W_ 4
Mvaa 52,32,116
s_nop 0
Ta 20,v1,24584
Ta 22,v1,25096
Mvaa 52,28,76
L_ v4,24,s65,0
Mvaa 44,32,112
S_ s65,s60,31232
Mvaa 44,28,72
S_ m0,s50,3072
Ta 24,v1,28680
Ta 26,v1,29192
Mvaa 36,32,108
Mvaa 36,28,68
L_ v5,24,s65,0
Mvaa 28,32,104
S_ s65,s60,41472
Mvaa 28,28,64
S_ m0,s50,4096
Mvaa 20,32,100
s_waitcnt vmcnt(14) lgkmcnt(0)
s_barrier
Mvaa 20,28,60
L_ v4,24,s65,0
Mvaa 56,36,60
S_ s65,s59,2359296
Ta 28,%0,0
Ta 30,%0,512
Mvaa 56,224,100
S_ m0,s52,24448
Mvaa 64,36,64
Mvaa 64,224,104
s_nop 0
Ta 32,%0,4096
Ta 34,%0,4608
Mvaa 72,36,68
L_ v6,28,s65,128
Mvaa 72,224,108
S_ s65,s59,2457600
Mvaa 80,36,72
S_ m0,s52,25472
Mvaa 80,224,112
Mvaa 88,36,76
L_ v6,28,s65,128
Mvaa 88,224,116
S_ s65,s59,2555904
Mvaa 92,24,116
S_ m0,s52,26496
Ta 36,%0,8
Ta 38,%0,520
Mvaa 92,20,76
S_ s55,s56,4718592
Mvaa 84,24,112
L_ v6,28,s65,128
Mvaa 84,20,72
S_ s65,s59,2654208
Ta 224,%0,4104
Ta 226,%0,4616
Mvaa 76,24,108
S_ m0,s52,27520
Mvaa 76,20,68
Mvaa 68,24,104
L_ v6,28,s65,128
Mvaa 68,20,64
Mvaa 60,24,100
Mvaa 60,20,60
W_ 4
Mvaa 96,28,60
s_nop 0
Ta 20,v1,0
Ta 22,v1,512
Mvaa 96,32,100
buffer_load_dword a254, v10, s[28:31], s55 offen
Mvaa 104,28,64
Mvaa 104,32,104
s_nop 0
Ta 24,v1,4096
Ta 26,v1,4608
Mvaa 112,28,68
Mvaa 112,32,108
buffer_load_dword a254, v12, s[24:27], s63 offen offset:768
Mvaa 120,28,72
Mvaa 120,32,112
Mvaa 128,28,76
Mvaa 128,32,116
W_ 4
Mvaa 132,224,116
s_nop 0
Ta 28,v1,8
Ta 30,v1,520
Mvaa 132,36,76
Mvaa 124,224,112
Mvaa 124,36,72
s_nop 0
Ta 32,v1,4104
Ta 34,v1,4616
Mvaa 116,224,108
Mvaa 116,36,68
Mvaa 108,224,104
Mvaa 108,36,64
Mvaa 100,224,100
s_waitcnt vmcnt(15) lgkmcnt(0)
s_barrier
Mvaa 100,36,60
S_ s65,s59,2359296
Da 140,v2,20480
Mvaa 136,20,60
S_ m0,s52,-256
Ta 36,%0,8192
Ta 38,%0,8704
Mvaa 136,24,100
L_ v6,28,s65,256
Mvaa 144,20,64
S_ s65,s59,2457600
Da 180,v3,20480
Mvaa 144,24,104
S_ m0,s52,768
Ta 224,%0,12288
Ta 226,%0,12800
Mvaa 152,20,68
Mvaa 152,24,108
Da 144,v2,22528
Mvaa 160,20,72
L_ v6,28,s65,256
Mvaa 160,24,112
S_ s65,s59,2555904
Mvaa 168,20,76
S_ m0,s52,1792
Da 184,v3,22528
Mvaa 168,24,116
Mvaa 172,32,116
s_nop 0
Ta 20,%0,8200
Ta 22,%0,8712
Mvaa 172,28,76
Da 148,v2,24576
Mvaa 164,32,112
L_ v6,28,s65,256
Mvaa 164,28,72
S_ s65,s59,2654208
Ta 24,%0,12296
Ta 26,%0,12808
Mvaa 156,32,108
S_ m0,s52,2816
Da 188,v3,24576
Mvaa 156,28,68
Mvaa 148,32,104
L_ v6,28,s65,256
Mvaa 148,28,64
Da 152,v2,26624
Mvaa 140,32,100
Mvaa 140,28,60
W_ 9
Mvaa 176,36,60
s_nop 0
Ta 28,v1,8192
Ta 30,v1,8704
Mvaa 176,224,100
Da 192,v3,26624
Mvaa 184,36,64
Mvaa 184,224,104
s_nop 0
Ta 32,v1,12288
Ta 34,v1,12800
Mvaa 192,36,68
Da 156,v2,28672
Mvaa 192,224,108
Mvaa 200,36,72
Da 196,v3,28672
Mvaa 200,224,112
Mvaa 208,36,76
Mvaa 208,224,116
W_ 9
Mvaa 212,24,116
s_nop 0
Ta 36,v1,8200
Ta 38,v1,8712
Mvaa 212,20,76
Mvaa 204,24,112
Mvaa 204,20,72
s_nop 0
Ta 224,v1,12296
Ta 226,v1,12808
Mvaa 196,24,108
Mvaa 196,20,68
Mvaa 188,24,104
Mvaa 188,20,64
Mvaa 180,24,100
s_waitcnt vmcnt(15) lgkmcnt(0)
s_barrier
Mvaa 180,20,60
S_ s65,s59,3145728
Mvaa 216,28,60
S_ m0,s52,8192
Ta 20,%0,16384
Ta 22,%0,16896
Mvaa 216,32,100
L_ v6,28,s65,0
Mvaa 224,28,64
S_ s65,s59,3244032
Mvaa 224,32,104
S_ m0,s52,9216
Ta 24,%0,20480
Ta 26,%0,20992
Mvaa 232,28,68
Mvaa 232,32,108
L_ v6,28,s65,0
Mvaa 240,28,72
S_ s65,s59,3342336
Mvaa 240,32,112
S_ m0,s52,10240
Mvaa 248,28,76
Mvaa 248,32,116
Mvaa 252,224,116
s_nop 0
Ta 28,%0,16392
Ta 30,%0,16904
Mvaa 252,36,76
L_ v6,28,s65,0
Mvaa 244,224,112
S_ s65,s59,3440640
Mvaa 244,36,72
S_ m0,s52,11264
Ta 32,%0,20488
Ta 34,%0,21000
Mvaa 236,224,108
Mvaa 236,36,68
L_ v6,28,s65,0
Mvaa 228,224,104
S_ s65,s60,640
Mvaa 228,36,64
S_ m0,s50,20480
Mvaa 220,224,100
Mvaa 220,36,60
W_ 4
Mvaa 16,20,140
s_nop 0
Ta 36,v1,16384
Ta 38,v1,16896
Mvaa 16,24,180
L_ v4,24,s65,0
Mvaa 24,20,144
S_ s65,s60,10880
Mvaa 24,24,184
S_ m0,s50,21504
Ta 224,v1,20480
Ta 226,v1,20992
Mvaa 32,20,148
Mvaa 32,24,188
L_ v5,24,s65,0
Mvaa 40,20,152
S_ s65,s60,21120
Mvaa 40,24,192
S_ m0,s50,22528
Mvaa 48,20,156
Mvaa 48,24,196
W_ 4
Mvaa 52,32,196
s_nop 0
Ta 20,v1,16392
Ta 22,v1,16904
Mvaa 52,28,156
L_ v4,24,s65,0
Mvaa 44,32,192
S_ s65,s60,31360
Mvaa 44,28,152
S_ m0,s50,23552
Ta 24,v1,20488
Ta 26,v1,21000
Mvaa 36,32,188
Mvaa 36,28,148
L_ v5,24,s65,0
Mvaa 28,32,184
S_ s65,s60,41600
Mvaa 28,28,144
S_ m0,s50,24576
Mvaa 20,32,180
s_waitcnt vmcnt(14) lgkmcnt(0)
s_barrier
Mvaa 20,28,140
L_ v4,24,s65,0
Mvaa 56,36,140
S_ s65,s59,3145728
Ta 28,%0,24576
Ta 30,%0,25088
Mvaa 56,224,180
S_ m0,s52,16256
Mvaa 64,36,144
Mvaa 64,224,184
s_nop 0
Ta 32,%0,28672
Ta 34,%0,29184
Mvaa 72,36,148
L_ v6,28,s65,128
Mvaa 72,224,188
S_ s65,s59,3244032
Mvaa 80,36,152
S_ m0,s52,17280
Mvaa 80,224,192
Mvaa 88,36,156
L_ v6,28,s65,128
Mvaa 88,224,196
S_ s65,s59,3342336
Mvaa 92,24,196
S_ m0,s52,18304
Ta 36,%0,24584
Ta 38,%0,25096
Mvaa 92,20,156
S_ s55,s56,5505024
Mvaa 84,24,192
L_ v6,28,s65,128
Mvaa 84,20,152
S_ s65,s59,3440640
Ta 224,%0,28680
Ta 226,%0,29192
Mvaa 76,24,188
S_ m0,s52,19328
Mvaa 76,20,148
Mvaa 68,24,184
L_ v6,28,s65,128
Mvaa 68,20,144
Mvaa 60,24,180
Mvaa 60,20,140
W_ 4
Mvaa 96,28,140
s_nop 0
Ta 20,v1,24576
Ta 22,v1,25088
Mvaa 96,32,180
buffer_load_dword a254, v10, s[28:31], s55 offen
Mvaa 104,28,144
Mvaa 104,32,184
s_nop 0
Ta 24,v1,28672
Ta 26,v1,29184
Mvaa 112,28,148
Mvaa 112,32,188
buffer_load_dword a254, v12, s[24:27], s63 offen offset:896
Mvaa 120,28,152
Mvaa 120,32,192
Mvaa 128,28,156
Mvaa 128,32,196
W_ 4
Mvaa 132,224,196
s_nop 0
Ta 28,v1,24584
Ta 30,v1,25096
Mvaa 132,36,156
Mvaa 124,224,192
Mvaa 124,36,152
s_nop 0
Ta 32,v1,28680
Ta 34,v1,29192
Mvaa 116,224,188
Mvaa 116,36,148
Mvaa 108,224,184
Mvaa 108,36,144
Mvaa 100,224,180
s_waitcnt vmcnt(15) lgkmcnt(0)
s_barrier
Mvaa 100,36,140
S_ s65,s59,3145728
Da 60,v2,0
Mvaa 136,20,140
S_ m0,s52,24320
Ta 36,%0,0
Ta 38,%0,512
Mvaa 136,24,180
L_ v6,28,s65,256
Mvaa 144,20,144
S_ s65,s59,3244032
Da 100,v3,0
Mvaa 144,24,184
S_ m0,s52,25344
Ta 224,%0,4096
Ta 226,%0,4608
Mvaa 152,20,148
Mvaa 152,24,188
Da 64,v2,2048
Mvaa 160,20,152
L_ v6,28,s65,256
Mvaa 160,24,192
S_ s65,s59,3342336
Mvaa 168,20,156
S_ m0,s52,26368
Da 104,v3,2048
Mvaa 168,24,196
Mvaa 172,32,196
s_nop 0
Ta 20,%0,8
Ta 22,%0,520
Mvaa 172,28,156
Da 68,v2,4096
Mvaa 164,32,192
L_ v6,28,s65,256
Mvaa 164,28,152
S_ s65,s59,3440640
Ta 24,%0,4104
Ta 26,%0,4616
Mvaa 156,32,188
S_ m0,s52,27392
Da 108,v3,4096
Mvaa 156,28,148
Mvaa 148,32,184
L_ v6,28,s65,256
Mvaa 148,28,144
Da 72,v2,6144
Mvaa 140,32,180
Mvaa 140,28,140
W_ 9
Mvaa 176,36,140
s_nop 0
Ta 28,v1,0
Ta 30,v1,512
Mvaa 176,224,180
Da 112,v3,6144
Mvaa 184,36,144
Mvaa 184,224,184
s_nop 0
Ta 32,v1,4096
Ta 34,v1,4608
Mvaa 192,36,148
Da 76,v2,8192
Mvaa 192,224,188
Mvaa 200,36,152
Da 116,v3,8192
Mvaa 200,224,192
Mvaa 208,36,156
Mvaa 208,224,196
W_ 9
Mvaa 212,24,196
s_nop 0
Ta 36,v1,8
Ta 38,v1,520
Mvaa 212,20,156
Mvaa 204,24,192
Mvaa 204,20,152
s_nop 0
Ta 224,v1,4104
Ta 226,v1,4616
Mvaa 196,24,188
Mvaa 196,20,148
Mvaa 188,24,184
Mvaa 188,20,144
Mvaa 180,24,180
s_waitcnt vmcnt(15) lgkmcnt(0)
s_barrier
Mvaa 180,20,140
S_ s65,s59,3932160
Mvaa 216,28,140
S_ m0,s52,0
Ta 20,%0,8192
Ta 22,%0,8704
Mvaa 216,32,180
L_ v6,28,s65,0
Mvaa 224,28,144
S_ s65,s59,4030464
Mvaa 224,32,184
S_ m0,s52,1024
Ta 24,%0,12288
Ta 26,%0,12800
Mvaa 232,28,148
Mvaa 232,32,188
L_ v6,28,s65,0
Mvaa 240,28,152
S_ s65,s59,4128768
Mvaa 240,32,192
S_ m0,s52,2048
Mvaa 248,28,156
Mvaa 248,32,196
Mvaa 252,224,196
s_nop 0
Ta 28,%0,8200
Ta 30,%0,8712
Mvaa 252,36,156
L_ v6,28,s65,0
Mvaa 244,224,192
S_ s65,s59,4227072
Mvaa 244,36,152
S_ m0,s52,3072
Ta 32,%0,12296
Ta 34,%0,12808
Mvaa 236,224,188
Mvaa 236,36,148
L_ v6,28,s65,0
Mvaa 228,224,184
S_ s65,s60,768
Mvaa 228,36,144
S_ m0,s50,0
Mvaa 220,224,180
Mvaa 220,36,140
W_ 4
Mvaa 16,20,60
s_nop 0
Ta 36,v1,8192
Ta 38,v1,8704
Mvaa 16,24,100
L_ v4,24,s65,0
Mvaa 24,20,64
S_ s65,s60,11008
R_ v13,a248
Mvaa 24,24,104
S_ m0,s50,1024
Ta 224,v1,12288
Ta 226,v1,12800
Mvaa 32,20,68
v_min_u32 v13, 240, v13
Mvaa 32,24,108
L_ v5,24,s65,0
Mvaa 40,20,72
S_ s65,s60,21248
v_lshlrev_b32 v13, 3, v13
Mvaa 40,24,112
S_ m0,s50,2048
Mvaa 48,20,76
Mvaa 48,24,116
W_ 4
Mvaa 52,32,116
s_nop 0
Ta 20,v1,8200
Ta 22,v1,8712
Mvaa 52,28,76
L_ v4,24,s65,0
Mvaa 44,32,112
S_ s65,s60,31488
Mvaa 44,28,72
S_ m0,s50,3072
Ta 24,v1,12296
Ta 26,v1,12808
Mvaa 36,32,108
Mvaa 36,28,68
L_ v5,24,s65,0
Mvaa 28,32,104
S_ s65,s60,41728
Mvaa 28,28,64
S_ m0,s50,4096
Mvaa 20,32,100
s_waitcnt vmcnt(14) lgkmcnt(0)
s_barrier
Mvaa 20,28,60
L_ v4,24,s65,0
Mvaa 56,36,60
S_ s65,s59,3932160
Ta 28,%0,16384
Ta 30,%0,16896
Mvaa 56,224,100
S_ m0,s52,8064
Mvaa 64,36,64
Mvaa 64,224,104
s_nop 0
Ta 32,%0,20480
Ta 34,%0,20992
Mvaa 72,36,68
L_ v6,28,s65,128
Mvaa 72,224,108
S_ s65,s59,4030464
Mvaa 80,36,72
S_ m0,s52,9088
Mvaa 80,224,112
Mvaa 88,36,76
L_ v6,28,s65,128
Mvaa 88,224,116
S_ s65,s59,4128768
Mvaa 92,24,116
S_ m0,s52,10112
Ta 36,%0,16392
Ta 38,%0,16904
Mvaa 92,20,76
S_ s55,s56,6291456
Mvaa 84,24,112
L_ v6,28,s65,128
Mvaa 84,20,72
S_ s65,s59,4227072
Ta 224,%0,20488
Ta 226,%0,21000
Mvaa 76,24,108
S_ m0,s52,11136
Mvaa 76,20,68
Mvaa 68,24,104
L_ v6,28,s65,128
Mvaa 68,20,64
Mvaa 60,24,100
Mvaa 60,20,60
W_ 4
Mvaa 96,28,60
s_nop 0
Ta 20,v1,16384
Ta 22,v1,16896
Mvaa 96,32,100
buffer_load_dword a220, v13, s[44:47], s66 offen
Mvaa 104,28,64
Mvaa 104,32,104
s_nop 0
Ta 24,v1,20480
Ta 26,v1,20992
Mvaa 112,28,68
Mvaa 112,32,108
buffer_load_dword a254, v10, s[28:31], s55 offen
Mvaa 120,28,72
Mvaa 120,32,112
Mvaa 128,28,76
Mvaa 128,32,116
W_ 4
Mvaa 132,224,116
s_nop 0
Ta 28,v1,16392
Ta 30,v1,16904
Mvaa 132,36,76
buffer_load_dword a254, v12, s[24:27], s63 offen offset:1024
Mvaa 124,224,112
Mvaa 124,36,72
s_nop 0
Ta 32,v1,20488
Ta 34,v1,21000
Mvaa 116,224,108
Mvaa 116,36,68
Mvaa 108,224,104
Mvaa 108,36,64
Mvaa 100,224,100
s_waitcnt vmcnt(16) lgkmcnt(0)
s_barrier
Mvaa 100,36,60
S_ s65,s59,3932160
Da 140,v2,20480
Mvaa 136,20,60
S_ m0,s52,16128
Ta 36,%0,24576
Ta 38,%0,25088
Mvaa 136,24,100
L_ v6,28,s65,256
Mvaa 144,20,64
S_ s65,s59,4030464
Da 180,v3,20480
Mvaa 144,24,104
S_ m0,s52,17152
Ta 224,%0,28672
Ta 226,%0,29184
Mvaa 152,20,68
Mvaa 152,24,108
Da 144,v2,22528
Mvaa 160,20,72
L_ v6,28,s65,256
Mvaa 160,24,112
S_ s65,s59,4128768
Mvaa 168,20,76
S_ m0,s52,18176
Da 184,v3,22528
Mvaa 168,24,116
Mvaa 172,32,116
s_nop 0
Ta 20,%0,24584
Ta 22,%0,25096
Mvaa 172,28,76
Da 148,v2,24576
Mvaa 164,32,112
L_ v6,28,s65,256
Mvaa 164,28,72
S_ s65,s59,4227072
Ta 24,%0,28680
Ta 26,%0,29192
Mvaa 156,32,108
S_ m0,s52,19200
Da 188,v3,24576
Mvaa 156,28,68
Mvaa 148,32,104
L_ v6,28,s65,256
Mvaa 148,28,64
Da 152,v2,26624
Mvaa 140,32,100
Mvaa 140,28,60
W_ 9
Mvaa 176,36,60
s_nop 0
Ta 28,v1,24576
Ta 30,v1,25088
Mvaa 176,224,100
Da 192,v3,26624
Mvaa 184,36,64
Mvaa 184,224,104
s_nop 0
Ta 32,v1,28672
Ta 34,v1,29184
Mvaa 192,36,68
Da 156,v2,28672
Mvaa 192,224,108
Mvaa 200,36,72
Da 196,v3,28672
Mvaa 200,224,112
Mvaa 208,36,76
Mvaa 208,224,116
W_ 9
Mvaa 212,24,116
s_nop 0
Ta 36,v1,24584
Ta 38,v1,25096
Mvaa 212,20,76
Mvaa 204,24,112
Mvaa 204,20,72
s_nop 0
Ta 224,v1,28680
Ta 226,v1,29192
Mvaa 196,24,108
Mvaa 196,20,68
Mvaa 188,24,104
Mvaa 188,20,64
Mvaa 180,24,100
s_waitcnt vmcnt(16) lgkmcnt(0)
s_barrier
Mvaa 180,20,60
S_ s65,s59,4718592
Mvaa 216,28,60
S_ m0,s52,24576
Ta 20,%0,0
Ta 22,%0,512
Mvaa 216,32,100
L_ v6,28,s65,0
Mvaa 224,28,64
S_ s65,s59,4816896
Mvaa 224,32,104
S_ m0,s52,25600
Ta 24,%0,4096
Ta 26,%0,4608
Mvaa 232,28,68
Mvaa 232,32,108
L_ v6,28,s65,0
Mvaa 240,28,72
S_ s65,s59,4915200
Mvaa 240,32,112
S_ m0,s52,26624
Mvaa 248,28,76
Mvaa 248,32,116
Mvaa 252,224,116
s_nop 0
Ta 28,%0,8
Ta 30,%0,520
Mvaa 252,36,76
L_ v6,28,s65,0
Mvaa 244,224,112
S_ s65,s59,5013504
Mvaa 244,36,72
S_ m0,s52,27648
Ta 32,%0,4104
Ta 34,%0,4616
Mvaa 236,224,108
Mvaa 236,36,68
L_ v6,28,s65,0
Mvaa 228,224,104
S_ s65,s60,896
Mvaa 228,36,64
S_ m0,s50,20480
Mvaa 220,224,100
Mvaa 220,36,60
W_ 4
Mvaa 16,20,140
s_nop 0
Ta 36,v1,0
Ta 38,v1,512
Mvaa 16,24,180
L_ v4,24,s65,0
Mvaa 24,20,144
S_ s65,s60,11136
Mvaa 24,24,184
S_ m0,s50,21504
Ta 224,v1,4096
Ta 226,v1,4608
Mvaa 32,20,148
Mvaa 32,24,188
L_ v5,24,s65,0
Mvaa 40,20,152
S_ s65,s60,21376
Mvaa 40,24,192
S_ m0,s50,22528
Mvaa 48,20,156
Mvaa 48,24,196
W_ 4
Mvaa 52,32,196
s_nop 0
Ta 20,v1,8
Ta 22,v1,520
Mvaa 52,28,156
L_ v4,24,s65,0
Mvaa 44,32,192
S_ s65,s60,31616
Mvaa 44,28,152
S_ m0,s50,23552
Ta 24,v1,4104
Ta 26,v1,4616
Mvaa 36,32,188
Mvaa 36,28,148
L_ v5,24,s65,0
Mvaa 28,32,184
S_ s65,s60,41856
Mvaa 28,28,144
S_ m0,s50,24576
Mvaa 20,32,180
s_waitcnt vmcnt(15) lgkmcnt(0)
s_barrier
Mvaa 20,28,140
L_ v4,24,s65,0
Mvaa 56,36,140
S_ s65,s59,4718592
Ta 28,%0,8192
Ta 30,%0,8704
Mvaa 56,224,180
S_ m0,s52,-128
Mvaa 64,36,144
Mvaa 64,224,184
s_nop 0
Ta 32,%0,12288
Ta 34,%0,12800
Mvaa 72,36,148
L_ v6,28,s65,128
Mvaa 72,224,188
S_ s65,s59,4816896
Mvaa 80,36,152
S_ m0,s52,896
Mvaa 80,224,192
Mvaa 88,36,156
L_ v6,28,s65,128
Mvaa 88,224,196
S_ s65,s59,4915200
Mvaa 92,24,196
S_ m0,s52,1920
Ta 36,%0,8200
Ta 38,%0,8712
Mvaa 92,20,156
S_ s55,s56,7077888
Mvaa 84,24,192
L_ v6,28,s65,128
Mvaa 84,20,152
S_ s65,s59,5013504
Ta 224,%0,12296
Ta 226,%0,12808
Mvaa 76,24,188
S_ m0,s52,2944
Mvaa 76,20,148
Mvaa 68,24,184
L_ v6,28,s65,128
Mvaa 68,20,144
Mvaa 60,24,180
Mvaa 60,20,140
W_ 4
Mvaa 96,28,140
s_nop 0
Ta 20,v1,8192
Ta 22,v1,8704
Mvaa 96,32,180
buffer_load_dword a254, v10, s[28:31], s55 offen
Mvaa 104,28,144
Mvaa 104,32,184
s_nop 0
Ta 24,v1,12288
Ta 26,v1,12800
Mvaa 112,28,148
Mvaa 112,32,188
buffer_load_dword a254, v12, s[24:27], s63 offen offset:1152
Mvaa 120,28,152
Mvaa 120,32,192
Mvaa 128,28,156
Mvaa 128,32,196
W_ 4
Mvaa 132,224,196
s_nop 0
Ta 28,v1,8200
Ta 30,v1,8712
Mvaa 132,36,156
Mvaa 124,224,192
Mvaa 124,36,152
s_nop 0
Ta 32,v1,12296
Ta 34,v1,12808
Mvaa 116,224,188
Mvaa 116,36,148
Mvaa 108,224,184
Mvaa 108,36,144
Mvaa 100,224,180
s_waitcnt vmcnt(15) lgkmcnt(0)
s_barrier
Mvaa 100,36,140
S_ s65,s59,4718592
Da 60,v2,0
Mvaa 136,20,140
S_ m0,s52,7936
Ta 36,%0,16384
Ta 38,%0,16896
Mvaa 136,24,180
L_ v6,28,s65,256
Mvaa 144,20,144
S_ s65,s59,4816896
Da 100,v3,0
Mvaa 144,24,184
S_ m0,s52,8960
Ta 224,%0,20480
Ta 226,%0,20992
Mvaa 152,20,148
Mvaa 152,24,188
Da 64,v2,2048
Mvaa 160,20,152
L_ v6,28,s65,256
Mvaa 160,24,192
S_ s65,s59,4915200
Mvaa 168,20,156
S_ m0,s52,9984
Da 104,v3,2048
Mvaa 168,24,196
Mvaa 172,32,196
s_nop 0
Ta 20,%0,16392
Ta 22,%0,16904
Mvaa 172,28,156
Da 68,v2,4096
Mvaa 164,32,192
L_ v6,28,s65,256
Mvaa 164,28,152
S_ s65,s59,5013504
Ta 24,%0,20488
Ta 26,%0,21000
Mvaa 156,32,188
S_ m0,s52,11008
Da 108,v3,4096
Mvaa 156,28,148
Mvaa 148,32,184
L_ v6,28,s65,256
Mvaa 148,28,144
Da 72,v2,6144
Mvaa 140,32,180
Mvaa 140,28,140
W_ 9
Mvaa 176,36,140
s_nop 0
Ta 28,v1,16384
Ta 30,v1,16896
Mvaa 176,224,180
Da 112,v3,6144
Mvaa 184,36,144
Mvaa 184,224,184
s_nop 0
Ta 32,v1,20480
Ta 34,v1,20992
Mvaa 192,36,148
Da 76,v2,8192
Mvaa 192,224,188
Mvaa 200,36,152
Da 116,v3,8192
)A"
R"A(Mvaa 200,224,192
Mvaa 208,36,156
Mvaa 208,224,196
W_ 9
Mvaa 212,24,196
s_nop 0
Ta 36,v1,16392
Ta 38,v1,16904
Mvaa 212,20,156
Mvaa 204,24,192
Mvaa 204,20,152
s_nop 0
Ta 224,v1,20488
Ta 226,v1,21000
Mvaa 196,24,188
Mvaa 196,20,148
Mvaa 188,24,184
Mvaa 188,20,144
Mvaa 180,24,180
s_waitcnt vmcnt(15) lgkmcnt(0)
s_barrier
Mvaa 180,20,140
S_ s65,s59,5505024
Mvaa 216,28,140
S_ m0,s52,16384
Ta 20,%0,24576
Ta 22,%0,25088
Mvaa 216,32,180
L_ v6,28,s65,0
Mvaa 224,28,144
S_ s65,s59,5603328
Mvaa 224,32,184
S_ m0,s52,17408
Ta 24,%0,28672
Ta 26,%0,29184
Mvaa 232,28,148
Mvaa 232,32,188
L_ v6,28,s65,0
Mvaa 240,28,152
S_ s65,s59,5701632
Mvaa 240,32,192
S_ m0,s52,18432
Mvaa 248,28,156
Mvaa 248,32,196
Mvaa 252,224,196
s_nop 0
Ta 28,%0,24584
Ta 30,%0,25096
Mvaa 252,36,156
L_ v6,28,s65,0
Mvaa 244,224,192
S_ s65,s59,5799936
Mvaa 244,36,152
S_ m0,s52,19456
Ta 32,%0,28680
Ta 34,%0,29192
Mvaa 236,224,188
Mvaa 236,36,148
L_ v6,28,s65,0
Mvaa 228,224,184
S_ s65,s60,1024
Mvaa 228,36,144
S_ m0,s50,0
Mvaa 220,224,180
Mvaa 220,36,140
W_ 4
Mvaa 16,20,60
s_nop 0
Ta 36,v1,24576
Ta 38,v1,25088
Mvaa 16,24,100
L_ v4,24,s65,0
Mvaa 24,20,64
S_ s65,s60,11264
Mvaa 24,24,104
S_ m0,s50,1024
Ta 224,v1,28672
Ta 226,v1,29184
Mvaa 32,20,68
Mvaa 32,24,108
L_ v5,24,s65,0
Mvaa 40,20,72
S_ s65,s60,21504
Mvaa 40,24,112
S_ m0,s50,2048
Mvaa 48,20,76
Mvaa 48,24,116
W_ 4
Mvaa 52,32,116
s_nop 0
Ta 20,v1,24584
Ta 22,v1,25096
Mvaa 52,28,76
L_ v4,24,s65,0
Mvaa 44,32,112
S_ s65,s60,31744
Mvaa 44,28,72
S_ m0,s50,3072
Ta 24,v1,28680
Ta 26,v1,29192
Mvaa 36,32,108
Mvaa 36,28,68
L_ v5,24,s65,0
Mvaa 28,32,104
S_ s65,s60,41984
Mvaa 28,28,64
S_ m0,s50,4096
Mvaa 20,32,100
s_waitcnt vmcnt(14) lgkmcnt(0)
s_barrier
Mvaa 20,28,60
L_ v4,24,s65,0
Mvaa 56,36,60
S_ s65,s59,5505024
Ta 28,%0,0
Ta 30,%0,512
Mvaa 56,224,100
S_ m0,s52,24448
Mvaa 64,36,64
Mvaa 64,224,104
s_nop 0
Ta 32,%0,4096
Ta 34,%0,4608
Mvaa 72,36,68
L_ v6,28,s65,128
Mvaa 72,224,108
S_ s65,s59,5603328
Mvaa 80,36,72
S_ m0,s52,25472
Mvaa 80,224,112
Mvaa 88,36,76
L_ v6,28,s65,128
Mvaa 88,224,116
S_ s65,s59,5701632
Mvaa 92,24,116
S_ m0,s52,26496
Ta 36,%0,8
Ta 38,%0,520
Mvaa 92,20,76
Mvaa 84,24,112
L_ v6,28,s65,128
Mvaa 84,20,72
S_ s65,s59,5799936
Ta 224,%0,4104
Ta 226,%0,4616
Mvaa 76,24,108
S_ m0,s52,27520
Mvaa 76,20,68
Mvaa 68,24,104
L_ v6,28,s65,128
Mvaa 68,20,64
Mvaa 60,24,100
Mvaa 60,20,60
W_ 4
Mvaa 96,28,60
s_nop 0
Ta 20,v1,0
Ta 22,v1,512
Mvaa 96,32,100
Mvaa 104,28,64
Mvaa 104,32,104
s_nop 0
Ta 24,v1,4096
Ta 26,v1,4608
Mvaa 112,28,68
Mvaa 112,32,108
Mvaa 120,28,72
Mvaa 120,32,112
Mvaa 128,28,76
Mvaa 128,32,116
W_ 4
Mvaa 132,224,116
s_nop 0
Ta 28,v1,8
Ta 30,v1,520
Mvaa 132,36,76
Mvaa 124,224,112
Mvaa 124,36,72
s_nop 0
Ta 32,v1,4104
Ta 34,v1,4616
Mvaa 116,224,108
Mvaa 116,36,68
Mvaa 108,224,104
Mvaa 108,36,64
Mvaa 100,224,100
s_waitcnt vmcnt(13) lgkmcnt(0)
s_barrier
Mvaa 100,36,60
S_ s65,s59,5505024
Da 140,v2,20480
Mvaa 136,20,60
S_ m0,s52,-256
Ta 36,%0,8192
Ta 38,%0,8704
Mvaa 136,24,100
L_ v6,28,s65,256
Mvaa 144,20,64
S_ s65,s59,5603328
Da 180,v3,20480
Mvaa 144,24,104
S_ m0,s52,768
Ta 224,%0,12288
Ta 226,%0,12800
Mvaa 152,20,68
Mvaa 152,24,108
Da 144,v2,22528
Mvaa 160,20,72
L_ v6,28,s65,256
Mvaa 160,24,112
S_ s65,s59,5701632
Mvaa 168,20,76
S_ m0,s52,1792
Da 184,v3,22528
Mvaa 168,24,116
Mvaa 172,32,116
s_nop 0
Ta 20,%0,8200
Ta 22,%0,8712
Mvaa 172,28,76
Da 148,v2,24576
Mvaa 164,32,112
L_ v6,28,s65,256
Mvaa 164,28,72
S_ s65,s59,5799936
Ta 24,%0,12296
Ta 26,%0,12808
Mvaa 156,32,108
S_ m0,s52,2816
Da 188,v3,24576
Mvaa 156,28,68
Mvaa 148,32,104
L_ v6,28,s65,256
Mvaa 148,28,64
Da 152,v2,26624
Mvaa 140,32,100
Mvaa 140,28,60
W_ 9
Mvaa 176,36,60
s_nop 0
Ta 28,v1,8192
Ta 30,v1,8704
Mvaa 176,224,100
Da 192,v3,26624
Mvaa 184,36,64
Mvaa 184,224,104
s_nop 0
Ta 32,v1,12288
Ta 34,v1,12800
Mvaa 192,36,68
Da 156,v2,28672
Mvaa 192,224,108
Mvaa 200,36,72
Da 196,v3,28672
Mvaa 200,224,112
Mvaa 208,36,76
Mvaa 208,224,116
W_ 9
Mvaa 212,24,116
s_nop 0
Ta 36,v1,8200
Ta 38,v1,8712
Mvaa 212,20,76
Mvaa 204,24,112
Mvaa 204,20,72
s_nop 0
Ta 224,v1,12296
Ta 226,v1,12808
Mvaa 196,24,108
Mvaa 196,20,68
Mvaa 188,24,104
Mvaa 188,20,64
Mvaa 180,24,100
s_waitcnt vmcnt(13) lgkmcnt(0)
s_barrier
Mvaa 180,20,60
S_ s65,s59,6291456
Mvaa 216,28,60
S_ m0,s52,7936
Ta 20,%0,16384
Ta 22,%0,16896
Mvaa 216,32,100
L_ v6,28,s65,256
Mvaa 224,28,64
S_ s65,s59,6389760
Mvaa 224,32,104
S_ m0,s52,8960
Ta 24,%0,20480
Ta 26,%0,20992
Mvaa 232,28,68
Mvaa 232,32,108
L_ v6,28,s65,256
Mvaa 240,28,72
S_ s65,s59,6488064
Mvaa 240,32,112
S_ m0,s52,9984
Mvaa 248,28,76
Mvaa 248,32,116
Mvaa 252,224,116
s_nop 0
Ta 28,%0,16392
Ta 30,%0,16904
Mvaa 252,36,76
L_ v6,28,s65,256
Mvaa 244,224,112
S_ s65,s59,6586368
Mvaa 244,36,72
S_ m0,s52,11008
Ta 32,%0,20488
Ta 34,%0,21000
Mvaa 236,224,108
Mvaa 236,36,68
L_ v6,28,s65,256
Mvaa 228,224,104
S_ s65,s60,1152
Mvaa 228,36,64
S_ m0,s50,20480
Mvaa 220,224,100
Mvaa 220,36,60
W_ 4
Mvaa 16,20,140
s_nop 0
Ta 36,v1,16384
Ta 38,v1,16896
Mvaa 16,24,180
L_ v4,24,s65,0
Mvaa 24,20,144
S_ s65,s60,11392
Mvaa 24,24,184
S_ m0,s50,21504
Ta 224,v1,20480
Ta 226,v1,20992
Mvaa 32,20,148
Mvaa 32,24,188
L_ v5,24,s65,0
Mvaa 40,20,152
S_ s65,s60,21632
Mvaa 40,24,192
S_ m0,s50,22528
Mvaa 48,20,156
Mvaa 48,24,196
W_ 4
Mvaa 52,32,196
s_nop 0
Ta 20,v1,16392
Ta 22,v1,16904
Mvaa 52,28,156
L_ v4,24,s65,0
Mvaa 44,32,192
S_ s65,s60,31872
Mvaa 44,28,152
S_ m0,s50,23552
Ta 24,v1,20488
Ta 26,v1,21000
Mvaa 36,32,188
Mvaa 36,28,148
L_ v5,24,s65,0
Mvaa 28,32,184
S_ s65,s60,42112
Mvaa 28,28,144
S_ m0,s50,24576
Mvaa 20,32,180
s_waitcnt vmcnt(12) lgkmcnt(0)
s_barrier
Mvaa 20,28,140
L_ v4,24,s65,0
Mvaa 56,36,140
S_ s65,s59,7077888
Ta 28,%0,24576
Ta 30,%0,25088
Mvaa 56,224,180
S_ m0,s52,16128
Mvaa 64,36,144
Mvaa 64,224,184
s_nop 0
Ta 32,%0,28672
Ta 34,%0,29184
Mvaa 72,36,148
L_ v6,28,s65,256
Mvaa 72,224,188
S_ s65,s59,7176192
Mvaa 80,36,152
S_ m0,s52,17152
Mvaa 80,224,192
Mvaa 88,36,156
L_ v6,28,s65,256
Mvaa 88,224,196
S_ s65,s59,7274496
Mvaa 92,24,196
S_ m0,s52,18176
Ta 36,%0,24584
Ta 38,%0,25096
Mvaa 92,20,156
Mvaa 84,24,192
L_ v6,28,s65,256
Mvaa 84,20,152
S_ s65,s59,7372800
Ta 224,%0,28680
Ta 226,%0,29192
Mvaa 76,24,188
S_ m0,s52,19200
Mvaa 76,20,148
Mvaa 68,24,184
L_ v6,28,s65,256
Mvaa 68,20,144
Mvaa 60,24,180
Mvaa 60,20,140
W_ 4
Mvaa 96,28,140
s_nop 0
Ta 20,v1,24576
Ta 22,v1,25088
Mvaa 96,32,180
Mvaa 104,28,144
Mvaa 104,32,184
s_nop 0
Ta 24,v1,28672
Ta 26,v1,29184
Mvaa 112,28,148
Mvaa 112,32,188
Mvaa 120,28,152
Mvaa 120,32,192
Mvaa 128,28,156
Mvaa 128,32,196
W_ 4
Mvaa 132,224,196
s_nop 0
Ta 28,v1,24584
Ta 30,v1,25096
Mvaa 132,36,156
Mvaa 124,224,192
Mvaa 124,36,152
s_nop 0
Ta 32,v1,28680
Ta 34,v1,29192
Mvaa 116,224,188
Mvaa 116,36,148
Mvaa 108,224,184
Mvaa 108,36,144
Mvaa 100,224,180
s_waitcnt vmcnt(13) lgkmcnt(0)
s_barrier
Mvaa 100,36,140
S_ s65,s59,6291456
Da 60,v2,0
Mvaa 136,20,140
S_ m0,s52,24448
Ta 36,%0,0
Ta 38,%0,512
Mvaa 136,24,180
L_ v6,28,s65,128
Mvaa 144,20,144
S_ s65,s59,6389760
Da 100,v3,0
Mvaa 144,24,184
S_ m0,s52,25472
Ta 224,%0,4096
Ta 226,%0,4608
Mvaa 152,20,148
Mvaa 152,24,188
Da 64,v2,2048
Mvaa 160,20,152
L_ v6,28,s65,128
Mvaa 160,24,192
S_ s65,s59,6488064
Mvaa 168,20,156
S_ m0,s52,26496
Da 104,v3,2048
Mvaa 168,24,196
Mvaa 172,32,196
s_nop 0
Ta 20,%0,8
Ta 22,%0,520
Mvaa 172,28,156
Da 68,v2,4096
Mvaa 164,32,192
L_ v6,28,s65,128
Mvaa 164,28,152
S_ s65,s59,6586368
Ta 24,%0,4104
Ta 26,%0,4616
Mvaa 156,32,188
S_ m0,s52,27520
Da 108,v3,4096
Mvaa 156,28,148
Mvaa 148,32,184
L_ v6,28,s65,128
Mvaa 148,28,144
Da 72,v2,6144
Mvaa 140,32,180
Mvaa 140,28,140
W_ 9
Mvaa 176,36,140
s_nop 0
Ta 28,v1,0
Ta 30,v1,512
Mvaa 176,224,180
Da 112,v3,6144
Mvaa 184,36,144
Mvaa 184,224,184
s_nop 0
Ta 32,v1,4096
Ta 34,v1,4608
Mvaa 192,36,148
Da 76,v2,8192
Mvaa 192,224,188
Mvaa 200,36,152
Da 116,v3,8192
Mvaa 200,224,192
Mvaa 208,36,156
Mvaa 208,224,196
W_ 9
Mvaa 212,24,196
s_nop 0
Ta 36,v1,8
Ta 38,v1,520
Mvaa 212,20,156
Mvaa 204,24,192
Mvaa 204,20,152
s_nop 0
Ta 224,v1,4104
Ta 226,v1,4616
Mvaa 196,24,188
Mvaa 196,20,148
Mvaa 188,24,184
Mvaa 188,20,144
Mvaa 180,24,180
s_waitcnt vmcnt(8) lgkmcnt(0)
s_barrier
Mvaa 180,20,140
S_ s65,s59,7077888
Mvaa 216,28,140
S_ m0,s52,-128
Ta 20,v1,8200
Ta 22,v1,8712
Mvaa 216,32,180
L_ v6,28,s65,128
Mvaa 224,28,144
S_ s65,s59,7176192
Mvaa 224,32,184
S_ m0,s52,896
Ta 24,v1,12296
Ta 26,v1,12808
Mvaa 232,28,148
Mvaa 232,32,188
L_ v6,28,s65,128
Mvaa 240,28,152
S_ s65,s59,7274496
Mvaa 240,32,192
S_ m0,s52,1920
Mvaa 248,28,156
Mvaa 248,32,196
Mvaa 252,224,196
s_nop 0
Ta 28,v1,8192
Ta 30,v1,8704
Mvaa 252,36,156
L_ v6,28,s65,128
Mvaa 244,224,192
S_ s65,s59,7372800
Mvaa 244,36,152
S_ m0,s52,2944
Ta 32,v1,12288
Ta 34,v1,12800
Mvaa 236,224,188
Mvaa 236,36,148
L_ v6,28,s65,128
Mvaa 228,224,184
S_ s65,s60,0
Mvaa 228,36,144
S_ m0,s50,0
Mvaa 220,224,180
Mvaa 220,36,140
W_ 4
Mvaa 220,20,60
s_nop 0
Ta 36,%0,8200
Ta 38,%0,8712
Mvaa 220,24,100
Da 140,v2,20480
Mvaa 228,20,64
L_ v4,24,s65,0
Mvaa 228,24,104
S_ s65,s60,10240
Ta 224,%0,12296
Ta 226,%0,12808
Mvaa 236,20,68
S_ m0,s50,1024
Da 180,v3,20480
Mvaa 236,24,108
Mvaa 244,20,72
Da 144,v2,22528
Mvaa 244,24,112
L_ v5,24,s65,0
Mvaa 252,20,76
S_ s65,s60,20480
Mvaa 252,24,116
W_ 7
Da 184,v3,22528
S_ m0,s50,2048
Mvaa 248,32,116
s_nop 0
Ta 20,%0,8192
Ta 22,%0,8704
Mvaa 248,28,76
L_ v4,24,s65,0
Mvaa 240,32,112
S_ s65,s60,30720
Da 148,v2,24576
Mvaa 240,28,72
S_ m0,s50,3072
Ta 24,%0,12288
Ta 26,%0,12800
Mvaa 232,32,108
Mvaa 232,28,68
Da 188,v3,24576
Mvaa 224,32,104
L_ v5,24,s65,0
Mvaa 224,28,64
S_ s65,s60,40960
Mvaa 216,32,100
s_waitcnt vmcnt(12) lgkmcnt(0)
s_barrier
Da 152,v2,26624
Mvaa 216,28,60
S_ m0,s50,4096
Mvaa 180,36,60
s_nop 0
Ta 28,v1,16392
Ta 30,v1,16904
Mvaa 180,224,100
Da 192,v3,26624
Mvaa 188,36,64
L_ v4,24,s65,0
Mvaa 188,224,104
S_ s65,s59,6291456
Ta 32,v1,20488
Ta 34,v1,21000
Mvaa 196,36,68
S_ m0,s52,8192
Da 156,v2,28672
Mvaa 196,224,108
Mvaa 204,36,72
L_ v6,28,s65,0
Mvaa 204,224,112
S_ s65,s59,6389760
Da 196,v3,28672
Mvaa 212,36,76
S_ m0,s52,9216
Mvaa 212,224,116
Mvaa 208,24,116
s_nop 0
Ta 36,v1,16384
Ta 38,v1,16896
Mvaa 208,20,76
L_ v6,28,s65,0
Mvaa 200,24,112
S_ s65,s59,6488064
Mvaa 200,20,72
S_ m0,s52,10240
Ta 224,v1,20480
Ta 226,v1,20992
Mvaa 192,24,108
Mvaa 192,20,68
L_ v6,28,s65,0
Mvaa 184,24,104
S_ s65,s59,6586368
Mvaa 184,20,64
S_ m0,s52,11264
Mvaa 176,24,100
Mvaa 176,20,60
W_ 6
Mvaa 220,28,140
s_nop 0
Ta 20,%0,16392
Ta 22,%0,16904
Mvaa 220,32,180
L_ v6,28,s65,0
Mvaa 228,28,144
Mvaa 228,32,184
s_nop 0
Ta 24,%0,20488
Ta 26,%0,21000
Mvaa 236,28,148
Mvaa 236,32,188
Mvaa 244,28,152
Mvaa 244,32,192
W_ 9
Mvaa 252,28,156
W_ 8
Mvaa 252,32,196
W_ 4
Mvaa 248,224,196
s_nop 0
Ta 28,%0,16384
Ta 30,%0,16896
Mvaa 248,36,156
Mvaa 240,224,192
Mvaa 240,36,152
s_nop 0
Ta 32,%0,20480
Ta 34,%0,20992
Mvaa 232,224,188
Mvaa 232,36,148
Mvaa 224,224,184
Mvaa 224,36,144
Mvaa 216,224,180
s_waitcnt vmcnt(13) lgkmcnt(0)
s_barrier
Mvaa 216,36,140
S_ s65,s59,7077888
Mvaa 180,20,140
S_ m0,s52,16384
Ta 36,v1,24584
Ta 38,v1,25096
Mvaa 180,24,180
L_ v6,28,s65,0
Mvaa 188,20,144
S_ s65,s59,7176192
Mvaa 188,24,184
S_ m0,s52,17408
Ta 224,v1,28680
Ta 226,v1,29192
Mvaa 196,20,148
U_ v248,s48,v248
U_ v249,s48,v249
Mvaa 196,24,188
L_ v6,28,s65,0
Mvaa 204,20,152
S_ s65,s59,7274496
U_ v250,s48,v250
U_ v251,s48,v251
Mvaa 204,24,192
S_ m0,s52,18432
U_ v252,s48,v252
U_ v253,s48,v253
Mvaa 212,20,156
U_ v254,s48,v254
U_ v255,s48,v255
Mvaa 212,24,196
C_ 255,254,255
C_ 254,252,253
Mvaa 208,32,196
s_nop 0
Ta 20,v1,24576
Ta 22,v1,25088
Mvaa 208,28,156
L_ v6,28,s65,0
Mvaa 200,32,192
S_ s65,s59,7372800
C_ 253,250,251
C_ 252,248,249
Mvaa 200,28,152
S_ m0,s52,19456
Ta 24,v1,28672
Ta 26,v1,29184
Mvaa 192,32,188
X_ a216,v252
X_ a217,v253
Mvaa 192,28,148
L_ v6,28,s65,0
Mvaa 184,32,184
X_ a218,v254
X_ a219,v255
Mvaa 184,28,144
U_ v240,s48,v240
U_ v241,s48,v241
Mvaa 176,32,180
U_ v242,s48,v242
U_ v243,s48,v243
Mvaa 176,28,140
W_ 4
U_ v244,s48,v244
U_ v245,s48,v245
Mvaa 140,36,60
s_nop 0
Ta 28,%0,24584
Ta 30,%0,25096
Mvaa 140,224,100
U_ v246,s48,v246
U_ v247,s48,v247
Mvaa 148,36,64
C_ 251,246,247
C_ 250,244,245
Mvaa 148,224,104
s_nop 0
Ta 32,%0,28680
Ta 34,%0,29192
Mvaa 156,36,68
C_ 249,242,243
C_ 248,240,241
Mvaa 156,224,108
X_ a212,v248
X_ a213,v249
Mvaa 164,36,72
X_ a214,v250
X_ a215,v251
Mvaa 164,224,112
U_ v232,s48,v232
U_ v233,s48,v233
Mvaa 172,36,76
U_ v234,s48,v234
U_ v235,s48,v235
Mvaa 172,224,116
W_ 4
U_ v236,s48,v236
U_ v237,s48,v237
Mvaa 168,24,116
s_nop 0
Ta 36,%0,24576
Ta 38,%0,25088
Mvaa 168,20,76
U_ v238,s48,v238
U_ v239,s48,v239
Mvaa 160,24,112
C_ 247,238,239
C_ 246,236,237
Mvaa 160,20,72
s_nop 0
Ta 224,%0,28672
Ta 226,%0,29184
Mvaa 152,24,108
C_ 245,234,235
C_ 244,232,233
Mvaa 152,20,68
X_ a208,v244
X_ a209,v245
Mvaa 144,24,104
X_ a210,v246
X_ a211,v247
Mvaa 144,20,64
U_ v224,s48,v224
U_ v225,s48,v225
Mvaa 136,24,100
s_waitcnt vmcnt(13) lgkmcnt(0)
s_barrier
U_ v226,s48,v226
Mvaa 136,20,60
S_ s65,s59,-768
U_ v227,s48,v227
Mvaa 100,28,60
S_ m0,s52,24320
Ta 20,v1,8
Ta 22,v1,520
Mvaa 100,32,100
L_ v6,28,s65,256
Mvaa 108,28,64
S_ s65,s59,97536
U_ v228,s48,v228
U_ v229,s48,v229
Mvaa 108,32,104
S_ m0,s52,25344
Ta 24,v1,4104
Ta 26,v1,4616
Mvaa 116,28,68
U_ v230,s48,v230
U_ v231,s48,v231
Mvaa 116,32,108
L_ v6,28,s65,256
Mvaa 124,28,72
S_ s65,s59,195840
C_ 243,230,231
C_ 242,228,229
Mvaa 124,32,112
S_ m0,s52,26368
C_ 241,226,227
C_ 240,224,225
Mvaa 132,28,76
X_ a204,v240
X_ a205,v241
Mvaa 132,32,116
X_ a206,v242
X_ a207,v243
Mvaa 128,224,116
s_nop 0
Ta 28,v1,0
Ta 30,v1,512
Mvaa 128,36,76
L_ v6,28,s65,256
Mvaa 120,224,112
S_ s65,s59,294144
U_ v216,s48,v216
U_ v217,s48,v217
Mvaa 120,36,72
S_ m0,s52,27392
Ta 32,v1,4096
Ta 34,v1,4608
Mvaa 112,224,108
U_ v218,s48,v218
U_ v219,s48,v219
Mvaa 112,36,68
L_ v6,28,s65,256
Mvaa 104,224,104
S_ s65,s60,128
U_ v220,s48,v220
U_ v221,s48,v221
Mvaa 104,36,64
S_ m0,s50,20480
U_ v222,s48,v222
U_ v223,s48,v223
Mvaa 96,224,100
C_ 239,222,223
C_ 238,220,221
Mvaa 96,36,60
W_ 4
C_ 237,218,219
C_ 236,216,217
Mvaa 140,20,140
s_nop 0
Ta 36,%0,8
Ta 38,%0,520
Mvaa 140,24,180
L_ v4,24,s65,0
Mvaa 148,20,144
S_ s65,s60,10368
X_ a200,v236
X_ a201,v237
Mvaa 148,24,184
S_ m0,s50,21504
Ta 224,%0,4104
Ta 226,%0,4616
Mvaa 156,20,148
X_ a202,v238
X_ a203,v239
Mvaa 156,24,188
L_ v5,24,s65,0
Mvaa 164,20,152
S_ s65,s60,20608
U_ v208,s48,v208
U_ v209,s48,v209
Mvaa 164,24,192
S_ m0,s50,22528
U_ v210,s48,v210
U_ v211,s48,v211
Mvaa 172,20,156
U_ v212,s48,v212
U_ v213,s48,v213
Mvaa 172,24,196
W_ 4
U_ v214,s48,v214
U_ v215,s48,v215
Mvaa 168,32,196
s_nop 0
Ta 20,%0,0
Ta 22,%0,512
Mvaa 168,28,156
L_ v4,24,s65,0
Mvaa 160,32,192
S_ s65,s60,30848
C_ 235,214,215
C_ 234,212,213
Mvaa 160,28,152
S_ m0,s50,23552
Ta 24,%0,4096
Ta 26,%0,4608
Mvaa 152,32,188
C_ 233,210,211
C_ 232,208,209
Mvaa 152,28,148
L_ v5,24,s65,0
Mvaa 144,32,184
S_ s65,s60,41088
X_ a176,v232
X_ a177,v233
Mvaa 144,28,144
S_ m0,s50,24576
X_ a178,v234
X_ a179,v235
Mvaa 136,32,180
s_waitcnt vmcnt(12) lgkmcnt(0)
s_barrier
U_ v200,s48,v200
Mvaa 136,28,140
L_ v4,24,s65,0
Mvaa 100,36,140
S_ s65,s59,785664
Ta 28,v1,8200
Ta 30,v1,8712
Mvaa 100,224,180
S_ m0,s52,0
U_ v201,s48,v201
Mvaa 108,36,144
U_ v202,s48,v202
U_ v203,s48,v203
Mvaa 108,224,184
s_nop 0
Ta 32,v1,12296
Ta 34,v1,12808
Mvaa 116,36,148
L_ v6,28,s65,0
Mvaa 116,224,188
S_ s65,s59,883968
U_ v204,s48,v204
U_ v205,s48,v205
Mvaa 124,36,152
S_ m0,s52,1024
U_ v206,s48,v206
U_ v207,s48,v207
Mvaa 124,224,192
C_ 231,206,207
C_ 230,204,205
Mvaa 132,36,156
L_ v6,28,s65,0
Mvaa 132,224,196
C_ 229,202,203
S_ s65,s59,982272
Mvaa 128,24,196
S_ m0,s52,2048
Ta 36,v1,8192
Ta 38,v1,8704
Mvaa 128,20,156
C_ 228,200,201
Mvaa 120,24,192
L_ v6,28,s65,0
Mvaa 120,20,152
S_ s65,s59,1080576
Ta 224,v1,12288
Ta 226,v1,12800
Mvaa 112,24,188
S_ m0,s52,3072
X_ a172,v228
X_ a173,v229
Mvaa 112,20,148
X_ a174,v230
X_ a175,v231
Mvaa 104,24,184
L_ v6,28,s65,0
Mvaa 104,20,144
U_ v192,s48,v192
U_ v193,s48,v193
Mvaa 96,24,180
U_ v194,s48,v194
U_ v195,s48,v195
Mvaa 96,20,140
W_ 4
U_ v196,s48,v196
U_ v197,s48,v197
Mvaa 60,28,60
s_nop 0
Ta 20,%0,8200
Ta 22,%0,8712
Mvaa 60,32,100
U_ v198,s48,v198
U_ v199,s48,v199
Mvaa 68,28,64
C_ 227,198,199
C_ 226,196,197
Mvaa 68,32,104
s_nop 0
Ta 24,%0,12296
Ta 26,%0,12808
Mvaa 76,28,68
C_ 225,194,195
C_ 224,192,193
Mvaa 76,32,108
X_ a168,v224
X_ a169,v225
Mvaa 84,28,72
X_ a170,v226
X_ a171,v227
Mvaa 84,32,112
U_ v184,s48,v184
U_ v185,s48,v185
Mvaa 92,28,76
U_ v186,s48,v186
U_ v187,s48,v187
Mvaa 92,32,116
W_ 4
U_ v188,s48,v188
U_ v189,s48,v189
Mvaa 88,224,116
s_nop 0
Ta 28,%0,8192
Ta 30,%0,8704
Mvaa 88,36,76
U_ v190,s48,v190
U_ v191,s48,v191
Mvaa 80,224,112
C_ 223,190,191
C_ 222,188,189
Mvaa 80,36,72
s_nop 0
Ta 32,%0,12288
Ta 34,%0,12800
Mvaa 72,224,108
C_ 221,186,187
C_ 220,184,185
Mvaa 72,36,68
X_ a164,v220
X_ a165,v221
Mvaa 64,224,104
X_ a166,v222
X_ a167,v223
Mvaa 64,36,64
U_ v176,s48,v176
U_ v177,s48,v177
Mvaa 56,224,100
s_waitcnt vmcnt(13) lgkmcnt(0)
s_barrier
U_ v178,s48,v178
Mvaa 56,36,60
S_ s65,s59,-768
U_ v179,s48,v179
Mvaa 20,20,60
S_ m0,s52,8192
Ta 36,v1,16392
Ta 38,v1,16904
Mvaa 20,24,100
L_ v6,28,s65,0
Mvaa 28,20,64
S_ s65,s59,97536
U_ v180,s48,v180
U_ v181,s48,v181
Mvaa 28,24,104
S_ m0,s52,9216
Ta 224,v1,20488
Ta 226,v1,21000
Mvaa 36,20,68
U_ v182,s48,v182
U_ v183,s48,v183
Mvaa 36,24,108
L_ v6,28,s65,0
Mvaa 44,20,72
S_ s65,s59,195840
C_ 219,182,183
C_ 218,180,181
Mvaa 44,24,112
S_ m0,s52,10240
C_ 217,178,179
C_ 216,176,177
Mvaa 52,20,76
X_ a160,v216
X_ a161,v217
Mvaa 52,24,116
X_ a162,v218
X_ a163,v219
Mvaa 48,32,116
s_nop 0
Ta 20,v1,16384
Ta 22,v1,16896
Mvaa 48,28,76
L_ v6,28,s65,0
Mvaa 40,32,112
S_ s65,s59,294144
U_ v168,s48,v168
U_ v169,s48,v169
Mvaa 40,28,72
S_ m0,s52,11264
Ta 24,v1,20480
Ta 26,v1,20992
Mvaa 32,32,108
U_ v170,s48,v170
U_ v171,s48,v171
Mvaa 32,28,68
L_ v6,28,s65,0
Mvaa 24,32,104
U_ v172,s48,v172
U_ v173,s48,v173
Mvaa 24,28,64
U_ v174,s48,v174
U_ v175,s48,v175
Mvaa 16,32,100
C_ 215,174,175
C_ 214,172,173
Mvaa 16,28,60
W_ 4
C_ 213,170,171
C_ 212,168,169
Mvaa 60,36,140
s_nop 0
Ta 28,%0,16392
Ta 30,%0,16904
Mvaa 60,224,180
X_ a136,v212
X_ a137,v213
Mvaa 68,36,144
X_ a138,v214
X_ a139,v215
Mvaa 68,224,184
s_nop 0
Ta 32,%0,20488
Ta 34,%0,21000
Mvaa 76,36,148
U_ v160,s48,v160
U_ v161,s48,v161
Mvaa 76,224,188
U_ v162,s48,v162
U_ v163,s48,v163
Mvaa 84,36,152
U_ v164,s48,v164
U_ v165,s48,v165
Mvaa 84,224,192
U_ v166,s48,v166
U_ v167,s48,v167
Mvaa 92,36,156
C_ 211,166,167
C_ 210,164,165
Mvaa 92,224,196
W_ 4
C_ 209,162,163
C_ 208,160,161
Mvaa 88,24,196
s_nop 0
Ta 36,%0,16384
Ta 38,%0,16896
Mvaa 88,20,156
X_ a132,v208
X_ a133,v209
Mvaa 80,24,192
X_ a134,v210
X_ a135,v211
Mvaa 80,20,152
s_nop 0
Ta 224,%0,20480
Ta 226,%0,20992
Mvaa 72,24,188
U_ v152,s48,v152
U_ v153,s48,v153
Mvaa 72,20,148
U_ v154,s48,v154
U_ v155,s48,v155
Mvaa 64,24,184
U_ v156,s48,v156
U_ v157,s48,v157
Mvaa 64,20,144
U_ v158,s48,v158
U_ v159,s48,v159
Mvaa 56,24,180
s_waitcnt vmcnt(63) lgkmcnt(0)
s_barrier
C_ 207,158,159
Mvaa 56,20,140
S_ s65,s59,-768
C_ 206,156,157
Mvaa 20,28,140
S_ m0,s52,16256
C_ 205,154,155
C_ 204,152,153
Mvaa 20,32,180
L_ v6,28,s65,128
Mvaa 28,28,144
S_ s65,s59,97536
X_ a128,v204
X_ a129,v205
Mvaa 28,32,184
S_ m0,s52,17280
X_ a130,v206
X_ a131,v207
Mvaa 36,28,148
U_ v144,s48,v144
U_ v145,s48,v145
Mvaa 36,32,188
L_ v6,28,s65,128
Mvaa 44,28,152
S_ s65,s59,195840
U_ v146,s48,v146
U_ v147,s48,v147
Mvaa 44,32,192
S_ m0,s52,18304
U_ v148,s48,v148
U_ v149,s48,v149
Mvaa 52,28,156
U_ v150,s48,v150
U_ v151,s48,v151
Mvaa 52,32,196
C_ 203,150,151
C_ 202,148,149
Mvaa 48,224,196
L_ v6,28,s65,128
Mvaa 48,36,156
S_ s65,s59,294144
C_ 201,146,147
C_ 200,144,145
Mvaa 40,224,192
S_ m0,s52,19328
X_ a124,v200
X_ a125,v201
Mvaa 40,36,152
X_ a126,v202
X_ a127,v203
Mvaa 32,224,188
L_ v6,28,s65,128
Mvaa 32,36,148
U_ v136,s48,v136
U_ v137,s48,v137
Mvaa 24,224,184
U_ v138,s48,v138
U_ v139,s48,v139
Mvaa 24,36,144
U_ v140,s48,v140
U_ v141,s48,v141
Mvaa 16,224,180
U_ v142,s48,v142
U_ v143,s48,v143
Mvaa 16,36,140
C_ 199,142,143
C_ 198,140,141
C_ 197,138,139
C_ 196,136,137
X_ a120,v196
X_ a121,v197
X_ a122,v198
X_ a123,v199
U_ v128,s48,v128
U_ v129,s48,v129
U_ v130,s48,v130
U_ v131,s48,v131
U_ v132,s48,v132
U_ v133,s48,v133
U_ v134,s48,v134
U_ v135,s48,v135
C_ 195,134,135
C_ 194,132,133
C_ 193,130,131
C_ 192,128,129
X_ a96,v192
X_ a97,v193
X_ a98,v194
X_ a99,v195
U_ v120,s48,v120
U_ v121,s48,v121
U_ v122,s48,v122
U_ v123,s48,v123
U_ v124,s48,v124
U_ v125,s48,v125
U_ v126,s48,v126
U_ v127,s48,v127
C_ 191,126,127
C_ 190,124,125
C_ 189,122,123
C_ 188,120,121
X_ a92,v188
X_ a93,v189
X_ a94,v190
X_ a95,v191
U_ v112,s48,v112
U_ v113,s48,v113
U_ v114,s48,v114
U_ v115,s48,v115
U_ v116,s48,v116
U_ v117,s48,v117
U_ v118,s48,v118
U_ v119,s48,v119
C_ 187,118,119
C_ 186,116,117
C_ 185,114,115
C_ 184,112,113
X_ a88,v184
X_ a89,v185
X_ a90,v186
X_ a91,v187
U_ v104,s48,v104
U_ v105,s48,v105
U_ v106,s48,v106
U_ v107,s48,v107
U_ v108,s48,v108
U_ v109,s48,v109
U_ v110,s48,v110
U_ v111,s48,v111
C_ 183,110,111
C_ 182,108,109
C_ 181,106,107
C_ 180,104,105
X_ a84,v180
X_ a85,v181
X_ a86,v182
X_ a87,v183
U_ v96,s48,v96
U_ v97,s48,v97
U_ v98,s48,v98
U_ v99,s48,v99
U_ v100,s48,v100
U_ v101,s48,v101
U_ v102,s48,v102
U_ v103,s48,v103
C_ 179,102,103
C_ 178,100,101
C_ 177,98,99
C_ 176,96,97
X_ a80,v176
X_ a81,v177
X_ a82,v178
X_ a83,v179
v_pk_mul_f32 v[88:89], v[88:89], s[48:49]
v_pk_mul_f32 v[90:91], v[90:91], s[48:49]
v_pk_mul_f32 v[92:93], v[92:93], s[48:49]
v_pk_mul_f32 v[94:95], v[94:95], s[48:49]
C_ 175,94,95
C_ 174,92,93
C_ 173,90,91
C_ 172,88,89
X_ a56,v172
X_ a57,v173
X_ a58,v174
X_ a59,v175
v_pk_mul_f32 v[80:81], v[80:81], s[48:49]
v_pk_mul_f32 v[82:83], v[82:83], s[48:49]
v_pk_mul_f32 v[84:85], v[84:85], s[48:49]
v_pk_mul_f32 v[86:87], v[86:87], s[48:49]
C_ 171,86,87
C_ 170,84,85
C_ 169,82,83
C_ 168,80,81
X_ a52,v168
X_ a53,v169
X_ a54,v170
X_ a55,v171
v_pk_mul_f32 v[72:73], v[72:73], s[48:49]
v_pk_mul_f32 v[74:75], v[74:75], s[48:49]
v_pk_mul_f32 v[76:77], v[76:77], s[48:49]
v_pk_mul_f32 v[78:79], v[78:79], s[48:49]
C_ 167,78,79
C_ 166,76,77
C_ 165,74,75
C_ 164,72,73
X_ a48,v164
X_ a49,v165
X_ a50,v166
X_ a51,v167
v_pk_mul_f32 v[64:65], v[64:65], s[48:49]
v_pk_mul_f32 v[66:67], v[66:67], s[48:49]
v_pk_mul_f32 v[68:69], v[68:69], s[48:49]
v_pk_mul_f32 v[70:71], v[70:71], s[48:49]
C_ 163,70,71
C_ 162,68,69
C_ 161,66,67
C_ 160,64,65
X_ a44,v160
X_ a45,v161
X_ a46,v162
X_ a47,v163
v_pk_mul_f32 v[56:57], v[56:57], s[48:49]
v_pk_mul_f32 v[58:59], v[58:59], s[48:49]
v_pk_mul_f32 v[60:61], v[60:61], s[48:49]
v_pk_mul_f32 v[62:63], v[62:63], s[48:49]
C_ 159,62,63
C_ 158,60,61
C_ 157,58,59
C_ 156,56,57
X_ a40,v156
X_ a41,v157
X_ a42,v158
X_ a43,v159
v_pk_mul_f32 v[48:49], v[48:49], s[48:49]
v_pk_mul_f32 v[50:51], v[50:51], s[48:49]
v_pk_mul_f32 v[52:53], v[52:53], s[48:49]
v_pk_mul_f32 v[54:55], v[54:55], s[48:49]
C_ 155,54,55
C_ 154,52,53
C_ 153,50,51
C_ 152,48,49
X_ a16,v152
X_ a17,v153
X_ a18,v154
X_ a19,v155
v_pk_mul_f32 v[40:41], v[40:41], s[48:49]
v_pk_mul_f32 v[42:43], v[42:43], s[48:49]
v_pk_mul_f32 v[44:45], v[44:45], s[48:49]
v_pk_mul_f32 v[46:47], v[46:47], s[48:49]
C_ 151,46,47
C_ 150,44,45
C_ 149,42,43
C_ 148,40,41
X_ a12,v148
X_ a13,v149
X_ a14,v150
X_ a15,v151
s_sub_u32 s59, s59, 768
s_waitcnt vmcnt(0) lgkmcnt(0)
s_barrier
Ta 20,%0,8192
Ta 22,%0,8704
Ta 24,%0,12288
Ta 26,%0,12800
Da 60,v2,0
Da 100,v3,0
Da 64,v2,2048
Da 104,v3,2048
Da 68,v2,4096
Da 108,v3,4096
Da 72,v2,6144
Da 112,v3,6144
Da 76,v2,8192
Da 116,v3,8192
Ta 28,%0,8200
Ta 30,%0,8712
Ta 32,%0,12296
Ta 34,%0,12808
v_pk_mul_f32 v[32:33], v[32:33], s[48:49]
v_pk_mul_f32 v[34:35], v[34:35], s[48:49]
v_pk_mul_f32 v[36:37], v[36:37], s[48:49]
v_pk_mul_f32 v[38:39], v[38:39], s[48:49]
C_ 147,38,39
C_ 146,36,37
C_ 145,34,35
C_ 144,32,33
X_ a8,v144
X_ a9,v145
X_ a10,v146
X_ a11,v147
v_pk_mul_f32 v[24:25], v[24:25], s[48:49]
v_pk_mul_f32 v[26:27], v[26:27], s[48:49]
v_pk_mul_f32 v[28:29], v[28:29], s[48:49]
v_pk_mul_f32 v[30:31], v[30:31], s[48:49]
C_ 143,30,31
C_ 142,28,29
C_ 141,26,27
C_ 140,24,25
X_ a4,v140
X_ a5,v141
X_ a6,v142
X_ a7,v143
v_pk_mul_f32 v[16:17], v[16:17], s[48:49]
v_pk_mul_f32 v[18:19], v[18:19], s[48:49]
v_pk_mul_f32 v[20:21], v[20:21], s[48:49]
v_pk_mul_f32 v[22:23], v[22:23], s[48:49]
C_ 139,22,23
C_ 138,20,21
C_ 137,18,19
C_ 136,16,17
X_ a0,v136
X_ a1,v137
X_ a2,v138
X_ a3,v139
s_sub_u32 s90, s90, s88
s_cmp_gt_u32 s90, 4000
s_cselect_b32 s11, 1, 0
s_sub_u32 s12, s68, s98
S_ s12,s12,1251
s_mov_b32 s13, 113
s_nop 1
.p2align 3
.Lq:
W_ 13
Zvaa 16,20,60
s_nop 0
Ta 36,v1,8192
Ta 38,v1,8704
W_ 14
Mvaa 16,24,100
R_ v12,a248
W_ 13
Zvaa 24,20,64
v_lshrrev_b32 v12, 4, v12
W_ 12
Mvaa 24,24,104
s_nop 0
Ta 224,v1,12288
Ta 226,v1,12800
W_ 13
Zvaa 32,20,68
v_min_u32 v10, 11, v12
W_ 12
Mvaa 32,24,108
s_mul_i32 s56, s4, 12
W_ 11
Zvaa 40,20,72
v_add_u32 v10, s56, v10
W_ 10
Mvaa 40,24,112
v_mul_u32_u24 v11, 43691, v10
W_ 9
Zvaa 48,20,76
v_lshrrev_b32 v11, 18, v11
W_ 8
Mvaa 48,24,116
W_ 4
v_lshlrev_b32 v10, 7, v10
Zvaa 52,32,116
s_nop 0
Ta 20,v1,8200
Ta 22,v1,8712
Mvaa 52,28,76
v_mul_u32_u24 v11, 11520, v11
Zvaa 44,32,112
v_add_u32 v10, v10, v11
Mvaa 44,28,72
s_nop 0
Ta 24,v1,12296
Ta 26,v1,12808
Zvaa 36,32,108
v_min_u32 v12, 9, v12
Mvaa 36,28,68
s_and_b32 s56, s6, 3
Zvaa 28,32,104
s_lshl_b32 s56, s56, 2
Mvaa 28,28,64
S_ s56,s56,s4
Zvaa 20,32,100
s_waitcnt vmcnt(18) lgkmcnt(0)
s_barrier
s_mul_i32 s56, s56, 10
Mvaa 20,28,60
S_ s65,s59,786432
v_add_u32 v12, s56, v12
Zvaa 56,36,60
S_ m0,s52,8064
Ta 28,%0,16384
Ta 30,%0,16896
Mvaa 56,224,100
L_ v6,28,s65,128
Zvaa 64,36,64
S_ s65,s59,884736
v_mul_u32_u24 v12, 1280, v12
Mvaa 64,224,104
S_ m0,s52,9088
Ta 32,%0,20480
Ta 34,%0,20992
Zvaa 72,36,68
s_lshr_b32 s56, s7, 5
Mvaa 72,224,108
L_ v6,28,s65,128
Zvaa 80,36,72
S_ s65,s59,983040
s_mul_i32 s56, s56, 98304
Mvaa 80,224,112
S_ m0,s52,10112
s_and_b32 s55, s59, 4095
Zvaa 88,36,76
S_ s56,s56,s55
Mvaa 88,224,116
s_mul_i32 s55, s77, 384
Zvaa 92,24,116
s_sub_u32 s56, s56, s55
Ta 36,%0,16392
Ta 38,%0,16904
Mvaa 92,20,76
L_ v6,28,s65,128
Zvaa 84,24,112
S_ s65,s59,1081344
s_mov_b32 s63, 0
Mvaa 84,20,72
S_ m0,s52,11136
Ta 224,%0,20488
Ta 226,%0,21000
Zvaa 76,24,108
s_bitcmp1_b32 1, s58
s_cselect_b32 s63, 0, 4026531840
Mvaa 76,20,68
L_ v6,28,s65,128
Zvaa 68,24,104
S_ s65,s60,256
S_ s56,s56,s63
Mvaa 68,20,64
S_ m0,s50,0
S_ s55,s56,3145728
Zvaa 60,24,100
Mvaa 60,20,60
W_ 4
Zvaa 96,28,60
s_nop 0
Ta 20,v1,16384
Ta 22,v1,16896
Mvaa 96,32,100
L_ v4,24,s65,0
Zvaa 104,28,64
S_ s65,s60,10496
Mvaa 104,32,104
S_ m0,s50,1024
Ta 24,v1,20480
Ta 26,v1,20992
Zvaa 112,28,68
Mvaa 112,32,108
L_ v5,24,s65,0
Zvaa 120,28,72
S_ s65,s60,20736
Mvaa 120,32,112
S_ m0,s50,2048
Zvaa 128,28,76
Mvaa 128,32,116
W_ 4
Zvaa 132,224,116
s_nop 0
Ta 28,v1,16392
Ta 30,v1,16904
Mvaa 132,36,76
L_ v4,24,s65,0
Zvaa 124,224,112
S_ s65,s60,30976
Mvaa 124,36,72
S_ m0,s50,3072
Ta 32,v1,20488
Ta 34,v1,21000
Zvaa 116,224,108
Mvaa 116,36,68
L_ v5,24,s65,0
Zvaa 108,224,104
S_ s65,s60,41216
Mvaa 108,36,64
S_ m0,s50,4096
Zvaa 100,224,100
s_waitcnt vmcnt(17) lgkmcnt(0)
s_barrier
s_cmp_eq_u32 s11, 1
s_cbranch_scc1 .Lpace1
.Lpace1_back:
Mvaa 100,36,60
Da 140,v2,20480
Zvaa 136,20,60
s_nop 0
Ta 36,%0,24576
Ta 38,%0,25088
Mvaa 136,24,100
L_ v4,24,s65,0
Zvaa 144,20,64
S_ s65,s59,786432
Da 180,v3,20480
Mvaa 144,24,104
S_ m0,s52,16128
Ta 224,%0,28672
Ta 226,%0,29184
Zvaa 152,20,68
Mvaa 152,24,108
Da 144,v2,22528
Zvaa 160,20,72
L_ v6,28,s65,256
Mvaa 160,24,112
S_ s65,s59,884736
Zvaa 168,20,76
S_ m0,s52,17152
Da 184,v3,22528
Mvaa 168,24,116
Zvaa 172,32,116
s_nop 0
Ta 20,%0,24584
Ta 22,%0,25096
Mvaa 172,28,76
Da 148,v2,24576
Zvaa 164,32,112
L_ v6,28,s65,256
Mvaa 164,28,72
S_ s65,s59,983040
Ta 24,%0,28680
Ta 26,%0,29192
Zvaa 156,32,108
S_ m0,s52,18176
Da 188,v3,24576
Mvaa 156,28,68
Zvaa 148,32,104
L_ v6,28,s65,256
Mvaa 148,28,64
S_ s65,s59,1081344
Da 152,v2,26624
Zvaa 140,32,100
S_ m0,s52,19200
Mvaa 140,28,60
W_ 9
Zvaa 176,36,60
s_nop 0
Ta 28,v1,24576
Ta 30,v1,25088
Mvaa 176,224,100
Da 192,v3,26624
Zvaa 184,36,64
L_ v6,28,s65,256
Mvaa 184,224,104
s_nop 0
Ta 32,v1,28672
Ta 34,v1,29184
Zvaa 192,36,68
Da 156,v2,28672
Mvaa 192,224,108
Zvaa 200,36,72
Da 196,v3,28672
Mvaa 200,224,112
buffer_load_dword a254, v10, s[28:31], s55 offen
Zvaa 208,36,76
Mvaa 208,224,116
W_ 9
Zvaa 212,24,116
s_nop 0
Ta 36,v1,24584
Ta 38,v1,25096
Mvaa 212,20,76
buffer_load_dword a254, v12, s[24:27], s63 offen offset:512
Zvaa 204,24,112
Mvaa 204,20,72
s_nop 0
Ta 224,v1,28680
Ta 226,v1,29192
Zvaa 196,24,108
Mvaa 196,20,68
Zvaa 188,24,104
Mvaa 188,20,64
Zvaa 180,24,100
s_waitcnt vmcnt(20) lgkmcnt(0)
s_barrier
Mvaa 180,20,60
S_ s65,s59,1572864
Zvaa 216,28,60
S_ m0,s52,24576
Ta 20,%0,0
Ta 22,%0,512
Mvaa 216,32,100
L_ v6,28,s65,0
Zvaa 224,28,64
S_ s65,s59,1671168
Mvaa 224,32,104
S_ m0,s52,25600
Ta 24,%0,4096
Ta 26,%0,4608
Zvaa 232,28,68
Mvaa 232,32,108
L_ v6,28,s65,0
Zvaa 240,28,72
S_ s65,s59,1769472
Mvaa 240,32,112
S_ m0,s52,26624
Zvaa 248,28,76
Mvaa 248,32,116
Zvaa 252,224,116
s_nop 0
Ta 28,%0,8
Ta 30,%0,520
Mvaa 252,36,76
L_ v6,28,s65,0
Zvaa 244,224,112
S_ s65,s59,1867776
Mvaa 244,36,72
S_ m0,s52,27648
Ta 32,%0,4104
Ta 34,%0,4616
Zvaa 236,224,108
Mvaa 236,36,68
L_ v6,28,s65,0
Zvaa 228,224,104
S_ s65,s60,384
Mvaa 228,36,64
S_ m0,s50,20480
Zvaa 220,224,100
Mvaa 220,36,60
W_ 4
Mvaa 16,20,140
s_nop 0
Ta 36,v1,0
Ta 38,v1,512
Mvaa 16,24,180
L_ v4,24,s65,0
Mvaa 24,20,144
S_ s65,s60,10624
Mvaa 24,24,184
S_ m0,s50,21504
Ta 224,v1,4096
Ta 226,v1,4608
Mvaa 32,20,148
Mvaa 32,24,188
L_ v5,24,s65,0
Mvaa 40,20,152
S_ s65,s60,20864
Mvaa 40,24,192
S_ m0,s50,22528
Mvaa 48,20,156
Mvaa 48,24,196
W_ 4
Mvaa 52,32,196
s_nop 0
Ta 20,v1,8
Ta 22,v1,520
Mvaa 52,28,156
L_ v4,24,s65,0
Mvaa 44,32,192
S_ s65,s60,31104
Mvaa 44,28,152
S_ m0,s50,23552
Ta 24,v1,4104
Ta 26,v1,4616
Mvaa 36,32,188
Mvaa 36,28,148
L_ v5,24,s65,0
Mvaa 28,32,184
S_ s65,s60,41344
Mvaa 28,28,144
S_ m0,s50,24576
Mvaa 20,32,180
s_waitcnt vmcnt(19) lgkmcnt(0)
s_barrier
s_cmp_eq_u32 s11, 1
s_cbranch_scc1 .Lpace3
.Lpace3_back:
Mvaa 20,28,140
L_ v4,24,s65,0
Mvaa 56,36,140
S_ s65,s59,1572864
Ta 28,%0,8192
Ta 30,%0,8704
Mvaa 56,224,180
S_ m0,s52,-128
Mvaa 64,36,144
Mvaa 64,224,184
s_nop 0
Ta 32,%0,12288
Ta 34,%0,12800
Mvaa 72,36,148
L_ v6,28,s65,128
Mvaa 72,224,188
S_ s65,s59,1671168
Mvaa 80,36,152
S_ m0,s52,896
Mvaa 80,224,192
Mvaa 88,36,156
L_ v6,28,s65,128
Mvaa 88,224,196
S_ s65,s59,1769472
Mvaa 92,24,196
S_ m0,s52,1920
Ta 36,%0,8200
Ta 38,%0,8712
Mvaa 92,20,156
S_ s55,s56,3932160
Mvaa 84,24,192
L_ v6,28,s65,128
Mvaa 84,20,152
S_ s65,s59,1867776
Ta 224,%0,12296
Ta 226,%0,12808
Mvaa 76,24,188
S_ m0,s52,2944
Mvaa 76,20,148
Mvaa 68,24,184
L_ v6,28,s65,128
Mvaa 68,20,144
Mvaa 60,24,180
Mvaa 60,20,140
W_ 4
Mvaa 96,28,140
s_nop 0
Ta 20,v1,8192
Ta 22,v1,8704
Mvaa 96,32,180
buffer_load_dword a254, v10, s[28:31], s55 offen
Mvaa 104,28,144
Mvaa 104,32,184
s_nop 0
Ta 24,v1,12288
Ta 26,v1,12800
Mvaa 112,28,148
Mvaa 112,32,188
buffer_load_dword a254, v12, s[24:27], s63 offen offset:640
Mvaa 120,28,152
Mvaa 120,32,192
Mvaa 128,28,156
Mvaa 128,32,196
W_ 4
Mvaa 132,224,196
s_nop 0
Ta 28,v1,8200
Ta 30,v1,8712
Mvaa 132,36,156
Mvaa 124,224,192
Mvaa 124,36,152
s_nop 0
Ta 32,v1,12296
Ta 34,v1,12808
Mvaa 116,224,188
Mvaa 116,36,148
Mvaa 108,224,184
Mvaa 108,36,144
Mvaa 100,224,180
s_waitcnt vmcnt(17) lgkmcnt(0)
s_barrier
Mvaa 100,36,140
S_ s65,s59,1572864
Da 60,v2,0
Mvaa 136,20,140
S_ m0,s52,7936
Ta 36,%0,16384
Ta 38,%0,16896
Mvaa 136,24,180
L_ v6,28,s65,256
Mvaa 144,20,144
S_ s65,s59,1671168
Da 100,v3,0
Mvaa 144,24,184
S_ m0,s52,8960
Ta 224,%0,20480
Ta 226,%0,20992
Mvaa 152,20,148
Mvaa 152,24,188
Da 64,v2,2048
Mvaa 160,20,152
L_ v6,28,s65,256
Mvaa 160,24,192
S_ s65,s59,1769472
Mvaa 168,20,156
S_ m0,s52,9984
Da 104,v3,2048
Mvaa 168,24,196
Mvaa 172,32,196
s_nop 0
Ta 20,%0,16392
Ta 22,%0,16904
Mvaa 172,28,156
Da 68,v2,4096
Mvaa 164,32,192
L_ v6,28,s65,256
Mvaa 164,28,152
S_ s65,s59,1867776
Ta 24,%0,20488
Ta 26,%0,21000
Mvaa 156,32,188
)A"
R"A(S_ m0,s52,11008
Da 108,v3,4096
Mvaa 156,28,148
Mvaa 148,32,184
L_ v6,28,s65,256
Mvaa 148,28,144
Da 72,v2,6144
Mvaa 140,32,180
Mvaa 140,28,140
W_ 9
Mvaa 176,36,140
s_nop 0
Ta 28,v1,16384
Ta 30,v1,16896
Mvaa 176,224,180
Da 112,v3,6144
Mvaa 184,36,144
Mvaa 184,224,184
s_nop 0
Ta 32,v1,20480
Ta 34,v1,20992
Mvaa 192,36,148
Da 76,v2,8192
Mvaa 192,224,188
Mvaa 200,36,152
Da 116,v3,8192
Mvaa 200,224,192
Mvaa 208,36,156
Mvaa 208,224,196
W_ 9
Mvaa 212,24,196
s_nop 0
Ta 36,v1,16392
Ta 38,v1,16904
Mvaa 212,20,156
Mvaa 204,24,192
Mvaa 204,20,152
s_nop 0
Ta 224,v1,20488
Ta 226,v1,21000
Mvaa 196,24,188
Mvaa 196,20,148
Mvaa 188,24,184
Mvaa 188,20,144
Mvaa 180,24,180
s_waitcnt vmcnt(15) lgkmcnt(0)
s_barrier
s_cmp_eq_u32 s11, 1
s_cbranch_scc1 .Lpace5
.Lpace5_back:
Mvaa 180,20,140
S_ s65,s59,2359296
Mvaa 216,28,140
S_ m0,s52,16384
Ta 20,%0,24576
Ta 22,%0,25088
Mvaa 216,32,180
L_ v6,28,s65,0
Mvaa 224,28,144
S_ s65,s59,2457600
Mvaa 224,32,184
S_ m0,s52,17408
Ta 24,%0,28672
Ta 26,%0,29184
Mvaa 232,28,148
Mvaa 232,32,188
L_ v6,28,s65,0
Mvaa 240,28,152
S_ s65,s59,2555904
Mvaa 240,32,192
S_ m0,s52,18432
Mvaa 248,28,156
Mvaa 248,32,196
Mvaa 252,224,196
s_nop 0
Ta 28,%0,24584
Ta 30,%0,25096
Mvaa 252,36,156
L_ v6,28,s65,0
Mvaa 244,224,192
S_ s65,s59,2654208
Mvaa 244,36,152
S_ m0,s52,19456
Ta 32,%0,28680
Ta 34,%0,29192
Mvaa 236,224,188
Mvaa 236,36,148
L_ v6,28,s65,0
Mvaa 228,224,184
S_ s65,s60,512
Mvaa 228,36,144
S_ m0,s50,0
Mvaa 220,224,180
Mvaa 220,36,140
W_ 4
Mvaa 16,20,60
s_nop 0
Ta 36,v1,24576
Ta 38,v1,25088
Mvaa 16,24,100
L_ v4,24,s65,0
Mvaa 24,20,64
S_ s65,s60,10752
Mvaa 24,24,104
S_ m0,s50,1024
Ta 224,v1,28672
Ta 226,v1,29184
Mvaa 32,20,68
Mvaa 32,24,108
L_ v5,24,s65,0
Mvaa 40,20,72
S_ s65,s60,20992
Mvaa 40,24,112
S_ m0,s50,2048
Mvaa 48,20,76
Mvaa 48,24,116
W_ 4
Mvaa 52,32,116
s_nop 0
Ta 20,v1,24584
Ta 22,v1,25096
Mvaa 52,28,76
L_ v4,24,s65,0
Mvaa 44,32,112
S_ s65,s60,31232
Mvaa 44,28,72
S_ m0,s50,3072
Ta 24,v1,28680
Ta 26,v1,29192
Mvaa 36,32,108
Mvaa 36,28,68
L_ v5,24,s65,0
Mvaa 28,32,104
S_ s65,s60,41472
Mvaa 28,28,64
S_ m0,s50,4096
Mvaa 20,32,100
s_waitcnt vmcnt(14) lgkmcnt(0)
s_barrier
Mvaa 20,28,60
L_ v4,24,s65,0
Mvaa 56,36,60
S_ s65,s59,2359296
Ta 28,%0,0
Ta 30,%0,512
Mvaa 56,224,100
S_ m0,s52,24448
Mvaa 64,36,64
Mvaa 64,224,104
s_nop 0
Ta 32,%0,4096
Ta 34,%0,4608
Mvaa 72,36,68
L_ v6,28,s65,128
Mvaa 72,224,108
S_ s65,s59,2457600
Mvaa 80,36,72
S_ m0,s52,25472
Mvaa 80,224,112
Mvaa 88,36,76
L_ v6,28,s65,128
Mvaa 88,224,116
S_ s65,s59,2555904
Mvaa 92,24,116
S_ m0,s52,26496
Ta 36,%0,8
Ta 38,%0,520
Mvaa 92,20,76
S_ s55,s56,4718592
Mvaa 84,24,112
L_ v6,28,s65,128
Mvaa 84,20,72
S_ s65,s59,2654208
Ta 224,%0,4104
Ta 226,%0,4616
Mvaa 76,24,108
S_ m0,s52,27520
Mvaa 76,20,68
Mvaa 68,24,104
L_ v6,28,s65,128
Mvaa 68,20,64
Mvaa 60,24,100
Mvaa 60,20,60
W_ 4
Mvaa 96,28,60
s_nop 0
Ta 20,v1,0
Ta 22,v1,512
Mvaa 96,32,100
buffer_load_dword a254, v10, s[28:31], s55 offen
Mvaa 104,28,64
Mvaa 104,32,104
s_nop 0
Ta 24,v1,4096
Ta 26,v1,4608
Mvaa 112,28,68
Mvaa 112,32,108
buffer_load_dword a254, v12, s[24:27], s63 offen offset:768
Mvaa 120,28,72
Mvaa 120,32,112
Mvaa 128,28,76
Mvaa 128,32,116
W_ 4
Mvaa 132,224,116
s_nop 0
Ta 28,v1,8
Ta 30,v1,520
Mvaa 132,36,76
Mvaa 124,224,112
Mvaa 124,36,72
s_nop 0
Ta 32,v1,4104
Ta 34,v1,4616
Mvaa 116,224,108
Mvaa 116,36,68
Mvaa 108,224,104
Mvaa 108,36,64
Mvaa 100,224,100
s_waitcnt vmcnt(15) lgkmcnt(0)
s_barrier
s_cmp_eq_u32 s11, 1
s_cbranch_scc1 .Lpace7
.Lpace7_back:
Mvaa 100,36,60
S_ s65,s59,2359296
Da 140,v2,20480
Mvaa 136,20,60
S_ m0,s52,-256
Ta 36,%0,8192
Ta 38,%0,8704
Mvaa 136,24,100
L_ v6,28,s65,256
Mvaa 144,20,64
S_ s65,s59,2457600
Da 180,v3,20480
Mvaa 144,24,104
S_ m0,s52,768
Ta 224,%0,12288
Ta 226,%0,12800
Mvaa 152,20,68
Mvaa 152,24,108
Da 144,v2,22528
Mvaa 160,20,72
L_ v6,28,s65,256
Mvaa 160,24,112
S_ s65,s59,2555904
Mvaa 168,20,76
S_ m0,s52,1792
Da 184,v3,22528
Mvaa 168,24,116
Mvaa 172,32,116
s_nop 0
Ta 20,%0,8200
Ta 22,%0,8712
Mvaa 172,28,76
Da 148,v2,24576
Mvaa 164,32,112
L_ v6,28,s65,256
Mvaa 164,28,72
S_ s65,s59,2654208
Ta 24,%0,12296
Ta 26,%0,12808
Mvaa 156,32,108
S_ m0,s52,2816
Da 188,v3,24576
Mvaa 156,28,68
Mvaa 148,32,104
L_ v6,28,s65,256
Mvaa 148,28,64
Da 152,v2,26624
Mvaa 140,32,100
Mvaa 140,28,60
W_ 9
Mvaa 176,36,60
s_nop 0
Ta 28,v1,8192
Ta 30,v1,8704
Mvaa 176,224,100
Da 192,v3,26624
Mvaa 184,36,64
Mvaa 184,224,104
s_nop 0
Ta 32,v1,12288
Ta 34,v1,12800
Mvaa 192,36,68
Da 156,v2,28672
Mvaa 192,224,108
Mvaa 200,36,72
Da 196,v3,28672
Mvaa 200,224,112
Mvaa 208,36,76
Mvaa 208,224,116
W_ 9
Mvaa 212,24,116
s_nop 0
Ta 36,v1,8200
Ta 38,v1,8712
Mvaa 212,20,76
Mvaa 204,24,112
Mvaa 204,20,72
s_nop 0
Ta 224,v1,12296
Ta 226,v1,12808
Mvaa 196,24,108
Mvaa 196,20,68
Mvaa 188,24,104
Mvaa 188,20,64
Mvaa 180,24,100
s_waitcnt vmcnt(15) lgkmcnt(0)
s_barrier
Mvaa 180,20,60
S_ s65,s59,3145728
Mvaa 216,28,60
S_ m0,s52,8192
Ta 20,%0,16384
Ta 22,%0,16896
Mvaa 216,32,100
L_ v6,28,s65,0
Mvaa 224,28,64
S_ s65,s59,3244032
Mvaa 224,32,104
S_ m0,s52,9216
Ta 24,%0,20480
Ta 26,%0,20992
Mvaa 232,28,68
Mvaa 232,32,108
L_ v6,28,s65,0
Mvaa 240,28,72
S_ s65,s59,3342336
Mvaa 240,32,112
S_ m0,s52,10240
Mvaa 248,28,76
Mvaa 248,32,116
Mvaa 252,224,116
s_nop 0
Ta 28,%0,16392
Ta 30,%0,16904
Mvaa 252,36,76
L_ v6,28,s65,0
Mvaa 244,224,112
S_ s65,s59,3440640
Mvaa 244,36,72
S_ m0,s52,11264
Ta 32,%0,20488
Ta 34,%0,21000
Mvaa 236,224,108
Mvaa 236,36,68
L_ v6,28,s65,0
Mvaa 228,224,104
S_ s65,s60,640
Mvaa 228,36,64
S_ m0,s50,20480
Mvaa 220,224,100
Mvaa 220,36,60
W_ 4
Mvaa 16,20,140
s_nop 0
Ta 36,v1,16384
Ta 38,v1,16896
Mvaa 16,24,180
L_ v4,24,s65,0
Mvaa 24,20,144
S_ s65,s60,10880
Mvaa 24,24,184
S_ m0,s50,21504
Ta 224,v1,20480
Ta 226,v1,20992
Mvaa 32,20,148
Mvaa 32,24,188
L_ v5,24,s65,0
Mvaa 40,20,152
S_ s65,s60,21120
Mvaa 40,24,192
S_ m0,s50,22528
Mvaa 48,20,156
Mvaa 48,24,196
W_ 4
Mvaa 52,32,196
s_nop 0
Ta 20,v1,16392
Ta 22,v1,16904
Mvaa 52,28,156
L_ v4,24,s65,0
Mvaa 44,32,192
S_ s65,s60,31360
Mvaa 44,28,152
S_ m0,s50,23552
Ta 24,v1,20488
Ta 26,v1,21000
Mvaa 36,32,188
Mvaa 36,28,148
L_ v5,24,s65,0
Mvaa 28,32,184
S_ s65,s60,41600
Mvaa 28,28,144
S_ m0,s50,24576
Mvaa 20,32,180
s_waitcnt vmcnt(14) lgkmcnt(0)
s_barrier
s_cmp_eq_u32 s11, 1
s_cbranch_scc1 .Lpace9
.Lpace9_back:
Mvaa 20,28,140
L_ v4,24,s65,0
Mvaa 56,36,140
S_ s65,s59,3145728
Ta 28,%0,24576
Ta 30,%0,25088
Mvaa 56,224,180
S_ m0,s52,16256
Mvaa 64,36,144
Mvaa 64,224,184
s_nop 0
Ta 32,%0,28672
Ta 34,%0,29184
Mvaa 72,36,148
L_ v6,28,s65,128
Mvaa 72,224,188
S_ s65,s59,3244032
Mvaa 80,36,152
S_ m0,s52,17280
Mvaa 80,224,192
Mvaa 88,36,156
L_ v6,28,s65,128
Mvaa 88,224,196
S_ s65,s59,3342336
Mvaa 92,24,196
S_ m0,s52,18304
Ta 36,%0,24584
Ta 38,%0,25096
Mvaa 92,20,156
S_ s55,s56,5505024
Mvaa 84,24,192
L_ v6,28,s65,128
Mvaa 84,20,152
S_ s65,s59,3440640
Ta 224,%0,28680
Ta 226,%0,29192
Mvaa 76,24,188
S_ m0,s52,19328
Mvaa 76,20,148
Mvaa 68,24,184
L_ v6,28,s65,128
Mvaa 68,20,144
Mvaa 60,24,180
Mvaa 60,20,140
W_ 4
Mvaa 96,28,140
s_nop 0
Ta 20,v1,24576
Ta 22,v1,25088
Mvaa 96,32,180
buffer_load_dword a254, v10, s[28:31], s55 offen
Mvaa 104,28,144
Mvaa 104,32,184
s_nop 0
Ta 24,v1,28672
Ta 26,v1,29184
Mvaa 112,28,148
Mvaa 112,32,188
buffer_load_dword a254, v12, s[24:27], s63 offen offset:896
Mvaa 120,28,152
Mvaa 120,32,192
Mvaa 128,28,156
Mvaa 128,32,196
W_ 4
Mvaa 132,224,196
s_nop 0
Ta 28,v1,24584
Ta 30,v1,25096
Mvaa 132,36,156
Mvaa 124,224,192
Mvaa 124,36,152
s_nop 0
Ta 32,v1,28680
Ta 34,v1,29192
Mvaa 116,224,188
Mvaa 116,36,148
Mvaa 108,224,184
Mvaa 108,36,144
Mvaa 100,224,180
s_waitcnt vmcnt(15) lgkmcnt(0)
s_barrier
Mvaa 100,36,140
S_ s65,s59,3145728
Da 60,v2,0
Mvaa 136,20,140
S_ m0,s52,24320
Ta 36,%0,0
Ta 38,%0,512
Mvaa 136,24,180
L_ v6,28,s65,256
Mvaa 144,20,144
S_ s65,s59,3244032
Da 100,v3,0
Mvaa 144,24,184
S_ m0,s52,25344
Ta 224,%0,4096
Ta 226,%0,4608
Mvaa 152,20,148
Mvaa 152,24,188
Da 64,v2,2048
Mvaa 160,20,152
L_ v6,28,s65,256
Mvaa 160,24,192
S_ s65,s59,3342336
Mvaa 168,20,156
S_ m0,s52,26368
Da 104,v3,2048
Mvaa 168,24,196
Mvaa 172,32,196
s_nop 0
Ta 20,%0,8
Ta 22,%0,520
Mvaa 172,28,156
Da 68,v2,4096
Mvaa 164,32,192
L_ v6,28,s65,256
Mvaa 164,28,152
S_ s65,s59,3440640
Ta 24,%0,4104
Ta 26,%0,4616
Mvaa 156,32,188
S_ m0,s52,27392
Da 108,v3,4096
Mvaa 156,28,148
Mvaa 148,32,184
L_ v6,28,s65,256
Mvaa 148,28,144
Da 72,v2,6144
Mvaa 140,32,180
Mvaa 140,28,140
W_ 9
Mvaa 176,36,140
s_nop 0
Ta 28,v1,0
Ta 30,v1,512
Mvaa 176,224,180
Da 112,v3,6144
Mvaa 184,36,144
Mvaa 184,224,184
s_nop 0
Ta 32,v1,4096
Ta 34,v1,4608
Mvaa 192,36,148
Da 76,v2,8192
Mvaa 192,224,188
Mvaa 200,36,152
Da 116,v3,8192
Mvaa 200,224,192
Mvaa 208,36,156
Mvaa 208,224,196
W_ 9
Mvaa 212,24,196
s_nop 0
Ta 36,v1,8
Ta 38,v1,520
Mvaa 212,20,156
Mvaa 204,24,192
Mvaa 204,20,152
s_nop 0
Ta 224,v1,4104
Ta 226,v1,4616
Mvaa 196,24,188
Mvaa 196,20,148
Mvaa 188,24,184
Mvaa 188,20,144
Mvaa 180,24,180
s_waitcnt vmcnt(15) lgkmcnt(0)
s_barrier
s_cmp_eq_u32 s11, 1
s_cbranch_scc1 .Lpace11
.Lpace11_back:
Mvaa 180,20,140
S_ s65,s59,3932160
Mvaa 216,28,140
S_ m0,s52,0
Ta 20,%0,8192
Ta 22,%0,8704
Mvaa 216,32,180
L_ v6,28,s65,0
Mvaa 224,28,144
S_ s65,s59,4030464
Mvaa 224,32,184
S_ m0,s52,1024
Ta 24,%0,12288
Ta 26,%0,12800
Mvaa 232,28,148
Mvaa 232,32,188
L_ v6,28,s65,0
Mvaa 240,28,152
S_ s65,s59,4128768
Mvaa 240,32,192
S_ m0,s52,2048
Mvaa 248,28,156
Mvaa 248,32,196
Mvaa 252,224,196
s_nop 0
Ta 28,%0,8200
Ta 30,%0,8712
Mvaa 252,36,156
L_ v6,28,s65,0
Mvaa 244,224,192
S_ s65,s59,4227072
Mvaa 244,36,152
S_ m0,s52,3072
Ta 32,%0,12296
Ta 34,%0,12808
Mvaa 236,224,188
Mvaa 236,36,148
L_ v6,28,s65,0
Mvaa 228,224,184
S_ s65,s60,768
Mvaa 228,36,144
S_ m0,s50,0
Mvaa 220,224,180
Mvaa 220,36,140
W_ 4
Mvaa 16,20,60
s_nop 0
Ta 36,v1,8192
Ta 38,v1,8704
Mvaa 16,24,100
L_ v4,24,s65,0
Mvaa 24,20,64
S_ s65,s60,11008
R_ v13,a248
Mvaa 24,24,104
S_ m0,s50,1024
Ta 224,v1,12288
Ta 226,v1,12800
Mvaa 32,20,68
v_min_u32 v13, 240, v13
Mvaa 32,24,108
L_ v5,24,s65,0
Mvaa 40,20,72
S_ s65,s60,21248
v_lshlrev_b32 v13, 3, v13
Mvaa 40,24,112
S_ m0,s50,2048
Mvaa 48,20,76
Mvaa 48,24,116
W_ 4
Mvaa 52,32,116
s_nop 0
Ta 20,v1,8200
Ta 22,v1,8712
Mvaa 52,28,76
L_ v4,24,s65,0
Mvaa 44,32,112
S_ s65,s60,31488
Mvaa 44,28,72
S_ m0,s50,3072
Ta 24,v1,12296
Ta 26,v1,12808
Mvaa 36,32,108
Mvaa 36,28,68
L_ v5,24,s65,0
Mvaa 28,32,104
S_ s65,s60,41728
Mvaa 28,28,64
S_ m0,s50,4096
Mvaa 20,32,100
s_waitcnt vmcnt(14) lgkmcnt(0)
s_barrier
Mvaa 20,28,60
L_ v4,24,s65,0
Mvaa 56,36,60
S_ s65,s59,3932160
Ta 28,%0,16384
Ta 30,%0,16896
Mvaa 56,224,100
S_ m0,s52,8064
Mvaa 64,36,64
Mvaa 64,224,104
s_nop 0
Ta 32,%0,20480
Ta 34,%0,20992
Mvaa 72,36,68
L_ v6,28,s65,128
Mvaa 72,224,108
S_ s65,s59,4030464
Mvaa 80,36,72
S_ m0,s52,9088
Mvaa 80,224,112
Mvaa 88,36,76
L_ v6,28,s65,128
Mvaa 88,224,116
S_ s65,s59,4128768
Mvaa 92,24,116
S_ m0,s52,10112
Ta 36,%0,16392
Ta 38,%0,16904
Mvaa 92,20,76
S_ s55,s56,6291456
Mvaa 84,24,112
L_ v6,28,s65,128
Mvaa 84,20,72
S_ s65,s59,4227072
Ta 224,%0,20488
Ta 226,%0,21000
Mvaa 76,24,108
S_ m0,s52,11136
Mvaa 76,20,68
Mvaa 68,24,104
L_ v6,28,s65,128
Mvaa 68,20,64
Mvaa 60,24,100
Mvaa 60,20,60
W_ 4
Mvaa 96,28,60
s_nop 0
Ta 20,v1,16384
Ta 22,v1,16896
Mvaa 96,32,100
buffer_load_dword a220, v13, s[44:47], s66 offen
Mvaa 104,28,64
Mvaa 104,32,104
s_nop 0
Ta 24,v1,20480
Ta 26,v1,20992
Mvaa 112,28,68
Mvaa 112,32,108
buffer_load_dword a254, v10, s[28:31], s55 offen
Mvaa 120,28,72
Mvaa 120,32,112
Mvaa 128,28,76
Mvaa 128,32,116
W_ 4
Mvaa 132,224,116
s_nop 0
Ta 28,v1,16392
Ta 30,v1,16904
Mvaa 132,36,76
buffer_load_dword a254, v12, s[24:27], s63 offen offset:1024
Mvaa 124,224,112
Mvaa 124,36,72
s_nop 0
Ta 32,v1,20488
Ta 34,v1,21000
Mvaa 116,224,108
Mvaa 116,36,68
Mvaa 108,224,104
Mvaa 108,36,64
Mvaa 100,224,100
s_waitcnt vmcnt(16) lgkmcnt(0)
s_barrier
s_cmp_eq_u32 s11, 1
s_cbranch_scc1 .Lpace13
.Lpace13_back:
Mvaa 100,36,60
S_ s65,s59,3932160
Da 140,v2,20480
Mvaa 136,20,60
S_ m0,s52,16128
Ta 36,%0,24576
Ta 38,%0,25088
Mvaa 136,24,100
L_ v6,28,s65,256
Mvaa 144,20,64
S_ s65,s59,4030464
Da 180,v3,20480
Mvaa 144,24,104
S_ m0,s52,17152
Ta 224,%0,28672
Ta 226,%0,29184
Mvaa 152,20,68
Mvaa 152,24,108
Da 144,v2,22528
Mvaa 160,20,72
L_ v6,28,s65,256
Mvaa 160,24,112
S_ s65,s59,4128768
Mvaa 168,20,76
S_ m0,s52,18176
Da 184,v3,22528
Mvaa 168,24,116
Mvaa 172,32,116
s_nop 0
Ta 20,%0,24584
Ta 22,%0,25096
Mvaa 172,28,76
Da 148,v2,24576
Mvaa 164,32,112
L_ v6,28,s65,256
Mvaa 164,28,72
S_ s65,s59,4227072
Ta 24,%0,28680
Ta 26,%0,29192
Mvaa 156,32,108
S_ m0,s52,19200
Da 188,v3,24576
Mvaa 156,28,68
Mvaa 148,32,104
L_ v6,28,s65,256
Mvaa 148,28,64
Da 152,v2,26624
Mvaa 140,32,100
Mvaa 140,28,60
W_ 9
Mvaa 176,36,60
s_nop 0
Ta 28,v1,24576
Ta 30,v1,25088
Mvaa 176,224,100
Da 192,v3,26624
Mvaa 184,36,64
Mvaa 184,224,104
s_nop 0
Ta 32,v1,28672
Ta 34,v1,29184
Mvaa 192,36,68
Da 156,v2,28672
Mvaa 192,224,108
Mvaa 200,36,72
Da 196,v3,28672
Mvaa 200,224,112
Mvaa 208,36,76
Mvaa 208,224,116
W_ 9
Mvaa 212,24,116
s_nop 0
Ta 36,v1,24584
Ta 38,v1,25096
Mvaa 212,20,76
Mvaa 204,24,112
Mvaa 204,20,72
s_nop 0
Ta 224,v1,28680
Ta 226,v1,29192
Mvaa 196,24,108
Mvaa 196,20,68
Mvaa 188,24,104
Mvaa 188,20,64
Mvaa 180,24,100
s_waitcnt vmcnt(16) lgkmcnt(0)
s_barrier
Mvaa 180,20,60
S_ s65,s59,4718592
Mvaa 216,28,60
S_ m0,s52,24576
Ta 20,%0,0
Ta 22,%0,512
Mvaa 216,32,100
L_ v6,28,s65,0
Mvaa 224,28,64
S_ s65,s59,4816896
Mvaa 224,32,104
S_ m0,s52,25600
Ta 24,%0,4096
Ta 26,%0,4608
Mvaa 232,28,68
Mvaa 232,32,108
L_ v6,28,s65,0
Mvaa 240,28,72
S_ s65,s59,4915200
Mvaa 240,32,112
S_ m0,s52,26624
Mvaa 248,28,76
Mvaa 248,32,116
Mvaa 252,224,116
s_nop 0
Ta 28,%0,8
Ta 30,%0,520
Mvaa 252,36,76
L_ v6,28,s65,0
Mvaa 244,224,112
S_ s65,s59,5013504
Mvaa 244,36,72
S_ m0,s52,27648
Ta 32,%0,4104
Ta 34,%0,4616
Mvaa 236,224,108
Mvaa 236,36,68
L_ v6,28,s65,0
Mvaa 228,224,104
S_ s65,s60,896
Mvaa 228,36,64
S_ m0,s50,20480
Mvaa 220,224,100
Mvaa 220,36,60
W_ 4
Mvaa 16,20,140
s_nop 0
Ta 36,v1,0
Ta 38,v1,512
Mvaa 16,24,180
L_ v4,24,s65,0
Mvaa 24,20,144
S_ s65,s60,11136
Mvaa 24,24,184
S_ m0,s50,21504
Ta 224,v1,4096
Ta 226,v1,4608
Mvaa 32,20,148
Mvaa 32,24,188
L_ v5,24,s65,0
Mvaa 40,20,152
S_ s65,s60,21376
Mvaa 40,24,192
S_ m0,s50,22528
Mvaa 48,20,156
Mvaa 48,24,196
W_ 4
Mvaa 52,32,196
s_nop 0
Ta 20,v1,8
Ta 22,v1,520
Mvaa 52,28,156
L_ v4,24,s65,0
Mvaa 44,32,192
S_ s65,s60,31616
Mvaa 44,28,152
S_ m0,s50,23552
Ta 24,v1,4104
Ta 26,v1,4616
Mvaa 36,32,188
Mvaa 36,28,148
L_ v5,24,s65,0
Mvaa 28,32,184
S_ s65,s60,41856
Mvaa 28,28,144
S_ m0,s50,24576
Mvaa 20,32,180
s_waitcnt vmcnt(15) lgkmcnt(0)
s_barrier
s_cmp_eq_u32 s11, 1
s_cbranch_scc1 .Lpace15
.Lpace15_back:
Mvaa 20,28,140
L_ v4,24,s65,0
Mvaa 56,36,140
S_ s65,s59,4718592
Ta 28,%0,8192
Ta 30,%0,8704
Mvaa 56,224,180
S_ m0,s52,-128
Mvaa 64,36,144
Mvaa 64,224,184
s_nop 0
Ta 32,%0,12288
Ta 34,%0,12800
Mvaa 72,36,148
L_ v6,28,s65,128
Mvaa 72,224,188
S_ s65,s59,4816896
Mvaa 80,36,152
S_ m0,s52,896
Mvaa 80,224,192
Mvaa 88,36,156
L_ v6,28,s65,128
Mvaa 88,224,196
S_ s65,s59,4915200
Mvaa 92,24,196
S_ m0,s52,1920
Ta 36,%0,8200
Ta 38,%0,8712
Mvaa 92,20,156
S_ s55,s56,7077888
Mvaa 84,24,192
L_ v6,28,s65,128
Mvaa 84,20,152
S_ s65,s59,5013504
Ta 224,%0,12296
Ta 226,%0,12808
Mvaa 76,24,188
S_ m0,s52,2944
Mvaa 76,20,148
Mvaa 68,24,184
L_ v6,28,s65,128
Mvaa 68,20,144
Mvaa 60,24,180
Mvaa 60,20,140
W_ 4
Mvaa 96,28,140
s_nop 0
Ta 20,v1,8192
Ta 22,v1,8704
Mvaa 96,32,180
buffer_load_dword a254, v10, s[28:31], s55 offen
Mvaa 104,28,144
Mvaa 104,32,184
s_nop 0
Ta 24,v1,12288
Ta 26,v1,12800
Mvaa 112,28,148
Mvaa 112,32,188
buffer_load_dword a254, v12, s[24:27], s63 offen offset:1152
Mvaa 120,28,152
Mvaa 120,32,192
Mvaa 128,28,156
Mvaa 128,32,196
W_ 4
Mvaa 132,224,196
s_nop 0
Ta 28,v1,8200
Ta 30,v1,8712
Mvaa 132,36,156
Mvaa 124,224,192
Mvaa 124,36,152
s_nop 0
Ta 32,v1,12296
Ta 34,v1,12808
Mvaa 116,224,188
Mvaa 116,36,148
Mvaa 108,224,184
Mvaa 108,36,144
Mvaa 100,224,180
s_waitcnt vmcnt(15) lgkmcnt(0)
s_barrier
Mvaa 100,36,140
S_ s65,s59,4718592
Da 60,v2,0
Mvaa 136,20,140
S_ m0,s52,7936
Ta 36,%0,16384
Ta 38,%0,16896
Mvaa 136,24,180
L_ v6,28,s65,256
Mvaa 144,20,144
S_ s65,s59,4816896
Da 100,v3,0
Mvaa 144,24,184
S_ m0,s52,8960
Ta 224,%0,20480
Ta 226,%0,20992
Mvaa 152,20,148
Mvaa 152,24,188
Da 64,v2,2048
Mvaa 160,20,152
L_ v6,28,s65,256
Mvaa 160,24,192
S_ s65,s59,4915200
Mvaa 168,20,156
S_ m0,s52,9984
Da 104,v3,2048
Mvaa 168,24,196
Mvaa 172,32,196
s_nop 0
Ta 20,%0,16392
Ta 22,%0,16904
Mvaa 172,28,156
Da 68,v2,4096
Mvaa 164,32,192
L_ v6,28,s65,256
Mvaa 164,28,152
S_ s65,s59,5013504
Ta 24,%0,20488
Ta 26,%0,21000
Mvaa 156,32,188
S_ m0,s52,11008
Da 108,v3,4096
Mvaa 156,28,148
Mvaa 148,32,184
L_ v6,28,s65,256
Mvaa 148,28,144
Da 72,v2,6144
Mvaa 140,32,180
Mvaa 140,28,140
W_ 9
Mvaa 176,36,140
s_nop 0
Ta 28,v1,16384
Ta 30,v1,16896
Mvaa 176,224,180
Da 112,v3,6144
Mvaa 184,36,144
Mvaa 184,224,184
s_nop 0
Ta 32,v1,20480
Ta 34,v1,20992
Mvaa 192,36,148
Da 76,v2,8192
Mvaa 192,224,188
Mvaa 200,36,152
Da 116,v3,8192
Mvaa 200,224,192
Mvaa 208,36,156
Mvaa 208,224,196
W_ 9
Mvaa 212,24,196
s_nop 0
Ta 36,v1,16392
Ta 38,v1,16904
Mvaa 212,20,156
Mvaa 204,24,192
Mvaa 204,20,152
s_nop 0
Ta 224,v1,20488
Ta 226,v1,21000
Mvaa 196,24,188
Mvaa 196,20,148
Mvaa 188,24,184
Mvaa 188,20,144
Mvaa 180,24,180
s_waitcnt vmcnt(15) lgkmcnt(0)
s_barrier
s_cmp_eq_u32 s11, 1
s_cbranch_scc1 .Lpace17
.Lpace17_back:
Mvaa 180,20,140
S_ s65,s59,5505024
Mvaa 216,28,140
S_ m0,s52,16384
Ta 20,%0,24576
Ta 22,%0,25088
Mvaa 216,32,180
L_ v6,28,s65,0
Mvaa 224,28,144
S_ s65,s59,5603328
Mvaa 224,32,184
S_ m0,s52,17408
Ta 24,%0,28672
Ta 26,%0,29184
Mvaa 232,28,148
Mvaa 232,32,188
L_ v6,28,s65,0
Mvaa 240,28,152
S_ s65,s59,5701632
Mvaa 240,32,192
S_ m0,s52,18432
Mvaa 248,28,156
Mvaa 248,32,196
Mvaa 252,224,196
s_nop 0
Ta 28,%0,24584
Ta 30,%0,25096
Mvaa 252,36,156
L_ v6,28,s65,0
Mvaa 244,224,192
S_ s65,s59,5799936
Mvaa 244,36,152
S_ m0,s52,19456
Ta 32,%0,28680
Ta 34,%0,29192
Mvaa 236,224,188
Mvaa 236,36,148
L_ v6,28,s65,0
Mvaa 228,224,184
S_ s65,s60,1024
Mvaa 228,36,144
S_ m0,s50,0
Mvaa 220,224,180
Mvaa 220,36,140
W_ 4
Mvaa 16,20,60
s_nop 0
Ta 36,v1,24576
Ta 38,v1,25088
Mvaa 16,24,100
L_ v4,24,s65,0
Mvaa 24,20,64
S_ s65,s60,11264
Mvaa 24,24,104
S_ m0,s50,1024
Ta 224,v1,28672
Ta 226,v1,29184
Mvaa 32,20,68
Mvaa 32,24,108
L_ v5,24,s65,0
Mvaa 40,20,72
S_ s65,s60,21504
Mvaa 40,24,112
S_ m0,s50,2048
Mvaa 48,20,76
Mvaa 48,24,116
W_ 4
Mvaa 52,32,116
s_nop 0
Ta 20,v1,24584
Ta 22,v1,25096
Mvaa 52,28,76
L_ v4,24,s65,0
Mvaa 44,32,112
S_ s65,s60,31744
Mvaa 44,28,72
S_ m0,s50,3072
Ta 24,v1,28680
Ta 26,v1,29192
Mvaa 36,32,108
Mvaa 36,28,68
L_ v5,24,s65,0
Mvaa 28,32,104
S_ s65,s60,41984
Mvaa 28,28,64
S_ m0,s50,4096
Mvaa 20,32,100
s_waitcnt vmcnt(14) lgkmcnt(0)
s_barrier
Mvaa 20,28,60
L_ v4,24,s65,0
Mvaa 56,36,60
S_ s65,s59,5505024
Ta 28,%0,0
Ta 30,%0,512
Mvaa 56,224,100
S_ m0,s52,24448
Mvaa 64,36,64
Mvaa 64,224,104
s_nop 0
Ta 32,%0,4096
Ta 34,%0,4608
Mvaa 72,36,68
L_ v6,28,s65,128
Mvaa 72,224,108
S_ s65,s59,5603328
Mvaa 80,36,72
S_ m0,s52,25472
Mvaa 80,224,112
Mvaa 88,36,76
L_ v6,28,s65,128
Mvaa 88,224,116
S_ s65,s59,5701632
Mvaa 92,24,116
S_ m0,s52,26496
Ta 36,%0,8
Ta 38,%0,520
Mvaa 92,20,76
Mvaa 84,24,112
L_ v6,28,s65,128
Mvaa 84,20,72
S_ s65,s59,5799936
Ta 224,%0,4104
Ta 226,%0,4616
Mvaa 76,24,108
S_ m0,s52,27520
Mvaa 76,20,68
Mvaa 68,24,104
L_ v6,28,s65,128
Mvaa 68,20,64
S_ s65,s61,0
Mvaa 60,24,100
S_ m0,s51,36864
Mvaa 60,20,60
W_ 4
Mvaa 96,28,60
s_nop 0
Ta 20,v1,0
Ta 22,v1,512
Mvaa 96,32,100
Ln v7,32,s65,0
Mvaa 104,28,64
Mvaa 104,32,104
s_nop 0
Ta 24,v1,4096
Ta 26,v1,4608
Mvaa 112,28,68
Mvaa 112,32,108
Ln v8,32,s65,3072
Mvaa 120,28,72
S_ m0,s51,37760
Mvaa 120,32,112
Mvaa 128,28,76
Mvaa 128,32,116
W_ 4
Mvaa 132,224,116
s_nop 0
Ta 28,v1,8
Ta 30,v1,520
Mvaa 132,36,76
Ln v7,32,s65,128
Mvaa 124,224,112
Mvaa 124,36,72
s_nop 0
Ta 32,v1,4104
Ta 34,v1,4616
Mvaa 116,224,108
Mvaa 116,36,68
Ln v8,32,s65,3200
Mvaa 108,224,104
S_ m0,s51,38656
Mvaa 108,36,64
Mvaa 100,224,100
s_waitcnt vmcnt(17) lgkmcnt(0)
s_barrier
s_cmp_eq_u32 s11, 1
s_cbranch_scc1 .Lpace19
.Lpace19_back:
Mvaa 100,36,60
Da 140,v2,20480
Mvaa 136,20,60
s_nop 0
Ta 36,%0,8192
Ta 38,%0,8704
Mvaa 136,24,100
Ln v7,32,s65,256
Mvaa 144,20,64
Da 180,v3,20480
Mvaa 144,24,104
s_nop 0
Ta 224,%0,12288
Ta 226,%0,12800
Mvaa 152,20,68
Mvaa 152,24,108
Da 144,v2,22528
Mvaa 160,20,72
Ln v8,32,s65,3328
Mvaa 160,24,112
S_ s65,s59,5505024
Mvaa 168,20,76
S_ m0,s52,-256
Da 184,v3,22528
Mvaa 168,24,116
Mvaa 172,32,116
s_nop 0
Ta 20,%0,8200
Ta 22,%0,8712
Mvaa 172,28,76
Da 148,v2,24576
Mvaa 164,32,112
L_ v6,28,s65,256
Mvaa 164,28,72
S_ s65,s59,5603328
Ta 24,%0,12296
Ta 26,%0,12808
Mvaa 156,32,108
S_ m0,s52,768
Da 188,v3,24576
Mvaa 156,28,68
Mvaa 148,32,104
L_ v6,28,s65,256
Mvaa 148,28,64
S_ s65,s59,5701632
Da 152,v2,26624
Mvaa 140,32,100
S_ m0,s52,1792
Mvaa 140,28,60
W_ 9
Mvaa 176,36,60
s_nop 0
Ta 28,v1,8192
Ta 30,v1,8704
Mvaa 176,224,100
Da 192,v3,26624
Mvaa 184,36,64
L_ v6,28,s65,256
Mvaa 184,224,104
S_ s65,s59,5799936
Ta 32,v1,12288
Ta 34,v1,12800
Mvaa 192,36,68
S_ m0,s52,2816
Da 156,v2,28672
Mvaa 192,224,108
Mvaa 200,36,72
Da 196,v3,28672
Mvaa 200,224,112
L_ v6,28,s65,256
Mvaa 208,36,76
Mvaa 208,224,116
W_ 9
Mvaa 212,24,116
s_nop 0
Ta 36,v1,8200
Ta 38,v1,8712
Mvaa 212,20,76
Mvaa 204,24,112
Mvaa 204,20,72
s_nop 0
Ta 224,v1,12296
Ta 226,v1,12808
Mvaa 196,24,108
Mvaa 196,20,68
Mvaa 188,24,104
Mvaa 188,20,64
Mvaa 180,24,100
s_waitcnt vmcnt(19) lgkmcnt(0)
s_barrier
Mvaa 180,20,60
S_ s65,s59,6291456
Mvaa 216,28,60
S_ m0,s52,7936
Ta 20,%0,16384
Ta 22,%0,16896
Mvaa 216,32,100
L_ v6,28,s65,256
Mvaa 224,28,64
S_ s65,s59,6389760
Mvaa 224,32,104
S_ m0,s52,8960
Ta 24,%0,20480
Ta 26,%0,20992
Mvaa 232,28,68
Mvaa 232,32,108
L_ v6,28,s65,256
Mvaa 240,28,72
S_ s65,s59,6488064
Mvaa 240,32,112
S_ m0,s52,9984
Mvaa 248,28,76
Mvaa 248,32,116
Mvaa 252,224,116
s_nop 0
Ta 28,%0,16392
Ta 30,%0,16904
Mvaa 252,36,76
L_ v6,28,s65,256
Mvaa 244,224,112
S_ s65,s59,6586368
Mvaa 244,36,72
S_ m0,s52,11008
Ta 32,%0,20488
Ta 34,%0,21000
Mvaa 236,224,108
Mvaa 236,36,68
L_ v6,28,s65,256
Mvaa 228,224,104
S_ s65,s60,1152
Mvaa 228,36,64
S_ m0,s50,20480
Mvaa 220,224,100
Mvaa 220,36,60
W_ 4
Mvaa 16,20,140
s_nop 0
Ta 36,v1,16384
Ta 38,v1,16896
Mvaa 16,24,180
L_ v4,24,s65,0
Mvaa 24,20,144
S_ s65,s60,11392
Mvaa 24,24,184
S_ m0,s50,21504
Ta 224,v1,20480
Ta 226,v1,20992
Mvaa 32,20,148
Mvaa 32,24,188
L_ v5,24,s65,0
Mvaa 40,20,152
S_ s65,s60,21632
Mvaa 40,24,192
S_ m0,s50,22528
Mvaa 48,20,156
Mvaa 48,24,196
W_ 4
Mvaa 52,32,196
s_nop 0
Ta 20,v1,16392
Ta 22,v1,16904
Mvaa 52,28,156
L_ v4,24,s65,0
Mvaa 44,32,192
S_ s65,s60,31872
Mvaa 44,28,152
S_ m0,s50,23552
Ta 24,v1,20488
Ta 26,v1,21000
Mvaa 36,32,188
Mvaa 36,28,148
L_ v5,24,s65,0
Mvaa 28,32,184
S_ s65,s60,42112
Mvaa 28,28,144
S_ m0,s50,24576
Mvaa 20,32,180
s_waitcnt vmcnt(18) lgkmcnt(0)
s_barrier
s_cmp_eq_u32 s11, 1
s_cbranch_scc1 .Lpace21
.Lpace21_back:
Mvaa 20,28,140
L_ v4,24,s65,0
Mvaa 56,36,140
S_ s65,s59,7077888
Ta 28,%0,24576
Ta 30,%0,25088
Mvaa 56,224,180
S_ m0,s52,16128
Mvaa 64,36,144
Mvaa 64,224,184
s_nop 0
Ta 32,%0,28672
Ta 34,%0,29184
Mvaa 72,36,148
L_ v6,28,s65,256
Mvaa 72,224,188
S_ s65,s59,7176192
Mvaa 80,36,152
S_ m0,s52,17152
Mvaa 80,224,192
Mvaa 88,36,156
L_ v6,28,s65,256
Mvaa 88,224,196
S_ s65,s59,7274496
Mvaa 92,24,196
S_ m0,s52,18176
Ta 36,%0,24584
Ta 38,%0,25096
Mvaa 92,20,156
Mvaa 84,24,192
L_ v6,28,s65,256
Mvaa 84,20,152
S_ s65,s59,7372800
Ta 224,%0,28680
Ta 226,%0,29192
Mvaa 76,24,188
S_ m0,s52,19200
Mvaa 76,20,148
Mvaa 68,24,184
L_ v6,28,s65,256
Mvaa 68,20,144
S_ s65,s61,12288
Mvaa 60,24,180
S_ m0,s51,49152
Mvaa 60,20,140
W_ 4
Mvaa 96,28,140
s_nop 0
Ta 20,v1,24576
Ta 22,v1,25088
Mvaa 96,32,180
Ln v7,32,s65,0
Mvaa 104,28,144
Mvaa 104,32,184
s_nop 0
Ta 24,v1,28672
Ta 26,v1,29184
Mvaa 112,28,148
Mvaa 112,32,188
Ln v8,32,s65,3072
Mvaa 120,28,152
S_ m0,s51,50048
Mvaa 120,32,192
Mvaa 128,28,156
Mvaa 128,32,196
W_ 4
Mvaa 132,224,196
s_nop 0
Ta 28,v1,24584
Ta 30,v1,25096
Mvaa 132,36,156
Ln v7,32,s65,128
Mvaa 124,224,192
Mvaa 124,36,152
s_nop 0
Ta 32,v1,28680
Ta 34,v1,29192
Mvaa 116,224,188
Mvaa 116,36,148
Ln v8,32,s65,3200
Mvaa 108,224,184
S_ m0,s51,50944
Mvaa 108,36,144
Mvaa 100,224,180
s_waitcnt vmcnt(17) lgkmcnt(0)
s_barrier
Mvaa 100,36,140
Da 60,v2,0
Mvaa 136,20,140
s_nop 0
Ta 36,%0,0
Ta 38,%0,512
Mvaa 136,24,180
Ln v7,32,s65,256
Mvaa 144,20,144
Da 100,v3,0
Mvaa 144,24,184
s_nop 0
Ta 224,%0,4096
Ta 226,%0,4608
Mvaa 152,20,148
Mvaa 152,24,188
Da 64,v2,2048
Mvaa 160,20,152
Ln v8,32,s65,3328
Mvaa 160,24,192
S_ s65,s59,6291456
Mvaa 168,20,156
S_ m0,s52,24448
Da 104,v3,2048
Mvaa 168,24,196
Mvaa 172,32,196
s_nop 0
Ta 20,%0,8
Ta 22,%0,520
Mvaa 172,28,156
Da 68,v2,4096
Mvaa 164,32,192
L_ v6,28,s65,128
Mvaa 164,28,152
S_ s65,s59,6389760
Ta 24,%0,4104
Ta 26,%0,4616
Mvaa 156,32,188
S_ m0,s52,25472
Da 108,v3,4096
Mvaa 156,28,148
Mvaa 148,32,184
L_ v6,28,s65,128
Mvaa 148,28,144
S_ s65,s59,6488064
Da 72,v2,6144
Mvaa 140,32,180
S_ m0,s52,26496
Mvaa 140,28,140
W_ 9
Mvaa 176,36,140
s_nop 0
Ta 28,v1,0
Ta 30,v1,512
Mvaa 176,224,180
Da 112,v3,6144
Mvaa 184,36,144
L_ v6,28,s65,128
Mvaa 184,224,184
S_ s65,s59,6586368
Ta 32,v1,4096
Ta 34,v1,4608
Mvaa 192,36,148
S_ m0,s52,27520
Da 76,v2,8192
Mvaa 192,224,188
Mvaa 200,36,152
Da 116,v3,8192
Mvaa 200,224,192
L_ v6,28,s65,128
Mvaa 208,36,156
Mvaa 208,224,196
W_ 9
Mvaa 212,24,196
s_nop 0
Ta 36,v1,8
Ta 38,v1,520
Mvaa 212,20,156
Mvaa 204,24,192
Mvaa 204,20,152
s_nop 0
Ta 224,v1,4104
Ta 226,v1,4616
Mvaa 196,24,188
Mvaa 196,20,148
Mvaa 188,24,184
Mvaa 188,20,144
Mvaa 180,24,180
s_waitcnt vmcnt(14) lgkmcnt(0)
s_barrier
s_cmp_eq_u32 s11, 1
s_cbranch_scc1 .Lpace23
.Lpace23_back:
Mvaa 180,20,140
S_ s65,s59,7077888
Mvaa 216,28,140
S_ m0,s52,-128
Ta 20,v1,8200
Ta 22,v1,8712
Mvaa 216,32,180
L_ v6,28,s65,128
Mvaa 224,28,144
S_ s65,s59,7176192
Mvaa 224,32,184
S_ m0,s52,896
Ta 24,v1,12296
Ta 26,v1,12808
Mvaa 232,28,148
Mvaa 232,32,188
L_ v6,28,s65,128
Mvaa 240,28,152
S_ s65,s59,7274496
Mvaa 240,32,192
S_ m0,s52,1920
Mvaa 248,28,156
Mvaa 248,32,196
Mvaa 252,224,196
s_nop 0
Ta 28,v1,8192
Ta 30,v1,8704
Mvaa 252,36,156
L_ v6,28,s65,128
Mvaa 244,224,192
S_ s65,s59,7372800
Mvaa 244,36,152
S_ m0,s52,2944
Ta 32,v1,12288
Ta 34,v1,12800
Mvaa 236,224,188
Mvaa 236,36,148
L_ v6,28,s65,128
Mvaa 228,224,184
Mvaa 228,36,144
Mvaa 220,224,180
Mvaa 220,36,140
W_ 4
Mvaa 220,20,60
s_nop 0
Ta 36,%0,8200
Ta 38,%0,8712
Mvaa 220,24,100
Da 140,v2,20480
Mvaa 228,20,64
Mvaa 228,24,104
s_nop 0
Ta 224,%0,12296
Ta 226,%0,12808
Mvaa 236,20,68
Da 180,v3,20480
Mvaa 236,24,108
Mvaa 244,20,72
Da 144,v2,22528
Mvaa 244,24,112
Mvaa 252,20,76
Mvaa 252,24,116
W_ 7
Da 184,v3,22528
Mvaa 248,32,116
s_nop 0
Ta 20,%0,8192
Ta 22,%0,8704
Mvaa 248,28,76
Mvaa 240,32,112
Da 148,v2,24576
Mvaa 240,28,72
s_nop 0
Ta 24,%0,12288
Ta 26,%0,12800
Mvaa 232,32,108
Mvaa 232,28,68
Da 188,v3,24576
Mvaa 224,32,104
Mvaa 224,28,64
Mvaa 216,32,100
s_waitcnt vmcnt(14) lgkmcnt(0)
s_barrier
Da 152,v2,26624
Mvaa 216,28,60
S_ s65,s59,6291456
Mvaa 180,36,60
S_ m0,s52,8192
Ta 28,v1,16392
Ta 30,v1,16904
Mvaa 180,224,100
Da 192,v3,26624
Mvaa 188,36,64
L_ v6,28,s65,0
Mvaa 188,224,104
S_ s65,s59,6389760
Ta 32,v1,20488
Ta 34,v1,21000
Mvaa 196,36,68
S_ m0,s52,9216
Da 156,v2,28672
Mvaa 196,224,108
Mvaa 204,36,72
L_ v6,28,s65,0
Mvaa 204,224,112
S_ s65,s59,6488064
Da 196,v3,28672
Mvaa 212,36,76
S_ m0,s52,10240
Mvaa 212,224,116
Mvaa 208,24,116
s_nop 0
Ta 36,v1,16384
Ta 38,v1,16896
Mvaa 208,20,76
L_ v6,28,s65,0
Mvaa 200,24,112
S_ s65,s59,6586368
Mvaa 200,20,72
S_ m0,s52,11264
Ta 224,v1,20480
Ta 226,v1,20992
Mvaa 192,24,108
Mvaa 192,20,68
L_ v6,28,s65,0
Mvaa 184,24,104
Mvaa 184,20,64
Mvaa 176,24,100
Mvaa 176,20,60
W_ 6
Mvaa 220,28,140
s_nop 0
Ta 20,%0,16392
Ta 22,%0,16904
Mvaa 220,32,180
Mvaa 228,28,144
Mvaa 228,32,184
s_nop 0
Ta 24,%0,20488
Ta 26,%0,21000
Mvaa 236,28,148
Mvaa 236,32,188
Mvaa 244,28,152
Mvaa 244,32,192
W_ 9
Mvaa 252,28,156
W_ 8
Mvaa 252,32,196
W_ 4
Mvaa 248,224,196
s_nop 0
Ta 28,%0,16384
Ta 30,%0,16896
Mvaa 248,36,156
Mvaa 240,224,192
Mvaa 240,36,152
s_nop 0
Ta 32,%0,20480
Ta 34,%0,20992
Mvaa 232,224,188
Mvaa 232,36,148
Mvaa 224,224,184
Mvaa 224,36,144
Mvaa 216,224,180
s_waitcnt vmcnt(8) lgkmcnt(0)
s_barrier
s_cmp_eq_u32 s11, 1
s_cbranch_scc1 .Lpace25
.Lpace25_back:
Mvaa 216,36,140
S_ s65,s59,7077888
Mvaa 180,20,140
S_ m0,s52,16384
Ta 36,v1,24584
Ta 38,v1,25096
Mvaa 180,24,180
L_ v6,28,s65,0
Mvaa 188,20,144
S_ s65,s59,7176192
Mvaa 188,24,184
S_ m0,s52,17408
Ta 224,v1,28680
Ta 226,v1,29192
Mvaa 196,20,148
U_ v248,s48,v248
U_ v249,s48,v249
Mvaa 196,24,188
L_ v6,28,s65,0
Mvaa 204,20,152
S_ s65,s59,7274496
U_ v250,s48,v250
U_ v251,s48,v251
Mvaa 204,24,192
S_ m0,s52,18432
U_ v252,s48,v252
U_ v253,s48,v253
Mvaa 212,20,156
U_ v254,s48,v254
U_ v255,s48,v255
Mvaa 212,24,196
C_ 255,254,255
C_ 254,252,253
Mvaa 208,32,196
s_nop 0
Ta 20,v1,24576
Ta 22,v1,25088
Mvaa 208,28,156
L_ v6,28,s65,0
Mvaa 200,32,192
S_ s65,s59,7372800
C_ 253,250,251
C_ 252,248,249
Mvaa 200,28,152
S_ m0,s52,19456
Ta 24,v1,28672
Ta 26,v1,29184
Mvaa 192,32,188
U_ v240,s48,v240
U_ v241,s48,v241
Mvaa 192,28,148
L_ v6,28,s65,0
Mvaa 184,32,184
U_ v242,s48,v242
U_ v243,s48,v243
Mvaa 184,28,144
U_ v244,s48,v244
U_ v245,s48,v245
Mvaa 176,32,180
U_ v246,s48,v246
U_ v247,s48,v247
Mvaa 176,28,140
W_ 4
C_ 251,246,247
C_ 250,244,245
Mvaa 140,36,60
s_nop 0
Ta 28,%0,24584
Ta 30,%0,25096
Mvaa 140,224,100
C_ 249,242,243
C_ 248,240,241
Mvaa 148,36,64
U_ v232,s48,v232
U_ v233,s48,v233
Mvaa 148,224,104
s_nop 0
Ta 32,%0,28680
Ta 34,%0,29192
Mvaa 156,36,68
U_ v234,s48,v234
U_ v235,s48,v235
Mvaa 156,224,108
U_ v236,s48,v236
U_ v237,s48,v237
Mvaa 164,36,72
U_ v238,s48,v238
U_ v239,s48,v239
Mvaa 164,224,112
C_ 247,238,239
C_ 246,236,237
Mvaa 172,36,76
C_ 245,234,235
C_ 244,232,233
Mvaa 172,224,116
W_ 4
U_ v224,s48,v224
U_ v225,s48,v225
Mvaa 168,24,116
s_nop 0
Ta 36,%0,24576
Ta 38,%0,25088
Mvaa 168,20,76
U_ v226,s48,v226
U_ v227,s48,v227
Mvaa 160,24,112
U_ v228,s48,v228
U_ v229,s48,v229
Mvaa 160,20,72
s_nop 0
Ta 224,%0,28672
Ta 226,%0,29184
Mvaa 152,24,108
U_ v230,s48,v230
U_ v231,s48,v231
Mvaa 152,20,68
C_ 243,230,231
C_ 242,228,229
Mvaa 144,24,104
C_ 241,226,227
C_ 240,224,225
Mvaa 144,20,64
U_ v216,s48,v216
U_ v217,s48,v217
Mvaa 136,24,100
s_waitcnt vmcnt(8) lgkmcnt(0)
s_barrier
U_ v218,s48,v218
Mvaa 136,20,60
S_ s65,s61,0
U_ v219,s48,v219
Mvaa 100,28,60
S_ m0,s51,24576
Ta 20,v1,8
Ta 22,v1,520
Mvaa 100,32,100
Ln v9,36,s65,0
Mvaa 108,28,64
U_ v220,s48,v220
U_ v221,s48,v221
Mvaa 108,32,104
s_nop 0
Ta 24,v1,4104
Ta 26,v1,4616
Mvaa 116,28,68
U_ v222,s48,v222
U_ v223,s48,v223
Mvaa 116,32,108
Ln v9,36,s65,3072
Mvaa 124,28,72
S_ m0,s51,25472
C_ 239,222,223
C_ 238,220,221
Mvaa 124,32,112
C_ 237,218,219
C_ 236,216,217
Mvaa 132,28,76
U_ v208,s48,v208
U_ v209,s48,v209
Mvaa 132,32,116
U_ v210,s48,v210
U_ v211,s48,v211
Mvaa 128,224,116
s_nop 0
Ta 28,v1,0
Ta 30,v1,512
Mvaa 128,36,76
Ln v9,36,s65,128
Mvaa 120,224,112
U_ v212,s48,v212
U_ v213,s48,v213
Mvaa 120,36,72
s_nop 0
Ta 32,v1,4096
Ta 34,v1,4608
Mvaa 112,224,108
U_ v214,s48,v214
U_ v215,s48,v215
Mvaa 112,36,68
Ln v9,36,s65,3200
Mvaa 104,224,104
S_ m0,s51,26368
C_ 235,214,215
C_ 234,212,213
Mvaa 104,36,64
C_ 233,210,211
C_ 232,208,209
Mvaa 96,224,100
U_ v200,s48,v200
U_ v201,s48,v201
Mvaa 96,36,60
W_ 4
)A"
R"A(U_ v202,s48,v202
U_ v203,s48,v203
Mvaa 140,20,140
s_nop 0
Ta 36,%0,8
Ta 38,%0,520
Mvaa 140,24,180
Ln v9,36,s65,256
Mvaa 148,20,144
U_ v204,s48,v204
U_ v205,s48,v205
Mvaa 148,24,184
s_nop 0
Ta 224,%0,4104
Ta 226,%0,4616
Mvaa 156,20,148
U_ v206,s48,v206
U_ v207,s48,v207
Mvaa 156,24,188
Ln v9,36,s65,3328
Mvaa 164,20,152
C_ 231,206,207
C_ 230,204,205
Mvaa 164,24,192
C_ 229,202,203
C_ 228,200,201
Mvaa 172,20,156
U_ v192,s48,v192
U_ v193,s48,v193
Mvaa 172,24,196
W_ 4
U_ v194,s48,v194
U_ v195,s48,v195
Mvaa 168,32,196
s_nop 0
Ta 20,%0,0
Ta 22,%0,512
Mvaa 168,28,156
U_ v196,s48,v196
U_ v197,s48,v197
Mvaa 160,32,192
U_ v198,s48,v198
U_ v199,s48,v199
Mvaa 160,28,152
s_nop 0
Ta 24,%0,4096
Ta 26,%0,4608
Mvaa 152,32,188
C_ 227,198,199
C_ 226,196,197
Mvaa 152,28,148
C_ 225,194,195
C_ 224,192,193
Mvaa 144,32,184
U_ v184,s48,v184
U_ v185,s48,v185
Mvaa 144,28,144
U_ v186,s48,v186
U_ v187,s48,v187
Mvaa 136,32,180
s_waitcnt vmcnt(10) lgkmcnt(0)
s_barrier
s_cmp_eq_u32 s11, 1
s_cbranch_scc1 .Lpace27
.Lpace27_back:
U_ v188,s48,v188
Mvaa 136,28,140
U_ v189,s48,v189
Mvaa 100,36,140
s_nop 0
Ta 28,v1,8200
Ta 30,v1,8712
Mvaa 100,224,180
U_ v190,s48,v190
U_ v191,s48,v191
Mvaa 108,36,144
C_ 223,190,191
C_ 222,188,189
Mvaa 108,224,184
s_nop 0
Ta 32,v1,12296
Ta 34,v1,12808
Mvaa 116,36,148
C_ 221,186,187
C_ 220,184,185
Mvaa 116,224,188
U_ v176,s48,v176
U_ v177,s48,v177
Mvaa 124,36,152
U_ v178,s48,v178
U_ v179,s48,v179
Mvaa 124,224,192
U_ v180,s48,v180
U_ v181,s48,v181
Mvaa 132,36,156
U_ v182,s48,v182
U_ v183,s48,v183
Mvaa 132,224,196
C_ 219,182,183
C_ 218,180,181
Mvaa 128,24,196
s_nop 0
Ta 36,v1,8192
Ta 38,v1,8704
Mvaa 128,20,156
C_ 217,178,179
C_ 216,176,177
Mvaa 120,24,192
U_ v168,s48,v168
U_ v169,s48,v169
Mvaa 120,20,152
s_nop 0
Ta 224,v1,12288
Ta 226,v1,12800
Mvaa 112,24,188
U_ v170,s48,v170
U_ v171,s48,v171
Mvaa 112,20,148
U_ v172,s48,v172
U_ v173,s48,v173
Mvaa 104,24,184
U_ v174,s48,v174
U_ v175,s48,v175
Mvaa 104,20,144
C_ 215,174,175
C_ 214,172,173
Mvaa 96,24,180
C_ 213,170,171
C_ 212,168,169
Mvaa 96,20,140
W_ 4
U_ v160,s48,v160
U_ v161,s48,v161
Mvaa 60,28,60
s_nop 0
Ta 20,%0,8200
Ta 22,%0,8712
Mvaa 60,32,100
U_ v162,s48,v162
U_ v163,s48,v163
Mvaa 68,28,64
U_ v164,s48,v164
U_ v165,s48,v165
Mvaa 68,32,104
s_nop 0
Ta 24,%0,12296
Ta 26,%0,12808
Mvaa 76,28,68
U_ v166,s48,v166
U_ v167,s48,v167
Mvaa 76,32,108
C_ 211,166,167
C_ 210,164,165
Mvaa 84,28,72
C_ 209,162,163
C_ 208,160,161
Mvaa 84,32,112
U_ v152,s48,v152
U_ v153,s48,v153
Mvaa 92,28,76
U_ v154,s48,v154
U_ v155,s48,v155
Mvaa 92,32,116
W_ 4
U_ v156,s48,v156
U_ v157,s48,v157
Mvaa 88,224,116
s_nop 0
Ta 28,%0,8192
Ta 30,%0,8704
Mvaa 88,36,76
U_ v158,s48,v158
U_ v159,s48,v159
Mvaa 80,224,112
C_ 207,158,159
C_ 206,156,157
Mvaa 80,36,72
s_nop 0
Ta 32,%0,12288
Ta 34,%0,12800
Mvaa 72,224,108
C_ 205,154,155
C_ 204,152,153
Mvaa 72,36,68
U_ v144,s48,v144
U_ v145,s48,v145
Mvaa 64,224,104
U_ v146,s48,v146
U_ v147,s48,v147
Mvaa 64,36,64
U_ v148,s48,v148
U_ v149,s48,v149
Mvaa 56,224,100
s_waitcnt vmcnt(6) lgkmcnt(0)
s_barrier
U_ v150,s48,v150
Mvaa 56,36,60
S_ s65,s61,24576
U_ v151,s48,v151
Mvaa 20,20,60
S_ m0,s51,0
Ta 36,v1,16392
Ta 38,v1,16904
Mvaa 20,24,100
Ln v7,32,s65,0
Mvaa 28,20,64
C_ 203,150,151
C_ 202,148,149
Mvaa 28,24,104
s_nop 0
Ta 224,v1,20488
Ta 226,v1,21000
Mvaa 36,20,68
C_ 201,146,147
C_ 200,144,145
Mvaa 36,24,108
Ln v8,32,s65,3072
Mvaa 44,20,72
S_ m0,s51,896
U_ v136,s48,v136
U_ v137,s48,v137
Mvaa 44,24,112
U_ v138,s48,v138
U_ v139,s48,v139
Mvaa 52,20,76
U_ v140,s48,v140
U_ v141,s48,v141
Mvaa 52,24,116
U_ v142,s48,v142
U_ v143,s48,v143
Mvaa 48,32,116
s_nop 0
Ta 20,v1,16384
Ta 22,v1,16896
Mvaa 48,28,76
Ln v7,32,s65,128
Mvaa 40,32,112
C_ 199,142,143
C_ 198,140,141
Mvaa 40,28,72
s_nop 0
Ta 24,v1,20480
Ta 26,v1,20992
Mvaa 32,32,108
C_ 197,138,139
C_ 196,136,137
Mvaa 32,28,68
Ln v8,32,s65,3200
Mvaa 24,32,104
S_ m0,s51,1792
U_ v128,s48,v128
U_ v129,s48,v129
Mvaa 24,28,64
U_ v130,s48,v130
U_ v131,s48,v131
Mvaa 16,32,100
U_ v132,s48,v132
U_ v133,s48,v133
Mvaa 16,28,60
W_ 4
U_ v134,s48,v134
U_ v135,s48,v135
Mvaa 60,36,140
s_nop 0
Ta 28,%0,16392
Ta 30,%0,16904
Mvaa 60,224,180
Ln v7,32,s65,256
Mvaa 68,36,144
C_ 195,134,135
C_ 194,132,133
Mvaa 68,224,184
s_nop 0
Ta 32,%0,20488
Ta 34,%0,21000
Mvaa 76,36,148
C_ 193,130,131
C_ 192,128,129
Mvaa 76,224,188
Ln v8,32,s65,3328
Mvaa 84,36,152
U_ v120,s48,v120
U_ v121,s48,v121
Mvaa 84,224,192
U_ v122,s48,v122
U_ v123,s48,v123
Mvaa 92,36,156
U_ v124,s48,v124
U_ v125,s48,v125
Mvaa 92,224,196
W_ 4
U_ v126,s48,v126
U_ v127,s48,v127
Mvaa 88,24,196
s_nop 0
Ta 36,%0,16384
Ta 38,%0,16896
Mvaa 88,20,156
C_ 191,126,127
C_ 190,124,125
Mvaa 80,24,192
C_ 189,122,123
C_ 188,120,121
Mvaa 80,20,152
s_nop 0
Ta 224,%0,20480
Ta 226,%0,20992
Mvaa 72,24,188
U_ v112,s48,v112
U_ v113,s48,v113
Mvaa 72,20,148
U_ v114,s48,v114
U_ v115,s48,v115
Mvaa 64,24,184
U_ v116,s48,v116
U_ v117,s48,v117
Mvaa 64,20,144
U_ v118,s48,v118
U_ v119,s48,v119
Mvaa 56,24,180
s_waitcnt vmcnt(51) lgkmcnt(0)
s_barrier
s_cmp_eq_u32 s11, 1
s_cbranch_scc1 .Lpace29
.Lpace29_back:
C_ 187,118,119
Mvaa 56,20,140
S_ s65,s61,12288
C_ 186,116,117
Mvaa 20,28,140
S_ m0,s51,12288
C_ 185,114,115
C_ 184,112,113
Mvaa 20,32,180
Ln v9,36,s65,0
Mvaa 28,28,144
U_ v104,s48,v104
U_ v105,s48,v105
Mvaa 28,32,184
U_ v106,s48,v106
U_ v107,s48,v107
Mvaa 36,28,148
U_ v108,s48,v108
U_ v109,s48,v109
Mvaa 36,32,188
Ln v9,36,s65,3072
Mvaa 44,28,152
S_ m0,s51,13184
U_ v110,s48,v110
U_ v111,s48,v111
Mvaa 44,32,192
C_ 183,110,111
C_ 182,108,109
Mvaa 52,28,156
C_ 181,106,107
C_ 180,104,105
Mvaa 52,32,196
U_ v96,s48,v96
U_ v97,s48,v97
Mvaa 48,224,196
Ln v9,36,s65,128
Mvaa 48,36,156
U_ v98,s48,v98
U_ v99,s48,v99
Mvaa 40,224,192
U_ v100,s48,v100
U_ v101,s48,v101
Mvaa 40,36,152
U_ v102,s48,v102
U_ v103,s48,v103
Mvaa 32,224,188
Ln v9,36,s65,3200
Mvaa 32,36,148
S_ m0,s51,14080
C_ 179,102,103
C_ 178,100,101
Mvaa 24,224,184
C_ 177,98,99
C_ 176,96,97
Mvaa 24,36,144
v_pk_mul_f32 v[88:89], v[88:89], s[48:49]
v_pk_mul_f32 v[90:91], v[90:91], s[48:49]
Mvaa 16,224,180
Ln v9,36,s65,256
Mvaa 16,36,140
v_pk_mul_f32 v[92:93], v[92:93], s[48:49]
v_pk_mul_f32 v[94:95], v[94:95], s[48:49]
Ln v9,36,s65,3328
C_ 175,94,95
C_ 174,92,93
C_ 173,90,91
C_ 172,88,89
v_pk_mul_f32 v[80:81], v[80:81], s[48:49]
v_pk_mul_f32 v[82:83], v[82:83], s[48:49]
v_pk_mul_f32 v[84:85], v[84:85], s[48:49]
v_pk_mul_f32 v[86:87], v[86:87], s[48:49]
C_ 171,86,87
C_ 170,84,85
C_ 169,82,83
C_ 168,80,81
v_pk_mul_f32 v[72:73], v[72:73], s[48:49]
v_pk_mul_f32 v[74:75], v[74:75], s[48:49]
v_pk_mul_f32 v[76:77], v[76:77], s[48:49]
v_pk_mul_f32 v[78:79], v[78:79], s[48:49]
C_ 167,78,79
C_ 166,76,77
C_ 165,74,75
C_ 164,72,73
v_pk_mul_f32 v[40:41], v[40:41], s[48:49]
v_pk_mul_f32 v[42:43], v[42:43], s[48:49]
v_pk_mul_f32 v[44:45], v[44:45], s[48:49]
v_pk_mul_f32 v[46:47], v[46:47], s[48:49]
C_ 151,46,47
C_ 150,44,45
C_ 149,42,43
C_ 148,40,41
v_pk_mul_f32 v[32:33], v[32:33], s[48:49]
v_pk_mul_f32 v[34:35], v[34:35], s[48:49]
v_pk_mul_f32 v[36:37], v[36:37], s[48:49]
v_pk_mul_f32 v[38:39], v[38:39], s[48:49]
C_ 147,38,39
C_ 146,36,37
C_ 145,34,35
C_ 144,32,33
v_pk_mul_f32 v[24:25], v[24:25], s[48:49]
v_pk_mul_f32 v[26:27], v[26:27], s[48:49]
v_pk_mul_f32 v[28:29], v[28:29], s[48:49]
v_pk_mul_f32 v[30:31], v[30:31], s[48:49]
C_ 143,30,31
C_ 142,28,29
C_ 141,26,27
C_ 140,24,25
R_ %0,a244
v_xor_b32 v1, 64, %0
Dv 30,%0,43008
Dv 34,v1,43008
Dv 38,%0,44032
v_pk_mul_f32 v[64:65], v[64:65], s[48:49]
v_pk_mul_f32 v[66:67], v[66:67], s[48:49]
v_pk_mul_f32 v[68:69], v[68:69], s[48:49]
v_pk_mul_f32 v[70:71], v[70:71], s[48:49]
C_ 163,70,71
C_ 162,68,69
C_ 161,66,67
C_ 160,64,65
v_pk_mul_f32 v[56:57], v[56:57], s[48:49]
v_pk_mul_f32 v[58:59], v[58:59], s[48:49]
v_pk_mul_f32 v[60:61], v[60:61], s[48:49]
v_pk_mul_f32 v[62:63], v[62:63], s[48:49]
C_ 159,62,63
C_ 158,60,61
C_ 157,58,59
C_ 156,56,57
v_pk_mul_f32 v[48:49], v[48:49], s[48:49]
v_pk_mul_f32 v[50:51], v[50:51], s[48:49]
v_pk_mul_f32 v[52:53], v[52:53], s[48:49]
v_pk_mul_f32 v[54:55], v[54:55], s[48:49]
C_ 155,54,55
C_ 154,52,53
C_ 153,50,51
C_ 152,48,49
v_pk_mul_f32 v[16:17], v[16:17], s[48:49]
v_pk_mul_f32 v[18:19], v[18:19], s[48:49]
v_pk_mul_f32 v[20:21], v[20:21], s[48:49]
v_pk_mul_f32 v[22:23], v[22:23], s[48:49]
C_ 139,22,23
C_ 138,20,21
C_ 137,18,19
C_ 136,16,17
s_cmp_eq_u32 s58, 1
s_cbranch_scc1 .Latt1
s_cmp_eq_u32 s58, 2
s_cbranch_scc1 .Latt2
.Latt0:
s_cmp_eq_u32 s4, 0
s_cbranch_scc0 .Lhs_tok0
R_ v11,a241
buffer_load_dword a255, v11, s[92:95], 0 offen offset:0 sc1
.Lhs_tok0:
S_ s59,s59,768
W_ 2
Zvvv 96,30,136
Dv 42,v1,44032
R_ v2,a245
Zvvv 100,30,140
v_xor_b32 v3, 32, v2
v_xor_b32 v4, 64, v2
Zvvv 104,30,144
v_xor_b32 v5, 96, v2
Zvvv 108,30,148
Zvvv 112,30,152
W_ 2
Mvvv 96,34,156
Dv 30,%0,45056
Mvvv 100,34,160
Mvvv 104,34,164
Mvvv 108,34,168
Mvvv 112,34,172
W_ 2
Mvvv 96,38,176
Dv 34,v1,45056
Mvvv 100,38,180
Mvvv 104,38,184
Mvvv 108,38,188
Mvvv 112,38,192
W_ 2
Mvvv 96,42,196
Dv 38,%0,36864
Mvvv 100,42,200
Mvvv 104,42,204
Mvvv 108,42,208
Mvvv 112,42,212
W_ 2
Mvvv 96,30,216
Dv 42,v1,36864
Mvvv 100,30,220
Mvvv 104,30,224
Mvvv 108,30,228
Mvvv 112,30,232
W_ 2
Mvvv 96,34,236
Dv 30,%0,37888
Mvvv 100,34,240
Mvvv 104,34,244
Mvvv 108,34,248
Mvvv 112,34,252
W_ 2
s_nop 1
v_max3_f32 v56, v96, v97, v98
v_max_f32 v56, v56, v99
Zvvv 76,38,136
Dv 34,v1,37888
v_max3_f32 v60, v100, v101, v102
Zvvv 80,38,140
v_max_f32 v60, v60, v103
Zvvv 84,38,144
v_max3_f32 v64, v104, v105, v106
v_max_f32 v64, v64, v107
Zvvv 88,38,148
v_max3_f32 v68, v108, v109, v110
v_max_f32 v68, v68, v111
Zvvv 92,38,152
W_ 2
v_max3_f32 v72, v112, v113, v114
v_max_f32 v72, v72, v115
Mvvv 76,42,156
Dv 38,%0,38912
Mvvv 80,42,160
Mvvv 84,42,164
Mvvv 88,42,168
Mvvv 92,42,172
W_ 2
Mvvv 76,30,176
Dv 42,v1,38912
Mvvv 80,30,180
Mvvv 84,30,184
Mvvv 88,30,188
Mvvv 92,30,192
s_waitcnt vmcnt(34) lgkmcnt(0)
s_barrier
Mvvv 76,34,196
S_ s65,s61,36864
Dv 30,%0,55296
Mvvv 76,38,216
S_ m0,s51,36864
Mvvv 76,42,236
Ln v7,32,s65,0
Mvvv 80,34,200
Mvvv 80,38,220
Ln v8,32,s65,3072
Mvvv 80,42,240
S_ m0,s51,37760
Mvvv 84,34,204
Ln v7,32,s65,128
Mvvv 84,38,224
v_max3_f32 v56, v56, v76, v77
v_max3_f32 v56, v56, v78, v79
Mvvv 84,42,244
Ln v8,32,s65,3200
Mvvv 88,34,208
S_ m0,s51,38656
v_max3_f32 v60, v60, v80, v81
v_max3_f32 v60, v60, v82, v83
Mvvv 88,38,228
Ln v7,32,s65,256
Mvvv 88,42,248
Mvvv 92,34,212
Ln v8,32,s65,3328
Mvvv 92,38,232
v_max3_f32 v64, v64, v84, v85
v_max3_f32 v64, v64, v86, v87
Mvvv 92,42,252
Dv 34,v1,55296
.p2align 3
v_mov_b32 v11, 0x3f803f80
v_permlane32_swap_b32 v56, v60
v_max3_f32 v68, v68, v88, v89
v_max3_f32 v68, v68, v90, v91
v_max_f32 v56, v56, v60
s_nop 1
v_max3_f32 v72, v72, v92, v93
v_max3_f32 v72, v72, v94, v95
v_mov_b32 v73, v72
v_permlane32_swap_b32 v64, v68
v_max_f32 v64, v64, v68
v_permlane32_swap_b32 v72, v73
v_max_f32 v72, v72, v73
v_mov_b32 v73, v72
v_permlane16_swap_b32 v56, v64
v_max_f32 v56, v56, v64
v_permlane16_swap_b32 v72, v73
B_ v56,0xc0e00000,v56
v_max_f32 v72, v72, v73
v_mov_b32 v64, v56
B_ v72,0xc0e00000,v72
v_mov_b32 v73, v72
v_permlane16_swap_b32 v56, v64
v_mov_b32 v60, v56
v_mov_b32 v68, v64
v_mov_b64 v[74:75], v[72:73]
v_permlane32_swap_b32 v56, v60
v_permlane32_swap_b32 v64, v68
v_mov_b32 v57, v56
v_mov_b32 v61, v60
v_mov_b32 v65, v64
v_mov_b32 v69, v68
v_pk_add_f32 v[96:97], v[96:97], v[56:57]
v_pk_add_f32 v[98:99], v[98:99], v[56:57]
v_pk_add_f32 v[76:77], v[76:77], v[56:57]
v_pk_add_f32 v[78:79], v[78:79], v[56:57]
v_mov_b64 v[58:59], v[56:57]
v_pk_add_f32 v[100:101], v[100:101], v[60:61]
v_pk_add_f32 v[102:103], v[102:103], v[60:61]
v_pk_add_f32 v[80:81], v[80:81], v[60:61]
v_pk_add_f32 v[82:83], v[82:83], v[60:61]
v_mov_b64 v[62:63], v[60:61]
v_pk_add_f32 v[104:105], v[104:105], v[64:65]
v_pk_add_f32 v[106:107], v[106:107], v[64:65]
v_pk_add_f32 v[84:85], v[84:85], v[64:65]
v_pk_add_f32 v[86:87], v[86:87], v[64:65]
v_mov_b64 v[66:67], v[64:65]
v_pk_add_f32 v[108:109], v[108:109], v[68:69]
v_pk_add_f32 v[110:111], v[110:111], v[68:69]
v_pk_add_f32 v[88:89], v[88:89], v[68:69]
v_pk_add_f32 v[90:91], v[90:91], v[68:69]
v_mov_b64 v[70:71], v[68:69]
v_pk_add_f32 v[112:113], v[112:113], v[72:73]
v_pk_add_f32 v[114:115], v[114:115], v[72:73]
v_pk_add_f32 v[92:93], v[92:93], v[72:73]
v_pk_add_f32 v[94:95], v[94:95], v[72:73]
E_ 96
E_ 97
E_ 98
E_ 99
E_ 76
E_ 77
E_ 78
E_ 79
C_ 99,98,99
C_ 98,96,97
C_ 96,76,77
C_ 97,78,79
E_ 100
E_ 101
E_ 102
E_ 103
E_ 80
E_ 81
E_ 82
E_ 83
C_ 103,102,103
C_ 102,100,101
C_ 100,80,81
C_ 101,82,83
.Lstep0_1:
W_ 1
Nvvv 116,30,136,56
s_nop 0
Dv 38,%0,56320
Dv 42,v1,56320
W_ 2
Mvvv 116,34,156
E_ 104
Nvvv 120,30,140,60
E_ 105
Mvvv 120,34,160
E_ 106
Nvvv 124,30,144,64
E_ 107
Mvvv 124,34,164
E_ 84
Nvvv 128,30,148,68
E_ 85
Mvvv 128,34,168
E_ 86
Nvvv 132,30,152,72
E_ 87
Mvvv 132,34,172
W_ 0
C_ 107,106,107
C_ 106,104,105
Mvvv 132,38,192
s_nop 0
Dv 30,%0,57344
Dv 34,v1,57344
Mvvv 132,42,212
C_ 104,84,85
C_ 105,86,87
Mvvv 128,38,188
E_ 108
Mvvv 128,42,208
E_ 109
Mvvv 124,38,184
E_ 110
Mvvv 124,42,204
E_ 111
Mvvv 120,38,180
E_ 88
Mvvv 120,42,200
E_ 89
Mvvv 116,38,176
E_ 90
Mvvv 116,42,196
W_ 0
E_ 91
Mvvv 116,30,216
s_nop 0
Dv 38,%0,49152
Dv 42,v1,49152
Mvvv 116,34,236
C_ 111,110,111
C_ 110,108,109
Mvvv 120,30,220
C_ 108,88,89
C_ 109,90,91
Mvvv 120,34,240
E_ 112
Mvvv 124,30,224
E_ 113
Mvvv 124,34,244
E_ 114
Mvvv 128,30,228
E_ 115
Mvvv 128,34,248
E_ 92
Mvvv 132,30,232
E_ 93
Mvvv 132,34,252
W_ 0
E_ 94
Nvvv 76,38,136,56
s_nop 0
Dv 30,%0,50176
Dv 34,v1,50176
Mvvv 76,42,156
E_ 95
Nvvv 80,38,140,60
C_ 115,114,115
C_ 114,112,113
Mvvv 80,42,160
C_ 112,92,93
C_ 113,94,95
Nvvv 84,38,144,64
E_ 116
Mvvv 84,42,164
E_ 117
Nvvv 88,38,148,68
E_ 118
Mvvv 88,42,168
E_ 119
Nvvv 92,38,152,72
C_ 119,118,119
C_ 118,116,117
Mvvv 92,42,172
W_ 0
E_ 120
Mvvv 92,30,192
s_nop 0
Dv 38,%0,51200
Dv 42,v1,51200
Mvvv 92,34,212
X_ a180,v11
X_ a181,v11
Mvvv 88,30,188
X_ a182,v11
X_ a183,v11
Mvvv 88,34,208
E_ 121
Mvvv 84,30,184
E_ 122
Mvvv 84,34,204
E_ 123
Mvvv 80,30,180
C_ 123,122,123
C_ 122,120,121
Mvvv 80,34,200
E_ 124
Mvvv 76,30,176
E_ 125
Mvvv 76,34,196
S_ s65,s61,24576
E_ 126
W_ 1
Mvvv 76,38,216
S_ m0,s51,49152
E_ 127
W_ 0
Mvvv 76,42,236
s_waitcnt vmcnt(18) lgkmcnt(0)
s_barrier
C_ 127,126,127
Mvvv 80,38,220
L_ v9,36,s65,0
Mvvv 80,42,240
C_ 126,124,125
Mvvv 84,38,224
L_ v9,36,s65,3072
S_ m0,s51,50048
Mvvv 84,42,244
s_nop 0
Tv 30,v2,24576
Tv 32,v2,30720
Mvvv 88,38,228
L_ v9,36,s65,128
Mvvv 88,42,248
s_nop 0
Tv 34,v3,24576
Tv 36,v3,30720
Mvvv 92,38,232
L_ v9,36,s65,3200
S_ m0,s51,50944
Mvvv 92,42,252
E_ 128
Zaav 220,180,96
L_ v9,36,s65,256
Zaav 224,180,100
E_ 129
Zaav 228,180,104
L_ v9,36,s65,3328
Zaav 232,180,108
E_ 130
Zaav 236,180,112
W_ 0
E_ 131
Zavv 0,30,96
s_nop 0
Tv 38,v4,24576
Tv 40,v4,30720
Zavv 20,34,96
C_ 131,130,131
C_ 130,128,129
Zavv 24,34,100
s_nop 0
Tv 42,v5,24576
Tv 44,v5,30720
Zavv 4,30,100
E_ 132
Zavv 8,30,104
E_ 133
Zavv 28,34,104
R_ v46,a220
R_ v47,a224
Zavv 32,34,108
R_ v48,a228
R_ v49,a232
Zavv 12,30,108
R_ v50,a236
Zavv 16,30,112
E_ 134
Zavv 36,34,112
W_ 0
E_ 135
Zavv 56,38,112
s_nop 0
Tv 30,v2,25600
Tv 32,v2,31744
Zavv 76,42,112
C_ 135,134,135
C_ 134,132,133
Zavv 72,42,108
s_nop 0
Tv 34,v3,25600
Tv 36,v3,31744
Zavv 52,38,108
E_ 76
Zavv 48,38,104
E_ 77
Zavv 68,42,104
E_ 78
Zavv 64,42,100
E_ 79
Zavv 44,38,100
C_ 116,76,77
C_ 117,78,79
Zavv 40,38,96
E_ 80
Zavv 60,42,96
W_ 0
E_ 81
Zavv 80,30,96
s_nop 0
Tv 38,v4,25600
Tv 40,v4,31744
Zavv 100,34,96
E_ 82
Zavv 104,34,100
s_nop 0
Tv 42,v5,25600
Tv 44,v5,31744
Zavv 84,30,100
E_ 83
Zavv 88,30,104
C_ 120,80,81
C_ 121,82,83
Zavv 108,34,104
E_ 84
Zavv 112,34,108
E_ 85
Zavv 92,30,108
E_ 86
Zavv 96,30,112
E_ 87
Zavv 116,34,112
W_ 0
C_ 124,84,85
C_ 125,86,87
Zavv 136,38,112
s_nop 0
Tv 30,v2,26624
Tv 32,v2,32768
Zavv 156,42,112
E_ 88
Zavv 152,42,108
s_nop 0
Tv 34,v3,26624
Tv 36,v3,32768
Zavv 132,38,108
E_ 89
Zavv 128,38,104
E_ 90
Zavv 148,42,104
E_ 91
Zavv 144,42,100
C_ 128,88,89
C_ 129,90,91
Zavv 124,38,100
E_ 92
Zavv 120,38,96
E_ 93
Zavv 140,42,96
W_ 0
E_ 94
Zavv 160,30,96
s_nop 0
Tv 38,v4,26624
Tv 40,v4,32768
Zavv 180,34,96
E_ 95
Zavv 184,34,100
s_nop 0
Tv 42,v5,26624
Tv 44,v5,32768
Zavv 164,30,100
C_ 132,92,93
C_ 133,94,95
Zavv 168,30,104
v_mov_b32 v92, 0x3f803f80
v_mov_b32 v93, 0x3f803f80
Zavv 188,34,104
S_ s65,s61,49152
v_mov_b32 v94, 0x3f803f80
v_mov_b32 v95, 0x3f803f80
Zavv 192,34,108
S_ m0,s51,24576
Zavv 172,30,108
s_waitcnt vmcnt(18) lgkmcnt(0)
s_barrier
Zavv 176,30,112
Ln v7,32,s65,0
Zavv 196,34,112
Zavv 216,38,112
Dv 30,%0,6144
Zavv 236,42,112
Dv 34,v1,6144
Zavv 232,42,108
Ln v8,32,s65,3072
S_ m0,s51,25472
Zavv 212,38,108
Zavv 208,38,104
Ln v7,32,s65,128
Zavv 228,42,104
Zavv 224,42,100
Ln v8,32,s65,3200
S_ m0,s51,26368
Zavv 204,38,100
Zavv 200,38,96
Ln v7,32,s65,256
Zavv 220,42,96
Ln v8,32,s65,3328
.Lstep0_2:
Zvvv 76,92,116
Dv 38,%0,7168
Zvvv 80,92,120
Dv 42,v1,7168
Zvvv 84,92,124
Zvvv 88,92,128
Zvvv 92,92,132
W_ 2
Nvvv 96,30,136,56
Mvvv 96,34,156
Nvvv 100,30,140,60
s_nop 1
v_max3_f32 v10, v76, v80, v84
s_nop 0
v_max3_f32 v10, v10, v88, v92
Mvvv 100,34,160
v_cmp_lt_f32 vcc, 0x42000000, v10
s_cbranch_vccnz .Ltramp0_1
Nvvv 104,30,144,64
A_ v46,v46,v76
A_ v47,v47,v80
Mvvv 104,34,164
A_ v48,v48,v84
A_ v49,v49,v88
Nvvv 108,30,148,68
A_ v50,v50,v92
Mvvv 108,34,168
Nvvv 112,30,152,72
Mvvv 112,34,172
W_ 0
Mvvv 112,38,192
Dv 30,%0,8192
Mvvv 112,42,212
Dv 34,v1,8192
Mvvv 108,38,188
Mvvv 108,42,208
Mvvv 104,38,184
Mvvv 104,42,204
Mvvv 100,38,180
Mvvv 100,42,200
Mvvv 96,38,176
Mvvv 96,42,196
W_ 0
Mvvv 96,30,216
Dv 38,%0,0
Mvvv 96,34,236
Dv 42,v1,0
Mvvv 100,30,220
Mvvv 100,34,240
Mvvv 104,30,224
Mvvv 104,34,244
Mvvv 108,30,228
Mvvv 108,34,248
s_nop 0
E_ 96
Mvvv 112,30,232
E_ 97
Mvvv 112,34,252
W_ 0
E_ 98
Nvvv 76,38,136,56
Dv 30,%0,1024
Mvvv 76,42,156
Dv 34,v1,1024
Nvvv 80,38,140,60
E_ 99
Mvvv 80,42,160
C_ 99,98,99
C_ 98,96,97
Nvvv 84,38,144,64
E_ 100
Mvvv 84,42,164
E_ 101
Nvvv 88,38,148,68
E_ 102
Mvvv 88,42,168
E_ 103
Nvvv 92,38,152,72
C_ 103,102,103
C_ 102,100,101
Mvvv 92,42,172
W_ 0
E_ 104
Mvvv 92,30,192
Dv 38,%0,2048
Mvvv 92,34,212
Dv 42,v1,2048
Mvvv 88,30,188
E_ 105
Mvvv 88,34,208
E_ 106
Mvvv 84,30,184
S_ s65,s61,36864
E_ 107
Mvvv 84,34,204
S_ m0,s51,0
C_ 107,106,107
C_ 106,104,105
Mvvv 80,30,180
s_waitcnt vmcnt(18) lgkmcnt(0)
s_barrier
Mvvv 80,34,200
L_ v9,36,s65,0
Mvvv 76,30,176
E_ 108
Mvvv 76,34,196
L_ v9,36,s65,3072
S_ m0,s51,896
Mvvv 76,38,216
Tv 30,v2,12288
Tv 32,v2,18432
Mvvv 76,42,236
L_ v9,36,s65,128
Mvvv 80,38,220
s_nop 0
Tv 34,v3,12288
Tv 36,v3,18432
Mvvv 80,42,240
L_ v9,36,s65,3200
S_ m0,s51,1792
Mvvv 84,38,224
E_ 109
Mvvv 84,42,244
L_ v9,36,s65,256
Mvvv 88,38,228
E_ 110
Mvvv 88,42,248
L_ v9,36,s65,3328
Mvvv 92,38,232
E_ 111
Mvvv 92,42,252
W_ 0
C_ 111,110,111
C_ 110,108,109
Mavv 0,30,116
s_nop 0
Tv 38,v4,12288
Tv 40,v4,18432
Mavv 20,34,116
E_ 112
Mavv 24,34,120
s_nop 0
Tv 42,v5,12288
Tv 44,v5,18432
Mavv 4,30,120
E_ 113
Mavv 8,30,124
E_ 114
Mavv 28,34,124
E_ 115
Mavv 32,34,128
C_ 115,114,115
C_ 114,112,113
Mavv 12,30,128
E_ 76
Mavv 16,30,132
E_ 77
Mavv 36,34,132
W_ 0
E_ 78
Mavv 56,38,132
s_nop 0
Tv 30,v2,13312
Tv 32,v2,19456
Mavv 76,42,132
E_ 79
Mavv 72,42,128
s_nop 0
Tv 34,v3,13312
Tv 36,v3,19456
Mavv 52,38,128
C_ 96,76,77
C_ 97,78,79
Mavv 48,38,124
E_ 80
Mavv 68,42,124
E_ 81
Mavv 64,42,120
E_ 82
Mavv 44,38,120
E_ 83
Mavv 40,38,116
C_ 100,80,81
C_ 101,82,83
Mavv 60,42,116
W_ 0
E_ 84
Mavv 80,30,116
s_nop 0
Tv 38,v4,13312
Tv 40,v4,19456
Mavv 100,34,116
E_ 85
Mavv 104,34,120
s_nop 0
Tv 42,v5,13312
Tv 44,v5,19456
Mavv 84,30,120
E_ 86
Mavv 88,30,124
E_ 87
Mavv 108,34,124
C_ 104,84,85
C_ 105,86,87
Mavv 112,34,128
E_ 88
Mavv 92,30,128
E_ 89
Mavv 96,30,132
E_ 90
Mavv 116,34,132
W_ 0
E_ 91
Mavv 136,38,132
s_nop 0
Tv 30,v2,14336
Tv 32,v2,20480
Mavv 156,42,132
C_ 108,88,89
C_ 109,90,91
Mavv 152,42,128
s_nop 0
Tv 34,v3,14336
Tv 36,v3,20480
Mavv 132,38,128
E_ 92
Mavv 128,38,124
E_ 93
Mavv 148,42,124
E_ 94
Mavv 144,42,120
E_ 95
Mavv 124,38,120
C_ 112,92,93
C_ 113,94,95
Mavv 120,38,116
v_mov_b32 v92, 0x3f803f80
v_mov_b32 v93, 0x3f803f80
Mavv 140,42,116
W_ 0
v_mov_b32 v94, 0x3f803f80
v_mov_b32 v95, 0x3f803f80
Mavv 160,30,116
s_nop 0
Tv 38,v4,14336
Tv 40,v4,20480
Mavv 180,34,116
Mavv 184,34,120
s_nop 0
Tv 42,v5,14336
Tv 44,v5,20480
Mavv 164,30,120
Mavv 168,30,124
Mavv 188,34,124
S_ s65,s61,61440
Mavv 192,34,128
S_ m0,s51,12288
Mavv 172,30,128
s_waitcnt vmcnt(18) lgkmcnt(0)
s_barrier
Mavv 176,30,132
Ln v7,32,s65,0
Mavv 196,34,132
Mavv 216,38,132
Dv 30,%0,43008
Mavv 236,42,132
Dv 34,v1,43008
Mavv 232,42,128
Ln v8,32,s65,3072
S_ m0,s51,13184
Mavv 212,38,128
Mavv 208,38,124
Ln v7,32,s65,128
Mavv 228,42,124
Mavv 224,42,120
Ln v8,32,s65,3200
S_ m0,s51,14080
Mavv 204,38,120
Mavv 200,38,116
Ln v7,32,s65,256
Mavv 220,42,116
s_cmp_eq_u32 s4, 0
s_cbranch_scc1 .Lhs_a0
.Lhs_a0_back:
.Lstep0_3:
Zvvv 76,92,96
Dv 38,%0,44032
Zvvv 80,92,100
Dv 42,v1,44032
Zvvv 84,92,104
Zvvv 88,92,108
Zvvv 92,92,112
W_ 2
Nvvv 116,30,136,56
Mvvv 116,34,156
Nvvv 120,30,140,60
s_nop 1
v_max3_f32 v10, v76, v80, v84
s_nop 0
v_max3_f32 v10, v10, v88, v92
Mvvv 120,34,160
v_cmp_lt_f32 vcc, 0x42000000, v10
s_cbranch_vccnz .Ltramp0_2
Nvvv 124,30,144,64
A_ v46,v46,v76
A_ v47,v47,v80
Mvvv 124,34,164
A_ v48,v48,v84
A_ v49,v49,v88
Nvvv 128,30,148,68
A_ v50,v50,v92
Mvvv 128,34,168
Ln v8,32,s65,3328
Nvvv 132,30,152,72
Mvvv 132,34,172
W_ 0
Mvvv 132,38,192
Dv 30,%0,45056
Mvvv 132,42,212
Dv 34,v1,45056
Mvvv 128,38,188
Mvvv 128,42,208
Mvvv 124,38,184
Mvvv 124,42,204
Mvvv 120,38,180
Mvvv 120,42,200
Mvvv 116,38,176
Mvvv 116,42,196
W_ 0
Mvvv 116,30,216
Dv 38,%0,36864
Mvvv 116,34,236
Dv 42,v1,36864
Mvvv 120,30,220
Mvvv 120,34,240
Mvvv 124,30,224
Mvvv 124,34,244
Mvvv 128,30,228
Mvvv 128,34,248
s_nop 0
E_ 116
Mvvv 132,30,232
E_ 117
Mvvv 132,34,252
W_ 0
E_ 118
Nvvv 76,38,136,56
Dv 30,%0,37888
Mvvv 76,42,156
Dv 34,v1,37888
Nvvv 80,38,140,60
E_ 119
Mvvv 80,42,160
C_ 119,118,119
C_ 118,116,117
Nvvv 84,38,144,64
E_ 120
Mvvv 84,42,164
E_ 121
Nvvv 88,38,148,68
E_ 122
Mvvv 88,42,168
E_ 123
Nvvv 92,38,152,72
C_ 123,122,123
C_ 122,120,121
Mvvv 92,42,172
W_ 0
E_ 124
Mvvv 92,30,192
Dv 38,%0,38912
Mvvv 92,34,212
Dv 42,v1,38912
Mvvv 88,30,188
E_ 125
Mvvv 88,34,208
E_ 126
Mvvv 84,30,184
S_ s65,s61,49152
E_ 127
Mvvv 84,34,204
S_ m0,s51,36864
C_ 127,126,127
C_ 126,124,125
Mvvv 80,30,180
s_waitcnt vmcnt(18) lgkmcnt(0)
s_barrier
Mvvv 80,34,200
L_ v9,36,s65,0
Mvvv 76,30,176
E_ 128
Mvvv 76,34,196
L_ v9,36,s65,3072
S_ m0,s51,37760
Mvvv 76,38,216
Tv 30,v2,49152
Tv 32,v2,55296
Mvvv 76,42,236
L_ v9,36,s65,128
Mvvv 80,38,220
s_nop 0
Tv 34,v3,49152
Tv 36,v3,55296
Mvvv 80,42,240
L_ v9,36,s65,3200
S_ m0,s51,38656
Mvvv 84,38,224
E_ 129
Mvvv 84,42,244
L_ v9,36,s65,256
Mvvv 88,38,228
E_ 130
Mvvv 88,42,248
L_ v9,36,s65,3328
Mvvv 92,38,232
E_ 131
Mvvv 92,42,252
W_ 0
C_ 131,130,131
C_ 130,128,129
Mavv 0,30,96
s_nop 0
Tv 38,v4,49152
Tv 40,v4,55296
Mavv 20,34,96
E_ 132
Mavv 24,34,100
s_nop 0
Tv 42,v5,49152
Tv 44,v5,55296
Mavv 4,30,100
E_ 133
Mavv 8,30,104
E_ 134
Mavv 28,34,104
E_ 135
Mavv 32,34,108
C_ 135,134,135
C_ 134,132,133
Mavv 12,30,108
E_ 76
Mavv 16,30,112
E_ 77
Mavv 36,34,112
W_ 0
E_ 78
Mavv 56,38,112
s_nop 0
Tv 30,v2,50176
Tv 32,v2,56320
Mavv 76,42,112
E_ 79
Mavv 72,42,108
s_nop 0
Tv 34,v3,50176
Tv 36,v3,56320
Mavv 52,38,108
C_ 116,76,77
C_ 117,78,79
Mavv 48,38,104
E_ 80
Mavv 68,42,104
E_ 81
Mavv 64,42,100
E_ 82
Mavv 44,38,100
E_ 83
Mavv 40,38,96
C_ 120,80,81
C_ 121,82,83
Mavv 60,42,96
W_ 0
E_ 84
Mavv 80,30,96
s_nop 0
Tv 38,v4,50176
Tv 40,v4,56320
Mavv 100,34,96
E_ 85
Mavv 104,34,100
s_nop 0
Tv 42,v5,50176
Tv 44,v5,56320
Mavv 84,30,100
E_ 86
Mavv 88,30,104
E_ 87
Mavv 108,34,104
C_ 124,84,85
C_ 125,86,87
Mavv 112,34,108
E_ 88
Mavv 92,30,108
E_ 89
Mavv 96,30,112
E_ 90
Mavv 116,34,112
W_ 0
E_ 91
Mavv 136,38,112
s_nop 0
Tv 30,v2,51200
Tv 32,v2,57344
Mavv 156,42,112
C_ 128,88,89
C_ 129,90,91
Mavv 152,42,108
s_nop 0
Tv 34,v3,51200
Tv 36,v3,57344
Mavv 132,38,108
E_ 92
Mavv 128,38,104
E_ 93
Mavv 148,42,104
E_ 94
Mavv 144,42,100
E_ 95
Mavv 124,38,100
C_ 132,92,93
C_ 133,94,95
Mavv 120,38,96
v_mov_b32 v92, 0x3f803f80
v_mov_b32 v93, 0x3f803f80
Mavv 140,42,96
W_ 0
v_mov_b32 v94, 0x3f803f80
v_mov_b32 v95, 0x3f803f80
Mavv 160,30,96
s_nop 0
Tv 38,v4,51200
Tv 40,v4,57344
Mavv 180,34,96
Mavv 184,34,100
s_nop 0
Tv 42,v5,51200
Tv 44,v5,57344
Mavv 164,30,100
Mavv 168,30,104
Mavv 188,34,104
S_ s65,s61,73728
Mavv 192,34,108
S_ m0,s51,49152
Mavv 172,30,108
s_waitcnt vmcnt(18) lgkmcnt(0)
s_barrier
Mavv 176,30,112
Ln v7,32,s65,0
Mavv 196,34,112
Mavv 216,38,112
Dv 30,%0,30720
Mavv 236,42,112
Dv 34,v1,30720
Mavv 232,42,108
Ln v8,32,s65,3072
S_ m0,s51,50048
Mavv 212,38,108
Mavv 208,38,104
Ln v7,32,s65,128
Mavv 228,42,104
Mavv 224,42,100
Ln v8,32,s65,3200
S_ m0,s51,50944
Mavv 204,38,100
Mavv 200,38,96
Ln v7,32,s65,256
Mavv 220,42,96
.Lstep0_4:
Zvvv 76,92,116
Dv 38,%0,31744
Zvvv 80,92,120
Dv 42,v1,31744
Zvvv 84,92,124
Zvvv 88,92,128
Zvvv 92,92,132
W_ 2
Nvvv 96,30,136,56
Mvvv 96,34,156
Nvvv 100,30,140,60
s_nop 1
v_max3_f32 v10, v76, v80, v84
s_nop 0
v_max3_f32 v10, v10, v88, v92
Mvvv 100,34,160
v_cmp_lt_f32 vcc, 0x42000000, v10
s_cbranch_vccnz .Ltramp0_3
Nvvv 104,30,144,64
A_ v46,v46,v76
A_ v47,v47,v80
Mvvv 104,34,164
A_ v48,v48,v84
A_ v49,v49,v88
Nvvv 108,30,148,68
A_ v50,v50,v92
Mvvv 108,34,168
Ln v8,32,s65,3328
Nvvv 112,30,152,72
Mvvv 112,34,172
W_ 0
Mvvv 112,38,192
Dv 30,%0,32768
Mvvv 112,42,212
Dv 34,v1,32768
Mvvv 108,38,188
Mvvv 108,42,208
Mvvv 104,38,184
Mvvv 104,42,204
Mvvv 100,38,180
Mvvv 100,42,200
Mvvv 96,38,176
Mvvv 96,42,196
W_ 0
Mvvv 96,30,216
Dv 38,%0,24576
Mvvv 96,34,236
Dv 42,v1,24576
Mvvv 100,30,220
Mvvv 100,34,240
Mvvv 104,30,224
Mvvv 104,34,244
Mvvv 108,30,228
Mvvv 108,34,248
s_nop 0
E_ 96
Mvvv 112,30,232
E_ 97
Mvvv 112,34,252
W_ 0
E_ 98
Nvvv 76,38,136,56
Dv 30,%0,25600
Mvvv 76,42,156
Dv 34,v1,25600
Nvvv 80,38,140,60
E_ 99
Mvvv 80,42,160
C_ 99,98,99
C_ 98,96,97
Nvvv 84,38,144,64
E_ 100
Mvvv 84,42,164
E_ 101
Nvvv 88,38,148,68
E_ 102
Mvvv 88,42,168
E_ 103
Nvvv 92,38,152,72
C_ 103,102,103
C_ 102,100,101
Mvvv 92,42,172
W_ 0
E_ 104
Mvvv 92,30,192
Dv 38,%0,26624
Mvvv 92,34,212
Dv 42,v1,26624
Mvvv 88,30,188
E_ 105
Mvvv 88,34,208
E_ 106
Mvvv 84,30,184
S_ s65,s61,61440
E_ 107
Mvvv 84,34,204
S_ m0,s51,24576
C_ 107,106,107
C_ 106,104,105
Mvvv 80,30,180
s_waitcnt vmcnt(18) lgkmcnt(0)
s_barrier
Mvvv 80,34,200
L_ v9,36,s65,0
Mvvv 76,30,176
E_ 108
Mvvv 76,34,196
L_ v9,36,s65,3072
S_ m0,s51,25472
Mvvv 76,38,216
Tv 30,v2,0
Tv 32,v2,6144
Mvvv 76,42,236
L_ v9,36,s65,128
Mvvv 80,38,220
s_nop 0
Tv 34,v3,0
Tv 36,v3,6144
Mvvv 80,42,240
L_ v9,36,s65,3200
S_ m0,s51,26368
Mvvv 84,38,224
E_ 109
Mvvv 84,42,244
L_ v9,36,s65,256
Mvvv 88,38,228
E_ 110
Mvvv 88,42,248
L_ v9,36,s65,3328
Mvvv 92,38,232
E_ 111
Mvvv 92,42,252
W_ 0
C_ 111,110,111
C_ 110,108,109
Mavv 0,30,116
s_nop 0
Tv 38,v4,0
Tv 40,v4,6144
Mavv 20,34,116
E_ 112
Mavv 24,34,120
s_nop 0
Tv 42,v5,0
Tv 44,v5,6144
Mavv 4,30,120
E_ 113
Mavv 8,30,124
E_ 114
Mavv 28,34,124
E_ 115
Mavv 32,34,128
C_ 115,114,115
C_ 114,112,113
Mavv 12,30,128
E_ 76
Mavv 16,30,132
E_ 77
Mavv 36,34,132
W_ 0
E_ 78
Mavv 56,38,132
s_nop 0
Tv 30,v2,1024
Tv 32,v2,7168
Mavv 76,42,132
E_ 79
Mavv 72,42,128
s_nop 0
Tv 34,v3,1024
Tv 36,v3,7168
Mavv 52,38,128
C_ 96,76,77
C_ 97,78,79
Mavv 48,38,124
E_ 80
Mavv 68,42,124
E_ 81
Mavv 64,42,120
E_ 82
Mavv 44,38,120
E_ 83
Mavv 40,38,116
C_ 100,80,81
C_ 101,82,83
Mavv 60,42,116
W_ 0
E_ 84
Mavv 80,30,116
s_nop 0
Tv 38,v4,1024
Tv 40,v4,7168
Mavv 100,34,116
E_ 85
Mavv 104,34,120
s_nop 0
Tv 42,v5,1024
Tv 44,v5,7168
Mavv 84,30,120
E_ 86
Mavv 88,30,124
E_ 87
Mavv 108,34,124
C_ 104,84,85
C_ 105,86,87
Mavv 112,34,128
E_ 88
Mavv 92,30,128
E_ 89
Mavv 96,30,132
E_ 90
Mavv 116,34,132
W_ 0
E_ 91
Mavv 136,38,132
s_nop 0
Tv 30,v2,2048
Tv 32,v2,8192
Mavv 156,42,132
C_ 108,88,89
C_ 109,90,91
Mavv 152,42,128
s_nop 0
Tv 34,v3,2048
Tv 36,v3,8192
Mavv 132,38,128
E_ 92
Mavv 128,38,124
E_ 93
Mavv 148,42,124
E_ 94
Mavv 144,42,120
E_ 95
Mavv 124,38,120
C_ 112,92,93
C_ 113,94,95
Mavv 120,38,116
v_mov_b32 v92, 0x3f803f80
v_mov_b32 v93, 0x3f803f80
Mavv 140,42,116
W_ 0
v_mov_b32 v94, 0x3f803f80
v_mov_b32 v95, 0x3f803f80
Mavv 160,30,116
s_nop 0
Tv 38,v4,2048
Tv 40,v4,8192
Mavv 180,34,116
Mavv 184,34,120
s_nop 0
Tv 42,v5,2048
Tv 44,v5,8192
Mavv 164,30,120
Mavv 168,30,124
Mavv 188,34,124
S_ s65,s61,86016
Mavv 192,34,128
S_ m0,s51,0
Mavv 172,30,128
s_waitcnt vmcnt(18) lgkmcnt(0)
s_barrier
Mavv 176,30,132
Ln v7,32,s65,0
Mavv 196,34,132
Mavv 216,38,132
Dv 30,%0,18432
Mavv 236,42,132
Dv 34,v1,18432
Mavv 232,42,128
Ln v8,32,s65,3072
S_ m0,s51,896
Mavv 212,38,128
Mavv 208,38,124
Ln v7,32,s65,128
Mavv 228,42,124
Mavv 224,42,120
Ln v8,32,s65,3200
S_ m0,s51,1792
Mavv 204,38,120
Mavv 200,38,116
Ln v7,32,s65,256
Mavv 220,42,116
.Lstep0_5:
Zvvv 76,92,96
Dv 38,%0,19456
Zvvv 80,92,100
Dv 42,v1,19456
Zvvv 84,92,104
Zvvv 88,92,108
Zvvv 92,92,112
W_ 2
Nvvv 116,30,136,56
Mvvv 116,34,156
Nvvv 120,30,140,60
s_nop 1
v_max3_f32 v10, v76, v80, v84
s_nop 0
v_max3_f32 v10, v10, v88, v92
Mvvv 120,34,160
v_cmp_lt_f32 vcc, 0x42000000, v10
s_cbranch_vccnz .Ltramp0_4
Nvvv 124,30,144,64
A_ v46,v46,v76
A_ v47,v47,v80
Mvvv 124,34,164
A_ v48,v48,v84
A_ v49,v49,v88
Nvvv 128,30,148,68
A_ v50,v50,v92
Mvvv 128,34,168
Ln v8,32,s65,3328
Nvvv 132,30,152,72
Mvvv 132,34,172
W_ 0
Mvvv 132,38,192
Dv 30,%0,20480
Mvvv 132,42,212
Dv 34,v1,20480
Mvvv 128,38,188
Mvvv 128,42,208
Mvvv 124,38,184
Mvvv 124,42,204
Mvvv 120,38,180
Mvvv 120,42,200
Mvvv 116,38,176
Mvvv 116,42,196
W_ 0
Mvvv 116,30,216
Dv 38,%0,12288
Mvvv 116,34,236
Dv 42,v1,12288
Mvvv 120,30,220
Mvvv 120,34,240
Mvvv 124,30,224
Mvvv 124,34,244
Mvvv 128,30,228
Mvvv 128,34,248
s_nop 0
E_ 116
Mvvv 132,30,232
E_ 117
Mvvv 132,34,252
W_ 0
E_ 118
Nvvv 76,38,136,56
Dv 30,%0,13312
Mvvv 76,42,156
Dv 34,v1,13312
Nvvv 80,38,140,60
E_ 119
Mvvv 80,42,160
C_ 119,118,119
C_ 118,116,117
Nvvv 84,38,144,64
E_ 120
Mvvv 84,42,164
E_ 121
Nvvv 88,38,148,68
E_ 122
Mvvv 88,42,168
E_ 123
Nvvv 92,38,152,72
C_ 123,122,123
C_ 122,120,121
Mvvv 92,42,172
W_ 0
E_ 124
Mvvv 92,30,192
Dv 38,%0,14336
Mvvv 92,34,212
Dv 42,v1,14336
Mvvv 88,30,188
E_ 125
Mvvv 88,34,208
E_ 126
Mvvv 84,30,184
S_ s65,s61,73728
E_ 127
Mvvv 84,34,204
S_ m0,s51,12288
C_ 127,126,127
C_ 126,124,125
Mvvv 80,30,180
s_waitcnt vmcnt(18) lgkmcnt(0)
s_barrier
Mvvv 80,34,200
L_ v9,36,s65,0
Mvvv 76,30,176
E_ 128
Mvvv 76,34,196
L_ v9,36,s65,3072
S_ m0,s51,13184
Mvvv 76,38,216
Tv 30,v2,36864
Tv 32,v2,43008
Mvvv 76,42,236
L_ v9,36,s65,128
Mvvv 80,38,220
s_nop 0
Tv 34,v3,36864
Tv 36,v3,43008
Mvvv 80,42,240
L_ v9,36,s65,3200
S_ m0,s51,14080
Mvvv 84,38,224
E_ 129
Mvvv 84,42,244
L_ v9,36,s65,256
Mvvv 88,38,228
E_ 130
Mvvv 88,42,248
L_ v9,36,s65,3328
Mvvv 92,38,232
E_ 131
Mvvv 92,42,252
W_ 0
C_ 131,130,131
C_ 130,128,129
Mavv 0,30,96
s_nop 0
Tv 38,v4,36864
Tv 40,v4,43008
Mavv 20,34,96
E_ 132
Mavv 24,34,100
s_nop 0
Tv 42,v5,36864
Tv 44,v5,43008
Mavv 4,30,100
E_ 133
Mavv 8,30,104
E_ 134
)A"
R"A(Mavv 28,34,104
E_ 135
Mavv 32,34,108
C_ 135,134,135
C_ 134,132,133
Mavv 12,30,108
E_ 76
Mavv 16,30,112
E_ 77
Mavv 36,34,112
W_ 0
E_ 78
Mavv 56,38,112
s_nop 0
Tv 30,v2,37888
Tv 32,v2,44032
Mavv 76,42,112
E_ 79
Mavv 72,42,108
s_nop 0
Tv 34,v3,37888
Tv 36,v3,44032
Mavv 52,38,108
C_ 116,76,77
C_ 117,78,79
Mavv 48,38,104
E_ 80
Mavv 68,42,104
E_ 81
Mavv 64,42,100
E_ 82
Mavv 44,38,100
E_ 83
Mavv 40,38,96
C_ 120,80,81
C_ 121,82,83
Mavv 60,42,96
W_ 0
E_ 84
Mavv 80,30,96
s_nop 0
Tv 38,v4,37888
Tv 40,v4,44032
Mavv 100,34,96
E_ 85
Mavv 104,34,100
s_nop 0
Tv 42,v5,37888
Tv 44,v5,44032
Mavv 84,30,100
E_ 86
Mavv 88,30,104
E_ 87
Mavv 108,34,104
C_ 124,84,85
C_ 125,86,87
Mavv 112,34,108
E_ 88
Mavv 92,30,108
E_ 89
Mavv 96,30,112
E_ 90
Mavv 116,34,112
W_ 0
E_ 91
Mavv 136,38,112
s_nop 0
Tv 30,v2,38912
Tv 32,v2,45056
Mavv 156,42,112
C_ 128,88,89
C_ 129,90,91
Mavv 152,42,108
s_nop 0
Tv 34,v3,38912
Tv 36,v3,45056
Mavv 132,38,108
E_ 92
Mavv 128,38,104
E_ 93
Mavv 148,42,104
E_ 94
Mavv 144,42,100
E_ 95
Mavv 124,38,100
C_ 132,92,93
C_ 133,94,95
Mavv 120,38,96
v_mov_b32 v92, 0x3f803f80
v_mov_b32 v93, 0x3f803f80
Mavv 140,42,96
W_ 0
v_mov_b32 v94, 0x3f803f80
v_mov_b32 v95, 0x3f803f80
Mavv 160,30,96
s_nop 0
Tv 38,v4,38912
Tv 40,v4,45056
Mavv 180,34,96
Mavv 184,34,100
s_nop 0
Tv 42,v5,38912
Tv 44,v5,45056
Mavv 164,30,100
Mavv 168,30,104
Mavv 188,34,104
S_ s65,s61,98304
Mavv 192,34,108
S_ m0,s51,36864
Mavv 172,30,108
s_waitcnt vmcnt(18) lgkmcnt(0)
s_barrier
Mavv 176,30,112
Ln v7,32,s65,0
Mavv 196,34,112
Mavv 216,38,112
Dv 30,%0,55296
Mavv 236,42,112
Dv 34,v1,55296
Mavv 232,42,108
Ln v8,32,s65,3072
S_ m0,s51,37760
Mavv 212,38,108
Mavv 208,38,104
Ln v7,32,s65,128
Mavv 228,42,104
Mavv 224,42,100
Ln v8,32,s65,3200
S_ m0,s51,38656
Mavv 204,38,100
Mavv 200,38,96
Ln v7,32,s65,256
Mavv 220,42,96
.Lstep0_6:
Zvvv 76,92,116
Dv 38,%0,56320
Zvvv 80,92,120
Dv 42,v1,56320
Zvvv 84,92,124
Zvvv 88,92,128
Zvvv 92,92,132
W_ 2
Nvvv 96,30,136,56
Mvvv 96,34,156
Nvvv 100,30,140,60
s_nop 1
v_max3_f32 v10, v76, v80, v84
s_nop 0
v_max3_f32 v10, v10, v88, v92
Mvvv 100,34,160
v_cmp_lt_f32 vcc, 0x42000000, v10
s_cbranch_vccnz .Ltramp0_5
Nvvv 104,30,144,64
A_ v46,v46,v76
A_ v47,v47,v80
Mvvv 104,34,164
A_ v48,v48,v84
A_ v49,v49,v88
Nvvv 108,30,148,68
A_ v50,v50,v92
Mvvv 108,34,168
Ln v8,32,s65,3328
Nvvv 112,30,152,72
R_ v11,a248
Mvvv 112,34,172
W_ 0
v_min_u32 v11, 240, v11
Mvvv 112,38,192
Dv 30,%0,57344
v_lshlrev_b32 v11, 3, v11
Mvvv 112,42,212
Dv 34,v1,57344
Mvvv 108,38,188
buffer_load_dword a254, v11, s[44:47], s66 offen
Mvvv 108,42,208
Mvvv 104,38,184
Mvvv 104,42,204
Mvvv 100,38,180
Mvvv 100,42,200
Mvvv 96,38,176
Mvvv 96,42,196
W_ 0
Mvvv 96,30,216
Dv 38,%0,49152
Mvvv 96,34,236
Dv 42,v1,49152
Mvvv 100,30,220
Mvvv 100,34,240
Mvvv 104,30,224
Mvvv 104,34,244
Mvvv 108,30,228
Mvvv 108,34,248
s_nop 0
E_ 96
Mvvv 112,30,232
E_ 97
Mvvv 112,34,252
W_ 0
E_ 98
Nvvv 76,38,136,56
Dv 30,%0,50176
Mvvv 76,42,156
Dv 34,v1,50176
Nvvv 80,38,140,60
E_ 99
Mvvv 80,42,160
C_ 99,98,99
C_ 98,96,97
Nvvv 84,38,144,64
E_ 100
Mvvv 84,42,164
E_ 101
Nvvv 88,38,148,68
E_ 102
Mvvv 88,42,168
E_ 103
Nvvv 92,38,152,72
C_ 103,102,103
C_ 102,100,101
Mvvv 92,42,172
W_ 0
E_ 104
Mvvv 92,30,192
Dv 38,%0,51200
Mvvv 92,34,212
Dv 42,v1,51200
Mvvv 88,30,188
E_ 105
Mvvv 88,34,208
E_ 106
Mvvv 84,30,184
S_ s65,s61,86016
E_ 107
Mvvv 84,34,204
S_ m0,s51,49152
C_ 107,106,107
C_ 106,104,105
Mvvv 80,30,180
s_waitcnt vmcnt(19) lgkmcnt(0)
s_barrier
Mvvv 80,34,200
L_ v9,36,s65,0
Mvvv 76,30,176
E_ 108
Mvvv 76,34,196
L_ v9,36,s65,3072
S_ m0,s51,50048
Mvvv 76,38,216
Tv 30,v2,24576
Tv 32,v2,30720
Mvvv 76,42,236
L_ v9,36,s65,128
Mvvv 80,38,220
s_nop 0
Tv 34,v3,24576
Tv 36,v3,30720
Mvvv 80,42,240
L_ v9,36,s65,3200
S_ m0,s51,50944
Mvvv 84,38,224
E_ 109
Mvvv 84,42,244
L_ v9,36,s65,256
Mvvv 88,38,228
E_ 110
Mvvv 88,42,248
L_ v9,36,s65,3328
Mvvv 92,38,232
E_ 111
Mvvv 92,42,252
W_ 0
C_ 111,110,111
C_ 110,108,109
Mavv 0,30,116
s_nop 0
Tv 38,v4,24576
Tv 40,v4,30720
Mavv 20,34,116
E_ 112
Mavv 24,34,120
s_nop 0
Tv 42,v5,24576
Tv 44,v5,30720
Mavv 4,30,120
E_ 113
Mavv 8,30,124
E_ 114
Mavv 28,34,124
E_ 115
Mavv 32,34,128
C_ 115,114,115
C_ 114,112,113
Mavv 12,30,128
E_ 76
Mavv 16,30,132
E_ 77
Mavv 36,34,132
W_ 0
E_ 78
Mavv 56,38,132
s_nop 0
Tv 30,v2,25600
Tv 32,v2,31744
Mavv 76,42,132
E_ 79
Mavv 72,42,128
s_nop 0
Tv 34,v3,25600
Tv 36,v3,31744
Mavv 52,38,128
C_ 96,76,77
C_ 97,78,79
Mavv 48,38,124
E_ 80
Mavv 68,42,124
E_ 81
Mavv 64,42,120
E_ 82
Mavv 44,38,120
E_ 83
Mavv 40,38,116
C_ 100,80,81
C_ 101,82,83
Mavv 60,42,116
W_ 0
E_ 84
Mavv 80,30,116
s_nop 0
Tv 38,v4,25600
Tv 40,v4,31744
Mavv 100,34,116
E_ 85
Mavv 104,34,120
s_nop 0
Tv 42,v5,25600
Tv 44,v5,31744
Mavv 84,30,120
E_ 86
Mavv 88,30,124
E_ 87
Mavv 108,34,124
C_ 104,84,85
C_ 105,86,87
Mavv 112,34,128
E_ 88
Mavv 92,30,128
E_ 89
Mavv 96,30,132
E_ 90
Mavv 116,34,132
W_ 0
E_ 91
Mavv 136,38,132
s_nop 0
Tv 30,v2,26624
Tv 32,v2,32768
Mavv 156,42,132
C_ 108,88,89
C_ 109,90,91
Mavv 152,42,128
s_nop 0
Tv 34,v3,26624
Tv 36,v3,32768
Mavv 132,38,128
E_ 92
Mavv 128,38,124
E_ 93
Mavv 148,42,124
E_ 94
Mavv 144,42,120
E_ 95
Mavv 124,38,120
C_ 112,92,93
C_ 113,94,95
Mavv 120,38,116
v_mov_b32 v92, 0x3f803f80
v_mov_b32 v93, 0x3f803f80
Mavv 140,42,116
W_ 0
v_mov_b32 v94, 0x3f803f80
v_mov_b32 v95, 0x3f803f80
Mavv 160,30,116
s_nop 0
Tv 38,v4,26624
Tv 40,v4,32768
Mavv 180,34,116
Mavv 184,34,120
s_nop 0
Tv 42,v5,26624
Tv 44,v5,32768
Mavv 164,30,120
Mavv 168,30,124
Mavv 188,34,124
S_ s65,s61,110592
Mavv 192,34,128
S_ m0,s51,24576
Mavv 172,30,128
s_waitcnt vmcnt(19) lgkmcnt(0)
s_barrier
Mavv 176,30,132
Ln v7,32,s65,0
Mavv 196,34,132
Mavv 216,38,132
Dv 30,%0,6144
Mavv 236,42,132
Dv 34,v1,6144
Mavv 232,42,128
Ln v8,32,s65,3072
S_ m0,s51,25472
Mavv 212,38,128
Mavv 208,38,124
Ln v7,32,s65,128
Mavv 228,42,124
Mavv 224,42,120
Ln v8,32,s65,3200
S_ m0,s51,26368
Mavv 204,38,120
Mavv 200,38,116
Ln v7,32,s65,256
Mavv 220,42,116
s_cmp_eq_u32 s4, 0
s_cbranch_scc1 .Lhs_c0
.Lhs_c0_back:
.Lstep0_7:
Zvvv 76,92,96
Dv 38,%0,7168
Zvvv 80,92,100
Dv 42,v1,7168
Zvvv 84,92,104
Zvvv 88,92,108
Zvvv 92,92,112
W_ 2
Nvvv 116,30,136,56
Mvvv 116,34,156
Nvvv 120,30,140,60
s_nop 1
v_max3_f32 v10, v76, v80, v84
s_nop 0
v_max3_f32 v10, v10, v88, v92
Mvvv 120,34,160
v_cmp_lt_f32 vcc, 0x42000000, v10
s_cbranch_vccnz .Ltramp0_6
Nvvv 124,30,144,64
A_ v46,v46,v76
A_ v47,v47,v80
Mvvv 124,34,164
A_ v48,v48,v84
A_ v49,v49,v88
Nvvv 128,30,148,68
A_ v50,v50,v92
Mvvv 128,34,168
Ln v8,32,s65,3328
Nvvv 132,30,152,72
Mvvv 132,34,172
W_ 0
Mvvv 132,38,192
Dv 30,%0,8192
Mvvv 132,42,212
Dv 34,v1,8192
Mvvv 128,38,188
Mvvv 128,42,208
Mvvv 124,38,184
Mvvv 124,42,204
Mvvv 120,38,180
Mvvv 120,42,200
Mvvv 116,38,176
Mvvv 116,42,196
W_ 0
Mvvv 116,30,216
Dv 38,%0,0
Mvvv 116,34,236
Dv 42,v1,0
Mvvv 120,30,220
Mvvv 120,34,240
Mvvv 124,30,224
Mvvv 124,34,244
Mvvv 128,30,228
Mvvv 128,34,248
s_nop 0
E_ 116
Mvvv 132,30,232
E_ 117
Mvvv 132,34,252
W_ 0
E_ 118
Nvvv 76,38,136,56
Dv 30,%0,1024
Mvvv 76,42,156
Dv 34,v1,1024
Nvvv 80,38,140,60
E_ 119
Mvvv 80,42,160
C_ 119,118,119
C_ 118,116,117
Nvvv 84,38,144,64
E_ 120
Mvvv 84,42,164
E_ 121
Nvvv 88,38,148,68
E_ 122
Mvvv 88,42,168
E_ 123
Nvvv 92,38,152,72
C_ 123,122,123
C_ 122,120,121
Mvvv 92,42,172
W_ 0
E_ 124
Mvvv 92,30,192
Dv 38,%0,2048
Mvvv 92,34,212
Dv 42,v1,2048
Mvvv 88,30,188
E_ 125
Mvvv 88,34,208
E_ 126
Mvvv 84,30,184
S_ s65,s61,98304
E_ 127
Mvvv 84,34,204
S_ m0,s51,0
C_ 127,126,127
C_ 126,124,125
Mvvv 80,30,180
s_waitcnt vmcnt(19) lgkmcnt(0)
s_barrier
Mvvv 80,34,200
L_ v9,36,s65,0
Mvvv 76,30,176
E_ 128
Mvvv 76,34,196
L_ v9,36,s65,3072
S_ m0,s51,896
Mvvv 76,38,216
Tv 30,v2,12288
Tv 32,v2,18432
Mvvv 76,42,236
L_ v9,36,s65,128
Mvvv 80,38,220
s_nop 0
Tv 34,v3,12288
Tv 36,v3,18432
Mvvv 80,42,240
L_ v9,36,s65,3200
S_ m0,s51,1792
Mvvv 84,38,224
E_ 129
Mvvv 84,42,244
L_ v9,36,s65,256
Mvvv 88,38,228
E_ 130
Mvvv 88,42,248
L_ v9,36,s65,3328
Mvvv 92,38,232
E_ 131
Mvvv 92,42,252
W_ 0
C_ 131,130,131
C_ 130,128,129
Mavv 0,30,96
s_nop 0
Tv 38,v4,12288
Tv 40,v4,18432
Mavv 20,34,96
E_ 132
Mavv 24,34,100
s_nop 0
Tv 42,v5,12288
Tv 44,v5,18432
Mavv 4,30,100
E_ 133
Mavv 8,30,104
E_ 134
Mavv 28,34,104
E_ 135
Mavv 32,34,108
C_ 135,134,135
C_ 134,132,133
Mavv 12,30,108
E_ 76
Mavv 16,30,112
E_ 77
Mavv 36,34,112
W_ 0
E_ 78
Mavv 56,38,112
s_nop 0
Tv 30,v2,13312
Tv 32,v2,19456
Mavv 76,42,112
E_ 79
Mavv 72,42,108
s_nop 0
Tv 34,v3,13312
Tv 36,v3,19456
Mavv 52,38,108
C_ 116,76,77
C_ 117,78,79
Mavv 48,38,104
E_ 80
Mavv 68,42,104
E_ 81
Mavv 64,42,100
E_ 82
Mavv 44,38,100
E_ 83
Mavv 40,38,96
C_ 120,80,81
C_ 121,82,83
Mavv 60,42,96
W_ 0
E_ 84
Mavv 80,30,96
s_nop 0
Tv 38,v4,13312
Tv 40,v4,19456
Mavv 100,34,96
E_ 85
Mavv 104,34,100
s_nop 0
Tv 42,v5,13312
Tv 44,v5,19456
Mavv 84,30,100
E_ 86
Mavv 88,30,104
E_ 87
Mavv 108,34,104
C_ 124,84,85
C_ 125,86,87
Mavv 112,34,108
E_ 88
Mavv 92,30,108
E_ 89
Mavv 96,30,112
E_ 90
Mavv 116,34,112
W_ 0
E_ 91
Mavv 136,38,112
s_nop 0
Tv 30,v2,14336
Tv 32,v2,20480
Mavv 156,42,112
C_ 128,88,89
C_ 129,90,91
Mavv 152,42,108
s_nop 0
Tv 34,v3,14336
Tv 36,v3,20480
Mavv 132,38,108
E_ 92
Mavv 128,38,104
E_ 93
Mavv 148,42,104
E_ 94
Mavv 144,42,100
E_ 95
Mavv 124,38,100
C_ 132,92,93
C_ 133,94,95
Mavv 120,38,96
v_mov_b32 v92, 0x3f803f80
v_mov_b32 v93, 0x3f803f80
Mavv 140,42,96
W_ 0
v_mov_b32 v94, 0x3f803f80
v_mov_b32 v95, 0x3f803f80
Mavv 160,30,96
s_nop 0
Tv 38,v4,14336
Tv 40,v4,20480
Mavv 180,34,96
Mavv 184,34,100
s_nop 0
Tv 42,v5,14336
Tv 44,v5,20480
Mavv 164,30,100
Mavv 168,30,104
Mavv 188,34,104
S_ s65,s61,122880
Mavv 192,34,108
S_ m0,s51,12288
Mavv 172,30,108
s_waitcnt vmcnt(19) lgkmcnt(0)
s_barrier
Mavv 176,30,112
Ln v7,32,s65,0
Mavv 196,34,112
Mavv 216,38,112
Dv 30,%0,43008
Mavv 236,42,112
Dv 34,v1,43008
Mavv 232,42,108
Ln v8,32,s65,3072
S_ m0,s51,13184
Mavv 212,38,108
Mavv 208,38,104
Ln v7,32,s65,128
Mavv 228,42,104
Mavv 224,42,100
Ln v8,32,s65,3200
S_ m0,s51,14080
Mavv 204,38,100
Mavv 200,38,96
Ln v7,32,s65,256
Mavv 220,42,96
.Lstep0_8:
Zvvv 76,92,116
Dv 38,%0,44032
Zvvv 80,92,120
Dv 42,v1,44032
Zvvv 84,92,124
Zvvv 88,92,128
Zvvv 92,92,132
W_ 2
Nvvv 96,30,136,56
Mvvv 96,34,156
Nvvv 100,30,140,60
s_nop 1
v_max3_f32 v10, v76, v80, v84
s_nop 0
v_max3_f32 v10, v10, v88, v92
Mvvv 100,34,160
v_cmp_lt_f32 vcc, 0x42000000, v10
s_cbranch_vccnz .Ltramp0_7
Nvvv 104,30,144,64
A_ v46,v46,v76
A_ v47,v47,v80
Mvvv 104,34,164
A_ v48,v48,v84
A_ v49,v49,v88
Nvvv 108,30,148,68
A_ v50,v50,v92
Mvvv 108,34,168
Ln v8,32,s65,3328
Nvvv 112,30,152,72
Mvvv 112,34,172
W_ 0
Mvvv 112,38,192
Dv 30,%0,45056
Mvvv 112,42,212
Dv 34,v1,45056
Mvvv 108,38,188
Mvvv 108,42,208
Mvvv 104,38,184
Mvvv 104,42,204
Mvvv 100,38,180
Mvvv 100,42,200
Mvvv 96,38,176
Mvvv 96,42,196
W_ 0
Mvvv 96,30,216
Dv 38,%0,36864
Mvvv 96,34,236
Dv 42,v1,36864
Mvvv 100,30,220
Mvvv 100,34,240
Mvvv 104,30,224
Mvvv 104,34,244
Mvvv 108,30,228
Mvvv 108,34,248
s_nop 0
E_ 96
Mvvv 112,30,232
E_ 97
Mvvv 112,34,252
W_ 0
E_ 98
Nvvv 76,38,136,56
Dv 30,%0,37888
Mvvv 76,42,156
Dv 34,v1,37888
Nvvv 80,38,140,60
E_ 99
Mvvv 80,42,160
C_ 99,98,99
C_ 98,96,97
Nvvv 84,38,144,64
E_ 100
Mvvv 84,42,164
E_ 101
Nvvv 88,38,148,68
E_ 102
Mvvv 88,42,168
E_ 103
Nvvv 92,38,152,72
C_ 103,102,103
C_ 102,100,101
Mvvv 92,42,172
W_ 0
E_ 104
Mvvv 92,30,192
Dv 38,%0,38912
Mvvv 92,34,212
Dv 42,v1,38912
Mvvv 88,30,188
E_ 105
Mvvv 88,34,208
E_ 106
Mvvv 84,30,184
S_ s65,s61,110592
E_ 107
Mvvv 84,34,204
S_ m0,s51,36864
C_ 107,106,107
C_ 106,104,105
Mvvv 80,30,180
s_waitcnt vmcnt(18) lgkmcnt(0)
s_barrier
Mvvv 80,34,200
L_ v9,36,s65,0
Mvvv 76,30,176
E_ 108
Mvvv 76,34,196
L_ v9,36,s65,3072
S_ m0,s51,37760
Mvvv 76,38,216
Tv 30,v2,49152
Tv 32,v2,55296
Mvvv 76,42,236
L_ v9,36,s65,128
Mvvv 80,38,220
s_nop 0
Tv 34,v3,49152
Tv 36,v3,55296
Mvvv 80,42,240
L_ v9,36,s65,3200
S_ m0,s51,38656
Mvvv 84,38,224
E_ 109
Mvvv 84,42,244
L_ v9,36,s65,256
Mvvv 88,38,228
E_ 110
Mvvv 88,42,248
L_ v9,36,s65,3328
Mvvv 92,38,232
E_ 111
Mvvv 92,42,252
W_ 0
C_ 111,110,111
C_ 110,108,109
Mavv 0,30,116
s_nop 0
Tv 38,v4,49152
Tv 40,v4,55296
Mavv 20,34,116
E_ 112
Mavv 24,34,120
s_nop 0
Tv 42,v5,49152
Tv 44,v5,55296
Mavv 4,30,120
E_ 113
Mavv 8,30,124
E_ 114
Mavv 28,34,124
E_ 115
Mavv 32,34,128
C_ 115,114,115
C_ 114,112,113
Mavv 12,30,128
E_ 76
Mavv 16,30,132
E_ 77
Mavv 36,34,132
W_ 0
E_ 78
Mavv 56,38,132
s_nop 0
Tv 30,v2,50176
Tv 32,v2,56320
Mavv 76,42,132
E_ 79
Mavv 72,42,128
s_nop 0
Tv 34,v3,50176
Tv 36,v3,56320
Mavv 52,38,128
C_ 96,76,77
C_ 97,78,79
Mavv 48,38,124
E_ 80
Mavv 68,42,124
E_ 81
Mavv 64,42,120
E_ 82
Mavv 44,38,120
E_ 83
Mavv 40,38,116
C_ 100,80,81
C_ 101,82,83
Mavv 60,42,116
W_ 0
E_ 84
Mavv 80,30,116
s_nop 0
Tv 38,v4,50176
Tv 40,v4,56320
Mavv 100,34,116
E_ 85
Mavv 104,34,120
s_nop 0
Tv 42,v5,50176
Tv 44,v5,56320
Mavv 84,30,120
E_ 86
Mavv 88,30,124
E_ 87
Mavv 108,34,124
C_ 104,84,85
C_ 105,86,87
Mavv 112,34,128
E_ 88
Mavv 92,30,128
E_ 89
Mavv 96,30,132
E_ 90
Mavv 116,34,132
W_ 0
E_ 91
Mavv 136,38,132
s_nop 0
Tv 30,v2,51200
Tv 32,v2,57344
Mavv 156,42,132
C_ 108,88,89
C_ 109,90,91
Mavv 152,42,128
s_nop 0
Tv 34,v3,51200
Tv 36,v3,57344
Mavv 132,38,128
E_ 92
Mavv 128,38,124
E_ 93
Mavv 148,42,124
E_ 94
Mavv 144,42,120
E_ 95
Mavv 124,38,120
C_ 112,92,93
C_ 113,94,95
Mavv 120,38,116
v_mov_b32 v92, 0x3f803f80
v_mov_b32 v93, 0x3f803f80
Mavv 140,42,116
W_ 0
v_mov_b32 v94, 0x3f803f80
v_mov_b32 v95, 0x3f803f80
Mavv 160,30,116
s_nop 0
Tv 38,v4,51200
Tv 40,v4,57344
Mavv 180,34,116
Mavv 184,34,120
s_nop 0
Tv 42,v5,51200
Tv 44,v5,57344
Mavv 164,30,120
Mavv 168,30,124
Mavv 188,34,124
S_ s65,s61,135168
Mavv 192,34,128
S_ m0,s51,49152
Mavv 172,30,128
s_waitcnt vmcnt(18) lgkmcnt(0)
s_barrier
Mavv 176,30,132
Ln v7,32,s65,0
Mavv 196,34,132
Mavv 216,38,132
Dv 30,%0,30720
Mavv 236,42,132
Dv 34,v1,30720
Mavv 232,42,128
Ln v8,32,s65,3072
S_ m0,s51,50048
Mavv 212,38,128
Mavv 208,38,124
Ln v7,32,s65,128
Mavv 228,42,124
Mavv 224,42,120
Ln v8,32,s65,3200
S_ m0,s51,50944
Mavv 204,38,120
Mavv 200,38,116
Ln v7,32,s65,256
Mavv 220,42,116
s_cmp_eq_u32 s4, 0
s_cbranch_scc1 .Lhs_d0
.Lhs_d0_back:
.Lstep0_9:
Zvvv 76,92,96
Dv 38,%0,31744
Zvvv 80,92,100
Dv 42,v1,31744
Zvvv 84,92,104
Zvvv 88,92,108
Zvvv 92,92,112
W_ 2
Nvvv 116,30,136,56
Mvvv 116,34,156
Nvvv 120,30,140,60
s_nop 1
v_max3_f32 v10, v76, v80, v84
s_nop 0
v_max3_f32 v10, v10, v88, v92
Mvvv 120,34,160
v_cmp_lt_f32 vcc, 0x42000000, v10
s_cbranch_vccnz .Ltramp0_8
Nvvv 124,30,144,64
A_ v46,v46,v76
A_ v47,v47,v80
Mvvv 124,34,164
A_ v48,v48,v84
A_ v49,v49,v88
Nvvv 128,30,148,68
A_ v50,v50,v92
Mvvv 128,34,168
Ln v8,32,s65,3328
Nvvv 132,30,152,72
Mvvv 132,34,172
W_ 0
Mvvv 132,38,192
Dv 30,%0,32768
Mvvv 132,42,212
Dv 34,v1,32768
Mvvv 128,38,188
Mvvv 128,42,208
Mvvv 124,38,184
Mvvv 124,42,204
Mvvv 120,38,180
Mvvv 120,42,200
Mvvv 116,38,176
Mvvv 116,42,196
W_ 0
Mvvv 116,30,216
Dv 38,%0,24576
Mvvv 116,34,236
Dv 42,v1,24576
Mvvv 120,30,220
Mvvv 120,34,240
Mvvv 124,30,224
Mvvv 124,34,244
Mvvv 128,30,228
Mvvv 128,34,248
s_nop 0
E_ 116
Mvvv 132,30,232
E_ 117
Mvvv 132,34,252
W_ 0
E_ 118
Nvvv 76,38,136,56
Dv 30,%0,25600
Mvvv 76,42,156
Dv 34,v1,25600
Nvvv 80,38,140,60
E_ 119
Mvvv 80,42,160
C_ 119,118,119
C_ 118,116,117
Nvvv 84,38,144,64
E_ 120
Mvvv 84,42,164
E_ 121
Nvvv 88,38,148,68
E_ 122
Mvvv 88,42,168
E_ 123
Nvvv 92,38,152,72
C_ 123,122,123
C_ 122,120,121
Mvvv 92,42,172
W_ 0
E_ 124
Mvvv 92,30,192
Dv 38,%0,26624
Mvvv 92,34,212
Dv 42,v1,26624
Mvvv 88,30,188
E_ 125
Mvvv 88,34,208
E_ 126
Mvvv 84,30,184
S_ s65,s61,122880
E_ 127
Mvvv 84,34,204
S_ m0,s51,24576
C_ 127,126,127
C_ 126,124,125
Mvvv 80,30,180
s_waitcnt vmcnt(18) lgkmcnt(0)
s_barrier
Mvvv 80,34,200
L_ v9,36,s65,0
Mvvv 76,30,176
E_ 128
Mvvv 76,34,196
L_ v9,36,s65,3072
S_ m0,s51,25472
Mvvv 76,38,216
Tv 30,v2,0
Tv 32,v2,6144
Mvvv 76,42,236
L_ v9,36,s65,128
Mvvv 80,38,220
s_nop 0
Tv 34,v3,0
Tv 36,v3,6144
Mvvv 80,42,240
L_ v9,36,s65,3200
S_ m0,s51,26368
Mvvv 84,38,224
E_ 129
Mvvv 84,42,244
L_ v9,36,s65,256
Mvvv 88,38,228
E_ 130
Mvvv 88,42,248
L_ v9,36,s65,3328
Mvvv 92,38,232
E_ 131
Mvvv 92,42,252
W_ 0
C_ 131,130,131
C_ 130,128,129
Mavv 0,30,96
s_nop 0
Tv 38,v4,0
Tv 40,v4,6144
Mavv 20,34,96
E_ 132
Mavv 24,34,100
s_nop 0
Tv 42,v5,0
Tv 44,v5,6144
Mavv 4,30,100
E_ 133
Mavv 8,30,104
E_ 134
Mavv 28,34,104
E_ 135
Mavv 32,34,108
C_ 135,134,135
C_ 134,132,133
Mavv 12,30,108
E_ 76
Mavv 16,30,112
E_ 77
Mavv 36,34,112
W_ 0
E_ 78
Mavv 56,38,112
s_nop 0
Tv 30,v2,1024
Tv 32,v2,7168
Mavv 76,42,112
E_ 79
Mavv 72,42,108
s_nop 0
Tv 34,v3,1024
Tv 36,v3,7168
Mavv 52,38,108
C_ 116,76,77
C_ 117,78,79
Mavv 48,38,104
E_ 80
Mavv 68,42,104
E_ 81
Mavv 64,42,100
E_ 82
Mavv 44,38,100
E_ 83
Mavv 40,38,96
C_ 120,80,81
C_ 121,82,83
Mavv 60,42,96
W_ 0
E_ 84
Mavv 80,30,96
s_nop 0
Tv 38,v4,1024
Tv 40,v4,7168
Mavv 100,34,96
E_ 85
Mavv 104,34,100
s_nop 0
Tv 42,v5,1024
Tv 44,v5,7168
Mavv 84,30,100
E_ 86
Mavv 88,30,104
E_ 87
Mavv 108,34,104
C_ 124,84,85
C_ 125,86,87
Mavv 112,34,108
E_ 88
Mavv 92,30,108
E_ 89
Mavv 96,30,112
E_ 90
Mavv 116,34,112
W_ 0
E_ 91
Mavv 136,38,112
s_nop 0
Tv 30,v2,2048
Tv 32,v2,8192
Mavv 156,42,112
C_ 128,88,89
C_ 129,90,91
Mavv 152,42,108
s_nop 0
Tv 34,v3,2048
Tv 36,v3,8192
Mavv 132,38,108
E_ 92
Mavv 128,38,104
E_ 93
Mavv 148,42,104
E_ 94
Mavv 144,42,100
E_ 95
Mavv 124,38,100
C_ 132,92,93
C_ 133,94,95
Mavv 120,38,96
v_mov_b32 v92, 0x3f803f80
v_mov_b32 v93, 0x3f803f80
Mavv 140,42,96
W_ 0
v_mov_b32 v94, 0x3f803f80
v_mov_b32 v95, 0x3f803f80
Mavv 160,30,96
s_nop 0
Tv 38,v4,2048
Tv 40,v4,8192
Mavv 180,34,96
Mavv 184,34,100
s_nop 0
Tv 42,v5,2048
Tv 44,v5,8192
Mavv 164,30,100
Mavv 168,30,104
Mavv 188,34,104
S_ s65,s61,147456
Mavv 192,34,108
S_ m0,s51,0
Mavv 172,30,108
s_waitcnt vmcnt(18) lgkmcnt(0)
s_barrier
Mavv 176,30,112
Ln v7,32,s65,0
Mavv 196,34,112
Mavv 216,38,112
Dv 30,%0,18432
Mavv 236,42,112
Dv 34,v1,18432
Mavv 232,42,108
Ln v8,32,s65,3072
S_ m0,s51,896
Mavv 212,38,108
Mavv 208,38,104
Ln v7,32,s65,128
Mavv 228,42,104
Mavv 224,42,100
Ln v8,32,s65,3200
S_ m0,s51,1792
Mavv 204,38,100
Mavv 200,38,96
Ln v7,32,s65,256
Mavv 220,42,96
.Lstep0_10:
Zvvv 76,92,116
Dv 38,%0,19456
Zvvv 80,92,120
Dv 42,v1,19456
Zvvv 84,92,124
Zvvv 88,92,128
Zvvv 92,92,132
W_ 2
Nvvv 96,30,136,56
Mvvv 96,34,156
Nvvv 100,30,140,60
s_nop 1
v_max3_f32 v10, v76, v80, v84
s_nop 0
v_max3_f32 v10, v10, v88, v92
Mvvv 100,34,160
v_cmp_lt_f32 vcc, 0x42000000, v10
s_cbranch_vccnz .Ltramp0_9
Nvvv 104,30,144,64
A_ v46,v46,v76
A_ v47,v47,v80
Mvvv 104,34,164
A_ v48,v48,v84
A_ v49,v49,v88
Nvvv 108,30,148,68
A_ v50,v50,v92
Mvvv 108,34,168
Ln v8,32,s65,3328
Nvvv 112,30,152,72
Mvvv 112,34,172
W_ 0
Mvvv 112,38,192
Dv 30,%0,20480
Mvvv 112,42,212
Dv 34,v1,20480
Mvvv 108,38,188
Mvvv 108,42,208
Mvvv 104,38,184
Mvvv 104,42,204
Mvvv 100,38,180
Mvvv 100,42,200
Mvvv 96,38,176
Mvvv 96,42,196
W_ 0
Mvvv 96,30,216
Dv 38,%0,12288
Mvvv 96,34,236
Dv 42,v1,12288
Mvvv 100,30,220
Mvvv 100,34,240
Mvvv 104,30,224
Mvvv 104,34,244
Mvvv 108,30,228
Mvvv 108,34,248
s_nop 0
E_ 96
Mvvv 112,30,232
E_ 97
Mvvv 112,34,252
W_ 0
E_ 98
Nvvv 76,38,136,56
Dv 30,%0,13312
Mvvv 76,42,156
Dv 34,v1,13312
Nvvv 80,38,140,60
E_ 99
Mvvv 80,42,160
C_ 99,98,99
C_ 98,96,97
Nvvv 84,38,144,64
E_ 100
Mvvv 84,42,164
E_ 101
Nvvv 88,38,148,68
E_ 102
Mvvv 88,42,168
E_ 103
Nvvv 92,38,152,72
C_ 103,102,103
C_ 102,100,101
Mvvv 92,42,172
W_ 0
E_ 104
Mvvv 92,30,192
Dv 38,%0,14336
Mvvv 92,34,212
Dv 42,v1,14336
Mvvv 88,30,188
E_ 105
Mvvv 88,34,208
E_ 106
Mvvv 84,30,184
S_ s65,s61,135168
E_ 107
Mvvv 84,34,204
S_ m0,s51,12288
C_ 107,106,107
C_ 106,104,105
Mvvv 80,30,180
s_waitcnt vmcnt(18) lgkmcnt(0)
s_barrier
Mvvv 80,34,200
L_ v9,36,s65,0
Mvvv 76,30,176
E_ 108
Mvvv 76,34,196
L_ v9,36,s65,3072
S_ m0,s51,13184
Mvvv 76,38,216
Tv 30,v2,36864
Tv 32,v2,43008
Mvvv 76,42,236
L_ v9,36,s65,128
Mvvv 80,38,220
s_nop 0
Tv 34,v3,36864
Tv 36,v3,43008
Mvvv 80,42,240
L_ v9,36,s65,3200
S_ m0,s51,14080
Mvvv 84,38,224
E_ 109
Mvvv 84,42,244
L_ v9,36,s65,256
Mvvv 88,38,228
E_ 110
Mvvv 88,42,248
L_ v9,36,s65,3328
Mvvv 92,38,232
E_ 111
Mvvv 92,42,252
W_ 0
C_ 111,110,111
C_ 110,108,109
Mavv 0,30,116
s_nop 0
Tv 38,v4,36864
Tv 40,v4,43008
Mavv 20,34,116
E_ 112
Mavv 24,34,120
s_nop 0
Tv 42,v5,36864
Tv 44,v5,43008
Mavv 4,30,120
E_ 113
Mavv 8,30,124
E_ 114
Mavv 28,34,124
E_ 115
Mavv 32,34,128
C_ 115,114,115
C_ 114,112,113
Mavv 12,30,128
E_ 76
Mavv 16,30,132
E_ 77
Mavv 36,34,132
W_ 0
E_ 78
Mavv 56,38,132
s_nop 0
Tv 30,v2,37888
Tv 32,v2,44032
Mavv 76,42,132
E_ 79
Mavv 72,42,128
s_nop 0
Tv 34,v3,37888
Tv 36,v3,44032
Mavv 52,38,128
C_ 96,76,77
C_ 97,78,79
Mavv 48,38,124
E_ 80
Mavv 68,42,124
E_ 81
Mavv 64,42,120
E_ 82
Mavv 44,38,120
E_ 83
Mavv 40,38,116
C_ 100,80,81
C_ 101,82,83
Mavv 60,42,116
W_ 0
E_ 84
Mavv 80,30,116
s_nop 0
Tv 38,v4,37888
Tv 40,v4,44032
Mavv 100,34,116
E_ 85
Mavv 104,34,120
s_nop 0
Tv 42,v5,37888
Tv 44,v5,44032
Mavv 84,30,120
E_ 86
Mavv 88,30,124
E_ 87
Mavv 108,34,124
C_ 104,84,85
C_ 105,86,87
Mavv 112,34,128
E_ 88
Mavv 92,30,128
E_ 89
Mavv 96,30,132
E_ 90
Mavv 116,34,132
W_ 0
E_ 91
Mavv 136,38,132
s_nop 0
Tv 30,v2,38912
Tv 32,v2,45056
Mavv 156,42,132
C_ 108,88,89
C_ 109,90,91
Mavv 152,42,128
s_nop 0
Tv 34,v3,38912
Tv 36,v3,45056
Mavv 132,38,128
E_ 92
Mavv 128,38,124
E_ 93
Mavv 148,42,124
E_ 94
Mavv 144,42,120
E_ 95
Mavv 124,38,120
C_ 112,92,93
C_ 113,94,95
Mavv 120,38,116
v_mov_b32 v92, 0x3f803f80
v_mov_b32 v93, 0x3f803f80
Mavv 140,42,116
W_ 0
v_mov_b32 v94, 0x3f803f80
v_mov_b32 v95, 0x3f803f80
Mavv 160,30,116
s_nop 0
Tv 38,v4,38912
Tv 40,v4,45056
Mavv 180,34,116
Mavv 184,34,120
s_nop 0
Tv 42,v5,38912
Tv 44,v5,45056
Mavv 164,30,120
Mavv 168,30,124
Mavv 188,34,124
S_ s65,s61,159744
Mavv 192,34,128
S_ m0,s51,36864
Mavv 172,30,128
s_waitcnt vmcnt(18) lgkmcnt(0)
s_barrier
Mavv 176,30,132
Ln v7,32,s65,0
Mavv 196,34,132
Mavv 216,38,132
Dv 30,%0,55296
Mavv 236,42,132
Dv 34,v1,55296
Mavv 232,42,128
Ln v8,32,s65,3072
S_ m0,s51,37760
Mavv 212,38,128
Mavv 208,38,124
Ln v7,32,s65,128
Mavv 228,42,124
Mavv 224,42,120
Ln v8,32,s65,3200
S_ m0,s51,38656
Mavv 204,38,120
Mavv 200,38,116
Ln v7,32,s65,256
Mavv 220,42,116
.Lstep0_11:
Zvvv 76,92,96
Dv 38,%0,56320
Zvvv 80,92,100
Dv 42,v1,56320
Zvvv 84,92,104
Zvvv 88,92,108
Zvvv 92,92,112
W_ 2
Nvvv 116,30,136,56
Mvvv 116,34,156
Nvvv 120,30,140,60
s_nop 1
v_max3_f32 v10, v76, v80, v84
s_nop 0
v_max3_f32 v10, v10, v88, v92
Mvvv 120,34,160
v_cmp_lt_f32 vcc, 0x42000000, v10
s_cbranch_vccnz .Ltramp0_10
Nvvv 124,30,144,64
A_ v46,v46,v76
A_ v47,v47,v80
Mvvv 124,34,164
A_ v48,v48,v84
A_ v49,v49,v88
Nvvv 128,30,148,68
A_ v50,v50,v92
Mvvv 128,34,168
Ln v8,32,s65,3328
Nvvv 132,30,152,72
Mvvv 132,34,172
W_ 0
Mvvv 132,38,192
Dv 30,%0,57344
Mvvv 132,42,212
Dv 34,v1,57344
Mvvv 128,38,188
Mvvv 128,42,208
Mvvv 124,38,184
Mvvv 124,42,204
Mvvv 120,38,180
Mvvv 120,42,200
Mvvv 116,38,176
Mvvv 116,42,196
W_ 0
Mvvv 116,30,216
Dv 38,%0,49152
Mvvv 116,34,236
Dv 42,v1,49152
Mvvv 120,30,220
Mvvv 120,34,240
Mvvv 124,30,224
Mvvv 124,34,244
Mvvv 128,30,228
Mvvv 128,34,248
s_nop 0
E_ 116
Mvvv 132,30,232
E_ 117
Mvvv 132,34,252
W_ 0
E_ 118
Nvvv 76,38,136,56
Dv 30,%0,50176
Mvvv 76,42,156
Dv 34,v1,50176
Nvvv 80,38,140,60
E_ 119
Mvvv 80,42,160
C_ 119,118,119
C_ 118,116,117
Nvvv 84,38,144,64
E_ 120
Mvvv 84,42,164
E_ 121
Nvvv 88,38,148,68
E_ 122
Mvvv 88,42,168
E_ 123
Nvvv 92,38,152,72
C_ 123,122,123
C_ 122,120,121
Mvvv 92,42,172
W_ 0
E_ 124
Mvvv 92,30,192
Dv 38,%0,51200
Mvvv 92,34,212
Dv 42,v1,51200
Mvvv 88,30,188
E_ 125
Mvvv 88,34,208
E_ 126
Mvvv 84,30,184
S_ s65,s61,147456
E_ 127
Mvvv 84,34,204
S_ m0,s51,49152
C_ 127,126,127
C_ 126,124,125
Mvvv 80,30,180
s_waitcnt vmcnt(18) lgkmcnt(0)
s_barrier
Mvvv 80,34,200
L_ v9,36,s65,0
Mvvv 76,30,176
E_ 128
Mvvv 76,34,196
L_ v9,36,s65,3072
S_ m0,s51,50048
Mvvv 76,38,216
Tv 30,v2,24576
Tv 32,v2,30720
Mvvv 76,42,236
L_ v9,36,s65,128
Mvvv 80,38,220
s_nop 0
Tv 34,v3,24576
Tv 36,v3,30720
Mvvv 80,42,240
L_ v9,36,s65,3200
S_ m0,s51,50944
Mvvv 84,38,224
E_ 129
Mvvv 84,42,244
L_ v9,36,s65,256
Mvvv 88,38,228
E_ 130
Mvvv 88,42,248
L_ v9,36,s65,3328
Mvvv 92,38,232
E_ 131
Mvvv 92,42,252
W_ 0
C_ 131,130,131
C_ 130,128,129
Mavv 0,30,96
s_nop 0
Tv 38,v4,24576
Tv 40,v4,30720
Mavv 20,34,96
E_ 132
Mavv 24,34,100
s_nop 0
Tv 42,v5,24576
Tv 44,v5,30720
Mavv 4,30,100
E_ 133
Mavv 8,30,104
E_ 134
Mavv 28,34,104
E_ 135
Mavv 32,34,108
C_ 135,134,135
C_ 134,132,133
Mavv 12,30,108
E_ 76
Mavv 16,30,112
E_ 77
Mavv 36,34,112
W_ 0
E_ 78
Mavv 56,38,112
s_nop 0
Tv 30,v2,25600
Tv 32,v2,31744
Mavv 76,42,112
E_ 79
Mavv 72,42,108
s_nop 0
Tv 34,v3,25600
Tv 36,v3,31744
Mavv 52,38,108
C_ 116,76,77
C_ 117,78,79
Mavv 48,38,104
E_ 80
Mavv 68,42,104
E_ 81
Mavv 64,42,100
E_ 82
Mavv 44,38,100
E_ 83
Mavv 40,38,96
C_ 120,80,81
C_ 121,82,83
Mavv 60,42,96
W_ 0
E_ 84
Mavv 80,30,96
s_nop 0
Tv 38,v4,25600
Tv 40,v4,31744
Mavv 100,34,96
E_ 85
Mavv 104,34,100
s_nop 0
Tv 42,v5,25600
Tv 44,v5,31744
Mavv 84,30,100
E_ 86
Mavv 88,30,104
E_ 87
Mavv 108,34,104
C_ 124,84,85
C_ 125,86,87
Mavv 112,34,108
E_ 88
Mavv 92,30,108
E_ 89
Mavv 96,30,112
E_ 90
Mavv 116,34,112
W_ 0
E_ 91
Mavv 136,38,112
s_nop 0
Tv 30,v2,26624
Tv 32,v2,32768
Mavv 156,42,112
C_ 128,88,89
C_ 129,90,91
Mavv 152,42,108
s_nop 0
Tv 34,v3,26624
Tv 36,v3,32768
Mavv 132,38,108
E_ 92
Mavv 128,38,104
E_ 93
Mavv 148,42,104
E_ 94
Mavv 144,42,100
E_ 95
Mavv 124,38,100
C_ 132,92,93
C_ 133,94,95
Mavv 120,38,96
v_mov_b32 v92, 0x3f803f80
v_mov_b32 v93, 0x3f803f80
Mavv 140,42,96
W_ 0
v_mov_b32 v94, 0x3f803f80
v_mov_b32 v95, 0x3f803f80
Mavv 160,30,96
s_nop 0
Tv 38,v4,26624
Tv 40,v4,32768
Mavv 180,34,96
Mavv 184,34,100
s_nop 0
Tv 42,v5,26624
Tv 44,v5,32768
Mavv 164,30,100
Mavv 168,30,104
Mavv 188,34,104
S_ s65,s61,172032
Mavv 192,34,108
S_ m0,s51,24576
Mavv 172,30,108
s_waitcnt vmcnt(18) lgkmcnt(0)
s_barrier
Mavv 176,30,112
Ln v7,32,s65,0
Mavv 196,34,112
Mavv 216,38,112
Dv 30,%0,6144
Mavv 236,42,112
Dv 34,v1,6144
Mavv 232,42,108
Ln v8,32,s65,3072
S_ m0,s51,25472
Mavv 212,38,108
Mavv 208,38,104
Ln v7,32,s65,128
Mavv 228,42,104
Mavv 224,42,100
Ln v8,32,s65,3200
S_ m0,s51,26368
Mavv 204,38,100
Mavv 200,38,96
Ln v7,32,s65,256
Mavv 220,42,96
.Lstep0_12:
Zvvv 76,92,116
Dv 38,%0,7168
Zvvv 80,92,120
Dv 42,v1,7168
Zvvv 84,92,124
Zvvv 88,92,128
Zvvv 92,92,132
W_ 2
Nvvv 96,30,136,56
Mvvv 96,34,156
Nvvv 100,30,140,60
s_nop 1
v_max3_f32 v10, v76, v80, v84
s_nop 0
v_max3_f32 v10, v10, v88, v92
Mvvv 100,34,160
v_cmp_lt_f32 vcc, 0x42000000, v10
s_cbranch_vccnz .Ltramp0_11
Nvvv 104,30,144,64
A_ v46,v46,v76
A_ v47,v47,v80
Mvvv 104,34,164
A_ v48,v48,v84
A_ v49,v49,v88
Nvvv 108,30,148,68
A_ v50,v50,v92
Mvvv 108,34,168
Ln v8,32,s65,3328
Nvvv 112,30,152,72
R_ v11,a248
Mvvv 112,34,172
W_ 0
v_min_u32 v11, 240, v11
Mvvv 112,38,192
Dv 30,%0,8192
v_lshlrev_b32 v11, 3, v11
Mvvv 112,42,212
Dv 34,v1,8192
Mvvv 108,38,188
buffer_load_dword a254, v11, s[44:47], s66 offen
Mvvv 108,42,208
Mvvv 104,38,184
Mvvv 104,42,204
Mvvv 100,38,180
Mvvv 100,42,200
Mvvv 96,38,176
Mvvv 96,42,196
W_ 0
Mvvv 96,30,216
Dv 38,%0,0
Mvvv 96,34,236
Dv 42,v1,0
Mvvv 100,30,220
Mvvv 100,34,240
Mvvv 104,30,224
Mvvv 104,34,244
Mvvv 108,30,228
Mvvv 108,34,248
s_nop 0
E_ 96
Mvvv 112,30,232
E_ 97
Mvvv 112,34,252
W_ 0
E_ 98
Nvvv 76,38,136,56
Dv 30,%0,1024
Mvvv 76,42,156
Dv 34,v1,1024
Nvvv 80,38,140,60
E_ 99
Mvvv 80,42,160
C_ 99,98,99
C_ 98,96,97
Nvvv 84,38,144,64
E_ 100
Mvvv 84,42,164
E_ 101
Nvvv 88,38,148,68
E_ 102
Mvvv 88,42,168
E_ 103
Nvvv 92,38,152,72
C_ 103,102,103
C_ 102,100,101
Mvvv 92,42,172
W_ 0
E_ 104
Mvvv 92,30,192
Dv 38,%0,2048
Mvvv 92,34,212
Dv 42,v1,2048
Mvvv 88,30,188
E_ 105
Mvvv 88,34,208
E_ 106
Mvvv 84,30,184
S_ s65,s61,159744
E_ 107
Mvvv 84,34,204
S_ m0,s51,0
C_ 107,106,107
C_ 106,104,105
Mvvv 80,30,180
s_waitcnt vmcnt(19) lgkmcnt(0)
s_barrier
Mvvv 80,34,200
L_ v9,36,s65,0
Mvvv 76,30,176
E_ 108
Mvvv 76,34,196
L_ v9,36,s65,3072
S_ m0,s51,896
Mvvv 76,38,216
Tv 30,v2,12288
Tv 32,v2,18432
Mvvv 76,42,236
L_ v9,36,s65,128
Mvvv 80,38,220
s_nop 0
Tv 34,v3,12288
Tv 36,v3,18432
Mvvv 80,42,240
L_ v9,36,s65,3200
S_ m0,s51,1792
Mvvv 84,38,224
E_ 109
Mvvv 84,42,244
L_ v9,36,s65,256
Mvvv 88,38,228
E_ 110
Mvvv 88,42,248
L_ v9,36,s65,3328
Mvvv 92,38,232
E_ 111
Mvvv 92,42,252
W_ 0
C_ 111,110,111
C_ 110,108,109
Mavv 0,30,116
s_nop 0
Tv 38,v4,12288
Tv 40,v4,18432
Mavv 20,34,116
E_ 112
Mavv 24,34,120
s_nop 0
Tv 42,v5,12288
Tv 44,v5,18432
Mavv 4,30,120
E_ 113
Mavv 8,30,124
E_ 114
Mavv 28,34,124
E_ 115
Mavv 32,34,128
C_ 115,114,115
C_ 114,112,113
)A"
R"A(Mavv 12,30,128
E_ 76
Mavv 16,30,132
E_ 77
Mavv 36,34,132
W_ 0
E_ 78
Mavv 56,38,132
s_nop 0
Tv 30,v2,13312
Tv 32,v2,19456
Mavv 76,42,132
E_ 79
Mavv 72,42,128
s_nop 0
Tv 34,v3,13312
Tv 36,v3,19456
Mavv 52,38,128
C_ 96,76,77
C_ 97,78,79
Mavv 48,38,124
E_ 80
Mavv 68,42,124
E_ 81
Mavv 64,42,120
E_ 82
Mavv 44,38,120
E_ 83
Mavv 40,38,116
C_ 100,80,81
C_ 101,82,83
Mavv 60,42,116
W_ 0
E_ 84
Mavv 80,30,116
s_nop 0
Tv 38,v4,13312
Tv 40,v4,19456
Mavv 100,34,116
E_ 85
Mavv 104,34,120
s_nop 0
Tv 42,v5,13312
Tv 44,v5,19456
Mavv 84,30,120
E_ 86
Mavv 88,30,124
E_ 87
Mavv 108,34,124
C_ 104,84,85
C_ 105,86,87
Mavv 112,34,128
E_ 88
Mavv 92,30,128
E_ 89
Mavv 96,30,132
E_ 90
Mavv 116,34,132
W_ 0
E_ 91
Mavv 136,38,132
s_nop 0
Tv 30,v2,14336
Tv 32,v2,20480
Mavv 156,42,132
C_ 108,88,89
C_ 109,90,91
Mavv 152,42,128
s_nop 0
Tv 34,v3,14336
Tv 36,v3,20480
Mavv 132,38,128
E_ 92
Mavv 128,38,124
E_ 93
Mavv 148,42,124
E_ 94
Mavv 144,42,120
E_ 95
Mavv 124,38,120
C_ 112,92,93
C_ 113,94,95
Mavv 120,38,116
v_mov_b32 v92, 0x3f803f80
v_mov_b32 v93, 0x3f803f80
Mavv 140,42,116
W_ 0
v_mov_b32 v94, 0x3f803f80
v_mov_b32 v95, 0x3f803f80
Mavv 160,30,116
s_nop 0
Tv 38,v4,14336
Tv 40,v4,20480
Mavv 180,34,116
Mavv 184,34,120
s_nop 0
Tv 42,v5,14336
Tv 44,v5,20480
Mavv 164,30,120
Mavv 168,30,124
Mavv 188,34,124
S_ s65,s61,184320
Mavv 192,34,128
S_ m0,s51,12288
Mavv 172,30,128
s_waitcnt vmcnt(19) lgkmcnt(0)
s_barrier
Mavv 176,30,132
Ln v7,32,s65,0
Mavv 196,34,132
Mavv 216,38,132
Dv 30,%0,43008
Mavv 236,42,132
Dv 34,v1,43008
Mavv 232,42,128
Ln v8,32,s65,3072
S_ m0,s51,13184
Mavv 212,38,128
Mavv 208,38,124
Ln v7,32,s65,128
Mavv 228,42,124
Mavv 224,42,120
Ln v8,32,s65,3200
S_ m0,s51,14080
Mavv 204,38,120
Mavv 200,38,116
Ln v7,32,s65,256
Mavv 220,42,116
.Lstep0_13:
Zvvv 76,92,96
Dv 38,%0,44032
Zvvv 80,92,100
Dv 42,v1,44032
Zvvv 84,92,104
Zvvv 88,92,108
Zvvv 92,92,112
W_ 2
Nvvv 116,30,136,56
Mvvv 116,34,156
Nvvv 120,30,140,60
s_nop 1
v_max3_f32 v10, v76, v80, v84
s_nop 0
v_max3_f32 v10, v10, v88, v92
Mvvv 120,34,160
v_cmp_lt_f32 vcc, 0x42000000, v10
s_cbranch_vccnz .Ltramp0_12
Nvvv 124,30,144,64
A_ v46,v46,v76
A_ v47,v47,v80
Mvvv 124,34,164
A_ v48,v48,v84
A_ v49,v49,v88
Nvvv 128,30,148,68
A_ v50,v50,v92
Mvvv 128,34,168
Ln v8,32,s65,3328
Nvvv 132,30,152,72
Mvvv 132,34,172
W_ 0
Mvvv 132,38,192
Dv 30,%0,45056
Mvvv 132,42,212
Dv 34,v1,45056
Mvvv 128,38,188
Mvvv 128,42,208
Mvvv 124,38,184
Mvvv 124,42,204
Mvvv 120,38,180
Mvvv 120,42,200
Mvvv 116,38,176
Mvvv 116,42,196
W_ 0
Mvvv 116,30,216
Dv 38,%0,36864
Mvvv 116,34,236
Dv 42,v1,36864
Mvvv 120,30,220
Mvvv 120,34,240
Mvvv 124,30,224
Mvvv 124,34,244
Mvvv 128,30,228
Mvvv 128,34,248
s_nop 0
E_ 116
Mvvv 132,30,232
E_ 117
Mvvv 132,34,252
W_ 0
E_ 118
Nvvv 76,38,136,56
Dv 30,%0,37888
Mvvv 76,42,156
Dv 34,v1,37888
Nvvv 80,38,140,60
E_ 119
Mvvv 80,42,160
C_ 119,118,119
C_ 118,116,117
Nvvv 84,38,144,64
E_ 120
Mvvv 84,42,164
E_ 121
Nvvv 88,38,148,68
E_ 122
Mvvv 88,42,168
E_ 123
Nvvv 92,38,152,72
C_ 123,122,123
C_ 122,120,121
Mvvv 92,42,172
W_ 0
E_ 124
Mvvv 92,30,192
Dv 38,%0,38912
Mvvv 92,34,212
Dv 42,v1,38912
Mvvv 88,30,188
E_ 125
Mvvv 88,34,208
E_ 126
Mvvv 84,30,184
S_ s65,s61,172032
E_ 127
Mvvv 84,34,204
S_ m0,s51,36864
C_ 127,126,127
C_ 126,124,125
Mvvv 80,30,180
s_waitcnt vmcnt(19) lgkmcnt(0)
s_barrier
Mvvv 80,34,200
L_ v9,36,s65,0
Mvvv 76,30,176
E_ 128
Mvvv 76,34,196
L_ v9,36,s65,3072
S_ m0,s51,37760
Mvvv 76,38,216
Tv 30,v2,49152
Tv 32,v2,55296
Mvvv 76,42,236
L_ v9,36,s65,128
Mvvv 80,38,220
s_nop 0
Tv 34,v3,49152
Tv 36,v3,55296
Mvvv 80,42,240
L_ v9,36,s65,3200
S_ m0,s51,38656
Mvvv 84,38,224
E_ 129
Mvvv 84,42,244
L_ v9,36,s65,256
Mvvv 88,38,228
E_ 130
Mvvv 88,42,248
L_ v9,36,s65,3328
Mvvv 92,38,232
E_ 131
Mvvv 92,42,252
W_ 0
C_ 131,130,131
C_ 130,128,129
Mavv 0,30,96
s_nop 0
Tv 38,v4,49152
Tv 40,v4,55296
Mavv 20,34,96
E_ 132
Mavv 24,34,100
s_nop 0
Tv 42,v5,49152
Tv 44,v5,55296
Mavv 4,30,100
E_ 133
Mavv 8,30,104
E_ 134
Mavv 28,34,104
E_ 135
Mavv 32,34,108
C_ 135,134,135
C_ 134,132,133
Mavv 12,30,108
E_ 76
Mavv 16,30,112
E_ 77
Mavv 36,34,112
W_ 0
E_ 78
Mavv 56,38,112
s_nop 0
Tv 30,v2,50176
Tv 32,v2,56320
Mavv 76,42,112
E_ 79
Mavv 72,42,108
s_nop 0
Tv 34,v3,50176
Tv 36,v3,56320
Mavv 52,38,108
C_ 116,76,77
C_ 117,78,79
Mavv 48,38,104
E_ 80
Mavv 68,42,104
E_ 81
Mavv 64,42,100
E_ 82
Mavv 44,38,100
E_ 83
Mavv 40,38,96
C_ 120,80,81
C_ 121,82,83
Mavv 60,42,96
W_ 0
E_ 84
Mavv 80,30,96
s_nop 0
Tv 38,v4,50176
Tv 40,v4,56320
Mavv 100,34,96
E_ 85
Mavv 104,34,100
s_nop 0
Tv 42,v5,50176
Tv 44,v5,56320
Mavv 84,30,100
E_ 86
Mavv 88,30,104
E_ 87
Mavv 108,34,104
C_ 124,84,85
C_ 125,86,87
Mavv 112,34,108
E_ 88
Mavv 92,30,108
E_ 89
Mavv 96,30,112
E_ 90
Mavv 116,34,112
W_ 0
E_ 91
Mavv 136,38,112
s_nop 0
Tv 30,v2,51200
Tv 32,v2,57344
Mavv 156,42,112
C_ 128,88,89
C_ 129,90,91
Mavv 152,42,108
s_nop 0
Tv 34,v3,51200
Tv 36,v3,57344
Mavv 132,38,108
E_ 92
Mavv 128,38,104
E_ 93
Mavv 148,42,104
E_ 94
Mavv 144,42,100
E_ 95
Mavv 124,38,100
C_ 132,92,93
C_ 133,94,95
Mavv 120,38,96
v_mov_b32 v92, 0x3f803f80
v_mov_b32 v93, 0x3f803f80
Mavv 140,42,96
W_ 0
v_mov_b32 v94, 0x3f803f80
v_mov_b32 v95, 0x3f803f80
Mavv 160,30,96
s_nop 0
Tv 38,v4,51200
Tv 40,v4,57344
Mavv 180,34,96
Mavv 184,34,100
s_nop 0
Tv 42,v5,51200
Tv 44,v5,57344
Mavv 164,30,100
Mavv 168,30,104
Mavv 188,34,104
S_ s65,s61,184320
Mavv 192,34,108
S_ m0,s51,49152
Mavv 172,30,108
s_waitcnt vmcnt(19) lgkmcnt(0)
s_barrier
Mavv 176,30,112
L_ v9,36,s65,0
Mavv 196,34,112
Mavv 216,38,112
Dv 30,%0,30720
Mavv 236,42,112
Dv 34,v1,30720
Mavv 232,42,108
L_ v9,36,s65,3072
S_ m0,s51,50048
Mavv 212,38,108
Mavv 208,38,104
L_ v9,36,s65,128
Mavv 228,42,104
Mavv 224,42,100
L_ v9,36,s65,3200
S_ m0,s51,50944
Mavv 204,38,100
Mavv 200,38,96
L_ v9,36,s65,256
Mavv 220,42,96
.Lstep0_14:
Zvvv 76,92,116
Dv 38,%0,31744
Zvvv 80,92,120
Dv 42,v1,31744
Zvvv 84,92,124
Zvvv 88,92,128
Zvvv 92,92,132
W_ 2
Nvvv 96,30,136,56
Mvvv 96,34,156
Nvvv 100,30,140,60
s_nop 1
v_max3_f32 v10, v76, v80, v84
s_nop 0
v_max3_f32 v10, v10, v88, v92
Mvvv 100,34,160
v_cmp_lt_f32 vcc, 0x42000000, v10
s_cbranch_vccnz .Ltramp0_13
Nvvv 104,30,144,64
A_ v46,v46,v76
A_ v47,v47,v80
Mvvv 104,34,164
A_ v48,v48,v84
A_ v49,v49,v88
Nvvv 108,30,148,68
A_ v50,v50,v92
Mvvv 108,34,168
L_ v9,36,s65,3328
Nvvv 112,30,152,72
R_ v20,a240
Mvvv 112,34,172
W_ 0
v_xor_b32 v21, 64, v20
Mvvv 112,38,192
S_ s65,s60,0
Dv 30,%0,32768
Mvvv 112,42,212
S_ m0,s50,0
Dv 34,v1,32768
Mvvv 108,38,188
L_ v20,24,s65,0
Mvvv 108,42,208
S_ s65,s60,10240
Mvvv 104,38,184
S_ m0,s50,1024
Mvvv 104,42,204
L_ v21,24,s65,0
Mvvv 100,38,180
S_ s65,s60,20480
Mvvv 100,42,200
S_ m0,s50,2048
Mvvv 96,38,176
L_ v20,24,s65,0
Mvvv 96,42,196
S_ s65,s60,30720
W_ 0
Mvvv 96,30,216
S_ m0,s50,3072
Dv 38,%0,24576
Mvvv 96,34,236
Dv 42,v1,24576
Mvvv 100,30,220
L_ v21,24,s65,0
Mvvv 100,34,240
S_ s65,s60,40960
Mvvv 104,30,224
S_ m0,s50,4096
Mvvv 104,34,244
L_ v20,24,s65,0
Mvvv 108,30,228
Mvvv 108,34,248
E_ 96
Mvvv 112,30,232
E_ 97
Mvvv 112,34,252
W_ 0
E_ 98
Nvvv 76,38,136,56
Dv 30,%0,25600
Mvvv 76,42,156
Dv 34,v1,25600
Nvvv 80,38,140,60
E_ 99
Mvvv 80,42,160
C_ 99,98,99
C_ 98,96,97
Nvvv 84,38,144,64
E_ 100
Mvvv 84,42,164
E_ 101
Nvvv 88,38,148,68
E_ 102
Mvvv 88,42,168
E_ 103
Nvvv 92,38,152,72
C_ 103,102,103
C_ 102,100,101
Mvvv 92,42,172
W_ 0
E_ 104
Mvvv 92,30,192
Dv 38,%0,26624
Mvvv 92,34,212
Dv 42,v1,26624
Mvvv 88,30,188
E_ 105
Mvvv 88,34,208
E_ 106
Mvvv 84,30,184
E_ 107
Mvvv 84,34,204
C_ 107,106,107
C_ 106,104,105
Mvvv 80,30,180
s_waitcnt vmcnt(23) lgkmcnt(0)
s_barrier
Mvvv 80,34,200
E_ 108
Mvvv 76,30,176
E_ 109
Mvvv 76,34,196
E_ 110
Mvvv 76,38,216
s_nop 0
Tv 30,v2,0
Tv 32,v2,6144
Mvvv 76,42,236
E_ 111
Mvvv 80,38,220
s_nop 0
Tv 34,v3,0
Tv 36,v3,6144
Mvvv 80,42,240
C_ 111,110,111
C_ 110,108,109
Mvvv 84,38,224
E_ 112
Mvvv 84,42,244
E_ 113
Mvvv 88,38,228
E_ 114
Mvvv 88,42,248
E_ 115
Mvvv 92,38,232
C_ 115,114,115
C_ 114,112,113
Mvvv 92,42,252
W_ 0
E_ 76
Mavv 0,30,116
s_nop 0
Tv 38,v4,0
Tv 40,v4,6144
Mavv 20,34,116
E_ 77
Mavv 24,34,120
s_nop 0
Tv 42,v5,0
Tv 44,v5,6144
Mavv 4,30,120
E_ 78
Mavv 8,30,124
E_ 79
Mavv 28,34,124
C_ 96,76,77
C_ 97,78,79
Mavv 32,34,128
E_ 80
Mavv 12,30,128
E_ 81
Mavv 16,30,132
E_ 82
Mavv 36,34,132
W_ 0
E_ 83
Mavv 56,38,132
s_nop 0
Tv 30,v2,1024
Tv 32,v2,7168
Mavv 76,42,132
C_ 100,80,81
C_ 101,82,83
Mavv 72,42,128
s_nop 0
Tv 34,v3,1024
Tv 36,v3,7168
Mavv 52,38,128
E_ 84
Mavv 48,38,124
E_ 85
Mavv 68,42,124
E_ 86
Mavv 64,42,120
E_ 87
Mavv 44,38,120
C_ 104,84,85
C_ 105,86,87
Mavv 40,38,116
E_ 88
Mavv 60,42,116
W_ 0
E_ 89
Mavv 80,30,116
s_nop 0
Tv 38,v4,1024
Tv 40,v4,7168
Mavv 100,34,116
E_ 90
Mavv 104,34,120
s_nop 0
Tv 42,v5,1024
Tv 44,v5,7168
Mavv 84,30,120
E_ 91
Mavv 88,30,124
C_ 108,88,89
C_ 109,90,91
Mavv 108,34,124
E_ 92
Mavv 112,34,128
E_ 93
Mavv 92,30,128
E_ 94
Mavv 96,30,132
E_ 95
Mavv 116,34,132
W_ 0
C_ 112,92,93
C_ 113,94,95
Mavv 136,38,132
s_nop 0
Tv 30,v2,2048
Tv 32,v2,8192
Mavv 156,42,132
v_mov_b32 v92, 0x3f803f80
v_mov_b32 v93, 0x3f803f80
Mavv 152,42,128
s_nop 0
Tv 34,v3,2048
Tv 36,v3,8192
Mavv 132,38,128
v_mov_b32 v94, 0x3f803f80
v_mov_b32 v95, 0x3f803f80
Mavv 128,38,124
Mavv 148,42,124
Mavv 144,42,120
Mavv 124,38,120
Mavv 120,38,116
Mavv 140,42,116
W_ 0
Mavv 160,30,116
s_nop 0
Tv 38,v4,2048
Tv 40,v4,8192
Mavv 180,34,116
Mavv 184,34,120
s_nop 0
Tv 42,v5,2048
Tv 44,v5,8192
Mavv 164,30,120
Mavv 168,30,124
Mavv 188,34,124
Mavv 192,34,128
Mavv 172,30,128
s_waitcnt vmcnt(17) lgkmcnt(0)
s_barrier
Mavv 176,30,132
S_ s65,s59,0
Mavv 196,34,132
S_ m0,s52,24320
Mavv 216,38,132
Dv 30,%0,18432
Mavv 236,42,132
Dv 34,v1,18432
Mavv 232,42,128
L_ v6,28,s65,256
Mavv 212,38,128
S_ s65,s59,98304
Mavv 208,38,124
S_ m0,s52,25344
Mavv 228,42,124
L_ v6,28,s65,256
Mavv 224,42,120
S_ s65,s59,196608
Mavv 204,38,120
S_ m0,s52,26368
Mavv 200,38,116
L_ v6,28,s65,256
Mavv 220,42,116
S_ s65,s59,294912
.Lstep0_15:
Zvvv 76,92,96
Dv 38,%0,19456
Zvvv 80,92,100
Dv 42,v1,19456
Zvvv 84,92,104
Zvvv 88,92,108
Zvvv 92,92,112
W_ 2
Nvvv 116,30,136,56
Mvvv 116,34,156
Nvvv 120,30,140,60
s_nop 1
v_max3_f32 v10, v76, v80, v84
s_nop 0
v_max3_f32 v10, v10, v88, v92
Mvvv 120,34,160
v_cmp_lt_f32 vcc, 0x42000000, v10
s_cbranch_vccnz .Ltramp0_14
Nvvv 124,30,144,64
S_ m0,s52,27392
A_ v46,v46,v76
A_ v47,v47,v80
Mvvv 124,34,164
A_ v48,v48,v84
A_ v49,v49,v88
Nvvv 128,30,148,68
A_ v50,v50,v92
Mvvv 128,34,168
L_ v6,28,s65,256
Nvvv 132,30,152,72
S_ s65,s59,786432
Mvvv 132,34,172
S_ m0,s52,0
W_ 0
Mvvv 132,38,192
Dv 30,%0,20480
Mvvv 132,42,212
Dv 34,v1,20480
Mvvv 128,38,188
L_ v6,28,s65,0
Mvvv 128,42,208
S_ s65,s59,884736
Mvvv 124,38,184
S_ m0,s52,1024
Mvvv 124,42,204
L_ v6,28,s65,0
Mvvv 120,38,180
S_ s65,s59,983040
Mvvv 120,42,200
S_ m0,s52,2048
Mvvv 116,38,176
L_ v6,28,s65,0
Mvvv 116,42,196
S_ s65,s59,1081344
W_ 0
Mvvv 116,30,216
S_ m0,s52,3072
Dv 38,%0,12288
Mvvv 116,34,236
Dv 42,v1,12288
Mvvv 120,30,220
L_ v6,28,s65,0
Mvvv 120,34,240
S_ s65,s60,128
Mvvv 124,30,224
S_ m0,s50,20480
Mvvv 124,34,244
L_ v20,24,s65,0
Mvvv 128,30,228
S_ s65,s60,10368
Mvvv 128,34,248
S_ m0,s50,21504
E_ 116
Mvvv 132,30,232
L_ v21,24,s65,0
Mvvv 132,34,252
S_ s65,s60,20608
E_ 117
W_ 1
Nvvv 76,38,136,56
S_ m0,s50,22528
Dv 30,%0,13312
W_ 1
Mvvv 76,42,156
Dv 34,v1,13312
Nvvv 80,38,140,60
L_ v20,24,s65,0
Mvvv 80,42,160
S_ s65,s60,30848
E_ 118
Nvvv 84,38,144,64
S_ m0,s50,23552
E_ 119
Mvvv 84,42,164
L_ v21,24,s65,0
Nvvv 88,38,148,68
S_ s65,s60,41088
C_ 119,118,119
C_ 118,116,117
Mvvv 88,42,168
S_ m0,s50,24576
E_ 120
Nvvv 92,38,152,72
L_ v20,24,s65,0
Mvvv 92,42,172
W_ 0
E_ 121
Mvvv 92,30,192
Dv 38,%0,14336
Mvvv 92,34,212
Dv 42,v1,14336
Mvvv 88,30,188
E_ 122
Mvvv 88,34,208
E_ 123
Mvvv 84,30,184
C_ 123,122,123
C_ 122,120,121
Mvvv 84,34,204
E_ 124
Mvvv 80,30,180
s_waitcnt vmcnt(24) lgkmcnt(0)
s_barrier
Mvvv 80,34,200
S_ s65,s59,0
E_ 125
Mvvv 76,30,176
S_ m0,s52,8192
E_ 126
Mvvv 76,34,196
L_ v6,28,s65,0
Mvvv 76,38,216
S_ s65,s59,98304
Tv 30,v2,36864
Tv 32,v2,43008
Mvvv 76,42,236
S_ m0,s52,9216
E_ 127
Mvvv 80,38,220
Tv 34,v3,36864
Tv 36,v3,43008
Mvvv 80,42,240
L_ v6,28,s65,0
Mvvv 84,38,224
S_ s65,s59,196608
C_ 127,126,127
C_ 126,124,125
Mvvv 84,42,244
S_ m0,s52,10240
E_ 128
Mvvv 88,38,228
L_ v6,28,s65,0
Mvvv 88,42,248
S_ s65,s59,294912
E_ 129
Mvvv 92,38,232
S_ m0,s52,11264
E_ 130
Mvvv 92,42,252
L_ v6,28,s65,0
W_ 2
Mavv 0,30,96
S_ s65,s59,0
Tv 38,v4,36864
Tv 40,v4,43008
W_ 2
Mavv 20,34,96
S_ m0,s52,16256
E_ 131
Mavv 24,34,100
Tv 42,v5,36864
Tv 44,v5,43008
Mavv 4,30,100
L_ v6,28,s65,128
Mavv 8,30,104
S_ s65,s59,98304
C_ 131,130,131
C_ 130,128,129
Mavv 28,34,104
S_ m0,s52,17280
E_ 132
Mavv 32,34,108
L_ v6,28,s65,128
Mavv 12,30,108
S_ s65,s59,196608
E_ 133
Mavv 16,30,112
S_ m0,s52,18304
E_ 134
Mavv 36,34,112
L_ v6,28,s65,128
W_ 2
Mavv 56,38,112
S_ s65,s59,294912
Tv 30,v2,37888
Tv 32,v2,44032
W_ 2
Mavv 76,42,112
S_ m0,s52,19328
E_ 135
Mavv 72,42,108
Tv 34,v3,37888
Tv 36,v3,44032
Mavv 52,38,108
L_ v6,28,s65,128
Mavv 48,38,104
C_ 135,134,135
C_ 134,132,133
Mavv 68,42,104
E_ 76
Mavv 64,42,100
E_ 77
Mavv 44,38,100
E_ 78
Mavv 40,38,96
E_ 79
Mavv 60,42,96
W_ 0
C_ 116,76,77
C_ 117,78,79
Mavv 80,30,96
s_nop 0
Tv 38,v4,37888
Tv 40,v4,44032
Mavv 100,34,96
E_ 80
Mavv 104,34,100
s_nop 0
Tv 42,v5,37888
Tv 44,v5,44032
Mavv 84,30,100
E_ 81
Mavv 88,30,104
E_ 82
Mavv 108,34,104
E_ 83
Mavv 112,34,108
C_ 120,80,81
C_ 121,82,83
Mavv 92,30,108
E_ 84
Mavv 96,30,112
E_ 85
Mavv 116,34,112
W_ 0
E_ 86
Mavv 136,38,112
s_nop 0
Tv 30,v2,38912
Tv 32,v2,45056
Mavv 156,42,112
E_ 87
Mavv 152,42,108
s_nop 0
Tv 34,v3,38912
Tv 36,v3,45056
Mavv 132,38,108
C_ 124,84,85
C_ 125,86,87
Mavv 128,38,104
E_ 88
Mavv 148,42,104
E_ 89
Mavv 144,42,100
E_ 90
Mavv 124,38,100
E_ 91
Mavv 120,38,96
C_ 128,88,89
C_ 129,90,91
Mavv 140,42,96
W_ 0
E_ 92
Mavv 160,30,96
s_nop 0
Tv 38,v4,38912
Tv 40,v4,45056
Mavv 180,34,96
E_ 93
Mavv 184,34,100
s_nop 0
Tv 42,v5,38912
Tv 44,v5,45056
Mavv 164,30,100
E_ 94
Mavv 168,30,104
E_ 95
Mavv 188,34,104
C_ 132,92,93
C_ 133,94,95
Mavv 192,34,108
v_mov_b32 v92, 0x3f803f80
v_mov_b32 v93, 0x3f803f80
Mavv 172,30,108
s_waitcnt vmcnt(26) lgkmcnt(0)
s_barrier
v_mov_b32 v94, 0x3f803f80
Mavv 176,30,112
v_mov_b32 v95, 0x3f803f80
Mavv 196,34,112
Mavv 216,38,112
s_nop 0
Tv 30,v2,49152
Tv 32,v2,55296
Zvvv 76,92,116
Zvvv 80,92,120
s_nop 0
Tv 34,v3,49152
Tv 36,v3,55296
Zvvv 84,92,124
Zvvv 88,92,128
Zvvv 92,92,132
Mavv 236,42,112
Mavv 232,42,108
Mavv 212,38,108
Mavv 208,38,104
s_nop 1
v_max3_f32 v10, v76, v80, v84
Mavv 228,42,104
v_max3_f32 v10, v10, v88, v92
v_cmp_lt_f32 vcc, 0x42000000, v10
Mavv 224,42,100
Mavv 204,38,100
Mavv 200,38,96
Mavv 220,42,96
s_cbranch_vccnz .Ltramp0_15
.Ljoin0:
W_ 2
Mavv 0,30,116
s_mov_b32 s58, 1
Tv 38,v4,49152
Tv 40,v4,55296
W_ 2
Mavv 20,34,116
S_ s61,s61,393216
R_ v12,a248
s_mov_b32 s85, 4026531840
Mavv 24,34,120
s_nop 0
Tv 42,v5,49152
Tv 44,v5,55296
Mavv 4,30,120
A_ v46,v46,v76
A_ v47,v47,v80
Mavv 8,30,124
A_ v48,v48,v84
A_ v49,v49,v88
Mavv 28,34,124
A_ v50,v50,v92
Mavv 32,34,128
v_rcp_f32 v46, v46
Mavv 12,30,128
v_rcp_f32 v47, v47
Mavv 16,30,132
v_rcp_f32 v48, v48
Mavv 36,34,132
W_ 0
v_rcp_f32 v49, v49
Mavv 56,38,132
s_nop 0
Tv 30,v2,50176
Tv 32,v2,56320
Mavv 76,42,132
v_rcp_f32 v50, v50
Mavv 72,42,128
s_nop 0
Tv 34,v3,50176
Tv 36,v3,56320
Mavv 52,38,128
R_ v96,a0
R_ v97,a1
Mavv 48,38,124
R_ v98,a2
R_ v99,a3
Mavv 68,42,124
R_ v100,a20
R_ v101,a21
Mavv 64,42,120
R_ v102,a22
R_ v103,a23
Mavv 44,38,120
U_ v96,v96,v46
U_ v97,v97,v46
Mavv 40,38,116
U_ v98,v98,v46
U_ v99,v99,v46
Mavv 60,42,116
W_ 0
U_ v100,v100,v46
U_ v101,v101,v46
Mavv 80,30,116
s_nop 0
Tv 38,v4,50176
Tv 40,v4,56320
Mavv 100,34,116
U_ v102,v102,v46
U_ v103,v103,v46
Mavv 104,34,120
s_nop 0
Tv 42,v5,50176
Tv 44,v5,56320
Mavv 84,30,120
C_ 76,96,97
C_ 77,98,99
Mavv 88,30,124
C_ 78,100,101
C_ 79,102,103
Mavv 108,34,124
X_ a0,v76
X_ a1,v77
Mavv 112,34,128
X_ a2,v78
X_ a3,v79
Mavv 92,30,128
R_ v104,a4
R_ v105,a5
Mavv 96,30,132
R_ v106,a6
R_ v107,a7
Mavv 116,34,132
W_ 0
R_ v108,a24
R_ v109,a25
Mavv 136,38,132
s_nop 0
Tv 30,v2,51200
Tv 32,v2,57344
Mavv 156,42,132
R_ v110,a26
R_ v111,a27
Mavv 152,42,128
s_nop 0
Tv 34,v3,51200
Tv 36,v3,57344
Mavv 132,38,128
U_ v104,v104,v47
U_ v105,v105,v47
Mavv 128,38,124
U_ v106,v106,v47
U_ v107,v107,v47
Mavv 148,42,124
U_ v108,v108,v47
U_ v109,v109,v47
Mavv 144,42,120
U_ v110,v110,v47
U_ v111,v111,v47
Mavv 124,38,120
C_ 80,104,105
C_ 81,106,107
Mavv 120,38,116
C_ 82,108,109
C_ 83,110,111
Mavv 140,42,116
W_ 0
X_ a4,v80
X_ a5,v81
Mavv 160,30,116
s_nop 0
Tv 38,v4,51200
Tv 40,v4,57344
Mavv 180,34,116
X_ a6,v82
X_ a7,v83
Mavv 184,34,120
s_nop 0
Tv 42,v5,51200
Tv 44,v5,57344
Mavv 164,30,120
R_ v96,a8
R_ v97,a9
Mavv 168,30,124
R_ v98,a10
R_ v99,a11
Mavv 188,34,124
R_ v100,a28
R_ v101,a29
Mavv 192,34,128
R_ v102,a30
R_ v103,a31
Mavv 172,30,128
U_ v96,v96,v48
U_ v97,v97,v48
Mavv 176,30,132
U_ v98,v98,v48
U_ v99,v99,v48
Mavv 196,34,132
W_ 0
U_ v100,v100,v48
U_ v101,v101,v48
Mavv 216,38,132
U_ v102,v102,v48
U_ v103,v103,v48
Mavv 236,42,132
C_ 84,96,97
C_ 85,98,99
Mavv 232,42,128
C_ 86,100,101
C_ 87,102,103
Mavv 212,38,128
X_ a8,v84
X_ a9,v85
Mavv 208,38,124
X_ a10,v86
X_ a11,v87
Mavv 228,42,124
Mavv 224,42,120
Mavv 204,38,120
Mavv 200,38,116
Mavv 220,42,116
.p2align 3
R_ v104,a12
R_ v105,a13
R_ v106,a14
R_ v107,a15
R_ v108,a32
R_ v109,a33
R_ v110,a34
R_ v111,a35
v_pk_mul_f32 v[104:105], v[104:105], v[48:49] op_sel:[0,1]
v_pk_mul_f32 v[106:107], v[106:107], v[48:49] op_sel:[0,1]
v_pk_mul_f32 v[108:109], v[108:109], v[48:49] op_sel:[0,1]
v_pk_mul_f32 v[110:111], v[110:111], v[48:49] op_sel:[0,1]
C_ 88,104,105
C_ 89,106,107
C_ 90,108,109
C_ 91,110,111
X_ a12,v88
X_ a13,v89
X_ a14,v90
X_ a15,v91
.p2align 3
R_ v96,a16
R_ v97,a17
R_ v98,a18
R_ v99,a19
R_ v100,a36
R_ v101,a37
R_ v102,a38
R_ v103,a39
v_pk_mul_f32 v[96:97], v[96:97], v[50:51] op_sel_hi:[1,0]
v_pk_mul_f32 v[98:99], v[98:99], v[50:51] op_sel_hi:[1,0]
v_pk_mul_f32 v[100:101], v[100:101], v[50:51] op_sel_hi:[1,0]
v_pk_mul_f32 v[102:103], v[102:103], v[50:51] op_sel_hi:[1,0]
C_ 92,96,97
C_ 93,98,99
C_ 94,100,101
C_ 95,102,103
X_ a16,v92
X_ a17,v93
X_ a18,v94
X_ a19,v95
.p2align 3
R_ v104,a40
R_ v105,a41
R_ v106,a42
R_ v107,a43
R_ v108,a60
R_ v109,a61
R_ v110,a62
R_ v111,a63
v_pk_mul_f32 v[104:105], v[104:105], v[46:47] op_sel_hi:[1,0]
v_pk_mul_f32 v[106:107], v[106:107], v[46:47] op_sel_hi:[1,0]
v_pk_mul_f32 v[108:109], v[108:109], v[46:47] op_sel_hi:[1,0]
v_pk_mul_f32 v[110:111], v[110:111], v[46:47] op_sel_hi:[1,0]
C_ 76,104,105
C_ 77,106,107
C_ 78,108,109
C_ 79,110,111
X_ a40,v76
X_ a41,v77
X_ a42,v78
X_ a43,v79
.p2align 3
R_ v96,a44
R_ v97,a45
R_ v98,a46
R_ v99,a47
R_ v100,a64
R_ v101,a65
R_ v102,a66
R_ v103,a67
v_pk_mul_f32 v[96:97], v[96:97], v[46:47] op_sel:[0,1]
v_pk_mul_f32 v[98:99], v[98:99], v[46:47] op_sel:[0,1]
v_pk_mul_f32 v[100:101], v[100:101], v[46:47] op_sel:[0,1]
v_pk_mul_f32 v[102:103], v[102:103], v[46:47] op_sel:[0,1]
C_ 80,96,97
C_ 81,98,99
C_ 82,100,101
C_ 83,102,103
X_ a44,v80
X_ a45,v81
X_ a46,v82
X_ a47,v83
.p2align 3
R_ v104,a48
R_ v105,a49
R_ v106,a50
R_ v107,a51
R_ v108,a68
R_ v109,a69
R_ v110,a70
R_ v111,a71
v_pk_mul_f32 v[104:105], v[104:105], v[48:49] op_sel_hi:[1,0]
v_pk_mul_f32 v[106:107], v[106:107], v[48:49] op_sel_hi:[1,0]
v_pk_mul_f32 v[108:109], v[108:109], v[48:49] op_sel_hi:[1,0]
v_pk_mul_f32 v[110:111], v[110:111], v[48:49] op_sel_hi:[1,0]
C_ 84,104,105
C_ 85,106,107
C_ 86,108,109
C_ 87,110,111
X_ a48,v84
X_ a49,v85
X_ a50,v86
X_ a51,v87
.p2align 3
R_ v96,a52
R_ v97,a53
R_ v98,a54
R_ v99,a55
R_ v100,a72
R_ v101,a73
R_ v102,a74
R_ v103,a75
v_pk_mul_f32 v[96:97], v[96:97], v[48:49] op_sel:[0,1]
v_pk_mul_f32 v[98:99], v[98:99], v[48:49] op_sel:[0,1]
v_pk_mul_f32 v[100:101], v[100:101], v[48:49] op_sel:[0,1]
v_pk_mul_f32 v[102:103], v[102:103], v[48:49] op_sel:[0,1]
C_ 88,96,97
C_ 89,98,99
C_ 90,100,101
C_ 91,102,103
X_ a52,v88
X_ a53,v89
X_ a54,v90
X_ a55,v91
.p2align 3
R_ v104,a56
R_ v105,a57
R_ v106,a58
R_ v107,a59
R_ v108,a76
R_ v109,a77
R_ v110,a78
R_ v111,a79
v_pk_mul_f32 v[104:105], v[104:105], v[50:51] op_sel_hi:[1,0]
v_pk_mul_f32 v[106:107], v[106:107], v[50:51] op_sel_hi:[1,0]
v_pk_mul_f32 v[108:109], v[108:109], v[50:51] op_sel_hi:[1,0]
v_pk_mul_f32 v[110:111], v[110:111], v[50:51] op_sel_hi:[1,0]
C_ 92,104,105
C_ 93,106,107
C_ 94,108,109
C_ 95,110,111
X_ a56,v92
X_ a57,v93
X_ a58,v94
X_ a59,v95
.p2align 3
R_ v96,a80
R_ v97,a81
R_ v98,a82
R_ v99,a83
R_ v100,a100
R_ v101,a101
R_ v102,a102
R_ v103,a103
v_pk_mul_f32 v[96:97], v[96:97], v[46:47] op_sel_hi:[1,0]
v_pk_mul_f32 v[98:99], v[98:99], v[46:47] op_sel_hi:[1,0]
v_pk_mul_f32 v[100:101], v[100:101], v[46:47] op_sel_hi:[1,0]
v_pk_mul_f32 v[102:103], v[102:103], v[46:47] op_sel_hi:[1,0]
C_ 76,96,97
C_ 77,98,99
C_ 78,100,101
C_ 79,102,103
X_ a80,v76
X_ a81,v77
X_ a82,v78
X_ a83,v79
.p2align 3
R_ v104,a84
R_ v105,a85
R_ v106,a86
R_ v107,a87
R_ v108,a104
R_ v109,a105
R_ v110,a106
R_ v111,a107
v_pk_mul_f32 v[104:105], v[104:105], v[46:47] op_sel:[0,1]
v_pk_mul_f32 v[106:107], v[106:107], v[46:47] op_sel:[0,1]
v_pk_mul_f32 v[108:109], v[108:109], v[46:47] op_sel:[0,1]
v_pk_mul_f32 v[110:111], v[110:111], v[46:47] op_sel:[0,1]
C_ 80,104,105
C_ 81,106,107
C_ 82,108,109
C_ 83,110,111
X_ a84,v80
X_ a85,v81
X_ a86,v82
X_ a87,v83
.p2align 3
R_ v96,a88
R_ v97,a89
R_ v98,a90
R_ v99,a91
R_ v100,a108
R_ v101,a109
R_ v102,a110
R_ v103,a111
v_pk_mul_f32 v[96:97], v[96:97], v[48:49] op_sel_hi:[1,0]
v_pk_mul_f32 v[98:99], v[98:99], v[48:49] op_sel_hi:[1,0]
v_pk_mul_f32 v[100:101], v[100:101], v[48:49] op_sel_hi:[1,0]
v_pk_mul_f32 v[102:103], v[102:103], v[48:49] op_sel_hi:[1,0]
C_ 84,96,97
C_ 85,98,99
C_ 86,100,101
C_ 87,102,103
X_ a88,v84
X_ a89,v85
X_ a90,v86
X_ a91,v87
.p2align 3
R_ v104,a92
R_ v105,a93
R_ v106,a94
R_ v107,a95
R_ v108,a112
R_ v109,a113
R_ v110,a114
R_ v111,a115
v_pk_mul_f32 v[104:105], v[104:105], v[48:49] op_sel:[0,1]
v_pk_mul_f32 v[106:107], v[106:107], v[48:49] op_sel:[0,1]
v_pk_mul_f32 v[108:109], v[108:109], v[48:49] op_sel:[0,1]
v_pk_mul_f32 v[110:111], v[110:111], v[48:49] op_sel:[0,1]
C_ 88,104,105
C_ 89,106,107
C_ 90,108,109
C_ 91,110,111
X_ a92,v88
X_ a93,v89
X_ a94,v90
X_ a95,v91
.p2align 3
R_ v96,a96
R_ v97,a97
R_ v98,a98
R_ v99,a99
R_ v100,a116
R_ v101,a117
R_ v102,a118
R_ v103,a119
v_pk_mul_f32 v[96:97], v[96:97], v[50:51] op_sel_hi:[1,0]
v_pk_mul_f32 v[98:99], v[98:99], v[50:51] op_sel_hi:[1,0]
v_pk_mul_f32 v[100:101], v[100:101], v[50:51] op_sel_hi:[1,0]
v_pk_mul_f32 v[102:103], v[102:103], v[50:51] op_sel_hi:[1,0]
C_ 92,96,97
C_ 93,98,99
C_ 94,100,101
C_ 95,102,103
X_ a96,v92
X_ a97,v93
X_ a98,v94
X_ a99,v95
.p2align 3
R_ v104,a120
R_ v105,a121
R_ v106,a122
R_ v107,a123
R_ v108,a140
R_ v109,a141
R_ v110,a142
R_ v111,a143
v_pk_mul_f32 v[104:105], v[104:105], v[46:47] op_sel_hi:[1,0]
v_pk_mul_f32 v[106:107], v[106:107], v[46:47] op_sel_hi:[1,0]
v_pk_mul_f32 v[108:109], v[108:109], v[46:47] op_sel_hi:[1,0]
v_pk_mul_f32 v[110:111], v[110:111], v[46:47] op_sel_hi:[1,0]
C_ 76,104,105
C_ 77,106,107
C_ 78,108,109
C_ 79,110,111
X_ a120,v76
X_ a121,v77
X_ a122,v78
X_ a123,v79
.p2align 3
R_ v96,a124
R_ v97,a125
R_ v98,a126
R_ v99,a127
R_ v100,a144
R_ v101,a145
R_ v102,a146
R_ v103,a147
v_pk_mul_f32 v[96:97], v[96:97], v[46:47] op_sel:[0,1]
v_pk_mul_f32 v[98:99], v[98:99], v[46:47] op_sel:[0,1]
v_pk_mul_f32 v[100:101], v[100:101], v[46:47] op_sel:[0,1]
v_pk_mul_f32 v[102:103], v[102:103], v[46:47] op_sel:[0,1]
C_ 80,96,97
C_ 81,98,99
C_ 82,100,101
C_ 83,102,103
X_ a124,v80
X_ a125,v81
X_ a126,v82
X_ a127,v83
.p2align 3
R_ v104,a128
R_ v105,a129
R_ v106,a130
R_ v107,a131
R_ v108,a148
R_ v109,a149
R_ v110,a150
R_ v111,a151
v_pk_mul_f32 v[104:105], v[104:105], v[48:49] op_sel_hi:[1,0]
v_pk_mul_f32 v[106:107], v[106:107], v[48:49] op_sel_hi:[1,0]
v_pk_mul_f32 v[108:109], v[108:109], v[48:49] op_sel_hi:[1,0]
v_pk_mul_f32 v[110:111], v[110:111], v[48:49] op_sel_hi:[1,0]
C_ 84,104,105
C_ 85,106,107
C_ 86,108,109
C_ 87,110,111
X_ a128,v84
X_ a129,v85
X_ a130,v86
X_ a131,v87
.p2align 3
R_ v96,a132
R_ v97,a133
R_ v98,a134
R_ v99,a135
R_ v100,a152
R_ v101,a153
R_ v102,a154
R_ v103,a155
v_pk_mul_f32 v[96:97], v[96:97], v[48:49] op_sel:[0,1]
v_pk_mul_f32 v[98:99], v[98:99], v[48:49] op_sel:[0,1]
v_pk_mul_f32 v[100:101], v[100:101], v[48:49] op_sel:[0,1]
v_pk_mul_f32 v[102:103], v[102:103], v[48:49] op_sel:[0,1]
C_ 88,96,97
C_ 89,98,99
C_ 90,100,101
C_ 91,102,103
X_ a132,v88
X_ a133,v89
X_ a134,v90
X_ a135,v91
.p2align 3
R_ v104,a136
R_ v105,a137
R_ v106,a138
R_ v107,a139
R_ v108,a156
R_ v109,a157
R_ v110,a158
R_ v111,a159
v_pk_mul_f32 v[104:105], v[104:105], v[50:51] op_sel_hi:[1,0]
v_pk_mul_f32 v[106:107], v[106:107], v[50:51] op_sel_hi:[1,0]
v_pk_mul_f32 v[108:109], v[108:109], v[50:51] op_sel_hi:[1,0]
v_pk_mul_f32 v[110:111], v[110:111], v[50:51] op_sel_hi:[1,0]
C_ 92,104,105
C_ 93,106,107
C_ 94,108,109
C_ 95,110,111
X_ a136,v92
X_ a137,v93
X_ a138,v94
X_ a139,v95
.p2align 3
R_ v96,a160
R_ v97,a161
R_ v98,a162
R_ v99,a163
R_ v100,a180
R_ v101,a181
R_ v102,a182
R_ v103,a183
v_pk_mul_f32 v[96:97], v[96:97], v[46:47] op_sel_hi:[1,0]
v_pk_mul_f32 v[98:99], v[98:99], v[46:47] op_sel_hi:[1,0]
v_pk_mul_f32 v[100:101], v[100:101], v[46:47] op_sel_hi:[1,0]
v_pk_mul_f32 v[102:103], v[102:103], v[46:47] op_sel_hi:[1,0]
C_ 76,96,97
C_ 77,98,99
C_ 78,100,101
C_ 79,102,103
X_ a160,v76
X_ a161,v77
X_ a162,v78
X_ a163,v79
.p2align 3
R_ v104,a164
R_ v105,a165
R_ v106,a166
R_ v107,a167
R_ v108,a184
R_ v109,a185
R_ v110,a186
R_ v111,a187
v_pk_mul_f32 v[104:105], v[104:105], v[46:47] op_sel:[0,1]
v_pk_mul_f32 v[106:107], v[106:107], v[46:47] op_sel:[0,1]
v_pk_mul_f32 v[108:109], v[108:109], v[46:47] op_sel:[0,1]
v_pk_mul_f32 v[110:111], v[110:111], v[46:47] op_sel:[0,1]
C_ 80,104,105
C_ 81,106,107
C_ 82,108,109
C_ 83,110,111
X_ a164,v80
X_ a165,v81
X_ a166,v82
X_ a167,v83
.p2align 3
R_ v96,a168
R_ v97,a169
R_ v98,a170
R_ v99,a171
R_ v100,a188
R_ v101,a189
R_ v102,a190
R_ v103,a191
v_pk_mul_f32 v[96:97], v[96:97], v[48:49] op_sel_hi:[1,0]
v_pk_mul_f32 v[98:99], v[98:99], v[48:49] op_sel_hi:[1,0]
v_pk_mul_f32 v[100:101], v[100:101], v[48:49] op_sel_hi:[1,0]
v_pk_mul_f32 v[102:103], v[102:103], v[48:49] op_sel_hi:[1,0]
C_ 84,96,97
C_ 85,98,99
C_ 86,100,101
C_ 87,102,103
X_ a168,v84
X_ a169,v85
X_ a170,v86
X_ a171,v87
.p2align 3
R_ v104,a172
R_ v105,a173
R_ v106,a174
R_ v107,a175
R_ v108,a192
R_ v109,a193
R_ v110,a194
R_ v111,a195
v_pk_mul_f32 v[104:105], v[104:105], v[48:49] op_sel:[0,1]
v_pk_mul_f32 v[106:107], v[106:107], v[48:49] op_sel:[0,1]
v_pk_mul_f32 v[108:109], v[108:109], v[48:49] op_sel:[0,1]
v_pk_mul_f32 v[110:111], v[110:111], v[48:49] op_sel:[0,1]
C_ 88,104,105
C_ 89,106,107
C_ 90,108,109
C_ 91,110,111
X_ a172,v88
X_ a173,v89
X_ a174,v90
X_ a175,v91
.p2align 3
R_ v96,a176
R_ v97,a177
R_ v98,a178
R_ v99,a179
R_ v100,a196
R_ v101,a197
R_ v102,a198
R_ v103,a199
v_pk_mul_f32 v[96:97], v[96:97], v[50:51] op_sel_hi:[1,0]
v_pk_mul_f32 v[98:99], v[98:99], v[50:51] op_sel_hi:[1,0]
v_pk_mul_f32 v[100:101], v[100:101], v[50:51] op_sel_hi:[1,0]
v_pk_mul_f32 v[102:103], v[102:103], v[50:51] op_sel_hi:[1,0]
C_ 92,96,97
C_ 93,98,99
C_ 94,100,101
C_ 95,102,103
X_ a176,v92
X_ a177,v93
X_ a178,v94
X_ a179,v95
.p2align 3
R_ v104,a200
R_ v105,a201
R_ v106,a202
R_ v107,a203
R_ v108,a220
R_ v109,a221
R_ v110,a222
R_ v111,a223
v_pk_mul_f32 v[104:105], v[104:105], v[46:47] op_sel_hi:[1,0]
v_pk_mul_f32 v[106:107], v[106:107], v[46:47] op_sel_hi:[1,0]
v_pk_mul_f32 v[108:109], v[108:109], v[46:47] op_sel_hi:[1,0]
v_pk_mul_f32 v[110:111], v[110:111], v[46:47] op_sel_hi:[1,0]
C_ 76,104,105
C_ 77,106,107
C_ 78,108,109
C_ 79,110,111
X_ a200,v76
X_ a201,v77
X_ a202,v78
X_ a203,v79
.p2align 3
R_ v96,a204
R_ v97,a205
R_ v98,a206
R_ v99,a207
R_ v100,a224
R_ v101,a225
R_ v102,a226
R_ v103,a227
v_pk_mul_f32 v[96:97], v[96:97], v[46:47] op_sel:[0,1]
v_pk_mul_f32 v[98:99], v[98:99], v[46:47] op_sel:[0,1]
v_pk_mul_f32 v[100:101], v[100:101], v[46:47] op_sel:[0,1]
v_pk_mul_f32 v[102:103], v[102:103], v[46:47] op_sel:[0,1]
C_ 80,96,97
C_ 81,98,99
C_ 82,100,101
C_ 83,102,103
X_ a204,v80
X_ a205,v81
X_ a206,v82
X_ a207,v83
v_mov_b32 v4, v20
v_mov_b32 v5, v21
R_ %0,a242
R_ v2,a243
v_xor_b32 v1, 64, %0
v_xor_b32 v3, 64, v2
s_waitcnt vmcnt(0) lgkmcnt(0)
s_barrier
Ta 20,%0,8192
Ta 22,%0,8704
Ta 24,%0,12288
Ta 26,%0,12800
Da 60,v2,0
Da 100,v3,0
Da 64,v2,2048
Da 104,v3,2048
Da 68,v2,4096
Da 108,v3,4096
Da 72,v2,6144
Da 112,v3,6144
Da 76,v2,8192
Da 116,v3,8192
Ta 28,%0,8200
Ta 30,%0,8712
Ta 32,%0,12296
Ta 34,%0,12808
.p2align 3
R_ v104,a208
R_ v105,a209
R_ v106,a210
R_ v107,a211
R_ v108,a228
R_ v109,a229
R_ v110,a230
R_ v111,a231
v_pk_mul_f32 v[104:105], v[104:105], v[48:49] op_sel_hi:[1,0]
v_pk_mul_f32 v[106:107], v[106:107], v[48:49] op_sel_hi:[1,0]
v_pk_mul_f32 v[108:109], v[108:109], v[48:49] op_sel_hi:[1,0]
v_pk_mul_f32 v[110:111], v[110:111], v[48:49] op_sel_hi:[1,0]
C_ 84,104,105
C_ 85,106,107
C_ 86,108,109
C_ 87,110,111
X_ a208,v84
X_ a209,v85
X_ a210,v86
X_ a211,v87
.p2align 3
R_ v96,a212
R_ v97,a213
R_ v98,a214
R_ v99,a215
R_ v100,a232
R_ v101,a233
R_ v102,a234
R_ v103,a235
v_pk_mul_f32 v[96:97], v[96:97], v[48:49] op_sel:[0,1]
v_pk_mul_f32 v[98:99], v[98:99], v[48:49] op_sel:[0,1]
v_pk_mul_f32 v[100:101], v[100:101], v[48:49] op_sel:[0,1]
v_pk_mul_f32 v[102:103], v[102:103], v[48:49] op_sel:[0,1]
C_ 88,96,97
C_ 89,98,99
C_ 90,100,101
C_ 91,102,103
X_ a212,v88
X_ a213,v89
X_ a214,v90
X_ a215,v91
.p2align 3
R_ v104,a216
R_ v105,a217
R_ v106,a218
R_ v107,a219
R_ v108,a236
R_ v109,a237
R_ v110,a238
R_ v111,a239
v_pk_mul_f32 v[104:105], v[104:105], v[50:51] op_sel_hi:[1,0]
v_pk_mul_f32 v[106:107], v[106:107], v[50:51] op_sel_hi:[1,0]
v_pk_mul_f32 v[108:109], v[108:109], v[50:51] op_sel_hi:[1,0]
v_pk_mul_f32 v[110:111], v[110:111], v[50:51] op_sel_hi:[1,0]
C_ 92,104,105
C_ 93,106,107
C_ 94,108,109
C_ 95,110,111
X_ a216,v92
X_ a217,v93
X_ a218,v94
X_ a219,v95
s_branch .Lq
.Ltramp0_1:
s_mov_b32 s63, 1
s_branch .Lslow1
.Ltramp0_2:
s_mov_b32 s63, 2
s_branch .Lslow0
.Ltramp0_3:
s_mov_b32 s63, 3
s_branch .Lslow1
.Ltramp0_4:
s_mov_b32 s63, 4
s_branch .Lslow0
.Ltramp0_5:
s_mov_b32 s63, 5
s_branch .Lslow1
.Ltramp0_6:
s_mov_b32 s63, 6
s_branch .Lslow0
.Ltramp0_7:
s_mov_b32 s63, 7
s_branch .Lslow1
.Ltramp0_8:
s_mov_b32 s63, 8
s_branch .Lslow0
.Ltramp0_9:
s_mov_b32 s63, 9
s_branch .Lslow1
.Ltramp0_10:
s_mov_b32 s63, 10
s_branch .Lslow0
.Ltramp0_11:
s_mov_b32 s63, 11
s_branch .Lslow1
.Ltramp0_12:
s_mov_b32 s63, 12
s_branch .Lslow0
.Ltramp0_13:
s_mov_b32 s63, 13
s_branch .Lslow1
.Ltramp0_14:
s_mov_b32 s63, 14
s_branch .Lslow0
.Ltramp0_15:
s_mov_b32 s63, 15
s_branch .Lslow1
.Lstep0_16:
Zvvv 76,92,116
Zvvv 80,92,120
Zvvv 84,92,124
Zvvv 88,92,128
Zvvv 92,92,132
s_nop 5
v_max3_f32 v10, v76, v80, v84
s_nop 0
v_max3_f32 v10, v10, v88, v92
v_cmp_lt_f32 vcc, 0x42000000, v10
s_cbranch_vccnz .Ltramp0_15
s_branch .Ljoin0
.Ltramp1_1:
s_mov_b32 s63, 1
s_branch .Lslow1
.Ltramp1_2:
s_mov_b32 s63, 2
s_branch .Lslow0
.Ltramp1_3:
s_mov_b32 s63, 3
s_branch .Lslow1
.Ltramp1_4:
s_mov_b32 s63, 4
s_branch .Lslow0
.Ltramp1_5:
s_mov_b32 s63, 5
s_branch .Lslow1
.Ltramp1_6:
s_mov_b32 s63, 6
s_branch .Lslow0
.Ltramp1_7:
s_mov_b32 s63, 7
s_branch .Lslow1
.Ltramp1_8:
s_mov_b32 s63, 8
s_branch .Lslow0
.Ltramp1_9:
s_mov_b32 s63, 9
s_branch .Lslow1
.Ltramp1_10:
s_mov_b32 s63, 10
s_branch .Lslow0
.Ltramp1_11:
s_mov_b32 s63, 11
s_branch .Lslow1
.Ltramp1_12:
s_mov_b32 s63, 12
s_branch .Lslow0
.Ltramp1_13:
s_mov_b32 s63, 13
s_branch .Lslow1
.Ltramp1_14:
s_mov_b32 s63, 14
s_branch .Lslow0
.Ltramp1_15:
s_mov_b32 s63, 15
s_branch .Lslow1
.Lstep1_16:
Zvvv 76,92,116
Zvvv 80,92,120
Zvvv 84,92,124
Zvvv 88,92,128
Zvvv 92,92,132
s_nop 5
v_max3_f32 v10, v76, v80, v84
s_nop 0
v_max3_f32 v10, v10, v88, v92
v_cmp_lt_f32 vcc, 0x42000000, v10
s_cbranch_vccnz .Ltramp1_15
s_branch .Ljoin1
.Ltramp2_1:
s_mov_b32 s63, 1
s_branch .Lslow1
.Ltramp2_2:
s_mov_b32 s63, 2
s_branch .Lslow0
.Ltramp2_3:
s_mov_b32 s63, 3
s_branch .Lslow1
.Ltramp2_4:
s_mov_b32 s63, 4
s_branch .Lslow0
.Ltramp2_5:
s_mov_b32 s63, 5
s_branch .Lslow1
.Ltramp2_6:
s_mov_b32 s63, 6
s_branch .Lslow0
.Ltramp2_7:
s_mov_b32 s63, 7
s_branch .Lslow1
.Ltramp2_8:
s_mov_b32 s63, 8
s_branch .Lslow0
.Ltramp2_9:
s_mov_b32 s63, 9
s_branch .Lslow1
.Ltramp2_10:
s_mov_b32 s63, 10
s_branch .Lslow0
.Ltramp2_11:
s_mov_b32 s63, 11
s_branch .Lslow1
.Ltramp2_12:
s_mov_b32 s63, 12
s_branch .Lslow0
.Ltramp2_13:
s_mov_b32 s63, 13
s_branch .Lslow1
.Ltramp2_14:
s_mov_b32 s63, 14
s_branch .Lslow0
.Ltramp2_15:
s_mov_b32 s63, 15
s_branch .Lslow1
.Lstep2_16:
Zvvv 76,92,116
Zvvv 80,92,120
Zvvv 84,92,124
Zvvv 88,92,128
Zvvv 92,92,132
s_nop 5
v_max3_f32 v10, v76, v80, v84
s_nop 0
v_max3_f32 v10, v10, v88, v92
v_cmp_lt_f32 vcc, 0x42000000, v10
s_cbranch_vccnz .Ltramp2_15
s_branch .Ljoin2
.Lslow0:
W_ 0
s_nop 7
s_nop 7
s_and_b32 s64, s61, 4294959104
s_mul_i32 s53, s63, 12288
S_ s64,s64,s53
R_ v11,a248
v_and_b32 v10, 0xf0, v11
v_mul_u32_u24 v10, 24, v10
v_lshrrev_b32 v11, 8, v11
v_lshl_add_u32 v10, v11, 4, v10
S_ s53,s64,6144
buffer_load_dwordx4 v[116:119], v10, s[32:35], s53 offen offset:0
S_ s53,s64,6144
buffer_load_dwordx4 v[120:123], v10, s[32:35], s53 offen offset:64
S_ s53,s64,6144
buffer_load_dwordx4 v[124:127], v10, s[32:35], s53 offen offset:128
S_ s53,s64,6144
buffer_load_dwordx4 v[128:131], v10, s[32:35], s53 offen offset:192
)A"
R"A(s_waitcnt vmcnt(0)
Nvvv 96,116,136,56
Nvvv 100,116,140,60
Nvvv 104,116,144,64
Nvvv 108,116,148,68
Nvvv 112,116,152,72
Mvvv 96,120,156
Mvvv 100,120,160
Mvvv 104,120,164
Mvvv 108,120,168
Mvvv 112,120,172
Mvvv 96,124,176
Mvvv 100,124,180
Mvvv 104,124,184
Mvvv 108,124,188
Mvvv 112,124,192
Mvvv 96,128,196
Mvvv 100,128,200
Mvvv 104,128,204
Mvvv 108,128,208
Mvvv 112,128,212
s_nop 7
S_ s53,s64,6144
buffer_load_dwordx4 v[116:119], v10, s[32:35], s53 offen offset:256
S_ s53,s64,6144
buffer_load_dwordx4 v[120:123], v10, s[32:35], s53 offen offset:320
S_ s53,s64,0
buffer_load_dwordx4 v[124:127], v10, s[32:35], s53 offen offset:0
S_ s53,s64,0
buffer_load_dwordx4 v[128:131], v10, s[32:35], s53 offen offset:64
s_waitcnt vmcnt(0)
Mvvv 96,116,216
Mvvv 100,116,220
Mvvv 104,116,224
Mvvv 108,116,228
Mvvv 112,116,232
Mvvv 96,120,236
Mvvv 100,120,240
Mvvv 104,120,244
Mvvv 108,120,248
Mvvv 112,120,252
Nvvv 76,124,136,56
Nvvv 80,124,140,60
Nvvv 84,124,144,64
Nvvv 88,124,148,68
Nvvv 92,124,152,72
Mvvv 76,128,156
Mvvv 80,128,160
Mvvv 84,128,164
Mvvv 88,128,168
Mvvv 92,128,172
s_nop 7
S_ s53,s64,0
buffer_load_dwordx4 v[116:119], v10, s[32:35], s53 offen offset:128
S_ s53,s64,0
buffer_load_dwordx4 v[120:123], v10, s[32:35], s53 offen offset:192
S_ s53,s64,0
buffer_load_dwordx4 v[124:127], v10, s[32:35], s53 offen offset:256
S_ s53,s64,0
buffer_load_dwordx4 v[128:131], v10, s[32:35], s53 offen offset:320
s_waitcnt vmcnt(0)
Mvvv 76,116,176
Mvvv 80,116,180
Mvvv 84,116,184
Mvvv 88,116,188
Mvvv 92,116,192
Mvvv 76,120,196
Mvvv 80,120,200
Mvvv 84,120,204
Mvvv 88,120,208
Mvvv 92,120,212
Mvvv 76,124,216
Mvvv 80,124,220
Mvvv 84,124,224
Mvvv 88,124,228
Mvvv 92,124,232
Mvvv 76,128,236
Mvvv 80,128,240
Mvvv 84,128,244
Mvvv 88,128,248
Mvvv 92,128,252
s_nop 7
s_nop 2
v_max3_f32 v10, v76, v77, v78
v_max3_f32 v10, v10, v79, v96
v_max3_f32 v10, v10, v97, v98
v_max_f32 v10, v10, v99
v_mov_b32 v11, v10
s_nop 1
v_permlane16_swap_b32 v10, v11
v_max_f32 v10, v10, v11
v_mov_b32 v11, v10
s_nop 1
v_permlane32_swap_b32 v10, v11
v_max_f32 v10, v10, v11
A_ v10,0x40e00000,v10
v_max_f32 v10, 0, v10
v_exp_f32 v11, -v10
s_nop 0
U_ v46,v46,v11
R_ v12,a0
U_ v12,v12,v11
X_ a0,v12
R_ v13,a1
U_ v13,v13,v11
X_ a1,v13
R_ v12,a2
U_ v12,v12,v11
X_ a2,v12
R_ v13,a3
U_ v13,v13,v11
X_ a3,v13
R_ v12,a20
U_ v12,v12,v11
X_ a20,v12
R_ v13,a21
U_ v13,v13,v11
X_ a21,v13
R_ v12,a22
U_ v12,v12,v11
X_ a22,v12
R_ v13,a23
U_ v13,v13,v11
X_ a23,v13
R_ v12,a40
U_ v12,v12,v11
X_ a40,v12
R_ v13,a41
U_ v13,v13,v11
X_ a41,v13
R_ v12,a42
U_ v12,v12,v11
X_ a42,v12
R_ v13,a43
U_ v13,v13,v11
X_ a43,v13
R_ v12,a60
U_ v12,v12,v11
X_ a60,v12
R_ v13,a61
U_ v13,v13,v11
X_ a61,v13
R_ v12,a62
U_ v12,v12,v11
X_ a62,v12
R_ v13,a63
U_ v13,v13,v11
X_ a63,v13
R_ v12,a80
U_ v12,v12,v11
X_ a80,v12
R_ v13,a81
U_ v13,v13,v11
X_ a81,v13
R_ v12,a82
U_ v12,v12,v11
X_ a82,v12
R_ v13,a83
U_ v13,v13,v11
X_ a83,v13
R_ v12,a100
U_ v12,v12,v11
X_ a100,v12
R_ v13,a101
U_ v13,v13,v11
X_ a101,v13
R_ v12,a102
U_ v12,v12,v11
X_ a102,v12
R_ v13,a103
U_ v13,v13,v11
X_ a103,v13
R_ v12,a120
U_ v12,v12,v11
X_ a120,v12
R_ v13,a121
U_ v13,v13,v11
X_ a121,v13
R_ v12,a122
U_ v12,v12,v11
X_ a122,v12
R_ v13,a123
U_ v13,v13,v11
X_ a123,v13
R_ v12,a140
U_ v12,v12,v11
X_ a140,v12
R_ v13,a141
U_ v13,v13,v11
X_ a141,v13
R_ v12,a142
U_ v12,v12,v11
X_ a142,v12
R_ v13,a143
U_ v13,v13,v11
X_ a143,v13
R_ v12,a160
U_ v12,v12,v11
X_ a160,v12
R_ v13,a161
U_ v13,v13,v11
X_ a161,v13
R_ v12,a162
U_ v12,v12,v11
X_ a162,v12
R_ v13,a163
U_ v13,v13,v11
X_ a163,v13
R_ v12,a180
U_ v12,v12,v11
X_ a180,v12
R_ v13,a181
U_ v13,v13,v11
X_ a181,v13
R_ v12,a182
U_ v12,v12,v11
X_ a182,v12
R_ v13,a183
U_ v13,v13,v11
X_ a183,v13
R_ v12,a200
U_ v12,v12,v11
X_ a200,v12
R_ v13,a201
U_ v13,v13,v11
X_ a201,v13
R_ v12,a202
U_ v12,v12,v11
X_ a202,v12
R_ v13,a203
U_ v13,v13,v11
X_ a203,v13
R_ v12,a220
U_ v12,v12,v11
X_ a220,v12
R_ v13,a221
U_ v13,v13,v11
X_ a221,v13
R_ v12,a222
U_ v12,v12,v11
X_ a222,v12
R_ v13,a223
U_ v13,v13,v11
X_ a223,v13
B_ v56,v56,v10
B_ v57,v57,v10
B_ v58,v58,v10
B_ v59,v59,v10
B_ v76,v76,v10
B_ v77,v77,v10
B_ v78,v78,v10
B_ v79,v79,v10
B_ v96,v96,v10
B_ v97,v97,v10
B_ v98,v98,v10
B_ v99,v99,v10
E_ 76
E_ 77
E_ 78
E_ 79
E_ 96
E_ 97
E_ 98
E_ 99
s_nop 0
C_ 99,98,99
C_ 98,96,97
C_ 96,76,77
C_ 97,78,79
v_max3_f32 v10, v80, v81, v82
v_max3_f32 v10, v10, v83, v100
v_max3_f32 v10, v10, v101, v102
v_max_f32 v10, v10, v103
v_mov_b32 v11, v10
s_nop 1
v_permlane16_swap_b32 v10, v11
v_max_f32 v10, v10, v11
v_mov_b32 v11, v10
s_nop 1
v_permlane32_swap_b32 v10, v11
v_max_f32 v10, v10, v11
A_ v10,0x40e00000,v10
v_max_f32 v10, 0, v10
v_exp_f32 v11, -v10
s_nop 0
U_ v47,v47,v11
R_ v12,a4
U_ v12,v12,v11
X_ a4,v12
R_ v13,a5
U_ v13,v13,v11
X_ a5,v13
R_ v12,a6
U_ v12,v12,v11
X_ a6,v12
R_ v13,a7
U_ v13,v13,v11
X_ a7,v13
R_ v12,a24
U_ v12,v12,v11
X_ a24,v12
R_ v13,a25
U_ v13,v13,v11
X_ a25,v13
R_ v12,a26
U_ v12,v12,v11
X_ a26,v12
R_ v13,a27
U_ v13,v13,v11
X_ a27,v13
R_ v12,a44
U_ v12,v12,v11
X_ a44,v12
R_ v13,a45
U_ v13,v13,v11
X_ a45,v13
R_ v12,a46
U_ v12,v12,v11
X_ a46,v12
R_ v13,a47
U_ v13,v13,v11
X_ a47,v13
R_ v12,a64
U_ v12,v12,v11
X_ a64,v12
R_ v13,a65
U_ v13,v13,v11
X_ a65,v13
R_ v12,a66
U_ v12,v12,v11
X_ a66,v12
R_ v13,a67
U_ v13,v13,v11
X_ a67,v13
R_ v12,a84
U_ v12,v12,v11
X_ a84,v12
R_ v13,a85
U_ v13,v13,v11
X_ a85,v13
R_ v12,a86
U_ v12,v12,v11
X_ a86,v12
R_ v13,a87
U_ v13,v13,v11
X_ a87,v13
R_ v12,a104
U_ v12,v12,v11
X_ a104,v12
R_ v13,a105
U_ v13,v13,v11
X_ a105,v13
R_ v12,a106
U_ v12,v12,v11
X_ a106,v12
R_ v13,a107
U_ v13,v13,v11
X_ a107,v13
R_ v12,a124
U_ v12,v12,v11
X_ a124,v12
R_ v13,a125
U_ v13,v13,v11
X_ a125,v13
R_ v12,a126
U_ v12,v12,v11
X_ a126,v12
R_ v13,a127
U_ v13,v13,v11
X_ a127,v13
R_ v12,a144
U_ v12,v12,v11
X_ a144,v12
R_ v13,a145
U_ v13,v13,v11
X_ a145,v13
R_ v12,a146
U_ v12,v12,v11
X_ a146,v12
R_ v13,a147
U_ v13,v13,v11
X_ a147,v13
R_ v12,a164
U_ v12,v12,v11
X_ a164,v12
R_ v13,a165
U_ v13,v13,v11
X_ a165,v13
R_ v12,a166
U_ v12,v12,v11
X_ a166,v12
R_ v13,a167
U_ v13,v13,v11
X_ a167,v13
R_ v12,a184
U_ v12,v12,v11
X_ a184,v12
R_ v13,a185
U_ v13,v13,v11
X_ a185,v13
R_ v12,a186
U_ v12,v12,v11
X_ a186,v12
R_ v13,a187
U_ v13,v13,v11
X_ a187,v13
R_ v12,a204
U_ v12,v12,v11
X_ a204,v12
R_ v13,a205
U_ v13,v13,v11
X_ a205,v13
R_ v12,a206
U_ v12,v12,v11
X_ a206,v12
R_ v13,a207
U_ v13,v13,v11
X_ a207,v13
R_ v12,a224
U_ v12,v12,v11
X_ a224,v12
R_ v13,a225
U_ v13,v13,v11
X_ a225,v13
R_ v12,a226
U_ v12,v12,v11
X_ a226,v12
R_ v13,a227
U_ v13,v13,v11
X_ a227,v13
B_ v60,v60,v10
B_ v61,v61,v10
B_ v62,v62,v10
B_ v63,v63,v10
B_ v80,v80,v10
B_ v81,v81,v10
B_ v82,v82,v10
B_ v83,v83,v10
B_ v100,v100,v10
B_ v101,v101,v10
B_ v102,v102,v10
B_ v103,v103,v10
E_ 80
E_ 81
E_ 82
E_ 83
E_ 100
E_ 101
E_ 102
E_ 103
s_nop 0
C_ 103,102,103
C_ 102,100,101
C_ 100,80,81
C_ 101,82,83
v_max3_f32 v10, v84, v85, v86
v_max3_f32 v10, v10, v87, v104
v_max3_f32 v10, v10, v105, v106
v_max_f32 v10, v10, v107
v_mov_b32 v11, v10
s_nop 1
v_permlane16_swap_b32 v10, v11
v_max_f32 v10, v10, v11
v_mov_b32 v11, v10
s_nop 1
v_permlane32_swap_b32 v10, v11
v_max_f32 v10, v10, v11
A_ v10,0x40e00000,v10
v_max_f32 v10, 0, v10
v_exp_f32 v11, -v10
s_nop 0
U_ v48,v48,v11
R_ v12,a8
U_ v12,v12,v11
X_ a8,v12
R_ v13,a9
U_ v13,v13,v11
X_ a9,v13
R_ v12,a10
U_ v12,v12,v11
X_ a10,v12
R_ v13,a11
U_ v13,v13,v11
X_ a11,v13
R_ v12,a28
U_ v12,v12,v11
X_ a28,v12
R_ v13,a29
U_ v13,v13,v11
X_ a29,v13
R_ v12,a30
U_ v12,v12,v11
X_ a30,v12
R_ v13,a31
U_ v13,v13,v11
X_ a31,v13
R_ v12,a48
U_ v12,v12,v11
X_ a48,v12
R_ v13,a49
U_ v13,v13,v11
X_ a49,v13
R_ v12,a50
U_ v12,v12,v11
X_ a50,v12
R_ v13,a51
U_ v13,v13,v11
X_ a51,v13
R_ v12,a68
U_ v12,v12,v11
X_ a68,v12
R_ v13,a69
U_ v13,v13,v11
X_ a69,v13
R_ v12,a70
U_ v12,v12,v11
X_ a70,v12
R_ v13,a71
U_ v13,v13,v11
X_ a71,v13
R_ v12,a88
U_ v12,v12,v11
X_ a88,v12
R_ v13,a89
U_ v13,v13,v11
X_ a89,v13
R_ v12,a90
U_ v12,v12,v11
X_ a90,v12
R_ v13,a91
U_ v13,v13,v11
X_ a91,v13
R_ v12,a108
U_ v12,v12,v11
X_ a108,v12
R_ v13,a109
U_ v13,v13,v11
X_ a109,v13
R_ v12,a110
U_ v12,v12,v11
X_ a110,v12
R_ v13,a111
U_ v13,v13,v11
X_ a111,v13
R_ v12,a128
U_ v12,v12,v11
X_ a128,v12
R_ v13,a129
U_ v13,v13,v11
X_ a129,v13
R_ v12,a130
U_ v12,v12,v11
X_ a130,v12
R_ v13,a131
U_ v13,v13,v11
X_ a131,v13
R_ v12,a148
U_ v12,v12,v11
X_ a148,v12
R_ v13,a149
U_ v13,v13,v11
X_ a149,v13
R_ v12,a150
U_ v12,v12,v11
X_ a150,v12
R_ v13,a151
U_ v13,v13,v11
X_ a151,v13
R_ v12,a168
U_ v12,v12,v11
X_ a168,v12
R_ v13,a169
U_ v13,v13,v11
X_ a169,v13
R_ v12,a170
U_ v12,v12,v11
X_ a170,v12
R_ v13,a171
U_ v13,v13,v11
X_ a171,v13
R_ v12,a188
U_ v12,v12,v11
X_ a188,v12
R_ v13,a189
U_ v13,v13,v11
X_ a189,v13
R_ v12,a190
U_ v12,v12,v11
X_ a190,v12
R_ v13,a191
U_ v13,v13,v11
X_ a191,v13
R_ v12,a208
U_ v12,v12,v11
X_ a208,v12
R_ v13,a209
U_ v13,v13,v11
X_ a209,v13
R_ v12,a210
U_ v12,v12,v11
X_ a210,v12
R_ v13,a211
U_ v13,v13,v11
X_ a211,v13
R_ v12,a228
U_ v12,v12,v11
X_ a228,v12
R_ v13,a229
U_ v13,v13,v11
X_ a229,v13
R_ v12,a230
U_ v12,v12,v11
X_ a230,v12
R_ v13,a231
U_ v13,v13,v11
X_ a231,v13
B_ v64,v64,v10
B_ v65,v65,v10
B_ v66,v66,v10
B_ v67,v67,v10
B_ v84,v84,v10
B_ v85,v85,v10
B_ v86,v86,v10
B_ v87,v87,v10
B_ v104,v104,v10
B_ v105,v105,v10
B_ v106,v106,v10
B_ v107,v107,v10
E_ 84
E_ 85
E_ 86
E_ 87
E_ 104
E_ 105
E_ 106
E_ 107
s_nop 0
C_ 107,106,107
C_ 106,104,105
C_ 104,84,85
C_ 105,86,87
v_max3_f32 v10, v88, v89, v90
v_max3_f32 v10, v10, v91, v108
v_max3_f32 v10, v10, v109, v110
v_max_f32 v10, v10, v111
v_mov_b32 v11, v10
s_nop 1
v_permlane16_swap_b32 v10, v11
v_max_f32 v10, v10, v11
v_mov_b32 v11, v10
s_nop 1
v_permlane32_swap_b32 v10, v11
v_max_f32 v10, v10, v11
A_ v10,0x40e00000,v10
v_max_f32 v10, 0, v10
v_exp_f32 v11, -v10
s_nop 0
U_ v49,v49,v11
R_ v12,a12
U_ v12,v12,v11
X_ a12,v12
R_ v13,a13
U_ v13,v13,v11
X_ a13,v13
R_ v12,a14
U_ v12,v12,v11
X_ a14,v12
R_ v13,a15
U_ v13,v13,v11
X_ a15,v13
R_ v12,a32
U_ v12,v12,v11
X_ a32,v12
R_ v13,a33
U_ v13,v13,v11
X_ a33,v13
R_ v12,a34
U_ v12,v12,v11
X_ a34,v12
R_ v13,a35
U_ v13,v13,v11
X_ a35,v13
R_ v12,a52
U_ v12,v12,v11
X_ a52,v12
R_ v13,a53
U_ v13,v13,v11
X_ a53,v13
R_ v12,a54
U_ v12,v12,v11
X_ a54,v12
R_ v13,a55
U_ v13,v13,v11
X_ a55,v13
R_ v12,a72
U_ v12,v12,v11
X_ a72,v12
R_ v13,a73
U_ v13,v13,v11
X_ a73,v13
R_ v12,a74
U_ v12,v12,v11
X_ a74,v12
R_ v13,a75
U_ v13,v13,v11
X_ a75,v13
R_ v12,a92
U_ v12,v12,v11
X_ a92,v12
R_ v13,a93
U_ v13,v13,v11
X_ a93,v13
R_ v12,a94
U_ v12,v12,v11
X_ a94,v12
R_ v13,a95
U_ v13,v13,v11
X_ a95,v13
R_ v12,a112
U_ v12,v12,v11
X_ a112,v12
R_ v13,a113
U_ v13,v13,v11
X_ a113,v13
R_ v12,a114
U_ v12,v12,v11
X_ a114,v12
R_ v13,a115
U_ v13,v13,v11
X_ a115,v13
R_ v12,a132
U_ v12,v12,v11
X_ a132,v12
R_ v13,a133
U_ v13,v13,v11
X_ a133,v13
R_ v12,a134
U_ v12,v12,v11
X_ a134,v12
R_ v13,a135
U_ v13,v13,v11
X_ a135,v13
R_ v12,a152
U_ v12,v12,v11
X_ a152,v12
R_ v13,a153
U_ v13,v13,v11
X_ a153,v13
R_ v12,a154
U_ v12,v12,v11
X_ a154,v12
R_ v13,a155
U_ v13,v13,v11
X_ a155,v13
R_ v12,a172
U_ v12,v12,v11
X_ a172,v12
R_ v13,a173
U_ v13,v13,v11
X_ a173,v13
R_ v12,a174
U_ v12,v12,v11
X_ a174,v12
R_ v13,a175
U_ v13,v13,v11
X_ a175,v13
R_ v12,a192
U_ v12,v12,v11
X_ a192,v12
R_ v13,a193
U_ v13,v13,v11
X_ a193,v13
R_ v12,a194
U_ v12,v12,v11
X_ a194,v12
R_ v13,a195
U_ v13,v13,v11
X_ a195,v13
R_ v12,a212
U_ v12,v12,v11
X_ a212,v12
R_ v13,a213
U_ v13,v13,v11
X_ a213,v13
R_ v12,a214
U_ v12,v12,v11
X_ a214,v12
R_ v13,a215
U_ v13,v13,v11
X_ a215,v13
R_ v12,a232
U_ v12,v12,v11
X_ a232,v12
R_ v13,a233
U_ v13,v13,v11
X_ a233,v13
R_ v12,a234
U_ v12,v12,v11
X_ a234,v12
R_ v13,a235
U_ v13,v13,v11
X_ a235,v13
B_ v68,v68,v10
B_ v69,v69,v10
B_ v70,v70,v10
B_ v71,v71,v10
B_ v88,v88,v10
B_ v89,v89,v10
B_ v90,v90,v10
B_ v91,v91,v10
B_ v108,v108,v10
B_ v109,v109,v10
B_ v110,v110,v10
B_ v111,v111,v10
E_ 88
E_ 89
E_ 90
E_ 91
E_ 108
E_ 109
E_ 110
E_ 111
s_nop 0
C_ 111,110,111
C_ 110,108,109
C_ 108,88,89
C_ 109,90,91
v_max3_f32 v10, v92, v93, v94
v_max3_f32 v10, v10, v95, v112
v_max3_f32 v10, v10, v113, v114
v_max_f32 v10, v10, v115
v_mov_b32 v11, v10
s_nop 1
v_permlane16_swap_b32 v10, v11
v_max_f32 v10, v10, v11
v_mov_b32 v11, v10
s_nop 1
v_permlane32_swap_b32 v10, v11
v_max_f32 v10, v10, v11
A_ v10,0x40e00000,v10
v_max_f32 v10, 0, v10
v_exp_f32 v11, -v10
s_nop 0
U_ v50,v50,v11
R_ v12,a16
U_ v12,v12,v11
X_ a16,v12
R_ v13,a17
U_ v13,v13,v11
X_ a17,v13
R_ v12,a18
U_ v12,v12,v11
X_ a18,v12
R_ v13,a19
U_ v13,v13,v11
X_ a19,v13
R_ v12,a36
U_ v12,v12,v11
X_ a36,v12
R_ v13,a37
U_ v13,v13,v11
X_ a37,v13
R_ v12,a38
U_ v12,v12,v11
X_ a38,v12
R_ v13,a39
U_ v13,v13,v11
X_ a39,v13
R_ v12,a56
U_ v12,v12,v11
X_ a56,v12
R_ v13,a57
U_ v13,v13,v11
X_ a57,v13
R_ v12,a58
U_ v12,v12,v11
X_ a58,v12
R_ v13,a59
U_ v13,v13,v11
X_ a59,v13
R_ v12,a76
U_ v12,v12,v11
X_ a76,v12
R_ v13,a77
U_ v13,v13,v11
X_ a77,v13
R_ v12,a78
U_ v12,v12,v11
X_ a78,v12
R_ v13,a79
U_ v13,v13,v11
X_ a79,v13
R_ v12,a96
U_ v12,v12,v11
X_ a96,v12
R_ v13,a97
U_ v13,v13,v11
X_ a97,v13
R_ v12,a98
U_ v12,v12,v11
X_ a98,v12
R_ v13,a99
U_ v13,v13,v11
X_ a99,v13
R_ v12,a116
U_ v12,v12,v11
X_ a116,v12
R_ v13,a117
U_ v13,v13,v11
X_ a117,v13
R_ v12,a118
U_ v12,v12,v11
X_ a118,v12
R_ v13,a119
U_ v13,v13,v11
X_ a119,v13
R_ v12,a136
U_ v12,v12,v11
X_ a136,v12
R_ v13,a137
U_ v13,v13,v11
X_ a137,v13
R_ v12,a138
U_ v12,v12,v11
X_ a138,v12
R_ v13,a139
U_ v13,v13,v11
X_ a139,v13
R_ v12,a156
U_ v12,v12,v11
X_ a156,v12
R_ v13,a157
U_ v13,v13,v11
X_ a157,v13
R_ v12,a158
U_ v12,v12,v11
X_ a158,v12
R_ v13,a159
U_ v13,v13,v11
X_ a159,v13
R_ v12,a176
U_ v12,v12,v11
X_ a176,v12
R_ v13,a177
U_ v13,v13,v11
X_ a177,v13
R_ v12,a178
U_ v12,v12,v11
X_ a178,v12
R_ v13,a179
U_ v13,v13,v11
X_ a179,v13
R_ v12,a196
U_ v12,v12,v11
X_ a196,v12
R_ v13,a197
U_ v13,v13,v11
X_ a197,v13
R_ v12,a198
U_ v12,v12,v11
X_ a198,v12
R_ v13,a199
U_ v13,v13,v11
X_ a199,v13
R_ v12,a216
U_ v12,v12,v11
X_ a216,v12
R_ v13,a217
U_ v13,v13,v11
X_ a217,v13
R_ v12,a218
U_ v12,v12,v11
X_ a218,v12
R_ v13,a219
U_ v13,v13,v11
X_ a219,v13
R_ v12,a236
U_ v12,v12,v11
X_ a236,v12
R_ v13,a237
U_ v13,v13,v11
X_ a237,v13
R_ v12,a238
U_ v12,v12,v11
X_ a238,v12
R_ v13,a239
U_ v13,v13,v11
X_ a239,v13
B_ v72,v72,v10
B_ v73,v73,v10
B_ v74,v74,v10
B_ v75,v75,v10
B_ v92,v92,v10
B_ v93,v93,v10
B_ v94,v94,v10
B_ v95,v95,v10
B_ v112,v112,v10
B_ v113,v113,v10
B_ v114,v114,v10
B_ v115,v115,v10
E_ 92
E_ 93
E_ 94
E_ 95
E_ 112
E_ 113
E_ 114
E_ 115
s_nop 0
C_ 115,114,115
C_ 114,112,113
C_ 112,92,93
C_ 113,94,95
v_mov_b32 v92, 0x3f803f80
v_mov_b32 v93, 0x3f803f80
v_mov_b32 v94, 0x3f803f80
v_mov_b32 v95, 0x3f803f80
s_nop 7
s_lshl_b32 s53, s58, 4
S_ s53,s53,s63
s_cmp_eq_u32 s53, 2
s_cbranch_scc1 .Lstep0_3
s_cmp_eq_u32 s53, 4
s_cbranch_scc1 .Lstep0_5
s_cmp_eq_u32 s53, 6
s_cbranch_scc1 .Lstep0_7
s_cmp_eq_u32 s53, 8
s_cbranch_scc1 .Lstep0_9
s_cmp_eq_u32 s53, 10
s_cbranch_scc1 .Lstep0_11
s_cmp_eq_u32 s53, 12
s_cbranch_scc1 .Lstep0_13
s_cmp_eq_u32 s53, 14
s_cbranch_scc1 .Lstep0_15
s_cmp_eq_u32 s53, 18
s_cbranch_scc1 .Lstep1_3
s_cmp_eq_u32 s53, 20
s_cbranch_scc1 .Lstep1_5
s_cmp_eq_u32 s53, 22
s_cbranch_scc1 .Lstep1_7
s_cmp_eq_u32 s53, 24
s_cbranch_scc1 .Lstep1_9
s_cmp_eq_u32 s53, 26
s_cbranch_scc1 .Lstep1_11
s_cmp_eq_u32 s53, 28
s_cbranch_scc1 .Lstep1_13
s_cmp_eq_u32 s53, 30
s_cbranch_scc1 .Lstep1_15
s_cmp_eq_u32 s53, 34
s_cbranch_scc1 .Lstep2_3
s_cmp_eq_u32 s53, 36
s_cbranch_scc1 .Lstep2_5
s_cmp_eq_u32 s53, 38
s_cbranch_scc1 .Lstep2_7
s_cmp_eq_u32 s53, 40
s_cbranch_scc1 .Lstep2_9
s_cmp_eq_u32 s53, 42
s_cbranch_scc1 .Lstep2_11
s_cmp_eq_u32 s53, 44
s_cbranch_scc1 .Lstep2_13
s_cmp_eq_u32 s53, 46
s_cbranch_scc1 .Lstep2_15
s_endpgm
.Lslow1:
W_ 0
s_nop 7
s_nop 7
s_and_b32 s64, s61, 4294959104
s_mul_i32 s53, s63, 12288
S_ s64,s64,s53
R_ v11,a248
v_and_b32 v10, 0xf0, v11
v_mul_u32_u24 v10, 24, v10
v_lshrrev_b32 v11, 8, v11
v_lshl_add_u32 v10, v11, 4, v10
S_ s53,s64,6144
buffer_load_dwordx4 v[96:99], v10, s[32:35], s53 offen offset:0
S_ s53,s64,6144
buffer_load_dwordx4 v[100:103], v10, s[32:35], s53 offen offset:64
S_ s53,s64,6144
buffer_load_dwordx4 v[104:107], v10, s[32:35], s53 offen offset:128
S_ s53,s64,6144
buffer_load_dwordx4 v[108:111], v10, s[32:35], s53 offen offset:192
s_waitcnt vmcnt(0)
Nvvv 116,96,136,56
Nvvv 120,96,140,60
Nvvv 124,96,144,64
Nvvv 128,96,148,68
Nvvv 132,96,152,72
Mvvv 116,100,156
Mvvv 120,100,160
Mvvv 124,100,164
Mvvv 128,100,168
Mvvv 132,100,172
Mvvv 116,104,176
Mvvv 120,104,180
Mvvv 124,104,184
Mvvv 128,104,188
Mvvv 132,104,192
Mvvv 116,108,196
Mvvv 120,108,200
Mvvv 124,108,204
Mvvv 128,108,208
Mvvv 132,108,212
s_nop 7
S_ s53,s64,6144
buffer_load_dwordx4 v[96:99], v10, s[32:35], s53 offen offset:256
S_ s53,s64,6144
buffer_load_dwordx4 v[100:103], v10, s[32:35], s53 offen offset:320
S_ s53,s64,0
buffer_load_dwordx4 v[104:107], v10, s[32:35], s53 offen offset:0
S_ s53,s64,0
buffer_load_dwordx4 v[108:111], v10, s[32:35], s53 offen offset:64
s_waitcnt vmcnt(0)
Mvvv 116,96,216
Mvvv 120,96,220
Mvvv 124,96,224
Mvvv 128,96,228
Mvvv 132,96,232
Mvvv 116,100,236
Mvvv 120,100,240
Mvvv 124,100,244
Mvvv 128,100,248
Mvvv 132,100,252
Nvvv 76,104,136,56
Nvvv 80,104,140,60
Nvvv 84,104,144,64
Nvvv 88,104,148,68
Nvvv 92,104,152,72
Mvvv 76,108,156
Mvvv 80,108,160
Mvvv 84,108,164
Mvvv 88,108,168
Mvvv 92,108,172
s_nop 7
S_ s53,s64,0
buffer_load_dwordx4 v[96:99], v10, s[32:35], s53 offen offset:128
S_ s53,s64,0
buffer_load_dwordx4 v[100:103], v10, s[32:35], s53 offen offset:192
S_ s53,s64,0
buffer_load_dwordx4 v[104:107], v10, s[32:35], s53 offen offset:256
S_ s53,s64,0
buffer_load_dwordx4 v[108:111], v10, s[32:35], s53 offen offset:320
s_waitcnt vmcnt(0)
Mvvv 76,96,176
Mvvv 80,96,180
Mvvv 84,96,184
Mvvv 88,96,188
Mvvv 92,96,192
Mvvv 76,100,196
Mvvv 80,100,200
Mvvv 84,100,204
Mvvv 88,100,208
Mvvv 92,100,212
Mvvv 76,104,216
Mvvv 80,104,220
Mvvv 84,104,224
Mvvv 88,104,228
Mvvv 92,104,232
Mvvv 76,108,236
Mvvv 80,108,240
Mvvv 84,108,244
Mvvv 88,108,248
Mvvv 92,108,252
s_nop 7
s_nop 2
v_max3_f32 v10, v76, v77, v78
v_max3_f32 v10, v10, v79, v116
v_max3_f32 v10, v10, v117, v118
v_max_f32 v10, v10, v119
v_mov_b32 v11, v10
s_nop 1
v_permlane16_swap_b32 v10, v11
v_max_f32 v10, v10, v11
v_mov_b32 v11, v10
s_nop 1
v_permlane32_swap_b32 v10, v11
v_max_f32 v10, v10, v11
A_ v10,0x40e00000,v10
v_max_f32 v10, 0, v10
v_exp_f32 v11, -v10
s_nop 0
U_ v46,v46,v11
R_ v12,a0
U_ v12,v12,v11
X_ a0,v12
R_ v13,a1
U_ v13,v13,v11
X_ a1,v13
R_ v12,a2
U_ v12,v12,v11
X_ a2,v12
R_ v13,a3
U_ v13,v13,v11
X_ a3,v13
R_ v12,a20
U_ v12,v12,v11
X_ a20,v12
R_ v13,a21
U_ v13,v13,v11
X_ a21,v13
R_ v12,a22
U_ v12,v12,v11
X_ a22,v12
R_ v13,a23
U_ v13,v13,v11
X_ a23,v13
R_ v12,a40
U_ v12,v12,v11
X_ a40,v12
R_ v13,a41
U_ v13,v13,v11
X_ a41,v13
R_ v12,a42
U_ v12,v12,v11
X_ a42,v12
R_ v13,a43
U_ v13,v13,v11
X_ a43,v13
R_ v12,a60
U_ v12,v12,v11
X_ a60,v12
R_ v13,a61
U_ v13,v13,v11
X_ a61,v13
R_ v12,a62
U_ v12,v12,v11
X_ a62,v12
R_ v13,a63
U_ v13,v13,v11
X_ a63,v13
R_ v12,a80
U_ v12,v12,v11
X_ a80,v12
R_ v13,a81
U_ v13,v13,v11
X_ a81,v13
R_ v12,a82
U_ v12,v12,v11
X_ a82,v12
R_ v13,a83
U_ v13,v13,v11
X_ a83,v13
R_ v12,a100
U_ v12,v12,v11
X_ a100,v12
R_ v13,a101
U_ v13,v13,v11
X_ a101,v13
R_ v12,a102
U_ v12,v12,v11
X_ a102,v12
R_ v13,a103
U_ v13,v13,v11
X_ a103,v13
R_ v12,a120
U_ v12,v12,v11
X_ a120,v12
R_ v13,a121
U_ v13,v13,v11
X_ a121,v13
R_ v12,a122
U_ v12,v12,v11
X_ a122,v12
R_ v13,a123
U_ v13,v13,v11
X_ a123,v13
R_ v12,a140
U_ v12,v12,v11
X_ a140,v12
R_ v13,a141
U_ v13,v13,v11
X_ a141,v13
R_ v12,a142
U_ v12,v12,v11
X_ a142,v12
R_ v13,a143
U_ v13,v13,v11
X_ a143,v13
R_ v12,a160
U_ v12,v12,v11
X_ a160,v12
R_ v13,a161
U_ v13,v13,v11
X_ a161,v13
R_ v12,a162
U_ v12,v12,v11
X_ a162,v12
R_ v13,a163
U_ v13,v13,v11
X_ a163,v13
R_ v12,a180
U_ v12,v12,v11
X_ a180,v12
R_ v13,a181
U_ v13,v13,v11
X_ a181,v13
R_ v12,a182
U_ v12,v12,v11
X_ a182,v12
R_ v13,a183
U_ v13,v13,v11
X_ a183,v13
R_ v12,a200
U_ v12,v12,v11
X_ a200,v12
R_ v13,a201
U_ v13,v13,v11
X_ a201,v13
R_ v12,a202
U_ v12,v12,v11
X_ a202,v12
R_ v13,a203
U_ v13,v13,v11
X_ a203,v13
R_ v12,a220
U_ v12,v12,v11
X_ a220,v12
R_ v13,a221
U_ v13,v13,v11
X_ a221,v13
R_ v12,a222
U_ v12,v12,v11
X_ a222,v12
R_ v13,a223
U_ v13,v13,v11
X_ a223,v13
B_ v56,v56,v10
B_ v57,v57,v10
B_ v58,v58,v10
B_ v59,v59,v10
B_ v76,v76,v10
B_ v77,v77,v10
B_ v78,v78,v10
B_ v79,v79,v10
B_ v116,v116,v10
B_ v117,v117,v10
B_ v118,v118,v10
B_ v119,v119,v10
E_ 76
E_ 77
E_ 78
E_ 79
E_ 116
E_ 117
E_ 118
E_ 119
s_nop 0
C_ 119,118,119
C_ 118,116,117
C_ 116,76,77
C_ 117,78,79
v_max3_f32 v10, v80, v81, v82
v_max3_f32 v10, v10, v83, v120
v_max3_f32 v10, v10, v121, v122
v_max_f32 v10, v10, v123
v_mov_b32 v11, v10
s_nop 1
v_permlane16_swap_b32 v10, v11
v_max_f32 v10, v10, v11
v_mov_b32 v11, v10
s_nop 1
v_permlane32_swap_b32 v10, v11
v_max_f32 v10, v10, v11
A_ v10,0x40e00000,v10
v_max_f32 v10, 0, v10
v_exp_f32 v11, -v10
s_nop 0
U_ v47,v47,v11
R_ v12,a4
U_ v12,v12,v11
X_ a4,v12
R_ v13,a5
U_ v13,v13,v11
X_ a5,v13
R_ v12,a6
U_ v12,v12,v11
X_ a6,v12
R_ v13,a7
U_ v13,v13,v11
X_ a7,v13
R_ v12,a24
U_ v12,v12,v11
X_ a24,v12
R_ v13,a25
U_ v13,v13,v11
X_ a25,v13
R_ v12,a26
U_ v12,v12,v11
X_ a26,v12
R_ v13,a27
U_ v13,v13,v11
X_ a27,v13
R_ v12,a44
U_ v12,v12,v11
X_ a44,v12
R_ v13,a45
U_ v13,v13,v11
X_ a45,v13
R_ v12,a46
U_ v12,v12,v11
X_ a46,v12
R_ v13,a47
U_ v13,v13,v11
X_ a47,v13
R_ v12,a64
U_ v12,v12,v11
X_ a64,v12
R_ v13,a65
U_ v13,v13,v11
X_ a65,v13
R_ v12,a66
U_ v12,v12,v11
X_ a66,v12
R_ v13,a67
U_ v13,v13,v11
X_ a67,v13
R_ v12,a84
U_ v12,v12,v11
X_ a84,v12
R_ v13,a85
U_ v13,v13,v11
X_ a85,v13
R_ v12,a86
U_ v12,v12,v11
X_ a86,v12
R_ v13,a87
U_ v13,v13,v11
X_ a87,v13
R_ v12,a104
U_ v12,v12,v11
X_ a104,v12
R_ v13,a105
U_ v13,v13,v11
X_ a105,v13
R_ v12,a106
U_ v12,v12,v11
X_ a106,v12
R_ v13,a107
U_ v13,v13,v11
X_ a107,v13
R_ v12,a124
U_ v12,v12,v11
X_ a124,v12
R_ v13,a125
U_ v13,v13,v11
X_ a125,v13
R_ v12,a126
U_ v12,v12,v11
X_ a126,v12
R_ v13,a127
U_ v13,v13,v11
X_ a127,v13
R_ v12,a144
U_ v12,v12,v11
X_ a144,v12
R_ v13,a145
U_ v13,v13,v11
X_ a145,v13
R_ v12,a146
U_ v12,v12,v11
X_ a146,v12
R_ v13,a147
U_ v13,v13,v11
X_ a147,v13
R_ v12,a164
U_ v12,v12,v11
X_ a164,v12
R_ v13,a165
U_ v13,v13,v11
X_ a165,v13
R_ v12,a166
U_ v12,v12,v11
X_ a166,v12
R_ v13,a167
U_ v13,v13,v11
X_ a167,v13
R_ v12,a184
U_ v12,v12,v11
X_ a184,v12
R_ v13,a185
U_ v13,v13,v11
X_ a185,v13
R_ v12,a186
U_ v12,v12,v11
X_ a186,v12
R_ v13,a187
U_ v13,v13,v11
X_ a187,v13
R_ v12,a204
U_ v12,v12,v11
X_ a204,v12
R_ v13,a205
U_ v13,v13,v11
X_ a205,v13
R_ v12,a206
U_ v12,v12,v11
X_ a206,v12
R_ v13,a207
U_ v13,v13,v11
X_ a207,v13
R_ v12,a224
U_ v12,v12,v11
X_ a224,v12
R_ v13,a225
U_ v13,v13,v11
X_ a225,v13
R_ v12,a226
U_ v12,v12,v11
X_ a226,v12
R_ v13,a227
U_ v13,v13,v11
X_ a227,v13
B_ v60,v60,v10
B_ v61,v61,v10
B_ v62,v62,v10
B_ v63,v63,v10
B_ v80,v80,v10
B_ v81,v81,v10
B_ v82,v82,v10
B_ v83,v83,v10
B_ v120,v120,v10
B_ v121,v121,v10
B_ v122,v122,v10
B_ v123,v123,v10
E_ 80
E_ 81
E_ 82
E_ 83
E_ 120
E_ 121
E_ 122
E_ 123
s_nop 0
C_ 123,122,123
C_ 122,120,121
C_ 120,80,81
C_ 121,82,83
v_max3_f32 v10, v84, v85, v86
v_max3_f32 v10, v10, v87, v124
v_max3_f32 v10, v10, v125, v126
v_max_f32 v10, v10, v127
v_mov_b32 v11, v10
s_nop 1
v_permlane16_swap_b32 v10, v11
v_max_f32 v10, v10, v11
v_mov_b32 v11, v10
s_nop 1
v_permlane32_swap_b32 v10, v11
v_max_f32 v10, v10, v11
A_ v10,0x40e00000,v10
v_max_f32 v10, 0, v10
v_exp_f32 v11, -v10
s_nop 0
U_ v48,v48,v11
R_ v12,a8
U_ v12,v12,v11
X_ a8,v12
R_ v13,a9
U_ v13,v13,v11
X_ a9,v13
R_ v12,a10
U_ v12,v12,v11
X_ a10,v12
R_ v13,a11
U_ v13,v13,v11
X_ a11,v13
R_ v12,a28
U_ v12,v12,v11
X_ a28,v12
R_ v13,a29
U_ v13,v13,v11
X_ a29,v13
R_ v12,a30
U_ v12,v12,v11
X_ a30,v12
R_ v13,a31
U_ v13,v13,v11
X_ a31,v13
R_ v12,a48
U_ v12,v12,v11
X_ a48,v12
R_ v13,a49
U_ v13,v13,v11
X_ a49,v13
R_ v12,a50
U_ v12,v12,v11
X_ a50,v12
R_ v13,a51
U_ v13,v13,v11
X_ a51,v13
R_ v12,a68
U_ v12,v12,v11
X_ a68,v12
R_ v13,a69
U_ v13,v13,v11
X_ a69,v13
R_ v12,a70
U_ v12,v12,v11
X_ a70,v12
R_ v13,a71
U_ v13,v13,v11
X_ a71,v13
R_ v12,a88
U_ v12,v12,v11
X_ a88,v12
R_ v13,a89
U_ v13,v13,v11
X_ a89,v13
R_ v12,a90
U_ v12,v12,v11
X_ a90,v12
R_ v13,a91
U_ v13,v13,v11
X_ a91,v13
R_ v12,a108
U_ v12,v12,v11
X_ a108,v12
R_ v13,a109
U_ v13,v13,v11
X_ a109,v13
R_ v12,a110
U_ v12,v12,v11
X_ a110,v12
R_ v13,a111
U_ v13,v13,v11
X_ a111,v13
R_ v12,a128
U_ v12,v12,v11
X_ a128,v12
R_ v13,a129
U_ v13,v13,v11
X_ a129,v13
R_ v12,a130
U_ v12,v12,v11
X_ a130,v12
R_ v13,a131
U_ v13,v13,v11
X_ a131,v13
R_ v12,a148
U_ v12,v12,v11
X_ a148,v12
R_ v13,a149
U_ v13,v13,v11
X_ a149,v13
R_ v12,a150
U_ v12,v12,v11
X_ a150,v12
R_ v13,a151
U_ v13,v13,v11
X_ a151,v13
R_ v12,a168
U_ v12,v12,v11
X_ a168,v12
R_ v13,a169
U_ v13,v13,v11
X_ a169,v13
R_ v12,a170
U_ v12,v12,v11
X_ a170,v12
R_ v13,a171
U_ v13,v13,v11
X_ a171,v13
R_ v12,a188
U_ v12,v12,v11
X_ a188,v12
R_ v13,a189
U_ v13,v13,v11
X_ a189,v13
R_ v12,a190
U_ v12,v12,v11
X_ a190,v12
R_ v13,a191
U_ v13,v13,v11
X_ a191,v13
R_ v12,a208
U_ v12,v12,v11
X_ a208,v12
R_ v13,a209
U_ v13,v13,v11
X_ a209,v13
R_ v12,a210
U_ v12,v12,v11
X_ a210,v12
R_ v13,a211
U_ v13,v13,v11
X_ a211,v13
R_ v12,a228
U_ v12,v12,v11
X_ a228,v12
R_ v13,a229
U_ v13,v13,v11
X_ a229,v13
R_ v12,a230
U_ v12,v12,v11
X_ a230,v12
R_ v13,a231
U_ v13,v13,v11
X_ a231,v13
B_ v64,v64,v10
B_ v65,v65,v10
B_ v66,v66,v10
B_ v67,v67,v10
B_ v84,v84,v10
B_ v85,v85,v10
B_ v86,v86,v10
B_ v87,v87,v10
B_ v124,v124,v10
B_ v125,v125,v10
B_ v126,v126,v10
B_ v127,v127,v10
E_ 84
E_ 85
E_ 86
E_ 87
E_ 124
E_ 125
E_ 126
E_ 127
s_nop 0
C_ 127,126,127
C_ 126,124,125
C_ 124,84,85
C_ 125,86,87
v_max3_f32 v10, v88, v89, v90
v_max3_f32 v10, v10, v91, v128
v_max3_f32 v10, v10, v129, v130
v_max_f32 v10, v10, v131
v_mov_b32 v11, v10
s_nop 1
v_permlane16_swap_b32 v10, v11
v_max_f32 v10, v10, v11
v_mov_b32 v11, v10
s_nop 1
v_permlane32_swap_b32 v10, v11
v_max_f32 v10, v10, v11
A_ v10,0x40e00000,v10
v_max_f32 v10, 0, v10
v_exp_f32 v11, -v10
s_nop 0
U_ v49,v49,v11
R_ v12,a12
U_ v12,v12,v11
X_ a12,v12
R_ v13,a13
U_ v13,v13,v11
X_ a13,v13
R_ v12,a14
U_ v12,v12,v11
X_ a14,v12
R_ v13,a15
U_ v13,v13,v11
X_ a15,v13
R_ v12,a32
U_ v12,v12,v11
X_ a32,v12
R_ v13,a33
U_ v13,v13,v11
X_ a33,v13
R_ v12,a34
U_ v12,v12,v11
X_ a34,v12
R_ v13,a35
U_ v13,v13,v11
X_ a35,v13
R_ v12,a52
U_ v12,v12,v11
X_ a52,v12
R_ v13,a53
U_ v13,v13,v11
X_ a53,v13
R_ v12,a54
U_ v12,v12,v11
X_ a54,v12
R_ v13,a55
U_ v13,v13,v11
X_ a55,v13
R_ v12,a72
U_ v12,v12,v11
X_ a72,v12
R_ v13,a73
U_ v13,v13,v11
X_ a73,v13
R_ v12,a74
U_ v12,v12,v11
X_ a74,v12
R_ v13,a75
U_ v13,v13,v11
X_ a75,v13
R_ v12,a92
U_ v12,v12,v11
X_ a92,v12
R_ v13,a93
U_ v13,v13,v11
X_ a93,v13
R_ v12,a94
U_ v12,v12,v11
X_ a94,v12
R_ v13,a95
U_ v13,v13,v11
X_ a95,v13
R_ v12,a112
U_ v12,v12,v11
X_ a112,v12
R_ v13,a113
U_ v13,v13,v11
X_ a113,v13
R_ v12,a114
U_ v12,v12,v11
X_ a114,v12
R_ v13,a115
U_ v13,v13,v11
X_ a115,v13
R_ v12,a132
U_ v12,v12,v11
X_ a132,v12
R_ v13,a133
U_ v13,v13,v11
X_ a133,v13
R_ v12,a134
U_ v12,v12,v11
X_ a134,v12
R_ v13,a135
U_ v13,v13,v11
X_ a135,v13
R_ v12,a152
U_ v12,v12,v11
X_ a152,v12
R_ v13,a153
U_ v13,v13,v11
X_ a153,v13
R_ v12,a154
U_ v12,v12,v11
X_ a154,v12
R_ v13,a155
U_ v13,v13,v11
X_ a155,v13
R_ v12,a172
U_ v12,v12,v11
X_ a172,v12
R_ v13,a173
U_ v13,v13,v11
X_ a173,v13
R_ v12,a174
U_ v12,v12,v11
X_ a174,v12
R_ v13,a175
U_ v13,v13,v11
X_ a175,v13
R_ v12,a192
U_ v12,v12,v11
X_ a192,v12
R_ v13,a193
U_ v13,v13,v11
X_ a193,v13
R_ v12,a194
U_ v12,v12,v11
X_ a194,v12
R_ v13,a195
U_ v13,v13,v11
X_ a195,v13
R_ v12,a212
U_ v12,v12,v11
X_ a212,v12
R_ v13,a213
U_ v13,v13,v11
X_ a213,v13
R_ v12,a214
U_ v12,v12,v11
X_ a214,v12
R_ v13,a215
U_ v13,v13,v11
X_ a215,v13
R_ v12,a232
U_ v12,v12,v11
X_ a232,v12
R_ v13,a233
U_ v13,v13,v11
X_ a233,v13
R_ v12,a234
U_ v12,v12,v11
X_ a234,v12
R_ v13,a235
U_ v13,v13,v11
X_ a235,v13
B_ v68,v68,v10
B_ v69,v69,v10
B_ v70,v70,v10
B_ v71,v71,v10
B_ v88,v88,v10
B_ v89,v89,v10
B_ v90,v90,v10
B_ v91,v91,v10
B_ v128,v128,v10
B_ v129,v129,v10
B_ v130,v130,v10
B_ v131,v131,v10
E_ 88
E_ 89
E_ 90
E_ 91
E_ 128
E_ 129
E_ 130
E_ 131
s_nop 0
C_ 131,130,131
C_ 130,128,129
C_ 128,88,89
C_ 129,90,91
v_max3_f32 v10, v92, v93, v94
v_max3_f32 v10, v10, v95, v132
v_max3_f32 v10, v10, v133, v134
v_max_f32 v10, v10, v135
v_mov_b32 v11, v10
s_nop 1
v_permlane16_swap_b32 v10, v11
v_max_f32 v10, v10, v11
v_mov_b32 v11, v10
s_nop 1
v_permlane32_swap_b32 v10, v11
v_max_f32 v10, v10, v11
A_ v10,0x40e00000,v10
v_max_f32 v10, 0, v10
v_exp_f32 v11, -v10
s_nop 0
U_ v50,v50,v11
R_ v12,a16
U_ v12,v12,v11
X_ a16,v12
R_ v13,a17
U_ v13,v13,v11
X_ a17,v13
R_ v12,a18
U_ v12,v12,v11
X_ a18,v12
R_ v13,a19
U_ v13,v13,v11
X_ a19,v13
R_ v12,a36
U_ v12,v12,v11
X_ a36,v12
R_ v13,a37
U_ v13,v13,v11
X_ a37,v13
R_ v12,a38
U_ v12,v12,v11
X_ a38,v12
R_ v13,a39
U_ v13,v13,v11
X_ a39,v13
R_ v12,a56
U_ v12,v12,v11
X_ a56,v12
R_ v13,a57
U_ v13,v13,v11
X_ a57,v13
R_ v12,a58
U_ v12,v12,v11
X_ a58,v12
R_ v13,a59
U_ v13,v13,v11
X_ a59,v13
R_ v12,a76
U_ v12,v12,v11
X_ a76,v12
R_ v13,a77
U_ v13,v13,v11
X_ a77,v13
R_ v12,a78
U_ v12,v12,v11
X_ a78,v12
R_ v13,a79
U_ v13,v13,v11
X_ a79,v13
R_ v12,a96
U_ v12,v12,v11
X_ a96,v12
R_ v13,a97
U_ v13,v13,v11
X_ a97,v13
R_ v12,a98
U_ v12,v12,v11
X_ a98,v12
R_ v13,a99
U_ v13,v13,v11
X_ a99,v13
R_ v12,a116
U_ v12,v12,v11
X_ a116,v12
R_ v13,a117
U_ v13,v13,v11
X_ a117,v13
R_ v12,a118
U_ v12,v12,v11
X_ a118,v12
R_ v13,a119
U_ v13,v13,v11
X_ a119,v13
R_ v12,a136
)A"
R"A(U_ v12,v12,v11
X_ a136,v12
R_ v13,a137
U_ v13,v13,v11
X_ a137,v13
R_ v12,a138
U_ v12,v12,v11
X_ a138,v12
R_ v13,a139
U_ v13,v13,v11
X_ a139,v13
R_ v12,a156
U_ v12,v12,v11
X_ a156,v12
R_ v13,a157
U_ v13,v13,v11
X_ a157,v13
R_ v12,a158
U_ v12,v12,v11
X_ a158,v12
R_ v13,a159
U_ v13,v13,v11
X_ a159,v13
R_ v12,a176
U_ v12,v12,v11
X_ a176,v12
R_ v13,a177
U_ v13,v13,v11
X_ a177,v13
R_ v12,a178
U_ v12,v12,v11
X_ a178,v12
R_ v13,a179
U_ v13,v13,v11
X_ a179,v13
R_ v12,a196
U_ v12,v12,v11
X_ a196,v12
R_ v13,a197
U_ v13,v13,v11
X_ a197,v13
R_ v12,a198
U_ v12,v12,v11
X_ a198,v12
R_ v13,a199
U_ v13,v13,v11
X_ a199,v13
R_ v12,a216
U_ v12,v12,v11
X_ a216,v12
R_ v13,a217
U_ v13,v13,v11
X_ a217,v13
R_ v12,a218
U_ v12,v12,v11
X_ a218,v12
R_ v13,a219
U_ v13,v13,v11
X_ a219,v13
R_ v12,a236
U_ v12,v12,v11
X_ a236,v12
R_ v13,a237
U_ v13,v13,v11
X_ a237,v13
R_ v12,a238
U_ v12,v12,v11
X_ a238,v12
R_ v13,a239
U_ v13,v13,v11
X_ a239,v13
B_ v72,v72,v10
B_ v73,v73,v10
B_ v74,v74,v10
B_ v75,v75,v10
B_ v92,v92,v10
B_ v93,v93,v10
B_ v94,v94,v10
B_ v95,v95,v10
B_ v132,v132,v10
B_ v133,v133,v10
B_ v134,v134,v10
B_ v135,v135,v10
E_ 92
E_ 93
E_ 94
E_ 95
E_ 132
E_ 133
E_ 134
E_ 135
s_nop 0
C_ 135,134,135
C_ 134,132,133
C_ 132,92,93
C_ 133,94,95
v_mov_b32 v92, 0x3f803f80
v_mov_b32 v93, 0x3f803f80
v_mov_b32 v94, 0x3f803f80
v_mov_b32 v95, 0x3f803f80
s_nop 7
s_lshl_b32 s53, s58, 4
S_ s53,s53,s63
s_cmp_eq_u32 s53, 1
s_cbranch_scc1 .Lstep0_2
s_cmp_eq_u32 s53, 3
s_cbranch_scc1 .Lstep0_4
s_cmp_eq_u32 s53, 5
s_cbranch_scc1 .Lstep0_6
s_cmp_eq_u32 s53, 7
s_cbranch_scc1 .Lstep0_8
s_cmp_eq_u32 s53, 9
s_cbranch_scc1 .Lstep0_10
s_cmp_eq_u32 s53, 11
s_cbranch_scc1 .Lstep0_12
s_cmp_eq_u32 s53, 13
s_cbranch_scc1 .Lstep0_14
s_cmp_eq_u32 s53, 15
s_cbranch_scc1 .Lstep0_16
s_cmp_eq_u32 s53, 17
s_cbranch_scc1 .Lstep1_2
s_cmp_eq_u32 s53, 19
s_cbranch_scc1 .Lstep1_4
s_cmp_eq_u32 s53, 21
s_cbranch_scc1 .Lstep1_6
s_cmp_eq_u32 s53, 23
s_cbranch_scc1 .Lstep1_8
s_cmp_eq_u32 s53, 25
s_cbranch_scc1 .Lstep1_10
s_cmp_eq_u32 s53, 27
s_cbranch_scc1 .Lstep1_12
s_cmp_eq_u32 s53, 29
s_cbranch_scc1 .Lstep1_14
s_cmp_eq_u32 s53, 31
s_cbranch_scc1 .Lstep1_16
s_cmp_eq_u32 s53, 33
s_cbranch_scc1 .Lstep2_2
s_cmp_eq_u32 s53, 35
s_cbranch_scc1 .Lstep2_4
s_cmp_eq_u32 s53, 37
s_cbranch_scc1 .Lstep2_6
s_cmp_eq_u32 s53, 39
s_cbranch_scc1 .Lstep2_8
s_cmp_eq_u32 s53, 41
s_cbranch_scc1 .Lstep2_10
s_cmp_eq_u32 s53, 43
s_cbranch_scc1 .Lstep2_12
s_cmp_eq_u32 s53, 45
s_cbranch_scc1 .Lstep2_14
s_cmp_eq_u32 s53, 47
s_cbranch_scc1 .Lstep2_16
s_endpgm
.Lpace1:
S_ s12,s12,s13
.Lpace1_l:
s_memrealtime s[100:101]
W_ 0
s_sub_u32 s100, s100, s98
s_cmp_ge_u32 s100, s12
s_cbranch_scc1 .Lpace1_back
s_sleep 2
s_branch .Lpace1_l
.Lpace3:
S_ s12,s12,s13
.Lpace3_l:
s_memrealtime s[100:101]
W_ 0
s_sub_u32 s100, s100, s98
s_cmp_ge_u32 s100, s12
s_cbranch_scc1 .Lpace3_back
s_sleep 2
s_branch .Lpace3_l
.Lpace5:
S_ s12,s12,s13
.Lpace5_l:
s_memrealtime s[100:101]
W_ 0
s_sub_u32 s100, s100, s98
s_cmp_ge_u32 s100, s12
s_cbranch_scc1 .Lpace5_back
s_sleep 2
s_branch .Lpace5_l
.Lpace7:
S_ s12,s12,s13
.Lpace7_l:
s_memrealtime s[100:101]
W_ 0
s_sub_u32 s100, s100, s98
s_cmp_ge_u32 s100, s12
s_cbranch_scc1 .Lpace7_back
s_sleep 2
s_branch .Lpace7_l
.Lpace9:
S_ s12,s12,s13
.Lpace9_l:
s_memrealtime s[100:101]
W_ 0
s_sub_u32 s100, s100, s98
s_cmp_ge_u32 s100, s12
s_cbranch_scc1 .Lpace9_back
s_sleep 2
s_branch .Lpace9_l
.Lpace11:
S_ s12,s12,s13
.Lpace11_l:
s_memrealtime s[100:101]
W_ 0
s_sub_u32 s100, s100, s98
s_cmp_ge_u32 s100, s12
s_cbranch_scc1 .Lpace11_back
s_sleep 2
s_branch .Lpace11_l
.Lpace13:
S_ s12,s12,s13
.Lpace13_l:
s_memrealtime s[100:101]
W_ 0
s_sub_u32 s100, s100, s98
s_cmp_ge_u32 s100, s12
s_cbranch_scc1 .Lpace13_back
s_sleep 2
s_branch .Lpace13_l
.Lpace15:
S_ s12,s12,s13
.Lpace15_l:
s_memrealtime s[100:101]
W_ 0
s_sub_u32 s100, s100, s98
s_cmp_ge_u32 s100, s12
s_cbranch_scc1 .Lpace15_back
s_sleep 2
s_branch .Lpace15_l
.Lpace17:
S_ s12,s12,s13
.Lpace17_l:
s_memrealtime s[100:101]
W_ 0
s_sub_u32 s100, s100, s98
s_cmp_ge_u32 s100, s12
s_cbranch_scc1 .Lpace17_back
s_sleep 2
s_branch .Lpace17_l
.Lpace19:
S_ s12,s12,s13
.Lpace19_l:
s_memrealtime s[100:101]
W_ 0
s_sub_u32 s100, s100, s98
s_cmp_ge_u32 s100, s12
s_cbranch_scc1 .Lpace19_back
s_sleep 2
s_branch .Lpace19_l
.Lpace21:
S_ s12,s12,s13
.Lpace21_l:
s_memrealtime s[100:101]
W_ 0
s_sub_u32 s100, s100, s98
s_cmp_ge_u32 s100, s12
s_cbranch_scc1 .Lpace21_back
s_sleep 2
s_branch .Lpace21_l
.Lpace23:
S_ s12,s12,s13
.Lpace23_l:
s_memrealtime s[100:101]
W_ 0
s_sub_u32 s100, s100, s98
s_cmp_ge_u32 s100, s12
s_cbranch_scc1 .Lpace23_back
s_sleep 2
s_branch .Lpace23_l
.Lpace25:
S_ s12,s12,s13
.Lpace25_l:
s_memrealtime s[100:101]
W_ 0
s_sub_u32 s100, s100, s98
s_cmp_ge_u32 s100, s12
s_cbranch_scc1 .Lpace25_back
s_sleep 2
s_branch .Lpace25_l
.Lpace27:
S_ s12,s12,s13
.Lpace27_l:
s_memrealtime s[100:101]
W_ 0
s_sub_u32 s100, s100, s98
s_cmp_ge_u32 s100, s12
s_cbranch_scc1 .Lpace27_back
s_sleep 2
s_branch .Lpace27_l
.Lpace29:
S_ s12,s12,s13
.Lpace29_l:
s_memrealtime s[100:101]
W_ 0
s_sub_u32 s100, s100, s98
s_cmp_ge_u32 s100, s12
s_cbranch_scc1 .Lpace29_back
s_sleep 2
s_branch .Lpace29_l
.Lhs_a0:
s_nop 4
s_waitcnt vmcnt(12)
R_ v10,a251
buffer_store_dword a255, v10, s[92:95], 0 offen offset:128 sc1
s_branch .Lhs_a0_back
.Lhs_a1:
s_nop 4
R_ v11,a248
v_lshrrev_b32 v11, 2, v11
buffer_load_dword a254, v11, s[92:95], s96 offen offset:128 sc1
s_branch .Lhs_a1_back
.Lhs_a2:
s_nop 4
s_waitcnt vmcnt(12)
R_ v10,a251
buffer_store_dword a255, v10, s[92:95], 0 offen offset:128 sc1
s_branch .Lhs_a2_back
.Lhs_b1:
s_nop 4
s_waitcnt vmcnt(12)
R_ v11,a254
v_cmp_eq_u32 vcc, s62, v11
R_ v10,a251
v_mov_b32 v11, 0x7fff0000
v_cndmask_b32 v10, v11, v10, vcc
R_ v11,a253
v_add_u32 v11, 1, v11
buffer_store_dword v11, v10, s[92:95], 0 offen offset:256 sc1
s_and_b32 s53, vcc_lo, 255
s_cmp_eq_u32 s53, 0xFF
s_cselect_b32 s97, 1, 0
s_branch .Lhs_b1_back
.Lhs_c0:
s_nop 4
R_ v11,a248
v_lshrrev_b32 v11, 2, v11
buffer_load_dword a254, v11, s[92:95], s96 offen offset:128 sc1
s_branch .Lhs_c0_back
.Lhs_c1:
s_nop 4
R_ v11,a248
v_lshrrev_b32 v11, 2, v11
buffer_load_dword a254, v11, s[92:95], s96 offen offset:256 sc1
s_branch .Lhs_c1_back
.Lhs_c2:
s_nop 4
R_ v11,a248
v_lshrrev_b32 v11, 2, v11
buffer_load_dword a254, v11, s[92:95], s96 offen offset:128 sc1
s_branch .Lhs_c2_back
.Lhs_d0:
s_nop 4
s_waitcnt vmcnt(12)
R_ v11,a254
v_cmp_eq_u32 vcc, s62, v11
R_ v10,a251
v_mov_b32 v11, 0x7fff0000
v_cndmask_b32 v10, v11, v10, vcc
R_ v11,a253
v_add_u32 v11, 1, v11
buffer_store_dword v11, v10, s[92:95], 0 offen offset:256 sc1
s_and_b32 s53, vcc_lo, 255
s_cmp_eq_u32 s53, 0xFF
s_cselect_b32 s97, 1, 0
s_branch .Lhs_d0_back
.Lhs_d2:
s_nop 4
s_waitcnt vmcnt(12)
R_ v11,a254
v_cmp_eq_u32 vcc, s62, v11
R_ v10,a251
v_mov_b32 v11, 0x7fff0000
v_cndmask_b32 v10, v11, v10, vcc
R_ v11,a253
v_add_u32 v11, 1, v11
buffer_store_dword v11, v10, s[92:95], 0 offen offset:256 sc1
s_and_b32 s53, vcc_lo, 255
s_cmp_eq_u32 s53, 0xFF
s_cselect_b32 s97, 1, 0
s_branch .Lhs_d2_back
.Lhs_d1:
s_nop 4
s_waitcnt vmcnt(12)
R_ v11,a254
R_ v10,a252
v_cmp_ne_u32 vcc, v11, v10
s_nop 1
s_and_b32 s53, vcc_lo, 255
s_cmp_eq_u32 s53, 0xFF
s_cselect_b32 s53, s97, 0
s_cmp_eq_u32 s53, 1
s_cbranch_scc1 .Lhs_ok
.Lhs_spin:
s_cmp_eq_u32 s57, 0
s_cbranch_scc1 .Lhs_ok
s_sub_u32 s57, s57, 1
s_sleep 8
R_ v10,a251
s_lshl_b32 s53, s6, 2
v_subrev_u32 v10, s53, v10
buffer_load_dword v11, v10, s[92:95], 0 offen offset:0 sc1
R_ v10,a248
v_lshrrev_b32 v10, 2, v10
buffer_load_dword v12, v10, s[92:95], s96 offen offset:128 sc1
buffer_load_dword v13, v10, s[92:95], s96 offen offset:256 sc1
s_waitcnt vmcnt(0)
X_ a255,v11
R_ v10,a251
buffer_store_dword v11, v10, s[92:95], 0 offen offset:128 sc1
s_nop 1
v_cmp_eq_u32 vcc, s62, v12
R_ v10,a251
v_mov_b32 v12, 0x7fff0000
v_cndmask_b32 v10, v12, v10, vcc
R_ v12,a253
v_add_u32 v12, 1, v12
buffer_store_dword v12, v10, s[92:95], 0 offen offset:256 sc1
s_and_b32 s53, vcc_lo, 255
s_cmp_eq_u32 s53, 0xFF
s_cselect_b32 s97, 1, 0
s_nop 1
R_ v10,a252
v_cmp_ne_u32 vcc, v13, v10
s_nop 1
s_and_b32 s53, vcc_lo, 255
s_cmp_eq_u32 s53, 0xFF
s_cselect_b32 s53, s97, 0
s_cmp_eq_u32 s53, 1
s_cbranch_scc0 .Lhs_spin
.Lhs_ok:
s_branch .Lhs_d1_back
.Latt2:
R_ v13,a248
S_ s53,s51,36864
v_add_u32 v12, s53, v13
s_mul_i32 s53, s4, 10240
S_ s53,s53,0
v_add_u32 v13, s53, v13
s_cmp_eq_u32 s4, 0
s_cbranch_scc0 .Lhs_tok2
R_ v11,a241
buffer_load_dword a255, v11, s[92:95], 0 offen offset:0 sc1
.Lhs_tok2:
W_ 2
Zvvv 96,30,136
Dv 42,v1,44032
R_ v2,a245
Zvvv 100,30,140
ds_write_b128 v13, a[0:3] offset:0
Zvvv 104,30,144
v_xor_b32 v3, 32, v2
v_xor_b32 v4, 64, v2
Zvvv 108,30,148
ds_write_b128 v13, a[4:7] offset:1024
Zvvv 112,30,152
W_ 4
v_xor_b32 v5, 96, v2
s_mov_b64 s[24:25], s[16:17]
Mvvv 96,34,156
s_mov_b32 s26, 7864320
Dv 30,%0,45056
s_mov_b64 s[28:29], s[22:23]
Mvvv 100,34,160
ds_write_b128 v13, a[8:11] offset:2048
Mvvv 104,34,164
s_mov_b32 s30, 1638400
R_ v20,a80
s_mul_i32 s52, s4, 10240
Mvvv 108,34,168
ds_write_b128 v13, a[12:15] offset:3072
Mvvv 112,34,172
W_ 6
R_ v21,a81
R_ v22,a82
Mvvv 96,38,176
Dv 34,v1,45056
R_ v23,a83
Mvvv 100,38,180
ds_write_b128 v13, a[16:19] offset:4096
Mvvv 104,38,184
R_ v24,a84
R_ v25,a85
Mvvv 108,38,188
ds_write_b128 v13, a[40:43] offset:5120
Mvvv 112,38,192
W_ 8
R_ v26,a86
R_ v27,a87
Mvvv 96,42,196
Dv 38,%0,36864
R_ v52,a88
Mvvv 100,42,200
ds_write_b128 v13, a[44:47] offset:6144
Mvvv 104,42,204
R_ v53,a89
R_ v54,a90
Mvvv 108,42,208
ds_write_b128 v13, a[48:51] offset:7168
Mvvv 112,42,212
W_ 8
R_ v55,a91
v_accvgpr_mov_b32 a240, a92
Mvvv 96,30,216
Dv 42,v1,36864
v_accvgpr_mov_b32 a241, a93
Mvvv 100,30,220
ds_write_b128 v13, a[52:55] offset:8192
Mvvv 104,30,224
v_accvgpr_mov_b32 a242, a94
v_accvgpr_mov_b32 a243, a95
Mvvv 108,30,228
ds_write_b128 v13, a[56:59] offset:9216
Mvvv 112,30,232
W_ 8
v_accvgpr_mov_b32 a244, a96
v_accvgpr_mov_b32 a245, a97
Mvvv 96,34,236
Dv 30,%0,37888
v_accvgpr_mov_b32 a246, a98
Mvvv 100,34,240
v_accvgpr_mov_b32 a247, a99
R_ v16,a120
Mvvv 104,34,244
R_ v17,a121
R_ v18,a122
Mvvv 108,34,248
R_ v19,a123
R_ v28,a124
Mvvv 112,34,252
W_ 6
R_ v29,a125
R_ v51,a126
Zvvv 76,38,136
Dv 34,v1,37888
R_ v6,a127
Zvvv 80,38,140
R_ v14,a128
R_ v15,a129
Zvvv 84,38,144
v_accvgpr_mov_b32 a249, a130
v_accvgpr_mov_b32 a250, a131
Zvvv 88,38,148
v_max3_f32 v56, v96, v97, v98
v_max_f32 v56, v56, v99
Zvvv 92,38,152
W_ 4
v_max3_f32 v60, v100, v101, v102
v_max_f32 v60, v60, v103
Mvvv 76,42,156
Dv 38,%0,38912
v_max3_f32 v64, v104, v105, v106
Mvvv 80,42,160
v_max_f32 v64, v64, v107
Mvvv 84,42,164
v_max3_f32 v68, v108, v109, v110
v_max_f32 v68, v68, v111
Mvvv 88,42,168
v_max3_f32 v72, v112, v113, v114
v_max_f32 v72, v72, v115
Mvvv 92,42,172
W_ 2
Mvvv 76,30,176
Dv 42,v1,38912
Mvvv 80,30,180
Mvvv 84,30,184
Mvvv 88,30,188
Mvvv 92,30,192
s_waitcnt vmcnt(34) lgkmcnt(0)
s_barrier
Mvvv 76,34,196
Dv 30,%0,55296
Mvvv 76,38,216
Mvvv 76,42,236
Mvvv 80,34,200
Mvvv 80,38,220
Mvvv 80,42,240
Mvvv 84,34,204
s_nop 3
v_max3_f32 v56, v56, v76, v77
v_max3_f32 v56, v56, v78, v79
Mvvv 84,38,224
Mvvv 84,42,244
Mvvv 88,34,208
v_max3_f32 v60, v60, v80, v81
v_max3_f32 v60, v60, v82, v83
Mvvv 88,38,228
Mvvv 88,42,248
Mvvv 92,34,212
s_nop 1
v_max3_f32 v64, v64, v84, v85
v_max3_f32 v64, v64, v86, v87
Mvvv 92,38,232
Mvvv 92,42,252
Dv 34,v1,55296
.p2align 3
v_mov_b32 v11, 0x3f803f80
v_permlane32_swap_b32 v56, v60
v_max3_f32 v68, v68, v88, v89
v_max3_f32 v68, v68, v90, v91
v_max_f32 v56, v56, v60
s_nop 1
v_max3_f32 v72, v72, v92, v93
v_max3_f32 v72, v72, v94, v95
v_mov_b32 v73, v72
v_permlane32_swap_b32 v64, v68
v_max_f32 v64, v64, v68
v_permlane32_swap_b32 v72, v73
v_max_f32 v72, v72, v73
v_mov_b32 v73, v72
v_permlane16_swap_b32 v56, v64
v_max_f32 v56, v56, v64
v_permlane16_swap_b32 v72, v73
B_ v56,0xc0e00000,v56
v_max_f32 v72, v72, v73
v_mov_b32 v64, v56
B_ v72,0xc0e00000,v72
v_mov_b32 v73, v72
v_permlane16_swap_b32 v56, v64
v_mov_b32 v60, v56
v_mov_b32 v68, v64
v_mov_b64 v[74:75], v[72:73]
v_permlane32_swap_b32 v56, v60
v_permlane32_swap_b32 v64, v68
v_mov_b32 v57, v56
v_mov_b32 v61, v60
v_mov_b32 v65, v64
v_mov_b32 v69, v68
v_pk_add_f32 v[96:97], v[96:97], v[56:57]
v_pk_add_f32 v[98:99], v[98:99], v[56:57]
v_pk_add_f32 v[76:77], v[76:77], v[56:57]
v_pk_add_f32 v[78:79], v[78:79], v[56:57]
v_mov_b64 v[58:59], v[56:57]
v_pk_add_f32 v[100:101], v[100:101], v[60:61]
v_pk_add_f32 v[102:103], v[102:103], v[60:61]
v_pk_add_f32 v[80:81], v[80:81], v[60:61]
v_pk_add_f32 v[82:83], v[82:83], v[60:61]
v_mov_b64 v[62:63], v[60:61]
v_pk_add_f32 v[104:105], v[104:105], v[64:65]
v_pk_add_f32 v[106:107], v[106:107], v[64:65]
v_pk_add_f32 v[84:85], v[84:85], v[64:65]
v_pk_add_f32 v[86:87], v[86:87], v[64:65]
v_mov_b64 v[66:67], v[64:65]
v_pk_add_f32 v[108:109], v[108:109], v[68:69]
v_pk_add_f32 v[110:111], v[110:111], v[68:69]
v_pk_add_f32 v[88:89], v[88:89], v[68:69]
v_pk_add_f32 v[90:91], v[90:91], v[68:69]
v_mov_b64 v[70:71], v[68:69]
v_pk_add_f32 v[112:113], v[112:113], v[72:73]
v_pk_add_f32 v[114:115], v[114:115], v[72:73]
v_pk_add_f32 v[92:93], v[92:93], v[72:73]
v_pk_add_f32 v[94:95], v[94:95], v[72:73]
E_ 96
E_ 97
E_ 98
E_ 99
E_ 76
E_ 77
E_ 78
E_ 79
C_ 99,98,99
C_ 98,96,97
C_ 96,76,77
C_ 97,78,79
E_ 100
E_ 101
E_ 102
E_ 103
E_ 80
E_ 81
E_ 82
E_ 83
C_ 103,102,103
C_ 102,100,101
C_ 100,80,81
C_ 101,82,83
.Lstep2_1:
W_ 1
Nvvv 116,30,136,56
s_nop 0
Dv 38,%0,56320
Dv 42,v1,56320
W_ 2
Mvvv 116,34,156
E_ 104
Nvvv 120,30,140,60
E_ 105
Mvvv 120,34,160
E_ 106
Nvvv 124,30,144,64
E_ 107
Mvvv 124,34,164
E_ 84
Nvvv 128,30,148,68
E_ 85
Mvvv 128,34,168
E_ 86
Nvvv 132,30,152,72
E_ 87
Mvvv 132,34,172
W_ 0
C_ 107,106,107
C_ 106,104,105
Mvvv 132,38,192
s_nop 0
Dv 30,%0,57344
Dv 34,v1,57344
Mvvv 132,42,212
C_ 104,84,85
C_ 105,86,87
Mvvv 128,38,188
E_ 108
Mvvv 128,42,208
E_ 109
Mvvv 124,38,184
E_ 110
Mvvv 124,42,204
E_ 111
Mvvv 120,38,180
E_ 88
Mvvv 120,42,200
E_ 89
Mvvv 116,38,176
E_ 90
Mvvv 116,42,196
W_ 0
E_ 91
Mvvv 116,30,216
s_nop 0
Dv 38,%0,49152
Dv 42,v1,49152
Mvvv 116,34,236
C_ 111,110,111
C_ 110,108,109
Mvvv 120,30,220
C_ 108,88,89
C_ 109,90,91
Mvvv 120,34,240
E_ 112
Mvvv 124,30,224
E_ 113
Mvvv 124,34,244
E_ 114
Mvvv 128,30,228
E_ 115
Mvvv 128,34,248
E_ 92
Mvvv 132,30,232
E_ 93
Mvvv 132,34,252
W_ 0
E_ 94
Nvvv 76,38,136,56
s_nop 0
Dv 30,%0,50176
Dv 34,v1,50176
Mvvv 76,42,156
E_ 95
Nvvv 80,38,140,60
C_ 115,114,115
C_ 114,112,113
Mvvv 80,42,160
C_ 112,92,93
C_ 113,94,95
Nvvv 84,38,144,64
E_ 116
Mvvv 84,42,164
E_ 117
Nvvv 88,38,148,68
E_ 118
Mvvv 88,42,168
E_ 119
Nvvv 92,38,152,72
C_ 119,118,119
C_ 118,116,117
Mvvv 92,42,172
W_ 0
E_ 120
Mvvv 92,30,192
s_nop 0
Dv 38,%0,51200
Dv 42,v1,51200
Mvvv 92,34,212
X_ a180,v11
X_ a181,v11
Mvvv 88,30,188
X_ a182,v11
X_ a183,v11
Mvvv 88,34,208
ds_write_b128 v12, a[132:135] offset:0
Mvvv 84,30,184
E_ 121
Mvvv 84,34,204
E_ 122
Mvvv 80,30,180
ds_write_b128 v12, a[136:139] offset:1024
Mvvv 80,34,200
E_ 123
Mvvv 76,30,176
C_ 123,122,123
C_ 122,120,121
Mvvv 76,34,196
ds_write_b128 v12, a[160:163] offset:2048
W_ 4
Mvvv 76,38,216
W_ 3
E_ 124
Mvvv 76,42,236
s_waitcnt vmcnt(12) lgkmcnt(0)
s_barrier
Mvvv 80,38,220
ds_write_b128 v12, a[164:167] offset:3072
Mvvv 80,42,240
E_ 125
Mvvv 84,38,224
E_ 126
Mvvv 84,42,244
s_nop 0
Tv 30,v2,24576
Tv 32,v2,30720
Mvvv 88,38,228
ds_write_b128 v12, a[168:171] offset:4096
Mvvv 88,42,248
s_nop 0
Tv 34,v3,24576
Tv 36,v3,30720
Mvvv 92,38,232
E_ 127
Mvvv 92,42,252
ds_write_b128 v12, a[172:175] offset:5120
Zaav 220,180,96
C_ 127,126,127
C_ 126,124,125
Zaav 224,180,100
E_ 128
Zaav 228,180,104
ds_write_b128 v12, a[176:179] offset:12288
Zaav 232,180,108
E_ 129
Zaav 236,180,112
W_ 2
E_ 130
Zavv 0,30,96
s_nop 0
Tv 38,v4,24576
Tv 40,v4,30720
Zavv 20,34,96
ds_write_b128 v12, a[200:203] offset:13312
Zavv 24,34,100
s_nop 0
Tv 42,v5,24576
Tv 44,v5,30720
Zavv 4,30,100
E_ 131
Zavv 8,30,104
ds_write_b128 v12, a[204:207] offset:14336
Zavv 28,34,104
R_ v46,a220
R_ v47,a224
Zavv 32,34,108
R_ v48,a228
R_ v49,a232
Zavv 12,30,108
R_ v50,a236
C_ 131,130,131
Zavv 16,30,112
ds_write_b128 v12, a[208:211] offset:15360
Zavv 36,34,112
W_ 2
C_ 130,128,129
Zavv 56,38,112
s_nop 0
Tv 30,v2,25600
Tv 32,v2,31744
Zavv 76,42,112
ds_write_b128 v12, a[212:215] offset:16384
Zavv 72,42,108
s_nop 0
Tv 34,v3,25600
Tv 36,v3,31744
Zavv 52,38,108
E_ 132
Zavv 48,38,104
ds_write_b128 v12, a[216:219] offset:17408
Zavv 68,42,104
E_ 133
Zavv 64,42,100
E_ 134
Zavv 44,38,100
E_ 135
Zavv 40,38,96
C_ 135,134,135
C_ 134,132,133
Zavv 60,42,96
W_ 1
E_ 76
Zavv 80,30,96
s_nop 0
Tv 38,v4,25600
Tv 40,v4,31744
Zavv 100,34,96
E_ 77
Zavv 104,34,100
s_nop 0
Tv 42,v5,25600
Tv 44,v5,31744
Zavv 84,30,100
E_ 78
Zavv 88,30,104
E_ 79
Zavv 108,34,104
C_ 116,76,77
C_ 117,78,79
Zavv 112,34,108
E_ 80
Zavv 92,30,108
E_ 81
Zavv 96,30,112
E_ 82
Zavv 116,34,112
W_ 0
E_ 83
Zavv 136,38,112
s_nop 0
Tv 30,v2,26624
Tv 32,v2,32768
Zavv 156,42,112
C_ 120,80,81
C_ 121,82,83
Zavv 152,42,108
s_nop 0
Tv 34,v3,26624
Tv 36,v3,32768
Zavv 132,38,108
E_ 84
Zavv 128,38,104
E_ 85
Zavv 148,42,104
E_ 86
Zavv 144,42,100
E_ 87
Zavv 124,38,100
C_ 124,84,85
C_ 125,86,87
Zavv 120,38,96
E_ 88
Zavv 140,42,96
W_ 0
E_ 89
Zavv 160,30,96
s_nop 0
Tv 38,v4,26624
Tv 40,v4,32768
Zavv 180,34,96
E_ 90
Zavv 184,34,100
s_nop 0
Tv 42,v5,26624
Tv 44,v5,32768
Zavv 164,30,100
E_ 91
Zavv 168,30,104
C_ 128,88,89
C_ 129,90,91
Zavv 188,34,104
S_ s65,s61,36864
E_ 92
Zavv 192,34,108
S_ m0,s51,24576
E_ 93
Zavv 172,30,108
s_waitcnt vmcnt(6) lgkmcnt(0)
s_barrier
Zavv 176,30,112
Ln v7,32,s65,0
Zavv 196,34,112
E_ 94
Zavv 216,38,112
Dv 30,%0,6144
Zavv 236,42,112
Dv 34,v1,6144
Zavv 232,42,108
Ln v8,32,s65,3072
S_ m0,s51,25472
Zavv 212,38,108
E_ 95
Zavv 208,38,104
Ln v7,32,s65,128
Zavv 228,42,104
C_ 132,92,93
C_ 133,94,95
Zavv 224,42,100
Ln v8,32,s65,3200
S_ m0,s51,26368
Zavv 204,38,100
v_mov_b32 v92, 0x3f803f80
v_mov_b32 v93, 0x3f803f80
Zavv 200,38,96
Ln v7,32,s65,256
Zavv 220,42,96
v_mov_b32 v94, 0x3f803f80
v_mov_b32 v95, 0x3f803f80
Ln v8,32,s65,3328
.Lstep2_2:
s_nop 0
Zvvv 76,92,116
Dv 38,%0,7168
Zvvv 80,92,120
Dv 42,v1,7168
Zvvv 84,92,124
Zvvv 88,92,128
Zvvv 92,92,132
W_ 2
Nvvv 96,30,136,56
Mvvv 96,34,156
Nvvv 100,30,140,60
s_nop 1
v_max3_f32 v10, v76, v80, v84
s_nop 0
v_max3_f32 v10, v10, v88, v92
Mvvv 100,34,160
v_cmp_lt_f32 vcc, 0x42000000, v10
s_cbranch_vccnz .Ltramp2_1
Nvvv 104,30,144,64
A_ v46,v46,v76
A_ v47,v47,v80
Mvvv 104,34,164
A_ v48,v48,v84
A_ v49,v49,v88
Nvvv 108,30,148,68
A_ v50,v50,v92
Mvvv 108,34,168
Nvvv 112,30,152,72
Mvvv 112,34,172
W_ 0
Mvvv 112,38,192
Dv 30,%0,8192
Mvvv 112,42,212
Dv 34,v1,8192
Mvvv 108,38,188
Mvvv 108,42,208
Mvvv 104,38,184
Mvvv 104,42,204
Mvvv 100,38,180
Mvvv 100,42,200
Mvvv 96,38,176
Mvvv 96,42,196
W_ 0
Mvvv 96,30,216
Dv 38,%0,0
Mvvv 96,34,236
Dv 42,v1,0
Mvvv 100,30,220
Mvvv 100,34,240
Mvvv 104,30,224
Mvvv 104,34,244
Mvvv 108,30,228
Mvvv 108,34,248
s_nop 0
E_ 96
Mvvv 112,30,232
E_ 97
Mvvv 112,34,252
W_ 0
E_ 98
Nvvv 76,38,136,56
Dv 30,%0,1024
Mvvv 76,42,156
Dv 34,v1,1024
Nvvv 80,38,140,60
E_ 99
Mvvv 80,42,160
C_ 99,98,99
C_ 98,96,97
Nvvv 84,38,144,64
E_ 100
Mvvv 84,42,164
E_ 101
Nvvv 88,38,148,68
E_ 102
Mvvv 88,42,168
E_ 103
Nvvv 92,38,152,72
C_ 103,102,103
C_ 102,100,101
Mvvv 92,42,172
W_ 0
E_ 104
Mvvv 92,30,192
Dv 38,%0,2048
Mvvv 92,34,212
Dv 42,v1,2048
Mvvv 88,30,188
E_ 105
Mvvv 88,34,208
E_ 106
Mvvv 84,30,184
S_ s65,s61,24576
E_ 107
Mvvv 84,34,204
S_ m0,s51,0
C_ 107,106,107
C_ 106,104,105
Mvvv 80,30,180
s_waitcnt vmcnt(6) lgkmcnt(0)
s_barrier
Mvvv 80,34,200
L_ v9,36,s65,0
Mvvv 76,30,176
E_ 108
Mvvv 76,34,196
L_ v9,36,s65,3072
S_ m0,s51,896
Mvvv 76,38,216
Tv 30,v2,12288
Tv 32,v2,18432
Mvvv 76,42,236
L_ v9,36,s65,128
Mvvv 80,38,220
s_nop 0
Tv 34,v3,12288
Tv 36,v3,18432
Mvvv 80,42,240
L_ v9,36,s65,3200
S_ m0,s51,1792
Mvvv 84,38,224
E_ 109
Mvvv 84,42,244
L_ v9,36,s65,256
Mvvv 88,38,228
E_ 110
Mvvv 88,42,248
L_ v9,36,s65,3328
Mvvv 92,38,232
E_ 111
Mvvv 92,42,252
W_ 0
C_ 111,110,111
C_ 110,108,109
Mavv 0,30,116
s_nop 0
Tv 38,v4,12288
Tv 40,v4,18432
Mavv 20,34,116
E_ 112
Mavv 24,34,120
s_nop 0
Tv 42,v5,12288
Tv 44,v5,18432
Mavv 4,30,120
E_ 113
Mavv 8,30,124
E_ 114
Mavv 28,34,124
E_ 115
Mavv 32,34,128
C_ 115,114,115
C_ 114,112,113
Mavv 12,30,128
E_ 76
Mavv 16,30,132
E_ 77
Mavv 36,34,132
W_ 0
E_ 78
Mavv 56,38,132
s_nop 0
Tv 30,v2,13312
Tv 32,v2,19456
Mavv 76,42,132
E_ 79
Mavv 72,42,128
s_nop 0
Tv 34,v3,13312
Tv 36,v3,19456
Mavv 52,38,128
C_ 96,76,77
C_ 97,78,79
Mavv 48,38,124
E_ 80
Mavv 68,42,124
E_ 81
Mavv 64,42,120
E_ 82
Mavv 44,38,120
E_ 83
Mavv 40,38,116
C_ 100,80,81
C_ 101,82,83
Mavv 60,42,116
W_ 0
E_ 84
Mavv 80,30,116
s_nop 0
Tv 38,v4,13312
Tv 40,v4,19456
Mavv 100,34,116
E_ 85
Mavv 104,34,120
s_nop 0
Tv 42,v5,13312
Tv 44,v5,19456
Mavv 84,30,120
E_ 86
Mavv 88,30,124
E_ 87
Mavv 108,34,124
C_ 104,84,85
C_ 105,86,87
Mavv 112,34,128
E_ 88
Mavv 92,30,128
E_ 89
Mavv 96,30,132
E_ 90
Mavv 116,34,132
W_ 0
E_ 91
Mavv 136,38,132
s_nop 0
Tv 30,v2,14336
Tv 32,v2,20480
Mavv 156,42,132
C_ 108,88,89
C_ 109,90,91
Mavv 152,42,128
s_nop 0
Tv 34,v3,14336
Tv 36,v3,20480
Mavv 132,38,128
E_ 92
Mavv 128,38,124
E_ 93
Mavv 148,42,124
E_ 94
Mavv 144,42,120
E_ 95
Mavv 124,38,120
C_ 112,92,93
C_ 113,94,95
Mavv 120,38,116
v_mov_b32 v92, 0x3f803f80
v_mov_b32 v93, 0x3f803f80
Mavv 140,42,116
W_ 0
v_mov_b32 v94, 0x3f803f80
v_mov_b32 v95, 0x3f803f80
Mavv 160,30,116
s_nop 0
Tv 38,v4,14336
Tv 40,v4,20480
Mavv 180,34,116
Mavv 184,34,120
s_nop 0
Tv 42,v5,14336
Tv 44,v5,20480
Mavv 164,30,120
Mavv 168,30,124
Mavv 188,34,124
S_ s65,s61,49152
Mavv 192,34,128
S_ m0,s51,12288
Mavv 172,30,128
s_waitcnt vmcnt(6) lgkmcnt(0)
s_barrier
Mavv 176,30,132
Ln v7,32,s65,0
Mavv 196,34,132
Mavv 216,38,132
Dv 30,%0,30720
Mavv 236,42,132
Dv 34,v1,30720
Mavv 232,42,128
Ln v8,32,s65,3072
S_ m0,s51,13184
Mavv 212,38,128
Mavv 208,38,124
Ln v7,32,s65,128
Mavv 228,42,124
Mavv 224,42,120
Ln v8,32,s65,3200
S_ m0,s51,14080
Mavv 204,38,120
Mavv 200,38,116
Ln v7,32,s65,256
Mavv 220,42,116
s_cmp_eq_u32 s4, 0
s_cbranch_scc1 .Lhs_a2
.Lhs_a2_back:
.Lstep2_3:
Zvvv 76,92,96
Dv 38,%0,31744
Zvvv 80,92,100
Dv 42,v1,31744
Zvvv 84,92,104
Zvvv 88,92,108
Zvvv 92,92,112
W_ 2
Nvvv 116,30,136,56
Mvvv 116,34,156
Nvvv 120,30,140,60
s_nop 1
v_max3_f32 v10, v76, v80, v84
s_nop 0
v_max3_f32 v10, v10, v88, v92
Mvvv 120,34,160
v_cmp_lt_f32 vcc, 0x42000000, v10
s_cbranch_vccnz .Ltramp2_2
Nvvv 124,30,144,64
A_ v46,v46,v76
A_ v47,v47,v80
Mvvv 124,34,164
A_ v48,v48,v84
A_ v49,v49,v88
Nvvv 128,30,148,68
A_ v50,v50,v92
Mvvv 128,34,168
Ln v8,32,s65,3328
Nvvv 132,30,152,72
Mvvv 132,34,172
W_ 0
Mvvv 132,38,192
Dv 30,%0,32768
Mvvv 132,42,212
Dv 34,v1,32768
Mvvv 128,38,188
Mvvv 128,42,208
Mvvv 124,38,184
Mvvv 124,42,204
Mvvv 120,38,180
Mvvv 120,42,200
Mvvv 116,38,176
Mvvv 116,42,196
W_ 0
Mvvv 116,30,216
Dv 38,%0,24576
Mvvv 116,34,236
Dv 42,v1,24576
Mvvv 120,30,220
Mvvv 120,34,240
Mvvv 124,30,224
Mvvv 124,34,244
Mvvv 128,30,228
Mvvv 128,34,248
s_nop 0
E_ 116
Mvvv 132,30,232
E_ 117
Mvvv 132,34,252
W_ 0
E_ 118
Nvvv 76,38,136,56
Dv 30,%0,25600
Mvvv 76,42,156
Dv 34,v1,25600
Nvvv 80,38,140,60
E_ 119
Mvvv 80,42,160
C_ 119,118,119
C_ 118,116,117
Nvvv 84,38,144,64
E_ 120
Mvvv 84,42,164
E_ 121
Nvvv 88,38,148,68
E_ 122
Mvvv 88,42,168
E_ 123
Nvvv 92,38,152,72
C_ 123,122,123
C_ 122,120,121
Mvvv 92,42,172
W_ 0
E_ 124
Mvvv 92,30,192
Dv 38,%0,26624
Mvvv 92,34,212
Dv 42,v1,26624
Mvvv 88,30,188
E_ 125
Mvvv 88,34,208
E_ 126
Mvvv 84,30,184
S_ s65,s61,36864
E_ 127
Mvvv 84,34,204
S_ m0,s51,24576
C_ 127,126,127
C_ 126,124,125
Mvvv 80,30,180
s_waitcnt vmcnt(6) lgkmcnt(0)
s_barrier
Mvvv 80,34,200
L_ v9,36,s65,0
Mvvv 76,30,176
E_ 128
Mvvv 76,34,196
L_ v9,36,s65,3072
S_ m0,s51,25472
Mvvv 76,38,216
Tv 30,v2,0
Tv 32,v2,6144
Mvvv 76,42,236
L_ v9,36,s65,128
Mvvv 80,38,220
s_nop 0
Tv 34,v3,0
Tv 36,v3,6144
Mvvv 80,42,240
L_ v9,36,s65,3200
S_ m0,s51,26368
Mvvv 84,38,224
E_ 129
Mvvv 84,42,244
L_ v9,36,s65,256
Mvvv 88,38,228
E_ 130
Mvvv 88,42,248
L_ v9,36,s65,3328
Mvvv 92,38,232
E_ 131
Mvvv 92,42,252
W_ 0
C_ 131,130,131
C_ 130,128,129
Mavv 0,30,96
s_nop 0
Tv 38,v4,0
Tv 40,v4,6144
Mavv 20,34,96
E_ 132
Mavv 24,34,100
s_nop 0
Tv 42,v5,0
Tv 44,v5,6144
Mavv 4,30,100
E_ 133
Mavv 8,30,104
E_ 134
Mavv 28,34,104
E_ 135
Mavv 32,34,108
C_ 135,134,135
C_ 134,132,133
Mavv 12,30,108
E_ 76
Mavv 16,30,112
E_ 77
Mavv 36,34,112
W_ 0
E_ 78
Mavv 56,38,112
s_nop 0
Tv 30,v2,1024
Tv 32,v2,7168
Mavv 76,42,112
E_ 79
Mavv 72,42,108
s_nop 0
Tv 34,v3,1024
Tv 36,v3,7168
Mavv 52,38,108
C_ 116,76,77
C_ 117,78,79
Mavv 48,38,104
E_ 80
Mavv 68,42,104
E_ 81
Mavv 64,42,100
E_ 82
Mavv 44,38,100
E_ 83
Mavv 40,38,96
C_ 120,80,81
C_ 121,82,83
Mavv 60,42,96
W_ 0
E_ 84
Mavv 80,30,96
s_nop 0
Tv 38,v4,1024
Tv 40,v4,7168
Mavv 100,34,96
E_ 85
Mavv 104,34,100
s_nop 0
Tv 42,v5,1024
Tv 44,v5,7168
Mavv 84,30,100
E_ 86
Mavv 88,30,104
E_ 87
Mavv 108,34,104
C_ 124,84,85
C_ 125,86,87
Mavv 112,34,108
E_ 88
Mavv 92,30,108
E_ 89
Mavv 96,30,112
E_ 90
Mavv 116,34,112
W_ 0
E_ 91
Mavv 136,38,112
s_nop 0
Tv 30,v2,2048
Tv 32,v2,8192
Mavv 156,42,112
C_ 128,88,89
C_ 129,90,91
Mavv 152,42,108
s_nop 0
Tv 34,v3,2048
Tv 36,v3,8192
Mavv 132,38,108
E_ 92
Mavv 128,38,104
E_ 93
Mavv 148,42,104
E_ 94
Mavv 144,42,100
E_ 95
Mavv 124,38,100
C_ 132,92,93
C_ 133,94,95
Mavv 120,38,96
v_mov_b32 v92, 0x3f803f80
v_mov_b32 v93, 0x3f803f80
Mavv 140,42,96
W_ 0
v_mov_b32 v94, 0x3f803f80
v_mov_b32 v95, 0x3f803f80
Mavv 160,30,96
s_nop 0
Tv 38,v4,2048
Tv 40,v4,8192
Mavv 180,34,96
Mavv 184,34,100
s_nop 0
Tv 42,v5,2048
Tv 44,v5,8192
Mavv 164,30,100
Mavv 168,30,104
Mavv 188,34,104
S_ s65,s61,61440
Mavv 192,34,108
S_ m0,s51,0
Mavv 172,30,108
s_waitcnt vmcnt(6) lgkmcnt(0)
s_barrier
Mavv 176,30,112
Ln v7,32,s65,0
Mavv 196,34,112
Mavv 216,38,112
Dv 30,%0,18432
Mavv 236,42,112
Dv 34,v1,18432
Mavv 232,42,108
Ln v8,32,s65,3072
S_ m0,s51,896
Mavv 212,38,108
Mavv 208,38,104
Ln v7,32,s65,128
Mavv 228,42,104
Mavv 224,42,100
Ln v8,32,s65,3200
S_ m0,s51,1792
Mavv 204,38,100
Mavv 200,38,96
Ln v7,32,s65,256
Mavv 220,42,96
.Lstep2_4:
Zvvv 76,92,116
Dv 38,%0,19456
Zvvv 80,92,120
Dv 42,v1,19456
Zvvv 84,92,124
Zvvv 88,92,128
Zvvv 92,92,132
W_ 2
Nvvv 96,30,136,56
Mvvv 96,34,156
Nvvv 100,30,140,60
s_nop 1
v_max3_f32 v10, v76, v80, v84
s_nop 0
v_max3_f32 v10, v10, v88, v92
Mvvv 100,34,160
v_cmp_lt_f32 vcc, 0x42000000, v10
s_cbranch_vccnz .Ltramp2_3
Nvvv 104,30,144,64
A_ v46,v46,v76
A_ v47,v47,v80
Mvvv 104,34,164
A_ v48,v48,v84
A_ v49,v49,v88
Nvvv 108,30,148,68
A_ v50,v50,v92
Mvvv 108,34,168
Ln v8,32,s65,3328
Nvvv 112,30,152,72
Mvvv 112,34,172
W_ 0
Mvvv 112,38,192
Dv 30,%0,20480
Mvvv 112,42,212
Dv 34,v1,20480
Mvvv 108,38,188
Mvvv 108,42,208
Mvvv 104,38,184
Mvvv 104,42,204
Mvvv 100,38,180
Mvvv 100,42,200
Mvvv 96,38,176
Mvvv 96,42,196
W_ 0
Mvvv 96,30,216
Dv 38,%0,12288
Mvvv 96,34,236
Dv 42,v1,12288
Mvvv 100,30,220
Mvvv 100,34,240
Mvvv 104,30,224
Mvvv 104,34,244
Mvvv 108,30,228
Mvvv 108,34,248
s_nop 0
E_ 96
Mvvv 112,30,232
E_ 97
Mvvv 112,34,252
W_ 0
E_ 98
Nvvv 76,38,136,56
Dv 30,%0,13312
Mvvv 76,42,156
Dv 34,v1,13312
Nvvv 80,38,140,60
E_ 99
Mvvv 80,42,160
C_ 99,98,99
C_ 98,96,97
Nvvv 84,38,144,64
E_ 100
Mvvv 84,42,164
E_ 101
Nvvv 88,38,148,68
E_ 102
Mvvv 88,42,168
E_ 103
Nvvv 92,38,152,72
C_ 103,102,103
C_ 102,100,101
Mvvv 92,42,172
W_ 0
E_ 104
Mvvv 92,30,192
Dv 38,%0,14336
Mvvv 92,34,212
Dv 42,v1,14336
Mvvv 88,30,188
E_ 105
Mvvv 88,34,208
E_ 106
Mvvv 84,30,184
S_ s65,s61,49152
E_ 107
Mvvv 84,34,204
S_ m0,s51,12288
C_ 107,106,107
C_ 106,104,105
Mvvv 80,30,180
s_waitcnt vmcnt(6) lgkmcnt(0)
s_barrier
Mvvv 80,34,200
L_ v9,36,s65,0
Mvvv 76,30,176
E_ 108
Mvvv 76,34,196
L_ v9,36,s65,3072
S_ m0,s51,13184
Mvvv 76,38,216
Tv 30,v2,24576
Tv 32,v2,30720
Mvvv 76,42,236
L_ v9,36,s65,128
Mvvv 80,38,220
s_nop 0
Tv 34,v3,24576
Tv 36,v3,30720
Mvvv 80,42,240
L_ v9,36,s65,3200
S_ m0,s51,14080
Mvvv 84,38,224
E_ 109
Mvvv 84,42,244
L_ v9,36,s65,256
Mvvv 88,38,228
E_ 110
Mvvv 88,42,248
L_ v9,36,s65,3328
Mvvv 92,38,232
E_ 111
Mvvv 92,42,252
W_ 0
C_ 111,110,111
C_ 110,108,109
Mavv 0,30,116
s_nop 0
Tv 38,v4,24576
Tv 40,v4,30720
Mavv 20,34,116
E_ 112
Mavv 24,34,120
s_nop 0
Tv 42,v5,24576
Tv 44,v5,30720
Mavv 4,30,120
E_ 113
Mavv 8,30,124
E_ 114
Mavv 28,34,124
E_ 115
Mavv 32,34,128
C_ 115,114,115
C_ 114,112,113
Mavv 12,30,128
E_ 76
Mavv 16,30,132
E_ 77
Mavv 36,34,132
W_ 0
E_ 78
Mavv 56,38,132
s_nop 0
Tv 30,v2,25600
Tv 32,v2,31744
Mavv 76,42,132
E_ 79
Mavv 72,42,128
s_nop 0
Tv 34,v3,25600
Tv 36,v3,31744
Mavv 52,38,128
C_ 96,76,77
C_ 97,78,79
Mavv 48,38,124
E_ 80
Mavv 68,42,124
E_ 81
Mavv 64,42,120
E_ 82
Mavv 44,38,120
E_ 83
Mavv 40,38,116
C_ 100,80,81
C_ 101,82,83
Mavv 60,42,116
W_ 0
E_ 84
Mavv 80,30,116
s_nop 0
Tv 38,v4,25600
Tv 40,v4,31744
Mavv 100,34,116
E_ 85
Mavv 104,34,120
s_nop 0
Tv 42,v5,25600
Tv 44,v5,31744
Mavv 84,30,120
E_ 86
Mavv 88,30,124
E_ 87
Mavv 108,34,124
C_ 104,84,85
C_ 105,86,87
Mavv 112,34,128
E_ 88
Mavv 92,30,128
E_ 89
Mavv 96,30,132
E_ 90
Mavv 116,34,132
W_ 0
E_ 91
Mavv 136,38,132
s_nop 0
Tv 30,v2,26624
Tv 32,v2,32768
Mavv 156,42,132
C_ 108,88,89
C_ 109,90,91
Mavv 152,42,128
s_nop 0
Tv 34,v3,26624
Tv 36,v3,32768
Mavv 132,38,128
E_ 92
Mavv 128,38,124
E_ 93
Mavv 148,42,124
E_ 94
Mavv 144,42,120
E_ 95
Mavv 124,38,120
C_ 112,92,93
C_ 113,94,95
Mavv 120,38,116
v_mov_b32 v92, 0x3f803f80
v_mov_b32 v93, 0x3f803f80
Mavv 140,42,116
W_ 0
v_mov_b32 v94, 0x3f803f80
v_mov_b32 v95, 0x3f803f80
Mavv 160,30,116
s_nop 0
Tv 38,v4,26624
Tv 40,v4,32768
Mavv 180,34,116
Mavv 184,34,120
s_nop 0
Tv 42,v5,26624
Tv 44,v5,32768
Mavv 164,30,120
Mavv 168,30,124
Mavv 188,34,124
S_ s65,s61,73728
Mavv 192,34,128
S_ m0,s51,24576
Mavv 172,30,128
s_waitcnt vmcnt(6) lgkmcnt(0)
s_barrier
Mavv 176,30,132
Ln v7,32,s65,0
Mavv 196,34,132
Mavv 216,38,132
Dv 30,%0,6144
Mavv 236,42,132
Dv 34,v1,6144
Mavv 232,42,128
Ln v8,32,s65,3072
S_ m0,s51,25472
Mavv 212,38,128
Mavv 208,38,124
Ln v7,32,s65,128
Mavv 228,42,124
Mavv 224,42,120
Ln v8,32,s65,3200
S_ m0,s51,26368
Mavv 204,38,120
Mavv 200,38,116
Ln v7,32,s65,256
Mavv 220,42,116
.Lstep2_5:
Zvvv 76,92,96
Dv 38,%0,7168
Zvvv 80,92,100
Dv 42,v1,7168
Zvvv 84,92,104
Zvvv 88,92,108
Zvvv 92,92,112
W_ 2
Nvvv 116,30,136,56
Mvvv 116,34,156
Nvvv 120,30,140,60
s_nop 1
v_max3_f32 v10, v76, v80, v84
s_nop 0
v_max3_f32 v10, v10, v88, v92
Mvvv 120,34,160
v_cmp_lt_f32 vcc, 0x42000000, v10
s_cbranch_vccnz .Ltramp2_4
Nvvv 124,30,144,64
A_ v46,v46,v76
A_ v47,v47,v80
Mvvv 124,34,164
A_ v48,v48,v84
A_ v49,v49,v88
Nvvv 128,30,148,68
A_ v50,v50,v92
Mvvv 128,34,168
Ln v8,32,s65,3328
Nvvv 132,30,152,72
Mvvv 132,34,172
W_ 0
Mvvv 132,38,192
Dv 30,%0,8192
Mvvv 132,42,212
Dv 34,v1,8192
Mvvv 128,38,188
Mvvv 128,42,208
Mvvv 124,38,184
Mvvv 124,42,204
Mvvv 120,38,180
Mvvv 120,42,200
Mvvv 116,38,176
Mvvv 116,42,196
W_ 0
Mvvv 116,30,216
Dv 38,%0,0
Mvvv 116,34,236
Dv 42,v1,0
Mvvv 120,30,220
Mvvv 120,34,240
Mvvv 124,30,224
Mvvv 124,34,244
Mvvv 128,30,228
Mvvv 128,34,248
s_nop 0
E_ 116
Mvvv 132,30,232
E_ 117
Mvvv 132,34,252
W_ 0
E_ 118
Nvvv 76,38,136,56
Dv 30,%0,1024
Mvvv 76,42,156
Dv 34,v1,1024
Nvvv 80,38,140,60
E_ 119
Mvvv 80,42,160
C_ 119,118,119
C_ 118,116,117
Nvvv 84,38,144,64
E_ 120
Mvvv 84,42,164
E_ 121
Nvvv 88,38,148,68
E_ 122
Mvvv 88,42,168
E_ 123
Nvvv 92,38,152,72
C_ 123,122,123
C_ 122,120,121
Mvvv 92,42,172
W_ 0
E_ 124
Mvvv 92,30,192
Dv 38,%0,2048
Mvvv 92,34,212
Dv 42,v1,2048
Mvvv 88,30,188
E_ 125
Mvvv 88,34,208
E_ 126
Mvvv 84,30,184
S_ s65,s61,61440
E_ 127
Mvvv 84,34,204
S_ m0,s51,0
C_ 127,126,127
C_ 126,124,125
Mvvv 80,30,180
s_waitcnt vmcnt(6) lgkmcnt(0)
s_barrier
Mvvv 80,34,200
L_ v9,36,s65,0
Mvvv 76,30,176
E_ 128
Mvvv 76,34,196
L_ v9,36,s65,3072
S_ m0,s51,896
Mvvv 76,38,216
Tv 30,v2,12288
Tv 32,v2,18432
Mvvv 76,42,236
L_ v9,36,s65,128
Mvvv 80,38,220
s_nop 0
Tv 34,v3,12288
Tv 36,v3,18432
Mvvv 80,42,240
L_ v9,36,s65,3200
S_ m0,s51,1792
Mvvv 84,38,224
E_ 129
Mvvv 84,42,244
L_ v9,36,s65,256
Mvvv 88,38,228
E_ 130
Mvvv 88,42,248
L_ v9,36,s65,3328
Mvvv 92,38,232
E_ 131
Mvvv 92,42,252
W_ 0
C_ 131,130,131
C_ 130,128,129
Mavv 0,30,96
s_nop 0
Tv 38,v4,12288
Tv 40,v4,18432
Mavv 20,34,96
E_ 132
Mavv 24,34,100
)A"
R"A(s_nop 0
Tv 42,v5,12288
Tv 44,v5,18432
Mavv 4,30,100
E_ 133
Mavv 8,30,104
E_ 134
Mavv 28,34,104
E_ 135
Mavv 32,34,108
C_ 135,134,135
C_ 134,132,133
Mavv 12,30,108
E_ 76
Mavv 16,30,112
E_ 77
Mavv 36,34,112
W_ 0
E_ 78
Mavv 56,38,112
s_nop 0
Tv 30,v2,13312
Tv 32,v2,19456
Mavv 76,42,112
E_ 79
Mavv 72,42,108
s_nop 0
Tv 34,v3,13312
Tv 36,v3,19456
Mavv 52,38,108
C_ 116,76,77
C_ 117,78,79
Mavv 48,38,104
E_ 80
Mavv 68,42,104
E_ 81
Mavv 64,42,100
E_ 82
Mavv 44,38,100
E_ 83
Mavv 40,38,96
C_ 120,80,81
C_ 121,82,83
Mavv 60,42,96
W_ 0
E_ 84
Mavv 80,30,96
s_nop 0
Tv 38,v4,13312
Tv 40,v4,19456
Mavv 100,34,96
E_ 85
Mavv 104,34,100
s_nop 0
Tv 42,v5,13312
Tv 44,v5,19456
Mavv 84,30,100
E_ 86
Mavv 88,30,104
E_ 87
Mavv 108,34,104
C_ 124,84,85
C_ 125,86,87
Mavv 112,34,108
E_ 88
Mavv 92,30,108
E_ 89
Mavv 96,30,112
E_ 90
Mavv 116,34,112
W_ 0
E_ 91
Mavv 136,38,112
s_nop 0
Tv 30,v2,14336
Tv 32,v2,20480
Mavv 156,42,112
C_ 128,88,89
C_ 129,90,91
Mavv 152,42,108
s_nop 0
Tv 34,v3,14336
Tv 36,v3,20480
Mavv 132,38,108
E_ 92
Mavv 128,38,104
E_ 93
Mavv 148,42,104
E_ 94
Mavv 144,42,100
E_ 95
Mavv 124,38,100
C_ 132,92,93
C_ 133,94,95
Mavv 120,38,96
v_mov_b32 v92, 0x3f803f80
v_mov_b32 v93, 0x3f803f80
Mavv 140,42,96
W_ 0
v_mov_b32 v94, 0x3f803f80
v_mov_b32 v95, 0x3f803f80
Mavv 160,30,96
s_nop 0
Tv 38,v4,14336
Tv 40,v4,20480
Mavv 180,34,96
Mavv 184,34,100
s_nop 0
Tv 42,v5,14336
Tv 44,v5,20480
Mavv 164,30,100
Mavv 168,30,104
Mavv 188,34,104
S_ s65,s61,86016
Mavv 192,34,108
S_ m0,s51,12288
Mavv 172,30,108
s_waitcnt vmcnt(6) lgkmcnt(0)
s_barrier
Mavv 176,30,112
Ln v7,32,s65,0
Mavv 196,34,112
Mavv 216,38,112
Dv 30,%0,30720
Mavv 236,42,112
Dv 34,v1,30720
Mavv 232,42,108
Ln v8,32,s65,3072
S_ m0,s51,13184
Mavv 212,38,108
Mavv 208,38,104
Ln v7,32,s65,128
Mavv 228,42,104
Mavv 224,42,100
Ln v8,32,s65,3200
S_ m0,s51,14080
Mavv 204,38,100
Mavv 200,38,96
Ln v7,32,s65,256
Mavv 220,42,96
.Lstep2_6:
Zvvv 76,92,116
Dv 38,%0,31744
Zvvv 80,92,120
Dv 42,v1,31744
Zvvv 84,92,124
Zvvv 88,92,128
Zvvv 92,92,132
W_ 2
Nvvv 96,30,136,56
Mvvv 96,34,156
Nvvv 100,30,140,60
s_nop 1
v_max3_f32 v10, v76, v80, v84
s_nop 0
v_max3_f32 v10, v10, v88, v92
Mvvv 100,34,160
v_cmp_lt_f32 vcc, 0x42000000, v10
s_cbranch_vccnz .Ltramp2_5
Nvvv 104,30,144,64
A_ v46,v46,v76
A_ v47,v47,v80
Mvvv 104,34,164
A_ v48,v48,v84
A_ v49,v49,v88
Nvvv 108,30,148,68
A_ v50,v50,v92
Mvvv 108,34,168
Ln v8,32,s65,3328
Nvvv 112,30,152,72
R_ v11,a248
Mvvv 112,34,172
W_ 0
v_min_u32 v11, 240, v11
Mvvv 112,38,192
Dv 30,%0,32768
v_lshlrev_b32 v11, 3, v11
Mvvv 112,42,212
Dv 34,v1,32768
Mvvv 108,38,188
buffer_load_dword a254, v11, s[44:47], s66 offen
Mvvv 108,42,208
Mvvv 104,38,184
Mvvv 104,42,204
Mvvv 100,38,180
Mvvv 100,42,200
Mvvv 96,38,176
Mvvv 96,42,196
W_ 0
Mvvv 96,30,216
Dv 38,%0,24576
Mvvv 96,34,236
Dv 42,v1,24576
Mvvv 100,30,220
Mvvv 100,34,240
Mvvv 104,30,224
Mvvv 104,34,244
Mvvv 108,30,228
Mvvv 108,34,248
s_nop 0
E_ 96
Mvvv 112,30,232
E_ 97
Mvvv 112,34,252
W_ 0
E_ 98
Nvvv 76,38,136,56
Dv 30,%0,25600
Mvvv 76,42,156
Dv 34,v1,25600
Nvvv 80,38,140,60
E_ 99
Mvvv 80,42,160
C_ 99,98,99
C_ 98,96,97
Nvvv 84,38,144,64
E_ 100
Mvvv 84,42,164
E_ 101
Nvvv 88,38,148,68
E_ 102
Mvvv 88,42,168
E_ 103
Nvvv 92,38,152,72
C_ 103,102,103
C_ 102,100,101
Mvvv 92,42,172
W_ 0
E_ 104
Mvvv 92,30,192
Dv 38,%0,26624
Mvvv 92,34,212
Dv 42,v1,26624
Mvvv 88,30,188
E_ 105
Mvvv 88,34,208
E_ 106
Mvvv 84,30,184
S_ s65,s61,73728
E_ 107
Mvvv 84,34,204
S_ m0,s51,24576
C_ 107,106,107
C_ 106,104,105
Mvvv 80,30,180
s_waitcnt vmcnt(7) lgkmcnt(0)
s_barrier
Mvvv 80,34,200
L_ v9,36,s65,0
Mvvv 76,30,176
E_ 108
Mvvv 76,34,196
L_ v9,36,s65,3072
S_ m0,s51,25472
Mvvv 76,38,216
Tv 30,v2,0
Tv 32,v2,6144
Mvvv 76,42,236
L_ v9,36,s65,128
Mvvv 80,38,220
s_nop 0
Tv 34,v3,0
Tv 36,v3,6144
Mvvv 80,42,240
L_ v9,36,s65,3200
S_ m0,s51,26368
Mvvv 84,38,224
E_ 109
Mvvv 84,42,244
L_ v9,36,s65,256
Mvvv 88,38,228
E_ 110
Mvvv 88,42,248
L_ v9,36,s65,3328
Mvvv 92,38,232
E_ 111
Mvvv 92,42,252
W_ 0
C_ 111,110,111
C_ 110,108,109
Mavv 0,30,116
s_nop 0
Tv 38,v4,0
Tv 40,v4,6144
Mavv 20,34,116
E_ 112
Mavv 24,34,120
s_nop 0
Tv 42,v5,0
Tv 44,v5,6144
Mavv 4,30,120
E_ 113
Mavv 8,30,124
E_ 114
Mavv 28,34,124
E_ 115
Mavv 32,34,128
C_ 115,114,115
C_ 114,112,113
Mavv 12,30,128
E_ 76
Mavv 16,30,132
E_ 77
Mavv 36,34,132
W_ 0
E_ 78
Mavv 56,38,132
s_nop 0
Tv 30,v2,1024
Tv 32,v2,7168
Mavv 76,42,132
E_ 79
Mavv 72,42,128
s_nop 0
Tv 34,v3,1024
Tv 36,v3,7168
Mavv 52,38,128
C_ 96,76,77
C_ 97,78,79
Mavv 48,38,124
E_ 80
Mavv 68,42,124
E_ 81
Mavv 64,42,120
E_ 82
Mavv 44,38,120
E_ 83
Mavv 40,38,116
C_ 100,80,81
C_ 101,82,83
Mavv 60,42,116
W_ 0
E_ 84
Mavv 80,30,116
s_nop 0
Tv 38,v4,1024
Tv 40,v4,7168
Mavv 100,34,116
E_ 85
Mavv 104,34,120
s_nop 0
Tv 42,v5,1024
Tv 44,v5,7168
Mavv 84,30,120
E_ 86
Mavv 88,30,124
E_ 87
Mavv 108,34,124
C_ 104,84,85
C_ 105,86,87
Mavv 112,34,128
E_ 88
Mavv 92,30,128
E_ 89
Mavv 96,30,132
E_ 90
Mavv 116,34,132
W_ 0
E_ 91
Mavv 136,38,132
s_nop 0
Tv 30,v2,2048
Tv 32,v2,8192
Mavv 156,42,132
C_ 108,88,89
C_ 109,90,91
Mavv 152,42,128
s_nop 0
Tv 34,v3,2048
Tv 36,v3,8192
Mavv 132,38,128
E_ 92
Mavv 128,38,124
E_ 93
Mavv 148,42,124
E_ 94
Mavv 144,42,120
E_ 95
Mavv 124,38,120
C_ 112,92,93
C_ 113,94,95
Mavv 120,38,116
v_mov_b32 v92, 0x3f803f80
v_mov_b32 v93, 0x3f803f80
Mavv 140,42,116
W_ 0
v_mov_b32 v94, 0x3f803f80
v_mov_b32 v95, 0x3f803f80
Mavv 160,30,116
s_nop 0
Tv 38,v4,2048
Tv 40,v4,8192
Mavv 180,34,116
Mavv 184,34,120
s_nop 0
Tv 42,v5,2048
Tv 44,v5,8192
Mavv 164,30,120
Mavv 168,30,124
Mavv 188,34,124
S_ s65,s61,98304
Mavv 192,34,128
S_ m0,s51,0
Mavv 172,30,128
s_waitcnt vmcnt(7) lgkmcnt(0)
s_barrier
Mavv 176,30,132
Ln v7,32,s65,0
Mavv 196,34,132
Mavv 216,38,132
Dv 30,%0,18432
Mavv 236,42,132
Dv 34,v1,18432
Mavv 232,42,128
Ln v8,32,s65,3072
S_ m0,s51,896
Mavv 212,38,128
Mavv 208,38,124
Ln v7,32,s65,128
Mavv 228,42,124
Mavv 224,42,120
Ln v8,32,s65,3200
S_ m0,s51,1792
Mavv 204,38,120
Mavv 200,38,116
Ln v7,32,s65,256
Mavv 220,42,116
s_cmp_eq_u32 s4, 0
s_cbranch_scc1 .Lhs_c2
.Lhs_c2_back:
.Lstep2_7:
Zvvv 76,92,96
Dv 38,%0,19456
Zvvv 80,92,100
Dv 42,v1,19456
Zvvv 84,92,104
Zvvv 88,92,108
Zvvv 92,92,112
W_ 2
Nvvv 116,30,136,56
Mvvv 116,34,156
Nvvv 120,30,140,60
s_nop 1
v_max3_f32 v10, v76, v80, v84
s_nop 0
v_max3_f32 v10, v10, v88, v92
Mvvv 120,34,160
v_cmp_lt_f32 vcc, 0x42000000, v10
s_cbranch_vccnz .Ltramp2_6
Nvvv 124,30,144,64
A_ v46,v46,v76
A_ v47,v47,v80
Mvvv 124,34,164
A_ v48,v48,v84
A_ v49,v49,v88
Nvvv 128,30,148,68
A_ v50,v50,v92
Mvvv 128,34,168
Ln v8,32,s65,3328
Nvvv 132,30,152,72
Mvvv 132,34,172
W_ 0
Mvvv 132,38,192
Dv 30,%0,20480
Mvvv 132,42,212
Dv 34,v1,20480
Mvvv 128,38,188
Mvvv 128,42,208
Mvvv 124,38,184
Mvvv 124,42,204
Mvvv 120,38,180
Mvvv 120,42,200
Mvvv 116,38,176
Mvvv 116,42,196
W_ 0
Mvvv 116,30,216
Dv 38,%0,12288
Mvvv 116,34,236
Dv 42,v1,12288
Mvvv 120,30,220
Mvvv 120,34,240
Mvvv 124,30,224
Mvvv 124,34,244
Mvvv 128,30,228
Mvvv 128,34,248
s_nop 0
E_ 116
Mvvv 132,30,232
E_ 117
Mvvv 132,34,252
W_ 0
E_ 118
Nvvv 76,38,136,56
Dv 30,%0,13312
Mvvv 76,42,156
Dv 34,v1,13312
Nvvv 80,38,140,60
E_ 119
Mvvv 80,42,160
C_ 119,118,119
C_ 118,116,117
Nvvv 84,38,144,64
E_ 120
Mvvv 84,42,164
E_ 121
Nvvv 88,38,148,68
E_ 122
Mvvv 88,42,168
E_ 123
Nvvv 92,38,152,72
C_ 123,122,123
C_ 122,120,121
Mvvv 92,42,172
W_ 0
E_ 124
Mvvv 92,30,192
Dv 38,%0,14336
Mvvv 92,34,212
Dv 42,v1,14336
Mvvv 88,30,188
E_ 125
Mvvv 88,34,208
E_ 126
Mvvv 84,30,184
S_ s65,s61,86016
E_ 127
Mvvv 84,34,204
S_ m0,s51,12288
C_ 127,126,127
C_ 126,124,125
Mvvv 80,30,180
s_waitcnt vmcnt(6) lgkmcnt(0)
s_barrier
Mvvv 80,34,200
L_ v9,36,s65,0
Mvvv 76,30,176
E_ 128
Mvvv 76,34,196
L_ v9,36,s65,3072
S_ m0,s51,13184
Mvvv 76,38,216
Tv 30,v2,24576
Tv 32,v2,30720
Mvvv 76,42,236
L_ v9,36,s65,128
Mvvv 80,38,220
s_nop 0
Tv 34,v3,24576
Tv 36,v3,30720
Mvvv 80,42,240
L_ v9,36,s65,3200
S_ m0,s51,14080
Mvvv 84,38,224
E_ 129
Mvvv 84,42,244
L_ v9,36,s65,256
Mvvv 88,38,228
E_ 130
Mvvv 88,42,248
L_ v9,36,s65,3328
Mvvv 92,38,232
E_ 131
Mvvv 92,42,252
W_ 0
C_ 131,130,131
C_ 130,128,129
Mavv 0,30,96
s_nop 0
Tv 38,v4,24576
Tv 40,v4,30720
Mavv 20,34,96
E_ 132
Mavv 24,34,100
s_nop 0
Tv 42,v5,24576
Tv 44,v5,30720
Mavv 4,30,100
E_ 133
Mavv 8,30,104
E_ 134
Mavv 28,34,104
E_ 135
Mavv 32,34,108
C_ 135,134,135
C_ 134,132,133
Mavv 12,30,108
E_ 76
Mavv 16,30,112
E_ 77
Mavv 36,34,112
W_ 0
E_ 78
Mavv 56,38,112
s_nop 0
Tv 30,v2,25600
Tv 32,v2,31744
Mavv 76,42,112
E_ 79
Mavv 72,42,108
s_nop 0
Tv 34,v3,25600
Tv 36,v3,31744
Mavv 52,38,108
C_ 116,76,77
C_ 117,78,79
Mavv 48,38,104
E_ 80
Mavv 68,42,104
E_ 81
Mavv 64,42,100
E_ 82
Mavv 44,38,100
E_ 83
Mavv 40,38,96
C_ 120,80,81
C_ 121,82,83
Mavv 60,42,96
W_ 0
E_ 84
Mavv 80,30,96
s_nop 0
Tv 38,v4,25600
Tv 40,v4,31744
Mavv 100,34,96
E_ 85
Mavv 104,34,100
s_nop 0
Tv 42,v5,25600
Tv 44,v5,31744
Mavv 84,30,100
E_ 86
Mavv 88,30,104
E_ 87
Mavv 108,34,104
C_ 124,84,85
C_ 125,86,87
Mavv 112,34,108
E_ 88
Mavv 92,30,108
E_ 89
Mavv 96,30,112
E_ 90
Mavv 116,34,112
W_ 0
E_ 91
Mavv 136,38,112
s_nop 0
Tv 30,v2,26624
Tv 32,v2,32768
Mavv 156,42,112
C_ 128,88,89
C_ 129,90,91
Mavv 152,42,108
s_nop 0
Tv 34,v3,26624
Tv 36,v3,32768
Mavv 132,38,108
E_ 92
Mavv 128,38,104
E_ 93
Mavv 148,42,104
E_ 94
Mavv 144,42,100
E_ 95
Mavv 124,38,100
C_ 132,92,93
C_ 133,94,95
Mavv 120,38,96
v_mov_b32 v92, 0x3f803f80
v_mov_b32 v93, 0x3f803f80
Mavv 140,42,96
W_ 0
v_mov_b32 v94, 0x3f803f80
v_mov_b32 v95, 0x3f803f80
Mavv 160,30,96
s_nop 0
Tv 38,v4,26624
Tv 40,v4,32768
Mavv 180,34,96
Mavv 184,34,100
s_nop 0
Tv 42,v5,26624
Tv 44,v5,32768
Mavv 164,30,100
Mavv 168,30,104
Mavv 188,34,104
S_ s65,s61,110592
Mavv 192,34,108
S_ m0,s51,24576
Mavv 172,30,108
s_waitcnt vmcnt(6) lgkmcnt(0)
s_barrier
Mavv 176,30,112
Ln v7,32,s65,0
Mavv 196,34,112
Mavv 216,38,112
Dv 30,%0,6144
Mavv 236,42,112
Dv 34,v1,6144
Mavv 232,42,108
Ln v8,32,s65,3072
S_ m0,s51,25472
Mavv 212,38,108
Mavv 208,38,104
Ln v7,32,s65,128
Mavv 228,42,104
Mavv 224,42,100
Ln v8,32,s65,3200
S_ m0,s51,26368
Mavv 204,38,100
Mavv 200,38,96
Ln v7,32,s65,256
Mavv 220,42,96
.Lstep2_8:
Zvvv 76,92,116
Dv 38,%0,7168
Zvvv 80,92,120
Dv 42,v1,7168
Zvvv 84,92,124
Zvvv 88,92,128
Zvvv 92,92,132
W_ 2
Nvvv 96,30,136,56
Mvvv 96,34,156
Nvvv 100,30,140,60
s_nop 1
v_max3_f32 v10, v76, v80, v84
s_nop 0
v_max3_f32 v10, v10, v88, v92
Mvvv 100,34,160
v_cmp_lt_f32 vcc, 0x42000000, v10
s_cbranch_vccnz .Ltramp2_7
Nvvv 104,30,144,64
A_ v46,v46,v76
A_ v47,v47,v80
Mvvv 104,34,164
A_ v48,v48,v84
A_ v49,v49,v88
Nvvv 108,30,148,68
A_ v50,v50,v92
Mvvv 108,34,168
Ln v8,32,s65,3328
Nvvv 112,30,152,72
Mvvv 112,34,172
W_ 0
Mvvv 112,38,192
Dv 30,%0,8192
Mvvv 112,42,212
Dv 34,v1,8192
Mvvv 108,38,188
Mvvv 108,42,208
Mvvv 104,38,184
Mvvv 104,42,204
Mvvv 100,38,180
Mvvv 100,42,200
Mvvv 96,38,176
Mvvv 96,42,196
W_ 0
Mvvv 96,30,216
Dv 38,%0,0
Mvvv 96,34,236
Dv 42,v1,0
Mvvv 100,30,220
Mvvv 100,34,240
Mvvv 104,30,224
Mvvv 104,34,244
Mvvv 108,30,228
Mvvv 108,34,248
s_nop 0
E_ 96
Mvvv 112,30,232
E_ 97
Mvvv 112,34,252
W_ 0
E_ 98
Nvvv 76,38,136,56
Dv 30,%0,1024
Mvvv 76,42,156
Dv 34,v1,1024
Nvvv 80,38,140,60
E_ 99
Mvvv 80,42,160
C_ 99,98,99
C_ 98,96,97
Nvvv 84,38,144,64
E_ 100
Mvvv 84,42,164
E_ 101
Nvvv 88,38,148,68
E_ 102
Mvvv 88,42,168
E_ 103
Nvvv 92,38,152,72
C_ 103,102,103
C_ 102,100,101
Mvvv 92,42,172
W_ 0
E_ 104
Mvvv 92,30,192
Dv 38,%0,2048
Mvvv 92,34,212
Dv 42,v1,2048
Mvvv 88,30,188
E_ 105
Mvvv 88,34,208
E_ 106
Mvvv 84,30,184
S_ s65,s61,98304
E_ 107
Mvvv 84,34,204
S_ m0,s51,0
C_ 107,106,107
C_ 106,104,105
Mvvv 80,30,180
s_waitcnt vmcnt(6) lgkmcnt(0)
s_barrier
Mvvv 80,34,200
L_ v9,36,s65,0
Mvvv 76,30,176
E_ 108
Mvvv 76,34,196
L_ v9,36,s65,3072
S_ m0,s51,896
Mvvv 76,38,216
Tv 30,v2,12288
Tv 32,v2,18432
Mvvv 76,42,236
L_ v9,36,s65,128
Mvvv 80,38,220
s_nop 0
Tv 34,v3,12288
Tv 36,v3,18432
Mvvv 80,42,240
L_ v9,36,s65,3200
S_ m0,s51,1792
Mvvv 84,38,224
E_ 109
Mvvv 84,42,244
L_ v9,36,s65,256
Mvvv 88,38,228
E_ 110
Mvvv 88,42,248
L_ v9,36,s65,3328
Mvvv 92,38,232
E_ 111
Mvvv 92,42,252
W_ 0
C_ 111,110,111
C_ 110,108,109
Mavv 0,30,116
s_nop 0
Tv 38,v4,12288
Tv 40,v4,18432
Mavv 20,34,116
E_ 112
Mavv 24,34,120
s_nop 0
Tv 42,v5,12288
Tv 44,v5,18432
Mavv 4,30,120
E_ 113
Mavv 8,30,124
E_ 114
Mavv 28,34,124
E_ 115
Mavv 32,34,128
C_ 115,114,115
C_ 114,112,113
Mavv 12,30,128
E_ 76
Mavv 16,30,132
E_ 77
Mavv 36,34,132
W_ 0
E_ 78
Mavv 56,38,132
s_nop 0
Tv 30,v2,13312
Tv 32,v2,19456
Mavv 76,42,132
E_ 79
Mavv 72,42,128
s_nop 0
Tv 34,v3,13312
Tv 36,v3,19456
Mavv 52,38,128
C_ 96,76,77
C_ 97,78,79
Mavv 48,38,124
E_ 80
Mavv 68,42,124
E_ 81
Mavv 64,42,120
E_ 82
Mavv 44,38,120
E_ 83
Mavv 40,38,116
C_ 100,80,81
C_ 101,82,83
Mavv 60,42,116
W_ 0
E_ 84
Mavv 80,30,116
s_nop 0
Tv 38,v4,13312
Tv 40,v4,19456
Mavv 100,34,116
E_ 85
Mavv 104,34,120
s_nop 0
Tv 42,v5,13312
Tv 44,v5,19456
Mavv 84,30,120
E_ 86
Mavv 88,30,124
E_ 87
Mavv 108,34,124
C_ 104,84,85
C_ 105,86,87
Mavv 112,34,128
E_ 88
Mavv 92,30,128
E_ 89
Mavv 96,30,132
E_ 90
Mavv 116,34,132
W_ 0
E_ 91
Mavv 136,38,132
s_nop 0
Tv 30,v2,14336
Tv 32,v2,20480
Mavv 156,42,132
C_ 108,88,89
C_ 109,90,91
Mavv 152,42,128
s_nop 0
Tv 34,v3,14336
Tv 36,v3,20480
Mavv 132,38,128
E_ 92
Mavv 128,38,124
E_ 93
Mavv 148,42,124
E_ 94
Mavv 144,42,120
E_ 95
Mavv 124,38,120
C_ 112,92,93
C_ 113,94,95
Mavv 120,38,116
v_mov_b32 v92, 0x3f803f80
v_mov_b32 v93, 0x3f803f80
Mavv 140,42,116
W_ 0
v_mov_b32 v94, 0x3f803f80
v_mov_b32 v95, 0x3f803f80
Mavv 160,30,116
s_nop 0
Tv 38,v4,14336
Tv 40,v4,20480
Mavv 180,34,116
Mavv 184,34,120
s_nop 0
Tv 42,v5,14336
Tv 44,v5,20480
Mavv 164,30,120
Mavv 168,30,124
Mavv 188,34,124
S_ s65,s61,122880
Mavv 192,34,128
S_ m0,s51,12288
Mavv 172,30,128
s_waitcnt vmcnt(6) lgkmcnt(0)
s_barrier
Mavv 176,30,132
Ln v7,32,s65,0
Mavv 196,34,132
Mavv 216,38,132
Dv 30,%0,30720
Mavv 236,42,132
Dv 34,v1,30720
Mavv 232,42,128
Ln v8,32,s65,3072
S_ m0,s51,13184
Mavv 212,38,128
Mavv 208,38,124
Ln v7,32,s65,128
Mavv 228,42,124
Mavv 224,42,120
Ln v8,32,s65,3200
S_ m0,s51,14080
Mavv 204,38,120
Mavv 200,38,116
Ln v7,32,s65,256
Mavv 220,42,116
s_cmp_eq_u32 s4, 0
s_cbranch_scc1 .Lhs_d2
.Lhs_d2_back:
.Lstep2_9:
Zvvv 76,92,96
Dv 38,%0,31744
Zvvv 80,92,100
Dv 42,v1,31744
Zvvv 84,92,104
Zvvv 88,92,108
Zvvv 92,92,112
W_ 2
Nvvv 116,30,136,56
Mvvv 116,34,156
Nvvv 120,30,140,60
s_nop 1
v_max3_f32 v10, v76, v80, v84
s_nop 0
v_max3_f32 v10, v10, v88, v92
Mvvv 120,34,160
v_cmp_lt_f32 vcc, 0x42000000, v10
s_cbranch_vccnz .Ltramp2_8
Nvvv 124,30,144,64
A_ v46,v46,v76
A_ v47,v47,v80
Mvvv 124,34,164
A_ v48,v48,v84
A_ v49,v49,v88
Nvvv 128,30,148,68
A_ v50,v50,v92
Mvvv 128,34,168
Ln v8,32,s65,3328
Nvvv 132,30,152,72
Mvvv 132,34,172
W_ 0
Mvvv 132,38,192
Dv 30,%0,32768
Mvvv 132,42,212
Dv 34,v1,32768
Mvvv 128,38,188
Mvvv 128,42,208
Mvvv 124,38,184
Mvvv 124,42,204
Mvvv 120,38,180
Mvvv 120,42,200
Mvvv 116,38,176
Mvvv 116,42,196
W_ 0
Mvvv 116,30,216
Dv 38,%0,24576
Mvvv 116,34,236
Dv 42,v1,24576
Mvvv 120,30,220
Mvvv 120,34,240
Mvvv 124,30,224
Mvvv 124,34,244
Mvvv 128,30,228
Mvvv 128,34,248
s_nop 0
E_ 116
Mvvv 132,30,232
E_ 117
Mvvv 132,34,252
W_ 0
E_ 118
Nvvv 76,38,136,56
Dv 30,%0,25600
Mvvv 76,42,156
Dv 34,v1,25600
Nvvv 80,38,140,60
E_ 119
Mvvv 80,42,160
C_ 119,118,119
C_ 118,116,117
Nvvv 84,38,144,64
E_ 120
Mvvv 84,42,164
E_ 121
Nvvv 88,38,148,68
E_ 122
Mvvv 88,42,168
E_ 123
Nvvv 92,38,152,72
C_ 123,122,123
C_ 122,120,121
Mvvv 92,42,172
W_ 0
E_ 124
Mvvv 92,30,192
Dv 38,%0,26624
Mvvv 92,34,212
Dv 42,v1,26624
Mvvv 88,30,188
E_ 125
Mvvv 88,34,208
E_ 126
Mvvv 84,30,184
S_ s65,s61,110592
E_ 127
Mvvv 84,34,204
S_ m0,s51,24576
C_ 127,126,127
C_ 126,124,125
Mvvv 80,30,180
s_waitcnt vmcnt(6) lgkmcnt(0)
s_barrier
Mvvv 80,34,200
Ln v9,36,s65,0
Mvvv 76,30,176
E_ 128
Mvvv 76,34,196
Ln v9,36,s65,3072
S_ m0,s51,25472
Mvvv 76,38,216
Tv 30,v2,0
Tv 32,v2,6144
Mvvv 76,42,236
Ln v9,36,s65,128
Mvvv 80,38,220
s_nop 0
Tv 34,v3,0
Tv 36,v3,6144
Mvvv 80,42,240
Ln v9,36,s65,3200
S_ m0,s51,26368
Mvvv 84,38,224
E_ 129
Mvvv 84,42,244
Ln v9,36,s65,256
Mvvv 88,38,228
E_ 130
Mvvv 88,42,248
Ln v9,36,s65,3328
Mvvv 92,38,232
E_ 131
Mvvv 92,42,252
W_ 0
C_ 131,130,131
C_ 130,128,129
Mavv 0,30,96
s_nop 0
Tv 38,v4,0
Tv 40,v4,6144
Mavv 20,34,96
E_ 132
Mavv 24,34,100
s_nop 0
Tv 42,v5,0
Tv 44,v5,6144
Mavv 4,30,100
E_ 133
Mavv 8,30,104
E_ 134
Mavv 28,34,104
E_ 135
Mavv 32,34,108
C_ 135,134,135
C_ 134,132,133
Mavv 12,30,108
E_ 76
Mavv 16,30,112
E_ 77
Mavv 36,34,112
W_ 0
E_ 78
Mavv 56,38,112
s_nop 0
Tv 30,v2,1024
Tv 32,v2,7168
Mavv 76,42,112
E_ 79
Mavv 72,42,108
s_nop 0
Tv 34,v3,1024
Tv 36,v3,7168
Mavv 52,38,108
C_ 116,76,77
C_ 117,78,79
Mavv 48,38,104
E_ 80
Mavv 68,42,104
E_ 81
Mavv 64,42,100
E_ 82
Mavv 44,38,100
E_ 83
Mavv 40,38,96
C_ 120,80,81
C_ 121,82,83
Mavv 60,42,96
W_ 0
E_ 84
Mavv 80,30,96
s_nop 0
Tv 38,v4,1024
Tv 40,v4,7168
Mavv 100,34,96
E_ 85
Mavv 104,34,100
s_nop 0
Tv 42,v5,1024
Tv 44,v5,7168
Mavv 84,30,100
E_ 86
Mavv 88,30,104
E_ 87
Mavv 108,34,104
C_ 124,84,85
C_ 125,86,87
Mavv 112,34,108
E_ 88
Mavv 92,30,108
E_ 89
Mavv 96,30,112
E_ 90
Mavv 116,34,112
W_ 0
E_ 91
Mavv 136,38,112
s_nop 0
Tv 30,v2,2048
Tv 32,v2,8192
Mavv 156,42,112
C_ 128,88,89
C_ 129,90,91
Mavv 152,42,108
s_nop 0
Tv 34,v3,2048
Tv 36,v3,8192
Mavv 132,38,108
E_ 92
Mavv 128,38,104
E_ 93
Mavv 148,42,104
E_ 94
Mavv 144,42,100
E_ 95
Mavv 124,38,100
C_ 132,92,93
C_ 133,94,95
Mavv 120,38,96
v_mov_b32 v92, 0x3f803f80
v_mov_b32 v93, 0x3f803f80
Mavv 140,42,96
W_ 0
v_mov_b32 v94, 0x3f803f80
v_mov_b32 v95, 0x3f803f80
Mavv 160,30,96
s_nop 0
Tv 38,v4,2048
Tv 40,v4,8192
Mavv 180,34,96
Mavv 184,34,100
s_nop 0
Tv 42,v5,2048
Tv 44,v5,8192
Mavv 164,30,100
Mavv 168,30,104
Mavv 188,34,104
S_ s65,s61,135168
Mavv 192,34,108
S_ m0,s51,0
Mavv 172,30,108
s_waitcnt vmcnt(6) lgkmcnt(0)
s_barrier
Mavv 176,30,112
Ln v7,32,s65,0
Mavv 196,34,112
Mavv 216,38,112
Dv 30,%0,18432
Mavv 236,42,112
Dv 34,v1,18432
Mavv 232,42,108
Ln v8,32,s65,3072
S_ m0,s51,896
Mavv 212,38,108
Mavv 208,38,104
Ln v7,32,s65,128
Mavv 228,42,104
Mavv 224,42,100
Ln v8,32,s65,3200
S_ m0,s51,1792
Mavv 204,38,100
Mavv 200,38,96
Ln v7,32,s65,256
Mavv 220,42,96
.Lstep2_10:
Zvvv 76,92,116
Dv 38,%0,19456
Zvvv 80,92,120
Dv 42,v1,19456
Zvvv 84,92,124
Zvvv 88,92,128
Zvvv 92,92,132
W_ 2
Nvvv 96,30,136,56
Mvvv 96,34,156
Nvvv 100,30,140,60
s_nop 1
v_max3_f32 v10, v76, v80, v84
s_nop 0
v_max3_f32 v10, v10, v88, v92
Mvvv 100,34,160
v_cmp_lt_f32 vcc, 0x42000000, v10
s_cbranch_vccnz .Ltramp2_9
Nvvv 104,30,144,64
A_ v46,v46,v76
A_ v47,v47,v80
Mvvv 104,34,164
A_ v48,v48,v84
A_ v49,v49,v88
Nvvv 108,30,148,68
A_ v50,v50,v92
Mvvv 108,34,168
Ln v8,32,s65,3328
Nvvv 112,30,152,72
Mvvv 112,34,172
W_ 0
Mvvv 112,38,192
Dv 30,%0,20480
Mvvv 112,42,212
Dv 34,v1,20480
Mvvv 108,38,188
Mvvv 108,42,208
Mvvv 104,38,184
Mvvv 104,42,204
Mvvv 100,38,180
Mvvv 100,42,200
Mvvv 96,38,176
Mvvv 96,42,196
W_ 0
Mvvv 96,30,216
Dv 38,%0,12288
Mvvv 96,34,236
Dv 42,v1,12288
Mvvv 100,30,220
Mvvv 100,34,240
Mvvv 104,30,224
Mvvv 104,34,244
Mvvv 108,30,228
Mvvv 108,34,248
s_nop 0
E_ 96
Mvvv 112,30,232
E_ 97
Mvvv 112,34,252
W_ 0
E_ 98
Nvvv 76,38,136,56
Dv 30,%0,13312
Mvvv 76,42,156
Dv 34,v1,13312
Nvvv 80,38,140,60
E_ 99
Mvvv 80,42,160
C_ 99,98,99
C_ 98,96,97
Nvvv 84,38,144,64
E_ 100
Mvvv 84,42,164
E_ 101
Nvvv 88,38,148,68
E_ 102
Mvvv 88,42,168
E_ 103
Nvvv 92,38,152,72
C_ 103,102,103
C_ 102,100,101
Mvvv 92,42,172
W_ 0
E_ 104
Mvvv 92,30,192
Dv 38,%0,14336
Mvvv 92,34,212
Dv 42,v1,14336
Mvvv 88,30,188
E_ 105
Mvvv 88,34,208
E_ 106
Mvvv 84,30,184
S_ s65,s61,122880
E_ 107
Mvvv 84,34,204
S_ m0,s51,12288
C_ 107,106,107
C_ 106,104,105
Mvvv 80,30,180
s_waitcnt vmcnt(6) lgkmcnt(0)
s_barrier
Mvvv 80,34,200
Ln v9,36,s65,0
Mvvv 76,30,176
E_ 108
Mvvv 76,34,196
Ln v9,36,s65,3072
S_ m0,s51,13184
Mvvv 76,38,216
Tv 30,v2,24576
Tv 32,v2,30720
Mvvv 76,42,236
Ln v9,36,s65,128
Mvvv 80,38,220
s_nop 0
Tv 34,v3,24576
Tv 36,v3,30720
Mvvv 80,42,240
Ln v9,36,s65,3200
S_ m0,s51,14080
Mvvv 84,38,224
E_ 109
Mvvv 84,42,244
Ln v9,36,s65,256
Mvvv 88,38,228
E_ 110
Mvvv 88,42,248
Ln v9,36,s65,3328
Mvvv 92,38,232
E_ 111
Mvvv 92,42,252
W_ 0
C_ 111,110,111
C_ 110,108,109
Mavv 0,30,116
s_nop 0
Tv 38,v4,24576
Tv 40,v4,30720
Mavv 20,34,116
E_ 112
Mavv 24,34,120
s_nop 0
Tv 42,v5,24576
Tv 44,v5,30720
Mavv 4,30,120
E_ 113
Mavv 8,30,124
E_ 114
Mavv 28,34,124
E_ 115
Mavv 32,34,128
C_ 115,114,115
C_ 114,112,113
Mavv 12,30,128
E_ 76
Mavv 16,30,132
E_ 77
Mavv 36,34,132
W_ 0
E_ 78
Mavv 56,38,132
s_nop 0
Tv 30,v2,25600
Tv 32,v2,31744
Mavv 76,42,132
E_ 79
Mavv 72,42,128
s_nop 0
Tv 34,v3,25600
Tv 36,v3,31744
Mavv 52,38,128
C_ 96,76,77
C_ 97,78,79
Mavv 48,38,124
E_ 80
Mavv 68,42,124
E_ 81
Mavv 64,42,120
E_ 82
Mavv 44,38,120
E_ 83
Mavv 40,38,116
C_ 100,80,81
C_ 101,82,83
Mavv 60,42,116
W_ 0
E_ 84
Mavv 80,30,116
s_nop 0
Tv 38,v4,25600
Tv 40,v4,31744
Mavv 100,34,116
E_ 85
Mavv 104,34,120
s_nop 0
Tv 42,v5,25600
Tv 44,v5,31744
Mavv 84,30,120
E_ 86
Mavv 88,30,124
E_ 87
Mavv 108,34,124
C_ 104,84,85
C_ 105,86,87
Mavv 112,34,128
E_ 88
Mavv 92,30,128
E_ 89
Mavv 96,30,132
E_ 90
Mavv 116,34,132
W_ 0
E_ 91
Mavv 136,38,132
s_nop 0
Tv 30,v2,26624
Tv 32,v2,32768
Mavv 156,42,132
C_ 108,88,89
C_ 109,90,91
Mavv 152,42,128
s_nop 0
Tv 34,v3,26624
Tv 36,v3,32768
Mavv 132,38,128
E_ 92
Mavv 128,38,124
E_ 93
Mavv 148,42,124
E_ 94
Mavv 144,42,120
E_ 95
Mavv 124,38,120
C_ 112,92,93
C_ 113,94,95
Mavv 120,38,116
v_mov_b32 v92, 0x3f803f80
v_mov_b32 v93, 0x3f803f80
Mavv 140,42,116
W_ 0
v_mov_b32 v94, 0x3f803f80
v_mov_b32 v95, 0x3f803f80
Mavv 160,30,116
s_nop 0
Tv 38,v4,26624
Tv 40,v4,32768
Mavv 180,34,116
Mavv 184,34,120
s_nop 0
Tv 42,v5,26624
Tv 44,v5,32768
Mavv 164,30,120
Mavv 168,30,124
Mavv 188,34,124
S_ s65,s61,147456
Mavv 192,34,128
S_ m0,s51,24576
Mavv 172,30,128
s_waitcnt vmcnt(6) lgkmcnt(0)
s_barrier
Mavv 176,30,132
Ln v7,32,s65,0
Mavv 196,34,132
Mavv 216,38,132
Dv 30,%0,6144
Mavv 236,42,132
Dv 34,v1,6144
Mavv 232,42,128
Ln v8,32,s65,3072
S_ m0,s51,25472
Mavv 212,38,128
Mavv 208,38,124
Ln v7,32,s65,128
Mavv 228,42,124
Mavv 224,42,120
Ln v8,32,s65,3200
S_ m0,s51,26368
Mavv 204,38,120
Mavv 200,38,116
Ln v7,32,s65,256
Mavv 220,42,116
.Lstep2_11:
Zvvv 76,92,96
Dv 38,%0,7168
Zvvv 80,92,100
Dv 42,v1,7168
Zvvv 84,92,104
Zvvv 88,92,108
Zvvv 92,92,112
W_ 2
Nvvv 116,30,136,56
Mvvv 116,34,156
Nvvv 120,30,140,60
s_nop 1
v_max3_f32 v10, v76, v80, v84
s_nop 0
v_max3_f32 v10, v10, v88, v92
Mvvv 120,34,160
v_cmp_lt_f32 vcc, 0x42000000, v10
s_cbranch_vccnz .Ltramp2_10
Nvvv 124,30,144,64
A_ v46,v46,v76
A_ v47,v47,v80
Mvvv 124,34,164
A_ v48,v48,v84
A_ v49,v49,v88
Nvvv 128,30,148,68
A_ v50,v50,v92
Mvvv 128,34,168
Ln v8,32,s65,3328
Nvvv 132,30,152,72
Mvvv 132,34,172
W_ 0
Mvvv 132,38,192
Dv 30,%0,8192
Mvvv 132,42,212
Dv 34,v1,8192
Mvvv 128,38,188
Mvvv 128,42,208
Mvvv 124,38,184
Mvvv 124,42,204
Mvvv 120,38,180
Mvvv 120,42,200
Mvvv 116,38,176
Mvvv 116,42,196
W_ 0
Mvvv 116,30,216
Dv 38,%0,0
Mvvv 116,34,236
Dv 42,v1,0
Mvvv 120,30,220
Mvvv 120,34,240
Mvvv 124,30,224
Mvvv 124,34,244
Mvvv 128,30,228
Mvvv 128,34,248
s_nop 0
E_ 116
Mvvv 132,30,232
E_ 117
Mvvv 132,34,252
W_ 0
E_ 118
Nvvv 76,38,136,56
Dv 30,%0,1024
Mvvv 76,42,156
Dv 34,v1,1024
Nvvv 80,38,140,60
E_ 119
Mvvv 80,42,160
C_ 119,118,119
C_ 118,116,117
Nvvv 84,38,144,64
E_ 120
Mvvv 84,42,164
E_ 121
Nvvv 88,38,148,68
E_ 122
Mvvv 88,42,168
E_ 123
Nvvv 92,38,152,72
C_ 123,122,123
C_ 122,120,121
Mvvv 92,42,172
W_ 0
E_ 124
Mvvv 92,30,192
Dv 38,%0,2048
Mvvv 92,34,212
Dv 42,v1,2048
Mvvv 88,30,188
E_ 125
Mvvv 88,34,208
E_ 126
Mvvv 84,30,184
S_ s65,s61,135168
E_ 127
Mvvv 84,34,204
S_ m0,s51,0
C_ 127,126,127
C_ 126,124,125
Mvvv 80,30,180
s_waitcnt vmcnt(6) lgkmcnt(0)
s_barrier
Mvvv 80,34,200
Ln v9,36,s65,0
Mvvv 76,30,176
E_ 128
Mvvv 76,34,196
Ln v9,36,s65,3072
S_ m0,s51,896
Mvvv 76,38,216
Tv 30,v2,12288
Tv 32,v2,18432
Mvvv 76,42,236
Ln v9,36,s65,128
Mvvv 80,38,220
s_nop 0
Tv 34,v3,12288
Tv 36,v3,18432
Mvvv 80,42,240
Ln v9,36,s65,3200
S_ m0,s51,1792
Mvvv 84,38,224
E_ 129
Mvvv 84,42,244
Ln v9,36,s65,256
Mvvv 88,38,228
E_ 130
Mvvv 88,42,248
Ln v9,36,s65,3328
Mvvv 92,38,232
E_ 131
Mvvv 92,42,252
W_ 0
C_ 131,130,131
C_ 130,128,129
Mavv 0,30,96
s_nop 0
Tv 38,v4,12288
Tv 40,v4,18432
Mavv 20,34,96
E_ 132
Mavv 24,34,100
s_nop 0
Tv 42,v5,12288
Tv 44,v5,18432
Mavv 4,30,100
E_ 133
Mavv 8,30,104
E_ 134
Mavv 28,34,104
E_ 135
Mavv 32,34,108
C_ 135,134,135
C_ 134,132,133
Mavv 12,30,108
E_ 76
Mavv 16,30,112
E_ 77
Mavv 36,34,112
W_ 0
E_ 78
Mavv 56,38,112
s_nop 0
Tv 30,v2,13312
Tv 32,v2,19456
Mavv 76,42,112
E_ 79
Mavv 72,42,108
s_nop 0
Tv 34,v3,13312
Tv 36,v3,19456
Mavv 52,38,108
C_ 116,76,77
C_ 117,78,79
Mavv 48,38,104
E_ 80
Mavv 68,42,104
E_ 81
Mavv 64,42,100
E_ 82
Mavv 44,38,100
E_ 83
Mavv 40,38,96
C_ 120,80,81
C_ 121,82,83
Mavv 60,42,96
W_ 0
E_ 84
Mavv 80,30,96
s_nop 0
Tv 38,v4,13312
Tv 40,v4,19456
Mavv 100,34,96
E_ 85
Mavv 104,34,100
s_nop 0
Tv 42,v5,13312
Tv 44,v5,19456
Mavv 84,30,100
E_ 86
Mavv 88,30,104
E_ 87
Mavv 108,34,104
C_ 124,84,85
C_ 125,86,87
Mavv 112,34,108
E_ 88
Mavv 92,30,108
E_ 89
Mavv 96,30,112
E_ 90
Mavv 116,34,112
W_ 0
E_ 91
Mavv 136,38,112
s_nop 0
Tv 30,v2,14336
Tv 32,v2,20480
Mavv 156,42,112
C_ 128,88,89
C_ 129,90,91
Mavv 152,42,108
s_nop 0
Tv 34,v3,14336
Tv 36,v3,20480
Mavv 132,38,108
E_ 92
Mavv 128,38,104
E_ 93
Mavv 148,42,104
E_ 94
Mavv 144,42,100
E_ 95
Mavv 124,38,100
C_ 132,92,93
C_ 133,94,95
Mavv 120,38,96
v_mov_b32 v92, 0x3f803f80
v_mov_b32 v93, 0x3f803f80
Mavv 140,42,96
W_ 0
v_mov_b32 v94, 0x3f803f80
v_mov_b32 v95, 0x3f803f80
Mavv 160,30,96
s_nop 0
Tv 38,v4,14336
Tv 40,v4,20480
Mavv 180,34,96
Mavv 184,34,100
s_nop 0
Tv 42,v5,14336
Tv 44,v5,20480
Mavv 164,30,100
Mavv 168,30,104
Mavv 188,34,104
S_ s65,s61,159744
Mavv 192,34,108
S_ m0,s51,12288
Mavv 172,30,108
s_waitcnt vmcnt(6) lgkmcnt(0)
s_barrier
Mavv 176,30,112
Ln v7,32,s65,0
Mavv 196,34,112
Mavv 216,38,112
Dv 30,%0,30720
Mavv 236,42,112
Dv 34,v1,30720
Mavv 232,42,108
Ln v8,32,s65,3072
S_ m0,s51,13184
Mavv 212,38,108
Mavv 208,38,104
Ln v7,32,s65,128
Mavv 228,42,104
Mavv 224,42,100
Ln v8,32,s65,3200
S_ m0,s51,14080
Mavv 204,38,100
Mavv 200,38,96
Ln v7,32,s65,256
Mavv 220,42,96
.Lstep2_12:
Zvvv 76,92,116
Dv 38,%0,31744
Zvvv 80,92,120
Dv 42,v1,31744
Zvvv 84,92,124
Zvvv 88,92,128
Zvvv 92,92,132
W_ 2
Nvvv 96,30,136,56
Mvvv 96,34,156
Nvvv 100,30,140,60
s_nop 1
v_max3_f32 v10, v76, v80, v84
s_nop 0
v_max3_f32 v10, v10, v88, v92
Mvvv 100,34,160
v_cmp_lt_f32 vcc, 0x42000000, v10
s_cbranch_vccnz .Ltramp2_11
Nvvv 104,30,144,64
A_ v46,v46,v76
A_ v47,v47,v80
Mvvv 104,34,164
A_ v48,v48,v84
A_ v49,v49,v88
Nvvv 108,30,148,68
A_ v50,v50,v92
Mvvv 108,34,168
Ln v8,32,s65,3328
Nvvv 112,30,152,72
R_ v11,a248
Mvvv 112,34,172
W_ 0
v_min_u32 v11, 240, v11
Mvvv 112,38,192
Dv 30,%0,32768
v_lshlrev_b32 v11, 3, v11
Mvvv 112,42,212
Dv 34,v1,32768
Mvvv 108,38,188
buffer_load_dword a254, v11, s[44:47], s66 offen
Mvvv 108,42,208
Mvvv 104,38,184
Mvvv 104,42,204
Mvvv 100,38,180
Mvvv 100,42,200
Mvvv 96,38,176
Mvvv 96,42,196
W_ 0
Mvvv 96,30,216
Dv 38,%0,24576
Mvvv 96,34,236
Dv 42,v1,24576
Mvvv 100,30,220
Mvvv 100,34,240
Mvvv 104,30,224
Mvvv 104,34,244
Mvvv 108,30,228
Mvvv 108,34,248
s_nop 0
E_ 96
Mvvv 112,30,232
E_ 97
Mvvv 112,34,252
W_ 0
E_ 98
Nvvv 76,38,136,56
Dv 30,%0,25600
Mvvv 76,42,156
Dv 34,v1,25600
Nvvv 80,38,140,60
E_ 99
Mvvv 80,42,160
C_ 99,98,99
C_ 98,96,97
Nvvv 84,38,144,64
E_ 100
Mvvv 84,42,164
E_ 101
Nvvv 88,38,148,68
E_ 102
Mvvv 88,42,168
E_ 103
Nvvv 92,38,152,72
C_ 103,102,103
C_ 102,100,101
Mvvv 92,42,172
W_ 0
E_ 104
Mvvv 92,30,192
Dv 38,%0,26624
Mvvv 92,34,212
Dv 42,v1,26624
Mvvv 88,30,188
E_ 105
Mvvv 88,34,208
E_ 106
Mvvv 84,30,184
S_ s65,s61,147456
E_ 107
Mvvv 84,34,204
S_ m0,s51,24576
C_ 107,106,107
C_ 106,104,105
Mvvv 80,30,180
s_waitcnt vmcnt(7) lgkmcnt(0)
s_barrier
Mvvv 80,34,200
Ln v9,36,s65,0
Mvvv 76,30,176
E_ 108
Mvvv 76,34,196
Ln v9,36,s65,3072
S_ m0,s51,25472
Mvvv 76,38,216
Tv 30,v2,0
Tv 32,v2,6144
Mvvv 76,42,236
Ln v9,36,s65,128
Mvvv 80,38,220
s_nop 0
Tv 34,v3,0
Tv 36,v3,6144
Mvvv 80,42,240
Ln v9,36,s65,3200
S_ m0,s51,26368
Mvvv 84,38,224
E_ 109
Mvvv 84,42,244
Ln v9,36,s65,256
Mvvv 88,38,228
E_ 110
Mvvv 88,42,248
Ln v9,36,s65,3328
Mvvv 92,38,232
E_ 111
Mvvv 92,42,252
W_ 0
C_ 111,110,111
C_ 110,108,109
Mavv 0,30,116
s_nop 0
Tv 38,v4,0
Tv 40,v4,6144
Mavv 20,34,116
E_ 112
Mavv 24,34,120
s_nop 0
Tv 42,v5,0
Tv 44,v5,6144
Mavv 4,30,120
E_ 113
)A"
R"A(Mavv 8,30,124
E_ 114
Mavv 28,34,124
E_ 115
Mavv 32,34,128
C_ 115,114,115
C_ 114,112,113
Mavv 12,30,128
E_ 76
Mavv 16,30,132
E_ 77
Mavv 36,34,132
W_ 0
E_ 78
Mavv 56,38,132
s_nop 0
Tv 30,v2,1024
Tv 32,v2,7168
Mavv 76,42,132
E_ 79
Mavv 72,42,128
s_nop 0
Tv 34,v3,1024
Tv 36,v3,7168
Mavv 52,38,128
C_ 96,76,77
C_ 97,78,79
Mavv 48,38,124
E_ 80
Mavv 68,42,124
E_ 81
Mavv 64,42,120
E_ 82
Mavv 44,38,120
E_ 83
Mavv 40,38,116
C_ 100,80,81
C_ 101,82,83
Mavv 60,42,116
W_ 0
E_ 84
Mavv 80,30,116
s_nop 0
Tv 38,v4,1024
Tv 40,v4,7168
Mavv 100,34,116
E_ 85
Mavv 104,34,120
s_nop 0
Tv 42,v5,1024
Tv 44,v5,7168
Mavv 84,30,120
E_ 86
Mavv 88,30,124
E_ 87
Mavv 108,34,124
C_ 104,84,85
C_ 105,86,87
Mavv 112,34,128
E_ 88
Mavv 92,30,128
E_ 89
Mavv 96,30,132
E_ 90
Mavv 116,34,132
W_ 0
E_ 91
Mavv 136,38,132
s_nop 0
Tv 30,v2,2048
Tv 32,v2,8192
Mavv 156,42,132
C_ 108,88,89
C_ 109,90,91
Mavv 152,42,128
s_nop 0
Tv 34,v3,2048
Tv 36,v3,8192
Mavv 132,38,128
E_ 92
Mavv 128,38,124
E_ 93
Mavv 148,42,124
E_ 94
Mavv 144,42,120
E_ 95
Mavv 124,38,120
C_ 112,92,93
C_ 113,94,95
Mavv 120,38,116
v_mov_b32 v92, 0x3f803f80
v_mov_b32 v93, 0x3f803f80
Mavv 140,42,116
W_ 0
v_mov_b32 v94, 0x3f803f80
v_mov_b32 v95, 0x3f803f80
Mavv 160,30,116
s_nop 0
Tv 38,v4,2048
Tv 40,v4,8192
Mavv 180,34,116
Mavv 184,34,120
s_nop 0
Tv 42,v5,2048
Tv 44,v5,8192
Mavv 164,30,120
Mavv 168,30,124
Mavv 188,34,124
S_ s65,s61,172032
Mavv 192,34,128
S_ m0,s51,0
Mavv 172,30,128
s_waitcnt vmcnt(7) lgkmcnt(0)
s_barrier
Mavv 176,30,132
Ln v7,32,s65,0
Mavv 196,34,132
Mavv 216,38,132
Dv 30,%0,18432
Mavv 236,42,132
Dv 34,v1,18432
Mavv 232,42,128
Ln v8,32,s65,3072
S_ m0,s51,896
Mavv 212,38,128
Mavv 208,38,124
Ln v7,32,s65,128
Mavv 228,42,124
Mavv 224,42,120
Ln v8,32,s65,3200
S_ m0,s51,1792
Mavv 204,38,120
Mavv 200,38,116
Ln v7,32,s65,256
Mavv 220,42,116
.Lstep2_13:
Zvvv 76,92,96
Dv 38,%0,19456
Zvvv 80,92,100
Dv 42,v1,19456
Zvvv 84,92,104
Zvvv 88,92,108
Zvvv 92,92,112
W_ 2
Nvvv 116,30,136,56
Mvvv 116,34,156
Nvvv 120,30,140,60
s_nop 1
v_max3_f32 v10, v76, v80, v84
s_nop 0
v_max3_f32 v10, v10, v88, v92
Mvvv 120,34,160
v_cmp_lt_f32 vcc, 0x42000000, v10
s_cbranch_vccnz .Ltramp2_12
Nvvv 124,30,144,64
A_ v46,v46,v76
A_ v47,v47,v80
Mvvv 124,34,164
A_ v48,v48,v84
A_ v49,v49,v88
Nvvv 128,30,148,68
A_ v50,v50,v92
Mvvv 128,34,168
Ln v8,32,s65,3328
Nvvv 132,30,152,72
Mvvv 132,34,172
W_ 0
Mvvv 132,38,192
Dv 30,%0,20480
Mvvv 132,42,212
Dv 34,v1,20480
Mvvv 128,38,188
Mvvv 128,42,208
Mvvv 124,38,184
Mvvv 124,42,204
Mvvv 120,38,180
Mvvv 120,42,200
Mvvv 116,38,176
Mvvv 116,42,196
W_ 0
Mvvv 116,30,216
Dv 38,%0,12288
Mvvv 116,34,236
Dv 42,v1,12288
Mvvv 120,30,220
Mvvv 120,34,240
Mvvv 124,30,224
Mvvv 124,34,244
Mvvv 128,30,228
Mvvv 128,34,248
s_nop 0
E_ 116
Mvvv 132,30,232
E_ 117
Mvvv 132,34,252
W_ 0
E_ 118
Nvvv 76,38,136,56
Dv 30,%0,13312
Mvvv 76,42,156
Dv 34,v1,13312
Nvvv 80,38,140,60
E_ 119
Mvvv 80,42,160
C_ 119,118,119
C_ 118,116,117
Nvvv 84,38,144,64
E_ 120
Mvvv 84,42,164
E_ 121
Nvvv 88,38,148,68
E_ 122
Mvvv 88,42,168
E_ 123
Nvvv 92,38,152,72
C_ 123,122,123
C_ 122,120,121
Mvvv 92,42,172
W_ 0
E_ 124
Mvvv 92,30,192
Dv 38,%0,14336
Mvvv 92,34,212
Dv 42,v1,14336
Mvvv 88,30,188
E_ 125
Mvvv 88,34,208
E_ 126
Mvvv 84,30,184
S_ s65,s61,159744
E_ 127
Mvvv 84,34,204
S_ m0,s51,12288
C_ 127,126,127
C_ 126,124,125
Mvvv 80,30,180
s_waitcnt vmcnt(6) lgkmcnt(0)
s_barrier
Mvvv 80,34,200
Ln v9,36,s65,0
Mvvv 76,30,176
E_ 128
Mvvv 76,34,196
Ln v9,36,s65,3072
S_ m0,s51,13184
Mvvv 76,38,216
Tv 30,v2,24576
Tv 32,v2,30720
Mvvv 76,42,236
Ln v9,36,s65,128
Mvvv 80,38,220
s_nop 0
Tv 34,v3,24576
Tv 36,v3,30720
Mvvv 80,42,240
Ln v9,36,s65,3200
S_ m0,s51,14080
Mvvv 84,38,224
E_ 129
Mvvv 84,42,244
Ln v9,36,s65,256
Mvvv 88,38,228
E_ 130
Mvvv 88,42,248
Ln v9,36,s65,3328
Mvvv 92,38,232
E_ 131
Mvvv 92,42,252
W_ 0
C_ 131,130,131
C_ 130,128,129
Mavv 0,30,96
s_nop 0
Tv 38,v4,24576
Tv 40,v4,30720
Mavv 20,34,96
E_ 132
Mavv 24,34,100
s_nop 0
Tv 42,v5,24576
Tv 44,v5,30720
Mavv 4,30,100
E_ 133
Mavv 8,30,104
E_ 134
Mavv 28,34,104
E_ 135
Mavv 32,34,108
C_ 135,134,135
C_ 134,132,133
Mavv 12,30,108
E_ 76
Mavv 16,30,112
E_ 77
Mavv 36,34,112
W_ 0
E_ 78
Mavv 56,38,112
s_nop 0
Tv 30,v2,25600
Tv 32,v2,31744
Mavv 76,42,112
E_ 79
Mavv 72,42,108
s_nop 0
Tv 34,v3,25600
Tv 36,v3,31744
Mavv 52,38,108
C_ 116,76,77
C_ 117,78,79
Mavv 48,38,104
E_ 80
Mavv 68,42,104
E_ 81
Mavv 64,42,100
E_ 82
Mavv 44,38,100
E_ 83
Mavv 40,38,96
C_ 120,80,81
C_ 121,82,83
Mavv 60,42,96
W_ 0
E_ 84
Mavv 80,30,96
s_nop 0
Tv 38,v4,25600
Tv 40,v4,31744
Mavv 100,34,96
E_ 85
Mavv 104,34,100
s_nop 0
Tv 42,v5,25600
Tv 44,v5,31744
Mavv 84,30,100
E_ 86
Mavv 88,30,104
E_ 87
Mavv 108,34,104
C_ 124,84,85
C_ 125,86,87
Mavv 112,34,108
E_ 88
Mavv 92,30,108
E_ 89
Mavv 96,30,112
E_ 90
Mavv 116,34,112
W_ 0
E_ 91
Mavv 136,38,112
s_nop 0
Tv 30,v2,26624
Tv 32,v2,32768
Mavv 156,42,112
C_ 128,88,89
C_ 129,90,91
Mavv 152,42,108
s_nop 0
Tv 34,v3,26624
Tv 36,v3,32768
Mavv 132,38,108
E_ 92
Mavv 128,38,104
E_ 93
Mavv 148,42,104
E_ 94
Mavv 144,42,100
E_ 95
Mavv 124,38,100
C_ 132,92,93
C_ 133,94,95
Mavv 120,38,96
v_mov_b32 v92, 0x3f803f80
v_mov_b32 v93, 0x3f803f80
Mavv 140,42,96
W_ 0
v_mov_b32 v94, 0x3f803f80
v_mov_b32 v95, 0x3f803f80
Mavv 160,30,96
s_nop 0
Tv 38,v4,26624
Tv 40,v4,32768
Mavv 180,34,96
Mavv 184,34,100
s_nop 0
Tv 42,v5,26624
Tv 44,v5,32768
Mavv 164,30,100
Mavv 168,30,104
Mavv 188,34,104
S_ s65,s61,184320
Mavv 192,34,108
S_ m0,s51,24576
Mavv 172,30,108
s_waitcnt vmcnt(6) lgkmcnt(0)
s_barrier
Mavv 176,30,112
Ln v7,32,s65,0
Mavv 196,34,112
Mavv 216,38,112
Dv 30,%0,6144
Mavv 236,42,112
Dv 34,v1,6144
Mavv 232,42,108
Ln v8,32,s65,3072
S_ m0,s51,25472
Mavv 212,38,108
Mavv 208,38,104
Ln v7,32,s65,128
Mavv 228,42,104
Mavv 224,42,100
Ln v8,32,s65,3200
S_ m0,s51,26368
Mavv 204,38,100
Mavv 200,38,96
Ln v7,32,s65,256
Mavv 220,42,96
.Lstep2_14:
Zvvv 76,92,116
Dv 38,%0,7168
Zvvv 80,92,120
Dv 42,v1,7168
Zvvv 84,92,124
Zvvv 88,92,128
Zvvv 92,92,132
W_ 2
Nvvv 96,30,136,56
Mvvv 96,34,156
Nvvv 100,30,140,60
s_nop 1
v_max3_f32 v10, v76, v80, v84
s_nop 0
v_max3_f32 v10, v10, v88, v92
Mvvv 100,34,160
v_cmp_lt_f32 vcc, 0x42000000, v10
s_cbranch_vccnz .Ltramp2_13
Nvvv 104,30,144,64
A_ v46,v46,v76
A_ v47,v47,v80
Mvvv 104,34,164
A_ v48,v48,v84
A_ v49,v49,v88
Nvvv 108,30,148,68
A_ v50,v50,v92
Mvvv 108,34,168
Ln v8,32,s65,3328
Nvvv 112,30,152,72
Mvvv 112,34,172
W_ 0
Mvvv 112,38,192
Dv 30,%0,8192
Mvvv 112,42,212
Dv 34,v1,8192
Mvvv 108,38,188
Mvvv 108,42,208
Mvvv 104,38,184
Mvvv 104,42,204
Mvvv 100,38,180
Mvvv 100,42,200
Mvvv 96,38,176
Mvvv 96,42,196
W_ 0
Mvvv 96,30,216
Dv 38,%0,0
Mvvv 96,34,236
Dv 42,v1,0
Mvvv 100,30,220
Mvvv 100,34,240
Mvvv 104,30,224
Mvvv 104,34,244
Mvvv 108,30,228
Mvvv 108,34,248
s_nop 0
E_ 96
Mvvv 112,30,232
E_ 97
Mvvv 112,34,252
W_ 0
E_ 98
Nvvv 76,38,136,56
Dv 30,%0,1024
Mvvv 76,42,156
Dv 34,v1,1024
Nvvv 80,38,140,60
E_ 99
Mvvv 80,42,160
C_ 99,98,99
C_ 98,96,97
Nvvv 84,38,144,64
E_ 100
Mvvv 84,42,164
E_ 101
Nvvv 88,38,148,68
E_ 102
Mvvv 88,42,168
E_ 103
Nvvv 92,38,152,72
C_ 103,102,103
C_ 102,100,101
Mvvv 92,42,172
W_ 0
E_ 104
Mvvv 92,30,192
Dv 38,%0,2048
Mvvv 92,34,212
Dv 42,v1,2048
Mvvv 88,30,188
E_ 105
Mvvv 88,34,208
E_ 106
Mvvv 84,30,184
S_ s65,s61,172032
E_ 107
Mvvv 84,34,204
S_ m0,s51,0
C_ 107,106,107
C_ 106,104,105
Mvvv 80,30,180
s_waitcnt vmcnt(6) lgkmcnt(0)
s_barrier
Mvvv 80,34,200
Ln v9,36,s65,0
Mvvv 76,30,176
E_ 108
Mvvv 76,34,196
Ln v9,36,s65,3072
S_ m0,s51,896
Mvvv 76,38,216
Tv 30,v2,12288
Tv 32,v2,18432
Mvvv 76,42,236
Ln v9,36,s65,128
Mvvv 80,38,220
s_nop 0
Tv 34,v3,12288
Tv 36,v3,18432
Mvvv 80,42,240
Ln v9,36,s65,3200
S_ m0,s51,1792
Mvvv 84,38,224
E_ 109
Mvvv 84,42,244
Ln v9,36,s65,256
Mvvv 88,38,228
E_ 110
Mvvv 88,42,248
Ln v9,36,s65,3328
Mvvv 92,38,232
E_ 111
Mvvv 92,42,252
W_ 0
C_ 111,110,111
C_ 110,108,109
Mavv 0,30,116
s_nop 0
Tv 38,v4,12288
Tv 40,v4,18432
Mavv 20,34,116
E_ 112
Mavv 24,34,120
s_nop 0
Tv 42,v5,12288
Tv 44,v5,18432
Mavv 4,30,120
E_ 113
Mavv 8,30,124
E_ 114
Mavv 28,34,124
E_ 115
Mavv 32,34,128
C_ 115,114,115
C_ 114,112,113
Mavv 12,30,128
E_ 76
Mavv 16,30,132
E_ 77
Mavv 36,34,132
W_ 0
E_ 78
Mavv 56,38,132
s_nop 0
Tv 30,v2,13312
Tv 32,v2,19456
Mavv 76,42,132
E_ 79
Mavv 72,42,128
s_nop 0
Tv 34,v3,13312
Tv 36,v3,19456
Mavv 52,38,128
C_ 96,76,77
C_ 97,78,79
Mavv 48,38,124
E_ 80
Mavv 68,42,124
E_ 81
Mavv 64,42,120
E_ 82
Mavv 44,38,120
E_ 83
Mavv 40,38,116
C_ 100,80,81
C_ 101,82,83
Mavv 60,42,116
W_ 0
E_ 84
Mavv 80,30,116
s_nop 0
Tv 38,v4,13312
Tv 40,v4,19456
Mavv 100,34,116
E_ 85
Mavv 104,34,120
s_nop 0
Tv 42,v5,13312
Tv 44,v5,19456
Mavv 84,30,120
E_ 86
Mavv 88,30,124
E_ 87
Mavv 108,34,124
C_ 104,84,85
C_ 105,86,87
Mavv 112,34,128
E_ 88
Mavv 92,30,128
E_ 89
Mavv 96,30,132
E_ 90
Mavv 116,34,132
W_ 0
E_ 91
Mavv 136,38,132
s_nop 0
Tv 30,v2,14336
Tv 32,v2,20480
Mavv 156,42,132
C_ 108,88,89
C_ 109,90,91
Mavv 152,42,128
s_nop 0
Tv 34,v3,14336
Tv 36,v3,20480
Mavv 132,38,128
E_ 92
Mavv 128,38,124
E_ 93
Mavv 148,42,124
E_ 94
Mavv 144,42,120
E_ 95
Mavv 124,38,120
C_ 112,92,93
C_ 113,94,95
Mavv 120,38,116
v_mov_b32 v92, 0x3f803f80
v_mov_b32 v93, 0x3f803f80
Mavv 140,42,116
W_ 0
v_mov_b32 v94, 0x3f803f80
v_mov_b32 v95, 0x3f803f80
Mavv 160,30,116
s_nop 0
Tv 38,v4,14336
Tv 40,v4,20480
Mavv 180,34,116
Mavv 184,34,120
s_nop 0
Tv 42,v5,14336
Tv 44,v5,20480
Mavv 164,30,120
Mavv 168,30,124
Mavv 188,34,124
S_ s65,s61,184320
Mavv 192,34,128
S_ m0,s51,12288
Mavv 172,30,128
s_waitcnt vmcnt(6) lgkmcnt(0)
s_barrier
Mavv 176,30,132
Ln v9,36,s65,0
Mavv 196,34,132
Mavv 216,38,132
Dv 30,%0,30720
Mavv 236,42,132
Dv 34,v1,30720
Mavv 232,42,128
Ln v9,36,s65,3072
S_ m0,s51,13184
Mavv 212,38,128
Mavv 208,38,124
Ln v9,36,s65,128
Mavv 228,42,124
Mavv 224,42,120
Ln v9,36,s65,3200
S_ m0,s51,14080
Mavv 204,38,120
Mavv 200,38,116
Ln v9,36,s65,256
Mavv 220,42,116
.Lstep2_15:
Zvvv 76,92,96
Dv 38,%0,31744
Zvvv 80,92,100
Dv 42,v1,31744
Zvvv 84,92,104
Zvvv 88,92,108
Zvvv 92,92,112
W_ 2
Nvvv 116,30,136,56
Mvvv 116,34,156
Nvvv 120,30,140,60
s_nop 1
v_max3_f32 v10, v76, v80, v84
s_nop 0
v_max3_f32 v10, v10, v88, v92
Mvvv 120,34,160
v_cmp_lt_f32 vcc, 0x42000000, v10
s_cbranch_vccnz .Ltramp2_14
Nvvv 124,30,144,64
A_ v46,v46,v76
A_ v47,v47,v80
Mvvv 124,34,164
A_ v48,v48,v84
A_ v49,v49,v88
Nvvv 128,30,148,68
A_ v50,v50,v92
Mvvv 128,34,168
Ln v9,36,s65,3328
Nvvv 132,30,152,72
Mvvv 132,34,172
W_ 0
Mvvv 132,38,192
Dv 30,%0,32768
Mvvv 132,42,212
Dv 34,v1,32768
Mvvv 128,38,188
Mvvv 128,42,208
Mvvv 124,38,184
Mvvv 124,42,204
Mvvv 120,38,180
Mvvv 120,42,200
Mvvv 116,38,176
Mvvv 116,42,196
W_ 0
Mvvv 116,30,216
Dv 38,%0,24576
Mvvv 116,34,236
Dv 42,v1,24576
Mvvv 120,30,220
Mvvv 120,34,240
Mvvv 124,30,224
Mvvv 124,34,244
Mvvv 128,30,228
Mvvv 128,34,248
s_nop 0
E_ 116
Mvvv 132,30,232
E_ 117
Mvvv 132,34,252
W_ 0
E_ 118
Nvvv 76,38,136,56
Dv 30,%0,25600
Mvvv 76,42,156
Dv 34,v1,25600
Nvvv 80,38,140,60
E_ 119
Mvvv 80,42,160
C_ 119,118,119
C_ 118,116,117
Nvvv 84,38,144,64
E_ 120
Mvvv 84,42,164
E_ 121
Nvvv 88,38,148,68
E_ 122
Mvvv 88,42,168
E_ 123
Nvvv 92,38,152,72
C_ 123,122,123
C_ 122,120,121
Mvvv 92,42,172
W_ 0
E_ 124
Mvvv 92,30,192
Dv 38,%0,26624
Mvvv 92,34,212
Dv 42,v1,26624
Mvvv 88,30,188
E_ 125
Mvvv 88,34,208
E_ 126
Mvvv 84,30,184
S_ s65,s61,393216
E_ 127
Mvvv 84,34,204
S_ m0,s51,24576
C_ 127,126,127
C_ 126,124,125
Mvvv 80,30,180
s_waitcnt vmcnt(6) lgkmcnt(0)
s_barrier
Mvvv 80,34,200
L_ v9,36,s65,0
Mvvv 76,30,176
E_ 128
Mvvv 76,34,196
L_ v9,36,s65,3072
S_ m0,s51,25472
Mvvv 76,38,216
Tv 30,v2,0
Tv 32,v2,6144
Mvvv 76,42,236
L_ v9,36,s65,128
Mvvv 80,38,220
s_nop 0
Tv 34,v3,0
Tv 36,v3,6144
Mvvv 80,42,240
L_ v9,36,s65,3200
S_ m0,s51,26368
Mvvv 84,38,224
E_ 129
Mvvv 84,42,244
L_ v9,36,s65,256
Mvvv 88,38,228
E_ 130
Mvvv 88,42,248
L_ v9,36,s65,3328
Mvvv 92,38,232
E_ 131
Mvvv 92,42,252
W_ 0
C_ 131,130,131
C_ 130,128,129
Mavv 0,30,96
s_nop 0
Tv 38,v4,0
Tv 40,v4,6144
Mavv 20,34,96
E_ 132
Mavv 24,34,100
s_nop 0
Tv 42,v5,0
Tv 44,v5,6144
Mavv 4,30,100
E_ 133
Mavv 8,30,104
E_ 134
Mavv 28,34,104
E_ 135
Mavv 32,34,108
C_ 135,134,135
C_ 134,132,133
Mavv 12,30,108
E_ 76
Mavv 16,30,112
E_ 77
Mavv 36,34,112
W_ 0
E_ 78
Mavv 56,38,112
s_nop 0
Tv 30,v2,1024
Tv 32,v2,7168
Mavv 76,42,112
E_ 79
Mavv 72,42,108
s_nop 0
Tv 34,v3,1024
Tv 36,v3,7168
Mavv 52,38,108
C_ 116,76,77
C_ 117,78,79
Mavv 48,38,104
E_ 80
Mavv 68,42,104
E_ 81
Mavv 64,42,100
E_ 82
Mavv 44,38,100
E_ 83
Mavv 40,38,96
C_ 120,80,81
C_ 121,82,83
Mavv 60,42,96
W_ 0
E_ 84
Mavv 80,30,96
s_nop 0
Tv 38,v4,1024
Tv 40,v4,7168
Mavv 100,34,96
E_ 85
Mavv 104,34,100
s_nop 0
Tv 42,v5,1024
Tv 44,v5,7168
Mavv 84,30,100
E_ 86
Mavv 88,30,104
E_ 87
Mavv 108,34,104
C_ 124,84,85
C_ 125,86,87
Mavv 112,34,108
E_ 88
Mavv 92,30,108
E_ 89
Mavv 96,30,112
E_ 90
Mavv 116,34,112
W_ 0
E_ 91
Mavv 136,38,112
s_nop 0
Tv 30,v2,2048
Tv 32,v2,8192
Mavv 156,42,112
C_ 128,88,89
C_ 129,90,91
Mavv 152,42,108
s_nop 0
Tv 34,v3,2048
Tv 36,v3,8192
Mavv 132,38,108
E_ 92
Mavv 128,38,104
E_ 93
Mavv 148,42,104
E_ 94
Mavv 144,42,100
E_ 95
Mavv 124,38,100
C_ 132,92,93
C_ 133,94,95
Mavv 120,38,96
v_mov_b32 v92, 0x3f803f80
v_mov_b32 v93, 0x3f803f80
Mavv 140,42,96
W_ 0
v_mov_b32 v94, 0x3f803f80
v_mov_b32 v95, 0x3f803f80
Mavv 160,30,96
s_nop 0
Tv 38,v4,2048
Tv 40,v4,8192
Mavv 180,34,96
Mavv 184,34,100
s_nop 0
Tv 42,v5,2048
Tv 44,v5,8192
Mavv 164,30,100
Mavv 168,30,104
Mavv 188,34,104
S_ s65,s61,393216
Mavv 192,34,108
S_ m0,s51,0
Mavv 172,30,108
s_waitcnt vmcnt(6) lgkmcnt(0)
s_barrier
Mavv 176,30,112
Ln v7,32,s65,0
Mavv 196,34,112
Mavv 216,38,112
s_nop 0
Tv 30,v2,12288
Tv 32,v2,18432
Zvvv 76,92,116
Ln v8,32,s65,3072
S_ m0,s51,896
Zvvv 80,92,120
s_nop 0
Tv 34,v3,12288
Tv 36,v3,18432
Zvvv 84,92,124
Ln v7,32,s65,128
Zvvv 88,92,128
Zvvv 92,92,132
Ln v8,32,s65,3200
S_ m0,s51,1792
Mavv 236,42,112
Mavv 232,42,108
Ln v7,32,s65,256
Mavv 212,38,108
Mavv 208,38,104
v_max3_f32 v10, v76, v80, v84
Mavv 228,42,104
v_max3_f32 v10, v10, v88, v92
v_cmp_lt_f32 vcc, 0x42000000, v10
Mavv 224,42,100
Ln v8,32,s65,3328
Mavv 204,38,100
Mavv 200,38,96
Mavv 220,42,96
s_cbranch_vccnz .Ltramp2_15
.Ljoin2:
W_ 2
Mavv 0,30,116
s_mov_b32 s58, 1
Tv 38,v4,12288
Tv 40,v4,18432
W_ 2
Mavv 20,34,116
R_ v11,a248
R_ v13,a248
Mavv 24,34,120
S_ s53,s51,36864
Tv 42,v5,12288
Tv 44,v5,18432
Mavv 4,30,120
v_add_u32 v12, s53, v13
s_mul_i32 s53, s4, 10240
Mavv 8,30,124
v_add_u32 v13, s53, v13
A_ v46,v46,v76
Mavv 28,34,124
A_ v47,v47,v80
Mavv 32,34,128
A_ v48,v48,v84
A_ v49,v49,v88
Mavv 12,30,128
A_ v50,v50,v92
Mavv 16,30,132
v_rcp_f32 v46, v46
Mavv 36,34,132
W_ 0
v_rcp_f32 v47, v47
Mavv 56,38,132
s_nop 0
Tv 30,v2,13312
Tv 32,v2,19456
Mavv 76,42,132
v_rcp_f32 v48, v48
Mavv 72,42,128
s_nop 0
Tv 34,v3,13312
Tv 36,v3,19456
Mavv 52,38,128
v_rcp_f32 v49, v49
Mavv 48,38,124
v_rcp_f32 v50, v50
Mavv 68,42,124
s_mov_b32 s56, 12288
v_mov_b32 v176, v20
v_mov_b32 v177, v21
Mavv 64,42,120
Dv 136,v13,0
v_mov_b32 v178, v22
Mavv 44,38,120
Dv 140,v13,1024
v_mov_b32 v179, v23
Mavv 40,38,116
Dv 144,v13,2048
v_mov_b32 v180, v24
Mavv 60,42,116
W_ 3
Dv 148,v13,3072
v_mov_b32 v181, v25
Mavv 80,30,116
s_nop 0
Tv 38,v4,13312
Tv 40,v4,19456
Mavv 100,34,116
Dv 152,v13,4096
v_mov_b32 v182, v26
Mavv 104,34,120
s_nop 0
Tv 42,v5,13312
Tv 44,v5,19456
Mavv 84,30,120
Dv 156,v13,5120
v_mov_b32 v183, v27
Mavv 88,30,124
Dv 160,v13,6144
v_mov_b32 v184, v52
Mavv 108,34,124
Dv 164,v13,7168
v_mov_b32 v185, v53
Mavv 112,34,128
Dv 168,v13,8192
v_mov_b32 v186, v54
Mavv 92,30,128
Dv 172,v13,9216
v_mov_b32 v187, v55
Mavv 96,30,132
Dv 208,v12,0
R_ v188,a240
Mavv 116,34,132
W_ 6
Dv 212,v12,1024
R_ v189,a241
Mavv 136,38,132
s_nop 0
Tv 30,v2,14336
Tv 32,v2,20480
Mavv 156,42,132
Dv 216,v12,2048
R_ v190,a242
Mavv 152,42,128
s_nop 0
Tv 34,v3,14336
Tv 36,v3,20480
Mavv 132,38,128
Dv 220,v12,3072
R_ v191,a243
Mavv 128,38,124
Dv 224,v12,4096
R_ v192,a244
Mavv 148,42,124
Dv 228,v12,5120
R_ v193,a245
Mavv 144,42,120
Dv 232,v12,12288
R_ v194,a246
Mavv 124,38,120
Dv 236,v12,13312
R_ v195,a247
Mavv 120,38,116
Dv 240,v12,14336
v_mov_b32 v196, v16
Mavv 140,42,116
W_ 6
Dv 244,v12,15360
v_mov_b32 v197, v17
Mavv 160,30,116
s_nop 0
Tv 38,v4,14336
Tv 40,v4,20480
Mavv 180,34,116
Dv 248,v12,16384
v_mov_b32 v198, v18
Mavv 184,34,120
s_nop 0
Tv 42,v5,14336
Tv 44,v5,20480
Mavv 164,30,120
Dv 252,v12,17408
v_mov_b32 v199, v19
Mavv 168,30,124
v_mov_b32 v200, v28
v_mov_b32 v201, v29
Mavv 188,34,124
v_mov_b32 v202, v51
v_mov_b32 v203, v6
Mavv 192,34,128
v_mov_b32 v204, v14
v_mov_b32 v205, v15
Mavv 172,30,128
S_ s65,s61,405504
R_ v206,a249
R_ v207,a250
Mavv 176,30,132
S_ m0,s51,12288
R_ v96,a0
R_ v97,a1
Mavv 196,34,132
s_waitcnt vmcnt(63) lgkmcnt(0)
s_barrier
R_ v98,a2
Mavv 216,38,132
Ln v7,32,s65,0
Mavv 236,42,132
R_ v99,a3
Mavv 232,42,128
Ln v8,32,s65,3072
S_ m0,s51,13184
Mavv 212,38,128
R_ v100,a20
R_ v101,a21
Mavv 208,38,124
Ln v7,32,s65,128
Mavv 228,42,124
R_ v102,a22
R_ v103,a23
Mavv 224,42,120
Ln v8,32,s65,3200
S_ m0,s51,14080
Mavv 204,38,120
U_ v96,v96,v46
U_ v97,v97,v46
Mavv 200,38,116
Ln v7,32,s65,256
Mavv 220,42,116
U_ v98,v98,v46
U_ v99,v99,v46
U_ v100,v100,v46
U_ v101,v101,v46
U_ v102,v102,v46
U_ v103,v103,v46
C_ 76,96,97
C_ 77,98,99
C_ 78,100,101
C_ 79,102,103
ds_write_b128 v13, v[76:79] offset:0
Ln v8,32,s65,3328
S_ s61,s61,393216
.p2align 3
R_ v104,a4
R_ v105,a5
R_ v106,a6
R_ v107,a7
R_ v108,a24
R_ v109,a25
R_ v110,a26
R_ v111,a27
v_pk_mul_f32 v[104:105], v[104:105], v[46:47] op_sel:[0,1]
v_pk_mul_f32 v[106:107], v[106:107], v[46:47] op_sel:[0,1]
v_pk_mul_f32 v[108:109], v[108:109], v[46:47] op_sel:[0,1]
v_pk_mul_f32 v[110:111], v[110:111], v[46:47] op_sel:[0,1]
C_ 80,104,105
C_ 81,106,107
C_ 82,108,109
C_ 83,110,111
ds_write_b128 v13, v[80:83] offset:1024
.p2align 3
R_ v96,a8
R_ v97,a9
R_ v98,a10
R_ v99,a11
R_ v100,a28
R_ v101,a29
R_ v102,a30
R_ v103,a31
v_pk_mul_f32 v[96:97], v[96:97], v[48:49] op_sel_hi:[1,0]
v_pk_mul_f32 v[98:99], v[98:99], v[48:49] op_sel_hi:[1,0]
v_pk_mul_f32 v[100:101], v[100:101], v[48:49] op_sel_hi:[1,0]
v_pk_mul_f32 v[102:103], v[102:103], v[48:49] op_sel_hi:[1,0]
C_ 84,96,97
C_ 85,98,99
C_ 86,100,101
C_ 87,102,103
ds_write_b128 v13, v[84:87] offset:2048
.p2align 3
R_ v104,a12
R_ v105,a13
R_ v106,a14
R_ v107,a15
R_ v108,a32
R_ v109,a33
R_ v110,a34
R_ v111,a35
v_pk_mul_f32 v[104:105], v[104:105], v[48:49] op_sel:[0,1]
v_pk_mul_f32 v[106:107], v[106:107], v[48:49] op_sel:[0,1]
v_pk_mul_f32 v[108:109], v[108:109], v[48:49] op_sel:[0,1]
v_pk_mul_f32 v[110:111], v[110:111], v[48:49] op_sel:[0,1]
C_ 88,104,105
C_ 89,106,107
C_ 90,108,109
C_ 91,110,111
ds_write_b128 v13, v[88:91] offset:3072
.p2align 3
R_ v96,a16
R_ v97,a17
R_ v98,a18
R_ v99,a19
R_ v100,a36
R_ v101,a37
R_ v102,a38
R_ v103,a39
v_pk_mul_f32 v[96:97], v[96:97], v[50:51] op_sel_hi:[1,0]
v_pk_mul_f32 v[98:99], v[98:99], v[50:51] op_sel_hi:[1,0]
v_pk_mul_f32 v[100:101], v[100:101], v[50:51] op_sel_hi:[1,0]
v_pk_mul_f32 v[102:103], v[102:103], v[50:51] op_sel_hi:[1,0]
C_ 92,96,97
C_ 93,98,99
C_ 94,100,101
C_ 95,102,103
ds_write_b128 v13, v[92:95] offset:4096
.p2align 3
R_ v104,a40
R_ v105,a41
R_ v106,a42
R_ v107,a43
R_ v108,a60
R_ v109,a61
R_ v110,a62
R_ v111,a63
v_pk_mul_f32 v[104:105], v[104:105], v[46:47] op_sel_hi:[1,0]
v_pk_mul_f32 v[106:107], v[106:107], v[46:47] op_sel_hi:[1,0]
v_pk_mul_f32 v[108:109], v[108:109], v[46:47] op_sel_hi:[1,0]
v_pk_mul_f32 v[110:111], v[110:111], v[46:47] op_sel_hi:[1,0]
C_ 76,104,105
C_ 77,106,107
C_ 78,108,109
C_ 79,110,111
ds_write_b128 v13, v[76:79] offset:5120
.p2align 3
R_ v96,a44
R_ v97,a45
R_ v98,a46
R_ v99,a47
R_ v100,a64
R_ v101,a65
R_ v102,a66
R_ v103,a67
v_pk_mul_f32 v[96:97], v[96:97], v[46:47] op_sel:[0,1]
v_pk_mul_f32 v[98:99], v[98:99], v[46:47] op_sel:[0,1]
v_pk_mul_f32 v[100:101], v[100:101], v[46:47] op_sel:[0,1]
v_pk_mul_f32 v[102:103], v[102:103], v[46:47] op_sel:[0,1]
C_ 80,96,97
C_ 81,98,99
C_ 82,100,101
C_ 83,102,103
ds_write_b128 v13, v[80:83] offset:6144
.p2align 3
R_ v104,a48
R_ v105,a49
R_ v106,a50
R_ v107,a51
R_ v108,a68
R_ v109,a69
R_ v110,a70
R_ v111,a71
v_pk_mul_f32 v[104:105], v[104:105], v[48:49] op_sel_hi:[1,0]
v_pk_mul_f32 v[106:107], v[106:107], v[48:49] op_sel_hi:[1,0]
v_pk_mul_f32 v[108:109], v[108:109], v[48:49] op_sel_hi:[1,0]
v_pk_mul_f32 v[110:111], v[110:111], v[48:49] op_sel_hi:[1,0]
C_ 84,104,105
C_ 85,106,107
C_ 86,108,109
C_ 87,110,111
ds_write_b128 v13, v[84:87] offset:7168
.p2align 3
R_ v96,a52
R_ v97,a53
R_ v98,a54
R_ v99,a55
R_ v100,a72
R_ v101,a73
R_ v102,a74
R_ v103,a75
v_pk_mul_f32 v[96:97], v[96:97], v[48:49] op_sel:[0,1]
v_pk_mul_f32 v[98:99], v[98:99], v[48:49] op_sel:[0,1]
v_pk_mul_f32 v[100:101], v[100:101], v[48:49] op_sel:[0,1]
v_pk_mul_f32 v[102:103], v[102:103], v[48:49] op_sel:[0,1]
C_ 88,96,97
C_ 89,98,99
C_ 90,100,101
C_ 91,102,103
ds_write_b128 v13, v[88:91] offset:8192
.p2align 3
R_ v104,a56
R_ v105,a57
R_ v106,a58
R_ v107,a59
R_ v108,a76
R_ v109,a77
R_ v110,a78
R_ v111,a79
v_pk_mul_f32 v[104:105], v[104:105], v[50:51] op_sel_hi:[1,0]
v_pk_mul_f32 v[106:107], v[106:107], v[50:51] op_sel_hi:[1,0]
v_pk_mul_f32 v[108:109], v[108:109], v[50:51] op_sel_hi:[1,0]
v_pk_mul_f32 v[110:111], v[110:111], v[50:51] op_sel_hi:[1,0]
C_ 92,104,105
C_ 93,106,107
C_ 94,108,109
C_ 95,110,111
ds_write_b128 v13, v[92:95] offset:9216
.p2align 3
R_ v104,a92
R_ v105,a93
R_ v106,a94
R_ v107,a95
R_ v108,a112
R_ v109,a113
R_ v110,a114
R_ v111,a115
v_pk_mul_f32 v[104:105], v[104:105], v[48:49] op_sel:[0,1]
v_pk_mul_f32 v[106:107], v[106:107], v[48:49] op_sel:[0,1]
v_pk_mul_f32 v[108:109], v[108:109], v[48:49] op_sel:[0,1]
v_pk_mul_f32 v[110:111], v[110:111], v[48:49] op_sel:[0,1]
C_ 88,104,105
C_ 89,106,107
C_ 90,108,109
C_ 91,110,111
X_ a240,v88
X_ a241,v89
X_ a242,v90
X_ a243,v91
.p2align 3
R_ v96,a96
R_ v97,a97
R_ v98,a98
R_ v99,a99
R_ v100,a116
R_ v101,a117
R_ v102,a118
R_ v103,a119
v_pk_mul_f32 v[96:97], v[96:97], v[50:51] op_sel_hi:[1,0]
v_pk_mul_f32 v[98:99], v[98:99], v[50:51] op_sel_hi:[1,0]
v_pk_mul_f32 v[100:101], v[100:101], v[50:51] op_sel_hi:[1,0]
v_pk_mul_f32 v[102:103], v[102:103], v[50:51] op_sel_hi:[1,0]
C_ 92,96,97
C_ 93,98,99
C_ 94,100,101
C_ 95,102,103
X_ a244,v92
X_ a245,v93
X_ a246,v94
X_ a247,v95
.p2align 3
R_ v96,a124
R_ v97,a125
R_ v98,a126
R_ v99,a127
R_ v100,a144
R_ v101,a145
R_ v102,a146
R_ v103,a147
v_pk_mul_f32 v[96:97], v[96:97], v[46:47] op_sel:[0,1]
v_pk_mul_f32 v[98:99], v[98:99], v[46:47] op_sel:[0,1]
v_pk_mul_f32 v[100:101], v[100:101], v[46:47] op_sel:[0,1]
v_pk_mul_f32 v[102:103], v[102:103], v[46:47] op_sel:[0,1]
C_ 80,96,97
C_ 81,98,99
C_ 82,100,101
C_ 83,102,103
v_mov_b32 v28, v80
v_mov_b32 v29, v81
v_mov_b32 v51, v82
v_mov_b32 v6, v83
.p2align 3
R_ v104,a128
R_ v105,a129
R_ v106,a130
R_ v107,a131
R_ v108,a148
R_ v109,a149
R_ v110,a150
R_ v111,a151
v_pk_mul_f32 v[104:105], v[104:105], v[48:49] op_sel_hi:[1,0]
v_pk_mul_f32 v[106:107], v[106:107], v[48:49] op_sel_hi:[1,0]
v_pk_mul_f32 v[108:109], v[108:109], v[48:49] op_sel_hi:[1,0]
v_pk_mul_f32 v[110:111], v[110:111], v[48:49] op_sel_hi:[1,0]
C_ 84,104,105
C_ 85,106,107
C_ 86,108,109
C_ 87,110,111
v_mov_b32 v14, v84
v_mov_b32 v15, v85
X_ a249,v86
X_ a250,v87
.p2align 3
R_ v96,a132
R_ v97,a133
R_ v98,a134
R_ v99,a135
R_ v100,a152
R_ v101,a153
R_ v102,a154
R_ v103,a155
v_pk_mul_f32 v[96:97], v[96:97], v[48:49] op_sel:[0,1]
v_pk_mul_f32 v[98:99], v[98:99], v[48:49] op_sel:[0,1]
v_pk_mul_f32 v[100:101], v[100:101], v[48:49] op_sel:[0,1]
v_pk_mul_f32 v[102:103], v[102:103], v[48:49] op_sel:[0,1]
C_ 88,96,97
C_ 89,98,99
C_ 90,100,101
C_ 91,102,103
ds_write_b128 v12, v[88:91] offset:0
.p2align 3
R_ v104,a136
R_ v105,a137
R_ v106,a138
R_ v107,a139
R_ v108,a156
R_ v109,a157
R_ v110,a158
R_ v111,a159
v_pk_mul_f32 v[104:105], v[104:105], v[50:51] op_sel_hi:[1,0]
v_pk_mul_f32 v[106:107], v[106:107], v[50:51] op_sel_hi:[1,0]
v_pk_mul_f32 v[108:109], v[108:109], v[50:51] op_sel_hi:[1,0]
v_pk_mul_f32 v[110:111], v[110:111], v[50:51] op_sel_hi:[1,0]
C_ 92,104,105
C_ 93,106,107
C_ 94,108,109
C_ 95,110,111
ds_write_b128 v12, v[92:95] offset:1024
.p2align 3
R_ v96,a160
R_ v97,a161
R_ v98,a162
R_ v99,a163
R_ v100,a180
R_ v101,a181
R_ v102,a182
R_ v103,a183
v_pk_mul_f32 v[96:97], v[96:97], v[46:47] op_sel_hi:[1,0]
v_pk_mul_f32 v[98:99], v[98:99], v[46:47] op_sel_hi:[1,0]
v_pk_mul_f32 v[100:101], v[100:101], v[46:47] op_sel_hi:[1,0]
v_pk_mul_f32 v[102:103], v[102:103], v[46:47] op_sel_hi:[1,0]
C_ 76,96,97
C_ 77,98,99
C_ 78,100,101
C_ 79,102,103
ds_write_b128 v12, v[76:79] offset:2048
.p2align 3
R_ v104,a164
R_ v105,a165
R_ v106,a166
R_ v107,a167
R_ v108,a184
R_ v109,a185
R_ v110,a186
R_ v111,a187
v_pk_mul_f32 v[104:105], v[104:105], v[46:47] op_sel:[0,1]
v_pk_mul_f32 v[106:107], v[106:107], v[46:47] op_sel:[0,1]
v_pk_mul_f32 v[108:109], v[108:109], v[46:47] op_sel:[0,1]
v_pk_mul_f32 v[110:111], v[110:111], v[46:47] op_sel:[0,1]
C_ 80,104,105
C_ 81,106,107
C_ 82,108,109
C_ 83,110,111
ds_write_b128 v12, v[80:83] offset:3072
.p2align 3
R_ v96,a168
R_ v97,a169
R_ v98,a170
R_ v99,a171
R_ v100,a188
R_ v101,a189
R_ v102,a190
R_ v103,a191
v_pk_mul_f32 v[96:97], v[96:97], v[48:49] op_sel_hi:[1,0]
v_pk_mul_f32 v[98:99], v[98:99], v[48:49] op_sel_hi:[1,0]
v_pk_mul_f32 v[100:101], v[100:101], v[48:49] op_sel_hi:[1,0]
v_pk_mul_f32 v[102:103], v[102:103], v[48:49] op_sel_hi:[1,0]
C_ 84,96,97
C_ 85,98,99
C_ 86,100,101
C_ 87,102,103
ds_write_b128 v12, v[84:87] offset:4096
.p2align 3
R_ v104,a172
R_ v105,a173
R_ v106,a174
R_ v107,a175
R_ v108,a192
R_ v109,a193
R_ v110,a194
R_ v111,a195
v_pk_mul_f32 v[104:105], v[104:105], v[48:49] op_sel:[0,1]
v_pk_mul_f32 v[106:107], v[106:107], v[48:49] op_sel:[0,1]
v_pk_mul_f32 v[108:109], v[108:109], v[48:49] op_sel:[0,1]
v_pk_mul_f32 v[110:111], v[110:111], v[48:49] op_sel:[0,1]
C_ 88,104,105
C_ 89,106,107
C_ 90,108,109
C_ 91,110,111
ds_write_b128 v12, v[88:91] offset:5120
.p2align 3
R_ v96,a176
R_ v97,a177
R_ v98,a178
R_ v99,a179
R_ v100,a196
R_ v101,a197
R_ v102,a198
R_ v103,a199
v_pk_mul_f32 v[96:97], v[96:97], v[50:51] op_sel_hi:[1,0]
v_pk_mul_f32 v[98:99], v[98:99], v[50:51] op_sel_hi:[1,0]
v_pk_mul_f32 v[100:101], v[100:101], v[50:51] op_sel_hi:[1,0]
v_pk_mul_f32 v[102:103], v[102:103], v[50:51] op_sel_hi:[1,0]
C_ 92,96,97
C_ 93,98,99
C_ 94,100,101
C_ 95,102,103
ds_write_b128 v12, v[92:95] offset:12288
.p2align 3
R_ v104,a200
R_ v105,a201
R_ v106,a202
R_ v107,a203
R_ v108,a220
R_ v109,a221
R_ v110,a222
R_ v111,a223
v_pk_mul_f32 v[104:105], v[104:105], v[46:47] op_sel_hi:[1,0]
v_pk_mul_f32 v[106:107], v[106:107], v[46:47] op_sel_hi:[1,0]
v_pk_mul_f32 v[108:109], v[108:109], v[46:47] op_sel_hi:[1,0]
v_pk_mul_f32 v[110:111], v[110:111], v[46:47] op_sel_hi:[1,0]
C_ 76,104,105
C_ 77,106,107
C_ 78,108,109
C_ 79,110,111
ds_write_b128 v12, v[76:79] offset:13312
.p2align 3
R_ v96,a204
R_ v97,a205
R_ v98,a206
R_ v99,a207
R_ v100,a224
R_ v101,a225
R_ v102,a226
R_ v103,a227
v_pk_mul_f32 v[96:97], v[96:97], v[46:47] op_sel:[0,1]
v_pk_mul_f32 v[98:99], v[98:99], v[46:47] op_sel:[0,1]
v_pk_mul_f32 v[100:101], v[100:101], v[46:47] op_sel:[0,1]
v_pk_mul_f32 v[102:103], v[102:103], v[46:47] op_sel:[0,1]
C_ 80,96,97
C_ 81,98,99
C_ 82,100,101
C_ 83,102,103
ds_write_b128 v12, v[80:83] offset:14336
.p2align 3
R_ v104,a208
R_ v105,a209
R_ v106,a210
R_ v107,a211
R_ v108,a228
R_ v109,a229
R_ v110,a230
R_ v111,a231
v_pk_mul_f32 v[104:105], v[104:105], v[48:49] op_sel_hi:[1,0]
v_pk_mul_f32 v[106:107], v[106:107], v[48:49] op_sel_hi:[1,0]
v_pk_mul_f32 v[108:109], v[108:109], v[48:49] op_sel_hi:[1,0]
v_pk_mul_f32 v[110:111], v[110:111], v[48:49] op_sel_hi:[1,0]
C_ 84,104,105
C_ 85,106,107
C_ 86,108,109
C_ 87,110,111
ds_write_b128 v12, v[84:87] offset:15360
s_waitcnt vmcnt(6) lgkmcnt(0)
s_barrier
Dv 30,%0,6144
Dv 34,v1,6144
Dv 38,%0,7168
.p2align 3
R_ v96,a212
R_ v97,a213
R_ v98,a214
R_ v99,a215
R_ v100,a232
R_ v101,a233
R_ v102,a234
R_ v103,a235
v_pk_mul_f32 v[96:97], v[96:97], v[48:49] op_sel:[0,1]
v_pk_mul_f32 v[98:99], v[98:99], v[48:49] op_sel:[0,1]
v_pk_mul_f32 v[100:101], v[100:101], v[48:49] op_sel:[0,1]
v_pk_mul_f32 v[102:103], v[102:103], v[48:49] op_sel:[0,1]
C_ 88,96,97
C_ 89,98,99
C_ 90,100,101
C_ 91,102,103
ds_write_b128 v12, v[88:91] offset:16384
.p2align 3
R_ v104,a216
R_ v105,a217
R_ v106,a218
R_ v107,a219
R_ v108,a236
R_ v109,a237
R_ v110,a238
R_ v111,a239
v_pk_mul_f32 v[104:105], v[104:105], v[50:51] op_sel_hi:[1,0]
v_pk_mul_f32 v[106:107], v[106:107], v[50:51] op_sel_hi:[1,0]
v_pk_mul_f32 v[108:109], v[108:109], v[50:51] op_sel_hi:[1,0]
v_pk_mul_f32 v[110:111], v[110:111], v[50:51] op_sel_hi:[1,0]
C_ 92,104,105
C_ 93,106,107
C_ 94,108,109
C_ 95,110,111
ds_write_b128 v12, v[92:95] offset:17408
.Latt3:
W_ 4
Zvvv 96,30,136
Dv 42,v1,7168
R_ v116,a80
Zvvv 100,30,140
R_ v117,a81
R_ v118,a82
Zvvv 104,30,144
R_ v119,a83
R_ v120,a100
Zvvv 108,30,148
R_ v121,a101
R_ v122,a102
Zvvv 112,30,152
W_ 4
R_ v123,a103
U_ v116,v116,v46
Mvvv 96,34,156
Dv 30,%0,8192
U_ v117,v117,v46
Mvvv 100,34,160
U_ v118,v118,v46
U_ v119,v119,v46
Mvvv 104,34,164
U_ v120,v120,v46
U_ v121,v121,v46
Mvvv 108,34,168
U_ v122,v122,v46
U_ v123,v123,v46
Mvvv 112,34,172
W_ 4
C_ 20,116,117
C_ 21,118,119
Mvvv 96,38,176
Dv 34,v1,8192
C_ 22,120,121
Mvvv 100,38,180
C_ 23,122,123
R_ v124,a84
Mvvv 104,38,184
R_ v125,a85
R_ v126,a86
Mvvv 108,38,188
R_ v127,a87
R_ v128,a104
Mvvv 112,38,192
W_ 2
R_ v129,a105
R_ v130,a106
Mvvv 96,42,196
Dv 38,%0,0
R_ v131,a107
Mvvv 100,42,200
U_ v124,v124,v47
U_ v125,v125,v47
Mvvv 104,42,204
U_ v126,v126,v47
U_ v127,v127,v47
Mvvv 108,42,208
U_ v128,v128,v47
U_ v129,v129,v47
Mvvv 112,42,212
W_ 2
U_ v130,v130,v47
U_ v131,v131,v47
Mvvv 96,30,216
Dv 42,v1,0
C_ 24,124,125
Mvvv 100,30,220
C_ 25,126,127
C_ 26,128,129
Mvvv 104,30,224
C_ 27,130,131
R_ v116,a88
Mvvv 108,30,228
R_ v117,a89
R_ v118,a90
Mvvv 112,30,232
W_ 2
R_ v119,a91
R_ v120,a108
Mvvv 96,34,236
Dv 30,%0,1024
R_ v121,a109
Mvvv 100,34,240
R_ v122,a110
R_ v123,a111
Mvvv 104,34,244
U_ v116,v116,v48
U_ v117,v117,v48
Mvvv 108,34,248
U_ v118,v118,v48
U_ v119,v119,v48
Mvvv 112,34,252
W_ 2
U_ v120,v120,v48
U_ v121,v121,v48
Zvvv 76,38,136
Dv 34,v1,1024
U_ v122,v122,v48
Zvvv 80,38,140
U_ v123,v123,v48
C_ 52,116,117
Zvvv 84,38,144
C_ 53,118,119
C_ 54,120,121
Zvvv 88,38,148
C_ 55,122,123
R_ v124,a120
Zvvv 92,38,152
W_ 2
R_ v125,a121
R_ v126,a122
Mvvv 76,42,156
Dv 38,%0,2048
R_ v127,a123
Mvvv 80,42,160
R_ v128,a140
R_ v129,a141
Mvvv 84,42,164
R_ v130,a142
R_ v131,a143
Mvvv 88,42,168
U_ v124,v124,v46
U_ v125,v125,v46
Mvvv 92,42,172
W_ 2
U_ v126,v126,v46
U_ v127,v127,v46
Mvvv 76,30,176
Dv 42,v1,2048
U_ v128,v128,v46
Mvvv 80,30,180
U_ v129,v129,v46
U_ v130,v130,v46
Mvvv 84,30,184
U_ v131,v131,v46
C_ 16,124,125
Mvvv 88,30,188
C_ 17,126,127
C_ 18,128,129
Mvvv 92,30,192
s_waitcnt vmcnt(0) lgkmcnt(0)
s_barrier
Mvvv 76,34,196
S_ s65,s61,24576
Dv 30,%0,18432
C_ 19,130,131
Mvvv 76,38,216
S_ m0,s51,0
v_max3_f32 v56, v96, v97, v98
v_max_f32 v56, v56, v99
Mvvv 76,42,236
Ln v7,32,s65,0
Mvvv 80,34,200
v_max3_f32 v60, v100, v101, v102
v_max_f32 v60, v60, v103
Mvvv 80,38,220
Ln v8,32,s65,3072
Mvvv 80,42,240
S_ m0,s51,896
v_max3_f32 v64, v104, v105, v106
v_max_f32 v64, v64, v107
Mvvv 84,34,204
Ln v7,32,s65,128
Mvvv 84,38,224
v_max3_f32 v68, v108, v109, v110
v_max_f32 v68, v68, v111
Mvvv 84,42,244
Ln v8,32,s65,3200
Mvvv 88,34,208
S_ m0,s51,1792
v_max3_f32 v72, v112, v113, v114
v_max_f32 v72, v72, v115
Mvvv 88,38,228
Ln v7,32,s65,256
Mvvv 88,42,248
v_max3_f32 v56, v56, v76, v77
v_max3_f32 v56, v56, v78, v79
Mvvv 92,34,212
Ln v8,32,s65,3328
Mvvv 92,38,232
v_max3_f32 v60, v60, v80, v81
v_max3_f32 v60, v60, v82, v83
Mvvv 92,42,252
v_max3_f32 v64, v64, v84, v85
v_max3_f32 v64, v64, v86, v87
Dv 34,v1,18432
.p2align 3
v_mov_b32 v11, 0x3f803f80
v_permlane32_swap_b32 v56, v60
v_max3_f32 v68, v68, v88, v89
v_max3_f32 v68, v68, v90, v91
v_max_f32 v56, v56, v60
v_max3_f32 v72, v72, v92, v93
v_max3_f32 v72, v72, v94, v95
v_mov_b32 v73, v72
v_permlane32_swap_b32 v64, v68
v_max_f32 v64, v64, v68
v_permlane32_swap_b32 v72, v73
v_max_f32 v72, v72, v73
v_mov_b32 v73, v72
v_permlane16_swap_b32 v56, v64
v_max_f32 v56, v56, v64
v_permlane16_swap_b32 v72, v73
B_ v56,0xc0e00000,v56
v_max_f32 v72, v72, v73
v_mov_b32 v64, v56
B_ v72,0xc0e00000,v72
v_mov_b32 v73, v72
v_permlane16_swap_b32 v56, v64
v_mov_b32 v60, v56
v_mov_b32 v68, v64
v_mov_b64 v[74:75], v[72:73]
v_permlane32_swap_b32 v56, v60
v_permlane32_swap_b32 v64, v68
v_mov_b32 v57, v56
v_mov_b32 v61, v60
v_mov_b32 v65, v64
v_mov_b32 v69, v68
v_pk_add_f32 v[96:97], v[96:97], v[56:57]
v_pk_add_f32 v[98:99], v[98:99], v[56:57]
v_pk_add_f32 v[76:77], v[76:77], v[56:57]
v_pk_add_f32 v[78:79], v[78:79], v[56:57]
v_mov_b64 v[58:59], v[56:57]
v_pk_add_f32 v[100:101], v[100:101], v[60:61]
v_pk_add_f32 v[102:103], v[102:103], v[60:61]
v_pk_add_f32 v[80:81], v[80:81], v[60:61]
v_pk_add_f32 v[82:83], v[82:83], v[60:61]
v_mov_b64 v[62:63], v[60:61]
v_pk_add_f32 v[104:105], v[104:105], v[64:65]
v_pk_add_f32 v[106:107], v[106:107], v[64:65]
v_pk_add_f32 v[84:85], v[84:85], v[64:65]
v_pk_add_f32 v[86:87], v[86:87], v[64:65]
v_mov_b64 v[66:67], v[64:65]
v_pk_add_f32 v[108:109], v[108:109], v[68:69]
v_pk_add_f32 v[110:111], v[110:111], v[68:69]
v_pk_add_f32 v[88:89], v[88:89], v[68:69]
v_pk_add_f32 v[90:91], v[90:91], v[68:69]
v_mov_b64 v[70:71], v[68:69]
v_pk_add_f32 v[112:113], v[112:113], v[72:73]
v_pk_add_f32 v[114:115], v[114:115], v[72:73]
v_pk_add_f32 v[92:93], v[92:93], v[72:73]
v_pk_add_f32 v[94:95], v[94:95], v[72:73]
E_ 96
E_ 97
E_ 98
E_ 99
E_ 76
E_ 77
E_ 78
E_ 79
C_ 99,98,99
C_ 98,96,97
C_ 96,76,77
C_ 97,78,79
E_ 100
E_ 101
E_ 102
E_ 103
E_ 80
E_ 81
E_ 82
E_ 83
C_ 103,102,103
C_ 102,100,101
C_ 100,80,81
C_ 101,82,83
.Lstep3_1:
W_ 1
Nvvv 116,30,136,56
s_nop 0
Dv 38,%0,19456
Dv 42,v1,19456
W_ 2
Mvvv 116,34,156
E_ 104
Nvvv 120,30,140,60
E_ 105
Mvvv 120,34,160
E_ 106
Nvvv 124,30,144,64
E_ 107
Mvvv 124,34,164
E_ 84
Nvvv 128,30,148,68
E_ 85
Mvvv 128,34,168
E_ 86
Nvvv 132,30,152,72
E_ 87
Mvvv 132,34,172
W_ 0
C_ 107,106,107
C_ 106,104,105
Mvvv 132,38,192
s_nop 0
Dv 30,%0,20480
Dv 34,v1,20480
Mvvv 132,42,212
C_ 104,84,85
C_ 105,86,87
Mvvv 128,38,188
E_ 108
Mvvv 128,42,208
E_ 109
Mvvv 124,38,184
E_ 110
Mvvv 124,42,204
E_ 111
Mvvv 120,38,180
E_ 88
Mvvv 120,42,200
E_ 89
Mvvv 116,38,176
E_ 90
Mvvv 116,42,196
W_ 0
E_ 91
Mvvv 116,30,216
s_nop 0
Dv 38,%0,12288
Dv 42,v1,12288
Mvvv 116,34,236
C_ 111,110,111
C_ 110,108,109
Mvvv 120,30,220
C_ 108,88,89
C_ 109,90,91
Mvvv 120,34,240
E_ 112
Mvvv 124,30,224
E_ 113
)A"
R"A(Mvvv 124,34,244
E_ 114
Mvvv 128,30,228
E_ 115
Mvvv 128,34,248
E_ 92
Mvvv 132,30,232
E_ 93
Mvvv 132,34,252
W_ 0
E_ 94
Nvvv 76,38,136,56
s_nop 0
Dv 30,%0,13312
Dv 34,v1,13312
Mvvv 76,42,156
E_ 95
Nvvv 80,38,140,60
C_ 115,114,115
C_ 114,112,113
Mvvv 80,42,160
C_ 112,92,93
C_ 113,94,95
Nvvv 84,38,144,64
E_ 116
Mvvv 84,42,164
E_ 117
Nvvv 88,38,148,68
E_ 118
Mvvv 88,42,168
E_ 119
Nvvv 92,38,152,72
C_ 119,118,119
C_ 118,116,117
Mvvv 92,42,172
W_ 0
E_ 120
Mvvv 92,30,192
s_nop 0
Dv 38,%0,14336
Dv 42,v1,14336
Mvvv 92,34,212
X_ a180,v11
X_ a181,v11
Mvvv 88,30,188
X_ a182,v11
X_ a183,v11
Mvvv 88,34,208
E_ 121
Mvvv 84,30,184
E_ 122
Mvvv 84,34,204
E_ 123
Mvvv 80,30,180
C_ 123,122,123
C_ 122,120,121
Mvvv 80,34,200
E_ 124
Mvvv 76,30,176
E_ 125
Mvvv 76,34,196
S_ s65,s61,12288
E_ 126
W_ 1
Mvvv 76,38,216
S_ m0,s51,12288
E_ 127
W_ 0
Mvvv 76,42,236
s_waitcnt vmcnt(18) lgkmcnt(0)
s_barrier
C_ 127,126,127
Mvvv 80,38,220
L_ v9,36,s65,0
Mvvv 80,42,240
C_ 126,124,125
Mvvv 84,38,224
L_ v9,36,s65,3072
S_ m0,s51,13184
Mvvv 84,42,244
s_nop 0
Tv 30,v2,24576
Tv 32,v2,30720
Mvvv 88,38,228
L_ v9,36,s65,128
Mvvv 88,42,248
s_nop 0
Tv 34,v3,24576
Tv 36,v3,30720
Mvvv 92,38,232
L_ v9,36,s65,3200
S_ m0,s51,14080
Mvvv 92,42,252
E_ 128
Zaav 220,180,96
L_ v9,36,s65,256
Zaav 224,180,100
E_ 129
Zaav 228,180,104
L_ v9,36,s65,3328
Zaav 232,180,108
E_ 130
Zaav 236,180,112
W_ 0
E_ 131
Zavv 0,30,96
s_nop 0
Tv 38,v4,24576
Tv 40,v4,30720
Zavv 20,34,96
C_ 131,130,131
C_ 130,128,129
Zavv 24,34,100
s_nop 0
Tv 42,v5,24576
Tv 44,v5,30720
Zavv 4,30,100
E_ 132
Zavv 8,30,104
E_ 133
Zavv 28,34,104
R_ v46,a220
R_ v47,a224
Zavv 32,34,108
R_ v48,a228
R_ v49,a232
Zavv 12,30,108
R_ v50,a236
Zavv 16,30,112
E_ 134
Zavv 36,34,112
W_ 0
E_ 135
Zavv 56,38,112
s_nop 0
Tv 30,v2,25600
Tv 32,v2,31744
Zavv 76,42,112
C_ 135,134,135
C_ 134,132,133
Zavv 72,42,108
s_nop 0
Tv 34,v3,25600
Tv 36,v3,31744
Zavv 52,38,108
E_ 76
Zavv 48,38,104
E_ 77
Zavv 68,42,104
E_ 78
Zavv 64,42,100
E_ 79
Zavv 44,38,100
C_ 116,76,77
C_ 117,78,79
Zavv 40,38,96
E_ 80
Zavv 60,42,96
W_ 0
E_ 81
Zavv 80,30,96
s_nop 0
Tv 38,v4,25600
Tv 40,v4,31744
Zavv 100,34,96
E_ 82
Zavv 104,34,100
s_nop 0
Tv 42,v5,25600
Tv 44,v5,31744
Zavv 84,30,100
E_ 83
Zavv 88,30,104
C_ 120,80,81
C_ 121,82,83
Zavv 108,34,104
E_ 84
Zavv 112,34,108
E_ 85
Zavv 92,30,108
E_ 86
Zavv 96,30,112
E_ 87
Zavv 116,34,112
W_ 0
C_ 124,84,85
C_ 125,86,87
Zavv 136,38,112
s_nop 0
Tv 30,v2,26624
Tv 32,v2,32768
Zavv 156,42,112
E_ 88
Zavv 152,42,108
s_nop 0
Tv 34,v3,26624
Tv 36,v3,32768
Zavv 132,38,108
E_ 89
Zavv 128,38,104
E_ 90
Zavv 148,42,104
E_ 91
Zavv 144,42,100
C_ 128,88,89
C_ 129,90,91
Zavv 124,38,100
E_ 92
Zavv 120,38,96
E_ 93
Zavv 140,42,96
W_ 0
E_ 94
Zavv 160,30,96
s_nop 0
Tv 38,v4,26624
Tv 40,v4,32768
Zavv 180,34,96
E_ 95
Zavv 184,34,100
s_nop 0
Tv 42,v5,26624
Tv 44,v5,32768
Zavv 164,30,100
C_ 132,92,93
C_ 133,94,95
Zavv 168,30,104
v_mov_b32 v92, 0x3f803f80
v_mov_b32 v93, 0x3f803f80
Zavv 188,34,104
S_ s65,s61,36864
v_mov_b32 v94, 0x3f803f80
v_mov_b32 v95, 0x3f803f80
Zavv 192,34,108
S_ m0,s51,24576
Zavv 172,30,108
s_waitcnt vmcnt(6) lgkmcnt(0)
s_barrier
Zavv 176,30,112
Ln v7,32,s65,0
Zavv 196,34,112
Zavv 216,38,112
Dv 30,%0,6144
Zavv 236,42,112
Dv 34,v1,6144
Zavv 232,42,108
Ln v8,32,s65,3072
S_ m0,s51,25472
Zavv 212,38,108
Zavv 208,38,104
Ln v7,32,s65,128
Zavv 228,42,104
Zavv 224,42,100
Ln v8,32,s65,3200
S_ m0,s51,26368
Zavv 204,38,100
Zavv 200,38,96
Ln v7,32,s65,256
Zavv 220,42,96
Ln v8,32,s65,3328
W_ 0
s_nop 7
s_branch .Lstep1_2
.Latt1:
R_ v13,a248
S_ s53,s51,36864
v_add_u32 v12, s53, v13
s_mul_i32 s53, s4, 10240
S_ s53,s53,0
v_add_u32 v13, s53, v13
W_ 2
Zvvv 96,30,136
Dv 42,v1,44032
R_ v2,a245
Zvvv 100,30,140
ds_write_b128 v13, a[0:3] offset:0
Zvvv 104,30,144
v_xor_b32 v3, 32, v2
v_xor_b32 v4, 64, v2
Zvvv 108,30,148
ds_write_b128 v13, a[4:7] offset:1024
Zvvv 112,30,152
W_ 4
v_xor_b32 v5, 96, v2
s_mov_b64 s[24:25], s[16:17]
Mvvv 96,34,156
s_mov_b32 s26, 7864320
Dv 30,%0,45056
s_mov_b64 s[28:29], s[22:23]
Mvvv 100,34,160
ds_write_b128 v13, a[8:11] offset:2048
Mvvv 104,34,164
s_mov_b32 s30, 1638400
R_ v20,a80
s_mul_i32 s52, s4, 10240
Mvvv 108,34,168
ds_write_b128 v13, a[12:15] offset:3072
Mvvv 112,34,172
W_ 6
R_ v21,a81
R_ v22,a82
Mvvv 96,38,176
Dv 34,v1,45056
R_ v23,a83
Mvvv 100,38,180
ds_write_b128 v13, a[16:19] offset:4096
Mvvv 104,38,184
R_ v24,a84
R_ v25,a85
Mvvv 108,38,188
ds_write_b128 v13, a[40:43] offset:5120
Mvvv 112,38,192
W_ 8
R_ v26,a86
R_ v27,a87
Mvvv 96,42,196
Dv 38,%0,36864
R_ v52,a88
Mvvv 100,42,200
ds_write_b128 v13, a[44:47] offset:6144
Mvvv 104,42,204
R_ v53,a89
R_ v54,a90
Mvvv 108,42,208
ds_write_b128 v13, a[48:51] offset:7168
Mvvv 112,42,212
W_ 8
R_ v55,a91
v_accvgpr_mov_b32 a240, a92
Mvvv 96,30,216
Dv 42,v1,36864
v_accvgpr_mov_b32 a241, a93
Mvvv 100,30,220
ds_write_b128 v13, a[52:55] offset:8192
Mvvv 104,30,224
v_accvgpr_mov_b32 a242, a94
v_accvgpr_mov_b32 a243, a95
Mvvv 108,30,228
ds_write_b128 v13, a[56:59] offset:9216
Mvvv 112,30,232
W_ 8
v_accvgpr_mov_b32 a244, a96
v_accvgpr_mov_b32 a245, a97
Mvvv 96,34,236
Dv 30,%0,37888
v_accvgpr_mov_b32 a246, a98
Mvvv 100,34,240
v_accvgpr_mov_b32 a247, a99
R_ v16,a120
Mvvv 104,34,244
R_ v17,a121
R_ v18,a122
Mvvv 108,34,248
R_ v19,a123
R_ v28,a124
Mvvv 112,34,252
W_ 6
R_ v29,a125
R_ v51,a126
Zvvv 76,38,136
Dv 34,v1,37888
R_ v6,a127
Zvvv 80,38,140
R_ v14,a128
R_ v15,a129
Zvvv 84,38,144
v_accvgpr_mov_b32 a249, a130
v_accvgpr_mov_b32 a250, a131
Zvvv 88,38,148
v_max3_f32 v56, v96, v97, v98
v_max_f32 v56, v56, v99
Zvvv 92,38,152
W_ 4
v_max3_f32 v60, v100, v101, v102
v_max_f32 v60, v60, v103
Mvvv 76,42,156
Dv 38,%0,38912
v_max3_f32 v64, v104, v105, v106
Mvvv 80,42,160
v_max_f32 v64, v64, v107
Mvvv 84,42,164
v_max3_f32 v68, v108, v109, v110
v_max_f32 v68, v68, v111
Mvvv 88,42,168
v_max3_f32 v72, v112, v113, v114
v_max_f32 v72, v72, v115
Mvvv 92,42,172
W_ 2
Mvvv 76,30,176
Dv 42,v1,38912
Mvvv 80,30,180
Mvvv 84,30,184
Mvvv 88,30,188
Mvvv 92,30,192
s_waitcnt vmcnt(34) lgkmcnt(0)
s_barrier
Mvvv 76,34,196
Dv 30,%0,55296
Mvvv 76,38,216
Mvvv 76,42,236
Mvvv 80,34,200
Mvvv 80,38,220
Mvvv 80,42,240
Mvvv 84,34,204
s_nop 3
v_max3_f32 v56, v56, v76, v77
v_max3_f32 v56, v56, v78, v79
Mvvv 84,38,224
Mvvv 84,42,244
Mvvv 88,34,208
v_max3_f32 v60, v60, v80, v81
v_max3_f32 v60, v60, v82, v83
Mvvv 88,38,228
Mvvv 88,42,248
Mvvv 92,34,212
s_nop 1
v_max3_f32 v64, v64, v84, v85
v_max3_f32 v64, v64, v86, v87
Mvvv 92,38,232
Mvvv 92,42,252
Dv 34,v1,55296
.p2align 3
v_mov_b32 v11, 0x3f803f80
v_permlane32_swap_b32 v56, v60
v_max3_f32 v68, v68, v88, v89
v_max3_f32 v68, v68, v90, v91
v_max_f32 v56, v56, v60
s_nop 1
v_max3_f32 v72, v72, v92, v93
v_max3_f32 v72, v72, v94, v95
v_mov_b32 v73, v72
v_permlane32_swap_b32 v64, v68
v_max_f32 v64, v64, v68
v_permlane32_swap_b32 v72, v73
v_max_f32 v72, v72, v73
v_mov_b32 v73, v72
v_permlane16_swap_b32 v56, v64
v_max_f32 v56, v56, v64
v_permlane16_swap_b32 v72, v73
B_ v56,0xc0e00000,v56
v_max_f32 v72, v72, v73
v_mov_b32 v64, v56
B_ v72,0xc0e00000,v72
v_mov_b32 v73, v72
v_permlane16_swap_b32 v56, v64
v_mov_b32 v60, v56
v_mov_b32 v68, v64
v_mov_b64 v[74:75], v[72:73]
v_permlane32_swap_b32 v56, v60
v_permlane32_swap_b32 v64, v68
v_mov_b32 v57, v56
v_mov_b32 v61, v60
v_mov_b32 v65, v64
v_mov_b32 v69, v68
v_pk_add_f32 v[96:97], v[96:97], v[56:57]
v_pk_add_f32 v[98:99], v[98:99], v[56:57]
v_pk_add_f32 v[76:77], v[76:77], v[56:57]
v_pk_add_f32 v[78:79], v[78:79], v[56:57]
v_mov_b64 v[58:59], v[56:57]
v_pk_add_f32 v[100:101], v[100:101], v[60:61]
v_pk_add_f32 v[102:103], v[102:103], v[60:61]
v_pk_add_f32 v[80:81], v[80:81], v[60:61]
v_pk_add_f32 v[82:83], v[82:83], v[60:61]
v_mov_b64 v[62:63], v[60:61]
v_pk_add_f32 v[104:105], v[104:105], v[64:65]
v_pk_add_f32 v[106:107], v[106:107], v[64:65]
v_pk_add_f32 v[84:85], v[84:85], v[64:65]
v_pk_add_f32 v[86:87], v[86:87], v[64:65]
v_mov_b64 v[66:67], v[64:65]
v_pk_add_f32 v[108:109], v[108:109], v[68:69]
v_pk_add_f32 v[110:111], v[110:111], v[68:69]
v_pk_add_f32 v[88:89], v[88:89], v[68:69]
v_pk_add_f32 v[90:91], v[90:91], v[68:69]
v_mov_b64 v[70:71], v[68:69]
v_pk_add_f32 v[112:113], v[112:113], v[72:73]
v_pk_add_f32 v[114:115], v[114:115], v[72:73]
v_pk_add_f32 v[92:93], v[92:93], v[72:73]
v_pk_add_f32 v[94:95], v[94:95], v[72:73]
E_ 96
E_ 97
E_ 98
E_ 99
E_ 76
E_ 77
E_ 78
E_ 79
C_ 99,98,99
C_ 98,96,97
C_ 96,76,77
C_ 97,78,79
E_ 100
E_ 101
E_ 102
E_ 103
E_ 80
E_ 81
E_ 82
E_ 83
C_ 103,102,103
C_ 102,100,101
C_ 100,80,81
C_ 101,82,83
.Lstep1_1:
W_ 1
Nvvv 116,30,136,56
s_nop 0
Dv 38,%0,56320
Dv 42,v1,56320
W_ 2
Mvvv 116,34,156
E_ 104
Nvvv 120,30,140,60
E_ 105
Mvvv 120,34,160
E_ 106
Nvvv 124,30,144,64
E_ 107
Mvvv 124,34,164
E_ 84
Nvvv 128,30,148,68
E_ 85
Mvvv 128,34,168
E_ 86
Nvvv 132,30,152,72
E_ 87
Mvvv 132,34,172
W_ 0
C_ 107,106,107
C_ 106,104,105
Mvvv 132,38,192
s_nop 0
Dv 30,%0,57344
Dv 34,v1,57344
Mvvv 132,42,212
C_ 104,84,85
C_ 105,86,87
Mvvv 128,38,188
E_ 108
Mvvv 128,42,208
E_ 109
Mvvv 124,38,184
E_ 110
Mvvv 124,42,204
E_ 111
Mvvv 120,38,180
E_ 88
Mvvv 120,42,200
E_ 89
Mvvv 116,38,176
E_ 90
Mvvv 116,42,196
W_ 0
E_ 91
Mvvv 116,30,216
s_nop 0
Dv 38,%0,49152
Dv 42,v1,49152
Mvvv 116,34,236
C_ 111,110,111
C_ 110,108,109
Mvvv 120,30,220
C_ 108,88,89
C_ 109,90,91
Mvvv 120,34,240
E_ 112
Mvvv 124,30,224
E_ 113
Mvvv 124,34,244
E_ 114
Mvvv 128,30,228
E_ 115
Mvvv 128,34,248
E_ 92
Mvvv 132,30,232
E_ 93
Mvvv 132,34,252
W_ 0
E_ 94
Nvvv 76,38,136,56
s_nop 0
Dv 30,%0,50176
Dv 34,v1,50176
Mvvv 76,42,156
E_ 95
Nvvv 80,38,140,60
C_ 115,114,115
C_ 114,112,113
Mvvv 80,42,160
C_ 112,92,93
C_ 113,94,95
Nvvv 84,38,144,64
E_ 116
Mvvv 84,42,164
E_ 117
Nvvv 88,38,148,68
E_ 118
Mvvv 88,42,168
E_ 119
Nvvv 92,38,152,72
C_ 119,118,119
C_ 118,116,117
Mvvv 92,42,172
W_ 0
E_ 120
Mvvv 92,30,192
s_nop 0
Dv 38,%0,51200
Dv 42,v1,51200
Mvvv 92,34,212
X_ a180,v11
X_ a181,v11
Mvvv 88,30,188
X_ a182,v11
X_ a183,v11
Mvvv 88,34,208
ds_write_b128 v12, a[132:135] offset:0
Mvvv 84,30,184
E_ 121
Mvvv 84,34,204
E_ 122
Mvvv 80,30,180
ds_write_b128 v12, a[136:139] offset:1024
Mvvv 80,34,200
E_ 123
Mvvv 76,30,176
C_ 123,122,123
C_ 122,120,121
Mvvv 76,34,196
ds_write_b128 v12, a[160:163] offset:2048
W_ 4
Mvvv 76,38,216
W_ 3
E_ 124
Mvvv 76,42,236
s_waitcnt vmcnt(12) lgkmcnt(0)
s_barrier
Mvvv 80,38,220
ds_write_b128 v12, a[164:167] offset:3072
Mvvv 80,42,240
E_ 125
Mvvv 84,38,224
E_ 126
Mvvv 84,42,244
s_nop 0
Tv 30,v2,24576
Tv 32,v2,30720
Mvvv 88,38,228
ds_write_b128 v12, a[168:171] offset:4096
Mvvv 88,42,248
s_nop 0
Tv 34,v3,24576
Tv 36,v3,30720
Mvvv 92,38,232
E_ 127
Mvvv 92,42,252
ds_write_b128 v12, a[172:175] offset:5120
Zaav 220,180,96
C_ 127,126,127
C_ 126,124,125
Zaav 224,180,100
E_ 128
Zaav 228,180,104
ds_write_b128 v12, a[176:179] offset:12288
Zaav 232,180,108
E_ 129
Zaav 236,180,112
W_ 2
E_ 130
Zavv 0,30,96
s_nop 0
Tv 38,v4,24576
Tv 40,v4,30720
Zavv 20,34,96
ds_write_b128 v12, a[200:203] offset:13312
Zavv 24,34,100
s_nop 0
Tv 42,v5,24576
Tv 44,v5,30720
Zavv 4,30,100
E_ 131
Zavv 8,30,104
ds_write_b128 v12, a[204:207] offset:14336
Zavv 28,34,104
R_ v46,a220
R_ v47,a224
Zavv 32,34,108
R_ v48,a228
R_ v49,a232
Zavv 12,30,108
R_ v50,a236
C_ 131,130,131
Zavv 16,30,112
ds_write_b128 v12, a[208:211] offset:15360
Zavv 36,34,112
W_ 2
C_ 130,128,129
Zavv 56,38,112
s_nop 0
Tv 30,v2,25600
Tv 32,v2,31744
Zavv 76,42,112
ds_write_b128 v12, a[212:215] offset:16384
Zavv 72,42,108
s_nop 0
Tv 34,v3,25600
Tv 36,v3,31744
Zavv 52,38,108
E_ 132
Zavv 48,38,104
ds_write_b128 v12, a[216:219] offset:17408
Zavv 68,42,104
E_ 133
Zavv 64,42,100
E_ 134
Zavv 44,38,100
E_ 135
Zavv 40,38,96
C_ 135,134,135
C_ 134,132,133
Zavv 60,42,96
W_ 1
E_ 76
Zavv 80,30,96
s_nop 0
Tv 38,v4,25600
Tv 40,v4,31744
Zavv 100,34,96
E_ 77
Zavv 104,34,100
s_nop 0
Tv 42,v5,25600
Tv 44,v5,31744
Zavv 84,30,100
E_ 78
Zavv 88,30,104
E_ 79
Zavv 108,34,104
C_ 116,76,77
C_ 117,78,79
Zavv 112,34,108
E_ 80
Zavv 92,30,108
E_ 81
Zavv 96,30,112
E_ 82
Zavv 116,34,112
W_ 0
E_ 83
Zavv 136,38,112
s_nop 0
Tv 30,v2,26624
Tv 32,v2,32768
Zavv 156,42,112
C_ 120,80,81
C_ 121,82,83
Zavv 152,42,108
s_nop 0
Tv 34,v3,26624
Tv 36,v3,32768
Zavv 132,38,108
E_ 84
Zavv 128,38,104
E_ 85
Zavv 148,42,104
E_ 86
Zavv 144,42,100
E_ 87
Zavv 124,38,100
C_ 124,84,85
C_ 125,86,87
Zavv 120,38,96
E_ 88
Zavv 140,42,96
W_ 0
E_ 89
Zavv 160,30,96
s_nop 0
Tv 38,v4,26624
Tv 40,v4,32768
Zavv 180,34,96
E_ 90
Zavv 184,34,100
s_nop 0
Tv 42,v5,26624
Tv 44,v5,32768
Zavv 164,30,100
E_ 91
Zavv 168,30,104
C_ 128,88,89
C_ 129,90,91
Zavv 188,34,104
S_ s65,s61,36864
E_ 92
Zavv 192,34,108
S_ m0,s51,24576
E_ 93
Zavv 172,30,108
s_waitcnt vmcnt(6) lgkmcnt(0)
s_barrier
Zavv 176,30,112
Ln v7,32,s65,0
Zavv 196,34,112
E_ 94
Zavv 216,38,112
Dv 30,%0,6144
Zavv 236,42,112
Dv 34,v1,6144
Zavv 232,42,108
Ln v8,32,s65,3072
S_ m0,s51,25472
Zavv 212,38,108
E_ 95
Zavv 208,38,104
Ln v7,32,s65,128
Zavv 228,42,104
C_ 132,92,93
C_ 133,94,95
Zavv 224,42,100
Ln v8,32,s65,3200
S_ m0,s51,26368
Zavv 204,38,100
v_mov_b32 v92, 0x3f803f80
v_mov_b32 v93, 0x3f803f80
Zavv 200,38,96
Ln v7,32,s65,256
Zavv 220,42,96
v_mov_b32 v94, 0x3f803f80
v_mov_b32 v95, 0x3f803f80
Ln v8,32,s65,3328
.Lstep1_2:
s_nop 0
Zvvv 76,92,116
Dv 38,%0,7168
Zvvv 80,92,120
Dv 42,v1,7168
Zvvv 84,92,124
Zvvv 88,92,128
Zvvv 92,92,132
W_ 2
Nvvv 96,30,136,56
Mvvv 96,34,156
Nvvv 100,30,140,60
s_nop 1
v_max3_f32 v10, v76, v80, v84
s_nop 0
v_max3_f32 v10, v10, v88, v92
Mvvv 100,34,160
v_cmp_lt_f32 vcc, 0x42000000, v10
s_cbranch_vccnz .Ltramp1_1
Nvvv 104,30,144,64
A_ v46,v46,v76
A_ v47,v47,v80
Mvvv 104,34,164
A_ v48,v48,v84
A_ v49,v49,v88
Nvvv 108,30,148,68
A_ v50,v50,v92
Mvvv 108,34,168
Nvvv 112,30,152,72
Mvvv 112,34,172
W_ 0
Mvvv 112,38,192
Dv 30,%0,8192
Mvvv 112,42,212
Dv 34,v1,8192
Mvvv 108,38,188
Mvvv 108,42,208
Mvvv 104,38,184
Mvvv 104,42,204
Mvvv 100,38,180
Mvvv 100,42,200
Mvvv 96,38,176
Mvvv 96,42,196
W_ 0
Mvvv 96,30,216
Dv 38,%0,0
Mvvv 96,34,236
Dv 42,v1,0
Mvvv 100,30,220
Mvvv 100,34,240
Mvvv 104,30,224
Mvvv 104,34,244
Mvvv 108,30,228
Mvvv 108,34,248
s_nop 0
E_ 96
Mvvv 112,30,232
E_ 97
Mvvv 112,34,252
W_ 0
E_ 98
Nvvv 76,38,136,56
Dv 30,%0,1024
Mvvv 76,42,156
Dv 34,v1,1024
Nvvv 80,38,140,60
E_ 99
Mvvv 80,42,160
C_ 99,98,99
C_ 98,96,97
Nvvv 84,38,144,64
E_ 100
Mvvv 84,42,164
E_ 101
Nvvv 88,38,148,68
E_ 102
Mvvv 88,42,168
E_ 103
Nvvv 92,38,152,72
C_ 103,102,103
C_ 102,100,101
Mvvv 92,42,172
W_ 0
E_ 104
Mvvv 92,30,192
Dv 38,%0,2048
Mvvv 92,34,212
Dv 42,v1,2048
Mvvv 88,30,188
E_ 105
Mvvv 88,34,208
E_ 106
Mvvv 84,30,184
S_ s65,s61,24576
E_ 107
Mvvv 84,34,204
S_ m0,s51,0
C_ 107,106,107
C_ 106,104,105
Mvvv 80,30,180
s_waitcnt vmcnt(6) lgkmcnt(0)
s_barrier
Mvvv 80,34,200
L_ v9,36,s65,0
Mvvv 76,30,176
E_ 108
Mvvv 76,34,196
L_ v9,36,s65,3072
S_ m0,s51,896
Mvvv 76,38,216
Tv 30,v2,12288
Tv 32,v2,18432
Mvvv 76,42,236
L_ v9,36,s65,128
Mvvv 80,38,220
s_nop 0
Tv 34,v3,12288
Tv 36,v3,18432
Mvvv 80,42,240
L_ v9,36,s65,3200
S_ m0,s51,1792
Mvvv 84,38,224
E_ 109
Mvvv 84,42,244
L_ v9,36,s65,256
Mvvv 88,38,228
E_ 110
Mvvv 88,42,248
L_ v9,36,s65,3328
Mvvv 92,38,232
E_ 111
Mvvv 92,42,252
W_ 0
C_ 111,110,111
C_ 110,108,109
Mavv 0,30,116
s_nop 0
Tv 38,v4,12288
Tv 40,v4,18432
Mavv 20,34,116
E_ 112
Mavv 24,34,120
s_nop 0
Tv 42,v5,12288
Tv 44,v5,18432
Mavv 4,30,120
E_ 113
Mavv 8,30,124
E_ 114
Mavv 28,34,124
E_ 115
Mavv 32,34,128
C_ 115,114,115
C_ 114,112,113
Mavv 12,30,128
E_ 76
Mavv 16,30,132
E_ 77
Mavv 36,34,132
W_ 0
E_ 78
Mavv 56,38,132
s_nop 0
Tv 30,v2,13312
Tv 32,v2,19456
Mavv 76,42,132
E_ 79
Mavv 72,42,128
s_nop 0
Tv 34,v3,13312
Tv 36,v3,19456
Mavv 52,38,128
C_ 96,76,77
C_ 97,78,79
Mavv 48,38,124
E_ 80
Mavv 68,42,124
E_ 81
Mavv 64,42,120
E_ 82
Mavv 44,38,120
E_ 83
Mavv 40,38,116
C_ 100,80,81
C_ 101,82,83
Mavv 60,42,116
W_ 0
E_ 84
Mavv 80,30,116
s_nop 0
Tv 38,v4,13312
Tv 40,v4,19456
Mavv 100,34,116
E_ 85
Mavv 104,34,120
s_nop 0
Tv 42,v5,13312
Tv 44,v5,19456
Mavv 84,30,120
E_ 86
Mavv 88,30,124
E_ 87
Mavv 108,34,124
C_ 104,84,85
C_ 105,86,87
Mavv 112,34,128
E_ 88
Mavv 92,30,128
E_ 89
Mavv 96,30,132
E_ 90
Mavv 116,34,132
W_ 0
E_ 91
Mavv 136,38,132
s_nop 0
Tv 30,v2,14336
Tv 32,v2,20480
Mavv 156,42,132
C_ 108,88,89
C_ 109,90,91
Mavv 152,42,128
s_nop 0
Tv 34,v3,14336
Tv 36,v3,20480
Mavv 132,38,128
E_ 92
Mavv 128,38,124
E_ 93
Mavv 148,42,124
E_ 94
Mavv 144,42,120
E_ 95
Mavv 124,38,120
C_ 112,92,93
C_ 113,94,95
Mavv 120,38,116
v_mov_b32 v92, 0x3f803f80
v_mov_b32 v93, 0x3f803f80
Mavv 140,42,116
W_ 0
v_mov_b32 v94, 0x3f803f80
v_mov_b32 v95, 0x3f803f80
Mavv 160,30,116
s_nop 0
Tv 38,v4,14336
Tv 40,v4,20480
Mavv 180,34,116
Mavv 184,34,120
s_nop 0
Tv 42,v5,14336
Tv 44,v5,20480
Mavv 164,30,120
Mavv 168,30,124
Mavv 188,34,124
S_ s65,s61,49152
Mavv 192,34,128
S_ m0,s51,12288
Mavv 172,30,128
s_waitcnt vmcnt(6) lgkmcnt(0)
s_barrier
Mavv 176,30,132
Ln v7,32,s65,0
Mavv 196,34,132
Mavv 216,38,132
Dv 30,%0,30720
Mavv 236,42,132
Dv 34,v1,30720
Mavv 232,42,128
Ln v8,32,s65,3072
S_ m0,s51,13184
Mavv 212,38,128
Mavv 208,38,124
Ln v7,32,s65,128
Mavv 228,42,124
Mavv 224,42,120
Ln v8,32,s65,3200
S_ m0,s51,14080
Mavv 204,38,120
Mavv 200,38,116
Ln v7,32,s65,256
Mavv 220,42,116
s_cmp_eq_u32 s4, 0
s_cbranch_scc1 .Lhs_a1
.Lhs_a1_back:
.Lstep1_3:
Zvvv 76,92,96
Dv 38,%0,31744
Zvvv 80,92,100
Dv 42,v1,31744
Zvvv 84,92,104
Zvvv 88,92,108
Zvvv 92,92,112
W_ 2
Nvvv 116,30,136,56
Mvvv 116,34,156
Nvvv 120,30,140,60
s_nop 1
v_max3_f32 v10, v76, v80, v84
s_nop 0
v_max3_f32 v10, v10, v88, v92
Mvvv 120,34,160
v_cmp_lt_f32 vcc, 0x42000000, v10
s_cbranch_vccnz .Ltramp1_2
Nvvv 124,30,144,64
A_ v46,v46,v76
A_ v47,v47,v80
Mvvv 124,34,164
A_ v48,v48,v84
A_ v49,v49,v88
Nvvv 128,30,148,68
A_ v50,v50,v92
Mvvv 128,34,168
Ln v8,32,s65,3328
Nvvv 132,30,152,72
Mvvv 132,34,172
W_ 0
Mvvv 132,38,192
Dv 30,%0,32768
Mvvv 132,42,212
Dv 34,v1,32768
Mvvv 128,38,188
Mvvv 128,42,208
Mvvv 124,38,184
Mvvv 124,42,204
Mvvv 120,38,180
Mvvv 120,42,200
Mvvv 116,38,176
Mvvv 116,42,196
W_ 0
Mvvv 116,30,216
Dv 38,%0,24576
Mvvv 116,34,236
Dv 42,v1,24576
Mvvv 120,30,220
Mvvv 120,34,240
Mvvv 124,30,224
Mvvv 124,34,244
Mvvv 128,30,228
Mvvv 128,34,248
s_nop 0
E_ 116
Mvvv 132,30,232
E_ 117
Mvvv 132,34,252
W_ 0
E_ 118
Nvvv 76,38,136,56
Dv 30,%0,25600
Mvvv 76,42,156
Dv 34,v1,25600
Nvvv 80,38,140,60
E_ 119
Mvvv 80,42,160
C_ 119,118,119
C_ 118,116,117
Nvvv 84,38,144,64
E_ 120
Mvvv 84,42,164
E_ 121
Nvvv 88,38,148,68
E_ 122
Mvvv 88,42,168
E_ 123
Nvvv 92,38,152,72
C_ 123,122,123
C_ 122,120,121
Mvvv 92,42,172
W_ 0
E_ 124
Mvvv 92,30,192
Dv 38,%0,26624
Mvvv 92,34,212
Dv 42,v1,26624
Mvvv 88,30,188
E_ 125
Mvvv 88,34,208
E_ 126
Mvvv 84,30,184
S_ s65,s61,36864
E_ 127
Mvvv 84,34,204
S_ m0,s51,24576
C_ 127,126,127
C_ 126,124,125
Mvvv 80,30,180
s_waitcnt vmcnt(6) lgkmcnt(0)
s_barrier
Mvvv 80,34,200
L_ v9,36,s65,0
Mvvv 76,30,176
E_ 128
Mvvv 76,34,196
L_ v9,36,s65,3072
S_ m0,s51,25472
Mvvv 76,38,216
Tv 30,v2,0
Tv 32,v2,6144
Mvvv 76,42,236
L_ v9,36,s65,128
Mvvv 80,38,220
s_nop 0
Tv 34,v3,0
Tv 36,v3,6144
Mvvv 80,42,240
L_ v9,36,s65,3200
S_ m0,s51,26368
Mvvv 84,38,224
E_ 129
Mvvv 84,42,244
L_ v9,36,s65,256
Mvvv 88,38,228
E_ 130
Mvvv 88,42,248
L_ v9,36,s65,3328
Mvvv 92,38,232
E_ 131
Mvvv 92,42,252
W_ 0
C_ 131,130,131
C_ 130,128,129
Mavv 0,30,96
s_nop 0
Tv 38,v4,0
Tv 40,v4,6144
Mavv 20,34,96
E_ 132
Mavv 24,34,100
s_nop 0
Tv 42,v5,0
Tv 44,v5,6144
Mavv 4,30,100
E_ 133
Mavv 8,30,104
E_ 134
Mavv 28,34,104
E_ 135
Mavv 32,34,108
C_ 135,134,135
C_ 134,132,133
Mavv 12,30,108
E_ 76
Mavv 16,30,112
E_ 77
Mavv 36,34,112
W_ 0
E_ 78
Mavv 56,38,112
s_nop 0
Tv 30,v2,1024
Tv 32,v2,7168
Mavv 76,42,112
E_ 79
Mavv 72,42,108
s_nop 0
Tv 34,v3,1024
Tv 36,v3,7168
Mavv 52,38,108
C_ 116,76,77
C_ 117,78,79
Mavv 48,38,104
E_ 80
Mavv 68,42,104
E_ 81
Mavv 64,42,100
E_ 82
Mavv 44,38,100
E_ 83
Mavv 40,38,96
C_ 120,80,81
C_ 121,82,83
Mavv 60,42,96
W_ 0
E_ 84
Mavv 80,30,96
s_nop 0
Tv 38,v4,1024
Tv 40,v4,7168
Mavv 100,34,96
E_ 85
Mavv 104,34,100
s_nop 0
Tv 42,v5,1024
Tv 44,v5,7168
Mavv 84,30,100
E_ 86
Mavv 88,30,104
E_ 87
Mavv 108,34,104
C_ 124,84,85
C_ 125,86,87
Mavv 112,34,108
E_ 88
Mavv 92,30,108
E_ 89
Mavv 96,30,112
E_ 90
Mavv 116,34,112
W_ 0
E_ 91
Mavv 136,38,112
s_nop 0
Tv 30,v2,2048
Tv 32,v2,8192
Mavv 156,42,112
C_ 128,88,89
C_ 129,90,91
Mavv 152,42,108
s_nop 0
Tv 34,v3,2048
Tv 36,v3,8192
Mavv 132,38,108
E_ 92
Mavv 128,38,104
E_ 93
Mavv 148,42,104
E_ 94
Mavv 144,42,100
E_ 95
Mavv 124,38,100
C_ 132,92,93
C_ 133,94,95
Mavv 120,38,96
v_mov_b32 v92, 0x3f803f80
v_mov_b32 v93, 0x3f803f80
Mavv 140,42,96
W_ 0
v_mov_b32 v94, 0x3f803f80
v_mov_b32 v95, 0x3f803f80
Mavv 160,30,96
s_nop 0
Tv 38,v4,2048
Tv 40,v4,8192
Mavv 180,34,96
Mavv 184,34,100
s_nop 0
Tv 42,v5,2048
Tv 44,v5,8192
Mavv 164,30,100
Mavv 168,30,104
Mavv 188,34,104
S_ s65,s61,61440
Mavv 192,34,108
S_ m0,s51,0
Mavv 172,30,108
s_waitcnt vmcnt(6) lgkmcnt(0)
s_barrier
Mavv 176,30,112
Ln v7,32,s65,0
Mavv 196,34,112
Mavv 216,38,112
Dv 30,%0,18432
Mavv 236,42,112
Dv 34,v1,18432
Mavv 232,42,108
Ln v8,32,s65,3072
S_ m0,s51,896
Mavv 212,38,108
Mavv 208,38,104
Ln v7,32,s65,128
Mavv 228,42,104
Mavv 224,42,100
Ln v8,32,s65,3200
S_ m0,s51,1792
Mavv 204,38,100
Mavv 200,38,96
Ln v7,32,s65,256
Mavv 220,42,96
.Lstep1_4:
Zvvv 76,92,116
Dv 38,%0,19456
Zvvv 80,92,120
Dv 42,v1,19456
Zvvv 84,92,124
Zvvv 88,92,128
Zvvv 92,92,132
W_ 2
Nvvv 96,30,136,56
Mvvv 96,34,156
Nvvv 100,30,140,60
s_nop 1
v_max3_f32 v10, v76, v80, v84
s_nop 0
v_max3_f32 v10, v10, v88, v92
Mvvv 100,34,160
v_cmp_lt_f32 vcc, 0x42000000, v10
s_cbranch_vccnz .Ltramp1_3
Nvvv 104,30,144,64
A_ v46,v46,v76
A_ v47,v47,v80
Mvvv 104,34,164
A_ v48,v48,v84
A_ v49,v49,v88
Nvvv 108,30,148,68
A_ v50,v50,v92
Mvvv 108,34,168
Ln v8,32,s65,3328
Nvvv 112,30,152,72
Mvvv 112,34,172
W_ 0
Mvvv 112,38,192
Dv 30,%0,20480
Mvvv 112,42,212
Dv 34,v1,20480
Mvvv 108,38,188
Mvvv 108,42,208
Mvvv 104,38,184
Mvvv 104,42,204
Mvvv 100,38,180
Mvvv 100,42,200
Mvvv 96,38,176
Mvvv 96,42,196
W_ 0
Mvvv 96,30,216
Dv 38,%0,12288
Mvvv 96,34,236
Dv 42,v1,12288
Mvvv 100,30,220
Mvvv 100,34,240
Mvvv 104,30,224
Mvvv 104,34,244
Mvvv 108,30,228
Mvvv 108,34,248
s_nop 0
E_ 96
Mvvv 112,30,232
E_ 97
Mvvv 112,34,252
W_ 0
E_ 98
Nvvv 76,38,136,56
Dv 30,%0,13312
Mvvv 76,42,156
Dv 34,v1,13312
Nvvv 80,38,140,60
E_ 99
Mvvv 80,42,160
C_ 99,98,99
C_ 98,96,97
Nvvv 84,38,144,64
E_ 100
Mvvv 84,42,164
E_ 101
Nvvv 88,38,148,68
E_ 102
Mvvv 88,42,168
E_ 103
Nvvv 92,38,152,72
C_ 103,102,103
C_ 102,100,101
Mvvv 92,42,172
W_ 0
E_ 104
Mvvv 92,30,192
Dv 38,%0,14336
Mvvv 92,34,212
Dv 42,v1,14336
Mvvv 88,30,188
E_ 105
Mvvv 88,34,208
E_ 106
Mvvv 84,30,184
S_ s65,s61,49152
E_ 107
Mvvv 84,34,204
S_ m0,s51,12288
C_ 107,106,107
C_ 106,104,105
Mvvv 80,30,180
s_waitcnt vmcnt(6) lgkmcnt(0)
s_barrier
Mvvv 80,34,200
L_ v9,36,s65,0
Mvvv 76,30,176
E_ 108
Mvvv 76,34,196
L_ v9,36,s65,3072
S_ m0,s51,13184
Mvvv 76,38,216
Tv 30,v2,24576
Tv 32,v2,30720
Mvvv 76,42,236
L_ v9,36,s65,128
Mvvv 80,38,220
s_nop 0
Tv 34,v3,24576
Tv 36,v3,30720
Mvvv 80,42,240
L_ v9,36,s65,3200
S_ m0,s51,14080
Mvvv 84,38,224
E_ 109
Mvvv 84,42,244
L_ v9,36,s65,256
Mvvv 88,38,228
E_ 110
Mvvv 88,42,248
L_ v9,36,s65,3328
Mvvv 92,38,232
E_ 111
Mvvv 92,42,252
W_ 0
C_ 111,110,111
C_ 110,108,109
Mavv 0,30,116
s_nop 0
Tv 38,v4,24576
Tv 40,v4,30720
Mavv 20,34,116
E_ 112
Mavv 24,34,120
s_nop 0
Tv 42,v5,24576
Tv 44,v5,30720
Mavv 4,30,120
E_ 113
Mavv 8,30,124
E_ 114
Mavv 28,34,124
E_ 115
Mavv 32,34,128
C_ 115,114,115
C_ 114,112,113
Mavv 12,30,128
E_ 76
Mavv 16,30,132
E_ 77
Mavv 36,34,132
W_ 0
E_ 78
Mavv 56,38,132
s_nop 0
Tv 30,v2,25600
Tv 32,v2,31744
Mavv 76,42,132
E_ 79
Mavv 72,42,128
s_nop 0
Tv 34,v3,25600
Tv 36,v3,31744
Mavv 52,38,128
C_ 96,76,77
C_ 97,78,79
Mavv 48,38,124
E_ 80
Mavv 68,42,124
E_ 81
Mavv 64,42,120
E_ 82
Mavv 44,38,120
E_ 83
Mavv 40,38,116
C_ 100,80,81
C_ 101,82,83
Mavv 60,42,116
W_ 0
E_ 84
Mavv 80,30,116
s_nop 0
Tv 38,v4,25600
Tv 40,v4,31744
Mavv 100,34,116
E_ 85
Mavv 104,34,120
s_nop 0
Tv 42,v5,25600
Tv 44,v5,31744
Mavv 84,30,120
E_ 86
Mavv 88,30,124
E_ 87
Mavv 108,34,124
C_ 104,84,85
C_ 105,86,87
Mavv 112,34,128
E_ 88
Mavv 92,30,128
E_ 89
Mavv 96,30,132
E_ 90
Mavv 116,34,132
W_ 0
E_ 91
Mavv 136,38,132
s_nop 0
Tv 30,v2,26624
Tv 32,v2,32768
Mavv 156,42,132
C_ 108,88,89
C_ 109,90,91
Mavv 152,42,128
s_nop 0
Tv 34,v3,26624
Tv 36,v3,32768
Mavv 132,38,128
E_ 92
Mavv 128,38,124
E_ 93
Mavv 148,42,124
E_ 94
Mavv 144,42,120
E_ 95
Mavv 124,38,120
C_ 112,92,93
C_ 113,94,95
Mavv 120,38,116
v_mov_b32 v92, 0x3f803f80
v_mov_b32 v93, 0x3f803f80
Mavv 140,42,116
W_ 0
v_mov_b32 v94, 0x3f803f80
v_mov_b32 v95, 0x3f803f80
Mavv 160,30,116
s_nop 0
Tv 38,v4,26624
Tv 40,v4,32768
Mavv 180,34,116
Mavv 184,34,120
s_nop 0
Tv 42,v5,26624
Tv 44,v5,32768
Mavv 164,30,120
Mavv 168,30,124
Mavv 188,34,124
S_ s65,s61,73728
Mavv 192,34,128
S_ m0,s51,24576
Mavv 172,30,128
s_waitcnt vmcnt(6) lgkmcnt(0)
s_barrier
Mavv 176,30,132
Ln v7,32,s65,0
Mavv 196,34,132
Mavv 216,38,132
Dv 30,%0,6144
Mavv 236,42,132
Dv 34,v1,6144
Mavv 232,42,128
Ln v8,32,s65,3072
S_ m0,s51,25472
Mavv 212,38,128
Mavv 208,38,124
Ln v7,32,s65,128
Mavv 228,42,124
Mavv 224,42,120
Ln v8,32,s65,3200
S_ m0,s51,26368
Mavv 204,38,120
Mavv 200,38,116
Ln v7,32,s65,256
Mavv 220,42,116
s_cmp_eq_u32 s4, 0
s_cbranch_scc1 .Lhs_b1
.Lhs_b1_back:
.Lstep1_5:
Zvvv 76,92,96
Dv 38,%0,7168
Zvvv 80,92,100
Dv 42,v1,7168
Zvvv 84,92,104
Zvvv 88,92,108
Zvvv 92,92,112
W_ 2
Nvvv 116,30,136,56
Mvvv 116,34,156
Nvvv 120,30,140,60
s_nop 1
v_max3_f32 v10, v76, v80, v84
s_nop 0
v_max3_f32 v10, v10, v88, v92
Mvvv 120,34,160
v_cmp_lt_f32 vcc, 0x42000000, v10
s_cbranch_vccnz .Ltramp1_4
Nvvv 124,30,144,64
A_ v46,v46,v76
A_ v47,v47,v80
Mvvv 124,34,164
A_ v48,v48,v84
A_ v49,v49,v88
Nvvv 128,30,148,68
A_ v50,v50,v92
Mvvv 128,34,168
Ln v8,32,s65,3328
Nvvv 132,30,152,72
Mvvv 132,34,172
W_ 0
Mvvv 132,38,192
Dv 30,%0,8192
Mvvv 132,42,212
Dv 34,v1,8192
Mvvv 128,38,188
Mvvv 128,42,208
Mvvv 124,38,184
Mvvv 124,42,204
Mvvv 120,38,180
Mvvv 120,42,200
Mvvv 116,38,176
Mvvv 116,42,196
W_ 0
Mvvv 116,30,216
Dv 38,%0,0
Mvvv 116,34,236
Dv 42,v1,0
Mvvv 120,30,220
Mvvv 120,34,240
Mvvv 124,30,224
Mvvv 124,34,244
Mvvv 128,30,228
Mvvv 128,34,248
s_nop 0
E_ 116
Mvvv 132,30,232
E_ 117
Mvvv 132,34,252
W_ 0
E_ 118
Nvvv 76,38,136,56
Dv 30,%0,1024
Mvvv 76,42,156
Dv 34,v1,1024
Nvvv 80,38,140,60
E_ 119
Mvvv 80,42,160
C_ 119,118,119
C_ 118,116,117
Nvvv 84,38,144,64
E_ 120
Mvvv 84,42,164
E_ 121
Nvvv 88,38,148,68
E_ 122
Mvvv 88,42,168
E_ 123
Nvvv 92,38,152,72
C_ 123,122,123
C_ 122,120,121
Mvvv 92,42,172
W_ 0
E_ 124
Mvvv 92,30,192
Dv 38,%0,2048
Mvvv 92,34,212
Dv 42,v1,2048
Mvvv 88,30,188
E_ 125
Mvvv 88,34,208
E_ 126
Mvvv 84,30,184
S_ s65,s61,61440
E_ 127
Mvvv 84,34,204
S_ m0,s51,0
C_ 127,126,127
C_ 126,124,125
Mvvv 80,30,180
s_waitcnt vmcnt(6) lgkmcnt(0)
s_barrier
Mvvv 80,34,200
L_ v9,36,s65,0
Mvvv 76,30,176
E_ 128
Mvvv 76,34,196
L_ v9,36,s65,3072
S_ m0,s51,896
Mvvv 76,38,216
Tv 30,v2,12288
Tv 32,v2,18432
Mvvv 76,42,236
L_ v9,36,s65,128
Mvvv 80,38,220
s_nop 0
Tv 34,v3,12288
Tv 36,v3,18432
Mvvv 80,42,240
L_ v9,36,s65,3200
S_ m0,s51,1792
Mvvv 84,38,224
E_ 129
Mvvv 84,42,244
L_ v9,36,s65,256
Mvvv 88,38,228
E_ 130
Mvvv 88,42,248
L_ v9,36,s65,3328
Mvvv 92,38,232
E_ 131
Mvvv 92,42,252
W_ 0
C_ 131,130,131
C_ 130,128,129
Mavv 0,30,96
s_nop 0
Tv 38,v4,12288
Tv 40,v4,18432
Mavv 20,34,96
E_ 132
Mavv 24,34,100
s_nop 0
Tv 42,v5,12288
Tv 44,v5,18432
Mavv 4,30,100
E_ 133
Mavv 8,30,104
E_ 134
Mavv 28,34,104
E_ 135
Mavv 32,34,108
C_ 135,134,135
C_ 134,132,133
Mavv 12,30,108
E_ 76
Mavv 16,30,112
E_ 77
Mavv 36,34,112
W_ 0
E_ 78
Mavv 56,38,112
s_nop 0
Tv 30,v2,13312
Tv 32,v2,19456
Mavv 76,42,112
E_ 79
Mavv 72,42,108
s_nop 0
Tv 34,v3,13312
Tv 36,v3,19456
Mavv 52,38,108
C_ 116,76,77
C_ 117,78,79
Mavv 48,38,104
E_ 80
Mavv 68,42,104
E_ 81
Mavv 64,42,100
E_ 82
Mavv 44,38,100
E_ 83
Mavv 40,38,96
C_ 120,80,81
C_ 121,82,83
Mavv 60,42,96
W_ 0
E_ 84
Mavv 80,30,96
s_nop 0
Tv 38,v4,13312
Tv 40,v4,19456
Mavv 100,34,96
E_ 85
Mavv 104,34,100
s_nop 0
Tv 42,v5,13312
Tv 44,v5,19456
Mavv 84,30,100
E_ 86
Mavv 88,30,104
E_ 87
Mavv 108,34,104
C_ 124,84,85
C_ 125,86,87
Mavv 112,34,108
E_ 88
Mavv 92,30,108
E_ 89
Mavv 96,30,112
E_ 90
Mavv 116,34,112
W_ 0
E_ 91
Mavv 136,38,112
s_nop 0
Tv 30,v2,14336
Tv 32,v2,20480
Mavv 156,42,112
C_ 128,88,89
C_ 129,90,91
Mavv 152,42,108
s_nop 0
Tv 34,v3,14336
Tv 36,v3,20480
Mavv 132,38,108
E_ 92
Mavv 128,38,104
E_ 93
Mavv 148,42,104
E_ 94
Mavv 144,42,100
E_ 95
Mavv 124,38,100
C_ 132,92,93
C_ 133,94,95
Mavv 120,38,96
v_mov_b32 v92, 0x3f803f80
v_mov_b32 v93, 0x3f803f80
Mavv 140,42,96
W_ 0
v_mov_b32 v94, 0x3f803f80
v_mov_b32 v95, 0x3f803f80
Mavv 160,30,96
s_nop 0
Tv 38,v4,14336
Tv 40,v4,20480
Mavv 180,34,96
Mavv 184,34,100
s_nop 0
Tv 42,v5,14336
Tv 44,v5,20480
Mavv 164,30,100
Mavv 168,30,104
Mavv 188,34,104
S_ s65,s61,86016
Mavv 192,34,108
S_ m0,s51,12288
Mavv 172,30,108
s_waitcnt vmcnt(6) lgkmcnt(0)
s_barrier
Mavv 176,30,112
Ln v7,32,s65,0
Mavv 196,34,112
Mavv 216,38,112
Dv 30,%0,30720
Mavv 236,42,112
Dv 34,v1,30720
Mavv 232,42,108
Ln v8,32,s65,3072
S_ m0,s51,13184
Mavv 212,38,108
Mavv 208,38,104
Ln v7,32,s65,128
Mavv 228,42,104
Mavv 224,42,100
Ln v8,32,s65,3200
S_ m0,s51,14080
Mavv 204,38,100
Mavv 200,38,96
Ln v7,32,s65,256
Mavv 220,42,96
.Lstep1_6:
Zvvv 76,92,116
Dv 38,%0,31744
Zvvv 80,92,120
Dv 42,v1,31744
Zvvv 84,92,124
Zvvv 88,92,128
Zvvv 92,92,132
W_ 2
Nvvv 96,30,136,56
Mvvv 96,34,156
Nvvv 100,30,140,60
s_nop 1
v_max3_f32 v10, v76, v80, v84
s_nop 0
v_max3_f32 v10, v10, v88, v92
Mvvv 100,34,160
v_cmp_lt_f32 vcc, 0x42000000, v10
s_cbranch_vccnz .Ltramp1_5
Nvvv 104,30,144,64
A_ v46,v46,v76
A_ v47,v47,v80
Mvvv 104,34,164
A_ v48,v48,v84
A_ v49,v49,v88
Nvvv 108,30,148,68
A_ v50,v50,v92
Mvvv 108,34,168
Ln v8,32,s65,3328
Nvvv 112,30,152,72
R_ v11,a248
Mvvv 112,34,172
W_ 0
)A"
R"A(v_min_u32 v11, 240, v11
Mvvv 112,38,192
Dv 30,%0,32768
v_lshlrev_b32 v11, 3, v11
Mvvv 112,42,212
Dv 34,v1,32768
Mvvv 108,38,188
buffer_load_dword a254, v11, s[44:47], s66 offen
Mvvv 108,42,208
Mvvv 104,38,184
Mvvv 104,42,204
Mvvv 100,38,180
Mvvv 100,42,200
Mvvv 96,38,176
Mvvv 96,42,196
W_ 0
Mvvv 96,30,216
Dv 38,%0,24576
Mvvv 96,34,236
Dv 42,v1,24576
Mvvv 100,30,220
Mvvv 100,34,240
Mvvv 104,30,224
Mvvv 104,34,244
Mvvv 108,30,228
Mvvv 108,34,248
s_nop 0
E_ 96
Mvvv 112,30,232
E_ 97
Mvvv 112,34,252
W_ 0
E_ 98
Nvvv 76,38,136,56
Dv 30,%0,25600
Mvvv 76,42,156
Dv 34,v1,25600
Nvvv 80,38,140,60
E_ 99
Mvvv 80,42,160
C_ 99,98,99
C_ 98,96,97
Nvvv 84,38,144,64
E_ 100
Mvvv 84,42,164
E_ 101
Nvvv 88,38,148,68
E_ 102
Mvvv 88,42,168
E_ 103
Nvvv 92,38,152,72
C_ 103,102,103
C_ 102,100,101
Mvvv 92,42,172
W_ 0
E_ 104
Mvvv 92,30,192
Dv 38,%0,26624
Mvvv 92,34,212
Dv 42,v1,26624
Mvvv 88,30,188
E_ 105
Mvvv 88,34,208
E_ 106
Mvvv 84,30,184
S_ s65,s61,73728
E_ 107
Mvvv 84,34,204
S_ m0,s51,24576
C_ 107,106,107
C_ 106,104,105
Mvvv 80,30,180
s_waitcnt vmcnt(7) lgkmcnt(0)
s_barrier
Mvvv 80,34,200
L_ v9,36,s65,0
Mvvv 76,30,176
E_ 108
Mvvv 76,34,196
L_ v9,36,s65,3072
S_ m0,s51,25472
Mvvv 76,38,216
Tv 30,v2,0
Tv 32,v2,6144
Mvvv 76,42,236
L_ v9,36,s65,128
Mvvv 80,38,220
s_nop 0
Tv 34,v3,0
Tv 36,v3,6144
Mvvv 80,42,240
L_ v9,36,s65,3200
S_ m0,s51,26368
Mvvv 84,38,224
E_ 109
Mvvv 84,42,244
L_ v9,36,s65,256
Mvvv 88,38,228
E_ 110
Mvvv 88,42,248
L_ v9,36,s65,3328
Mvvv 92,38,232
E_ 111
Mvvv 92,42,252
W_ 0
C_ 111,110,111
C_ 110,108,109
Mavv 0,30,116
s_nop 0
Tv 38,v4,0
Tv 40,v4,6144
Mavv 20,34,116
E_ 112
Mavv 24,34,120
s_nop 0
Tv 42,v5,0
Tv 44,v5,6144
Mavv 4,30,120
E_ 113
Mavv 8,30,124
E_ 114
Mavv 28,34,124
E_ 115
Mavv 32,34,128
C_ 115,114,115
C_ 114,112,113
Mavv 12,30,128
E_ 76
Mavv 16,30,132
E_ 77
Mavv 36,34,132
W_ 0
E_ 78
Mavv 56,38,132
s_nop 0
Tv 30,v2,1024
Tv 32,v2,7168
Mavv 76,42,132
E_ 79
Mavv 72,42,128
s_nop 0
Tv 34,v3,1024
Tv 36,v3,7168
Mavv 52,38,128
C_ 96,76,77
C_ 97,78,79
Mavv 48,38,124
E_ 80
Mavv 68,42,124
E_ 81
Mavv 64,42,120
E_ 82
Mavv 44,38,120
E_ 83
Mavv 40,38,116
C_ 100,80,81
C_ 101,82,83
Mavv 60,42,116
W_ 0
E_ 84
Mavv 80,30,116
s_nop 0
Tv 38,v4,1024
Tv 40,v4,7168
Mavv 100,34,116
E_ 85
Mavv 104,34,120
s_nop 0
Tv 42,v5,1024
Tv 44,v5,7168
Mavv 84,30,120
E_ 86
Mavv 88,30,124
E_ 87
Mavv 108,34,124
C_ 104,84,85
C_ 105,86,87
Mavv 112,34,128
E_ 88
Mavv 92,30,128
E_ 89
Mavv 96,30,132
E_ 90
Mavv 116,34,132
W_ 0
E_ 91
Mavv 136,38,132
s_nop 0
Tv 30,v2,2048
Tv 32,v2,8192
Mavv 156,42,132
C_ 108,88,89
C_ 109,90,91
Mavv 152,42,128
s_nop 0
Tv 34,v3,2048
Tv 36,v3,8192
Mavv 132,38,128
E_ 92
Mavv 128,38,124
E_ 93
Mavv 148,42,124
E_ 94
Mavv 144,42,120
E_ 95
Mavv 124,38,120
C_ 112,92,93
C_ 113,94,95
Mavv 120,38,116
v_mov_b32 v92, 0x3f803f80
v_mov_b32 v93, 0x3f803f80
Mavv 140,42,116
W_ 0
v_mov_b32 v94, 0x3f803f80
v_mov_b32 v95, 0x3f803f80
Mavv 160,30,116
s_nop 0
Tv 38,v4,2048
Tv 40,v4,8192
Mavv 180,34,116
Mavv 184,34,120
s_nop 0
Tv 42,v5,2048
Tv 44,v5,8192
Mavv 164,30,120
Mavv 168,30,124
Mavv 188,34,124
S_ s65,s61,98304
Mavv 192,34,128
S_ m0,s51,0
Mavv 172,30,128
s_waitcnt vmcnt(7) lgkmcnt(0)
s_barrier
Mavv 176,30,132
Ln v7,32,s65,0
Mavv 196,34,132
Mavv 216,38,132
Dv 30,%0,18432
Mavv 236,42,132
Dv 34,v1,18432
Mavv 232,42,128
Ln v8,32,s65,3072
S_ m0,s51,896
Mavv 212,38,128
Mavv 208,38,124
Ln v7,32,s65,128
Mavv 228,42,124
Mavv 224,42,120
Ln v8,32,s65,3200
S_ m0,s51,1792
Mavv 204,38,120
Mavv 200,38,116
Ln v7,32,s65,256
Mavv 220,42,116
s_cmp_eq_u32 s4, 0
s_cbranch_scc1 .Lhs_c1
.Lhs_c1_back:
.Lstep1_7:
Zvvv 76,92,96
Dv 38,%0,19456
Zvvv 80,92,100
Dv 42,v1,19456
Zvvv 84,92,104
Zvvv 88,92,108
Zvvv 92,92,112
W_ 2
Nvvv 116,30,136,56
Mvvv 116,34,156
Nvvv 120,30,140,60
s_nop 1
v_max3_f32 v10, v76, v80, v84
s_nop 0
v_max3_f32 v10, v10, v88, v92
Mvvv 120,34,160
v_cmp_lt_f32 vcc, 0x42000000, v10
s_cbranch_vccnz .Ltramp1_6
Nvvv 124,30,144,64
A_ v46,v46,v76
A_ v47,v47,v80
Mvvv 124,34,164
A_ v48,v48,v84
A_ v49,v49,v88
Nvvv 128,30,148,68
A_ v50,v50,v92
Mvvv 128,34,168
Ln v8,32,s65,3328
Nvvv 132,30,152,72
Mvvv 132,34,172
W_ 0
Mvvv 132,38,192
Dv 30,%0,20480
Mvvv 132,42,212
Dv 34,v1,20480
Mvvv 128,38,188
Mvvv 128,42,208
Mvvv 124,38,184
Mvvv 124,42,204
Mvvv 120,38,180
Mvvv 120,42,200
Mvvv 116,38,176
Mvvv 116,42,196
W_ 0
Mvvv 116,30,216
Dv 38,%0,12288
Mvvv 116,34,236
Dv 42,v1,12288
Mvvv 120,30,220
Mvvv 120,34,240
Mvvv 124,30,224
Mvvv 124,34,244
Mvvv 128,30,228
Mvvv 128,34,248
s_nop 0
E_ 116
Mvvv 132,30,232
E_ 117
Mvvv 132,34,252
W_ 0
E_ 118
Nvvv 76,38,136,56
Dv 30,%0,13312
Mvvv 76,42,156
Dv 34,v1,13312
Nvvv 80,38,140,60
E_ 119
Mvvv 80,42,160
C_ 119,118,119
C_ 118,116,117
Nvvv 84,38,144,64
E_ 120
Mvvv 84,42,164
E_ 121
Nvvv 88,38,148,68
E_ 122
Mvvv 88,42,168
E_ 123
Nvvv 92,38,152,72
C_ 123,122,123
C_ 122,120,121
Mvvv 92,42,172
W_ 0
E_ 124
Mvvv 92,30,192
Dv 38,%0,14336
Mvvv 92,34,212
Dv 42,v1,14336
Mvvv 88,30,188
E_ 125
Mvvv 88,34,208
E_ 126
Mvvv 84,30,184
S_ s65,s61,86016
E_ 127
Mvvv 84,34,204
S_ m0,s51,12288
C_ 127,126,127
C_ 126,124,125
Mvvv 80,30,180
s_waitcnt vmcnt(6) lgkmcnt(0)
s_barrier
Mvvv 80,34,200
L_ v9,36,s65,0
Mvvv 76,30,176
E_ 128
Mvvv 76,34,196
L_ v9,36,s65,3072
S_ m0,s51,13184
Mvvv 76,38,216
Tv 30,v2,24576
Tv 32,v2,30720
Mvvv 76,42,236
L_ v9,36,s65,128
Mvvv 80,38,220
s_nop 0
Tv 34,v3,24576
Tv 36,v3,30720
Mvvv 80,42,240
L_ v9,36,s65,3200
S_ m0,s51,14080
Mvvv 84,38,224
E_ 129
Mvvv 84,42,244
L_ v9,36,s65,256
Mvvv 88,38,228
E_ 130
Mvvv 88,42,248
L_ v9,36,s65,3328
Mvvv 92,38,232
E_ 131
Mvvv 92,42,252
W_ 0
C_ 131,130,131
C_ 130,128,129
Mavv 0,30,96
s_nop 0
Tv 38,v4,24576
Tv 40,v4,30720
Mavv 20,34,96
E_ 132
Mavv 24,34,100
s_nop 0
Tv 42,v5,24576
Tv 44,v5,30720
Mavv 4,30,100
E_ 133
Mavv 8,30,104
E_ 134
Mavv 28,34,104
E_ 135
Mavv 32,34,108
C_ 135,134,135
C_ 134,132,133
Mavv 12,30,108
E_ 76
Mavv 16,30,112
E_ 77
Mavv 36,34,112
W_ 0
E_ 78
Mavv 56,38,112
s_nop 0
Tv 30,v2,25600
Tv 32,v2,31744
Mavv 76,42,112
E_ 79
Mavv 72,42,108
s_nop 0
Tv 34,v3,25600
Tv 36,v3,31744
Mavv 52,38,108
C_ 116,76,77
C_ 117,78,79
Mavv 48,38,104
E_ 80
Mavv 68,42,104
E_ 81
Mavv 64,42,100
E_ 82
Mavv 44,38,100
E_ 83
Mavv 40,38,96
C_ 120,80,81
C_ 121,82,83
Mavv 60,42,96
W_ 0
E_ 84
Mavv 80,30,96
s_nop 0
Tv 38,v4,25600
Tv 40,v4,31744
Mavv 100,34,96
E_ 85
Mavv 104,34,100
s_nop 0
Tv 42,v5,25600
Tv 44,v5,31744
Mavv 84,30,100
E_ 86
Mavv 88,30,104
E_ 87
Mavv 108,34,104
C_ 124,84,85
C_ 125,86,87
Mavv 112,34,108
E_ 88
Mavv 92,30,108
E_ 89
Mavv 96,30,112
E_ 90
Mavv 116,34,112
W_ 0
E_ 91
Mavv 136,38,112
s_nop 0
Tv 30,v2,26624
Tv 32,v2,32768
Mavv 156,42,112
C_ 128,88,89
C_ 129,90,91
Mavv 152,42,108
s_nop 0
Tv 34,v3,26624
Tv 36,v3,32768
Mavv 132,38,108
E_ 92
Mavv 128,38,104
E_ 93
Mavv 148,42,104
E_ 94
Mavv 144,42,100
E_ 95
Mavv 124,38,100
C_ 132,92,93
C_ 133,94,95
Mavv 120,38,96
v_mov_b32 v92, 0x3f803f80
v_mov_b32 v93, 0x3f803f80
Mavv 140,42,96
W_ 0
v_mov_b32 v94, 0x3f803f80
v_mov_b32 v95, 0x3f803f80
Mavv 160,30,96
s_nop 0
Tv 38,v4,26624
Tv 40,v4,32768
Mavv 180,34,96
Mavv 184,34,100
s_nop 0
Tv 42,v5,26624
Tv 44,v5,32768
Mavv 164,30,100
Mavv 168,30,104
Mavv 188,34,104
S_ s65,s61,110592
Mavv 192,34,108
S_ m0,s51,24576
Mavv 172,30,108
s_waitcnt vmcnt(6) lgkmcnt(0)
s_barrier
Mavv 176,30,112
Ln v7,32,s65,0
Mavv 196,34,112
Mavv 216,38,112
Dv 30,%0,6144
Mavv 236,42,112
Dv 34,v1,6144
Mavv 232,42,108
Ln v8,32,s65,3072
S_ m0,s51,25472
Mavv 212,38,108
Mavv 208,38,104
Ln v7,32,s65,128
Mavv 228,42,104
Mavv 224,42,100
Ln v8,32,s65,3200
S_ m0,s51,26368
Mavv 204,38,100
Mavv 200,38,96
Ln v7,32,s65,256
Mavv 220,42,96
.Lstep1_8:
Zvvv 76,92,116
Dv 38,%0,7168
Zvvv 80,92,120
Dv 42,v1,7168
Zvvv 84,92,124
Zvvv 88,92,128
Zvvv 92,92,132
W_ 2
Nvvv 96,30,136,56
Mvvv 96,34,156
Nvvv 100,30,140,60
s_nop 1
v_max3_f32 v10, v76, v80, v84
s_nop 0
v_max3_f32 v10, v10, v88, v92
Mvvv 100,34,160
v_cmp_lt_f32 vcc, 0x42000000, v10
s_cbranch_vccnz .Ltramp1_7
Nvvv 104,30,144,64
A_ v46,v46,v76
A_ v47,v47,v80
Mvvv 104,34,164
A_ v48,v48,v84
A_ v49,v49,v88
Nvvv 108,30,148,68
A_ v50,v50,v92
Mvvv 108,34,168
Ln v8,32,s65,3328
Nvvv 112,30,152,72
Mvvv 112,34,172
W_ 0
Mvvv 112,38,192
Dv 30,%0,8192
Mvvv 112,42,212
Dv 34,v1,8192
Mvvv 108,38,188
Mvvv 108,42,208
Mvvv 104,38,184
Mvvv 104,42,204
Mvvv 100,38,180
Mvvv 100,42,200
Mvvv 96,38,176
Mvvv 96,42,196
W_ 0
Mvvv 96,30,216
Dv 38,%0,0
Mvvv 96,34,236
Dv 42,v1,0
Mvvv 100,30,220
Mvvv 100,34,240
Mvvv 104,30,224
Mvvv 104,34,244
Mvvv 108,30,228
Mvvv 108,34,248
s_nop 0
E_ 96
Mvvv 112,30,232
E_ 97
Mvvv 112,34,252
W_ 0
E_ 98
Nvvv 76,38,136,56
Dv 30,%0,1024
Mvvv 76,42,156
Dv 34,v1,1024
Nvvv 80,38,140,60
E_ 99
Mvvv 80,42,160
C_ 99,98,99
C_ 98,96,97
Nvvv 84,38,144,64
E_ 100
Mvvv 84,42,164
E_ 101
Nvvv 88,38,148,68
E_ 102
Mvvv 88,42,168
E_ 103
Nvvv 92,38,152,72
C_ 103,102,103
C_ 102,100,101
Mvvv 92,42,172
W_ 0
E_ 104
Mvvv 92,30,192
Dv 38,%0,2048
Mvvv 92,34,212
Dv 42,v1,2048
Mvvv 88,30,188
E_ 105
Mvvv 88,34,208
E_ 106
Mvvv 84,30,184
S_ s65,s61,98304
E_ 107
Mvvv 84,34,204
S_ m0,s51,0
C_ 107,106,107
C_ 106,104,105
Mvvv 80,30,180
s_waitcnt vmcnt(6) lgkmcnt(0)
s_barrier
Mvvv 80,34,200
L_ v9,36,s65,0
Mvvv 76,30,176
E_ 108
Mvvv 76,34,196
L_ v9,36,s65,3072
S_ m0,s51,896
Mvvv 76,38,216
Tv 30,v2,12288
Tv 32,v2,18432
Mvvv 76,42,236
L_ v9,36,s65,128
Mvvv 80,38,220
s_nop 0
Tv 34,v3,12288
Tv 36,v3,18432
Mvvv 80,42,240
L_ v9,36,s65,3200
S_ m0,s51,1792
Mvvv 84,38,224
E_ 109
Mvvv 84,42,244
L_ v9,36,s65,256
Mvvv 88,38,228
E_ 110
Mvvv 88,42,248
L_ v9,36,s65,3328
Mvvv 92,38,232
E_ 111
Mvvv 92,42,252
W_ 0
C_ 111,110,111
C_ 110,108,109
Mavv 0,30,116
s_nop 0
Tv 38,v4,12288
Tv 40,v4,18432
Mavv 20,34,116
E_ 112
Mavv 24,34,120
s_nop 0
Tv 42,v5,12288
Tv 44,v5,18432
Mavv 4,30,120
E_ 113
Mavv 8,30,124
E_ 114
Mavv 28,34,124
E_ 115
Mavv 32,34,128
C_ 115,114,115
C_ 114,112,113
Mavv 12,30,128
E_ 76
Mavv 16,30,132
E_ 77
Mavv 36,34,132
W_ 0
E_ 78
Mavv 56,38,132
s_nop 0
Tv 30,v2,13312
Tv 32,v2,19456
Mavv 76,42,132
E_ 79
Mavv 72,42,128
s_nop 0
Tv 34,v3,13312
Tv 36,v3,19456
Mavv 52,38,128
C_ 96,76,77
C_ 97,78,79
Mavv 48,38,124
E_ 80
Mavv 68,42,124
E_ 81
Mavv 64,42,120
E_ 82
Mavv 44,38,120
E_ 83
Mavv 40,38,116
C_ 100,80,81
C_ 101,82,83
Mavv 60,42,116
W_ 0
E_ 84
Mavv 80,30,116
s_nop 0
Tv 38,v4,13312
Tv 40,v4,19456
Mavv 100,34,116
E_ 85
Mavv 104,34,120
s_nop 0
Tv 42,v5,13312
Tv 44,v5,19456
Mavv 84,30,120
E_ 86
Mavv 88,30,124
E_ 87
Mavv 108,34,124
C_ 104,84,85
C_ 105,86,87
Mavv 112,34,128
E_ 88
Mavv 92,30,128
E_ 89
Mavv 96,30,132
E_ 90
Mavv 116,34,132
W_ 0
E_ 91
Mavv 136,38,132
s_nop 0
Tv 30,v2,14336
Tv 32,v2,20480
Mavv 156,42,132
C_ 108,88,89
C_ 109,90,91
Mavv 152,42,128
s_nop 0
Tv 34,v3,14336
Tv 36,v3,20480
Mavv 132,38,128
E_ 92
Mavv 128,38,124
E_ 93
Mavv 148,42,124
E_ 94
Mavv 144,42,120
E_ 95
Mavv 124,38,120
C_ 112,92,93
C_ 113,94,95
Mavv 120,38,116
v_mov_b32 v92, 0x3f803f80
v_mov_b32 v93, 0x3f803f80
Mavv 140,42,116
W_ 0
v_mov_b32 v94, 0x3f803f80
v_mov_b32 v95, 0x3f803f80
Mavv 160,30,116
s_nop 0
Tv 38,v4,14336
Tv 40,v4,20480
Mavv 180,34,116
Mavv 184,34,120
s_nop 0
Tv 42,v5,14336
Tv 44,v5,20480
Mavv 164,30,120
Mavv 168,30,124
Mavv 188,34,124
S_ s65,s61,122880
Mavv 192,34,128
S_ m0,s51,12288
Mavv 172,30,128
s_waitcnt vmcnt(6) lgkmcnt(0)
s_barrier
Mavv 176,30,132
Ln v7,32,s65,0
Mavv 196,34,132
Mavv 216,38,132
Dv 30,%0,30720
Mavv 236,42,132
Dv 34,v1,30720
Mavv 232,42,128
Ln v8,32,s65,3072
S_ m0,s51,13184
Mavv 212,38,128
Mavv 208,38,124
Ln v7,32,s65,128
Mavv 228,42,124
Mavv 224,42,120
Ln v8,32,s65,3200
S_ m0,s51,14080
Mavv 204,38,120
Mavv 200,38,116
Ln v7,32,s65,256
Mavv 220,42,116
s_cmp_eq_u32 s4, 0
s_cbranch_scc1 .Lhs_d1
.Lhs_d1_back:
.Lstep1_9:
Zvvv 76,92,96
Dv 38,%0,31744
Zvvv 80,92,100
Dv 42,v1,31744
Zvvv 84,92,104
Zvvv 88,92,108
Zvvv 92,92,112
W_ 2
Nvvv 116,30,136,56
Mvvv 116,34,156
Nvvv 120,30,140,60
s_nop 1
v_max3_f32 v10, v76, v80, v84
s_nop 0
v_max3_f32 v10, v10, v88, v92
Mvvv 120,34,160
v_cmp_lt_f32 vcc, 0x42000000, v10
s_cbranch_vccnz .Ltramp1_8
Nvvv 124,30,144,64
A_ v46,v46,v76
A_ v47,v47,v80
Mvvv 124,34,164
A_ v48,v48,v84
A_ v49,v49,v88
Nvvv 128,30,148,68
A_ v50,v50,v92
Mvvv 128,34,168
Ln v8,32,s65,3328
Nvvv 132,30,152,72
Mvvv 132,34,172
W_ 0
Mvvv 132,38,192
Dv 30,%0,32768
Mvvv 132,42,212
Dv 34,v1,32768
Mvvv 128,38,188
Mvvv 128,42,208
Mvvv 124,38,184
Mvvv 124,42,204
Mvvv 120,38,180
Mvvv 120,42,200
Mvvv 116,38,176
Mvvv 116,42,196
W_ 0
Mvvv 116,30,216
Dv 38,%0,24576
Mvvv 116,34,236
Dv 42,v1,24576
Mvvv 120,30,220
Mvvv 120,34,240
Mvvv 124,30,224
Mvvv 124,34,244
Mvvv 128,30,228
Mvvv 128,34,248
s_nop 0
E_ 116
Mvvv 132,30,232
E_ 117
Mvvv 132,34,252
W_ 0
E_ 118
Nvvv 76,38,136,56
Dv 30,%0,25600
Mvvv 76,42,156
Dv 34,v1,25600
Nvvv 80,38,140,60
E_ 119
Mvvv 80,42,160
C_ 119,118,119
C_ 118,116,117
Nvvv 84,38,144,64
E_ 120
Mvvv 84,42,164
E_ 121
Nvvv 88,38,148,68
E_ 122
Mvvv 88,42,168
E_ 123
Nvvv 92,38,152,72
C_ 123,122,123
C_ 122,120,121
Mvvv 92,42,172
W_ 0
E_ 124
Mvvv 92,30,192
Dv 38,%0,26624
Mvvv 92,34,212
Dv 42,v1,26624
Mvvv 88,30,188
E_ 125
Mvvv 88,34,208
E_ 126
Mvvv 84,30,184
S_ s65,s61,110592
E_ 127
Mvvv 84,34,204
S_ m0,s51,24576
C_ 127,126,127
C_ 126,124,125
Mvvv 80,30,180
s_waitcnt vmcnt(6) lgkmcnt(0)
s_barrier
Mvvv 80,34,200
L_ v9,36,s65,0
Mvvv 76,30,176
E_ 128
Mvvv 76,34,196
L_ v9,36,s65,3072
S_ m0,s51,25472
Mvvv 76,38,216
Tv 30,v2,0
Tv 32,v2,6144
Mvvv 76,42,236
L_ v9,36,s65,128
Mvvv 80,38,220
s_nop 0
Tv 34,v3,0
Tv 36,v3,6144
Mvvv 80,42,240
L_ v9,36,s65,3200
S_ m0,s51,26368
Mvvv 84,38,224
E_ 129
Mvvv 84,42,244
L_ v9,36,s65,256
Mvvv 88,38,228
E_ 130
Mvvv 88,42,248
L_ v9,36,s65,3328
Mvvv 92,38,232
E_ 131
Mvvv 92,42,252
W_ 0
C_ 131,130,131
C_ 130,128,129
Mavv 0,30,96
s_nop 0
Tv 38,v4,0
Tv 40,v4,6144
Mavv 20,34,96
E_ 132
Mavv 24,34,100
s_nop 0
Tv 42,v5,0
Tv 44,v5,6144
Mavv 4,30,100
E_ 133
Mavv 8,30,104
E_ 134
Mavv 28,34,104
E_ 135
Mavv 32,34,108
C_ 135,134,135
C_ 134,132,133
Mavv 12,30,108
E_ 76
Mavv 16,30,112
E_ 77
Mavv 36,34,112
W_ 0
E_ 78
Mavv 56,38,112
s_nop 0
Tv 30,v2,1024
Tv 32,v2,7168
Mavv 76,42,112
E_ 79
Mavv 72,42,108
s_nop 0
Tv 34,v3,1024
Tv 36,v3,7168
Mavv 52,38,108
C_ 116,76,77
C_ 117,78,79
Mavv 48,38,104
E_ 80
Mavv 68,42,104
E_ 81
Mavv 64,42,100
E_ 82
Mavv 44,38,100
E_ 83
Mavv 40,38,96
C_ 120,80,81
C_ 121,82,83
Mavv 60,42,96
W_ 0
E_ 84
Mavv 80,30,96
s_nop 0
Tv 38,v4,1024
Tv 40,v4,7168
Mavv 100,34,96
E_ 85
Mavv 104,34,100
s_nop 0
Tv 42,v5,1024
Tv 44,v5,7168
Mavv 84,30,100
E_ 86
Mavv 88,30,104
E_ 87
Mavv 108,34,104
C_ 124,84,85
C_ 125,86,87
Mavv 112,34,108
E_ 88
Mavv 92,30,108
E_ 89
Mavv 96,30,112
E_ 90
Mavv 116,34,112
W_ 0
E_ 91
Mavv 136,38,112
s_nop 0
Tv 30,v2,2048
Tv 32,v2,8192
Mavv 156,42,112
C_ 128,88,89
C_ 129,90,91
Mavv 152,42,108
s_nop 0
Tv 34,v3,2048
Tv 36,v3,8192
Mavv 132,38,108
E_ 92
Mavv 128,38,104
E_ 93
Mavv 148,42,104
E_ 94
Mavv 144,42,100
E_ 95
Mavv 124,38,100
C_ 132,92,93
C_ 133,94,95
Mavv 120,38,96
v_mov_b32 v92, 0x3f803f80
v_mov_b32 v93, 0x3f803f80
Mavv 140,42,96
W_ 0
v_mov_b32 v94, 0x3f803f80
v_mov_b32 v95, 0x3f803f80
Mavv 160,30,96
s_nop 0
Tv 38,v4,2048
Tv 40,v4,8192
Mavv 180,34,96
Mavv 184,34,100
s_nop 0
Tv 42,v5,2048
Tv 44,v5,8192
Mavv 164,30,100
Mavv 168,30,104
Mavv 188,34,104
S_ s65,s61,135168
Mavv 192,34,108
S_ m0,s51,0
Mavv 172,30,108
s_waitcnt vmcnt(6) lgkmcnt(0)
s_barrier
Mavv 176,30,112
Ln v7,32,s65,0
Mavv 196,34,112
Mavv 216,38,112
Dv 30,%0,18432
Mavv 236,42,112
Dv 34,v1,18432
Mavv 232,42,108
Ln v8,32,s65,3072
S_ m0,s51,896
Mavv 212,38,108
Mavv 208,38,104
Ln v7,32,s65,128
Mavv 228,42,104
Mavv 224,42,100
Ln v8,32,s65,3200
S_ m0,s51,1792
Mavv 204,38,100
Mavv 200,38,96
Ln v7,32,s65,256
Mavv 220,42,96
.Lstep1_10:
Zvvv 76,92,116
Dv 38,%0,19456
Zvvv 80,92,120
Dv 42,v1,19456
Zvvv 84,92,124
Zvvv 88,92,128
Zvvv 92,92,132
W_ 2
Nvvv 96,30,136,56
Mvvv 96,34,156
Nvvv 100,30,140,60
s_nop 1
v_max3_f32 v10, v76, v80, v84
s_nop 0
v_max3_f32 v10, v10, v88, v92
Mvvv 100,34,160
v_cmp_lt_f32 vcc, 0x42000000, v10
s_cbranch_vccnz .Ltramp1_9
Nvvv 104,30,144,64
A_ v46,v46,v76
A_ v47,v47,v80
Mvvv 104,34,164
A_ v48,v48,v84
A_ v49,v49,v88
Nvvv 108,30,148,68
A_ v50,v50,v92
Mvvv 108,34,168
Ln v8,32,s65,3328
Nvvv 112,30,152,72
Mvvv 112,34,172
W_ 0
Mvvv 112,38,192
Dv 30,%0,20480
Mvvv 112,42,212
Dv 34,v1,20480
Mvvv 108,38,188
Mvvv 108,42,208
Mvvv 104,38,184
Mvvv 104,42,204
Mvvv 100,38,180
Mvvv 100,42,200
Mvvv 96,38,176
Mvvv 96,42,196
W_ 0
Mvvv 96,30,216
Dv 38,%0,12288
Mvvv 96,34,236
Dv 42,v1,12288
Mvvv 100,30,220
Mvvv 100,34,240
Mvvv 104,30,224
Mvvv 104,34,244
Mvvv 108,30,228
Mvvv 108,34,248
s_nop 0
E_ 96
Mvvv 112,30,232
E_ 97
Mvvv 112,34,252
W_ 0
E_ 98
Nvvv 76,38,136,56
Dv 30,%0,13312
Mvvv 76,42,156
Dv 34,v1,13312
Nvvv 80,38,140,60
E_ 99
Mvvv 80,42,160
C_ 99,98,99
C_ 98,96,97
Nvvv 84,38,144,64
E_ 100
Mvvv 84,42,164
E_ 101
Nvvv 88,38,148,68
E_ 102
Mvvv 88,42,168
E_ 103
Nvvv 92,38,152,72
C_ 103,102,103
C_ 102,100,101
Mvvv 92,42,172
W_ 0
E_ 104
Mvvv 92,30,192
Dv 38,%0,14336
Mvvv 92,34,212
Dv 42,v1,14336
Mvvv 88,30,188
E_ 105
Mvvv 88,34,208
E_ 106
Mvvv 84,30,184
S_ s65,s61,122880
E_ 107
Mvvv 84,34,204
S_ m0,s51,12288
C_ 107,106,107
C_ 106,104,105
Mvvv 80,30,180
s_waitcnt vmcnt(6) lgkmcnt(0)
s_barrier
Mvvv 80,34,200
L_ v9,36,s65,0
Mvvv 76,30,176
E_ 108
Mvvv 76,34,196
L_ v9,36,s65,3072
S_ m0,s51,13184
Mvvv 76,38,216
Tv 30,v2,24576
Tv 32,v2,30720
Mvvv 76,42,236
L_ v9,36,s65,128
Mvvv 80,38,220
s_nop 0
Tv 34,v3,24576
Tv 36,v3,30720
Mvvv 80,42,240
L_ v9,36,s65,3200
S_ m0,s51,14080
Mvvv 84,38,224
E_ 109
Mvvv 84,42,244
L_ v9,36,s65,256
Mvvv 88,38,228
E_ 110
Mvvv 88,42,248
L_ v9,36,s65,3328
Mvvv 92,38,232
E_ 111
Mvvv 92,42,252
W_ 0
C_ 111,110,111
C_ 110,108,109
Mavv 0,30,116
s_nop 0
Tv 38,v4,24576
Tv 40,v4,30720
Mavv 20,34,116
E_ 112
Mavv 24,34,120
s_nop 0
Tv 42,v5,24576
Tv 44,v5,30720
Mavv 4,30,120
E_ 113
Mavv 8,30,124
E_ 114
Mavv 28,34,124
E_ 115
Mavv 32,34,128
C_ 115,114,115
C_ 114,112,113
Mavv 12,30,128
E_ 76
Mavv 16,30,132
E_ 77
Mavv 36,34,132
W_ 0
E_ 78
Mavv 56,38,132
s_nop 0
Tv 30,v2,25600
Tv 32,v2,31744
Mavv 76,42,132
E_ 79
Mavv 72,42,128
s_nop 0
Tv 34,v3,25600
Tv 36,v3,31744
Mavv 52,38,128
C_ 96,76,77
C_ 97,78,79
Mavv 48,38,124
E_ 80
Mavv 68,42,124
E_ 81
Mavv 64,42,120
E_ 82
Mavv 44,38,120
E_ 83
Mavv 40,38,116
C_ 100,80,81
C_ 101,82,83
Mavv 60,42,116
W_ 0
E_ 84
Mavv 80,30,116
s_nop 0
Tv 38,v4,25600
Tv 40,v4,31744
Mavv 100,34,116
E_ 85
Mavv 104,34,120
s_nop 0
Tv 42,v5,25600
Tv 44,v5,31744
Mavv 84,30,120
E_ 86
Mavv 88,30,124
E_ 87
Mavv 108,34,124
C_ 104,84,85
C_ 105,86,87
Mavv 112,34,128
E_ 88
Mavv 92,30,128
E_ 89
Mavv 96,30,132
E_ 90
Mavv 116,34,132
W_ 0
E_ 91
Mavv 136,38,132
s_nop 0
Tv 30,v2,26624
Tv 32,v2,32768
Mavv 156,42,132
C_ 108,88,89
C_ 109,90,91
Mavv 152,42,128
s_nop 0
Tv 34,v3,26624
Tv 36,v3,32768
Mavv 132,38,128
E_ 92
Mavv 128,38,124
E_ 93
Mavv 148,42,124
E_ 94
Mavv 144,42,120
E_ 95
Mavv 124,38,120
C_ 112,92,93
C_ 113,94,95
Mavv 120,38,116
v_mov_b32 v92, 0x3f803f80
v_mov_b32 v93, 0x3f803f80
Mavv 140,42,116
W_ 0
v_mov_b32 v94, 0x3f803f80
v_mov_b32 v95, 0x3f803f80
Mavv 160,30,116
s_nop 0
Tv 38,v4,26624
Tv 40,v4,32768
Mavv 180,34,116
Mavv 184,34,120
s_nop 0
Tv 42,v5,26624
Tv 44,v5,32768
Mavv 164,30,120
Mavv 168,30,124
Mavv 188,34,124
S_ s65,s61,147456
Mavv 192,34,128
S_ m0,s51,24576
Mavv 172,30,128
s_waitcnt vmcnt(6) lgkmcnt(0)
s_barrier
Mavv 176,30,132
Ln v7,32,s65,0
Mavv 196,34,132
Mavv 216,38,132
Dv 30,%0,6144
Mavv 236,42,132
Dv 34,v1,6144
Mavv 232,42,128
Ln v8,32,s65,3072
S_ m0,s51,25472
Mavv 212,38,128
Mavv 208,38,124
Ln v7,32,s65,128
Mavv 228,42,124
Mavv 224,42,120
Ln v8,32,s65,3200
S_ m0,s51,26368
Mavv 204,38,120
Mavv 200,38,116
Ln v7,32,s65,256
Mavv 220,42,116
.Lstep1_11:
Zvvv 76,92,96
Dv 38,%0,7168
Zvvv 80,92,100
Dv 42,v1,7168
Zvvv 84,92,104
Zvvv 88,92,108
Zvvv 92,92,112
W_ 2
Nvvv 116,30,136,56
Mvvv 116,34,156
Nvvv 120,30,140,60
s_nop 1
v_max3_f32 v10, v76, v80, v84
s_nop 0
v_max3_f32 v10, v10, v88, v92
Mvvv 120,34,160
v_cmp_lt_f32 vcc, 0x42000000, v10
s_cbranch_vccnz .Ltramp1_10
Nvvv 124,30,144,64
A_ v46,v46,v76
A_ v47,v47,v80
Mvvv 124,34,164
A_ v48,v48,v84
A_ v49,v49,v88
Nvvv 128,30,148,68
A_ v50,v50,v92
Mvvv 128,34,168
Ln v8,32,s65,3328
Nvvv 132,30,152,72
Mvvv 132,34,172
W_ 0
Mvvv 132,38,192
Dv 30,%0,8192
Mvvv 132,42,212
Dv 34,v1,8192
Mvvv 128,38,188
Mvvv 128,42,208
Mvvv 124,38,184
Mvvv 124,42,204
Mvvv 120,38,180
Mvvv 120,42,200
Mvvv 116,38,176
Mvvv 116,42,196
W_ 0
Mvvv 116,30,216
Dv 38,%0,0
Mvvv 116,34,236
Dv 42,v1,0
Mvvv 120,30,220
Mvvv 120,34,240
Mvvv 124,30,224
Mvvv 124,34,244
Mvvv 128,30,228
Mvvv 128,34,248
s_nop 0
E_ 116
Mvvv 132,30,232
E_ 117
Mvvv 132,34,252
W_ 0
E_ 118
Nvvv 76,38,136,56
Dv 30,%0,1024
Mvvv 76,42,156
Dv 34,v1,1024
Nvvv 80,38,140,60
E_ 119
Mvvv 80,42,160
C_ 119,118,119
C_ 118,116,117
Nvvv 84,38,144,64
E_ 120
Mvvv 84,42,164
E_ 121
Nvvv 88,38,148,68
E_ 122
Mvvv 88,42,168
E_ 123
Nvvv 92,38,152,72
C_ 123,122,123
C_ 122,120,121
Mvvv 92,42,172
W_ 0
E_ 124
Mvvv 92,30,192
Dv 38,%0,2048
Mvvv 92,34,212
Dv 42,v1,2048
Mvvv 88,30,188
E_ 125
Mvvv 88,34,208
E_ 126
Mvvv 84,30,184
S_ s65,s61,135168
E_ 127
Mvvv 84,34,204
S_ m0,s51,0
C_ 127,126,127
C_ 126,124,125
Mvvv 80,30,180
s_waitcnt vmcnt(6) lgkmcnt(0)
s_barrier
Mvvv 80,34,200
L_ v9,36,s65,0
Mvvv 76,30,176
E_ 128
Mvvv 76,34,196
L_ v9,36,s65,3072
S_ m0,s51,896
Mvvv 76,38,216
Tv 30,v2,12288
Tv 32,v2,18432
Mvvv 76,42,236
L_ v9,36,s65,128
Mvvv 80,38,220
s_nop 0
Tv 34,v3,12288
Tv 36,v3,18432
Mvvv 80,42,240
L_ v9,36,s65,3200
S_ m0,s51,1792
Mvvv 84,38,224
E_ 129
Mvvv 84,42,244
L_ v9,36,s65,256
Mvvv 88,38,228
E_ 130
Mvvv 88,42,248
L_ v9,36,s65,3328
Mvvv 92,38,232
E_ 131
Mvvv 92,42,252
W_ 0
C_ 131,130,131
C_ 130,128,129
Mavv 0,30,96
s_nop 0
Tv 38,v4,12288
Tv 40,v4,18432
Mavv 20,34,96
E_ 132
Mavv 24,34,100
s_nop 0
Tv 42,v5,12288
Tv 44,v5,18432
Mavv 4,30,100
E_ 133
Mavv 8,30,104
E_ 134
Mavv 28,34,104
E_ 135
Mavv 32,34,108
C_ 135,134,135
C_ 134,132,133
Mavv 12,30,108
E_ 76
Mavv 16,30,112
E_ 77
Mavv 36,34,112
W_ 0
E_ 78
Mavv 56,38,112
s_nop 0
Tv 30,v2,13312
Tv 32,v2,19456
Mavv 76,42,112
E_ 79
Mavv 72,42,108
s_nop 0
Tv 34,v3,13312
Tv 36,v3,19456
Mavv 52,38,108
C_ 116,76,77
C_ 117,78,79
Mavv 48,38,104
E_ 80
Mavv 68,42,104
E_ 81
Mavv 64,42,100
E_ 82
Mavv 44,38,100
E_ 83
Mavv 40,38,96
C_ 120,80,81
C_ 121,82,83
Mavv 60,42,96
W_ 0
E_ 84
Mavv 80,30,96
s_nop 0
Tv 38,v4,13312
Tv 40,v4,19456
Mavv 100,34,96
E_ 85
Mavv 104,34,100
s_nop 0
Tv 42,v5,13312
Tv 44,v5,19456
Mavv 84,30,100
E_ 86
Mavv 88,30,104
E_ 87
Mavv 108,34,104
C_ 124,84,85
C_ 125,86,87
Mavv 112,34,108
E_ 88
Mavv 92,30,108
E_ 89
Mavv 96,30,112
E_ 90
Mavv 116,34,112
W_ 0
E_ 91
Mavv 136,38,112
s_nop 0
Tv 30,v2,14336
Tv 32,v2,20480
Mavv 156,42,112
C_ 128,88,89
C_ 129,90,91
Mavv 152,42,108
s_nop 0
Tv 34,v3,14336
Tv 36,v3,20480
Mavv 132,38,108
E_ 92
Mavv 128,38,104
E_ 93
Mavv 148,42,104
E_ 94
Mavv 144,42,100
E_ 95
Mavv 124,38,100
C_ 132,92,93
C_ 133,94,95
Mavv 120,38,96
v_mov_b32 v92, 0x3f803f80
v_mov_b32 v93, 0x3f803f80
Mavv 140,42,96
W_ 0
v_mov_b32 v94, 0x3f803f80
v_mov_b32 v95, 0x3f803f80
Mavv 160,30,96
s_nop 0
Tv 38,v4,14336
Tv 40,v4,20480
Mavv 180,34,96
Mavv 184,34,100
s_nop 0
Tv 42,v5,14336
Tv 44,v5,20480
Mavv 164,30,100
Mavv 168,30,104
Mavv 188,34,104
S_ s65,s61,159744
Mavv 192,34,108
S_ m0,s51,12288
Mavv 172,30,108
s_waitcnt vmcnt(6) lgkmcnt(0)
s_barrier
Mavv 176,30,112
Ln v7,32,s65,0
Mavv 196,34,112
Mavv 216,38,112
Dv 30,%0,30720
Mavv 236,42,112
Dv 34,v1,30720
Mavv 232,42,108
Ln v8,32,s65,3072
S_ m0,s51,13184
Mavv 212,38,108
Mavv 208,38,104
Ln v7,32,s65,128
Mavv 228,42,104
Mavv 224,42,100
Ln v8,32,s65,3200
S_ m0,s51,14080
Mavv 204,38,100
Mavv 200,38,96
Ln v7,32,s65,256
Mavv 220,42,96
.Lstep1_12:
Zvvv 76,92,116
Dv 38,%0,31744
Zvvv 80,92,120
Dv 42,v1,31744
Zvvv 84,92,124
Zvvv 88,92,128
Zvvv 92,92,132
W_ 2
Nvvv 96,30,136,56
Mvvv 96,34,156
Nvvv 100,30,140,60
s_nop 1
v_max3_f32 v10, v76, v80, v84
s_nop 0
v_max3_f32 v10, v10, v88, v92
Mvvv 100,34,160
v_cmp_lt_f32 vcc, 0x42000000, v10
s_cbranch_vccnz .Ltramp1_11
Nvvv 104,30,144,64
A_ v46,v46,v76
A_ v47,v47,v80
Mvvv 104,34,164
A_ v48,v48,v84
A_ v49,v49,v88
Nvvv 108,30,148,68
A_ v50,v50,v92
Mvvv 108,34,168
Ln v8,32,s65,3328
Nvvv 112,30,152,72
R_ v11,a248
Mvvv 112,34,172
W_ 0
v_min_u32 v11, 240, v11
Mvvv 112,38,192
Dv 30,%0,32768
v_lshlrev_b32 v11, 3, v11
Mvvv 112,42,212
Dv 34,v1,32768
Mvvv 108,38,188
buffer_load_dword a254, v11, s[44:47], s66 offen
Mvvv 108,42,208
Mvvv 104,38,184
Mvvv 104,42,204
Mvvv 100,38,180
Mvvv 100,42,200
Mvvv 96,38,176
Mvvv 96,42,196
W_ 0
Mvvv 96,30,216
Dv 38,%0,24576
Mvvv 96,34,236
Dv 42,v1,24576
Mvvv 100,30,220
Mvvv 100,34,240
Mvvv 104,30,224
Mvvv 104,34,244
Mvvv 108,30,228
Mvvv 108,34,248
s_nop 0
E_ 96
Mvvv 112,30,232
E_ 97
Mvvv 112,34,252
W_ 0
E_ 98
Nvvv 76,38,136,56
Dv 30,%0,25600
Mvvv 76,42,156
Dv 34,v1,25600
Nvvv 80,38,140,60
E_ 99
Mvvv 80,42,160
C_ 99,98,99
C_ 98,96,97
Nvvv 84,38,144,64
E_ 100
Mvvv 84,42,164
E_ 101
Nvvv 88,38,148,68
E_ 102
Mvvv 88,42,168
E_ 103
Nvvv 92,38,152,72
C_ 103,102,103
C_ 102,100,101
Mvvv 92,42,172
W_ 0
E_ 104
Mvvv 92,30,192
Dv 38,%0,26624
Mvvv 92,34,212
Dv 42,v1,26624
Mvvv 88,30,188
E_ 105
Mvvv 88,34,208
E_ 106
Mvvv 84,30,184
S_ s65,s61,147456
E_ 107
Mvvv 84,34,204
S_ m0,s51,24576
C_ 107,106,107
C_ 106,104,105
Mvvv 80,30,180
s_waitcnt vmcnt(7) lgkmcnt(0)
s_barrier
Mvvv 80,34,200
Ln v9,36,s65,0
Mvvv 76,30,176
E_ 108
Mvvv 76,34,196
Ln v9,36,s65,3072
S_ m0,s51,25472
Mvvv 76,38,216
Tv 30,v2,0
Tv 32,v2,6144
Mvvv 76,42,236
Ln v9,36,s65,128
Mvvv 80,38,220
s_nop 0
Tv 34,v3,0
Tv 36,v3,6144
Mvvv 80,42,240
Ln v9,36,s65,3200
S_ m0,s51,26368
Mvvv 84,38,224
E_ 109
Mvvv 84,42,244
Ln v9,36,s65,256
Mvvv 88,38,228
E_ 110
Mvvv 88,42,248
Ln v9,36,s65,3328
Mvvv 92,38,232
E_ 111
Mvvv 92,42,252
W_ 0
C_ 111,110,111
C_ 110,108,109
Mavv 0,30,116
s_nop 0
Tv 38,v4,0
Tv 40,v4,6144
Mavv 20,34,116
E_ 112
Mavv 24,34,120
s_nop 0
Tv 42,v5,0
Tv 44,v5,6144
Mavv 4,30,120
E_ 113
Mavv 8,30,124
E_ 114
Mavv 28,34,124
E_ 115
Mavv 32,34,128
C_ 115,114,115
C_ 114,112,113
Mavv 12,30,128
E_ 76
Mavv 16,30,132
E_ 77
Mavv 36,34,132
W_ 0
E_ 78
Mavv 56,38,132
s_nop 0
Tv 30,v2,1024
Tv 32,v2,7168
Mavv 76,42,132
E_ 79
Mavv 72,42,128
s_nop 0
Tv 34,v3,1024
Tv 36,v3,7168
Mavv 52,38,128
C_ 96,76,77
C_ 97,78,79
Mavv 48,38,124
E_ 80
Mavv 68,42,124
E_ 81
Mavv 64,42,120
E_ 82
Mavv 44,38,120
E_ 83
Mavv 40,38,116
C_ 100,80,81
C_ 101,82,83
Mavv 60,42,116
W_ 0
E_ 84
Mavv 80,30,116
s_nop 0
Tv 38,v4,1024
Tv 40,v4,7168
Mavv 100,34,116
E_ 85
Mavv 104,34,120
s_nop 0
Tv 42,v5,1024
Tv 44,v5,7168
Mavv 84,30,120
E_ 86
Mavv 88,30,124
E_ 87
Mavv 108,34,124
C_ 104,84,85
C_ 105,86,87
Mavv 112,34,128
E_ 88
Mavv 92,30,128
E_ 89
Mavv 96,30,132
E_ 90
Mavv 116,34,132
W_ 0
E_ 91
Mavv 136,38,132
s_nop 0
Tv 30,v2,2048
Tv 32,v2,8192
Mavv 156,42,132
C_ 108,88,89
C_ 109,90,91
Mavv 152,42,128
s_nop 0
Tv 34,v3,2048
Tv 36,v3,8192
Mavv 132,38,128
E_ 92
Mavv 128,38,124
E_ 93
Mavv 148,42,124
E_ 94
Mavv 144,42,120
E_ 95
Mavv 124,38,120
C_ 112,92,93
C_ 113,94,95
Mavv 120,38,116
v_mov_b32 v92, 0x3f803f80
v_mov_b32 v93, 0x3f803f80
Mavv 140,42,116
W_ 0
v_mov_b32 v94, 0x3f803f80
v_mov_b32 v95, 0x3f803f80
Mavv 160,30,116
s_nop 0
Tv 38,v4,2048
Tv 40,v4,8192
Mavv 180,34,116
Mavv 184,34,120
s_nop 0
Tv 42,v5,2048
Tv 44,v5,8192
Mavv 164,30,120
Mavv 168,30,124
Mavv 188,34,124
S_ s65,s61,172032
Mavv 192,34,128
S_ m0,s51,0
Mavv 172,30,128
s_waitcnt vmcnt(7) lgkmcnt(0)
s_barrier
Mavv 176,30,132
Ln v7,32,s65,0
Mavv 196,34,132
Mavv 216,38,132
Dv 30,%0,18432
Mavv 236,42,132
Dv 34,v1,18432
Mavv 232,42,128
Ln v8,32,s65,3072
S_ m0,s51,896
Mavv 212,38,128
Mavv 208,38,124
Ln v7,32,s65,128
Mavv 228,42,124
Mavv 224,42,120
Ln v8,32,s65,3200
S_ m0,s51,1792
Mavv 204,38,120
Mavv 200,38,116
Ln v7,32,s65,256
Mavv 220,42,116
.Lstep1_13:
Zvvv 76,92,96
Dv 38,%0,19456
Zvvv 80,92,100
Dv 42,v1,19456
Zvvv 84,92,104
Zvvv 88,92,108
Zvvv 92,92,112
W_ 2
Nvvv 116,30,136,56
Mvvv 116,34,156
Nvvv 120,30,140,60
s_nop 1
v_max3_f32 v10, v76, v80, v84
s_nop 0
v_max3_f32 v10, v10, v88, v92
Mvvv 120,34,160
v_cmp_lt_f32 vcc, 0x42000000, v10
s_cbranch_vccnz .Ltramp1_12
Nvvv 124,30,144,64
A_ v46,v46,v76
A_ v47,v47,v80
Mvvv 124,34,164
A_ v48,v48,v84
A_ v49,v49,v88
Nvvv 128,30,148,68
A_ v50,v50,v92
Mvvv 128,34,168
Ln v8,32,s65,3328
Nvvv 132,30,152,72
Mvvv 132,34,172
W_ 0
Mvvv 132,38,192
Dv 30,%0,20480
Mvvv 132,42,212
Dv 34,v1,20480
Mvvv 128,38,188
Mvvv 128,42,208
)A"
R"A(Mvvv 124,38,184
Mvvv 124,42,204
Mvvv 120,38,180
Mvvv 120,42,200
Mvvv 116,38,176
Mvvv 116,42,196
W_ 0
Mvvv 116,30,216
Dv 38,%0,12288
Mvvv 116,34,236
Dv 42,v1,12288
Mvvv 120,30,220
Mvvv 120,34,240
Mvvv 124,30,224
Mvvv 124,34,244
Mvvv 128,30,228
Mvvv 128,34,248
s_nop 0
E_ 116
Mvvv 132,30,232
E_ 117
Mvvv 132,34,252
W_ 0
E_ 118
Nvvv 76,38,136,56
Dv 30,%0,13312
Mvvv 76,42,156
Dv 34,v1,13312
Nvvv 80,38,140,60
E_ 119
Mvvv 80,42,160
C_ 119,118,119
C_ 118,116,117
Nvvv 84,38,144,64
E_ 120
Mvvv 84,42,164
E_ 121
Nvvv 88,38,148,68
E_ 122
Mvvv 88,42,168
E_ 123
Nvvv 92,38,152,72
C_ 123,122,123
C_ 122,120,121
Mvvv 92,42,172
W_ 0
E_ 124
Mvvv 92,30,192
Dv 38,%0,14336
Mvvv 92,34,212
Dv 42,v1,14336
Mvvv 88,30,188
E_ 125
Mvvv 88,34,208
E_ 126
Mvvv 84,30,184
S_ s65,s61,159744
E_ 127
Mvvv 84,34,204
S_ m0,s51,12288
C_ 127,126,127
C_ 126,124,125
Mvvv 80,30,180
s_waitcnt vmcnt(6) lgkmcnt(0)
s_barrier
Mvvv 80,34,200
Ln v9,36,s65,0
Mvvv 76,30,176
E_ 128
Mvvv 76,34,196
Ln v9,36,s65,3072
S_ m0,s51,13184
Mvvv 76,38,216
Tv 30,v2,24576
Tv 32,v2,30720
Mvvv 76,42,236
Ln v9,36,s65,128
Mvvv 80,38,220
s_nop 0
Tv 34,v3,24576
Tv 36,v3,30720
Mvvv 80,42,240
Ln v9,36,s65,3200
S_ m0,s51,14080
Mvvv 84,38,224
E_ 129
Mvvv 84,42,244
Ln v9,36,s65,256
Mvvv 88,38,228
E_ 130
Mvvv 88,42,248
Ln v9,36,s65,3328
Mvvv 92,38,232
E_ 131
Mvvv 92,42,252
W_ 0
C_ 131,130,131
C_ 130,128,129
Mavv 0,30,96
s_nop 0
Tv 38,v4,24576
Tv 40,v4,30720
Mavv 20,34,96
E_ 132
Mavv 24,34,100
s_nop 0
Tv 42,v5,24576
Tv 44,v5,30720
Mavv 4,30,100
E_ 133
Mavv 8,30,104
E_ 134
Mavv 28,34,104
E_ 135
Mavv 32,34,108
C_ 135,134,135
C_ 134,132,133
Mavv 12,30,108
E_ 76
Mavv 16,30,112
E_ 77
Mavv 36,34,112
W_ 0
E_ 78
Mavv 56,38,112
s_nop 0
Tv 30,v2,25600
Tv 32,v2,31744
Mavv 76,42,112
E_ 79
Mavv 72,42,108
s_nop 0
Tv 34,v3,25600
Tv 36,v3,31744
Mavv 52,38,108
C_ 116,76,77
C_ 117,78,79
Mavv 48,38,104
E_ 80
Mavv 68,42,104
E_ 81
Mavv 64,42,100
E_ 82
Mavv 44,38,100
E_ 83
Mavv 40,38,96
C_ 120,80,81
C_ 121,82,83
Mavv 60,42,96
W_ 0
E_ 84
Mavv 80,30,96
s_nop 0
Tv 38,v4,25600
Tv 40,v4,31744
Mavv 100,34,96
E_ 85
Mavv 104,34,100
s_nop 0
Tv 42,v5,25600
Tv 44,v5,31744
Mavv 84,30,100
E_ 86
Mavv 88,30,104
E_ 87
Mavv 108,34,104
C_ 124,84,85
C_ 125,86,87
Mavv 112,34,108
E_ 88
Mavv 92,30,108
E_ 89
Mavv 96,30,112
E_ 90
Mavv 116,34,112
W_ 0
E_ 91
Mavv 136,38,112
s_nop 0
Tv 30,v2,26624
Tv 32,v2,32768
Mavv 156,42,112
C_ 128,88,89
C_ 129,90,91
Mavv 152,42,108
s_nop 0
Tv 34,v3,26624
Tv 36,v3,32768
Mavv 132,38,108
E_ 92
Mavv 128,38,104
E_ 93
Mavv 148,42,104
E_ 94
Mavv 144,42,100
E_ 95
Mavv 124,38,100
C_ 132,92,93
C_ 133,94,95
Mavv 120,38,96
v_mov_b32 v92, 0x3f803f80
v_mov_b32 v93, 0x3f803f80
Mavv 140,42,96
W_ 0
v_mov_b32 v94, 0x3f803f80
v_mov_b32 v95, 0x3f803f80
Mavv 160,30,96
s_nop 0
Tv 38,v4,26624
Tv 40,v4,32768
Mavv 180,34,96
Mavv 184,34,100
s_nop 0
Tv 42,v5,26624
Tv 44,v5,32768
Mavv 164,30,100
Mavv 168,30,104
Mavv 188,34,104
S_ s65,s61,184320
Mavv 192,34,108
S_ m0,s51,24576
Mavv 172,30,108
s_waitcnt vmcnt(6) lgkmcnt(0)
s_barrier
Mavv 176,30,112
Ln v7,32,s65,0
Mavv 196,34,112
Mavv 216,38,112
Dv 30,%0,6144
Mavv 236,42,112
Dv 34,v1,6144
Mavv 232,42,108
Ln v8,32,s65,3072
S_ m0,s51,25472
Mavv 212,38,108
Mavv 208,38,104
Ln v7,32,s65,128
Mavv 228,42,104
Mavv 224,42,100
Ln v8,32,s65,3200
S_ m0,s51,26368
Mavv 204,38,100
Mavv 200,38,96
Ln v7,32,s65,256
Mavv 220,42,96
.Lstep1_14:
Zvvv 76,92,116
Dv 38,%0,7168
Zvvv 80,92,120
Dv 42,v1,7168
Zvvv 84,92,124
Zvvv 88,92,128
Zvvv 92,92,132
W_ 2
Nvvv 96,30,136,56
Mvvv 96,34,156
Nvvv 100,30,140,60
s_nop 1
v_max3_f32 v10, v76, v80, v84
s_nop 0
v_max3_f32 v10, v10, v88, v92
Mvvv 100,34,160
v_cmp_lt_f32 vcc, 0x42000000, v10
s_cbranch_vccnz .Ltramp1_13
Nvvv 104,30,144,64
A_ v46,v46,v76
A_ v47,v47,v80
Mvvv 104,34,164
A_ v48,v48,v84
A_ v49,v49,v88
Nvvv 108,30,148,68
A_ v50,v50,v92
Mvvv 108,34,168
Ln v8,32,s65,3328
Nvvv 112,30,152,72
Mvvv 112,34,172
W_ 0
Mvvv 112,38,192
Dv 30,%0,8192
Mvvv 112,42,212
Dv 34,v1,8192
Mvvv 108,38,188
Mvvv 108,42,208
Mvvv 104,38,184
Mvvv 104,42,204
Mvvv 100,38,180
Mvvv 100,42,200
Mvvv 96,38,176
Mvvv 96,42,196
W_ 0
Mvvv 96,30,216
Dv 38,%0,0
Mvvv 96,34,236
Dv 42,v1,0
Mvvv 100,30,220
Mvvv 100,34,240
Mvvv 104,30,224
Mvvv 104,34,244
Mvvv 108,30,228
Mvvv 108,34,248
s_nop 0
E_ 96
Mvvv 112,30,232
E_ 97
Mvvv 112,34,252
W_ 0
E_ 98
Nvvv 76,38,136,56
Dv 30,%0,1024
Mvvv 76,42,156
Dv 34,v1,1024
Nvvv 80,38,140,60
E_ 99
Mvvv 80,42,160
C_ 99,98,99
C_ 98,96,97
Nvvv 84,38,144,64
E_ 100
Mvvv 84,42,164
E_ 101
Nvvv 88,38,148,68
E_ 102
Mvvv 88,42,168
E_ 103
Nvvv 92,38,152,72
C_ 103,102,103
C_ 102,100,101
Mvvv 92,42,172
W_ 0
E_ 104
Mvvv 92,30,192
Dv 38,%0,2048
Mvvv 92,34,212
Dv 42,v1,2048
Mvvv 88,30,188
E_ 105
Mvvv 88,34,208
E_ 106
Mvvv 84,30,184
S_ s65,s61,172032
E_ 107
Mvvv 84,34,204
S_ m0,s51,0
C_ 107,106,107
C_ 106,104,105
Mvvv 80,30,180
s_waitcnt vmcnt(6) lgkmcnt(0)
s_barrier
Mvvv 80,34,200
Ln v9,36,s65,0
Mvvv 76,30,176
E_ 108
Mvvv 76,34,196
Ln v9,36,s65,3072
S_ m0,s51,896
Mvvv 76,38,216
Tv 30,v2,12288
Tv 32,v2,18432
Mvvv 76,42,236
Ln v9,36,s65,128
Mvvv 80,38,220
s_nop 0
Tv 34,v3,12288
Tv 36,v3,18432
Mvvv 80,42,240
Ln v9,36,s65,3200
S_ m0,s51,1792
Mvvv 84,38,224
E_ 109
Mvvv 84,42,244
Ln v9,36,s65,256
Mvvv 88,38,228
E_ 110
Mvvv 88,42,248
Ln v9,36,s65,3328
Mvvv 92,38,232
E_ 111
Mvvv 92,42,252
W_ 0
C_ 111,110,111
C_ 110,108,109
Mavv 0,30,116
s_nop 0
Tv 38,v4,12288
Tv 40,v4,18432
Mavv 20,34,116
E_ 112
Mavv 24,34,120
s_nop 0
Tv 42,v5,12288
Tv 44,v5,18432
Mavv 4,30,120
E_ 113
Mavv 8,30,124
E_ 114
Mavv 28,34,124
E_ 115
Mavv 32,34,128
C_ 115,114,115
C_ 114,112,113
Mavv 12,30,128
E_ 76
Mavv 16,30,132
E_ 77
Mavv 36,34,132
W_ 0
E_ 78
Mavv 56,38,132
s_nop 0
Tv 30,v2,13312
Tv 32,v2,19456
Mavv 76,42,132
E_ 79
Mavv 72,42,128
s_nop 0
Tv 34,v3,13312
Tv 36,v3,19456
Mavv 52,38,128
C_ 96,76,77
C_ 97,78,79
Mavv 48,38,124
E_ 80
Mavv 68,42,124
E_ 81
Mavv 64,42,120
E_ 82
Mavv 44,38,120
E_ 83
Mavv 40,38,116
C_ 100,80,81
C_ 101,82,83
Mavv 60,42,116
W_ 0
E_ 84
Mavv 80,30,116
s_nop 0
Tv 38,v4,13312
Tv 40,v4,19456
Mavv 100,34,116
E_ 85
Mavv 104,34,120
s_nop 0
Tv 42,v5,13312
Tv 44,v5,19456
Mavv 84,30,120
E_ 86
Mavv 88,30,124
E_ 87
Mavv 108,34,124
C_ 104,84,85
C_ 105,86,87
Mavv 112,34,128
E_ 88
Mavv 92,30,128
E_ 89
Mavv 96,30,132
E_ 90
Mavv 116,34,132
W_ 0
E_ 91
Mavv 136,38,132
s_nop 0
Tv 30,v2,14336
Tv 32,v2,20480
Mavv 156,42,132
C_ 108,88,89
C_ 109,90,91
Mavv 152,42,128
s_nop 0
Tv 34,v3,14336
Tv 36,v3,20480
Mavv 132,38,128
E_ 92
Mavv 128,38,124
E_ 93
Mavv 148,42,124
E_ 94
Mavv 144,42,120
E_ 95
Mavv 124,38,120
C_ 112,92,93
C_ 113,94,95
Mavv 120,38,116
v_mov_b32 v92, 0x3f803f80
v_mov_b32 v93, 0x3f803f80
Mavv 140,42,116
W_ 0
v_mov_b32 v94, 0x3f803f80
v_mov_b32 v95, 0x3f803f80
Mavv 160,30,116
s_nop 0
Tv 38,v4,14336
Tv 40,v4,20480
Mavv 180,34,116
Mavv 184,34,120
s_nop 0
Tv 42,v5,14336
Tv 44,v5,20480
Mavv 164,30,120
Mavv 168,30,124
Mavv 188,34,124
S_ s65,s61,184320
Mavv 192,34,128
S_ m0,s51,12288
Mavv 172,30,128
s_waitcnt vmcnt(6) lgkmcnt(0)
s_barrier
Mavv 176,30,132
Ln v9,36,s65,0
Mavv 196,34,132
Mavv 216,38,132
Dv 30,%0,30720
Mavv 236,42,132
Dv 34,v1,30720
Mavv 232,42,128
Ln v9,36,s65,3072
S_ m0,s51,13184
Mavv 212,38,128
Mavv 208,38,124
Ln v9,36,s65,128
Mavv 228,42,124
Mavv 224,42,120
Ln v9,36,s65,3200
S_ m0,s51,14080
Mavv 204,38,120
Mavv 200,38,116
Ln v9,36,s65,256
Mavv 220,42,116
.Lstep1_15:
Zvvv 76,92,96
Dv 38,%0,31744
Zvvv 80,92,100
Dv 42,v1,31744
Zvvv 84,92,104
Zvvv 88,92,108
Zvvv 92,92,112
W_ 2
Nvvv 116,30,136,56
Mvvv 116,34,156
Nvvv 120,30,140,60
s_nop 1
v_max3_f32 v10, v76, v80, v84
s_nop 0
v_max3_f32 v10, v10, v88, v92
Mvvv 120,34,160
v_cmp_lt_f32 vcc, 0x42000000, v10
s_cbranch_vccnz .Ltramp1_14
Nvvv 124,30,144,64
A_ v46,v46,v76
A_ v47,v47,v80
Mvvv 124,34,164
A_ v48,v48,v84
A_ v49,v49,v88
Nvvv 128,30,148,68
A_ v50,v50,v92
Mvvv 128,34,168
Ln v9,36,s65,3328
Nvvv 132,30,152,72
Mvvv 132,34,172
W_ 0
Mvvv 132,38,192
Dv 30,%0,32768
Mvvv 132,42,212
Dv 34,v1,32768
Mvvv 128,38,188
Mvvv 128,42,208
Mvvv 124,38,184
Mvvv 124,42,204
Mvvv 120,38,180
Mvvv 120,42,200
Mvvv 116,38,176
Mvvv 116,42,196
W_ 0
Mvvv 116,30,216
Dv 38,%0,24576
Mvvv 116,34,236
Dv 42,v1,24576
Mvvv 120,30,220
Mvvv 120,34,240
Mvvv 124,30,224
Mvvv 124,34,244
Mvvv 128,30,228
Mvvv 128,34,248
s_nop 0
E_ 116
Mvvv 132,30,232
E_ 117
Mvvv 132,34,252
W_ 0
E_ 118
Nvvv 76,38,136,56
Dv 30,%0,25600
Mvvv 76,42,156
Dv 34,v1,25600
Nvvv 80,38,140,60
E_ 119
Mvvv 80,42,160
C_ 119,118,119
C_ 118,116,117
Nvvv 84,38,144,64
E_ 120
Mvvv 84,42,164
E_ 121
Nvvv 88,38,148,68
E_ 122
Mvvv 88,42,168
E_ 123
Nvvv 92,38,152,72
C_ 123,122,123
C_ 122,120,121
Mvvv 92,42,172
W_ 0
E_ 124
Mvvv 92,30,192
Dv 38,%0,26624
Mvvv 92,34,212
Dv 42,v1,26624
Mvvv 88,30,188
E_ 125
Mvvv 88,34,208
E_ 126
Mvvv 84,30,184
E_ 127
Mvvv 84,34,204
C_ 127,126,127
C_ 126,124,125
Mvvv 80,30,180
s_waitcnt vmcnt(6) lgkmcnt(0)
s_barrier
Mvvv 80,34,200
R_ v7,a248
Mvvv 76,30,176
v_lshrrev_b32 v7, 7, v7
Mvvv 76,34,196
v_mul_u32_u24 v7, 896, v7
Mvvv 76,38,216
s_nop 0
Tv 30,v2,0
Tv 32,v2,6144
Mvvv 76,42,236
v_add_u32 v7, v7, v9
Mvvv 80,38,220
S_ s71,s72,1024
Tv 34,v3,0
Tv 36,v3,6144
Mvvv 80,42,240
S_ s55,s71,491520
E_ 128
Mvvv 84,38,224
S_ m0,s51,24576
E_ 129
Mvvv 84,42,244
L_ v7,24,s55,0
Mvvv 88,38,228
S_ s55,s71,501760
E_ 130
Mvvv 88,42,248
S_ m0,s51,25600
E_ 131
Mvvv 92,38,232
L_ v7,24,s55,0
Mvvv 92,42,252
S_ s55,s71,512000
C_ 131,130,131
C_ 130,128,129
W_ 2
Mavv 0,30,96
S_ m0,s51,26624
Tv 38,v4,0
Tv 40,v4,6144
W_ 2
Mavv 20,34,96
L_ v7,24,s55,0
Mavv 24,34,100
S_ s55,s71,522240
Tv 42,v5,0
Tv 44,v5,6144
Mavv 4,30,100
S_ m0,s51,27648
E_ 132
Mavv 8,30,104
L_ v7,24,s55,0
Mavv 28,34,104
S_ s55,s71,532480
E_ 133
Mavv 32,34,108
S_ m0,s51,28672
E_ 134
Mavv 12,30,108
L_ v7,24,s55,0
Mavv 16,30,112
S_ s55,s71,542720
E_ 135
Mavv 36,34,112
S_ m0,s51,29696
C_ 135,134,135
C_ 134,132,133
W_ 2
Mavv 56,38,112
Tv 30,v2,1024
Tv 32,v2,7168
W_ 2
Mavv 76,42,112
L_ v7,24,s55,0
Mavv 72,42,108
Tv 34,v3,1024
Tv 36,v3,7168
Mavv 52,38,108
E_ 76
Mavv 48,38,104
E_ 77
Mavv 68,42,104
E_ 78
Mavv 64,42,100
E_ 79
Mavv 44,38,100
C_ 116,76,77
C_ 117,78,79
Mavv 40,38,96
E_ 80
Mavv 60,42,96
W_ 0
E_ 81
Mavv 80,30,96
s_nop 0
Tv 38,v4,1024
Tv 40,v4,7168
Mavv 100,34,96
E_ 82
Mavv 104,34,100
s_nop 0
Tv 42,v5,1024
Tv 44,v5,7168
Mavv 84,30,100
E_ 83
Mavv 88,30,104
C_ 120,80,81
C_ 121,82,83
Mavv 108,34,104
E_ 84
Mavv 112,34,108
E_ 85
Mavv 92,30,108
E_ 86
Mavv 96,30,112
E_ 87
Mavv 116,34,112
W_ 0
C_ 124,84,85
C_ 125,86,87
Mavv 136,38,112
s_nop 0
Tv 30,v2,2048
Tv 32,v2,8192
Mavv 156,42,112
E_ 88
Mavv 152,42,108
s_nop 0
Tv 34,v3,2048
Tv 36,v3,8192
Mavv 132,38,108
E_ 89
Mavv 128,38,104
E_ 90
Mavv 148,42,104
E_ 91
Mavv 144,42,100
C_ 128,88,89
C_ 129,90,91
Mavv 124,38,100
E_ 92
Mavv 120,38,96
E_ 93
Mavv 140,42,96
W_ 0
E_ 94
Mavv 160,30,96
s_nop 0
Tv 38,v4,2048
Tv 40,v4,8192
Mavv 180,34,96
E_ 95
Mavv 184,34,100
s_nop 0
Tv 42,v5,2048
Tv 44,v5,8192
Mavv 164,30,100
C_ 132,92,93
C_ 133,94,95
Mavv 168,30,104
v_mov_b32 v92, 0x3f803f80
v_mov_b32 v93, 0x3f803f80
Mavv 188,34,104
v_mov_b32 v94, 0x3f803f80
v_mov_b32 v95, 0x3f803f80
Mavv 192,34,108
R_ v11,a248
Mavv 172,30,108
s_waitcnt vmcnt(6) lgkmcnt(0)
s_barrier
v_mul_u32_u24 v11, 80, v11
Mavv 176,30,112
S_ s55,s71,614400
v_min_u32 v11, 29440, v11
Mavv 196,34,112
S_ m0,s51,0
S_ s85,s87,1024
Mavv 216,38,112
s_nop 0
Tv 30,v2,12288
Tv 32,v2,18432
Zvvv 76,92,116
L_ v7,24,s55,0
Zvvv 80,92,120
S_ s55,s71,624640
Tv 34,v3,12288
Tv 36,v3,18432
Zvvv 84,92,124
buffer_load_dword a254, v11, s[24:27], s85 offen
Zvvv 88,92,128
S_ m0,s51,1024
S_ s85,s87,1152
Zvvv 92,92,132
L_ v7,24,s55,0
Mavv 236,42,112
S_ s55,s71,634880
Mavv 232,42,108
buffer_load_dword a254, v11, s[24:27], s85 offen
Mavv 212,38,108
S_ m0,s51,2048
S_ s85,s6,1
Mavv 208,38,104
s_and_b32 s85, s85, 7
v_max3_f32 v10, v76, v80, v84
Mavv 228,42,104
s_lshl_b32 s85, s85, 7
v_max3_f32 v10, v10, v88, v92
v_cmp_lt_f32 vcc, 0x42000000, v10
Mavv 224,42,100
L_ v7,24,s55,0
Mavv 204,38,100
S_ s55,s71,645120
S_ s85,s85,s87
Mavv 200,38,96
buffer_load_dword a254, v11, s[24:27], s85 offen
Mavv 220,42,96
S_ m0,s51,3072
s_cbranch_vccnz .Ltramp1_15
.Ljoin1:
W_ 2
Mavv 0,30,116
s_nop 0
Tv 38,v4,12288
Tv 40,v4,18432
W_ 2
Mavv 20,34,116
L_ v7,24,s55,0
Mavv 24,34,120
S_ s55,s71,655360
Tv 42,v5,12288
Tv 44,v5,18432
Mavv 4,30,120
S_ m0,s51,4096
R_ v13,a248
S_ s53,s51,36864
Mavv 8,30,124
L_ v7,24,s55,0
Mavv 28,34,124
S_ s55,s71,665600
v_add_u32 v12, s53, v13
s_mul_i32 s53, s4, 10240
Mavv 32,34,128
S_ m0,s51,5120
v_add_u32 v13, s53, v13
R_ v10,a248
Mavv 12,30,128
L_ v7,24,s55,0
Mavv 16,30,132
A_ v46,v46,v76
A_ v47,v47,v80
Mavv 36,34,132
buffer_load_dword v11, off, s[92:95], s96 offset:0 sc1
W_ 2
Mavv 56,38,132
W_ 0
Tv 30,v2,13312
Tv 32,v2,19456
Mavv 76,42,132
A_ v48,v48,v84
A_ v49,v49,v88
Mavv 72,42,128
s_nop 0
Tv 34,v3,13312
Tv 36,v3,19456
Mavv 52,38,128
A_ v50,v50,v92
Mavv 48,38,124
v_rcp_f32 v46, v46
Mavv 68,42,124
v_rcp_f32 v47, v47
Mavv 64,42,120
v_rcp_f32 v48, v48
Mavv 44,38,120
v_rcp_f32 v49, v49
Mavv 40,38,116
v_rcp_f32 v50, v50
Mavv 60,42,116
W_ 0
R_ v96,a0
R_ v97,a1
Mavv 80,30,116
s_nop 0
Tv 38,v4,13312
Tv 40,v4,19456
Mavv 100,34,116
R_ v98,a2
R_ v99,a3
Mavv 104,34,120
s_nop 0
Tv 42,v5,13312
Tv 44,v5,19456
Mavv 84,30,120
R_ v100,a20
R_ v101,a21
Mavv 88,30,124
R_ v102,a22
R_ v103,a23
Mavv 108,34,124
U_ v96,v96,v46
U_ v97,v97,v46
Mavv 112,34,128
U_ v98,v98,v46
U_ v99,v99,v46
Mavv 92,30,128
U_ v100,v100,v46
U_ v101,v101,v46
Mavv 96,30,132
U_ v102,v102,v46
U_ v103,v103,v46
Mavv 116,34,132
W_ 0
C_ 136,96,97
C_ 137,98,99
Mavv 136,38,132
s_nop 0
Tv 30,v2,14336
Tv 32,v2,20480
Mavv 156,42,132
C_ 138,100,101
C_ 139,102,103
Mavv 152,42,128
s_nop 0
Tv 34,v3,14336
Tv 36,v3,20480
Mavv 132,38,128
Da 0,v13,0
Mavv 128,38,124
R_ v104,a4
R_ v105,a5
Mavv 148,42,124
R_ v106,a6
R_ v107,a7
Mavv 144,42,120
R_ v108,a24
R_ v109,a25
Mavv 124,38,120
R_ v110,a26
R_ v111,a27
Mavv 120,38,116
U_ v104,v104,v47
U_ v105,v105,v47
Mavv 140,42,116
W_ 1
U_ v106,v106,v47
U_ v107,v107,v47
Mavv 160,30,116
s_nop 0
Tv 38,v4,14336
Tv 40,v4,20480
Mavv 180,34,116
U_ v108,v108,v47
U_ v109,v109,v47
Mavv 184,34,120
s_nop 0
Tv 42,v5,14336
Tv 44,v5,20480
Mavv 164,30,120
U_ v110,v110,v47
U_ v111,v111,v47
Mavv 168,30,124
C_ 140,104,105
C_ 141,106,107
Mavv 188,34,124
C_ 142,108,109
C_ 143,110,111
Mavv 192,34,128
Da 4,v13,1024
Mavv 172,30,128
S_ s55,s71,0
R_ v96,a8
R_ v97,a9
Mavv 176,30,132
S_ m0,s51,12288
R_ v98,a10
R_ v99,a11
Mavv 196,34,132
s_waitcnt vmcnt(63) lgkmcnt(0)
s_barrier
R_ v100,a28
Mavv 216,38,132
L_ v7,24,s55,0
S_ s55,s71,10240
S_ m0,s51,13312
Mavv 236,42,132
R_ v101,a29
Mavv 232,42,128
L_ v7,24,s55,0
S_ s55,s71,20480
S_ m0,s51,14336
Mavv 212,38,128
R_ v102,a30
R_ v103,a31
Mavv 208,38,124
L_ v7,24,s55,0
S_ s55,s71,30720
S_ m0,s51,15360
Mavv 228,42,124
U_ v96,v96,v48
U_ v97,v97,v48
Mavv 224,42,120
L_ v7,24,s55,0
S_ s55,s71,40960
S_ m0,s51,16384
Mavv 204,38,120
U_ v98,v98,v48
U_ v99,v99,v48
Mavv 200,38,116
L_ v7,24,s55,0
S_ s55,s71,51200
S_ m0,s51,17408
Mavv 220,42,116
U_ v100,v100,v48
U_ v101,v101,v48
U_ v102,v102,v48
U_ v103,v103,v48
C_ 144,96,97
C_ 145,98,99
C_ 146,100,101
C_ 147,102,103
Da 8,v13,2048
L_ v7,24,s55,0
.p2align 3
R_ v104,a12
R_ v105,a13
R_ v106,a14
R_ v107,a15
R_ v108,a32
R_ v109,a33
R_ v110,a34
R_ v111,a35
v_pk_mul_f32 v[104:105], v[104:105], v[48:49] op_sel:[0,1]
v_pk_mul_f32 v[106:107], v[106:107], v[48:49] op_sel:[0,1]
v_pk_mul_f32 v[108:109], v[108:109], v[48:49] op_sel:[0,1]
v_pk_mul_f32 v[110:111], v[110:111], v[48:49] op_sel:[0,1]
C_ 148,104,105
C_ 149,106,107
C_ 150,108,109
C_ 151,110,111
Da 12,v13,3072
.p2align 3
R_ v96,a16
R_ v97,a17
R_ v98,a18
R_ v99,a19
R_ v100,a36
R_ v101,a37
R_ v102,a38
R_ v103,a39
v_pk_mul_f32 v[96:97], v[96:97], v[50:51] op_sel_hi:[1,0]
v_pk_mul_f32 v[98:99], v[98:99], v[50:51] op_sel_hi:[1,0]
v_pk_mul_f32 v[100:101], v[100:101], v[50:51] op_sel_hi:[1,0]
v_pk_mul_f32 v[102:103], v[102:103], v[50:51] op_sel_hi:[1,0]
C_ 152,96,97
C_ 153,98,99
C_ 154,100,101
C_ 155,102,103
Da 16,v13,4096
.p2align 3
R_ v104,a40
R_ v105,a41
R_ v106,a42
R_ v107,a43
R_ v108,a60
R_ v109,a61
R_ v110,a62
R_ v111,a63
v_pk_mul_f32 v[104:105], v[104:105], v[46:47] op_sel_hi:[1,0]
v_pk_mul_f32 v[106:107], v[106:107], v[46:47] op_sel_hi:[1,0]
v_pk_mul_f32 v[108:109], v[108:109], v[46:47] op_sel_hi:[1,0]
v_pk_mul_f32 v[110:111], v[110:111], v[46:47] op_sel_hi:[1,0]
C_ 156,104,105
C_ 157,106,107
C_ 158,108,109
C_ 159,110,111
Da 40,v13,5120
.p2align 3
R_ v96,a44
R_ v97,a45
R_ v98,a46
R_ v99,a47
R_ v100,a64
R_ v101,a65
R_ v102,a66
R_ v103,a67
v_pk_mul_f32 v[96:97], v[96:97], v[46:47] op_sel:[0,1]
v_pk_mul_f32 v[98:99], v[98:99], v[46:47] op_sel:[0,1]
v_pk_mul_f32 v[100:101], v[100:101], v[46:47] op_sel:[0,1]
v_pk_mul_f32 v[102:103], v[102:103], v[46:47] op_sel:[0,1]
C_ 160,96,97
C_ 161,98,99
C_ 162,100,101
C_ 163,102,103
Da 44,v13,6144
.p2align 3
R_ v104,a48
R_ v105,a49
R_ v106,a50
R_ v107,a51
R_ v108,a68
R_ v109,a69
R_ v110,a70
R_ v111,a71
v_pk_mul_f32 v[104:105], v[104:105], v[48:49] op_sel_hi:[1,0]
v_pk_mul_f32 v[106:107], v[106:107], v[48:49] op_sel_hi:[1,0]
v_pk_mul_f32 v[108:109], v[108:109], v[48:49] op_sel_hi:[1,0]
v_pk_mul_f32 v[110:111], v[110:111], v[48:49] op_sel_hi:[1,0]
C_ 164,104,105
C_ 165,106,107
C_ 166,108,109
C_ 167,110,111
Da 48,v13,7168
.p2align 3
R_ v96,a52
R_ v97,a53
R_ v98,a54
R_ v99,a55
R_ v100,a72
R_ v101,a73
R_ v102,a74
R_ v103,a75
v_pk_mul_f32 v[96:97], v[96:97], v[48:49] op_sel:[0,1]
v_pk_mul_f32 v[98:99], v[98:99], v[48:49] op_sel:[0,1]
v_pk_mul_f32 v[100:101], v[100:101], v[48:49] op_sel:[0,1]
v_pk_mul_f32 v[102:103], v[102:103], v[48:49] op_sel:[0,1]
C_ 168,96,97
C_ 169,98,99
C_ 170,100,101
C_ 171,102,103
Da 52,v13,8192
.p2align 3
R_ v104,a56
R_ v105,a57
R_ v106,a58
R_ v107,a59
R_ v108,a76
R_ v109,a77
R_ v110,a78
R_ v111,a79
v_pk_mul_f32 v[104:105], v[104:105], v[50:51] op_sel_hi:[1,0]
v_pk_mul_f32 v[106:107], v[106:107], v[50:51] op_sel_hi:[1,0]
v_pk_mul_f32 v[108:109], v[108:109], v[50:51] op_sel_hi:[1,0]
v_pk_mul_f32 v[110:111], v[110:111], v[50:51] op_sel_hi:[1,0]
C_ 172,104,105
C_ 173,106,107
C_ 174,108,109
C_ 175,110,111
Da 56,v13,9216
.p2align 3
R_ v96,a80
R_ v97,a81
R_ v98,a82
R_ v99,a83
R_ v100,a100
R_ v101,a101
R_ v102,a102
R_ v103,a103
v_pk_mul_f32 v[96:97], v[96:97], v[46:47] op_sel_hi:[1,0]
v_pk_mul_f32 v[98:99], v[98:99], v[46:47] op_sel_hi:[1,0]
v_pk_mul_f32 v[100:101], v[100:101], v[46:47] op_sel_hi:[1,0]
v_pk_mul_f32 v[102:103], v[102:103], v[46:47] op_sel_hi:[1,0]
C_ 176,96,97
C_ 177,98,99
C_ 178,100,101
C_ 179,102,103
X_ a80,v20
X_ a81,v21
X_ a82,v22
X_ a83,v23
.p2align 3
R_ v104,a84
R_ v105,a85
R_ v106,a86
R_ v107,a87
R_ v108,a104
R_ v109,a105
R_ v110,a106
R_ v111,a107
v_pk_mul_f32 v[104:105], v[104:105], v[46:47] op_sel:[0,1]
v_pk_mul_f32 v[106:107], v[106:107], v[46:47] op_sel:[0,1]
v_pk_mul_f32 v[108:109], v[108:109], v[46:47] op_sel:[0,1]
v_pk_mul_f32 v[110:111], v[110:111], v[46:47] op_sel:[0,1]
C_ 180,104,105
C_ 181,106,107
C_ 182,108,109
C_ 183,110,111
X_ a84,v24
X_ a85,v25
X_ a86,v26
X_ a87,v27
.p2align 3
R_ v96,a88
R_ v97,a89
R_ v98,a90
R_ v99,a91
R_ v100,a108
R_ v101,a109
R_ v102,a110
R_ v103,a111
v_pk_mul_f32 v[96:97], v[96:97], v[48:49] op_sel_hi:[1,0]
v_pk_mul_f32 v[98:99], v[98:99], v[48:49] op_sel_hi:[1,0]
v_pk_mul_f32 v[100:101], v[100:101], v[48:49] op_sel_hi:[1,0]
v_pk_mul_f32 v[102:103], v[102:103], v[48:49] op_sel_hi:[1,0]
C_ 184,96,97
C_ 185,98,99
C_ 186,100,101
C_ 187,102,103
X_ a88,v52
X_ a89,v53
X_ a90,v54
X_ a91,v55
.p2align 3
R_ v104,a92
R_ v105,a93
R_ v106,a94
R_ v107,a95
R_ v108,a112
R_ v109,a113
R_ v110,a114
R_ v111,a115
v_pk_mul_f32 v[104:105], v[104:105], v[48:49] op_sel:[0,1]
v_pk_mul_f32 v[106:107], v[106:107], v[48:49] op_sel:[0,1]
v_pk_mul_f32 v[108:109], v[108:109], v[48:49] op_sel:[0,1]
v_pk_mul_f32 v[110:111], v[110:111], v[48:49] op_sel:[0,1]
C_ 188,104,105
C_ 189,106,107
C_ 190,108,109
C_ 191,110,111
.p2align 3
R_ v96,a96
R_ v97,a97
R_ v98,a98
R_ v99,a99
R_ v100,a116
R_ v101,a117
R_ v102,a118
R_ v103,a119
v_pk_mul_f32 v[96:97], v[96:97], v[50:51] op_sel_hi:[1,0]
v_pk_mul_f32 v[98:99], v[98:99], v[50:51] op_sel_hi:[1,0]
v_pk_mul_f32 v[100:101], v[100:101], v[50:51] op_sel_hi:[1,0]
v_pk_mul_f32 v[102:103], v[102:103], v[50:51] op_sel_hi:[1,0]
C_ 192,96,97
C_ 193,98,99
C_ 194,100,101
C_ 195,102,103
.p2align 3
R_ v104,a120
R_ v105,a121
R_ v106,a122
R_ v107,a123
R_ v108,a140
R_ v109,a141
R_ v110,a142
R_ v111,a143
v_pk_mul_f32 v[104:105], v[104:105], v[46:47] op_sel_hi:[1,0]
v_pk_mul_f32 v[106:107], v[106:107], v[46:47] op_sel_hi:[1,0]
v_pk_mul_f32 v[108:109], v[108:109], v[46:47] op_sel_hi:[1,0]
v_pk_mul_f32 v[110:111], v[110:111], v[46:47] op_sel_hi:[1,0]
C_ 196,104,105
C_ 197,106,107
C_ 198,108,109
C_ 199,110,111
X_ a120,v16
X_ a121,v17
X_ a122,v18
X_ a123,v19
.p2align 3
R_ v96,a124
R_ v97,a125
R_ v98,a126
R_ v99,a127
R_ v100,a144
R_ v101,a145
R_ v102,a146
R_ v103,a147
v_pk_mul_f32 v[96:97], v[96:97], v[46:47] op_sel:[0,1]
v_pk_mul_f32 v[98:99], v[98:99], v[46:47] op_sel:[0,1]
v_pk_mul_f32 v[100:101], v[100:101], v[46:47] op_sel:[0,1]
v_pk_mul_f32 v[102:103], v[102:103], v[46:47] op_sel:[0,1]
C_ 200,96,97
C_ 201,98,99
C_ 202,100,101
C_ 203,102,103
X_ a124,v28
X_ a125,v29
X_ a126,v51
X_ a127,v6
.p2align 3
R_ v104,a128
R_ v105,a129
R_ v106,a130
R_ v107,a131
R_ v108,a148
R_ v109,a149
R_ v110,a150
R_ v111,a151
v_pk_mul_f32 v[104:105], v[104:105], v[48:49] op_sel_hi:[1,0]
v_pk_mul_f32 v[106:107], v[106:107], v[48:49] op_sel_hi:[1,0]
v_pk_mul_f32 v[108:109], v[108:109], v[48:49] op_sel_hi:[1,0]
v_pk_mul_f32 v[110:111], v[110:111], v[48:49] op_sel_hi:[1,0]
C_ 204,104,105
C_ 205,106,107
C_ 206,108,109
C_ 207,110,111
X_ a128,v14
X_ a129,v15
v_accvgpr_mov_b32 a130, a249
v_accvgpr_mov_b32 a131, a250
.p2align 3
R_ v96,a132
R_ v97,a133
R_ v98,a134
R_ v99,a135
R_ v100,a152
R_ v101,a153
R_ v102,a154
R_ v103,a155
v_pk_mul_f32 v[96:97], v[96:97], v[48:49] op_sel:[0,1]
v_pk_mul_f32 v[98:99], v[98:99], v[48:49] op_sel:[0,1]
v_pk_mul_f32 v[100:101], v[100:101], v[48:49] op_sel:[0,1]
v_pk_mul_f32 v[102:103], v[102:103], v[48:49] op_sel:[0,1]
C_ 208,96,97
C_ 209,98,99
C_ 210,100,101
C_ 211,102,103
Da 132,v12,0
.p2align 3
R_ v104,a136
R_ v105,a137
R_ v106,a138
R_ v107,a139
R_ v108,a156
R_ v109,a157
R_ v110,a158
R_ v111,a159
v_pk_mul_f32 v[104:105], v[104:105], v[50:51] op_sel_hi:[1,0]
v_pk_mul_f32 v[106:107], v[106:107], v[50:51] op_sel_hi:[1,0]
v_pk_mul_f32 v[108:109], v[108:109], v[50:51] op_sel_hi:[1,0]
v_pk_mul_f32 v[110:111], v[110:111], v[50:51] op_sel_hi:[1,0]
C_ 212,104,105
C_ 213,106,107
C_ 214,108,109
C_ 215,110,111
Da 136,v12,1024
.p2align 3
R_ v96,a160
R_ v97,a161
R_ v98,a162
R_ v99,a163
R_ v100,a180
R_ v101,a181
R_ v102,a182
R_ v103,a183
v_pk_mul_f32 v[96:97], v[96:97], v[46:47] op_sel_hi:[1,0]
v_pk_mul_f32 v[98:99], v[98:99], v[46:47] op_sel_hi:[1,0]
v_pk_mul_f32 v[100:101], v[100:101], v[46:47] op_sel_hi:[1,0]
v_pk_mul_f32 v[102:103], v[102:103], v[46:47] op_sel_hi:[1,0]
C_ 216,96,97
C_ 217,98,99
C_ 218,100,101
C_ 219,102,103
Da 160,v12,2048
.p2align 3
R_ v104,a164
R_ v105,a165
R_ v106,a166
R_ v107,a167
R_ v108,a184
R_ v109,a185
R_ v110,a186
R_ v111,a187
v_pk_mul_f32 v[104:105], v[104:105], v[46:47] op_sel:[0,1]
v_pk_mul_f32 v[106:107], v[106:107], v[46:47] op_sel:[0,1]
v_pk_mul_f32 v[108:109], v[108:109], v[46:47] op_sel:[0,1]
v_pk_mul_f32 v[110:111], v[110:111], v[46:47] op_sel:[0,1]
C_ 220,104,105
C_ 221,106,107
C_ 222,108,109
C_ 223,110,111
Da 164,v12,3072
.p2align 3
R_ v96,a168
R_ v97,a169
R_ v98,a170
R_ v99,a171
R_ v100,a188
R_ v101,a189
R_ v102,a190
R_ v103,a191
v_pk_mul_f32 v[96:97], v[96:97], v[48:49] op_sel_hi:[1,0]
v_pk_mul_f32 v[98:99], v[98:99], v[48:49] op_sel_hi:[1,0]
v_pk_mul_f32 v[100:101], v[100:101], v[48:49] op_sel_hi:[1,0]
v_pk_mul_f32 v[102:103], v[102:103], v[48:49] op_sel_hi:[1,0]
C_ 224,96,97
C_ 225,98,99
C_ 226,100,101
C_ 227,102,103
Da 168,v12,4096
.p2align 3
R_ v104,a172
R_ v105,a173
R_ v106,a174
R_ v107,a175
R_ v108,a192
R_ v109,a193
R_ v110,a194
R_ v111,a195
v_pk_mul_f32 v[104:105], v[104:105], v[48:49] op_sel:[0,1]
v_pk_mul_f32 v[106:107], v[106:107], v[48:49] op_sel:[0,1]
v_pk_mul_f32 v[108:109], v[108:109], v[48:49] op_sel:[0,1]
v_pk_mul_f32 v[110:111], v[110:111], v[48:49] op_sel:[0,1]
C_ 228,104,105
C_ 229,106,107
C_ 230,108,109
C_ 231,110,111
Da 172,v12,5120
.p2align 3
R_ v96,a176
R_ v97,a177
R_ v98,a178
R_ v99,a179
R_ v100,a196
R_ v101,a197
R_ v102,a198
R_ v103,a199
v_pk_mul_f32 v[96:97], v[96:97], v[50:51] op_sel_hi:[1,0]
v_pk_mul_f32 v[98:99], v[98:99], v[50:51] op_sel_hi:[1,0]
v_pk_mul_f32 v[100:101], v[100:101], v[50:51] op_sel_hi:[1,0]
v_pk_mul_f32 v[102:103], v[102:103], v[50:51] op_sel_hi:[1,0]
C_ 232,96,97
C_ 233,98,99
C_ 234,100,101
C_ 235,102,103
Da 176,v12,12288
.p2align 3
R_ v104,a200
R_ v105,a201
R_ v106,a202
R_ v107,a203
R_ v108,a220
R_ v109,a221
R_ v110,a222
R_ v111,a223
v_pk_mul_f32 v[104:105], v[104:105], v[46:47] op_sel_hi:[1,0]
v_pk_mul_f32 v[106:107], v[106:107], v[46:47] op_sel_hi:[1,0]
v_pk_mul_f32 v[108:109], v[108:109], v[46:47] op_sel_hi:[1,0]
v_pk_mul_f32 v[110:111], v[110:111], v[46:47] op_sel_hi:[1,0]
C_ 236,104,105
C_ 237,106,107
C_ 238,108,109
C_ 239,110,111
Da 200,v12,13312
.p2align 3
R_ v96,a204
R_ v97,a205
R_ v98,a206
R_ v99,a207
R_ v100,a224
R_ v101,a225
R_ v102,a226
R_ v103,a227
v_pk_mul_f32 v[96:97], v[96:97], v[46:47] op_sel:[0,1]
v_pk_mul_f32 v[98:99], v[98:99], v[46:47] op_sel:[0,1]
v_pk_mul_f32 v[100:101], v[100:101], v[46:47] op_sel:[0,1]
v_pk_mul_f32 v[102:103], v[102:103], v[46:47] op_sel:[0,1]
C_ 240,96,97
C_ 241,98,99
C_ 242,100,101
C_ 243,102,103
Da 204,v12,14336
v_mov_b32 v6, v10
v_lshrrev_b32 v10, 9, v6
v_lshlrev_b32 v10, 11, v10
v_sub_u32 %0, v2, v10
v_xor_b32 %0, s83, %0
s_waitcnt vmcnt(16) lgkmcnt(0)
s_barrier
Ta 180,%0,24576
Ta 182,%0,26624
Ta 184,%0,28672
Ta 186,%0,30720
Ta 188,%0,32768
Ta 190,%0,34816
.p2align 3
R_ v104,a208
R_ v105,a209
R_ v106,a210
R_ v107,a211
R_ v108,a228
R_ v109,a229
R_ v110,a230
R_ v111,a231
v_pk_mul_f32 v[104:105], v[104:105], v[48:49] op_sel_hi:[1,0]
v_pk_mul_f32 v[106:107], v[106:107], v[48:49] op_sel_hi:[1,0]
v_pk_mul_f32 v[108:109], v[108:109], v[48:49] op_sel_hi:[1,0]
v_pk_mul_f32 v[110:111], v[110:111], v[48:49] op_sel_hi:[1,0]
C_ 244,104,105
C_ 245,106,107
C_ 246,108,109
C_ 247,110,111
Da 208,v12,15360
.p2align 3
R_ v96,a212
R_ v97,a213
R_ v98,a214
R_ v99,a215
R_ v100,a232
R_ v101,a233
R_ v102,a234
R_ v103,a235
v_pk_mul_f32 v[96:97], v[96:97], v[48:49] op_sel:[0,1]
v_pk_mul_f32 v[98:99], v[98:99], v[48:49] op_sel:[0,1]
v_pk_mul_f32 v[100:101], v[100:101], v[48:49] op_sel:[0,1]
v_pk_mul_f32 v[102:103], v[102:103], v[48:49] op_sel:[0,1]
C_ 248,96,97
C_ 249,98,99
C_ 250,100,101
C_ 251,102,103
Da 212,v12,16384
.p2align 3
R_ v104,a216
R_ v105,a217
R_ v106,a218
R_ v107,a219
R_ v108,a236
R_ v109,a237
R_ v110,a238
R_ v111,a239
v_pk_mul_f32 v[104:105], v[104:105], v[50:51] op_sel_hi:[1,0]
v_pk_mul_f32 v[106:107], v[106:107], v[50:51] op_sel_hi:[1,0]
v_pk_mul_f32 v[108:109], v[108:109], v[50:51] op_sel_hi:[1,0]
v_pk_mul_f32 v[110:111], v[110:111], v[50:51] op_sel_hi:[1,0]
C_ 252,104,105
C_ 253,106,107
C_ 254,108,109
C_ 255,110,111
Da 216,v12,17408
v_xor_b32 v1, 32, %0
W_ 7
Zvav 56,180,136
s_mul_i32 s76, s6, 204800
Ta 192,v1,24576
Ta 194,v1,26624
W_ 7
Mvav 56,184,156
v_xor_b32 v2, 64, %0
v_xor_b32 v3, 96, %0
W_ 5
Mvav 56,188,176
s_nop 0
Ta 196,v1,28672
Ta 198,v1,30720
Zvav 60,180,140
W_ 4
R_ v4,a248
v_mul_u32_u24 v4, 80, v4
Mvav 60,184,160
S_ s55,s71,122880
Ta 220,v1,32768
Ta 222,v1,34816
Mvav 60,188,180
S_ m0,s51,49152
v_min_u32 v4, 29440, v4
s_waitcnt vmcnt(6)
Zvav 64,180,144
v_readfirstlane_b32 s62, v11
v_lshrrev_b32 v8, 2, v6
Mvav 64,184,164
v_add_u32 v5, s75, v8
R_ v9,a251
Mvav 64,188,184
v_or_b32 v9, s73, v9
v_lshrrev_b32 v10, 8, v6
Zvav 68,180,148
v_bfe_u32 v12, v6, 6, 2
v_cmp_eq_u32 vcc, v10, v12
Mvav 68,184,168
v_and_b32 v10, 16, v6
v_mov_b32 v12, 0x3f80
Mvav 68,188,188
v_lshlrev_b32 v12, v10, v12
v_cndmask_b32 v12, 0, v12, vcc
Zvav 72,180,152
v_and_b32 v10, 32, v6
v_cmp_eq_u32 vcc, 0, v10
Mvav 72,184,172
X_ a68,0
X_ a69,0
Mvav 72,188,192
W_ 0
X_ a72,0
X_ a73,0
Zvav 92,220,192
s_nop 0
Ta 180,v2,24576
Ta 182,v2,26624
Mvav 92,196,172
v_cndmask_b32 v10, 0, v12, vcc
v_sub_u32 v12, v12, v10
Mvav 92,192,152
s_nop 0
Ta 184,v2,28672
Ta 186,v2,30720
Zvav 88,220,188
X_ a70,v10
X_ a71,v12
Mvav 88,196,168
s_mov_b64 s[32:33], s[20:21]
Ta 188,v2,32768
Ta 190,v2,34816
Mvav 88,192,148
s_mov_b32 s34, 409600
S_ s85,s6,2
Zvav 84,220,184
L_ v7,24,s55,0
Mvav 84,196,164
S_ s55,s71,133120
s_and_b32 s85, s85, 7
Mvav 84,192,144
S_ m0,s51,50176
s_lshl_b32 s85, s85, 7
Zvav 80,220,180
S_ s85,s85,s87
Mvav 80,196,160
L_ v7,24,s55,0
Mvav 80,192,140
S_ s55,s71,143360
Zvav 76,220,176
S_ m0,s51,51200
Mvav 76,196,156
Mvav 76,192,136
W_ 0
Zvav 16,180,136
s_nop 0
Ta 192,v3,24576
Ta 194,v3,26624
Mvav 16,184,156
L_ v7,24,s55,0
Mvav 16,188,176
S_ s55,s71,153600
Ta 196,v3,28672
Ta 198,v3,30720
Zvav 20,180,140
S_ m0,s51,52224
Mvav 20,184,160
s_nop 0
Ta 220,v3,32768
Ta 222,v3,34816
Mvav 20,188,180
L_ v7,24,s55,0
Zvav 24,180,144
S_ s55,s71,163840
Mvav 24,184,164
S_ m0,s51,53248
Mvav 24,188,184
Zvav 28,180,148
L_ v7,24,s55,0
Mvav 28,184,168
S_ s55,s71,174080
Mvav 28,188,188
S_ m0,s51,54272
Zvav 32,180,152
Mvav 32,184,172
L_ v7,24,s55,0
Mvav 32,188,192
s_waitcnt vmcnt(13) lgkmcnt(0)
s_barrier
S_ s71,s72,1152
Zvav 52,220,192
S_ s55,s71,491520
Ta 180,%0,0
Ta 182,%0,2048
Mvav 52,196,172
S_ m0,s51,36864
Mvav 52,192,152
s_nop 0
Ta 184,%0,4096
Ta 186,%0,6144
Zvav 48,220,188
L_ v7,24,s55,0
Mvav 48,196,168
S_ s55,s71,501760
Ta 188,%0,8192
Ta 190,%0,10240
Mvav 48,192,148
S_ m0,s51,37888
Zvav 44,220,184
Mvav 44,196,164
L_ v7,24,s55,0
Mvav 44,192,144
S_ s55,s71,512000
Zvav 40,220,180
S_ m0,s51,38912
Mvav 40,196,160
Mvav 40,192,140
L_ v7,24,s55,0
Zvav 36,220,176
S_ s55,s71,522240
Mvav 36,196,156
S_ m0,s51,39936
Mvav 36,192,136
W_ 0
Mvav 56,180,196
s_nop 0
Ta 192,v1,0
Ta 194,v1,2048
Mvav 56,184,216
L_ v7,24,s55,0
Mvav 56,188,236
S_ s55,s71,532480
Ta 196,v1,4096
Ta 198,v1,6144
Mvav 60,180,200
S_ m0,s51,40960
Mvav 60,184,220
s_nop 0
Ta 220,v1,8192
Ta 222,v1,10240
Mvav 60,188,240
L_ v7,24,s55,0
Mvav 64,180,204
S_ s55,s71,542720
Mvav 64,184,224
S_ m0,s51,41984
Mvav 64,188,244
Mvav 68,180,208
L_ v7,24,s55,0
Mvav 68,184,228
S_ s55,s71,614400
Mvav 68,188,248
S_ m0,s51,24576
Mvav 72,180,212
Mvav 72,184,232
L_ v7,24,s55,0
Mvav 72,188,252
W_ 0
S_ s55,s71,624640
Mvav 92,220,252
S_ m0,s51,25600
Ta 180,v2,0
Ta 182,v2,2048
Mvav 92,196,232
Mvav 92,192,212
s_nop 0
Ta 184,v2,4096
Ta 186,v2,6144
Mvav 88,220,248
L_ v7,24,s55,0
Mvav 88,196,228
S_ s55,s71,634880
Ta 188,v2,8192
Ta 190,v2,10240
Mvav 88,192,208
S_ m0,s51,26624
Mvav 84,220,244
Mvav 84,196,224
L_ v7,24,s55,0
Mvav 84,192,204
S_ s55,s71,645120
Mvav 80,220,240
S_ m0,s51,27648
Mvav 80,196,220
Mvav 80,192,200
L_ v7,24,s55,0
Mvav 76,220,236
S_ s55,s71,655360
Mvav 76,196,216
S_ m0,s51,28672
Mvav 76,192,196
W_ 0
Mvav 16,180,196
s_nop 0
Ta 192,v3,0
Ta 194,v3,2048
Mvav 16,184,216
L_ v7,24,s55,0
Mvav 16,188,236
S_ s55,s71,665600
Ta 196,v3,4096
Ta 198,v3,6144
Mvav 20,180,200
S_ m0,s51,29696
Mvav 20,184,220
s_nop 0
Ta 220,v3,8192
Ta 222,v3,10240
Mvav 20,188,240
L_ v7,24,s55,0
Mvav 24,180,204
Mvav 24,184,224
Mvav 24,188,244
Mvav 28,180,208
buffer_load_dword a74, v4, s[24:27], s85 offen
Mvav 28,184,228
Mvav 28,188,248
Mvav 32,180,212
Mvav 32,184,232
Mvav 32,188,252
s_waitcnt vmcnt(19) lgkmcnt(0)
s_barrier
Mvav 52,220,252
S_ s55,s71,0
Ta 180,%0,12288
Ta 182,%0,14336
Mvav 52,196,232
S_ m0,s51,0
Mvav 52,192,212
s_nop 0
Ta 184,%0,16384
Ta 186,%0,18432
Mvav 48,220,248
L_ v7,24,s55,0
Mvav 48,196,228
S_ s55,s71,10240
Ta 188,%0,20480
Ta 190,%0,22528
Mvav 48,192,208
S_ m0,s51,1024
Mvav 44,220,244
Mvav 44,196,224
L_ v7,24,s55,0
Mvav 44,192,204
S_ s55,s71,20480
Mvav 40,220,240
S_ m0,s51,2048
Mvav 40,196,220
Mvav 40,192,200
L_ v7,24,s55,0
Mvav 36,220,236
S_ s55,s71,30720
Mvav 36,196,216
S_ m0,s51,3072
Mvav 36,192,196
W_ 0
Mvaa 56,180,0
s_nop 0
Ta 192,v1,12288
Ta 194,v1,14336
Mvaa 56,184,40
L_ v7,24,s55,0
Mvaa 56,188,80
S_ s55,s71,40960
Ta 196,v1,16384
Ta 198,v1,18432
Mvaa 60,180,4
S_ m0,s51,4096
Mvaa 60,184,44
s_nop 0
Ta 220,v1,20480
Ta 222,v1,22528
Mvaa 60,188,84
L_ v7,24,s55,0
Mvaa 64,180,8
S_ s55,s71,51200
Mvaa 64,184,48
S_ m0,s51,5120
Mvaa 64,188,88
Mvaa 68,180,12
L_ v7,24,s55,0
Mvaa 68,184,52
Mvaa 68,188,240
Mvaa 72,180,16
Mvaa 72,184,56
Mvaa 72,188,244
W_ 0
Mvaa 92,220,244
s_nop 0
Ta 180,v2,12288
Ta 182,v2,14336
Mvaa 92,196,56
Mvaa 92,192,16
s_nop 0
Ta 184,v2,16384
Ta 186,v2,18432
Mvaa 88,220,240
Mvaa 88,196,52
s_nop 0
Ta 188,v2,20480
Ta 190,v2,22528
Mvaa 88,192,12
Mvaa 84,220,88
Mvaa 84,196,48
Mvaa 84,192,8
Mvaa 80,220,84
Mvaa 80,196,44
Mvaa 80,192,4
Mvaa 76,220,80
Mvaa 76,196,40
Mvaa 76,192,0
W_ 0
Mvaa 16,180,0
s_nop 0
Ta 192,v3,12288
Ta 194,v3,14336
Mvaa 16,184,40
Mvaa 16,188,80
s_nop 0
Ta 196,v3,16384
Ta 198,v3,18432
Mvaa 20,180,4
Mvaa 20,184,44
s_nop 0
Ta 220,v3,20480
Ta 222,v3,22528
Mvaa 20,188,84
Mvaa 24,180,8
Mvaa 24,184,48
Mvaa 24,188,88
Mvaa 28,180,12
Mvaa 28,184,52
Mvaa 28,188,240
Mvaa 32,180,16
Mvaa 32,184,56
Mvaa 32,188,244
s_waitcnt vmcnt(19) lgkmcnt(0)
s_barrier
Mvaa 52,220,244
s_nop 0
Ta 180,%0,49152
Ta 182,%0,51200
Mvaa 52,196,56
Mvaa 52,192,16
s_nop 0
Ta 184,%0,53248
Ta 186,%0,55296
Mvaa 48,220,240
Mvaa 48,196,52
s_nop 0
Ta 188,%0,57344
Ta 190,%0,59392
Mvaa 48,192,12
Mvaa 44,220,88
Mvaa 44,196,48
Mvaa 44,192,8
Mvaa 40,220,84
Mvaa 40,196,44
Mvaa 40,192,4
Mvaa 36,220,80
Mvaa 36,196,40
Mvaa 36,192,0
W_ 0
Mvaa 56,180,120
S_ s73,s78,163840
Ta 192,v1,49152
Ta 194,v1,51200
Mvaa 56,184,160
S_ s73,s73,4096
Mvaa 56,188,200
s_nop 0
Ta 196,v1,53248
Ta 198,v1,55296
Mvaa 60,180,124
s_mov_b32 m0, s52
Mvaa 60,184,164
s_nop 0
Ta 220,v1,57344
Ta 222,v1,59392
Mvaa 60,188,204
s_nop 0
Y_ 56,0
Y_ 57,256
Mvaa 64,180,128
s_nop 0
Y_ 58,512
Y_ 59,768
Mvaa 64,184,168
Mvaa 64,188,208
s_nop 0
Y_ 60,1024
Y_ 61,1280
Mvaa 68,180,132
)A"
R"A(s_nop 0
Y_ 62,1536
Y_ 63,1792
Mvaa 68,184,172
Mvaa 68,188,212
s_nop 0
Y_ 64,2048
Y_ 65,2304
Mvaa 72,180,136
s_nop 0
Y_ 66,2560
Y_ 67,2816
Mvaa 72,184,176
Mvaa 72,188,216
W_ 12
Y_ 68,3072
Y_ 69,3328
Mvaa 92,220,216
s_nop 0
Ta 180,v2,49152
Ta 182,v2,51200
Mvaa 92,196,176
s_nop 0
Y_ 70,3584
Y_ 71,3840
Mvaa 92,192,136
s_nop 0
Ta 184,v2,53248
Ta 186,v2,55296
Mvaa 88,220,212
s_nop 0
Y_ 72,4096
Y_ 73,4352
Mvaa 88,196,172
s_nop 0
Ta 188,v2,57344
Ta 190,v2,59392
Mvaa 88,192,132
s_nop 0
Y_ 74,4608
Y_ 75,4864
Mvaa 84,220,208
s_nop 0
Y_ 92,9216
Y_ 93,9472
Mvaa 84,196,168
s_nop 0
Y_ 94,9728
Y_ 95,9984
Mvaa 84,192,128
s_nop 0
Y_ 88,8192
Y_ 89,8448
Mvaa 80,220,204
s_nop 0
Y_ 90,8704
Y_ 91,8960
Mvaa 80,196,164
Mvaa 80,192,124
s_nop 0
Y_ 84,7168
Y_ 85,7424
Mvaa 76,220,200
s_nop 0
Y_ 86,7680
Y_ 87,7936
Mvaa 76,196,160
Mvaa 76,192,120
W_ 14
Y_ 80,6144
Y_ 81,6400
Mvaa 16,180,120
s_nop 0
Ta 192,v3,49152
Ta 194,v3,51200
Mvaa 16,184,160
s_nop 0
Y_ 82,6656
Y_ 83,6912
Mvaa 16,188,200
s_nop 0
Ta 196,v3,53248
Ta 198,v3,55296
Mvaa 20,180,124
s_nop 0
Y_ 76,5120
Y_ 77,5376
Mvaa 20,184,164
s_nop 0
Ta 220,v3,57344
Ta 222,v3,59392
Mvaa 20,188,204
s_nop 0
Y_ 78,5632
Y_ 79,5888
Mvaa 24,180,128
Mvaa 24,184,168
Mvaa 24,188,208
s_waitcnt vmcnt(13) lgkmcnt(0)
s_barrier
Mvaa 28,180,132
S_ s55,s71,122880
ds_read2st64_b32 v[96:97], v5 offset0:0 offset1:1
Mvaa 28,184,172
S_ m0,s51,12288
ds_read2st64_b32 v[98:99], v5 offset0:2 offset1:3
Mvaa 28,188,212
L_ v7,24,s55,0
Mvaa 32,180,136
S_ s55,s71,133120
ds_read2st64_b32 v[104:105], v5 offset0:4 offset1:5
Mvaa 32,184,176
S_ m0,s51,13312
ds_read2st64_b32 v[106:107], v5 offset0:6 offset1:7
Mvaa 32,188,216
ds_read2st64_b32 v[112:113], v5 offset0:8 offset1:9
Mvaa 52,220,216
s_nop 0
Ta 180,%0,36864
Ta 182,%0,38912
Mvaa 52,196,176
L_ v7,24,s55,0
Mvaa 52,192,136
W_ 5
Ta 184,%0,40960
Ta 186,%0,43008
Mvaa 48,220,212
S_ s55,s71,143360
ds_read2st64_b32 v[114:115], v5 offset0:10 offset1:11
A_ v96,v16,v96
Mvaa 48,196,172
S_ m0,s51,14336
Ta 188,%0,45056
Ta 190,%0,47104
Mvaa 48,192,132
L_ v7,24,s55,0
Mvaa 44,220,208
S_ s55,s71,153600
ds_read2st64_b32 v[120:121], v5 offset0:12 offset1:13
A_ v97,v17,v97
Mvaa 44,196,168
S_ m0,s51,15360
ds_read2st64_b32 v[122:123], v5 offset0:14 offset1:15
A_ v98,v18,v98
Mvaa 44,192,128
ds_read2st64_b32 v[128:129], v5 offset0:16 offset1:17
A_ v99,v19,v99
Mvaa 40,220,204
L_ v7,24,s55,0
Mvaa 40,196,164
W_ 11
ds_read2st64_b32 v[130:131], v5 offset0:18 offset1:19
A_ v104,v20,v104
Mvaa 40,192,124
S_ s55,s71,163840
ds_read2st64_b32 v[132:133], v5 offset0:36 offset1:37
A_ v105,v21,v105
Mvaa 36,220,200
S_ m0,s51,16384
ds_read2st64_b32 v[134:135], v5 offset0:38 offset1:39
A_ v106,v22,v106
Mvaa 36,196,160
L_ v7,24,s55,0
Mvaa 36,192,120
W_ 6
ds_read2st64_b32 v[124:125], v5 offset0:32 offset1:33
A_ v107,v23,v107
Zvav 56,180,136
Ta 192,v1,36864
Ta 194,v1,38912
Mvav 56,184,156
S_ s55,s71,174080
ds_read2st64_b32 v[126:127], v5 offset0:34 offset1:35
A_ v112,v24,v112
Mvav 56,188,176
S_ m0,s51,17408
Ta 196,v1,40960
Ta 198,v1,43008
Zvav 60,180,140
L_ v7,24,s55,0
Mvav 60,184,160
S_ s79,s6,1
Ta 220,v1,45056
Ta 222,v1,47104
Mvav 60,188,180
s_and_b32 s79, s79, 7
ds_read2st64_b32 v[116:117], v5 offset0:28 offset1:29
A_ v113,v25,v113
Zvav 64,180,144
s_lshl_b32 s79, s79, 7
ds_read2st64_b32 v[118:119], v5 offset0:30 offset1:31
A_ v114,v26,v114
Mvav 64,184,164
S_ s71,s72,s79
ds_read2st64_b32 v[108:109], v5 offset0:24 offset1:25
A_ v115,v27,v115
Mvav 64,188,184
W_ 14
ds_read2st64_b32 v[110:111], v5 offset0:26 offset1:27
A_ v120,v28,v120
Zvav 68,180,148
S_ s55,s71,491520
ds_read2st64_b32 v[100:101], v5 offset0:20 offset1:21
A_ v121,v29,v121
Mvav 68,184,168
S_ m0,s51,49152
ds_read2st64_b32 v[102:103], v5 offset0:22 offset1:23
A_ v122,v30,v122
Mvav 68,188,188
L_ v7,24,s55,0
Zvav 72,180,152
S_ s55,s71,501760
A_ v123,v31,v123
R_ v12,a248
Mvav 72,184,172
W_ 14
A_ v128,v32,v128
A_ v129,v33,v129
Mvav 72,188,192
W_ 6
A_ v130,v34,v130
A_ v131,v35,v131
Zvav 92,220,192
Ta 180,v2,36864
Ta 182,v2,38912
Mvav 92,196,172
S_ m0,s51,50176
A_ v132,v52,v132
A_ v133,v53,v133
Mvav 92,192,152
s_nop 0
Ta 184,v2,40960
Ta 186,v2,43008
Zvav 88,220,188
L_ v7,24,s55,0
Mvav 88,196,168
S_ s55,s71,512000
Ta 188,v2,45056
Ta 190,v2,47104
Mvav 88,192,148
S_ m0,s51,51200
A_ v134,v54,v134
A_ v135,v55,v135
Zvav 84,220,184
A_ v124,v48,v124
A_ v125,v49,v125
Mvav 84,196,164
L_ v7,24,s55,0
Mvav 84,192,144
S_ s55,s71,522240
A_ v126,v50,v126
A_ v127,v51,v127
Zvav 80,220,180
W_ 10
A_ v116,v44,v116
A_ v117,v45,v117
Mvav 80,196,160
S_ m0,s51,52224
A_ v118,v46,v118
A_ v119,v47,v119
Mvav 80,192,140
L_ v7,24,s55,0
Zvav 76,220,176
W_ 8
A_ v108,v40,v108
A_ v109,v41,v109
Mvav 76,196,156
S_ s55,s71,532480
A_ v110,v42,v110
A_ v111,v43,v111
Mvav 76,192,136
W_ 0
A_ v100,v36,v100
Zvav 16,180,136
S_ m0,s51,53248
Ta 192,v3,36864
Ta 194,v3,38912
Mvav 16,184,156
L_ v7,24,s55,0
Mvav 16,188,176
S_ s55,s71,542720
Ta 196,v3,40960
Ta 198,v3,43008
Zvav 20,180,140
S_ m0,s51,54272
A_ v101,v37,v101
A_ v102,v38,v102
Mvav 20,184,160
S_ s85,s6,3
Ta 220,v3,45056
Ta 222,v3,47104
Mvav 20,188,180
L_ v7,24,s55,0
Zvav 24,180,144
s_and_b32 s85, s85, 7
A_ v103,v39,v103
C_ 131,130,131
Mvav 24,184,164
s_lshl_b32 s85, s85, 7
C_ 130,128,129
C_ 132,132,133
Mvav 24,188,184
S_ s85,s85,s87
C_ 133,134,135
v_min_u32 v12, 240, v12
Zvav 28,180,148
buffer_store_dwordx4 v[130:133], v6, s[28:31], s73 offen offset:0 sc1
Mvav 28,184,168
s_sub_u32 s73, s73, 4096
v_lshlrev_b32 v12, 3, v12
Mvav 28,188,188
C_ 123,122,123
C_ 122,120,121
Zvav 32,180,152
C_ 124,124,125
C_ 125,126,127
Mvav 32,184,172
buffer_store_dwordx4 v[122:125], v6, s[28:31], s73 offen offset:3072 sc1
Mvav 32,188,192
s_waitcnt vmcnt(21) lgkmcnt(0)
s_barrier
C_ 115,114,115
Zvav 52,220,192
S_ s55,s71,614400
Ta 180,%0,24576
Ta 182,%0,26624
Mvav 52,196,172
S_ m0,s51,36864
C_ 114,112,113
C_ 116,116,117
Mvav 52,192,152
s_nop 0
Ta 184,%0,28672
Ta 186,%0,30720
Zvav 48,220,188
L_ v7,24,s55,0
Mvav 48,196,168
S_ s55,s71,624640
Ta 188,%0,32768
Ta 190,%0,34816
Mvav 48,192,148
S_ m0,s51,37888
C_ 117,118,119
Zvav 44,220,184
Mvav 44,196,164
buffer_store_dwordx4 v[114:117], v6, s[28:31], s73 offen offset:2048 sc1
Mvav 44,192,144
C_ 107,106,107
C_ 106,104,105
Zvav 40,220,180
C_ 108,108,109
C_ 109,110,111
Mvav 40,196,160
Mvav 40,192,140
buffer_store_dwordx4 v[106:109], v6, s[28:31], s73 offen offset:1024 sc1
Zvav 36,220,176
C_ 99,98,99
C_ 98,96,97
Mvav 36,196,156
C_ 100,100,101
C_ 101,102,103
Mvav 36,192,136
W_ 0
Mvav 56,180,196
s_nop 0
Ta 192,v1,24576
Ta 194,v1,26624
Mvav 56,184,216
buffer_store_dwordx4 v[98:101], v6, s[28:31], s73 offen offset:0 sc1
Mvav 56,188,236
s_nop 0
Ta 196,v1,28672
Ta 198,v1,30720
Mvav 60,180,200
Mvav 60,184,220
s_nop 0
Ta 220,v1,32768
Ta 222,v1,34816
Mvav 60,188,240
L_ v7,24,s55,0
Mvav 64,180,204
S_ s55,s71,634880
Mvav 64,184,224
S_ m0,s51,38912
Mvav 64,188,244
Mvav 68,180,208
L_ v7,24,s55,0
Mvav 68,184,228
S_ s55,s71,645120
Mvav 68,188,248
S_ m0,s51,39936
Mvav 72,180,212
Mvav 72,184,232
L_ v7,24,s55,0
Mvav 72,188,252
W_ 0
S_ s55,s71,655360
Mvav 92,220,252
S_ m0,s51,40960
Ta 180,v2,24576
Ta 182,v2,26624
Mvav 92,196,232
Mvav 92,192,212
s_nop 0
Ta 184,v2,28672
Ta 186,v2,30720
Mvav 88,220,248
L_ v7,24,s55,0
Mvav 88,196,228
S_ s55,s71,665600
Ta 188,v2,32768
Ta 190,v2,34816
Mvav 88,192,208
S_ m0,s51,41984
Mvav 84,220,244
Mvav 84,196,224
L_ v7,24,s55,0
Mvav 84,192,204
Mvav 80,220,240
Mvav 80,196,220
Mvav 80,192,200
buffer_load_dword a74, v12, s[44:47], s66 offen
Mvav 76,220,236
Mvav 76,196,216
Mvav 76,192,196
W_ 0
Mvav 16,180,196
s_nop 0
Ta 192,v3,24576
Ta 194,v3,26624
Mvav 16,184,216
buffer_load_dword a74, v4, s[24:27], s85 offen
Mvav 16,188,236
s_nop 0
Ta 196,v3,28672
Ta 198,v3,30720
Mvav 20,180,200
Mvav 20,184,220
s_nop 0
Ta 220,v3,32768
Ta 222,v3,34816
Mvav 20,188,240
Mvav 24,180,204
Mvav 24,184,224
Mvav 24,188,244
Mvav 28,180,208
Mvav 28,184,228
Mvav 28,188,248
Mvav 32,180,212
Mvav 32,184,232
Mvav 32,188,252
s_waitcnt vmcnt(25) lgkmcnt(0)
s_barrier
Mvav 52,220,252
S_ s55,s71,0
Ta 180,%0,0
Ta 182,%0,2048
Mvav 52,196,232
S_ m0,s51,24576
Mvav 52,192,212
s_nop 0
Ta 184,%0,4096
Ta 186,%0,6144
Mvav 48,220,248
L_ v7,24,s55,0
Mvav 48,196,228
S_ s55,s71,10240
Ta 188,%0,8192
Ta 190,%0,10240
Mvav 48,192,208
S_ m0,s51,25600
Mvav 44,220,244
Mvav 44,196,224
L_ v7,24,s55,0
Mvav 44,192,204
S_ s55,s71,20480
Mvav 40,220,240
S_ m0,s51,26624
Mvav 40,196,220
Mvav 40,192,200
L_ v7,24,s55,0
Mvav 36,220,236
S_ s55,s71,30720
Mvav 36,196,216
S_ m0,s51,27648
Mvav 36,192,196
W_ 0
Mvaa 56,180,0
s_nop 0
Ta 192,v1,0
Ta 194,v1,2048
Mvaa 56,184,40
L_ v7,24,s55,0
Mvaa 56,188,80
S_ s55,s71,40960
Ta 196,v1,4096
Ta 198,v1,6144
Mvaa 60,180,4
S_ m0,s51,28672
Mvaa 60,184,44
s_nop 0
Ta 220,v1,8192
Ta 222,v1,10240
Mvaa 60,188,84
L_ v7,24,s55,0
Mvaa 64,180,8
S_ s55,s71,51200
Mvaa 64,184,48
S_ m0,s51,29696
Mvaa 64,188,88
Mvaa 68,180,12
L_ v7,24,s55,0
Mvaa 68,184,52
Mvaa 68,188,240
Mvaa 72,180,16
Mvaa 72,184,56
Mvaa 72,188,244
W_ 0
Mvaa 92,220,244
s_nop 0
Ta 180,v2,0
Ta 182,v2,2048
Mvaa 92,196,56
Mvaa 92,192,16
s_nop 0
Ta 184,v2,4096
Ta 186,v2,6144
Mvaa 88,220,240
Mvaa 88,196,52
s_nop 0
Ta 188,v2,8192
Ta 190,v2,10240
Mvaa 88,192,12
Mvaa 84,220,88
Mvaa 84,196,48
Mvaa 84,192,8
Mvaa 80,220,84
Mvaa 80,196,44
Mvaa 80,192,4
Mvaa 76,220,80
Mvaa 76,196,40
Mvaa 76,192,0
W_ 0
Mvaa 16,180,0
s_nop 0
Ta 192,v3,0
Ta 194,v3,2048
Mvaa 16,184,40
Mvaa 16,188,80
s_nop 0
Ta 196,v3,4096
Ta 198,v3,6144
Mvaa 20,180,4
Mvaa 20,184,44
s_nop 0
Ta 220,v3,8192
Ta 222,v3,10240
Mvaa 20,188,84
Mvaa 24,180,8
Mvaa 24,184,48
Mvaa 24,188,88
Mvaa 28,180,12
Mvaa 28,184,52
Mvaa 28,188,240
Mvaa 32,180,16
Mvaa 32,184,56
Mvaa 32,188,244
s_waitcnt vmcnt(25) lgkmcnt(0)
s_barrier
Mvaa 52,220,244
s_nop 0
Ta 180,%0,12288
Ta 182,%0,14336
Mvaa 52,196,56
Mvaa 52,192,16
s_nop 0
Ta 184,%0,16384
Ta 186,%0,18432
Mvaa 48,220,240
Mvaa 48,196,52
s_nop 0
Ta 188,%0,20480
Ta 190,%0,22528
Mvaa 48,192,12
Mvaa 44,220,88
Mvaa 44,196,48
Mvaa 44,192,8
Mvaa 40,220,84
Mvaa 40,196,44
Mvaa 40,192,4
Mvaa 36,220,80
Mvaa 36,196,40
Mvaa 36,192,0
W_ 0
Mvaa 56,180,120
S_ s73,s78,184320
Ta 192,v1,12288
Ta 194,v1,14336
Mvaa 56,184,160
S_ s73,s73,4096
Mvaa 56,188,200
s_nop 0
Ta 196,v1,16384
Ta 198,v1,18432
Mvaa 60,180,124
s_mov_b32 m0, s52
Mvaa 60,184,164
s_nop 0
Ta 220,v1,20480
Ta 222,v1,22528
Mvaa 60,188,204
s_nop 0
Y_ 56,0
Y_ 57,256
Mvaa 64,180,128
s_nop 0
Y_ 58,512
Y_ 59,768
Mvaa 64,184,168
Mvaa 64,188,208
s_nop 0
Y_ 60,1024
Y_ 61,1280
Mvaa 68,180,132
s_nop 0
Y_ 62,1536
Y_ 63,1792
Mvaa 68,184,172
Mvaa 68,188,212
s_nop 0
Y_ 64,2048
Y_ 65,2304
Mvaa 72,180,136
s_nop 0
Y_ 66,2560
Y_ 67,2816
Mvaa 72,184,176
Mvaa 72,188,216
W_ 12
Y_ 68,3072
Y_ 69,3328
Mvaa 92,220,216
s_nop 0
Ta 180,v2,12288
Ta 182,v2,14336
Mvaa 92,196,176
s_nop 0
Y_ 70,3584
Y_ 71,3840
Mvaa 92,192,136
s_nop 0
Ta 184,v2,16384
Ta 186,v2,18432
Mvaa 88,220,212
s_nop 0
Y_ 72,4096
Y_ 73,4352
Mvaa 88,196,172
s_nop 0
Ta 188,v2,20480
Ta 190,v2,22528
Mvaa 88,192,132
s_nop 0
Y_ 74,4608
Y_ 75,4864
Mvaa 84,220,208
s_nop 0
Y_ 92,9216
Y_ 93,9472
Mvaa 84,196,168
s_nop 0
Y_ 94,9728
Y_ 95,9984
Mvaa 84,192,128
s_nop 0
Y_ 88,8192
Y_ 89,8448
Mvaa 80,220,204
s_nop 0
Y_ 90,8704
Y_ 91,8960
Mvaa 80,196,164
Mvaa 80,192,124
s_nop 0
Y_ 84,7168
Y_ 85,7424
Mvaa 76,220,200
s_nop 0
Y_ 86,7680
Y_ 87,7936
Mvaa 76,196,160
Mvaa 76,192,120
W_ 14
Y_ 80,6144
Y_ 81,6400
Mvaa 16,180,120
s_nop 0
Ta 192,v3,12288
Ta 194,v3,14336
Mvaa 16,184,160
s_nop 0
Y_ 82,6656
Y_ 83,6912
Mvaa 16,188,200
s_nop 0
Ta 196,v3,16384
Ta 198,v3,18432
Mvaa 20,180,124
s_nop 0
Y_ 76,5120
Y_ 77,5376
Mvaa 20,184,164
s_nop 0
Ta 220,v3,20480
Ta 222,v3,22528
Mvaa 20,188,204
s_nop 0
Y_ 78,5632
Y_ 79,5888
Mvaa 24,180,128
Mvaa 24,184,168
Mvaa 24,188,208
s_waitcnt vmcnt(19) lgkmcnt(0)
s_barrier
Mvaa 28,180,132
S_ s55,s71,122880
ds_read2st64_b32 v[96:97], v5 offset0:0 offset1:1
Mvaa 28,184,172
S_ m0,s51,0
ds_read2st64_b32 v[98:99], v5 offset0:2 offset1:3
Mvaa 28,188,212
L_ v7,24,s55,0
Mvaa 32,180,136
S_ s55,s71,133120
ds_read2st64_b32 v[104:105], v5 offset0:4 offset1:5
Mvaa 32,184,176
S_ m0,s51,1024
ds_read2st64_b32 v[106:107], v5 offset0:6 offset1:7
Mvaa 32,188,216
ds_read2st64_b32 v[112:113], v5 offset0:8 offset1:9
Mvaa 52,220,216
s_nop 0
Ta 180,%0,49152
Ta 182,%0,51200
Mvaa 52,196,176
L_ v7,24,s55,0
Mvaa 52,192,136
W_ 5
Ta 184,%0,53248
Ta 186,%0,55296
Mvaa 48,220,212
S_ s55,s71,143360
ds_read2st64_b32 v[114:115], v5 offset0:10 offset1:11
A_ v96,v16,v96
Mvaa 48,196,172
S_ m0,s51,2048
Ta 188,%0,57344
Ta 190,%0,59392
Mvaa 48,192,132
L_ v7,24,s55,0
Mvaa 44,220,208
S_ s55,s71,153600
ds_read2st64_b32 v[120:121], v5 offset0:12 offset1:13
A_ v97,v17,v97
Mvaa 44,196,168
S_ m0,s51,3072
ds_read2st64_b32 v[122:123], v5 offset0:14 offset1:15
A_ v98,v18,v98
Mvaa 44,192,128
ds_read2st64_b32 v[128:129], v5 offset0:16 offset1:17
A_ v99,v19,v99
Mvaa 40,220,204
L_ v7,24,s55,0
Mvaa 40,196,164
W_ 11
ds_read2st64_b32 v[130:131], v5 offset0:18 offset1:19
A_ v104,v20,v104
Mvaa 40,192,124
S_ s55,s71,163840
ds_read2st64_b32 v[132:133], v5 offset0:36 offset1:37
A_ v105,v21,v105
Mvaa 36,220,200
S_ m0,s51,4096
ds_read2st64_b32 v[134:135], v5 offset0:38 offset1:39
A_ v106,v22,v106
Mvaa 36,196,160
L_ v7,24,s55,0
Mvaa 36,192,120
W_ 6
ds_read2st64_b32 v[124:125], v5 offset0:32 offset1:33
A_ v107,v23,v107
Zvav 56,180,136
Ta 192,v1,49152
Ta 194,v1,51200
Mvav 56,184,156
S_ s55,s71,174080
ds_read2st64_b32 v[126:127], v5 offset0:34 offset1:35
A_ v112,v24,v112
Mvav 56,188,176
S_ m0,s51,5120
Ta 196,v1,53248
Ta 198,v1,55296
Zvav 60,180,140
L_ v7,24,s55,0
Mvav 60,184,160
S_ s79,s6,2
Ta 220,v1,57344
Ta 222,v1,59392
Mvav 60,188,180
s_and_b32 s79, s79, 7
ds_read2st64_b32 v[116:117], v5 offset0:28 offset1:29
A_ v113,v25,v113
Zvav 64,180,144
s_lshl_b32 s79, s79, 7
ds_read2st64_b32 v[118:119], v5 offset0:30 offset1:31
A_ v114,v26,v114
Mvav 64,184,164
S_ s71,s72,s79
ds_read2st64_b32 v[108:109], v5 offset0:24 offset1:25
A_ v115,v27,v115
Mvav 64,188,184
W_ 14
ds_read2st64_b32 v[110:111], v5 offset0:26 offset1:27
A_ v120,v28,v120
Zvav 68,180,148
S_ s55,s71,491520
ds_read2st64_b32 v[100:101], v5 offset0:20 offset1:21
A_ v121,v29,v121
Mvav 68,184,168
S_ m0,s51,12288
ds_read2st64_b32 v[102:103], v5 offset0:22 offset1:23
A_ v122,v30,v122
Mvav 68,188,188
L_ v7,24,s55,0
Zvav 72,180,152
S_ s55,s71,501760
A_ v123,v31,v123
Mvav 72,184,172
W_ 14
A_ v128,v32,v128
A_ v129,v33,v129
Mvav 72,188,192
W_ 6
A_ v130,v34,v130
A_ v131,v35,v131
Zvav 92,220,192
Ta 180,v2,49152
Ta 182,v2,51200
Mvav 92,196,172
S_ m0,s51,13312
A_ v132,v52,v132
A_ v133,v53,v133
Mvav 92,192,152
s_nop 0
Ta 184,v2,53248
Ta 186,v2,55296
Zvav 88,220,188
L_ v7,24,s55,0
Mvav 88,196,168
S_ s55,s71,512000
Ta 188,v2,57344
Ta 190,v2,59392
Mvav 88,192,148
S_ m0,s51,14336
A_ v134,v54,v134
A_ v135,v55,v135
Zvav 84,220,184
A_ v124,v48,v124
A_ v125,v49,v125
Mvav 84,196,164
L_ v7,24,s55,0
Mvav 84,192,144
S_ s55,s71,522240
A_ v126,v50,v126
A_ v127,v51,v127
Zvav 80,220,180
W_ 10
A_ v116,v44,v116
A_ v117,v45,v117
Mvav 80,196,160
S_ m0,s51,15360
A_ v118,v46,v118
A_ v119,v47,v119
Mvav 80,192,140
L_ v7,24,s55,0
Zvav 76,220,176
W_ 8
A_ v108,v40,v108
A_ v109,v41,v109
Mvav 76,196,156
S_ s55,s71,532480
A_ v110,v42,v110
A_ v111,v43,v111
Mvav 76,192,136
W_ 0
A_ v100,v36,v100
Zvav 16,180,136
S_ m0,s51,16384
Ta 192,v3,49152
Ta 194,v3,51200
Mvav 16,184,156
L_ v7,24,s55,0
Mvav 16,188,176
S_ s55,s71,542720
Ta 196,v3,53248
Ta 198,v3,55296
Zvav 20,180,140
S_ m0,s51,17408
A_ v101,v37,v101
A_ v102,v38,v102
Mvav 20,184,160
S_ s85,s6,4
Ta 220,v3,57344
Ta 222,v3,59392
Mvav 20,188,180
L_ v7,24,s55,0
Zvav 24,180,144
s_and_b32 s85, s85, 7
A_ v103,v39,v103
C_ 131,130,131
Mvav 24,184,164
s_lshl_b32 s85, s85, 7
C_ 130,128,129
C_ 132,132,133
Mvav 24,188,184
S_ s85,s85,s87
C_ 133,134,135
Zvav 28,180,148
buffer_store_dwordx4 v[130:133], v6, s[28:31], s73 offen offset:0 sc1
Mvav 28,184,168
s_sub_u32 s73, s73, 4096
Mvav 28,188,188
C_ 123,122,123
C_ 122,120,121
Zvav 32,180,152
C_ 124,124,125
C_ 125,126,127
Mvav 32,184,172
buffer_store_dwordx4 v[122:125], v6, s[28:31], s73 offen offset:3072 sc1
Mvav 32,188,192
s_waitcnt vmcnt(22) lgkmcnt(0)
s_barrier
C_ 115,114,115
Zvav 52,220,192
S_ s55,s71,614400
Ta 180,%0,36864
Ta 182,%0,38912
Mvav 52,196,172
S_ m0,s51,49152
C_ 114,112,113
C_ 116,116,117
Mvav 52,192,152
s_nop 0
Ta 184,%0,40960
Ta 186,%0,43008
Zvav 48,220,188
L_ v7,24,s55,0
Mvav 48,196,168
S_ s55,s71,624640
Ta 188,%0,45056
Ta 190,%0,47104
Mvav 48,192,148
S_ m0,s51,50176
C_ 117,118,119
Zvav 44,220,184
Mvav 44,196,164
buffer_store_dwordx4 v[114:117], v6, s[28:31], s73 offen offset:2048 sc1
Mvav 44,192,144
C_ 107,106,107
C_ 106,104,105
Zvav 40,220,180
C_ 108,108,109
C_ 109,110,111
Mvav 40,196,160
Mvav 40,192,140
buffer_store_dwordx4 v[106:109], v6, s[28:31], s73 offen offset:1024 sc1
Zvav 36,220,176
C_ 99,98,99
C_ 98,96,97
Mvav 36,196,156
C_ 100,100,101
C_ 101,102,103
Mvav 36,192,136
W_ 0
Mvav 56,180,196
s_nop 0
Ta 192,v1,36864
Ta 194,v1,38912
Mvav 56,184,216
buffer_store_dwordx4 v[98:101], v6, s[28:31], s73 offen offset:0 sc1
Mvav 56,188,236
s_nop 0
Ta 196,v1,40960
Ta 198,v1,43008
Mvav 60,180,200
X_ a20,-1
X_ a21,-1
Mvav 60,184,220
s_nop 0
Ta 220,v1,45056
Ta 222,v1,47104
Mvav 60,188,240
L_ v7,24,s55,0
Mvav 64,180,204
S_ s55,s71,634880
X_ a22,-1
X_ a23,-1
Mvav 64,184,224
S_ s81,s6,7
S_ m0,s51,51200
Mvav 64,188,244
s_and_b32 s81, s81, 7
Mvav 68,180,208
L_ v7,24,s55,0
Mvav 68,184,228
s_mul_i32 s81, s81, 20480
S_ s55,s71,645120
Mvav 68,188,248
S_ s81,s81,s78
S_ m0,s51,52224
Mvav 72,180,212
Mvav 72,184,232
buffer_store_dwordx4 a[20:23], v6, s[28:31], s81 offen offset:0 sc1
Mvav 72,188,252
W_ 0
Mvav 92,220,252
s_nop 0
Ta 180,v2,36864
Ta 182,v2,38912
Mvav 92,196,232
Mvav 92,192,212
s_nop 0
Ta 184,v2,40960
Ta 186,v2,43008
Mvav 88,220,248
buffer_store_dwordx4 a[20:23], v6, s[28:31], s81 offen offset:1024 sc1
Mvav 88,196,228
s_nop 0
Ta 188,v2,45056
Ta 190,v2,47104
Mvav 88,192,208
Mvav 84,220,244
Mvav 84,196,224
buffer_store_dwordx4 a[20:23], v6, s[28:31], s81 offen offset:2048 sc1
Mvav 84,192,204
Mvav 80,220,240
Mvav 80,196,220
Mvav 80,192,200
buffer_store_dwordx4 a[20:23], v6, s[28:31], s81 offen offset:3072 sc1
Mvav 76,220,236
S_ s81,s81,4096
Mvav 76,196,216
Mvav 76,192,196
W_ 0
Mvav 16,180,196
s_nop 0
Ta 192,v3,36864
Ta 194,v3,38912
Mvav 16,184,216
buffer_store_dwordx4 a[20:23], v6, s[28:31], s81 offen offset:0 sc1
Mvav 16,188,236
s_nop 0
Ta 196,v3,40960
Ta 198,v3,43008
Mvav 20,180,200
Mvav 20,184,220
s_nop 0
Ta 220,v3,45056
Ta 222,v3,47104
Mvav 20,188,240
L_ v7,24,s55,0
Mvav 24,180,204
S_ s55,s71,655360
Mvav 24,184,224
S_ m0,s51,53248
Mvav 24,188,244
Mvav 28,180,208
L_ v7,24,s55,0
Mvav 28,184,228
S_ s55,s71,665600
Mvav 28,188,248
S_ m0,s51,54272
Mvav 32,180,212
Mvav 32,184,232
L_ v7,24,s55,0
Mvav 32,188,252
s_waitcnt vmcnt(28) lgkmcnt(0)
s_barrier
Mvav 52,220,252
S_ s55,s71,0
Ta 180,%0,24576
Ta 182,%0,26624
Mvav 52,196,232
S_ m0,s51,36864
Mvav 52,192,212
s_nop 0
Ta 184,%0,28672
Ta 186,%0,30720
Mvav 48,220,248
L_ v7,24,s55,0
Mvav 48,196,228
S_ s55,s71,10240
Ta 188,%0,32768
Ta 190,%0,34816
Mvav 48,192,208
S_ m0,s51,37888
Mvav 44,220,244
Mvav 44,196,224
L_ v7,24,s55,0
Mvav 44,192,204
S_ s55,s71,20480
Mvav 40,220,240
S_ m0,s51,38912
Mvav 40,196,220
Mvav 40,192,200
L_ v7,24,s55,0
Mvav 36,220,236
S_ s55,s71,30720
Mvav 36,196,216
S_ m0,s51,39936
Mvav 36,192,196
W_ 0
Mvaa 56,180,0
s_nop 0
Ta 192,v1,24576
Ta 194,v1,26624
Mvaa 56,184,40
L_ v7,24,s55,0
Mvaa 56,188,80
S_ s55,s71,40960
Ta 196,v1,28672
Ta 198,v1,30720
Mvaa 60,180,4
S_ m0,s51,40960
Mvaa 60,184,44
s_nop 0
Ta 220,v1,32768
Ta 222,v1,34816
Mvaa 60,188,84
L_ v7,24,s55,0
Mvaa 64,180,8
S_ s55,s71,51200
Mvaa 64,184,48
S_ m0,s51,41984
Mvaa 64,188,88
Mvaa 68,180,12
L_ v7,24,s55,0
Mvaa 68,184,52
Mvaa 68,188,240
Mvaa 72,180,16
Mvaa 72,184,56
buffer_load_dword a74, v4, s[24:27], s85 offen
Mvaa 72,188,244
W_ 0
Mvaa 92,220,244
s_nop 0
Ta 180,v2,24576
Ta 182,v2,26624
Mvaa 92,196,56
Mvaa 92,192,16
s_nop 0
Ta 184,v2,28672
Ta 186,v2,30720
Mvaa 88,220,240
Mvaa 88,196,52
s_nop 0
Ta 188,v2,32768
Ta 190,v2,34816
Mvaa 88,192,12
Mvaa 84,220,88
Mvaa 84,196,48
Mvaa 84,192,8
Mvaa 80,220,84
Mvaa 80,196,44
Mvaa 80,192,4
Mvaa 76,220,80
Mvaa 76,196,40
Mvaa 76,192,0
W_ 0
Mvaa 16,180,0
s_nop 0
Ta 192,v3,24576
Ta 194,v3,26624
Mvaa 16,184,40
Mvaa 16,188,80
s_nop 0
Ta 196,v3,28672
Ta 198,v3,30720
Mvaa 20,180,4
Mvaa 20,184,44
s_nop 0
Ta 220,v3,32768
Ta 222,v3,34816
Mvaa 20,188,84
Mvaa 24,180,8
Mvaa 24,184,48
Mvaa 24,188,88
Mvaa 28,180,12
Mvaa 28,184,52
Mvaa 28,188,240
Mvaa 32,180,16
Mvaa 32,184,56
Mvaa 32,188,244
s_waitcnt vmcnt(29) lgkmcnt(0)
s_barrier
Mvaa 52,220,244
s_nop 0
Ta 180,%0,0
Ta 182,%0,2048
Mvaa 52,196,56
Mvaa 52,192,16
s_nop 0
Ta 184,%0,4096
Ta 186,%0,6144
Mvaa 48,220,240
Mvaa 48,196,52
s_nop 0
Ta 188,%0,8192
Ta 190,%0,10240
Mvaa 48,192,12
Mvaa 44,220,88
Mvaa 44,196,48
Mvaa 44,192,8
Mvaa 40,220,84
Mvaa 40,196,44
Mvaa 40,192,4
Mvaa 36,220,80
Mvaa 36,196,40
Mvaa 36,192,0
W_ 0
Mvaa 56,180,120
S_ s81,s6,1
Ta 192,v1,0
Ta 194,v1,2048
Mvaa 56,184,160
s_and_b32 s81, s81, 7
Mvaa 56,188,200
s_mul_i32 s81, s81, 20480
Ta 196,v1,4096
Ta 198,v1,6144
Mvaa 60,180,124
s_mov_b32 m0, s52
S_ s73,s81,s78
Mvaa 60,184,164
S_ s73,s73,4096
Ta 220,v1,8192
Ta 222,v1,10240
Mvaa 60,188,204
s_nop 0
Y_ 56,0
Y_ 57,256
Mvaa 64,180,128
s_nop 0
Y_ 58,512
Y_ 59,768
Mvaa 64,184,168
Mvaa 64,188,208
s_nop 0
Y_ 60,1024
Y_ 61,1280
Mvaa 68,180,132
s_nop 0
Y_ 62,1536
Y_ 63,1792
Mvaa 68,184,172
Mvaa 68,188,212
s_nop 0
Y_ 64,2048
Y_ 65,2304
Mvaa 72,180,136
s_nop 0
Y_ 66,2560
Y_ 67,2816
Mvaa 72,184,176
Mvaa 72,188,216
W_ 12
Y_ 68,3072
Y_ 69,3328
Mvaa 92,220,216
s_nop 0
Ta 180,v2,0
Ta 182,v2,2048
Mvaa 92,196,176
s_nop 0
Y_ 70,3584
Y_ 71,3840
Mvaa 92,192,136
s_nop 0
Ta 184,v2,4096
Ta 186,v2,6144
Mvaa 88,220,212
s_nop 0
Y_ 72,4096
Y_ 73,4352
Mvaa 88,196,172
s_nop 0
Ta 188,v2,8192
Ta 190,v2,10240
Mvaa 88,192,132
s_nop 0
Y_ 74,4608
Y_ 75,4864
Mvaa 84,220,208
s_nop 0
Y_ 92,9216
Y_ 93,9472
Mvaa 84,196,168
s_nop 0
Y_ 94,9728
Y_ 95,9984
Mvaa 84,192,128
s_nop 0
Y_ 88,8192
Y_ 89,8448
Mvaa 80,220,204
s_nop 0
Y_ 90,8704
Y_ 91,8960
Mvaa 80,196,164
Mvaa 80,192,124
s_nop 0
Y_ 84,7168
Y_ 85,7424
Mvaa 76,220,200
s_nop 0
Y_ 86,7680
Y_ 87,7936
Mvaa 76,196,160
Mvaa 76,192,120
W_ 14
Y_ 80,6144
Y_ 81,6400
Mvaa 16,180,120
s_nop 0
Ta 192,v3,0
Ta 194,v3,2048
Mvaa 16,184,160
s_nop 0
Y_ 82,6656
Y_ 83,6912
Mvaa 16,188,200
s_nop 0
Ta 196,v3,4096
Ta 198,v3,6144
Mvaa 20,180,124
s_nop 0
Y_ 76,5120
Y_ 77,5376
Mvaa 20,184,164
s_nop 0
Ta 220,v3,8192
Ta 222,v3,10240
Mvaa 20,188,204
s_nop 0
Y_ 78,5632
Y_ 79,5888
Mvaa 24,180,128
Mvaa 24,184,168
Mvaa 24,188,208
s_waitcnt vmcnt(23) lgkmcnt(0)
s_barrier
Mvaa 28,180,132
S_ s55,s71,122880
ds_read2st64_b32 v[96:97], v5 offset0:0 offset1:1
Mvaa 28,184,172
S_ m0,s51,24576
ds_read2st64_b32 v[98:99], v5 offset0:2 offset1:3
Mvaa 28,188,212
L_ v7,24,s55,0
Mvaa 32,180,136
S_ s55,s71,133120
ds_read2st64_b32 v[104:105], v5 offset0:4 offset1:5
Mvaa 32,184,176
S_ m0,s51,25600
ds_read2st64_b32 v[106:107], v5 offset0:6 offset1:7
Mvaa 32,188,216
ds_read2st64_b32 v[112:113], v5 offset0:8 offset1:9
Mvaa 52,220,216
s_nop 0
Ta 180,%0,12288
Ta 182,%0,14336
Mvaa 52,196,176
L_ v7,24,s55,0
Mvaa 52,192,136
W_ 5
Ta 184,%0,16384
Ta 186,%0,18432
Mvaa 48,220,212
S_ s55,s71,143360
ds_read2st64_b32 v[114:115], v5 offset0:10 offset1:11
A_ v96,v16,v96
Mvaa 48,196,172
S_ m0,s51,26624
Ta 188,%0,20480
Ta 190,%0,22528
Mvaa 48,192,132
L_ v7,24,s55,0
Mvaa 44,220,208
S_ s55,s71,153600
ds_read2st64_b32 v[120:121], v5 offset0:12 offset1:13
A_ v97,v17,v97
Mvaa 44,196,168
S_ m0,s51,27648
ds_read2st64_b32 v[122:123], v5 offset0:14 offset1:15
A_ v98,v18,v98
Mvaa 44,192,128
ds_read2st64_b32 v[128:129], v5 offset0:16 offset1:17
A_ v99,v19,v99
Mvaa 40,220,204
L_ v7,24,s55,0
Mvaa 40,196,164
W_ 11
ds_read2st64_b32 v[130:131], v5 offset0:18 offset1:19
A_ v104,v20,v104
Mvaa 40,192,124
S_ s55,s71,163840
ds_read2st64_b32 v[132:133], v5 offset0:36 offset1:37
A_ v105,v21,v105
Mvaa 36,220,200
S_ m0,s51,28672
ds_read2st64_b32 v[134:135], v5 offset0:38 offset1:39
A_ v106,v22,v106
Mvaa 36,196,160
L_ v7,24,s55,0
Mvaa 36,192,120
W_ 6
ds_read2st64_b32 v[124:125], v5 offset0:32 offset1:33
A_ v107,v23,v107
Zvav 56,180,136
Ta 192,v1,12288
Ta 194,v1,14336
Mvav 56,184,156
S_ s55,s71,174080
ds_read2st64_b32 v[126:127], v5 offset0:34 offset1:35
A_ v112,v24,v112
Mvav 56,188,176
S_ m0,s51,29696
Ta 196,v1,16384
Ta 198,v1,18432
Zvav 60,180,140
L_ v7,24,s55,0
Mvav 60,184,160
S_ s79,s6,3
Ta 220,v1,20480
Ta 222,v1,22528
Mvav 60,188,180
s_and_b32 s79, s79, 7
ds_read2st64_b32 v[116:117], v5 offset0:28 offset1:29
A_ v113,v25,v113
Zvav 64,180,144
s_lshl_b32 s79, s79, 7
ds_read2st64_b32 v[118:119], v5 offset0:30 offset1:31
A_ v114,v26,v114
Mvav 64,184,164
S_ s71,s72,s79
ds_read2st64_b32 v[108:109], v5 offset0:24 offset1:25
A_ v115,v27,v115
Mvav 64,188,184
W_ 14
ds_read2st64_b32 v[110:111], v5 offset0:26 offset1:27
A_ v120,v28,v120
Zvav 68,180,148
S_ s55,s71,491520
ds_read2st64_b32 v[100:101], v5 offset0:20 offset1:21
A_ v121,v29,v121
Mvav 68,184,168
S_ m0,s51,0
ds_read2st64_b32 v[102:103], v5 offset0:22 offset1:23
A_ v122,v30,v122
Mvav 68,188,188
L_ v7,24,s55,0
Zvav 72,180,152
S_ s55,s71,501760
A_ v123,v31,v123
Mvav 72,184,172
W_ 14
A_ v128,v32,v128
A_ v129,v33,v129
Mvav 72,188,192
W_ 6
A_ v130,v34,v130
A_ v131,v35,v131
Zvav 92,220,192
Ta 180,v2,12288
Ta 182,v2,14336
Mvav 92,196,172
S_ m0,s51,1024
A_ v132,v52,v132
A_ v133,v53,v133
Mvav 92,192,152
s_nop 0
Ta 184,v2,16384
Ta 186,v2,18432
Zvav 88,220,188
L_ v7,24,s55,0
Mvav 88,196,168
S_ s55,s71,512000
Ta 188,v2,20480
Ta 190,v2,22528
Mvav 88,192,148
S_ m0,s51,2048
A_ v134,v54,v134
A_ v135,v55,v135
Zvav 84,220,184
A_ v124,v48,v124
A_ v125,v49,v125
Mvav 84,196,164
L_ v7,24,s55,0
Mvav 84,192,144
S_ s55,s71,522240
A_ v126,v50,v126
A_ v127,v51,v127
Zvav 80,220,180
W_ 10
A_ v116,v44,v116
A_ v117,v45,v117
Mvav 80,196,160
S_ m0,s51,3072
A_ v118,v46,v118
A_ v119,v47,v119
Mvav 80,192,140
L_ v7,24,s55,0
Zvav 76,220,176
W_ 8
A_ v108,v40,v108
A_ v109,v41,v109
Mvav 76,196,156
S_ s55,s71,532480
A_ v110,v42,v110
A_ v111,v43,v111
Mvav 76,192,136
W_ 0
A_ v100,v36,v100
Zvav 16,180,136
S_ m0,s51,4096
Ta 192,v3,12288
Ta 194,v3,14336
Mvav 16,184,156
L_ v7,24,s55,0
Mvav 16,188,176
S_ s55,s71,542720
Ta 196,v3,16384
Ta 198,v3,18432
Zvav 20,180,140
S_ m0,s51,5120
A_ v101,v37,v101
A_ v102,v38,v102
Mvav 20,184,160
S_ s85,s6,5
Ta 220,v3,20480
Ta 222,v3,22528
Mvav 20,188,180
L_ v7,24,s55,0
Zvav 24,180,144
s_and_b32 s85, s85, 7
A_ v103,v39,v103
C_ 131,130,131
Mvav 24,184,164
s_lshl_b32 s85, s85, 7
C_ 130,128,129
C_ 132,132,133
Mvav 24,188,184
S_ s85,s85,s87
C_ 133,134,135
Zvav 28,180,148
buffer_store_dwordx4 v[130:133], v6, s[28:31], s73 offen offset:0 sc1
Mvav 28,184,168
s_sub_u32 s73, s73, 4096
Mvav 28,188,188
C_ 123,122,123
C_ 122,120,121
Zvav 32,180,152
C_ 124,124,125
C_ 125,126,127
Mvav 32,184,172
buffer_store_dwordx4 v[122:125], v6, s[28:31], s73 offen offset:3072 sc1
Mvav 32,188,192
s_waitcnt vmcnt(21) lgkmcnt(0)
s_barrier
C_ 115,114,115
Zvav 52,220,192
S_ s56,s76,s74
Ta 180,%0,49152
Ta 182,%0,51200
Mvav 52,196,172
S_ s55,s71,614400
C_ 114,112,113
C_ 116,116,117
Mvav 52,192,152
S_ m0,s51,12288
Ta 184,%0,53248
Ta 186,%0,55296
Zvav 48,220,188
buffer_load_dwordx4 a[60:63], v6, s[28:31], s56 offen offset:0 sc1
Mvav 48,196,168
S_ s56,s76,s82
Ta 188,%0,57344
Ta 190,%0,59392
Mvav 48,192,148
C_ 117,118,119
Zvav 44,220,184
Mvav 44,196,164
buffer_load_dwordx4 a[64:67], v6, s[28:31], s56 offen offset:0 sc1
Mvav 44,192,144
Zvav 40,220,180
Mvav 40,196,160
Mvav 40,192,140
buffer_store_dwordx4 v[114:117], v6, s[28:31], s73 offen offset:2048 sc1
Zvav 36,220,176
C_ 107,106,107
C_ 106,104,105
Mvav 36,196,156
C_ 108,108,109
C_ 109,110,111
Mvav 36,192,136
W_ 0
Mvav 56,180,196
s_nop 0
Ta 192,v1,49152
Ta 194,v1,51200
Mvav 56,184,216
buffer_store_dwordx4 v[106:109], v6, s[28:31], s73 offen offset:1024 sc1
Mvav 56,188,236
s_nop 0
Ta 196,v1,53248
Ta 198,v1,55296
Mvav 60,180,200
C_ 99,98,99
C_ 98,96,97
Mvav 60,184,220
s_nop 0
Ta 220,v1,57344
Ta 222,v1,59392
Mvav 60,188,240
L_ v7,24,s55,0
Mvav 64,180,204
S_ s55,s71,624640
C_ 100,100,101
C_ 101,102,103
Mvav 64,184,224
S_ m0,s51,13312
Mvav 64,188,244
Mvav 68,180,208
buffer_store_dwordx4 v[98:101], v6, s[28:31], s73 offen offset:0 sc1
Mvav 68,184,228
Mvav 68,188,248
Mvav 72,180,212
Mvav 72,184,232
L_ v7,24,s55,0
Mvav 72,188,252
W_ 0
S_ s55,s71,634880
Mvav 92,220,252
S_ m0,s51,14336
Ta 180,v2,49152
Ta 182,v2,51200
Mvav 92,196,232
Mvav 92,192,212
s_nop 0
Ta 184,v2,53248
Ta 186,v2,55296
Mvav 88,220,248
L_ v7,24,s55,0
Mvav 88,196,228
S_ s55,s71,645120
Ta 188,v2,57344
Ta 190,v2,59392
Mvav 88,192,208
S_ m0,s51,15360
Mvav 84,220,244
Mvav 84,196,224
L_ v7,24,s55,0
Mvav 84,192,204
S_ s55,s71,655360
Mvav 80,220,240
S_ m0,s51,16384
Mvav 80,196,220
S_ s83,s6,7
Mvav 80,192,200
L_ v7,24,s55,0
Mvav 76,220,236
s_and_b32 s83, s83, 7
S_ s55,s71,665600
Mvav 76,196,216
s_lshl_b32 s83, s83, 2
S_ m0,s51,17408
Mvav 76,192,196
W_ 0
v_cmp_eq_u32_e64 s[36:37], s83, v8
Mvav 16,180,196
s_nop 0
Ta 192,v3,49152
Ta 194,v3,51200
Mvav 16,184,216
L_ v7,24,s55,0
Mvav 16,188,236
s_nop 0
Ta 196,v3,53248
Ta 198,v3,55296
Mvav 20,180,200
v_mov_b32 v10, 0x7fff0000
Mvav 20,184,220
s_nop 0
Ta 220,v3,57344
Ta 222,v3,59392
Mvav 20,188,240
buffer_load_dword a74, v4, s[24:27], s85 offen
Mvav 24,180,204
v_cndmask_b32_e64 v10, v10, v9, s[36:37]
Mvav 24,184,224
Mvav 24,188,244
Mvav 28,180,208
Mvav 28,184,228
Mvav 28,188,248
Mvav 32,180,212
Mvav 32,184,232
Mvav 32,188,252
s_waitcnt vmcnt(27) lgkmcnt(0)
s_barrier
Mvav 52,220,252
S_ s55,s71,0
Ta 180,%0,36864
Ta 182,%0,38912
Mvav 52,196,232
buffer_store_dword a255, v10, s[92:95], 0 offen offset:384 sc1
Mvav 52,192,212
S_ m0,s51,49152
Ta 184,%0,40960
Ta 186,%0,43008
Mvav 48,220,248
Mvav 48,196,228
s_nop 0
Ta 188,%0,45056
Ta 190,%0,47104
Mvav 48,192,208
L_ v7,24,s55,0
Mvav 44,220,244
S_ s55,s71,10240
Mvav 44,196,224
S_ m0,s51,50176
Mvav 44,192,204
Mvav 40,220,240
L_ v7,24,s55,0
Mvav 40,196,220
S_ s55,s71,20480
Mvav 40,192,200
S_ m0,s51,51200
Mvav 36,220,236
Mvav 36,196,216
L_ v7,24,s55,0
Mvav 36,192,196
W_ 0
S_ s55,s71,30720
Mvaa 56,180,0
S_ m0,s51,52224
Ta 192,v1,36864
Ta 194,v1,38912
Mvaa 56,184,40
Mvaa 56,188,80
s_nop 0
Ta 196,v1,40960
Ta 198,v1,43008
Mvaa 60,180,4
L_ v7,24,s55,0
Mvaa 60,184,44
S_ s55,s71,40960
Ta 220,v1,45056
Ta 222,v1,47104
Mvaa 60,188,84
S_ m0,s51,53248
Mvaa 64,180,8
Mvaa 64,184,48
L_ v7,24,s55,0
Mvaa 64,188,88
S_ s55,s71,51200
Mvaa 68,180,12
S_ m0,s51,54272
Mvaa 68,184,52
Mvaa 68,188,240
L_ v7,24,s55,0
Mvaa 72,180,16
Mvaa 72,184,56
Mvaa 72,188,244
W_ 0
Mvaa 92,220,244
s_nop 0
Ta 180,v2,36864
Ta 182,v2,38912
Mvaa 92,196,56
Mvaa 92,192,16
s_nop 0
Ta 184,v2,40960
Ta 186,v2,43008
Mvaa 88,220,240
Mvaa 88,196,52
s_nop 0
Ta 188,v2,45056
Ta 190,v2,47104
Mvaa 88,192,12
Mvaa 84,220,88
Mvaa 84,196,48
Mvaa 84,192,8
Mvaa 80,220,84
Mvaa 80,196,44
Mvaa 80,192,4
Mvaa 76,220,80
Mvaa 76,196,40
Mvaa 76,192,0
W_ 0
Mvaa 16,180,0
s_nop 0
Ta 192,v3,36864
Ta 194,v3,38912
Mvaa 16,184,40
Mvaa 16,188,80
s_nop 0
Ta 196,v3,40960
Ta 198,v3,43008
Mvaa 20,180,4
Mvaa 20,184,44
s_nop 0
Ta 220,v3,45056
Ta 222,v3,47104
Mvaa 20,188,84
Mvaa 24,180,8
Mvaa 24,184,48
Mvaa 24,188,88
Mvaa 28,180,12
Mvaa 28,184,52
Mvaa 28,188,240
Mvaa 32,180,16
Mvaa 32,184,56
Mvaa 32,188,244
s_waitcnt vmcnt(27) lgkmcnt(0)
s_barrier
Mvaa 52,220,244
s_nop 0
Ta 180,%0,24576
Ta 182,%0,26624
Mvaa 52,196,56
Mvaa 52,192,16
s_nop 0
Ta 184,%0,28672
Ta 186,%0,30720
Mvaa 48,220,240
Mvaa 48,196,52
s_nop 0
Ta 188,%0,32768
Ta 190,%0,34816
Mvaa 48,192,12
Mvaa 44,220,88
Mvaa 44,196,48
Mvaa 44,192,8
Mvaa 40,220,84
Mvaa 40,196,44
Mvaa 40,192,4
Mvaa 36,220,80
Mvaa 36,196,40
Mvaa 36,192,0
W_ 0
Mvaa 56,180,120
S_ s81,s6,2
Ta 192,v1,24576
Ta 194,v1,26624
Mvaa 56,184,160
s_and_b32 s81, s81, 7
Mvaa 56,188,200
s_mul_i32 s81, s81, 20480
Ta 196,v1,28672
Ta 198,v1,30720
Mvaa 60,180,124
s_mov_b32 m0, s52
S_ s73,s81,s78
Mvaa 60,184,164
S_ s73,s73,4096
Ta 220,v1,32768
Ta 222,v1,34816
Mvaa 60,188,204
s_nop 0
Y_ 56,0
Y_ 57,256
Mvaa 64,180,128
s_nop 0
Y_ 58,512
Y_ 59,768
Mvaa 64,184,168
Mvaa 64,188,208
s_nop 0
Y_ 60,1024
Y_ 61,1280
Mvaa 68,180,132
s_nop 0
Y_ 62,1536
Y_ 63,1792
Mvaa 68,184,172
Mvaa 68,188,212
s_nop 0
Y_ 64,2048
Y_ 65,2304
Mvaa 72,180,136
s_nop 0
Y_ 66,2560
Y_ 67,2816
Mvaa 72,184,176
Mvaa 72,188,216
W_ 12
Y_ 68,3072
Y_ 69,3328
Mvaa 92,220,216
s_nop 0
Ta 180,v2,24576
Ta 182,v2,26624
Mvaa 92,196,176
s_nop 0
Y_ 70,3584
Y_ 71,3840
Mvaa 92,192,136
s_nop 0
Ta 184,v2,28672
Ta 186,v2,30720
Mvaa 88,220,212
s_nop 0
Y_ 72,4096
Y_ 73,4352
Mvaa 88,196,172
S_ s83,s6,1
Ta 188,v2,32768
Ta 190,v2,34816
Mvaa 88,192,132
s_and_b32 s83, s83, 7
Y_ 74,4608
Y_ 75,4864
Mvaa 84,220,208
s_lshl_b32 s83, s83, 2
Y_ 92,9216
Y_ 93,9472
Mvaa 84,196,168
s_nop 0
Y_ 94,9728
Y_ 95,9984
Mvaa 84,192,128
s_nop 0
Y_ 88,8192
Y_ 89,8448
Mvaa 80,220,204
s_nop 0
Y_ 90,8704
Y_ 91,8960
Mvaa 80,196,164
v_cmp_eq_u32_e64 s[36:37], s83, v8
Mvaa 80,192,124
s_nop 0
Y_ 84,7168
Y_ 85,7424
Mvaa 76,220,200
s_nop 0
Y_ 86,7680
Y_ 87,7936
Mvaa 76,196,160
v_mov_b32 v10, 0x7fff0000
Mvaa 76,192,120
W_ 14
Y_ 80,6144
Y_ 81,6400
Mvaa 16,180,120
s_nop 0
Ta 192,v3,24576
Ta 194,v3,26624
Mvaa 16,184,160
s_nop 0
Y_ 82,6656
Y_ 83,6912
Mvaa 16,188,200
s_nop 0
Ta 196,v3,28672
Ta 198,v3,30720
Mvaa 20,180,124
s_nop 0
Y_ 76,5120
Y_ 77,5376
Mvaa 20,184,164
s_nop 0
Ta 220,v3,32768
Ta 222,v3,34816
Mvaa 20,188,204
s_nop 0
Y_ 78,5632
Y_ 79,5888
Mvaa 24,180,128
v_cndmask_b32_e64 v10, v10, v9, s[36:37]
Mvaa 24,184,168
Mvaa 24,188,208
s_waitcnt vmcnt(13) lgkmcnt(0)
s_barrier
Mvaa 28,180,132
buffer_store_dword a255, v10, s[92:95], 0 offen offset:384 sc1
Mvaa 28,184,172
S_ s55,s71,122880
ds_read2st64_b32 v[96:97], v5 offset0:0 offset1:1
Mvaa 28,188,212
)A"
R"A(S_ m0,s51,36864
ds_read2st64_b32 v[98:99], v5 offset0:2 offset1:3
Mvaa 32,180,136
ds_read2st64_b32 v[104:105], v5 offset0:4 offset1:5
Mvaa 32,184,176
L_ v7,24,s55,0
Mvaa 32,188,216
S_ s55,s71,133120
ds_read2st64_b32 v[106:107], v5 offset0:6 offset1:7
Mvaa 52,220,216
S_ m0,s51,37888
Ta 180,%0,0
Ta 182,%0,2048
Mvaa 52,196,176
ds_read2st64_b32 v[112:113], v5 offset0:8 offset1:9
Mvaa 52,192,136
W_ 5
Ta 184,%0,4096
Ta 186,%0,6144
Mvaa 48,220,212
L_ v7,24,s55,0
Mvaa 48,196,172
S_ s55,s71,143360
Ta 188,%0,8192
Ta 190,%0,10240
Mvaa 48,192,132
S_ m0,s51,38912
ds_read2st64_b32 v[114:115], v5 offset0:10 offset1:11
A_ v96,v16,v96
Mvaa 44,220,208
ds_read2st64_b32 v[120:121], v5 offset0:12 offset1:13
A_ v97,v17,v97
Mvaa 44,196,168
L_ v7,24,s55,0
Mvaa 44,192,128
S_ s55,s71,153600
ds_read2st64_b32 v[122:123], v5 offset0:14 offset1:15
A_ v98,v18,v98
Mvaa 40,220,204
S_ m0,s51,39936
ds_read2st64_b32 v[128:129], v5 offset0:16 offset1:17
A_ v99,v19,v99
Mvaa 40,196,164
W_ 11
ds_read2st64_b32 v[130:131], v5 offset0:18 offset1:19
A_ v104,v20,v104
Mvaa 40,192,124
L_ v7,24,s55,0
Mvaa 36,220,200
S_ s55,s71,163840
ds_read2st64_b32 v[132:133], v5 offset0:36 offset1:37
A_ v105,v21,v105
Mvaa 36,196,160
S_ m0,s51,40960
ds_read2st64_b32 v[134:135], v5 offset0:38 offset1:39
A_ v106,v22,v106
Mvaa 36,192,120
ds_read2st64_b32 v[124:125], v5 offset0:32 offset1:33
A_ v107,v23,v107
s_waitcnt vmcnt(22)
Zaaa 224,70,60
L_ v7,24,s55,0
Zaaa 228,68,60
W_ 7
ds_read2st64_b32 v[126:127], v5 offset0:34 offset1:35
A_ v112,v24,v112
Zaaa 232,70,64
S_ s55,s71,174080
ds_read2st64_b32 v[116:117], v5 offset0:28 offset1:29
A_ v113,v25,v113
Zaaa 236,68,64
ds_read2st64_b32 v[118:119], v5 offset0:30 offset1:31
A_ v114,v26,v114
Zvav 56,180,136
S_ m0,s51,41984
Ta 192,v1,0
Ta 194,v1,2048
Mvav 56,184,156
L_ v7,24,s55,0
Mvav 56,188,176
S_ s79,s6,4
Ta 196,v1,4096
Ta 198,v1,6144
Zvav 60,180,140
s_and_b32 s79, s79, 7
ds_read2st64_b32 v[108:109], v5 offset0:24 offset1:25
A_ v115,v27,v115
Mvav 60,184,160
W_ 13
Ta 220,v1,8192
Ta 222,v1,10240
Mvav 60,188,180
s_lshl_b32 s79, s79, 7
ds_read2st64_b32 v[110:111], v5 offset0:26 offset1:27
A_ v120,v28,v120
Zvav 64,180,144
S_ s71,s72,s79
ds_read2st64_b32 v[100:101], v5 offset0:20 offset1:21
A_ v121,v29,v121
Mvav 64,184,164
S_ s55,s71,491520
ds_read2st64_b32 v[102:103], v5 offset0:22 offset1:23
A_ v122,v30,v122
Mvav 64,188,184
S_ m0,s51,24576
A_ v123,v31,v123
R_ v12,a248
Zvav 68,180,148
L_ v7,24,s55,0
Mvav 68,184,168
W_ 14
A_ v128,v32,v128
A_ v129,v33,v129
Mvav 68,188,188
S_ s55,s71,501760
A_ v130,v34,v130
A_ v131,v35,v131
Zvav 72,180,152
A_ v132,v52,v132
A_ v133,v53,v133
Mvav 72,184,172
S_ m0,s51,25600
A_ v134,v54,v134
A_ v135,v55,v135
Mvav 72,188,192
W_ 3
A_ v124,v48,v124
Zvav 92,220,192
s_nop 0
Ta 180,v2,0
Ta 182,v2,2048
Mvav 92,196,172
L_ v7,24,s55,0
Mvav 92,192,152
S_ s55,s71,512000
Ta 184,v2,4096
Ta 186,v2,6144
Zvav 88,220,188
S_ m0,s51,26624
A_ v125,v49,v125
A_ v126,v50,v126
Mvav 88,196,168
s_nop 0
Ta 188,v2,8192
Ta 190,v2,10240
Mvav 88,192,148
L_ v7,24,s55,0
Zvav 84,220,184
S_ s55,s71,522240
A_ v127,v51,v127
C_ 131,130,131
Mvav 84,196,164
A_ v116,v44,v116
A_ v117,v45,v117
Mvav 84,192,144
S_ m0,s51,27648
A_ v118,v46,v118
A_ v119,v47,v119
Zvav 80,220,180
L_ v7,24,s55,0
Mvav 80,196,160
W_ 8
A_ v108,v40,v108
A_ v109,v41,v109
Mvav 80,192,140
S_ s55,s71,532480
A_ v110,v42,v110
A_ v111,v43,v111
Zvav 76,220,176
W_ 6
A_ v100,v36,v100
A_ v101,v37,v101
Mvav 76,196,156
S_ m0,s51,28672
A_ v102,v38,v102
A_ v103,v39,v103
Mvav 76,192,136
W_ 0
C_ 130,128,129
C_ 132,132,133
Zvav 16,180,136
S_ s85,s6,6
Ta 192,v3,0
Ta 194,v3,2048
Mvav 16,184,156
L_ v7,24,s55,0
Mvav 16,188,176
S_ s55,s71,542720
Ta 196,v3,4096
Ta 198,v3,6144
Zvav 20,180,140
S_ m0,s51,29696
C_ 133,134,135
v_min_u32 v12, 240, v12
Mvav 20,184,160
s_and_b32 s85, s85, 7
Ta 220,v3,8192
Ta 222,v3,10240
Mvav 20,188,180
buffer_store_dwordx4 v[130:133], v6, s[28:31], s73 offen offset:0 sc1
Zvav 24,180,144
s_sub_u32 s73, s73, 4096
v_lshlrev_b32 v12, 3, v12
s_lshl_b32 s85, s85, 7
Mvav 24,184,164
S_ s85,s85,s87
C_ 123,122,123
C_ 122,120,121
Mvav 24,188,184
C_ 124,124,125
C_ 125,126,127
Zvav 28,180,148
buffer_store_dwordx4 v[122:125], v6, s[28:31], s73 offen offset:3072 sc1
Mvav 28,184,168
C_ 115,114,115
C_ 114,112,113
Mvav 28,188,188
C_ 116,116,117
C_ 117,118,119
Zvav 32,180,152
Mvav 32,184,172
buffer_store_dwordx4 v[114:117], v6, s[28:31], s73 offen offset:2048 sc1
Mvav 32,188,192
s_waitcnt vmcnt(23) lgkmcnt(0)
s_barrier
C_ 107,106,107
Zvav 52,220,192
s_nop 0
Ta 180,%0,12288
Ta 182,%0,14336
Mvav 52,196,172
C_ 106,104,105
C_ 108,108,109
Mvav 52,192,152
s_nop 0
Ta 184,%0,16384
Ta 186,%0,18432
Zvav 48,220,188
L_ v7,24,s55,0
Mvav 48,196,168
S_ s55,s71,614400
Ta 188,%0,20480
Ta 190,%0,22528
Mvav 48,192,148
S_ m0,s51,0
C_ 109,110,111
Zvav 44,220,184
Mvav 44,196,164
buffer_store_dwordx4 v[106:109], v6, s[28:31], s73 offen offset:1024 sc1
Mvav 44,192,144
C_ 99,98,99
C_ 98,96,97
Zvav 40,220,180
C_ 100,100,101
C_ 101,102,103
Mvav 40,196,160
Mvav 40,192,140
buffer_store_dwordx4 v[98:101], v6, s[28:31], s73 offen offset:0 sc1
Zvav 36,220,176
Mvav 36,196,156
Mvav 36,192,136
W_ 0
Mvav 56,180,196
s_nop 0
Ta 192,v1,12288
Ta 194,v1,14336
Mvav 56,184,216
L_ v7,24,s55,0
Mvav 56,188,236
S_ s55,s71,624640
Ta 196,v1,16384
Ta 198,v1,18432
Mvav 60,180,200
S_ m0,s51,1024
Mvav 60,184,220
s_nop 0
Ta 220,v1,20480
Ta 222,v1,22528
Mvav 60,188,240
L_ v7,24,s55,0
Mvav 64,180,204
S_ s55,s71,634880
Mvav 64,184,224
S_ m0,s51,2048
Mvav 64,188,244
Mvav 68,180,208
L_ v7,24,s55,0
Mvav 68,184,228
S_ s55,s71,645120
Mvav 68,188,248
S_ m0,s51,3072
Mvav 72,180,212
Mvav 72,184,232
L_ v7,24,s55,0
Mvav 72,188,252
W_ 0
S_ s55,s71,655360
Mvav 92,220,252
S_ m0,s51,4096
Ta 180,v2,12288
Ta 182,v2,14336
Mvav 92,196,232
Mvav 92,192,212
s_nop 0
Ta 184,v2,16384
Ta 186,v2,18432
Mvav 88,220,248
L_ v7,24,s55,0
Mvav 88,196,228
S_ s55,s71,665600
Ta 188,v2,20480
Ta 190,v2,22528
Mvav 88,192,208
S_ m0,s51,5120
Mvav 84,220,244
Mvav 84,196,224
L_ v7,24,s55,0
Mvav 84,192,204
Mvav 80,220,240
Mvav 80,196,220
Mvav 80,192,200
buffer_load_dword a74, v12, s[44:47], s66 offen
Mvav 76,220,236
Mvav 76,196,216
Mvav 76,192,196
W_ 0
Mvav 16,180,196
s_nop 0
Ta 192,v3,12288
Ta 194,v3,14336
Mvav 16,184,216
buffer_load_dword a74, v4, s[24:27], s85 offen
Mvav 16,188,236
s_nop 0
Ta 196,v3,16384
Ta 198,v3,18432
Mvav 20,180,200
Mvav 20,184,220
s_nop 0
Ta 220,v3,20480
Ta 222,v3,22528
Mvav 20,188,240
Mvav 24,180,204
Mvav 24,184,224
Mvav 24,188,244
Mvav 28,180,208
Mvav 28,184,228
Mvav 28,188,248
Mvav 32,180,212
Mvav 32,184,232
Mvav 32,188,252
s_waitcnt vmcnt(26) lgkmcnt(0)
s_barrier
Mvav 52,220,252
S_ s55,s71,0
Ta 180,%0,49152
Ta 182,%0,51200
Mvav 52,196,232
S_ m0,s51,12288
Mvav 52,192,212
s_nop 0
Ta 184,%0,53248
Ta 186,%0,55296
Mvav 48,220,248
L_ v7,24,s55,0
Mvav 48,196,228
S_ s55,s71,10240
Ta 188,%0,57344
Ta 190,%0,59392
Mvav 48,192,208
S_ m0,s51,13312
Mvav 44,220,244
Mvav 44,196,224
L_ v7,24,s55,0
Mvav 44,192,204
S_ s55,s71,20480
Mvav 40,220,240
S_ m0,s51,14336
Mvav 40,196,220
Mvav 40,192,200
L_ v7,24,s55,0
Mvav 36,220,236
S_ s55,s71,30720
Mvav 36,196,216
S_ m0,s51,15360
Mvav 36,192,196
W_ 0
Mvaa 56,180,0
s_nop 0
Ta 192,v1,49152
Ta 194,v1,51200
Mvaa 56,184,40
L_ v7,24,s55,0
Mvaa 56,188,80
S_ s55,s71,40960
Ta 196,v1,53248
Ta 198,v1,55296
Mvaa 60,180,4
S_ m0,s51,16384
Mvaa 60,184,44
s_nop 0
Ta 220,v1,57344
Ta 222,v1,59392
Mvaa 60,188,84
L_ v7,24,s55,0
Mvaa 64,180,8
S_ s55,s71,51200
Mvaa 64,184,48
S_ m0,s51,17408
Mvaa 64,188,88
Mvaa 68,180,12
L_ v7,24,s55,0
Mvaa 68,184,52
Mvaa 68,188,240
Mvaa 72,180,16
Mvaa 72,184,56
Mvaa 72,188,244
W_ 0
Mvaa 92,220,244
s_nop 0
Ta 180,v2,49152
Ta 182,v2,51200
Mvaa 92,196,56
Mvaa 92,192,16
s_nop 0
Ta 184,v2,53248
Ta 186,v2,55296
Mvaa 88,220,240
Mvaa 88,196,52
s_nop 0
Ta 188,v2,57344
Ta 190,v2,59392
Mvaa 88,192,12
Mvaa 84,220,88
Mvaa 84,196,48
Mvaa 84,192,8
Mvaa 80,220,84
Mvaa 80,196,44
Mvaa 80,192,4
Mvaa 76,220,80
Mvaa 76,196,40
Mvaa 76,192,0
W_ 0
Mvaa 16,180,0
s_nop 0
Ta 192,v3,49152
Ta 194,v3,51200
Mvaa 16,184,40
Mvaa 16,188,80
s_nop 0
Ta 196,v3,53248
Ta 198,v3,55296
Mvaa 20,180,4
Mvaa 20,184,44
s_nop 0
Ta 220,v3,57344
Ta 222,v3,59392
Mvaa 20,188,84
Mvaa 24,180,8
Mvaa 24,184,48
Mvaa 24,188,88
Mvaa 28,180,12
Mvaa 28,184,52
Mvaa 28,188,240
Mvaa 32,180,16
Mvaa 32,184,56
Mvaa 32,188,244
s_waitcnt vmcnt(25) lgkmcnt(0)
s_barrier
Mvaa 52,220,244
s_nop 0
Ta 180,%0,36864
Ta 182,%0,38912
Mvaa 52,196,56
Mvaa 52,192,16
s_nop 0
Ta 184,%0,40960
Ta 186,%0,43008
Mvaa 48,220,240
Mvaa 48,196,52
s_nop 0
Ta 188,%0,45056
Ta 190,%0,47104
Mvaa 48,192,12
Mvaa 44,220,88
Mvaa 44,196,48
Mvaa 44,192,8
Mvaa 40,220,84
Mvaa 40,196,44
Mvaa 40,192,4
Mvaa 36,220,80
Mvaa 36,196,40
Mvaa 36,192,0
W_ 0
Mvaa 56,180,120
S_ s81,s6,3
Ta 192,v1,36864
Ta 194,v1,38912
Mvaa 56,184,160
s_and_b32 s81, s81, 7
Mvaa 56,188,200
s_mul_i32 s81, s81, 20480
Ta 196,v1,40960
Ta 198,v1,43008
Mvaa 60,180,124
s_mov_b32 m0, s52
S_ s73,s81,s78
Mvaa 60,184,164
S_ s73,s73,4096
Ta 220,v1,45056
Ta 222,v1,47104
Mvaa 60,188,204
s_nop 0
Y_ 56,0
Y_ 57,256
Mvaa 64,180,128
s_nop 0
Y_ 58,512
Y_ 59,768
Mvaa 64,184,168
Mvaa 64,188,208
s_nop 0
Y_ 60,1024
Y_ 61,1280
Mvaa 68,180,132
s_nop 0
Y_ 62,1536
Y_ 63,1792
Mvaa 68,184,172
Mvaa 68,188,212
s_nop 0
Y_ 64,2048
Y_ 65,2304
Mvaa 72,180,136
s_nop 0
Y_ 66,2560
Y_ 67,2816
Mvaa 72,184,176
Mvaa 72,188,216
W_ 12
Y_ 68,3072
Y_ 69,3328
Mvaa 92,220,216
s_nop 0
Ta 180,v2,36864
Ta 182,v2,38912
Mvaa 92,196,176
s_nop 0
Y_ 70,3584
Y_ 71,3840
Mvaa 92,192,136
s_nop 0
Ta 184,v2,40960
Ta 186,v2,43008
Mvaa 88,220,212
s_nop 0
Y_ 72,4096
Y_ 73,4352
Mvaa 88,196,172
S_ s83,s6,2
Ta 188,v2,45056
Ta 190,v2,47104
Mvaa 88,192,132
s_and_b32 s83, s83, 7
Y_ 74,4608
Y_ 75,4864
Mvaa 84,220,208
s_lshl_b32 s83, s83, 2
Y_ 92,9216
Y_ 93,9472
Mvaa 84,196,168
s_nop 0
Y_ 94,9728
Y_ 95,9984
Mvaa 84,192,128
s_nop 0
Y_ 88,8192
Y_ 89,8448
Mvaa 80,220,204
s_nop 0
Y_ 90,8704
Y_ 91,8960
Mvaa 80,196,164
v_cmp_eq_u32_e64 s[36:37], s83, v8
Mvaa 80,192,124
s_nop 0
Y_ 84,7168
Y_ 85,7424
Mvaa 76,220,200
s_nop 0
Y_ 86,7680
Y_ 87,7936
Mvaa 76,196,160
v_mov_b32 v10, 0x7fff0000
Mvaa 76,192,120
W_ 14
Y_ 80,6144
Y_ 81,6400
Mvaa 16,180,120
s_nop 0
Ta 192,v3,36864
Ta 194,v3,38912
Mvaa 16,184,160
s_nop 0
Y_ 82,6656
Y_ 83,6912
Mvaa 16,188,200
s_nop 0
Ta 196,v3,40960
Ta 198,v3,43008
Mvaa 20,180,124
s_nop 0
Y_ 76,5120
Y_ 77,5376
Mvaa 20,184,164
s_nop 0
Ta 220,v3,45056
Ta 222,v3,47104
Mvaa 20,188,204
s_nop 0
Y_ 78,5632
Y_ 79,5888
Mvaa 24,180,128
v_cndmask_b32_e64 v10, v10, v9, s[36:37]
Mvaa 24,184,168
Mvaa 24,188,208
s_waitcnt vmcnt(14) lgkmcnt(0)
s_barrier
Mvaa 28,180,132
buffer_store_dword a255, v10, s[92:95], 0 offen offset:384 sc1
Mvaa 28,184,172
s_sub_u32 s75, s6, 1
ds_read2st64_b32 v[96:97], v5 offset0:0 offset1:1
S_ s55,s71,122880
Mvaa 28,188,212
s_and_b32 s75, s75, 7
ds_read2st64_b32 v[98:99], v5 offset0:2 offset1:3
S_ m0,s51,49152
Mvaa 32,180,136
ds_read2st64_b32 v[104:105], v5 offset0:4 offset1:5
Mvaa 32,184,176
buffer_load_dword v11, v8, s[92:95], s96 offen offset:384 sc1
Mvaa 32,188,216
ds_read2st64_b32 v[106:107], v5 offset0:6 offset1:7
Mvaa 52,220,216
s_nop 0
Ta 180,%0,24576
Ta 182,%0,26624
Mvaa 52,196,176
ds_read2st64_b32 v[112:113], v5 offset0:8 offset1:9
Mvaa 52,192,136
W_ 5
Ta 184,%0,28672
Ta 186,%0,30720
Mvaa 48,220,212
L_ v7,24,s55,0
Mvaa 48,196,172
S_ s55,s71,133120
Ta 188,%0,32768
Ta 190,%0,34816
Mvaa 48,192,132
S_ m0,s51,50176
ds_read2st64_b32 v[114:115], v5 offset0:10 offset1:11
A_ v96,v16,v96
Mvaa 44,220,208
ds_read2st64_b32 v[120:121], v5 offset0:12 offset1:13
A_ v97,v17,v97
Mvaa 44,196,168
L_ v7,24,s55,0
Mvaa 44,192,128
S_ s55,s71,143360
ds_read2st64_b32 v[122:123], v5 offset0:14 offset1:15
A_ v98,v18,v98
Mvaa 40,220,204
S_ m0,s51,51200
ds_read2st64_b32 v[128:129], v5 offset0:16 offset1:17
A_ v99,v19,v99
Mvaa 40,196,164
W_ 11
ds_read2st64_b32 v[130:131], v5 offset0:18 offset1:19
A_ v104,v20,v104
Mvaa 40,192,124
L_ v7,24,s55,0
Mvaa 36,220,200
S_ s55,s71,153600
ds_read2st64_b32 v[132:133], v5 offset0:36 offset1:37
A_ v105,v21,v105
Mvaa 36,196,160
S_ m0,s51,52224
ds_read2st64_b32 v[134:135], v5 offset0:38 offset1:39
A_ v106,v22,v106
Mvaa 36,192,120
W_ 7
ds_read2st64_b32 v[124:125], v5 offset0:32 offset1:33
A_ v107,v23,v107
Zvav 56,180,136
W_ 7
Ta 192,v1,24576
Ta 194,v1,26624
Mvav 56,184,156
L_ v7,24,s55,0
Mvav 56,188,176
S_ s55,s71,163840
Ta 196,v1,28672
Ta 198,v1,30720
Zvav 60,180,140
S_ m0,s51,53248
ds_read2st64_b32 v[126:127], v5 offset0:34 offset1:35
A_ v112,v24,v112
Mvav 60,184,160
s_nop 0
Ta 220,v1,32768
Ta 222,v1,34816
Mvav 60,188,180
L_ v7,24,s55,0
Zvav 64,180,144
S_ s55,s71,174080
ds_read2st64_b32 v[116:117], v5 offset0:28 offset1:29
A_ v113,v25,v113
Mvav 64,184,164
S_ m0,s51,54272
ds_read2st64_b32 v[118:119], v5 offset0:30 offset1:31
A_ v114,v26,v114
Mvav 64,188,184
ds_read2st64_b32 v[108:109], v5 offset0:24 offset1:25
A_ v115,v27,v115
Zvav 68,180,148
L_ v7,24,s55,0
Mvav 68,184,168
W_ 14
ds_read2st64_b32 v[110:111], v5 offset0:26 offset1:27
A_ v120,v28,v120
Mvav 68,188,188
S_ s79,s6,5
ds_read2st64_b32 v[100:101], v5 offset0:20 offset1:21
A_ v121,v29,v121
Zvav 72,180,152
s_and_b32 s79, s79, 7
ds_read2st64_b32 v[102:103], v5 offset0:22 offset1:23
A_ v122,v30,v122
Mvav 72,184,172
s_lshl_b32 s79, s79, 7
A_ v123,v31,v123
Mvav 72,188,192
W_ 6
A_ v128,v32,v128
Zvav 92,220,192
S_ s71,s72,s79
Ta 180,v2,24576
Ta 182,v2,26624
Mvav 92,196,172
S_ s55,s71,491520
A_ v129,v33,v129
A_ v130,v34,v130
Mvav 92,192,152
S_ m0,s51,36864
Ta 184,v2,28672
Ta 186,v2,30720
Zvav 88,220,188
L_ v7,24,s55,0
Mvav 88,196,168
S_ s55,s71,501760
Ta 188,v2,32768
Ta 190,v2,34816
Mvav 88,192,148
S_ m0,s51,37888
A_ v131,v35,v131
S_ s85,s6,7
Zvav 84,220,184
A_ v132,v52,v132
A_ v133,v53,v133
Mvav 84,196,164
L_ v7,24,s55,0
Mvav 84,192,144
S_ s55,s71,512000
A_ v134,v54,v134
A_ v135,v55,v135
Zvav 80,220,180
A_ v124,v48,v124
A_ v125,v49,v125
Mvav 80,196,160
S_ m0,s51,38912
A_ v126,v50,v126
A_ v127,v51,v127
Mvav 80,192,140
L_ v7,24,s55,0
Zvav 76,220,176
W_ 10
A_ v116,v44,v116
A_ v117,v45,v117
Mvav 76,196,156
S_ s55,s71,522240
A_ v118,v46,v118
A_ v119,v47,v119
Mvav 76,192,136
W_ 0
A_ v108,v40,v108
Zvav 16,180,136
S_ m0,s51,39936
Ta 192,v3,24576
Ta 194,v3,26624
Mvav 16,184,156
L_ v7,24,s55,0
Mvav 16,188,176
S_ s55,s71,532480
Ta 196,v3,28672
Ta 198,v3,30720
Zvav 20,180,140
S_ m0,s51,40960
A_ v109,v41,v109
A_ v110,v42,v110
Mvav 20,184,160
s_and_b32 s85, s85, 7
Ta 220,v3,32768
Ta 222,v3,34816
Mvav 20,188,180
L_ v7,24,s55,0
Zvav 24,180,144
S_ s55,s71,542720
A_ v111,v43,v111
C_ 131,130,131
Mvav 24,184,164
A_ v100,v36,v100
A_ v101,v37,v101
Mvav 24,188,184
S_ m0,s51,41984
A_ v102,v38,v102
A_ v103,v39,v103
Zvav 28,180,148
L_ v7,24,s55,0
Mvav 28,184,168
s_lshl_b32 s85, s85, 7
C_ 130,128,129
C_ 132,132,133
Mvav 28,188,188
S_ s85,s85,s87
C_ 133,134,135
Zvav 32,180,152
Mvav 32,184,172
buffer_store_dwordx4 v[130:133], v6, s[28:31], s73 offen offset:0 sc1
Mvav 32,188,192
s_waitcnt vmcnt(23) lgkmcnt(0)
s_barrier
s_sub_u32 s73, s73, 4096
Zvav 52,220,192
S_ s55,s71,614400
Ta 180,%0,0
Ta 182,%0,2048
Mvav 52,196,172
S_ m0,s51,24576
C_ 123,122,123
C_ 122,120,121
Mvav 52,192,152
s_nop 0
Ta 184,%0,4096
Ta 186,%0,6144
Zvav 48,220,188
L_ v7,24,s55,0
Mvav 48,196,168
S_ s55,s71,624640
Ta 188,%0,8192
Ta 190,%0,10240
Mvav 48,192,148
S_ m0,s51,25600
C_ 124,124,125
C_ 125,126,127
Zvav 44,220,184
Mvav 44,196,164
buffer_store_dwordx4 v[122:125], v6, s[28:31], s73 offen offset:3072 sc1
Mvav 44,192,144
C_ 115,114,115
C_ 114,112,113
Zvav 40,220,180
C_ 116,116,117
C_ 117,118,119
Mvav 40,196,160
Mvav 40,192,140
buffer_store_dwordx4 v[114:117], v6, s[28:31], s73 offen offset:2048 sc1
Zvav 36,220,176
C_ 107,106,107
C_ 106,104,105
Mvav 36,196,156
C_ 108,108,109
C_ 109,110,111
Mvav 36,192,136
W_ 0
Mvav 56,180,196
s_nop 0
Ta 192,v1,0
Ta 194,v1,2048
Mvav 56,184,216
buffer_store_dwordx4 v[106:109], v6, s[28:31], s73 offen offset:1024 sc1
Mvav 56,188,236
s_nop 0
Ta 196,v1,4096
Ta 198,v1,6144
Mvav 60,180,200
C_ 99,98,99
C_ 98,96,97
Mvav 60,184,220
s_nop 0
Ta 220,v1,8192
Ta 222,v1,10240
Mvav 60,188,240
L_ v7,24,s55,0
Mvav 64,180,204
S_ s55,s71,634880
C_ 100,100,101
C_ 101,102,103
Mvav 64,184,224
S_ m0,s51,26624
Mvav 64,188,244
Mvav 68,180,208
buffer_store_dwordx4 v[98:101], v6, s[28:31], s73 offen offset:0 sc1
Mvav 68,184,228
Mvav 68,188,248
Mvav 72,180,212
Mvav 72,184,232
L_ v7,24,s55,0
Mvav 72,188,252
W_ 0
S_ s55,s71,645120
Mvav 92,220,252
S_ m0,s51,27648
Ta 180,v2,0
Ta 182,v2,2048
Mvav 92,196,232
Mvav 92,192,212
s_nop 0
Ta 184,v2,4096
Ta 186,v2,6144
Mvav 88,220,248
L_ v7,24,s55,0
Mvav 88,196,228
s_waitcnt vmcnt(21)
Ta 188,v2,8192
Ta 190,v2,10240
Mvav 88,192,208
S_ s55,s71,655360
v_cmp_eq_u32 vcc, s62, v11
Mvav 84,220,244
s_mul_i32 s76, s75, 204800
S_ m0,s51,28672
Mvav 84,196,224
L_ v7,24,s55,0
Mvav 84,192,204
s_bitcmp1_b32 vcc_lo, s75
s_cbranch_scc0 .Lspin0
.Lspun0:
S_ s55,s71,665600
Mvav 80,220,240
S_ s56,s76,s84
S_ m0,s51,29696
Mvav 80,196,220
Mvav 80,192,200
buffer_load_dwordx4 a[20:23], v6, s[28:31], s56 offen offset:0 sc1
Mvav 76,220,236
Mvav 76,196,216
Mvav 76,192,196
W_ 0
Mvav 16,180,196
s_nop 0
Ta 192,v3,0
Ta 194,v3,2048
Mvav 16,184,216
buffer_load_dwordx4 a[24:27], v6, s[28:31], s56 offen offset:1024 sc1
Mvav 16,188,236
s_nop 0
Ta 196,v3,4096
Ta 198,v3,6144
Mvav 20,180,200
Mvav 20,184,220
s_nop 0
Ta 220,v3,8192
Ta 222,v3,10240
Mvav 20,188,240
buffer_load_dwordx4 a[28:31], v6, s[28:31], s56 offen offset:2048 sc1
Mvav 24,180,204
Mvav 24,184,224
Mvav 24,188,244
Mvav 28,180,208
buffer_load_dwordx4 a[32:35], v6, s[28:31], s56 offen offset:3072 sc1
Mvav 28,184,228
S_ s56,s56,4096
Mvav 28,188,248
Mvav 32,180,212
Mvav 32,184,232
buffer_load_dwordx4 a[36:39], v6, s[28:31], s56 offen offset:0 sc1
Mvav 32,188,252
s_waitcnt vmcnt(29) lgkmcnt(0)
s_barrier
S_ s56,s76,s74
Mvav 52,220,252
s_nop 0
Ta 180,%0,12288
Ta 182,%0,14336
Mvav 52,196,232
Mvav 52,192,212
s_nop 0
Ta 184,%0,16384
Ta 186,%0,18432
Mvav 48,220,248
buffer_load_dwordx4 a[60:63], v6, s[28:31], s56 offen offset:0 sc1
Mvav 48,196,228
S_ s56,s76,s82
Ta 188,%0,20480
Ta 190,%0,22528
Mvav 48,192,208
Mvav 44,220,244
Mvav 44,196,224
buffer_load_dwordx4 a[64:67], v6, s[28:31], s56 offen offset:0 sc1
Mvav 44,192,204
Mvav 40,220,240
Mvav 40,196,220
Mvav 40,192,200
L_ v7,24,s55,0
Mvav 36,220,236
S_ s55,s71,0
Mvav 36,196,216
S_ m0,s51,0
Mvav 36,192,196
W_ 0
Mvaa 56,180,0
s_nop 0
Ta 192,v1,12288
Ta 194,v1,14336
Mvaa 56,184,40
L_ v7,24,s55,0
Mvaa 56,188,80
S_ s55,s71,10240
Ta 196,v1,16384
Ta 198,v1,18432
Mvaa 60,180,4
S_ m0,s51,1024
Mvaa 60,184,44
s_nop 0
Ta 220,v1,20480
Ta 222,v1,22528
Mvaa 60,188,84
L_ v7,24,s55,0
Mvaa 64,180,8
S_ s55,s71,20480
Mvaa 64,184,48
S_ m0,s51,2048
Mvaa 64,188,88
Mvaa 68,180,12
L_ v7,24,s55,0
Mvaa 68,184,52
S_ s55,s71,30720
Mvaa 68,188,240
S_ m0,s51,3072
Mvaa 72,180,16
Mvaa 72,184,56
L_ v7,24,s55,0
Mvaa 72,188,244
W_ 0
S_ s55,s71,40960
Mvaa 92,220,244
S_ m0,s51,4096
Ta 180,v2,12288
Ta 182,v2,14336
Mvaa 92,196,56
Mvaa 92,192,16
s_nop 0
Ta 184,v2,16384
Ta 186,v2,18432
Mvaa 88,220,240
L_ v7,24,s55,0
Mvaa 88,196,52
S_ s55,s71,51200
Ta 188,v2,20480
Ta 190,v2,22528
Mvaa 88,192,12
S_ m0,s51,5120
Mvaa 84,220,88
Mvaa 84,196,48
L_ v7,24,s55,0
Mvaa 84,192,8
Mvaa 80,220,84
Mvaa 80,196,44
Mvaa 80,192,4
buffer_load_dword a74, v4, s[24:27], s85 offen
Mvaa 76,220,80
Mvaa 76,196,40
Mvaa 76,192,0
W_ 0
Mvaa 16,180,0
s_nop 0
Ta 192,v3,12288
Ta 194,v3,14336
Mvaa 16,184,40
Mvaa 16,188,80
s_nop 0
Ta 196,v3,16384
Ta 198,v3,18432
Mvaa 20,180,4
Mvaa 20,184,44
s_nop 0
Ta 220,v3,20480
Ta 222,v3,22528
Mvaa 20,188,84
Mvaa 24,180,8
Mvaa 24,184,48
Mvaa 24,188,88
Mvaa 28,180,12
Mvaa 28,184,52
Mvaa 28,188,240
Mvaa 32,180,16
Mvaa 32,184,56
Mvaa 32,188,244
s_waitcnt vmcnt(31) lgkmcnt(0)
s_barrier
Mvaa 52,220,244
s_nop 0
Ta 180,%0,49152
Ta 182,%0,51200
Mvaa 52,196,56
Mvaa 52,192,16
s_nop 0
Ta 184,%0,53248
Ta 186,%0,55296
Mvaa 48,220,240
Mvaa 48,196,52
s_nop 0
Ta 188,%0,57344
Ta 190,%0,59392
Mvaa 48,192,12
Mvaa 44,220,88
Mvaa 44,196,48
Mvaa 44,192,8
Mvaa 40,220,84
Mvaa 40,196,44
Mvaa 40,192,4
Mvaa 36,220,80
Mvaa 36,196,40
Mvaa 36,192,0
W_ 0
Mvaa 56,180,120
S_ s81,s6,4
Ta 192,v1,49152
Ta 194,v1,51200
Mvaa 56,184,160
s_and_b32 s81, s81, 7
Mvaa 56,188,200
s_mul_i32 s81, s81, 20480
Ta 196,v1,53248
Ta 198,v1,55296
Mvaa 60,180,124
s_mov_b32 m0, s52
S_ s73,s81,s78
Mvaa 60,184,164
S_ s73,s73,4096
Ta 220,v1,57344
Ta 222,v1,59392
Mvaa 60,188,204
s_nop 0
Y_ 56,0
Y_ 57,256
Mvaa 64,180,128
s_nop 0
Y_ 58,512
Y_ 59,768
Mvaa 64,184,168
Mvaa 64,188,208
s_nop 0
Y_ 60,1024
Y_ 61,1280
Mvaa 68,180,132
s_nop 0
Y_ 62,1536
Y_ 63,1792
Mvaa 68,184,172
Mvaa 68,188,212
s_nop 0
Y_ 64,2048
Y_ 65,2304
Mvaa 72,180,136
s_nop 0
Y_ 66,2560
Y_ 67,2816
Mvaa 72,184,176
Mvaa 72,188,216
W_ 12
Y_ 68,3072
Y_ 69,3328
Mvaa 92,220,216
s_nop 0
Ta 180,v2,49152
Ta 182,v2,51200
Mvaa 92,196,176
s_nop 0
Y_ 70,3584
Y_ 71,3840
Mvaa 92,192,136
s_nop 0
Ta 184,v2,53248
Ta 186,v2,55296
Mvaa 88,220,212
s_nop 0
Y_ 72,4096
Y_ 73,4352
Mvaa 88,196,172
S_ s83,s6,3
Ta 188,v2,57344
Ta 190,v2,59392
Mvaa 88,192,132
s_and_b32 s83, s83, 7
Y_ 74,4608
Y_ 75,4864
Mvaa 84,220,208
s_lshl_b32 s83, s83, 2
Y_ 92,9216
Y_ 93,9472
Mvaa 84,196,168
s_nop 0
Y_ 94,9728
Y_ 95,9984
Mvaa 84,192,128
s_nop 0
Y_ 88,8192
Y_ 89,8448
Mvaa 80,220,204
s_nop 0
Y_ 90,8704
Y_ 91,8960
Mvaa 80,196,164
v_cmp_eq_u32_e64 s[36:37], s83, v8
Mvaa 80,192,124
s_nop 0
Y_ 84,7168
Y_ 85,7424
Mvaa 76,220,200
s_nop 0
Y_ 86,7680
Y_ 87,7936
Mvaa 76,196,160
v_mov_b32 v10, 0x7fff0000
Mvaa 76,192,120
W_ 14
Y_ 80,6144
Y_ 81,6400
Mvaa 16,180,120
s_nop 0
Ta 192,v3,49152
Ta 194,v3,51200
Mvaa 16,184,160
s_nop 0
Y_ 82,6656
Y_ 83,6912
Mvaa 16,188,200
s_nop 0
Ta 196,v3,53248
Ta 198,v3,55296
Mvaa 20,180,124
s_nop 0
Y_ 76,5120
Y_ 77,5376
Mvaa 20,184,164
s_nop 0
Ta 220,v3,57344
Ta 222,v3,59392
Mvaa 20,188,204
s_nop 0
Y_ 78,5632
Y_ 79,5888
Mvaa 24,180,128
v_cndmask_b32_e64 v10, v10, v9, s[36:37]
Mvaa 24,184,168
Mvaa 24,188,208
s_waitcnt vmcnt(18) lgkmcnt(0)
s_barrier
Mvaa 28,180,132
buffer_store_dword a255, v10, s[92:95], 0 offen offset:384 sc1
Mvaa 28,184,172
s_sub_u32 s75, s6, 2
ds_read2st64_b32 v[96:97], v5 offset0:0 offset1:1
S_ s55,s71,122880
Mvaa 28,188,212
s_and_b32 s75, s75, 7
ds_read2st64_b32 v[98:99], v5 offset0:2 offset1:3
S_ m0,s51,12288
Mvaa 32,180,136
ds_read2st64_b32 v[104:105], v5 offset0:4 offset1:5
Mvaa 32,184,176
buffer_load_dword v11, v8, s[92:95], s96 offen offset:384 sc1
Mvaa 32,188,216
ds_read2st64_b32 v[106:107], v5 offset0:6 offset1:7
Mvaa 52,220,216
s_nop 0
Ta 180,%0,36864
Ta 182,%0,38912
Mvaa 52,196,176
ds_read2st64_b32 v[112:113], v5 offset0:8 offset1:9
Mvaa 52,192,136
W_ 5
Ta 184,%0,40960
Ta 186,%0,43008
Mvaa 48,220,212
L_ v7,24,s55,0
Mvaa 48,196,172
S_ s55,s71,133120
Ta 188,%0,45056
Ta 190,%0,47104
Mvaa 48,192,132
S_ m0,s51,13312
ds_read2st64_b32 v[114:115], v5 offset0:10 offset1:11
A_ v96,v16,v96
Mvaa 44,220,208
ds_read2st64_b32 v[120:121], v5 offset0:12 offset1:13
A_ v97,v17,v97
Mvaa 44,196,168
L_ v7,24,s55,0
Mvaa 44,192,128
S_ s55,s71,143360
ds_read2st64_b32 v[122:123], v5 offset0:14 offset1:15
A_ v98,v18,v98
Mvaa 40,220,204
S_ m0,s51,14336
ds_read2st64_b32 v[128:129], v5 offset0:16 offset1:17
A_ v99,v19,v99
Mvaa 40,196,164
W_ 11
ds_read2st64_b32 v[130:131], v5 offset0:18 offset1:19
A_ v104,v20,v104
Mvaa 40,192,124
L_ v7,24,s55,0
Mvaa 36,220,200
S_ s55,s71,153600
ds_read2st64_b32 v[132:133], v5 offset0:36 offset1:37
A_ v105,v21,v105
Mvaa 36,196,160
S_ m0,s51,15360
ds_read2st64_b32 v[134:135], v5 offset0:38 offset1:39
A_ v106,v22,v106
Mvaa 36,192,120
ds_read2st64_b32 v[124:125], v5 offset0:32 offset1:33
A_ v107,v23,v107
s_waitcnt vmcnt(13)
Zaaa 100,70,20
L_ v7,24,s55,0
Zaaa 140,68,20
W_ 7
ds_read2st64_b32 v[126:127], v5 offset0:34 offset1:35
A_ v112,v24,v112
Zaaa 104,70,24
S_ s55,s71,163840
ds_read2st64_b32 v[116:117], v5 offset0:28 offset1:29
A_ v113,v25,v113
Zaaa 144,68,24
S_ m0,s51,16384
ds_read2st64_b32 v[118:119], v5 offset0:30 offset1:31
A_ v114,v26,v114
Zaaa 108,70,28
L_ v7,24,s55,0
Zaaa 148,68,28
S_ s55,s71,174080
ds_read2st64_b32 v[108:109], v5 offset0:24 offset1:25
A_ v115,v27,v115
Zaaa 112,70,32
W_ 9
ds_read2st64_b32 v[110:111], v5 offset0:26 offset1:27
A_ v120,v28,v120
Zaaa 152,68,32
S_ m0,s51,17408
ds_read2st64_b32 v[100:101], v5 offset0:20 offset1:21
A_ v121,v29,v121
Zaaa 116,70,36
L_ v7,24,s55,0
Zaaa 156,68,36
S_ s79,s6,6
ds_read2st64_b32 v[102:103], v5 offset0:22 offset1:23
A_ v122,v30,v122
Maaa 224,70,60
s_and_b32 s79, s79, 7
A_ v123,v31,v123
Maaa 228,68,60
W_ 10
A_ v128,v32,v128
A_ v129,v33,v129
Maaa 232,70,64
s_lshl_b32 s79, s79, 7
A_ v130,v34,v130
A_ v131,v35,v131
Maaa 236,68,64
W_ 9
A_ v132,v52,v132
W_ 8
Zvav 56,180,136
S_ s71,s72,s79
Ta 192,v1,36864
Ta 194,v1,38912
Mvav 56,184,156
S_ s55,s71,491520
A_ v133,v53,v133
A_ v134,v54,v134
Mvav 56,188,176
S_ m0,s51,49152
Ta 196,v1,40960
Ta 198,v1,43008
Zvav 60,180,140
L_ v7,24,s55,0
Mvav 60,184,160
S_ s55,s71,501760
Ta 220,v1,45056
Ta 222,v1,47104
Mvav 60,188,180
S_ m0,s51,50176
A_ v135,v55,v135
Zvav 64,180,144
W_ 12
A_ v124,v48,v124
A_ v125,v49,v125
Mvav 64,184,164
L_ v7,24,s55,0
Mvav 64,188,184
S_ s55,s71,512000
A_ v126,v50,v126
A_ v127,v51,v127
Zvav 68,180,148
W_ 10
A_ v116,v44,v116
A_ v117,v45,v117
Mvav 68,184,168
S_ m0,s51,51200
A_ v118,v46,v118
A_ v119,v47,v119
Mvav 68,188,188
L_ v7,24,s55,0
Zvav 72,180,152
W_ 8
A_ v108,v40,v108
A_ v109,v41,v109
Mvav 72,184,172
S_ s55,s71,522240
A_ v110,v42,v110
A_ v111,v43,v111
Mvav 72,188,192
W_ 0
A_ v100,v36,v100
Zvav 92,220,192
S_ m0,s51,52224
Ta 180,v2,36864
Ta 182,v2,38912
Mvav 92,196,172
L_ v7,24,s55,0
Mvav 92,192,152
S_ s55,s71,532480
Ta 184,v2,40960
Ta 186,v2,43008
Zvav 88,220,188
S_ m0,s51,53248
A_ v101,v37,v101
A_ v102,v38,v102
Mvav 88,196,168
s_nop 0
Ta 188,v2,45056
Ta 190,v2,47104
Mvav 88,192,148
L_ v7,24,s55,0
Zvav 84,220,184
S_ s55,s71,542720
A_ v103,v39,v103
C_ 131,130,131
Mvav 84,196,164
S_ m0,s51,54272
C_ 130,128,129
C_ 132,132,133
Mvav 84,192,144
S_ s85,s6,8
C_ 133,134,135
Zvav 80,220,180
buffer_store_dwordx4 v[130:133], v6, s[28:31], s73 offen offset:0 sc1
Mvav 80,196,160
s_sub_u32 s73, s73, 4096
s_and_b32 s85, s85, 7
Mvav 80,192,140
s_lshl_b32 s85, s85, 7
C_ 123,122,123
C_ 122,120,121
Zvav 76,220,176
S_ s85,s85,s87
C_ 124,124,125
C_ 125,126,127
Mvav 76,196,156
buffer_store_dwordx4 v[122:125], v6, s[28:31], s73 offen offset:3072 sc1
Mvav 76,192,136
W_ 0
C_ 115,114,115
C_ 114,112,113
Zvav 16,180,136
s_nop 0
Ta 192,v3,36864
Ta 194,v3,38912
Mvav 16,184,156
C_ 116,116,117
C_ 117,118,119
Mvav 16,188,176
s_nop 0
Ta 196,v3,40960
Ta 198,v3,43008
Zvav 20,180,140
buffer_store_dwordx4 v[114:117], v6, s[28:31], s73 offen offset:2048 sc1
Mvav 20,184,160
s_nop 0
Ta 220,v3,45056
Ta 222,v3,47104
Mvav 20,188,180
C_ 107,106,107
C_ 106,104,105
Zvav 24,180,144
C_ 108,108,109
C_ 109,110,111
Mvav 24,184,164
buffer_store_dwordx4 v[106:109], v6, s[28:31], s73 offen offset:1024 sc1
Mvav 24,188,184
C_ 99,98,99
C_ 98,96,97
Zvav 28,180,148
C_ 100,100,101
C_ 101,102,103
Mvav 28,184,168
Mvav 28,188,188
buffer_store_dwordx4 v[98:101], v6, s[28:31], s73 offen offset:0 sc1
Zvav 32,180,152
Mvav 32,184,172
Mvav 32,188,192
s_waitcnt vmcnt(25) lgkmcnt(0)
s_barrier
Zvav 52,220,192
s_nop 0
Ta 180,%0,24576
Ta 182,%0,26624
Mvav 52,196,172
L_ v7,24,s55,0
Mvav 52,192,152
S_ s55,s71,614400
Ta 184,%0,28672
Ta 186,%0,30720
Zvav 48,220,188
S_ m0,s51,36864
Mvav 48,196,168
s_nop 0
Ta 188,%0,32768
Ta 190,%0,34816
Mvav 48,192,148
L_ v7,24,s55,0
Zvav 44,220,184
S_ s55,s71,624640
Mvav 44,196,164
S_ m0,s51,37888
Mvav 44,192,144
Zvav 40,220,180
L_ v7,24,s55,0
Mvav 40,196,160
S_ s55,s71,634880
Mvav 40,192,140
S_ m0,s51,38912
Zvav 36,220,176
Mvav 36,196,156
L_ v7,24,s55,0
Mvav 36,192,136
W_ 0
S_ s55,s71,645120
Mvav 56,180,196
S_ m0,s51,39936
Ta 192,v1,24576
Ta 194,v1,26624
Mvav 56,184,216
Mvav 56,188,236
s_nop 0
Ta 196,v1,28672
Ta 198,v1,30720
Mvav 60,180,200
L_ v7,24,s55,0
Mvav 60,184,220
S_ s55,s71,655360
Ta 220,v1,32768
Ta 222,v1,34816
Mvav 60,188,240
S_ m0,s51,40960
Mvav 64,180,204
Mvav 64,184,224
L_ v7,24,s55,0
Mvav 64,188,244
S_ s55,s71,665600
Mvav 68,180,208
S_ m0,s51,41984
Mvav 68,184,228
Mvav 68,188,248
L_ v7,24,s55,0
Mvav 72,180,212
Mvav 72,184,232
Mvav 72,188,252
W_ 0
Mvav 92,220,252
s_nop 0
Ta 180,v2,24576
Ta 182,v2,26624
Mvav 92,196,232
buffer_load_dword a74, v4, s[24:27], s85 offen
Mvav 92,192,212
s_nop 0
Ta 184,v2,28672
Ta 186,v2,30720
Mvav 88,220,248
s_waitcnt vmcnt(24)
Mvav 88,196,228
s_nop 0
Ta 188,v2,32768
Ta 190,v2,34816
Mvav 88,192,208
v_cmp_eq_u32 vcc, s62, v11
Mvav 84,220,244
s_mul_i32 s76, s75, 204800
Mvav 84,196,224
s_bitcmp1_b32 vcc_lo, s75
s_cbranch_scc0 .Lspin1
.Lspun1:
Mvav 84,192,204
S_ s56,s76,s84
Mvav 80,220,240
buffer_load_dwordx4 a[20:23], v6, s[28:31], s56 offen offset:0 sc1
Mvav 80,196,220
Mvav 80,192,200
Mvav 76,220,236
Mvav 76,196,216
buffer_load_dwordx4 a[24:27], v6, s[28:31], s56 offen offset:1024 sc1
Mvav 76,192,196
W_ 0
Mvav 16,180,196
s_nop 0
Ta 192,v3,24576
Ta 194,v3,26624
Mvav 16,184,216
Mvav 16,188,236
s_nop 0
Ta 196,v3,28672
Ta 198,v3,30720
Mvav 20,180,200
buffer_load_dwordx4 a[28:31], v6, s[28:31], s56 offen offset:2048 sc1
Mvav 20,184,220
s_nop 0
Ta 220,v3,32768
Ta 222,v3,34816
Mvav 20,188,240
Mvav 24,180,204
Mvav 24,184,224
buffer_load_dwordx4 a[32:35], v6, s[28:31], s56 offen offset:3072 sc1
Mvav 24,188,244
S_ s56,s56,4096
Mvav 28,180,208
Mvav 28,184,228
Mvav 28,188,248
buffer_load_dwordx4 a[36:39], v6, s[28:31], s56 offen offset:0 sc1
Mvav 32,180,212
S_ s56,s76,s74
Mvav 32,184,232
Mvav 32,188,252
s_waitcnt vmcnt(32) lgkmcnt(0)
s_barrier
Mvav 52,220,252
S_ s55,s71,0
Ta 180,%0,0
Ta 182,%0,2048
Mvav 52,196,232
buffer_load_dwordx4 a[60:63], v6, s[28:31], s56 offen offset:0 sc1
Mvav 52,192,212
S_ s56,s76,s82
Ta 184,%0,4096
Ta 186,%0,6144
Mvav 48,220,248
S_ m0,s51,24576
Mvav 48,196,228
s_nop 0
Ta 188,%0,8192
Ta 190,%0,10240
Mvav 48,192,208
buffer_load_dwordx4 a[64:67], v6, s[28:31], s56 offen offset:0 sc1
Mvav 44,220,244
Mvav 44,196,224
Mvav 44,192,204
Mvav 40,220,240
L_ v7,24,s55,0
Mvav 40,196,220
S_ s55,s71,10240
Mvav 40,192,200
S_ m0,s51,25600
Mvav 36,220,236
Mvav 36,196,216
L_ v7,24,s55,0
Mvav 36,192,196
W_ 0
S_ s55,s71,20480
Mvaa 56,180,0
S_ m0,s51,26624
Ta 192,v1,0
Ta 194,v1,2048
Mvaa 56,184,40
Mvaa 56,188,80
s_nop 0
Ta 196,v1,4096
Ta 198,v1,6144
Mvaa 60,180,4
L_ v7,24,s55,0
Mvaa 60,184,44
S_ s55,s71,30720
Ta 220,v1,8192
Ta 222,v1,10240
Mvaa 60,188,84
S_ m0,s51,27648
Mvaa 64,180,8
Mvaa 64,184,48
L_ v7,24,s55,0
Mvaa 64,188,88
S_ s55,s71,40960
Mvaa 68,180,12
S_ m0,s51,28672
Mvaa 68,184,52
Mvaa 68,188,240
L_ v7,24,s55,0
Mvaa 72,180,16
S_ s55,s71,51200
Mvaa 72,184,56
S_ m0,s51,29696
Mvaa 72,188,244
W_ 0
Mvaa 92,220,244
s_nop 0
Ta 180,v2,0
Ta 182,v2,2048
Mvaa 92,196,56
L_ v7,24,s55,0
Mvaa 92,192,16
s_nop 0
Ta 184,v2,4096
Ta 186,v2,6144
Mvaa 88,220,240
Mvaa 88,196,52
s_nop 0
Ta 188,v2,8192
Ta 190,v2,10240
Mvaa 88,192,12
Mvaa 84,220,88
Mvaa 84,196,48
Mvaa 84,192,8
Mvaa 80,220,84
Mvaa 80,196,44
Mvaa 80,192,4
Mvaa 76,220,80
Mvaa 76,196,40
Mvaa 76,192,0
W_ 0
Mvaa 16,180,0
s_nop 0
Ta 192,v3,0
Ta 194,v3,2048
Mvaa 16,184,40
Mvaa 16,188,80
s_nop 0
Ta 196,v3,4096
Ta 198,v3,6144
Mvaa 20,180,4
Mvaa 20,184,44
s_nop 0
Ta 220,v3,8192
Ta 222,v3,10240
Mvaa 20,188,84
Mvaa 24,180,8
Mvaa 24,184,48
Mvaa 24,188,88
Mvaa 28,180,12
Mvaa 28,184,52
Mvaa 28,188,240
Mvaa 32,180,16
Mvaa 32,184,56
Mvaa 32,188,244
s_waitcnt vmcnt(31) lgkmcnt(0)
s_barrier
Mvaa 52,220,244
s_nop 0
Ta 180,%0,12288
Ta 182,%0,14336
Mvaa 52,196,56
Mvaa 52,192,16
s_nop 0
Ta 184,%0,16384
Ta 186,%0,18432
Mvaa 48,220,240
Mvaa 48,196,52
s_nop 0
Ta 188,%0,20480
Ta 190,%0,22528
Mvaa 48,192,12
Mvaa 44,220,88
Mvaa 44,196,48
Mvaa 44,192,8
Mvaa 40,220,84
Mvaa 40,196,44
Mvaa 40,192,4
Mvaa 36,220,80
Mvaa 36,196,40
Mvaa 36,192,0
W_ 0
Mvaa 56,180,120
S_ s81,s6,5
Ta 192,v1,12288
Ta 194,v1,14336
Mvaa 56,184,160
s_and_b32 s81, s81, 7
Mvaa 56,188,200
s_mul_i32 s81, s81, 20480
Ta 196,v1,16384
Ta 198,v1,18432
Mvaa 60,180,124
s_mov_b32 m0, s52
S_ s73,s81,s78
Mvaa 60,184,164
S_ s73,s73,4096
Ta 220,v1,20480
Ta 222,v1,22528
Mvaa 60,188,204
s_nop 0
Y_ 56,0
Y_ 57,256
Mvaa 64,180,128
s_nop 0
Y_ 58,512
Y_ 59,768
Mvaa 64,184,168
Mvaa 64,188,208
s_nop 0
Y_ 60,1024
Y_ 61,1280
Mvaa 68,180,132
s_nop 0
Y_ 62,1536
Y_ 63,1792
Mvaa 68,184,172
Mvaa 68,188,212
s_nop 0
Y_ 64,2048
Y_ 65,2304
Mvaa 72,180,136
s_nop 0
Y_ 66,2560
Y_ 67,2816
Mvaa 72,184,176
Mvaa 72,188,216
W_ 12
Y_ 68,3072
Y_ 69,3328
Mvaa 92,220,216
s_nop 0
Ta 180,v2,12288
Ta 182,v2,14336
Mvaa 92,196,176
s_nop 0
Y_ 70,3584
Y_ 71,3840
Mvaa 92,192,136
s_nop 0
Ta 184,v2,16384
Ta 186,v2,18432
Mvaa 88,220,212
s_nop 0
Y_ 72,4096
Y_ 73,4352
Mvaa 88,196,172
S_ s83,s6,4
Ta 188,v2,20480
Ta 190,v2,22528
Mvaa 88,192,132
s_and_b32 s83, s83, 7
Y_ 74,4608
Y_ 75,4864
Mvaa 84,220,208
s_lshl_b32 s83, s83, 2
Y_ 92,9216
Y_ 93,9472
Mvaa 84,196,168
s_nop 0
Y_ 94,9728
Y_ 95,9984
Mvaa 84,192,128
s_nop 0
Y_ 88,8192
Y_ 89,8448
Mvaa 80,220,204
s_nop 0
Y_ 90,8704
Y_ 91,8960
Mvaa 80,196,164
v_cmp_eq_u32_e64 s[36:37], s83, v8
Mvaa 80,192,124
s_nop 0
Y_ 84,7168
Y_ 85,7424
Mvaa 76,220,200
s_nop 0
Y_ 86,7680
Y_ 87,7936
Mvaa 76,196,160
v_mov_b32 v10, 0x7fff0000
Mvaa 76,192,120
W_ 14
Y_ 80,6144
Y_ 81,6400
Mvaa 16,180,120
s_nop 0
Ta 192,v3,12288
Ta 194,v3,14336
Mvaa 16,184,160
s_nop 0
Y_ 82,6656
Y_ 83,6912
Mvaa 16,188,200
s_nop 0
Ta 196,v3,16384
Ta 198,v3,18432
Mvaa 20,180,124
s_nop 0
Y_ 76,5120
Y_ 77,5376
Mvaa 20,184,164
s_nop 0
Ta 220,v3,20480
Ta 222,v3,22528
Mvaa 20,188,204
s_nop 0
Y_ 78,5632
Y_ 79,5888
Mvaa 24,180,128
v_cndmask_b32_e64 v10, v10, v9, s[36:37]
Mvaa 24,184,168
Mvaa 24,188,208
s_waitcnt vmcnt(20) lgkmcnt(0)
s_barrier
Mvaa 28,180,132
buffer_store_dword a255, v10, s[92:95], 0 offen offset:384 sc1
Mvaa 28,184,172
s_sub_u32 s75, s6, 3
ds_read2st64_b32 v[96:97], v5 offset0:0 offset1:1
S_ s55,s71,122880
Mvaa 28,188,212
s_and_b32 s75, s75, 7
ds_read2st64_b32 v[98:99], v5 offset0:2 offset1:3
S_ m0,s51,0
Mvaa 32,180,136
ds_read2st64_b32 v[104:105], v5 offset0:4 offset1:5
Mvaa 32,184,176
buffer_load_dword v11, v8, s[92:95], s96 offen offset:384 sc1
Mvaa 32,188,216
ds_read2st64_b32 v[106:107], v5 offset0:6 offset1:7
Mvaa 52,220,216
s_nop 0
Ta 180,%0,49152
Ta 182,%0,51200
Mvaa 52,196,176
ds_read2st64_b32 v[112:113], v5 offset0:8 offset1:9
Mvaa 52,192,136
W_ 5
Ta 184,%0,53248
Ta 186,%0,55296
Mvaa 48,220,212
L_ v7,24,s55,0
Mvaa 48,196,172
S_ s55,s71,133120
Ta 188,%0,57344
Ta 190,%0,59392
Mvaa 48,192,132
S_ m0,s51,1024
ds_read2st64_b32 v[114:115], v5 offset0:10 offset1:11
A_ v96,v16,v96
Mvaa 44,220,208
ds_read2st64_b32 v[120:121], v5 offset0:12 offset1:13
A_ v97,v17,v97
Mvaa 44,196,168
L_ v7,24,s55,0
Mvaa 44,192,128
S_ s55,s71,143360
ds_read2st64_b32 v[122:123], v5 offset0:14 offset1:15
A_ v98,v18,v98
Mvaa 40,220,204
S_ m0,s51,2048
ds_read2st64_b32 v[128:129], v5 offset0:16 offset1:17
A_ v99,v19,v99
Mvaa 40,196,164
W_ 11
ds_read2st64_b32 v[130:131], v5 offset0:18 offset1:19
A_ v104,v20,v104
Mvaa 40,192,124
L_ v7,24,s55,0
Mvaa 36,220,200
S_ s55,s71,153600
ds_read2st64_b32 v[132:133], v5 offset0:36 offset1:37
A_ v105,v21,v105
Mvaa 36,196,160
S_ m0,s51,3072
ds_read2st64_b32 v[134:135], v5 offset0:38 offset1:39
A_ v106,v22,v106
)A"
R"A(Mvaa 36,192,120
ds_read2st64_b32 v[124:125], v5 offset0:32 offset1:33
A_ v107,v23,v107
s_waitcnt vmcnt(11)
Maaa 100,70,20
L_ v7,24,s55,0
Maaa 140,68,20
W_ 7
ds_read2st64_b32 v[126:127], v5 offset0:34 offset1:35
A_ v112,v24,v112
Maaa 104,70,24
S_ s55,s71,163840
ds_read2st64_b32 v[116:117], v5 offset0:28 offset1:29
A_ v113,v25,v113
Maaa 144,68,24
S_ m0,s51,4096
ds_read2st64_b32 v[118:119], v5 offset0:30 offset1:31
A_ v114,v26,v114
Maaa 108,70,28
L_ v7,24,s55,0
Maaa 148,68,28
S_ s55,s71,174080
ds_read2st64_b32 v[108:109], v5 offset0:24 offset1:25
A_ v115,v27,v115
Maaa 112,70,32
W_ 9
ds_read2st64_b32 v[110:111], v5 offset0:26 offset1:27
A_ v120,v28,v120
Maaa 152,68,32
S_ m0,s51,5120
ds_read2st64_b32 v[100:101], v5 offset0:20 offset1:21
A_ v121,v29,v121
Maaa 116,70,36
L_ v7,24,s55,0
Maaa 156,68,36
S_ s79,s6,7
ds_read2st64_b32 v[102:103], v5 offset0:22 offset1:23
A_ v122,v30,v122
Maaa 224,70,60
s_and_b32 s79, s79, 7
A_ v123,v31,v123
Maaa 228,68,60
W_ 10
A_ v128,v32,v128
A_ v129,v33,v129
Maaa 232,70,64
s_lshl_b32 s79, s79, 7
A_ v130,v34,v130
A_ v131,v35,v131
Maaa 236,68,64
W_ 9
A_ v132,v52,v132
W_ 8
Zvav 56,180,136
S_ s71,s72,s79
Ta 192,v1,49152
Ta 194,v1,51200
Mvav 56,184,156
S_ s55,s71,491520
A_ v133,v53,v133
A_ v134,v54,v134
Mvav 56,188,176
S_ m0,s51,12288
Ta 196,v1,53248
Ta 198,v1,55296
Zvav 60,180,140
L_ v7,24,s55,0
Mvav 60,184,160
S_ s55,s71,501760
Ta 220,v1,57344
Ta 222,v1,59392
Mvav 60,188,180
S_ m0,s51,13312
A_ v135,v55,v135
Zvav 64,180,144
W_ 12
A_ v124,v48,v124
A_ v125,v49,v125
Mvav 64,184,164
L_ v7,24,s55,0
Mvav 64,188,184
S_ s55,s71,512000
A_ v126,v50,v126
A_ v127,v51,v127
Zvav 68,180,148
W_ 10
A_ v116,v44,v116
A_ v117,v45,v117
Mvav 68,184,168
S_ m0,s51,14336
A_ v118,v46,v118
A_ v119,v47,v119
Mvav 68,188,188
L_ v7,24,s55,0
Zvav 72,180,152
W_ 8
A_ v108,v40,v108
A_ v109,v41,v109
Mvav 72,184,172
S_ s55,s71,522240
A_ v110,v42,v110
A_ v111,v43,v111
Mvav 72,188,192
W_ 0
A_ v100,v36,v100
Zvav 92,220,192
S_ m0,s51,15360
Ta 180,v2,49152
Ta 182,v2,51200
Mvav 92,196,172
L_ v7,24,s55,0
Mvav 92,192,152
S_ s55,s71,532480
Ta 184,v2,53248
Ta 186,v2,55296
Zvav 88,220,188
S_ m0,s51,16384
A_ v101,v37,v101
A_ v102,v38,v102
Mvav 88,196,168
s_nop 0
Ta 188,v2,57344
Ta 190,v2,59392
Mvav 88,192,148
L_ v7,24,s55,0
Zvav 84,220,184
S_ s55,s71,542720
A_ v103,v39,v103
C_ 131,130,131
Mvav 84,196,164
S_ m0,s51,17408
C_ 130,128,129
C_ 132,132,133
Mvav 84,192,144
C_ 133,134,135
Zvav 80,220,180
buffer_store_dwordx4 v[130:133], v6, s[28:31], s73 offen offset:0 sc1
Mvav 80,196,160
s_sub_u32 s73, s73, 4096
Mvav 80,192,140
C_ 123,122,123
C_ 122,120,121
Zvav 76,220,176
C_ 124,124,125
C_ 125,126,127
Mvav 76,196,156
buffer_store_dwordx4 v[122:125], v6, s[28:31], s73 offen offset:3072 sc1
Mvav 76,192,136
W_ 0
C_ 115,114,115
C_ 114,112,113
Zvav 16,180,136
s_nop 0
Ta 192,v3,49152
Ta 194,v3,51200
Mvav 16,184,156
C_ 116,116,117
C_ 117,118,119
Mvav 16,188,176
s_nop 0
Ta 196,v3,53248
Ta 198,v3,55296
Zvav 20,180,140
buffer_store_dwordx4 v[114:117], v6, s[28:31], s73 offen offset:2048 sc1
Mvav 20,184,160
s_nop 0
Ta 220,v3,57344
Ta 222,v3,59392
Mvav 20,188,180
C_ 107,106,107
C_ 106,104,105
Zvav 24,180,144
C_ 108,108,109
C_ 109,110,111
Mvav 24,184,164
buffer_store_dwordx4 v[106:109], v6, s[28:31], s73 offen offset:1024 sc1
Mvav 24,188,184
C_ 99,98,99
C_ 98,96,97
Zvav 28,180,148
C_ 100,100,101
C_ 101,102,103
Mvav 28,184,168
Mvav 28,188,188
buffer_store_dwordx4 v[98:101], v6, s[28:31], s73 offen offset:0 sc1
Zvav 32,180,152
Mvav 32,184,172
Mvav 32,188,192
s_waitcnt vmcnt(32) lgkmcnt(0)
s_barrier
Zvav 52,220,192
s_nop 0
Ta 180,%0,36864
Ta 182,%0,38912
Mvav 52,196,172
L_ v7,24,s55,0
Mvav 52,192,152
S_ s55,s71,614400
Ta 184,%0,40960
Ta 186,%0,43008
Zvav 48,220,188
S_ m0,s51,49152
Mvav 48,196,168
s_nop 0
Ta 188,%0,45056
Ta 190,%0,47104
Mvav 48,192,148
L_ v7,24,s55,0
Zvav 44,220,184
S_ s55,s71,624640
Mvav 44,196,164
S_ m0,s51,50176
Mvav 44,192,144
Zvav 40,220,180
L_ v7,24,s55,0
Mvav 40,196,160
S_ s55,s71,634880
Mvav 40,192,140
S_ m0,s51,51200
Zvav 36,220,176
Mvav 36,196,156
L_ v7,24,s55,0
Mvav 36,192,136
W_ 0
S_ s55,s71,645120
Mvav 56,180,196
S_ m0,s51,52224
Ta 192,v1,36864
Ta 194,v1,38912
Mvav 56,184,216
Mvav 56,188,236
s_nop 0
Ta 196,v1,40960
Ta 198,v1,43008
Mvav 60,180,200
L_ v7,24,s55,0
Mvav 60,184,220
S_ s55,s71,655360
Ta 220,v1,45056
Ta 222,v1,47104
Mvav 60,188,240
S_ m0,s51,53248
Mvav 64,180,204
Mvav 64,184,224
L_ v7,24,s55,0
Mvav 64,188,244
S_ s55,s71,665600
Mvav 68,180,208
S_ m0,s51,54272
Mvav 68,184,228
Mvav 68,188,248
L_ v7,24,s55,0
Mvav 72,180,212
Mvav 72,184,232
Mvav 72,188,252
W_ 0
Mvav 92,220,252
s_nop 0
Ta 180,v2,36864
Ta 182,v2,38912
Mvav 92,196,232
Mvav 92,192,212
s_nop 0
Ta 184,v2,40960
Ta 186,v2,43008
Mvav 88,220,248
s_waitcnt vmcnt(23)
Mvav 88,196,228
s_nop 0
Ta 188,v2,45056
Ta 190,v2,47104
Mvav 88,192,208
v_cmp_eq_u32 vcc, s62, v11
Mvav 84,220,244
s_mul_i32 s76, s75, 204800
Mvav 84,196,224
s_bitcmp1_b32 vcc_lo, s75
s_cbranch_scc0 .Lspin2
.Lspun2:
Mvav 84,192,204
S_ s56,s76,s84
Mvav 80,220,240
buffer_load_dwordx4 a[20:23], v6, s[28:31], s56 offen offset:0 sc1
Mvav 80,196,220
Mvav 80,192,200
Mvav 76,220,236
Mvav 76,196,216
buffer_load_dwordx4 a[24:27], v6, s[28:31], s56 offen offset:1024 sc1
Mvav 76,192,196
W_ 0
Mvav 16,180,196
s_nop 0
Ta 192,v3,36864
Ta 194,v3,38912
Mvav 16,184,216
Mvav 16,188,236
s_nop 0
Ta 196,v3,40960
Ta 198,v3,43008
Mvav 20,180,200
buffer_load_dwordx4 a[28:31], v6, s[28:31], s56 offen offset:2048 sc1
Mvav 20,184,220
s_nop 0
Ta 220,v3,45056
Ta 222,v3,47104
Mvav 20,188,240
Mvav 24,180,204
Mvav 24,184,224
buffer_load_dwordx4 a[32:35], v6, s[28:31], s56 offen offset:3072 sc1
Mvav 24,188,244
S_ s56,s56,4096
Mvav 28,180,208
Mvav 28,184,228
Mvav 28,188,248
buffer_load_dwordx4 a[36:39], v6, s[28:31], s56 offen offset:0 sc1
Mvav 32,180,212
S_ s56,s76,s74
Mvav 32,184,232
Mvav 32,188,252
s_waitcnt vmcnt(30) lgkmcnt(0)
s_barrier
Mvav 52,220,252
S_ s55,s71,0
Ta 180,%0,24576
Ta 182,%0,26624
Mvav 52,196,232
buffer_load_dwordx4 a[60:63], v6, s[28:31], s56 offen offset:0 sc1
Mvav 52,192,212
S_ s56,s76,s82
Ta 184,%0,28672
Ta 186,%0,30720
Mvav 48,220,248
S_ m0,s51,36864
Mvav 48,196,228
s_nop 0
Ta 188,%0,32768
Ta 190,%0,34816
Mvav 48,192,208
buffer_load_dwordx4 a[64:67], v6, s[28:31], s56 offen offset:0 sc1
Mvav 44,220,244
Mvav 44,196,224
Mvav 44,192,204
Mvav 40,220,240
L_ v7,24,s55,0
Mvav 40,196,220
S_ s55,s71,10240
Mvav 40,192,200
S_ m0,s51,37888
Mvav 36,220,236
Mvav 36,196,216
L_ v7,24,s55,0
Mvav 36,192,196
W_ 0
S_ s55,s71,20480
Mvaa 56,180,0
S_ m0,s51,38912
Ta 192,v1,24576
Ta 194,v1,26624
Mvaa 56,184,40
Mvaa 56,188,80
s_nop 0
Ta 196,v1,28672
Ta 198,v1,30720
Mvaa 60,180,4
L_ v7,24,s55,0
Mvaa 60,184,44
S_ s55,s71,30720
Ta 220,v1,32768
Ta 222,v1,34816
Mvaa 60,188,84
S_ m0,s51,39936
Mvaa 64,180,8
Mvaa 64,184,48
L_ v7,24,s55,0
Mvaa 64,188,88
S_ s55,s71,40960
Mvaa 68,180,12
S_ m0,s51,40960
Mvaa 68,184,52
Mvaa 68,188,240
L_ v7,24,s55,0
Mvaa 72,180,16
S_ s55,s71,51200
Mvaa 72,184,56
S_ m0,s51,41984
Mvaa 72,188,244
W_ 0
Mvaa 92,220,244
s_nop 0
Ta 180,v2,24576
Ta 182,v2,26624
Mvaa 92,196,56
L_ v7,24,s55,0
Mvaa 92,192,16
s_nop 0
Ta 184,v2,28672
Ta 186,v2,30720
Mvaa 88,220,240
Mvaa 88,196,52
s_nop 0
Ta 188,v2,32768
Ta 190,v2,34816
Mvaa 88,192,12
Mvaa 84,220,88
Mvaa 84,196,48
Mvaa 84,192,8
Mvaa 80,220,84
Mvaa 80,196,44
Mvaa 80,192,4
Mvaa 76,220,80
Mvaa 76,196,40
Mvaa 76,192,0
W_ 0
Mvaa 16,180,0
s_nop 0
Ta 192,v3,24576
Ta 194,v3,26624
Mvaa 16,184,40
Mvaa 16,188,80
s_nop 0
Ta 196,v3,28672
Ta 198,v3,30720
Mvaa 20,180,4
Mvaa 20,184,44
s_nop 0
Ta 220,v3,32768
Ta 222,v3,34816
Mvaa 20,188,84
Mvaa 24,180,8
Mvaa 24,184,48
Mvaa 24,188,88
Mvaa 28,180,12
Mvaa 28,184,52
Mvaa 28,188,240
Mvaa 32,180,16
Mvaa 32,184,56
Mvaa 32,188,244
s_waitcnt vmcnt(30) lgkmcnt(0)
s_barrier
Mvaa 52,220,244
s_nop 0
Ta 180,%0,0
Ta 182,%0,2048
Mvaa 52,196,56
Mvaa 52,192,16
s_nop 0
Ta 184,%0,4096
Ta 186,%0,6144
Mvaa 48,220,240
Mvaa 48,196,52
s_nop 0
Ta 188,%0,8192
Ta 190,%0,10240
Mvaa 48,192,12
Mvaa 44,220,88
Mvaa 44,196,48
Mvaa 44,192,8
Mvaa 40,220,84
Mvaa 40,196,44
Mvaa 40,192,4
Mvaa 36,220,80
Mvaa 36,196,40
Mvaa 36,192,0
W_ 0
Mvaa 56,180,120
S_ s81,s6,6
Ta 192,v1,0
Ta 194,v1,2048
Mvaa 56,184,160
s_and_b32 s81, s81, 7
Mvaa 56,188,200
s_mul_i32 s81, s81, 20480
Ta 196,v1,4096
Ta 198,v1,6144
Mvaa 60,180,124
s_mov_b32 m0, s52
S_ s73,s81,s78
Mvaa 60,184,164
S_ s73,s73,4096
Ta 220,v1,8192
Ta 222,v1,10240
Mvaa 60,188,204
s_nop 0
Y_ 56,0
Y_ 57,256
Mvaa 64,180,128
s_nop 0
Y_ 58,512
Y_ 59,768
Mvaa 64,184,168
Mvaa 64,188,208
s_nop 0
Y_ 60,1024
Y_ 61,1280
Mvaa 68,180,132
s_nop 0
Y_ 62,1536
Y_ 63,1792
Mvaa 68,184,172
Mvaa 68,188,212
s_nop 0
Y_ 64,2048
Y_ 65,2304
Mvaa 72,180,136
s_nop 0
Y_ 66,2560
Y_ 67,2816
Mvaa 72,184,176
Mvaa 72,188,216
W_ 12
Y_ 68,3072
Y_ 69,3328
Mvaa 92,220,216
s_nop 0
Ta 180,v2,0
Ta 182,v2,2048
Mvaa 92,196,176
s_nop 0
Y_ 70,3584
Y_ 71,3840
Mvaa 92,192,136
s_nop 0
Ta 184,v2,4096
Ta 186,v2,6144
Mvaa 88,220,212
s_nop 0
Y_ 72,4096
Y_ 73,4352
Mvaa 88,196,172
S_ s83,s6,5
Ta 188,v2,8192
Ta 190,v2,10240
Mvaa 88,192,132
s_and_b32 s83, s83, 7
Y_ 74,4608
Y_ 75,4864
Mvaa 84,220,208
s_lshl_b32 s83, s83, 2
Y_ 92,9216
Y_ 93,9472
Mvaa 84,196,168
s_nop 0
Y_ 94,9728
Y_ 95,9984
Mvaa 84,192,128
s_nop 0
Y_ 88,8192
Y_ 89,8448
Mvaa 80,220,204
s_nop 0
Y_ 90,8704
Y_ 91,8960
Mvaa 80,196,164
v_cmp_eq_u32_e64 s[36:37], s83, v8
Mvaa 80,192,124
s_nop 0
Y_ 84,7168
Y_ 85,7424
Mvaa 76,220,200
s_nop 0
Y_ 86,7680
Y_ 87,7936
Mvaa 76,196,160
v_mov_b32 v10, 0x7fff0000
Mvaa 76,192,120
W_ 14
Y_ 80,6144
Y_ 81,6400
Mvaa 16,180,120
s_nop 0
Ta 192,v3,0
Ta 194,v3,2048
Mvaa 16,184,160
s_nop 0
Y_ 82,6656
Y_ 83,6912
Mvaa 16,188,200
s_nop 0
Ta 196,v3,4096
Ta 198,v3,6144
Mvaa 20,180,124
s_nop 0
Y_ 76,5120
Y_ 77,5376
Mvaa 20,184,164
s_nop 0
Ta 220,v3,8192
Ta 222,v3,10240
Mvaa 20,188,204
s_nop 0
Y_ 78,5632
Y_ 79,5888
Mvaa 24,180,128
v_cndmask_b32_e64 v10, v10, v9, s[36:37]
Mvaa 24,184,168
Mvaa 24,188,208
s_waitcnt vmcnt(19) lgkmcnt(0)
s_barrier
Mvaa 28,180,132
buffer_store_dword a255, v10, s[92:95], 0 offen offset:384 sc1
Mvaa 28,184,172
s_sub_u32 s75, s6, 4
ds_read2st64_b32 v[96:97], v5 offset0:0 offset1:1
S_ s55,s71,122880
Mvaa 28,188,212
s_and_b32 s75, s75, 7
ds_read2st64_b32 v[98:99], v5 offset0:2 offset1:3
S_ m0,s51,24576
Mvaa 32,180,136
ds_read2st64_b32 v[104:105], v5 offset0:4 offset1:5
Mvaa 32,184,176
buffer_load_dword v11, v8, s[92:95], s96 offen offset:384 sc1
Mvaa 32,188,216
ds_read2st64_b32 v[106:107], v5 offset0:6 offset1:7
Mvaa 52,220,216
s_nop 0
Ta 180,%0,12288
Ta 182,%0,14336
Mvaa 52,196,176
ds_read2st64_b32 v[112:113], v5 offset0:8 offset1:9
Mvaa 52,192,136
W_ 5
Ta 184,%0,16384
Ta 186,%0,18432
Mvaa 48,220,212
L_ v7,24,s55,0
Mvaa 48,196,172
S_ s55,s71,133120
Ta 188,%0,20480
Ta 190,%0,22528
Mvaa 48,192,132
S_ m0,s51,25600
ds_read2st64_b32 v[114:115], v5 offset0:10 offset1:11
A_ v96,v16,v96
Mvaa 44,220,208
ds_read2st64_b32 v[120:121], v5 offset0:12 offset1:13
A_ v97,v17,v97
Mvaa 44,196,168
L_ v7,24,s55,0
Mvaa 44,192,128
S_ s55,s71,143360
ds_read2st64_b32 v[122:123], v5 offset0:14 offset1:15
A_ v98,v18,v98
Mvaa 40,220,204
S_ m0,s51,26624
ds_read2st64_b32 v[128:129], v5 offset0:16 offset1:17
A_ v99,v19,v99
Mvaa 40,196,164
W_ 11
ds_read2st64_b32 v[130:131], v5 offset0:18 offset1:19
A_ v104,v20,v104
Mvaa 40,192,124
L_ v7,24,s55,0
Mvaa 36,220,200
S_ s55,s71,153600
ds_read2st64_b32 v[132:133], v5 offset0:36 offset1:37
A_ v105,v21,v105
Mvaa 36,196,160
S_ m0,s51,27648
ds_read2st64_b32 v[134:135], v5 offset0:38 offset1:39
A_ v106,v22,v106
Mvaa 36,192,120
ds_read2st64_b32 v[124:125], v5 offset0:32 offset1:33
A_ v107,v23,v107
s_waitcnt vmcnt(11)
Maaa 100,70,20
L_ v7,24,s55,0
Maaa 140,68,20
W_ 7
ds_read2st64_b32 v[126:127], v5 offset0:34 offset1:35
A_ v112,v24,v112
Maaa 104,70,24
S_ s55,s71,163840
ds_read2st64_b32 v[116:117], v5 offset0:28 offset1:29
A_ v113,v25,v113
Maaa 144,68,24
S_ m0,s51,28672
ds_read2st64_b32 v[118:119], v5 offset0:30 offset1:31
A_ v114,v26,v114
Maaa 108,70,28
L_ v7,24,s55,0
Maaa 148,68,28
S_ s55,s71,174080
ds_read2st64_b32 v[108:109], v5 offset0:24 offset1:25
A_ v115,v27,v115
Maaa 112,70,32
W_ 9
ds_read2st64_b32 v[110:111], v5 offset0:26 offset1:27
A_ v120,v28,v120
Maaa 152,68,32
S_ m0,s51,29696
ds_read2st64_b32 v[100:101], v5 offset0:20 offset1:21
A_ v121,v29,v121
Maaa 116,70,36
L_ v7,24,s55,0
Maaa 156,68,36
S_ s79,s6,8
ds_read2st64_b32 v[102:103], v5 offset0:22 offset1:23
A_ v122,v30,v122
Maaa 224,70,60
s_and_b32 s79, s79, 7
A_ v123,v31,v123
Maaa 228,68,60
W_ 10
A_ v128,v32,v128
A_ v129,v33,v129
Maaa 232,70,64
s_lshl_b32 s79, s79, 7
A_ v130,v34,v130
A_ v131,v35,v131
Maaa 236,68,64
W_ 9
A_ v132,v52,v132
W_ 8
Zvav 56,180,136
S_ s71,s72,s79
Ta 192,v1,12288
Ta 194,v1,14336
Mvav 56,184,156
S_ s55,s71,491520
A_ v133,v53,v133
A_ v134,v54,v134
Mvav 56,188,176
S_ m0,s51,0
Ta 196,v1,16384
Ta 198,v1,18432
Zvav 60,180,140
L_ v7,24,s55,0
Mvav 60,184,160
S_ s55,s71,501760
Ta 220,v1,20480
Ta 222,v1,22528
Mvav 60,188,180
S_ m0,s51,1024
A_ v135,v55,v135
Zvav 64,180,144
W_ 12
A_ v124,v48,v124
A_ v125,v49,v125
Mvav 64,184,164
L_ v7,24,s55,0
Mvav 64,188,184
S_ s55,s71,512000
A_ v126,v50,v126
A_ v127,v51,v127
Zvav 68,180,148
W_ 10
A_ v116,v44,v116
A_ v117,v45,v117
Mvav 68,184,168
S_ m0,s51,2048
A_ v118,v46,v118
A_ v119,v47,v119
Mvav 68,188,188
L_ v7,24,s55,0
Zvav 72,180,152
W_ 8
A_ v108,v40,v108
A_ v109,v41,v109
Mvav 72,184,172
S_ s55,s71,522240
A_ v110,v42,v110
A_ v111,v43,v111
Mvav 72,188,192
W_ 0
A_ v100,v36,v100
Zvav 92,220,192
S_ m0,s51,3072
Ta 180,v2,12288
Ta 182,v2,14336
Mvav 92,196,172
L_ v7,24,s55,0
Mvav 92,192,152
S_ s55,s71,532480
Ta 184,v2,16384
Ta 186,v2,18432
Zvav 88,220,188
S_ m0,s51,4096
A_ v101,v37,v101
A_ v102,v38,v102
Mvav 88,196,168
s_nop 0
Ta 188,v2,20480
Ta 190,v2,22528
Mvav 88,192,148
L_ v7,24,s55,0
Zvav 84,220,184
S_ s55,s71,542720
A_ v103,v39,v103
C_ 131,130,131
Mvav 84,196,164
S_ m0,s51,5120
C_ 130,128,129
C_ 132,132,133
Mvav 84,192,144
C_ 133,134,135
Zvav 80,220,180
buffer_store_dwordx4 v[130:133], v6, s[28:31], s73 offen offset:0 sc1
Mvav 80,196,160
s_sub_u32 s73, s73, 4096
Mvav 80,192,140
C_ 123,122,123
C_ 122,120,121
Zvav 76,220,176
C_ 124,124,125
C_ 125,126,127
Mvav 76,196,156
buffer_store_dwordx4 v[122:125], v6, s[28:31], s73 offen offset:3072 sc1
Mvav 76,192,136
W_ 0
C_ 115,114,115
C_ 114,112,113
Zvav 16,180,136
s_nop 0
Ta 192,v3,12288
Ta 194,v3,14336
Mvav 16,184,156
C_ 116,116,117
C_ 117,118,119
Mvav 16,188,176
s_nop 0
Ta 196,v3,16384
Ta 198,v3,18432
Zvav 20,180,140
buffer_store_dwordx4 v[114:117], v6, s[28:31], s73 offen offset:2048 sc1
Mvav 20,184,160
s_nop 0
Ta 220,v3,20480
Ta 222,v3,22528
Mvav 20,188,180
C_ 107,106,107
C_ 106,104,105
Zvav 24,180,144
C_ 108,108,109
C_ 109,110,111
Mvav 24,184,164
buffer_store_dwordx4 v[106:109], v6, s[28:31], s73 offen offset:1024 sc1
Mvav 24,188,184
C_ 99,98,99
C_ 98,96,97
Zvav 28,180,148
C_ 100,100,101
C_ 101,102,103
Mvav 28,184,168
Mvav 28,188,188
buffer_store_dwordx4 v[98:101], v6, s[28:31], s73 offen offset:0 sc1
Zvav 32,180,152
Mvav 32,184,172
Mvav 32,188,192
s_waitcnt vmcnt(31) lgkmcnt(0)
s_barrier
Zvav 52,220,192
s_nop 0
Ta 180,%0,49152
Ta 182,%0,51200
Mvav 52,196,172
L_ v7,24,s55,0
Mvav 52,192,152
S_ s55,s71,614400
Ta 184,%0,53248
Ta 186,%0,55296
Zvav 48,220,188
S_ m0,s51,12288
Mvav 48,196,168
s_nop 0
Ta 188,%0,57344
Ta 190,%0,59392
Mvav 48,192,148
L_ v7,24,s55,0
Zvav 44,220,184
S_ s55,s71,624640
Mvav 44,196,164
S_ m0,s51,13312
Mvav 44,192,144
Zvav 40,220,180
L_ v7,24,s55,0
Mvav 40,196,160
S_ s55,s71,634880
Mvav 40,192,140
S_ m0,s51,14336
Zvav 36,220,176
Mvav 36,196,156
L_ v7,24,s55,0
Mvav 36,192,136
W_ 0
S_ s55,s71,645120
Mvav 56,180,196
S_ m0,s51,15360
Ta 192,v1,49152
Ta 194,v1,51200
Mvav 56,184,216
Mvav 56,188,236
s_nop 0
Ta 196,v1,53248
Ta 198,v1,55296
Mvav 60,180,200
L_ v7,24,s55,0
Mvav 60,184,220
S_ s55,s71,655360
Ta 220,v1,57344
Ta 222,v1,59392
Mvav 60,188,240
S_ m0,s51,16384
Mvav 64,180,204
Mvav 64,184,224
L_ v7,24,s55,0
Mvav 64,188,244
S_ s55,s71,665600
Mvav 68,180,208
S_ m0,s51,17408
Mvav 68,184,228
Mvav 68,188,248
L_ v7,24,s55,0
Mvav 72,180,212
Mvav 72,184,232
Mvav 72,188,252
W_ 0
Mvav 92,220,252
s_nop 0
Ta 180,v2,49152
Ta 182,v2,51200
Mvav 92,196,232
Mvav 92,192,212
s_nop 0
Ta 184,v2,53248
Ta 186,v2,55296
Mvav 88,220,248
s_waitcnt vmcnt(23)
Mvav 88,196,228
s_nop 0
Ta 188,v2,57344
Ta 190,v2,59392
Mvav 88,192,208
v_cmp_eq_u32 vcc, s62, v11
Mvav 84,220,244
s_mul_i32 s76, s75, 204800
Mvav 84,196,224
s_bitcmp1_b32 vcc_lo, s75
s_cbranch_scc0 .Lspin3
.Lspun3:
Mvav 84,192,204
S_ s56,s76,s84
Mvav 80,220,240
buffer_load_dwordx4 a[20:23], v6, s[28:31], s56 offen offset:0 sc1
Mvav 80,196,220
Mvav 80,192,200
Mvav 76,220,236
Mvav 76,196,216
buffer_load_dwordx4 a[24:27], v6, s[28:31], s56 offen offset:1024 sc1
Mvav 76,192,196
W_ 0
Mvav 16,180,196
s_nop 0
Ta 192,v3,49152
Ta 194,v3,51200
Mvav 16,184,216
Mvav 16,188,236
s_nop 0
Ta 196,v3,53248
Ta 198,v3,55296
Mvav 20,180,200
buffer_load_dwordx4 a[28:31], v6, s[28:31], s56 offen offset:2048 sc1
Mvav 20,184,220
s_nop 0
Ta 220,v3,57344
Ta 222,v3,59392
Mvav 20,188,240
Mvav 24,180,204
Mvav 24,184,224
buffer_load_dwordx4 a[32:35], v6, s[28:31], s56 offen offset:3072 sc1
Mvav 24,188,244
S_ s56,s56,4096
Mvav 28,180,208
Mvav 28,184,228
Mvav 28,188,248
buffer_load_dwordx4 a[36:39], v6, s[28:31], s56 offen offset:0 sc1
Mvav 32,180,212
S_ s56,s76,s74
Mvav 32,184,232
Mvav 32,188,252
s_waitcnt vmcnt(30) lgkmcnt(0)
s_barrier
Mvav 52,220,252
S_ s55,s71,0
Ta 180,%0,36864
Ta 182,%0,38912
Mvav 52,196,232
buffer_load_dwordx4 a[60:63], v6, s[28:31], s56 offen offset:0 sc1
Mvav 52,192,212
S_ s56,s76,s82
Ta 184,%0,40960
Ta 186,%0,43008
Mvav 48,220,248
S_ m0,s51,49152
Mvav 48,196,228
s_nop 0
Ta 188,%0,45056
Ta 190,%0,47104
Mvav 48,192,208
buffer_load_dwordx4 a[64:67], v6, s[28:31], s56 offen offset:0 sc1
Mvav 44,220,244
s_sub_u32 s75, s6, 5
Mvav 44,196,224
s_and_b32 s75, s75, 7
Mvav 44,192,204
Mvav 40,220,240
buffer_load_dword v11, v8, s[92:95], s96 offen offset:384 sc1
Mvav 40,196,220
Mvav 40,192,200
Mvav 36,220,236
Mvav 36,196,216
L_ v7,24,s55,0
Mvav 36,192,196
W_ 0
S_ s55,s71,10240
Mvaa 56,180,0
S_ m0,s51,50176
Ta 192,v1,36864
Ta 194,v1,38912
Mvaa 56,184,40
Mvaa 56,188,80
s_nop 0
Ta 196,v1,40960
Ta 198,v1,43008
Mvaa 60,180,4
L_ v7,24,s55,0
Mvaa 60,184,44
S_ s55,s71,20480
Ta 220,v1,45056
Ta 222,v1,47104
Mvaa 60,188,84
S_ m0,s51,51200
Mvaa 64,180,8
Mvaa 64,184,48
L_ v7,24,s55,0
Mvaa 64,188,88
S_ s55,s71,30720
Mvaa 68,180,12
S_ m0,s51,52224
Mvaa 68,184,52
Mvaa 68,188,240
L_ v7,24,s55,0
Mvaa 72,180,16
S_ s55,s71,40960
Mvaa 72,184,56
S_ m0,s51,53248
Mvaa 72,188,244
W_ 0
Mvaa 92,220,244
s_nop 0
Ta 180,v2,36864
Ta 182,v2,38912
Mvaa 92,196,56
L_ v7,24,s55,0
Mvaa 92,192,16
S_ s55,s71,51200
Ta 184,v2,40960
Ta 186,v2,43008
Mvaa 88,220,240
S_ m0,s51,54272
Mvaa 88,196,52
s_nop 0
Ta 188,v2,45056
Ta 190,v2,47104
Mvaa 88,192,12
L_ v7,24,s55,0
Mvaa 84,220,88
Mvaa 84,196,48
Mvaa 84,192,8
Mvaa 80,220,84
Mvaa 80,196,44
S_ s83,s6,6
Mvaa 80,192,4
s_and_b32 s83, s83, 7
Mvaa 76,220,80
s_lshl_b32 s83, s83, 2
Mvaa 76,196,40
v_cmp_eq_u32_e64 s[36:37], s83, v8
Mvaa 76,192,0
W_ 0
v_mov_b32 v10, 0x7fff0000
Mvaa 16,180,0
s_nop 0
Ta 192,v3,36864
Ta 194,v3,38912
Mvaa 16,184,40
v_cndmask_b32_e64 v10, v10, v9, s[36:37]
Mvaa 16,188,80
s_nop 0
Ta 196,v3,40960
Ta 198,v3,43008
Mvaa 20,180,4
Mvaa 20,184,44
s_nop 0
Ta 220,v3,45056
Ta 222,v3,47104
Mvaa 20,188,84
Mvaa 24,180,8
Mvaa 24,184,48
Mvaa 24,188,88
Mvaa 28,180,12
Mvaa 28,184,52
Mvaa 28,188,240
Mvaa 32,180,16
Mvaa 32,184,56
Mvaa 32,188,244
s_waitcnt vmcnt(21) lgkmcnt(0)
s_barrier
Mvaa 52,220,244
s_nop 0
Ta 180,%0,24576
Ta 182,%0,26624
Mvaa 52,196,56
buffer_store_dword a255, v10, s[92:95], 0 offen offset:384 sc1
Mvaa 52,192,16
s_nop 0
Ta 184,%0,28672
Ta 186,%0,30720
Mvaa 48,220,240
Mvaa 48,196,52
s_nop 0
Ta 188,%0,32768
Ta 190,%0,34816
Mvaa 48,192,12
Mvaa 44,220,88
Mvaa 44,196,48
Mvaa 44,192,8
Mvaa 40,220,84
Mvaa 40,196,44
Mvaa 40,192,4
Mvaa 36,220,80
Mvaa 36,196,40
Mvaa 36,192,0
W_ 0
Mvaa 56,180,120
S_ s81,s6,7
Ta 192,v1,24576
Ta 194,v1,26624
Mvaa 56,184,160
s_and_b32 s81, s81, 7
Mvaa 56,188,200
s_mul_i32 s81, s81, 20480
Ta 196,v1,28672
Ta 198,v1,30720
Mvaa 60,180,124
s_mov_b32 m0, s52
S_ s73,s81,s78
Mvaa 60,184,164
S_ s73,s73,4096
Ta 220,v1,32768
Ta 222,v1,34816
Mvaa 60,188,204
s_nop 0
Y_ 56,0
Y_ 57,256
Mvaa 64,180,128
s_nop 0
Y_ 58,512
Y_ 59,768
Mvaa 64,184,168
Mvaa 64,188,208
s_nop 0
Y_ 60,1024
Y_ 61,1280
Mvaa 68,180,132
s_nop 0
Y_ 62,1536
Y_ 63,1792
Mvaa 68,184,172
Mvaa 68,188,212
s_nop 0
Y_ 64,2048
Y_ 65,2304
Mvaa 72,180,136
s_nop 0
Y_ 66,2560
Y_ 67,2816
Mvaa 72,184,176
Mvaa 72,188,216
W_ 12
Y_ 68,3072
Y_ 69,3328
Mvaa 92,220,216
s_nop 0
Ta 180,v2,24576
Ta 182,v2,26624
Mvaa 92,196,176
s_nop 0
Y_ 70,3584
Y_ 71,3840
Mvaa 92,192,136
s_nop 0
Ta 184,v2,28672
Ta 186,v2,30720
Mvaa 88,220,212
s_nop 0
Y_ 72,4096
Y_ 73,4352
Mvaa 88,196,172
s_nop 0
Ta 188,v2,32768
Ta 190,v2,34816
Mvaa 88,192,132
s_nop 0
Y_ 74,4608
Y_ 75,4864
Mvaa 84,220,208
s_nop 0
Y_ 92,9216
Y_ 93,9472
Mvaa 84,196,168
s_nop 0
Y_ 94,9728
Y_ 95,9984
Mvaa 84,192,128
s_nop 0
Y_ 88,8192
Y_ 89,8448
Mvaa 80,220,204
s_nop 0
Y_ 90,8704
Y_ 91,8960
Mvaa 80,196,164
Mvaa 80,192,124
s_nop 0
Y_ 84,7168
Y_ 85,7424
Mvaa 76,220,200
s_nop 0
Y_ 86,7680
Y_ 87,7936
Mvaa 76,196,160
Mvaa 76,192,120
W_ 14
Y_ 80,6144
Y_ 81,6400
Mvaa 16,180,120
s_nop 0
Ta 192,v3,24576
Ta 194,v3,26624
Mvaa 16,184,160
s_nop 0
Y_ 82,6656
Y_ 83,6912
Mvaa 16,188,200
s_nop 0
Ta 196,v3,28672
Ta 198,v3,30720
Mvaa 20,180,124
s_nop 0
Y_ 76,5120
Y_ 77,5376
Mvaa 20,184,164
s_nop 0
Ta 220,v3,32768
Ta 222,v3,34816
Mvaa 20,188,204
s_nop 0
Y_ 78,5632
Y_ 79,5888
Mvaa 24,180,128
Mvaa 24,184,168
Mvaa 24,188,208
s_waitcnt vmcnt(21) lgkmcnt(0)
s_barrier
Mvaa 28,180,132
S_ s55,s71,122880
ds_read2st64_b32 v[96:97], v5 offset0:0 offset1:1
Mvaa 28,184,172
S_ m0,s51,36864
ds_read2st64_b32 v[98:99], v5 offset0:2 offset1:3
Mvaa 28,188,212
L_ v7,24,s55,0
Mvaa 32,180,136
S_ s55,s71,133120
ds_read2st64_b32 v[104:105], v5 offset0:4 offset1:5
Mvaa 32,184,176
s_waitcnt vmcnt(8)
ds_read2st64_b32 v[106:107], v5 offset0:6 offset1:7
S_ m0,s51,37888
Mvaa 32,188,216
ds_read2st64_b32 v[112:113], v5 offset0:8 offset1:9
v_cmp_eq_u32 vcc, s62, v11
Mvaa 52,220,216
s_mul_i32 s76, s75, 204800
Ta 180,%0,0
Ta 182,%0,2048
Mvaa 52,196,176
L_ v7,24,s55,0
Mvaa 52,192,136
W_ 5
Ta 184,%0,4096
Ta 186,%0,6144
Mvaa 48,220,212
s_bitcmp1_b32 vcc_lo, s75
s_cbranch_scc0 .Lspin4
.Lspun4:
ds_read2st64_b32 v[114:115], v5 offset0:10 offset1:11
Mvaa 48,196,172
S_ s55,s71,143360
Ta 188,%0,8192
Ta 190,%0,10240
Mvaa 48,192,132
S_ m0,s51,38912
ds_read2st64_b32 v[120:121], v5 offset0:12 offset1:13
A_ v96,v16,v96
Mvaa 44,220,208
L_ v7,24,s55,0
Mvaa 44,196,168
S_ s55,s71,153600
ds_read2st64_b32 v[122:123], v5 offset0:14 offset1:15
A_ v97,v17,v97
Mvaa 44,192,128
S_ m0,s51,39936
ds_read2st64_b32 v[128:129], v5 offset0:16 offset1:17
A_ v98,v18,v98
Mvaa 40,220,204
ds_read2st64_b32 v[130:131], v5 offset0:18 offset1:19
A_ v99,v19,v99
Mvaa 40,196,164
L_ v7,24,s55,0
Mvaa 40,192,124
W_ 12
ds_read2st64_b32 v[132:133], v5 offset0:36 offset1:37
A_ v104,v20,v104
Mvaa 36,220,200
S_ s55,s71,163840
ds_read2st64_b32 v[134:135], v5 offset0:38 offset1:39
A_ v105,v21,v105
Mvaa 36,196,160
S_ m0,s51,40960
ds_read2st64_b32 v[124:125], v5 offset0:32 offset1:33
A_ v106,v22,v106
Mvaa 36,192,120
L_ v7,24,s55,0
s_waitcnt vmcnt(13)
Maaa 100,70,20
S_ s55,s71,174080
ds_read2st64_b32 v[126:127], v5 offset0:34 offset1:35
A_ v107,v23,v107
Maaa 140,68,20
W_ 10
ds_read2st64_b32 v[116:117], v5 offset0:28 offset1:29
A_ v112,v24,v112
Maaa 104,70,24
S_ m0,s51,41984
ds_read2st64_b32 v[118:119], v5 offset0:30 offset1:31
A_ v113,v25,v113
Maaa 144,68,24
L_ v7,24,s55,0
Maaa 108,70,28
ds_read2st64_b32 v[108:109], v5 offset0:24 offset1:25
A_ v114,v26,v114
Maaa 148,68,28
ds_read2st64_b32 v[110:111], v5 offset0:26 offset1:27
A_ v115,v27,v115
Maaa 112,70,32
W_ 10
ds_read2st64_b32 v[100:101], v5 offset0:20 offset1:21
A_ v120,v28,v120
Maaa 152,68,32
ds_read2st64_b32 v[102:103], v5 offset0:22 offset1:23
A_ v121,v29,v121
Maaa 116,70,36
A_ v122,v30,v122
A_ v123,v31,v123
Maaa 156,68,36
W_ 10
A_ v128,v32,v128
A_ v129,v33,v129
Maaa 224,70,60
A_ v130,v34,v130
A_ v131,v35,v131
Maaa 228,68,60
W_ 8
A_ v132,v52,v132
A_ v133,v53,v133
Maaa 232,70,64
A_ v134,v54,v134
A_ v135,v55,v135
Maaa 236,68,64
W_ 7
A_ v124,v48,v124
W_ 6
Zvav 56,180,136
s_nop 0
Ta 192,v1,0
Ta 194,v1,2048
Mvav 56,184,156
A_ v125,v49,v125
A_ v126,v50,v126
Mvav 56,188,176
S_ s56,s76,s84
Ta 196,v1,4096
Ta 198,v1,6144
Zvav 60,180,140
buffer_load_dwordx4 a[20:23], v6, s[28:31], s56 offen offset:0 sc1
Mvav 60,184,160
s_nop 0
Ta 220,v1,8192
Ta 222,v1,10240
Mvav 60,188,180
A_ v127,v51,v127
C_ 131,130,131
Zvav 64,180,144
W_ 10
A_ v116,v44,v116
A_ v117,v45,v117
Mvav 64,184,164
buffer_load_dwordx4 a[24:27], v6, s[28:31], s56 offen offset:1024 sc1
Mvav 64,188,184
A_ v118,v46,v118
A_ v119,v47,v119
Zvav 68,180,148
W_ 8
A_ v108,v40,v108
A_ v109,v41,v109
Mvav 68,184,168
A_ v110,v42,v110
A_ v111,v43,v111
Mvav 68,188,188
buffer_load_dwordx4 a[28:31], v6, s[28:31], s56 offen offset:2048 sc1
Zvav 72,180,152
W_ 6
A_ v100,v36,v100
A_ v101,v37,v101
Mvav 72,184,172
A_ v102,v38,v102
A_ v103,v39,v103
Mvav 72,188,192
W_ 0
C_ 130,128,129
C_ 132,132,133
Zvav 92,220,192
s_nop 0
Ta 180,v2,0
Ta 182,v2,2048
Mvav 92,196,172
buffer_load_dwordx4 a[32:35], v6, s[28:31], s56 offen offset:3072 sc1
Mvav 92,192,152
S_ s56,s56,4096
Ta 184,v2,4096
Ta 186,v2,6144
Zvav 88,220,188
C_ 133,134,135
Mvav 88,196,168
s_nop 0
Ta 188,v2,8192
Ta 190,v2,10240
Mvav 88,192,148
buffer_load_dwordx4 a[36:39], v6, s[28:31], s56 offen offset:0 sc1
Zvav 84,220,184
S_ s56,s76,s74
Mvav 84,196,164
Mvav 84,192,144
Zvav 80,220,180
buffer_load_dwordx4 a[60:63], v6, s[28:31], s56 offen offset:0 sc1
Mvav 80,196,160
S_ s56,s76,s82
Mvav 80,192,140
Zvav 76,220,176
Mvav 76,196,156
buffer_load_dwordx4 a[64:67], v6, s[28:31], s56 offen offset:0 sc1
Mvav 76,192,136
W_ 0
s_sub_u32 s75, s6, 6
Maav 100,180,136
s_and_b32 s75, s75, 7
Ta 192,v3,0
Ta 194,v3,2048
Maav 100,184,156
Maav 100,188,176
s_nop 0
Ta 196,v3,4096
Ta 198,v3,6144
Maav 104,180,140
buffer_load_dword v11, v8, s[92:95], s96 offen offset:384 sc1
Maav 104,184,160
s_nop 0
Ta 220,v3,8192
Ta 222,v3,10240
Maav 104,188,180
Maav 108,180,144
Maav 108,184,164
buffer_store_dwordx4 v[130:133], v6, s[28:31], s73 offen offset:0 sc1
Maav 108,188,184
s_sub_u32 s73, s73, 4096
Maav 112,180,148
C_ 123,122,123
C_ 122,120,121
Maav 112,184,168
C_ 124,124,125
C_ 125,126,127
Maav 112,188,188
buffer_store_dwordx4 v[122:125], v6, s[28:31], s73 offen offset:3072 sc1
Maav 116,180,152
C_ 115,114,115
C_ 114,112,113
Maav 116,184,172
C_ 116,116,117
C_ 117,118,119
Maav 116,188,192
s_waitcnt vmcnt(31) lgkmcnt(0)
s_barrier
Maav 156,220,192
s_nop 0
Ta 180,%0,12288
Ta 182,%0,14336
Maav 156,196,172
buffer_store_dwordx4 v[114:117], v6, s[28:31], s73 offen offset:2048 sc1
Maav 156,192,152
s_nop 0
Ta 184,%0,16384
Ta 186,%0,18432
Maav 152,220,188
C_ 107,106,107
C_ 106,104,105
Maav 152,196,168
s_nop 0
Ta 188,%0,20480
Ta 190,%0,22528
Maav 152,192,148
C_ 108,108,109
C_ 109,110,111
Maav 148,220,184
buffer_store_dwordx4 v[106:109], v6, s[28:31], s73 offen offset:1024 sc1
Maav 148,196,164
C_ 99,98,99
C_ 98,96,97
Maav 148,192,144
C_ 100,100,101
C_ 101,102,103
Maav 144,220,180
Maav 144,196,160
buffer_store_dwordx4 v[98:101], v6, s[28:31], s73 offen offset:0 sc1
Maav 144,192,140
Maav 140,220,176
Maav 140,196,156
Maav 140,192,136
W_ 0
Mvav 56,180,196
s_nop 0
Ta 192,v1,12288
Ta 194,v1,14336
Mvav 56,184,216
Mvav 56,188,236
s_nop 0
Ta 196,v1,16384
Ta 198,v1,18432
Mvav 60,180,200
Mvav 60,184,220
s_nop 0
Ta 220,v1,20480
Ta 222,v1,22528
Mvav 60,188,240
v_bfe_u32 v4, v6, 4, 4
Mvav 64,180,204
v_mul_u32_u24 v4, 2560, v4
Mvav 64,184,224
v_bfe_u32 v10, v6, 8, 1
Mvav 64,188,244
v_lshl_add_u32 v4, v10, 5, v4
Mvav 68,180,208
v_lshrrev_b32 v10, 9, v6
Mvav 68,184,228
v_lshl_add_u32 v4, v10, 4, v4
Mvav 68,188,248
Mvav 72,180,212
Mvav 72,184,232
Mvav 72,188,252
W_ 0
Mvav 92,220,252
s_nop 0
Ta 180,v2,12288
Ta 182,v2,14336
Mvav 92,196,232
Mvav 92,192,212
s_nop 0
Ta 184,v2,16384
Ta 186,v2,18432
Mvav 88,220,248
Mvav 88,196,228
s_nop 0
Ta 188,v2,20480
Ta 190,v2,22528
Mvav 88,192,208
Mvav 84,220,244
Mvav 84,196,224
Mvav 84,192,204
Mvav 80,220,240
Mvav 80,196,220
Mvav 80,192,200
Mvav 76,220,236
Mvav 76,196,216
Mvav 76,192,196
W_ 0
Maav 100,180,196
s_nop 0
Ta 192,v3,12288
Ta 194,v3,14336
Maav 100,184,216
Maav 100,188,236
s_nop 0
Ta 196,v3,16384
Ta 198,v3,18432
Maav 104,180,200
Maav 104,184,220
s_nop 0
Ta 220,v3,20480
Ta 222,v3,22528
Maav 104,188,240
Maav 108,180,204
S_ s86,s6,1
Maav 108,184,224
s_and_b32 s86, s86, 7
Maav 108,188,244
s_waitcnt vmcnt(5)
Maav 112,180,208
v_cmp_eq_u32 vcc, s62, v11
Maav 112,184,228
s_mul_i32 s76, s75, 204800
Maav 112,188,248
s_bitcmp1_b32 vcc_lo, s75
s_cbranch_scc0 .Lspin5
.Lspun5:
Maav 116,180,212
s_bitcmp1_b32 vcc_lo, s86
s_cbranch_scc0 .Lspin6
.Lspun6:
Maav 116,184,232
S_ s56,s76,s84
Maav 116,188,252
s_waitcnt vmcnt(20) lgkmcnt(0)
s_barrier
Maav 156,220,252
s_nop 0
Ta 180,%0,49152
Ta 182,%0,51200
Maav 156,196,232
buffer_load_dwordx4 v[16:19], v6, s[28:31], s56 offen offset:0 sc1
Maav 156,192,212
s_nop 0
Ta 184,%0,53248
Ta 186,%0,55296
Maav 152,220,248
Maav 152,196,228
s_nop 0
Ta 188,%0,57344
Ta 190,%0,59392
Maav 152,192,208
buffer_load_dwordx4 v[20:23], v6, s[28:31], s56 offen offset:1024 sc1
Maav 148,220,244
Maav 148,196,224
Maav 148,192,204
Maav 144,220,240
buffer_load_dwordx4 v[24:27], v6, s[28:31], s56 offen offset:2048 sc1
Maav 144,196,220
Maav 144,192,200
Maav 140,220,236
Maav 140,196,216
buffer_load_dwordx4 v[28:31], v6, s[28:31], s56 offen offset:3072 sc1
Maav 140,192,196
W_ 0
S_ s56,s56,4096
Mvaa 56,180,0
s_nop 0
Ta 192,v1,49152
Ta 194,v1,51200
Mvaa 56,184,40
R_ v164,a224
R_ v165,a225
Mvaa 56,188,80
s_nop 0
Ta 196,v1,53248
Ta 198,v1,55296
Mvaa 60,180,4
buffer_load_dwordx4 v[32:35], v6, s[28:31], s56 offen offset:0 sc1
Mvaa 60,184,44
S_ s56,s76,s74
Ta 220,v1,57344
Ta 222,v1,59392
Mvaa 60,188,84
R_ v166,a226
R_ v167,a227
Mvaa 64,180,8
R_ v168,a228
R_ v169,a229
Mvaa 64,184,48
buffer_load_dwordx4 v[36:39], v6, s[28:31], s56 offen offset:0 sc1
Mvaa 64,188,88
S_ s56,s76,s82
R_ v170,a230
R_ v171,a231
Mvaa 68,180,12
R_ v172,a232
R_ v173,a233
Mvaa 68,184,52
R_ v174,a234
R_ v175,a235
Mvaa 68,188,240
buffer_load_dwordx4 v[40:43], v6, s[28:31], s56 offen offset:0 sc1
Mvaa 72,180,16
s_mul_i32 s76, s86, 204800
R_ v176,a236
R_ v177,a237
Mvaa 72,184,56
S_ s80,s76,s84
R_ v178,a238
R_ v179,a239
Mvaa 72,188,244
W_ 0
S_ s56,s76,s74
Mvaa 92,220,244
s_nop 0
Ta 180,v2,49152
Ta 182,v2,51200
Mvaa 92,196,56
buffer_load_dwordx4 v[156:159], v6, s[28:31], s56 offen offset:0 sc1
Mvaa 92,192,16
S_ s56,s76,s82
Ta 184,v2,53248
Ta 186,v2,55296
Mvaa 88,220,240
Mvaa 88,196,52
s_nop 0
Ta 188,v2,57344
Ta 190,v2,59392
Mvaa 88,192,12
buffer_load_dwordx4 v[160:163], v6, s[28:31], s56 offen offset:0 sc1
Mvaa 84,220,88
Mvaa 84,196,48
Mvaa 84,192,8
Mvaa 80,220,84
Mvaa 80,196,44
Mvaa 80,192,4
Mvaa 76,220,80
Mvaa 76,196,40
Mvaa 76,192,0
W_ 0
Maaa 100,180,0
s_nop 0
Ta 192,v3,49152
Ta 194,v3,51200
Maaa 100,184,40
Maaa 100,188,80
s_nop 0
Ta 196,v3,53248
Ta 198,v3,55296
Maaa 104,180,4
Maaa 104,184,44
s_nop 0
Ta 220,v3,57344
Ta 222,v3,59392
Maaa 104,188,84
Maaa 108,180,8
R_ v180,a100
R_ v181,a101
Maaa 108,184,48
R_ v182,a102
R_ v183,a103
Maaa 108,188,88
Maaa 112,180,12
R_ v184,a104
R_ v185,a105
Maaa 112,184,52
R_ v186,a106
R_ v187,a107
Maaa 112,188,240
Maaa 116,180,16
R_ v188,a108
R_ v189,a109
Maaa 116,184,56
R_ v190,a110
R_ v191,a111
Maaa 116,188,244
s_waitcnt vmcnt(22) lgkmcnt(0)
s_barrier
Maaa 156,220,244
s_nop 0
Ta 180,%0,36864
Ta 182,%0,38912
Maaa 156,196,56
R_ v192,a112
R_ v193,a113
Maaa 156,192,16
s_nop 0
Ta 184,%0,40960
Ta 186,%0,43008
Maaa 152,220,240
R_ v194,a114
R_ v195,a115
Maaa 152,196,52
s_nop 0
Ta 188,%0,45056
Ta 190,%0,47104
Maaa 152,192,12
R_ v196,a116
R_ v197,a117
Maaa 148,220,88
R_ v198,a118
R_ v199,a119
Maaa 148,196,48
R_ v216,a156
R_ v217,a157
Maaa 148,192,8
R_ v218,a158
R_ v219,a159
Maaa 144,220,84
R_ v212,a152
R_ v213,a153
Maaa 144,196,44
R_ v214,a154
R_ v215,a155
Maaa 144,192,4
Maaa 140,220,80
R_ v208,a148
R_ v209,a149
Maaa 140,196,40
R_ v210,a150
R_ v211,a151
Maaa 140,192,0
s_waitcnt vmcnt(15)
Mvaa 164,70,60
R_ v204,a144
R_ v205,a145
Mvaa 168,68,60
R_ v206,a146
R_ v207,a147
Mvaa 172,70,64
Mvaa 176,68,64
R_ v200,a140
R_ v201,a141
Mvaa 180,70,20
R_ v202,a142
R_ v203,a143
Mvaa 184,70,24
Mvaa 188,70,28
Mvaa 192,70,32
Mvaa 196,70,36
Mvaa 216,68,36
Mvaa 212,68,32
Mvaa 208,68,28
Mvaa 204,68,24
Mvaa 200,68,20
W_ 0
Mvaa 56,180,120
S_ s56,s76,s84
Ta 192,v1,36864
Ta 194,v1,38912
Mvaa 56,184,160
buffer_load_dwordx4 v[136:139], v6, s[28:31], s56 offen offset:0 sc1
Mvaa 56,188,200
s_nop 0
Ta 196,v1,40960
Ta 198,v1,43008
Mvaa 60,180,124
s_mov_b32 m0, s52
Mvaa 60,184,164
s_nop 0
Ta 220,v1,45056
Ta 222,v1,47104
Mvaa 60,188,204
s_nop 0
Y_ 56,0
Y_ 57,256
Mvaa 64,180,128
s_nop 0
Y_ 58,512
Y_ 59,768
Mvaa 64,184,168
buffer_load_dwordx4 v[140:143], v6, s[28:31], s56 offen offset:1024 sc1
Mvaa 64,188,208
s_nop 0
Y_ 60,1024
Y_ 61,1280
Mvaa 68,180,132
s_nop 0
Y_ 62,1536
Y_ 63,1792
Mvaa 68,184,172
Mvaa 68,188,212
s_nop 0
Y_ 64,2048
Y_ 65,2304
Mvaa 72,180,136
s_nop 0
Y_ 66,2560
Y_ 67,2816
Mvaa 72,184,176
buffer_load_dwordx4 v[144:147], v6, s[28:31], s56 offen offset:2048 sc1
Mvaa 72,188,216
W_ 12
Y_ 68,3072
Y_ 69,3328
Mvaa 92,220,216
s_nop 0
Ta 180,v2,36864
Ta 182,v2,38912
Mvaa 92,196,176
s_nop 0
Y_ 70,3584
Y_ 71,3840
Mvaa 92,192,136
s_nop 0
Ta 184,v2,40960
Ta 186,v2,43008
Mvaa 88,220,212
s_nop 0
Y_ 72,4096
Y_ 73,4352
Mvaa 88,196,172
s_nop 0
Ta 188,v2,45056
Ta 190,v2,47104
Mvaa 88,192,132
s_nop 0
Y_ 74,4608
Y_ 75,4864
Mvaa 84,220,208
s_nop 0
Y_ 92,9216
Y_ 93,9472
Mvaa 84,196,168
s_nop 0
Y_ 94,9728
Y_ 95,9984
Mvaa 84,192,128
s_nop 0
Y_ 88,8192
Y_ 89,8448
Mvaa 80,220,204
s_nop 0
Y_ 90,8704
Y_ 91,8960
Mvaa 80,196,164
buffer_load_dwordx4 v[148:151], v6, s[28:31], s56 offen offset:3072 sc1
Mvaa 80,192,124
S_ s56,s56,4096
Y_ 84,7168
Y_ 85,7424
Mvaa 76,220,200
s_nop 0
Y_ 86,7680
Y_ 87,7936
Mvaa 76,196,160
Mvaa 76,192,120
W_ 14
Y_ 80,6144
Y_ 81,6400
Mvaa 180,180,120
s_nop 0
Ta 192,v3,36864
Ta 194,v3,38912
Mvaa 180,184,160
s_nop 0
Y_ 82,6656
Y_ 83,6912
Mvaa 180,188,200
s_nop 0
Ta 196,v3,40960
Ta 198,v3,43008
Mvaa 184,180,124
s_nop 0
Y_ 76,5120
Y_ 77,5376
Mvaa 184,184,164
s_nop 0
Ta 220,v3,45056
Ta 222,v3,47104
Mvaa 184,188,204
s_nop 0
Y_ 78,5632
Y_ 79,5888
Mvaa 188,180,128
buffer_load_dwordx4 v[152:155], v6, s[28:31], s56 offen offset:0 sc1
Mvaa 188,184,168
Mvaa 188,188,208
s_waitcnt vmcnt(63) lgkmcnt(0)
s_barrier
Mvaa 192,180,132
ds_read2st64_b32 v[96:97], v5 offset0:0 offset1:1
Mvaa 192,184,172
ds_read2st64_b32 v[98:99], v5 offset0:2 offset1:3
Mvaa 192,188,212
ds_read2st64_b32 v[104:105], v5 offset0:4 offset1:5
Mvaa 196,180,136
ds_read2st64_b32 v[106:107], v5 offset0:6 offset1:7
Mvaa 196,184,176
ds_read2st64_b32 v[112:113], v5 offset0:8 offset1:9
Mvaa 196,188,216
ds_read2st64_b32 v[114:115], v5 offset0:10 offset1:11
Mvaa 216,220,216
ds_read2st64_b32 v[120:121], v5 offset0:12 offset1:13
Mvaa 216,196,176
ds_read2st64_b32 v[122:123], v5 offset0:14 offset1:15
W_ 7
A_ v96,v96,v180
Mvaa 216,192,136
ds_read2st64_b32 v[128:129], v5 offset0:16 offset1:17
A_ v97,v97,v181
)A"
R"A(Mvaa 212,220,212
ds_read2st64_b32 v[130:131], v5 offset0:18 offset1:19
W_ 8
A_ v98,v98,v182
Mvaa 212,196,172
ds_read2st64_b32 v[132:133], v5 offset0:36 offset1:37
A_ v99,v99,v183
Mvaa 212,192,132
ds_read2st64_b32 v[134:135], v5 offset0:38 offset1:39
W_ 9
A_ v104,v104,v184
Mvaa 208,220,208
ds_read2st64_b32 v[124:125], v5 offset0:32 offset1:33
A_ v105,v105,v185
Mvaa 208,196,168
ds_read2st64_b32 v[126:127], v5 offset0:34 offset1:35
W_ 10
A_ v106,v106,v186
Mvaa 208,192,128
ds_read2st64_b32 v[116:117], v5 offset0:28 offset1:29
A_ v107,v107,v187
Mvaa 204,220,204
ds_read2st64_b32 v[118:119], v5 offset0:30 offset1:31
W_ 11
A_ v112,v112,v188
Mvaa 204,196,164
ds_read2st64_b32 v[108:109], v5 offset0:24 offset1:25
A_ v113,v113,v189
Mvaa 204,192,124
ds_read2st64_b32 v[110:111], v5 offset0:26 offset1:27
W_ 12
A_ v114,v114,v190
Mvaa 200,220,200
ds_read2st64_b32 v[100:101], v5 offset0:20 offset1:21
A_ v115,v115,v191
Mvaa 200,196,160
ds_read2st64_b32 v[102:103], v5 offset0:22 offset1:23
W_ 13
A_ v120,v120,v192
Mvaa 200,192,120
A_ v121,v121,v193
W_ 12
A_ v122,v122,v194
A_ v123,v123,v195
W_ 11
A_ v128,v128,v196
A_ v129,v129,v197
W_ 10
A_ v130,v130,v198
A_ v131,v131,v199
s_waitcnt vmcnt(5)
Mvav 164,70,36
W_ 9
A_ v132,v132,v216
A_ v133,v133,v217
Mvav 168,68,36
W_ 8
A_ v134,v134,v218
A_ v135,v135,v219
Mvav 172,70,40
W_ 7
A_ v124,v124,v212
A_ v125,v125,v213
Mvav 176,68,40
W_ 6
A_ v126,v126,v214
A_ v127,v127,v215
Mvav 164,70,156
W_ 5
A_ v116,v116,v208
A_ v117,v117,v209
Mvav 168,68,156
W_ 4
A_ v118,v118,v210
A_ v119,v119,v211
Mvav 172,70,160
W_ 3
A_ v108,v108,v204
A_ v109,v109,v205
Mvav 176,68,160
W_ 2
A_ v110,v110,v206
A_ v111,v111,v207
Mvav 96,70,16
W_ 1
A_ v100,v100,v200
A_ v101,v101,v201
Mvav 104,70,20
W_ 0
A_ v102,v102,v202
A_ v103,v103,v203
Mvav 112,70,24
C_ 220,164,165
C_ 221,166,167
Mvav 120,70,28
C_ 222,168,169
C_ 223,170,171
Mvav 128,70,32
v_permlane16_swap_b32 v220, v222
v_permlane16_swap_b32 v221, v223
Mvav 132,68,32
C_ 228,172,173
C_ 229,174,175
Mvav 124,68,28
C_ 230,176,177
C_ 231,178,179
Mvav 116,68,24
v_permlane16_swap_b32 v228, v230
v_permlane16_swap_b32 v229, v231
Mvav 108,68,20
Mvav 100,68,16
s_waitcnt vmcnt(0)
.Lsel6:
Nvav 180,70,136,96
Nvav 188,70,140,104
Nvav 196,70,144,112
Nvav 204,70,148,120
Nvav 212,70,152,128
Nvav 184,68,136,100
s_nop 3
A_ v10,v180,v188
Nvav 192,68,140,108
A_ v11,v196,v204
Nvav 200,68,144,116
A_ v10,v10,v212
v_cmp_u_f32 vcc, v10, v11
Nvav 208,68,148,124
s_cbranch_vccnz .Lretry
.Lgood:
Nvav 216,68,152,132
buffer_store_dwordx4 v[220:223], v4, s[32:35], s9 offen sc1
buffer_store_dwordx4 v[228:231], v4, s[32:35], s10 offen sc1
C_ 180,180,181
C_ 181,182,183
C_ 182,184,185
C_ 183,186,187
s_nop 0
v_permlane16_swap_b32 v180, v182
v_permlane16_swap_b32 v181, v183
buffer_store_dwordx4 v[180:183], v4, s[32:35], s8 offen sc1
C_ 188,188,189
C_ 189,190,191
C_ 190,192,193
C_ 191,194,195
S_ s8,s8,40960
v_permlane16_swap_b32 v188, v190
v_permlane16_swap_b32 v189, v191
buffer_store_dwordx4 v[188:191], v4, s[32:35], s8 offen sc1
C_ 196,196,197
C_ 197,198,199
C_ 198,200,201
C_ 199,202,203
S_ s8,s8,40960
v_permlane16_swap_b32 v196, v198
v_permlane16_swap_b32 v197, v199
buffer_store_dwordx4 v[196:199], v4, s[32:35], s8 offen sc1
C_ 204,204,205
C_ 205,206,207
C_ 206,208,209
C_ 207,210,211
S_ s8,s8,40960
v_permlane16_swap_b32 v204, v206
v_permlane16_swap_b32 v205, v207
buffer_store_dwordx4 v[204:207], v4, s[32:35], s8 offen sc1
C_ 212,212,213
C_ 213,214,215
C_ 214,216,217
C_ 215,218,219
S_ s8,s8,40960
v_permlane16_swap_b32 v212, v214
v_permlane16_swap_b32 v213, v215
buffer_store_dwordx4 v[212:215], v4, s[32:35], s8 offen sc1
s_endpgm
.Lspin0:
s_cmp_eq_u32 s57, 0
s_cbranch_scc1 .Lspun0
s_sub_u32 s57, s57, 1
s_sleep 4
buffer_load_dword v11, v8, s[92:95], s96 offen offset:384 sc1
s_waitcnt vmcnt(0)
v_cmp_eq_u32 vcc, s62, v11
s_nop 1
s_bitcmp1_b32 vcc_lo, s75
s_cbranch_scc0 .Lspin0
s_branch .Lspun0
.Lspin1:
s_cmp_eq_u32 s57, 0
s_cbranch_scc1 .Lspun1
s_sub_u32 s57, s57, 1
s_sleep 4
buffer_load_dword v11, v8, s[92:95], s96 offen offset:384 sc1
s_waitcnt vmcnt(0)
v_cmp_eq_u32 vcc, s62, v11
s_nop 1
s_bitcmp1_b32 vcc_lo, s75
s_cbranch_scc0 .Lspin1
s_branch .Lspun1
.Lspin2:
s_cmp_eq_u32 s57, 0
s_cbranch_scc1 .Lspun2
s_sub_u32 s57, s57, 1
s_sleep 4
buffer_load_dword v11, v8, s[92:95], s96 offen offset:384 sc1
s_waitcnt vmcnt(0)
v_cmp_eq_u32 vcc, s62, v11
s_nop 1
s_bitcmp1_b32 vcc_lo, s75
s_cbranch_scc0 .Lspin2
s_branch .Lspun2
.Lspin3:
s_cmp_eq_u32 s57, 0
s_cbranch_scc1 .Lspun3
s_sub_u32 s57, s57, 1
s_sleep 4
buffer_load_dword v11, v8, s[92:95], s96 offen offset:384 sc1
s_waitcnt vmcnt(0)
v_cmp_eq_u32 vcc, s62, v11
s_nop 1
s_bitcmp1_b32 vcc_lo, s75
s_cbranch_scc0 .Lspin3
s_branch .Lspun3
.Lspin4:
s_cmp_eq_u32 s57, 0
s_cbranch_scc1 .Lspun4
s_sub_u32 s57, s57, 1
s_sleep 4
buffer_load_dword v11, v8, s[92:95], s96 offen offset:384 sc1
s_waitcnt vmcnt(0)
v_cmp_eq_u32 vcc, s62, v11
s_nop 1
s_bitcmp1_b32 vcc_lo, s75
s_cbranch_scc0 .Lspin4
s_branch .Lspun4
.Lspin5:
s_cmp_eq_u32 s57, 0
s_cbranch_scc1 .Lspun5
s_sub_u32 s57, s57, 1
s_sleep 4
buffer_load_dword v11, v8, s[92:95], s96 offen offset:384 sc1
s_waitcnt vmcnt(0)
v_cmp_eq_u32 vcc, s62, v11
s_nop 1
s_bitcmp1_b32 vcc_lo, s75
s_cbranch_scc0 .Lspin5
s_branch .Lspun5
.Lspin6:
s_cmp_eq_u32 s57, 0
s_cbranch_scc1 .Lspun6
s_sub_u32 s57, s57, 1
s_sleep 4
buffer_load_dword v11, v8, s[92:95], s96 offen offset:384 sc1
s_waitcnt vmcnt(0)
v_cmp_eq_u32 vcc, s62, v11
s_nop 1
s_bitcmp1_b32 vcc_lo, s86
s_cbranch_scc0 .Lspin6
s_branch .Lspun6
.Lretry:
s_cmp_eq_u32 s57, 0
s_cbranch_scc1 .Lgood
s_sub_u32 s57, s57, 1
v_max_u32 v10, v136, v137
v_max3_u32 v10, v10, v138, v139
v_max3_u32 v10, v10, v140, v141
v_max3_u32 v10, v10, v142, v143
v_max3_u32 v10, v10, v144, v145
v_max3_u32 v10, v10, v146, v147
v_max3_u32 v10, v10, v148, v149
v_max3_u32 v10, v10, v150, v151
v_max3_u32 v10, v10, v152, v153
v_max3_u32 v10, v10, v154, v155
v_cmp_eq_u32 vcc, -1, v10
s_nop 4
s_cbranch_vccz .Lgood
s_nop 7
s_nop 7
S_ s53,s80,4096
buffer_load_dwordx4 v[136:139], v6, s[28:31], s80 offen offset:0 sc1
buffer_load_dwordx4 v[140:143], v6, s[28:31], s80 offen offset:1024 sc1
buffer_load_dwordx4 v[144:147], v6, s[28:31], s80 offen offset:2048 sc1
buffer_load_dwordx4 v[148:151], v6, s[28:31], s80 offen offset:3072 sc1
buffer_load_dwordx4 v[152:155], v6, s[28:31], s53 offen offset:0 sc1
s_waitcnt vmcnt(0)
s_branch .Lsel6
.Ldone:
s_waitcnt vmcnt(0)
.purgem S_
.purgem W_
.purgem L_
.purgem X_
.purgem R_
.purgem Ta
.purgem Da
.purgem Zvaa
.purgem Mvaa
.purgem U_
.purgem C_
.purgem Ln
.purgem Dv
.purgem Zvvv
.purgem Mvvv
.purgem B_
.purgem E_
.purgem Nvvv
.purgem Tv
.purgem Zaav
.purgem Zavv
.purgem A_
.purgem Mavv
.purgem Zvav
.purgem Mvav
.purgem Y_
.purgem Zaaa
.purgem Maaa
.purgem Maav
.purgem Nvav
)A"
    : "+v"(tid)
    : "s"(__builtin_amdgcn_kernarg_segment_ptr()), "s"(blockIdx.x)
    : "v1", "v2", "v3", "v4", "v5", "v6", "v7", "v8", "v9", "v10", "v11", "v12", "v13", "v14", "v15", "v16", "v17", "v18", "v19", "v20", "v21", "v22", "v23", "v24", "v25", "v26", "v27", "v28", "v29", "v30", "v31", "v32", "v33", "v34", "v35", "v36", "v37", "v38", "v39", "v40", "v41", "v42", "v43", "v44", "v45", "v46", "v47", "v48", "v49", "v50", "v51", "v52", "v53", "v54", "v55", "v56", "v57", "v58", "v59", "v60", "v61", "v62", "v63", "v64", "v65", "v66", "v67", "v68", "v69", "v70", "v71", "v72", "v73", "v74", "v75", "v76", "v77", "v78", "v79", "v80", "v81", "v82", "v83", "v84", "v85", "v86", "v87", "v88", "v89", "v90", "v91", "v92", "v93", "v94", "v95", "v96", "v97", "v98", "v99", "v100", "v101", "v102", "v103", "v104", "v105", "v106", "v107", "v108", "v109", "v110", "v111", "v112", "v113", "v114", "v115", "v116", "v117", "v118", "v119", "v120", "v121", "v122", "v123", "v124", "v125", "v126", "v127", "v128", "v129", "v130", "v131", "v132", "v133", "v134", "v135", "v136", "v137", "v138", "v139", "v140", "v141", "v142", "v143", "v144", "v145", "v146", "v147", "v148", "v149", "v150", "v151", "v152", "v153", "v154", "v155", "v156", "v157", "v158", "v159", "v160", "v161", "v162", "v163", "v164", "v165", "v166", "v167", "v168", "v169", "v170", "v171", "v172", "v173", "v174", "v175", "v176", "v177", "v178", "v179", "v180", "v181", "v182", "v183", "v184", "v185", "v186", "v187", "v188", "v189", "v190", "v191", "v192", "v193", "v194", "v195", "v196", "v197", "v198", "v199", "v200", "v201", "v202", "v203", "v204", "v205", "v206", "v207", "v208", "v209", "v210", "v211", "v212", "v213", "v214", "v215", "v216", "v217", "v218", "v219", "v220", "v221", "v222", "v223", "v224", "v225", "v226", "v227", "v228", "v229", "v230", "v231", "v232", "v233", "v234", "v235", "v236", "v237", "v238", "v239", "v240", "v241", "v242", "v243", "v244", "v245", "v246", "v247", "v248", "v249", "v250", "v251", "v252", "v253", "v254", "v255", "a0", "a1", "a2", "a3", "a4", "a5", "a6", "a7", "a8", "a9", "a10", "a11", "a12", "a13", "a14", "a15", "a16", "a17", "a18", "a19", "a20", "a21", "a22", "a23", "a24", "a25", "a26", "a27", "a28", "a29", "a30", "a31", "a32", "a33", "a34", "a35", "a36", "a37", "a38", "a39", "a40", "a41", "a42", "a43", "a44", "a45", "a46", "a47", "a48", "a49", "a50", "a51", "a52", "a53", "a54", "a55", "a56", "a57", "a58", "a59", "a60", "a61", "a62", "a63", "a64", "a65", "a66", "a67", "a68", "a69", "a70", "a71", "a72", "a73", "a74", "a75", "a76", "a77", "a78", "a79", "a80", "a81", "a82", "a83", "a84", "a85", "a86", "a87", "a88", "a89", "a90", "a91", "a92", "a93", "a94", "a95", "a96", "a97", "a98", "a99", "a100", "a101", "a102", "a103", "a104", "a105", "a106", "a107", "a108", "a109", "a110", "a111", "a112", "a113", "a114", "a115", "a116", "a117", "a118", "a119", "a120", "a121", "a122", "a123", "a124", "a125", "a126", "a127", "a128", "a129", "a130", "a131", "a132", "a133", "a134", "a135", "a136", "a137", "a138", "a139", "a140", "a141", "a142", "a143", "a144", "a145", "a146", "a147", "a148", "a149", "a150", "a151", "a152", "a153", "a154", "a155", "a156", "a157", "a158", "a159", "a160", "a161", "a162", "a163", "a164", "a165", "a166", "a167", "a168", "a169", "a170", "a171", "a172", "a173", "a174", "a175", "a176", "a177", "a178", "a179", "a180", "a181", "a182", "a183", "a184", "a185", "a186", "a187", "a188", "a189", "a190", "a191", "a192", "a193", "a194", "a195", "a196", "a197", "a198", "a199", "a200", "a201", "a202", "a203", "a204", "a205", "a206", "a207", "a208", "a209", "a210", "a211", "a212", "a213", "a214", "a215", "a216", "a217", "a218", "a219", "a220", "a221", "a222", "a223", "a224", "a225", "a226", "a227", "a228", "a229", "a230", "a231", "a232", "a233", "a234", "a235", "a236", "a237", "a238", "a239", "a240", "a241", "a242", "a243", "a244", "a245", "a246", "a247", "a248", "a249", "a250", "a251", "a252", "a253", "a254", "a255", "s3", "s4", "s5", "s6", "s7", "s8", "s9", "s10", "s11", "s12", "s13", "s14", "s15", "s16", "s17", "s18", "s19", "s20", "s21", "s22", "s23", "s24", "s25", "s26", "s27", "s28", "s29", "s30", "s31", "s32", "s33", "s34", "s35", "s36", "s37", "s38", "s39", "s40", "s41", "s42", "s43", "s44", "s45", "s46", "s47", "s48", "s49", "s50", "s51", "s52", "s53", "s54", "s55", "s56", "s57", "s58", "s59", "s60", "s61", "s62", "s63", "s64", "s65", "s66", "s67", "s68", "s69", "s70", "s71", "s72", "s73", "s74", "s75", "s76", "s77", "s78", "s79", "s80", "s81", "s82", "s83", "s84", "s85", "s86", "s87", "s88", "s89", "s90", "s91", "s92", "s93", "s94", "s95", "s96", "s97", "s98", "s99", "s100", "s101", "vcc", "scc", "memory");
}

extern "C" void kernel_launch(void* const* d_in, const int* in_sizes, int n_in, void* d_out, int out_size, void* d_ws, size_t ws_size, hipStream_t stream) {
  Args P;
  for (int br = 0; br < 2; ++br) {
    P.p[br][0] = d_in[5 * br]; P.p[br][1] = d_in[5 * br + 3]; P.p[br][2] = d_in[5 * br + 1]; P.p[br][3] = d_in[5 * br + 2]; P.p[br][4] = d_in[5 * br + 4];
    P.p[br][5] = d_ws; P.p[br][6] = (char*)d_out + br * 1280; P.p[br][7] = d_ws;
  }
  (void)hipFuncSetAttribute((const void*)fused_kernel, hipFuncAttributeMaxDynamicSharedMemorySize, LDS1);
  fused_kernel<<<256, 256, LDS1, stream>>>(P);
}
```

```cpp
#include <hip/hip_runtime.h>
#include <hip/hip_bf16.h>

constexpr unsigned LDS1 = 160 * 1024;

struct Args { const void* p[2][8]; };

__global__ __launch_bounds__(256, 1) void fused_kernel(Args P) {
  unsigned tid = threadIdx.x;
  asm volatile(
R"A(.macro S_ a,b,c
s_add_u32 \a, \b, \c
.endm
.macro W_ a
s_waitcnt lgkmcnt(\a)
.endm
.macro L_ x,r,s,o
buffer_load_dwordx4 \x, s[\r:\r+3], \s offen offset:\o lds
.endm
.macro X_ a,b
v_accvgpr_write_b32 \a, \b
.endm
.macro R_ a,b
v_accvgpr_read_b32 \a, \b
.endm
.macro Ta d,x,o
ds_read_b64_tr_b16 a[\d:\d+1], \x offset:\o
.endm
.macro Da d,x,o
ds_read_b128 a[\d:\d+3], \x offset:\o
.endm
.macro Zvaa d,a,b
v_mfma_f32_16x16x32_bf16 v[\d:\d+3], a[\a:\a+3], a[\b:\b+3], 0
.endm
.macro Mvaa d,a,b
v_mfma_f32_16x16x32_bf16 v[\d:\d+3], a[\a:\a+3], a[\b:\b+3], v[\d:\d+3]
.endm
.macro U_ a,b,c
v_mul_f32 \a, \b, \c
.endm
.macro C_ d,a,b
v_cvt_pk_bf16_f32 v\d, v\a, v\b
.endm
.macro Ln x,r,s,o
buffer_load_dwordx4 \x, s[\r:\r+3], \s offen offset:\o nt lds
.endm
.macro Dv d,x,o
ds_read_b128 v[\d:\d+3], \x offset:\o
.endm
.macro Zvvv d,a,b
v_mfma_f32_16x16x32_bf16 v[\d:\d+3], v[\a:\a+3], v[\b:\b+3], 0
.endm
.macro Mvvv d,a,b
v_mfma_f32_16x16x32_bf16 v[\d:\d+3], v[\a:\a+3], v[\b:\b+3], v[\d:\d+3]
.endm
.macro B_ a,b,c
v_sub_f32 \a, \b, \c
.endm
.macro E_ r
v_exp_f32 v\r, v\r
.endm
.macro Nvvv d,a,b,c
v_mfma_f32_16x16x32_bf16 v[\d:\d+3], v[\a:\a+3], v[\b:\b+3], v[\c:\c+3]
.endm
.macro Tv d,x,o
ds_read_b64_tr_b16 v[\d:\d+1], \x offset:\o
.endm
.macro Zaav d,a,b
v_mfma_f32_16x16x32_bf16 a[\d:\d+3], a[\a:\a+3], v[\b:\b+3], 0
.endm
.macro Zavv d,a,b
v_mfma_f32_16x16x32_bf16 a[\d:\d+3], v[\a:\a+3], v[\b:\b+3], 0
.endm
.macro A_ a,b,c
v_add_f32 \a, \b, \c
.endm
.macro Mavv d,a,b
v_mfma_f32_16x16x32_bf16 a[\d:\d+3], v[\a:\a+3], v[\b:\b+3], a[\d:\d+3]
.endm
.macro Zvav d,a,b
v_mfma_f32_16x16x32_bf16 v[\d:\d+3], a[\a:\a+3], v[\b:\b+3], 0
.endm
.macro Mvav d,a,b
v_mfma_f32_16x16x32_bf16 v[\d:\d+3], a[\a:\a+3], v[\b:\b+3], v[\d:\d+3]
.endm
.macro Y_ r,o
ds_write_addtid_b32 v\r offset:\o
.endm
.macro Zaaa d,a,b
v_mfma_f32_16x16x32_bf16 a[\d:\d+3], a[\a:\a+3], a[\b:\b+3], 0
.endm
.macro Maaa d,a,b
v_mfma_f32_16x16x32_bf16 a[\d:\d+3], a[\a:\a+3], a[\b:\b+3], a[\d:\d+3]
.endm
.macro Maav d,a,b
v_mfma_f32_16x16x32_bf16 a[\d:\d+3], a[\a:\a+3], v[\b:\b+3], a[\d:\d+3]
.endm
.macro Nvav d,a,b,c
v_mfma_f32_16x16x32_bf16 v[\d:\d+3], a[\a:\a+3], v[\b:\b+3], v[\c:\c+3]
.endm
s_and_b32 s3, %2, 4
s_lshl_b32 s3, s3, 4
s_load_dwordx16 s[8:23], %1, s3
s_memrealtime s[100:101]
s_memtime s[90:91]
v_readfirstlane_b32 s4, %0
v_and_b32 v16, 63, %0
v_lshlrev_b32 v10, 4, v16
s_mov_b32 s7, %2
s_lshr_b32 s4, s4, 6
.Lcode0:
s_getpc_b64 s[44:45]
s_mov_b32 s46, .Ldone-.Lcode0
s_mov_b32 s47, 0x00020000
s_lshr_b32 s66, s7, 3
s_lshl_b32 s66, s66, 2
S_ s66,s66,s4
s_lshl_b32 s66, s66, 11
v_min_u32 v40, 15, v16
v_lshlrev_b32 v40, 7, v40
buffer_load_dword v247, v40, s[44:47], s66 offen
s_and_b32 s3, s7, 1
s_lshl_b32 s3, s3, 3
s_lshr_b32 s53, s7, 5
S_ s3,s3,s53
s_and_b32 s6, s7, 2
s_lshl_b32 s6, s6, 1
s_bfe_u32 s53, s7, 0x20003
S_ s6,s6,s53
s_and_b32 s5, s7, 4
s_lshl_b32 s5, s5, 2
S_ s5,s5,s3
s_mul_i32 s50, s4, 5120
s_mul_i32 s60, s4, 51200
s_lshr_b32 s77, s4, 1
s_and_b32 s53, s4, 1
s_mul_i32 s59, s53, 393216
s_lshl_b32 s52, s53, 11
s_mul_i32 s61, s53, 6144
s_mul_i32 s51, s77, 61440
S_ s55,s51,40960
S_ s51,s55,s61
s_sub_u32 s52, s51, s52
s_lshl_b32 s86, s53, 2
s_mul_i32 s56, s53, 10240
v_lshrrev_b32 v17, 3, v16
v_and_b32 v18, 7, v16
v_lshrrev_b32 v23, 4, v16
v_and_b32 v24, 1, v23
v_lshlrev_b32 v24, 2, v24
v_xor_b32 v24, v18, v24
v_mul_u32_u24 v6, 12288, v17
v_lshl_add_u32 v6, v24, 4, v6
v_add_u32 v24, s86, v23
v_xor_b32 v24, v18, v24
v_mul_u32_u24 v4, 1280, v17
v_lshl_add_u32 v4, v24, 4, v4
v_xor_b32 v5, 64, v4
s_getreg_b32 s53, hwreg(HW_REG_XCC_ID, 0, 4)
s_bitcmp1_b32 0xaaaa, s53
s_cselect_b32 s58, 2, 0
s_cselect_b32 s89, 1, 0
s_cselect_b32 s53, 768, 0
S_ s59,s59,s53
s_lshl_b32 s54, s6, 2
S_ s54,s54,s77
s_mul_i32 s86, s54, 384
s_mul_i32 s87, s3, 204800
s_lshl_b32 s53, s3, 5
S_ s53,s53,s54
s_mul_i32 s85, s53, 196608
s_mov_b32 s30, 7853184
s_mov_b32 s31, 0x00020000
s_mov_b32 s26, 204800
s_mov_b32 s27, 0x00020000
W_ 0
s_mov_b32 s98, s100
s_mov_b32 s99, 0
s_mov_b32 s88, s90
S_ s28,s10,s86
s_addc_u32 s29, s11, 0
S_ s24,s8,s87
s_addc_u32 s25, s9, 0
s_buffer_load_dword s67, s[24:27], s60
s_mul_i32 s64, s66, 30
s_buffer_load_dword s67, s[28:31], s64
s_mov_b32 s11, 0
S_ s65,s59,0
S_ m0,s52,8192
s_mov_b64 s[92:93], s[18:19]
s_mov_b32 s53, 251658240
L_ v6,28,s65,0
S_ s65,s59,98304
S_ m0,s52,9216
S_ s92,s92,s53
s_addc_u32 s93, s93, 0
L_ v6,28,s65,0
S_ s65,s59,196608
S_ m0,s52,10240
s_mov_b32 s94, 131072
s_mov_b32 s95, 0x00020000
L_ v6,28,s65,0
S_ s65,s59,294912
S_ m0,s52,11264
s_lshl_b32 s96, s5, 3
S_ s96,s96,s6
L_ v6,28,s65,0
S_ s65,s60,0
S_ m0,s50,0
s_lshl_b32 s96, s96, 9
v_cmp_gt_u32 vcc, 8, v16
L_ v4,24,s65,0
S_ s65,s60,10240
S_ m0,s50,1024
s_lshl_b32 s54, s5, 12
v_lshl_add_u32 v24, v16, 9, s54
L_ v5,24,s65,0
S_ s65,s60,20480
S_ m0,s50,2048
v_mov_b32 v12, 0x7fff0000
v_cndmask_b32 v12, v12, v24, vcc
L_ v4,24,s65,0
S_ s65,s60,30720
S_ m0,s50,3072
s_lshl_b32 s54, s6, 2
v_add_u32 v13, s54, v12
L_ v5,24,s65,0
S_ s65,s60,40960
S_ m0,s50,4096
v_lshrrev_b32 v19, 2, v10
v_mul_u32_u24 v25, 384, v17
L_ v4,24,s65,0
s_load_dword s67, s[12:13], s85
s_load_dword s67, s[14:15], s85
S_ s64,s85,393216
s_load_dword s67, s[12:13], s64
s_load_dword s67, s[14:15], s64
s_lshl_b32 s64, s6, 2
S_ s64,s64,s4
s_mul_i32 s64, s64, 245760
s_load_dword s67, s[16:17], s64
s_cmp_eq_u32 s4, 0
s_cbranch_scc0 .Lhs0a
buffer_load_dword v20, v19, s[92:95], s96 offen offset:128 sc1
buffer_load_dword v21, v19, s[92:95], s96 offen offset:384 sc1
buffer_load_dword a252, v19, s[92:95], s96 offen offset:256 sc1
buffer_load_dword a253, v13, s[92:95], 0 offen offset:256 sc1
buffer_load_dword v22, off, s[92:95], s96 offset:0 sc1
.Lhs0a:
S_ s65,s59,0
S_ m0,s52,16256
v_xor_b32 v24, v18, v23
v_lshl_add_u32 v7, v24, 4, v25
v_lshlrev_b32 v24, 1, v23
v_xor_b32 v24, v18, v24
v_lshl_add_u32 v9, v24, 4, v25
v_and_b32 v26, 15, v16
v_lshrrev_b32 v27, 2, v26
v_and_b32 v28, 3, v16
v_lshrrev_b32 v24, 1, v26
v_xor_b32 v24, v23, v24
L_ v6,28,s65,128
S_ s65,s59,98304
S_ m0,s52,17280
v_lshlrev_b32 v24, 4, v24
v_lshl_add_u32 v24, v18, 7, v24
v_lshrrev_b32 v25, 3, v26
v_lshl_add_u32 v2, v25, 10, v24
v_add_u32 v2, s56, v2
v_mul_u32_u24 v1, 3072, v25
v_add3_u32 v1, v1, v24, s55
v_lshlrev_b32 v24, 2, v25
v_xor_b32 v24, v28, v24
v_lshlrev_b32 v24, 4, v24
L_ v6,28,s65,128
S_ s65,s59,196608
S_ m0,s52,18304
v_lshl_add_u32 v24, v27, 7, v24
v_lshl_add_u32 %0, v23, 10, v24
v_add_u32 %0, s55, %0
v_and_b32 v29, 1, v23
v_lshl_add_u32 v24, v29, 1, v25
v_lshlrev_b32 v24, 1, v24
v_lshrrev_b32 v30, 1, v28
v_xor_b32 v24, v30, v24
v_lshlrev_b32 v24, 4, v24
v_lshl_add_u32 v29, v29, 2, v27
L_ v6,28,s65,128
S_ s65,s59,294912
S_ m0,s52,19328
v_lshl_add_u32 v24, v29, 7, v24
v_and_b32 v29, 1, v28
v_lshl_add_u32 v24, v29, 3, v24
v_lshrrev_b32 v29, 1, v23
v_mul_u32_u24 v3, 3072, v29
v_add3_u32 v3, v3, v24, s55
X_ a242,%0
X_ a244,v1
X_ a243,v2
X_ a245,v3
L_ v6,28,s65,128
S_ s65,s59,0
S_ m0,s52,24320
X_ a240,v4
X_ a248,v10
X_ a241,v12
X_ a251,v13
v_xor_b32 v1, 64, %0
v_xor_b32 v3, 64, v2
v_xor_b32 v8, 64, v7
S_ s32,s12,s85
s_addc_u32 s33, s13, 0
s_mov_b32 s34, 589824
L_ v6,28,s65,256
S_ s65,s59,98304
S_ m0,s52,25344
s_mov_b32 s35, 0x00020000
S_ s36,s14,s85
s_addc_u32 s37, s15, 0
s_mov_b32 s38, 589824
s_mov_b32 s39, 0x00020000
s_lshl_b32 s87, s87, 1
S_ s20,s20,s87
s_addc_u32 s21, s21, 0
s_mov_b32 s57, 20000
s_mov_b32 s48, 1037384597
L_ v6,28,s65,256
S_ s65,s59,196608
S_ m0,s52,26368
s_mov_b32 s49, 1037384597
s_mul_i32 s53, s5, 1638400
S_ s53,s53,134217728
S_ s22,s18,s53
s_addc_u32 s23, s19, 0
s_lshl_b32 s53, s6, 3
s_lshr_b32 s54, s7, 5
S_ s53,s53,s54
s_lshl_b32 s53, s53, 2
S_ s53,s53,s4
L_ v6,28,s65,256
S_ s65,s59,294912
S_ m0,s52,27392
s_mul_i32 s87, s53, 30720
s_lshl_b32 s53, s6, 2
S_ s53,s53,s77
s_mul_i32 s72, s53, 245760
s_and_b32 s54, s4, 1
s_mul_i32 s53, s54, 61440
S_ s72,s72,s53
s_lshl_b32 s53, s77, 6
s_xor_b32 s83, s53, 64
s_xor_b32 s53, s4, 2
L_ v6,28,s65,256
S_ s65,s60,128
S_ m0,s50,20480
s_mul_i32 s75, s53, 10240
s_cmp_eq_u32 s4, 0
s_cselect_b32 s73, 0, 2147418112
s_mul_i32 s53, s4, 5120
s_mul_i32 s78, s6, 204800
S_ s78,s78,s53
s_and_b32 s84, s6, 7
s_mul_i32 s84, s84, 20480
S_ s84,s84,s53
s_mul_i32 s53, s6, 5120
L_ v4,24,s65,0
S_ s65,s60,10368
S_ m0,s50,21504
S_ s53,s53,163840
s_lshl_b32 s54, s4, 10
S_ s74,s53,s54
S_ s53,s53,4096
s_cmp_eq_u32 s4, 0
s_cselect_b32 s82, s53, 4026531840
s_and_b32 s53, s4, 1
s_mul_i32 s53, s53, 204800
s_lshl_b32 s54, s6, 7
S_ s53,s53,s54
L_ v5,24,s65,0
S_ s65,s60,20608
S_ m0,s50,22528
s_lshl_b32 s54, s77, 6
S_ s8,s53,s54
s_and_b32 s53, s6, 1
s_mul_i32 s53, s53, 204800
s_lshr_b32 s54, s6, 1
s_lshl_b32 s54, s54, 6
S_ s53,s53,s54
S_ s53,s53,1024
s_mul_i32 s54, s4, 40960
S_ s9,s53,s54
L_ v4,24,s65,0
S_ s65,s60,30848
S_ m0,s50,23552
S_ s53,s53,163840
s_cmp_eq_u32 s4, 0
s_cselect_b32 s10, s53, 4026531840
R_ v12,a248
v_lshrrev_b32 v12, 4, v12
v_min_u32 v10, 11, v12
s_mul_i32 s56, s4, 12
v_add_u32 v10, s56, v10
v_mul_u32_u24 v11, 43691, v10
v_lshrrev_b32 v11, 18, v11
L_ v5,24,s65,0
S_ s65,s60,41088
S_ m0,s50,24576
v_lshlrev_b32 v10, 7, v10
v_mul_u32_u24 v11, 11520, v11
v_add_u32 v10, v10, v11
v_min_u32 v12, 9, v12
s_and_b32 s56, s6, 3
s_lshl_b32 s56, s56, 2
S_ s56,s56,s4
s_mul_i32 s56, s56, 10
v_add_u32 v12, s56, v12
v_mul_u32_u24 v12, 1280, v12
L_ v4,24,s65,0
S_ s65,s59,786432
S_ m0,s52,0
s_lshr_b32 s56, s7, 5
s_mul_i32 s56, s56, 98304
s_and_b32 s55, s59, 4095
S_ s56,s56,s55
s_mul_i32 s55, s77, 384
s_sub_u32 s56, s56, s55
s_mov_b32 s63, 0
L_ v6,28,s65,0
S_ s65,s59,884736
S_ m0,s52,1024
s_nop 0
L_ v6,28,s65,0
S_ s65,s59,983040
S_ m0,s52,2048
s_nop 0
L_ v6,28,s65,0
S_ s65,s59,1081344
S_ m0,s52,3072
s_nop 0
L_ v6,28,s65,0
S_ s55,s56,786432
buffer_load_dword a254, v10, s[28:31], s55 offen
S_ s55,s56,1572864
buffer_load_dword a254, v10, s[28:31], s55 offen
buffer_load_dword a254, v12, s[24:27], s63 offen offset:256
S_ s55,s56,2359296
buffer_load_dword a254, v10, s[28:31], s55 offen
buffer_load_dword a254, v12, s[24:27], s63 offen offset:384
s_waitcnt vmcnt(22) lgkmcnt(0)
s_barrier
s_memtime s[90:91]
s_memrealtime s[68:69]
Ta 20,%0,8192
Ta 22,%0,8704
Ta 24,%0,12288
Ta 26,%0,12800
Da 60,v2,0
Da 100,v3,0
Da 64,v2,2048
Da 104,v3,2048
Da 68,v2,4096
Da 108,v3,4096
Da 72,v2,6144
Da 112,v3,6144
Da 76,v2,8192
Da 116,v3,8192
Ta 28,%0,8200
Ta 30,%0,8712
Ta 32,%0,12296
Ta 34,%0,12808
s_cmp_eq_u32 s4, 0
s_cbranch_scc0 .Lhs0b
v_readfirstlane_b32 s62, v22
.Lhs0l:
s_nop 3
S_ s62,s62,1
v_cmp_eq_u32 vcc, s62, v20
s_nop 1
s_or_b32 s53, vcc_lo, vcc_hi
v_cmp_eq_u32 vcc, s62, v21
s_nop 1
s_or_b32 s54, vcc_lo, vcc_hi
s_or_b32 s53, s53, s54
s_cbranch_scc1 .Lhs0l
v_mov_b32 v22, s62
s_mov_b64 exec, 1
s_nop 1
buffer_store_dword v22, off, s[92:95], s96 sc1
s_mov_b64 exec, -1
s_nop 1
.Lhs0b:
s_cmp_eq_u32 s58, 0
s_cbranch_scc1 .Lq
.Lqy:
W_ 13
Zvaa 16,20,60
s_nop 0
Ta 36,v1,8192
Ta 38,v1,8704
W_ 14
Mvaa 16,24,100
R_ v12,a248
W_ 13
Zvaa 24,20,64
v_lshrrev_b32 v12, 4, v12
W_ 12
Mvaa 24,24,104
s_nop 0
Ta 224,v1,12288
Ta 226,v1,12800
W_ 13
Zvaa 32,20,68
v_min_u32 v10, 11, v12
W_ 12
Mvaa 32,24,108
s_mul_i32 s56, s4, 12
W_ 11
Zvaa 40,20,72
v_add_u32 v10, s56, v10
W_ 10
Mvaa 40,24,112
v_mul_u32_u24 v11, 43691, v10
W_ 9
Zvaa 48,20,76
v_lshrrev_b32 v11, 18, v11
W_ 8
Mvaa 48,24,116
W_ 4
v_lshlrev_b32 v10, 7, v10
Zvaa 52,32,116
s_nop 0
Ta 20,v1,8200
Ta 22,v1,8712
Mvaa 52,28,76
v_mul_u32_u24 v11, 11520, v11
Zvaa 44,32,112
v_add_u32 v10, v10, v11
Mvaa 44,28,72
s_nop 0
Ta 24,v1,12296
Ta 26,v1,12808
Zvaa 36,32,108
v_min_u32 v12, 9, v12
Mvaa 36,28,68
s_and_b32 s56, s6, 3
Zvaa 28,32,104
s_lshl_b32 s56, s56, 2
Mvaa 28,28,64
S_ s56,s56,s4
Zvaa 20,32,100
s_waitcnt vmcnt(18) lgkmcnt(0)
s_barrier
s_mul_i32 s56, s56, 10
Mvaa 20,28,60
S_ s65,s59,786432
v_add_u32 v12, s56, v12
Zvaa 56,36,60
S_ m0,s52,8064
Ta 28,%0,16384
Ta 30,%0,16896
Mvaa 56,224,100
L_ v6,28,s65,128
Zvaa 64,36,64
S_ s65,s59,884736
v_mul_u32_u24 v12, 1280, v12
Mvaa 64,224,104
S_ m0,s52,9088
Ta 32,%0,20480
Ta 34,%0,20992
Zvaa 72,36,68
s_lshr_b32 s56, s7, 5
Mvaa 72,224,108
L_ v6,28,s65,128
Zvaa 80,36,72
S_ s65,s59,983040
s_mul_i32 s56, s56, 98304
Mvaa 80,224,112
S_ m0,s52,10112
s_and_b32 s55, s59, 4095
Zvaa 88,36,76
S_ s56,s56,s55
Mvaa 88,224,116
s_mul_i32 s55, s77, 384
Zvaa 92,24,116
s_sub_u32 s56, s56, s55
Ta 36,%0,16392
Ta 38,%0,16904
Mvaa 92,20,76
L_ v6,28,s65,128
Zvaa 84,24,112
S_ s65,s59,1081344
s_mov_b32 s63, 0
Mvaa 84,20,72
S_ m0,s52,11136
Ta 224,%0,20488
Ta 226,%0,21000
Zvaa 76,24,108
S_ s55,s56,3145728
Mvaa 76,20,68
L_ v6,28,s65,128
Zvaa 68,24,104
S_ s65,s60,256
Mvaa 68,20,64
S_ m0,s50,0
Zvaa 60,24,100
Mvaa 60,20,60
W_ 4
Zvaa 96,28,60
s_nop 0
Ta 20,v1,16384
Ta 22,v1,16896
Mvaa 96,32,100
L_ v4,24,s65,0
Zvaa 104,28,64
S_ s65,s60,10496
Mvaa 104,32,104
S_ m0,s50,1024
Ta 24,v1,20480
Ta 26,v1,20992
Zvaa 112,28,68
Mvaa 112,32,108
L_ v5,24,s65,0
Zvaa 120,28,72
S_ s65,s60,20736
Mvaa 120,32,112
S_ m0,s50,2048
Zvaa 128,28,76
Mvaa 128,32,116
W_ 4
Zvaa 132,224,116
s_nop 0
Ta 28,v1,16392
Ta 30,v1,16904
Mvaa 132,36,76
L_ v4,24,s65,0
Zvaa 124,224,112
S_ s65,s60,30976
Mvaa 124,36,72
S_ m0,s50,3072
Ta 32,v1,20488
Ta 34,v1,21000
Zvaa 116,224,108
Mvaa 116,36,68
L_ v5,24,s65,0
Zvaa 108,224,104
S_ s65,s60,41216
Mvaa 108,36,64
S_ m0,s50,4096
Zvaa 100,224,100
s_waitcnt vmcnt(17) lgkmcnt(0)
s_barrier
Mvaa 100,36,60
Da 140,v2,20480
Zvaa 136,20,60
s_nop 0
Ta 36,%0,24576
Ta 38,%0,25088
Mvaa 136,24,100
L_ v4,24,s65,0
Zvaa 144,20,64
S_ s65,s59,786432
Da 180,v3,20480
Mvaa 144,24,104
S_ m0,s52,16128
Ta 224,%0,28672
Ta 226,%0,29184
Zvaa 152,20,68
Mvaa 152,24,108
Da 144,v2,22528
Zvaa 160,20,72
L_ v6,28,s65,256
Mvaa 160,24,112
S_ s65,s59,884736
Zvaa 168,20,76
S_ m0,s52,17152
Da 184,v3,22528
Mvaa 168,24,116
Zvaa 172,32,116
s_nop 0
Ta 20,%0,24584
Ta 22,%0,25096
Mvaa 172,28,76
Da 148,v2,24576
Zvaa 164,32,112
L_ v6,28,s65,256
Mvaa 164,28,72
S_ s65,s59,983040
Ta 24,%0,28680
Ta 26,%0,29192
Zvaa 156,32,108
S_ m0,s52,18176
Da 188,v3,24576
Mvaa 156,28,68
Zvaa 148,32,104
L_ v6,28,s65,256
Mvaa 148,28,64
S_ s65,s59,1081344
Da 152,v2,26624
Zvaa 140,32,100
S_ m0,s52,19200
Mvaa 140,28,60
W_ 9
Zvaa 176,36,60
s_nop 0
Ta 28,v1,24576
Ta 30,v1,25088
Mvaa 176,224,100
Da 192,v3,26624
Zvaa 184,36,64
L_ v6,28,s65,256
Mvaa 184,224,104
s_nop 0
Ta 32,v1,28672
Ta 34,v1,29184
Zvaa 192,36,68
Da 156,v2,28672
Mvaa 192,224,108
Zvaa 200,36,72
Da 196,v3,28672
Mvaa 200,224,112
buffer_load_dword a254, v10, s[28:31], s55 offen
Zvaa 208,36,76
Mvaa 208,224,116
W_ 9
Zvaa 212,24,116
s_nop 0
Ta 36,v1,24584
Ta 38,v1,25096
Mvaa 212,20,76
buffer_load_dword a254, v12, s[24:27], s63 offen offset:512
Zvaa 204,24,112
Mvaa 204,20,72
s_nop 0
Ta 224,v1,28680
Ta 226,v1,29192
Zvaa 196,24,108
Mvaa 196,20,68
Zvaa 188,24,104
Mvaa 188,20,64
Zvaa 180,24,100
s_waitcnt vmcnt(20) lgkmcnt(0)
s_barrier
Mvaa 180,20,60
S_ s65,s59,1572864
Zvaa 216,28,60
S_ m0,s52,24576
Ta 20,%0,0
Ta 22,%0,512
Mvaa 216,32,100
L_ v6,28,s65,0
Zvaa 224,28,64
S_ s65,s59,1671168
Mvaa 224,32,104
S_ m0,s52,25600
Ta 24,%0,4096
Ta 26,%0,4608
Zvaa 232,28,68
Mvaa 232,32,108
L_ v6,28,s65,0
Zvaa 240,28,72
S_ s65,s59,1769472
Mvaa 240,32,112
S_ m0,s52,26624
Zvaa 248,28,76
Mvaa 248,32,116
Zvaa 252,224,116
s_nop 0
Ta 28,%0,8
Ta 30,%0,520
Mvaa 252,36,76
L_ v6,28,s65,0
Zvaa 244,224,112
S_ s65,s59,1867776
Mvaa 244,36,72
S_ m0,s52,27648
Ta 32,%0,4104
Ta 34,%0,4616
Zvaa 236,224,108
Mvaa 236,36,68
L_ v6,28,s65,0
Zvaa 228,224,104
S_ s65,s60,384
Mvaa 228,36,64
S_ m0,s50,20480
Zvaa 220,224,100
Mvaa 220,36,60
W_ 4
Mvaa 16,20,140
s_nop 0
Ta 36,v1,0
Ta 38,v1,512
Mvaa 16,24,180
L_ v4,24,s65,0
Mvaa 24,20,144
S_ s65,s60,10624
Mvaa 24,24,184
S_ m0,s50,21504
Ta 224,v1,4096
Ta 226,v1,4608
Mvaa 32,20,148
Mvaa 32,24,188
L_ v5,24,s65,0
Mvaa 40,20,152
S_ s65,s60,20864
Mvaa 40,24,192
S_ m0,s50,22528
Mvaa 48,20,156
Mvaa 48,24,196
W_ 4
Mvaa 52,32,196
s_nop 0
Ta 20,v1,8
Ta 22,v1,520
Mvaa 52,28,156
L_ v4,24,s65,0
Mvaa 44,32,192
S_ s65,s60,31104
Mvaa 44,28,152
S_ m0,s50,23552
Ta 24,v1,4104
Ta 26,v1,4616
Mvaa 36,32,188
Mvaa 36,28,148
L_ v5,24,s65,0
Mvaa 28,32,184
S_ s65,s60,41344
Mvaa 28,28,144
S_ m0,s50,24576
Mvaa 20,32,180
s_waitcnt vmcnt(19) lgkmcnt(0)
s_barrier
Mvaa 20,28,140
L_ v4,24,s65,0
Mvaa 56,36,140
S_ s65,s59,1572864
Ta 28,%0,8192
Ta 30,%0,8704
Mvaa 56,224,180
S_ m0,s52,-128
Mvaa 64,36,144
Mvaa 64,224,184
s_nop 0
Ta 32,%0,12288
Ta 34,%0,12800
Mvaa 72,36,148
L_ v6,28,s65,128
Mvaa 72,224,188
S_ s65,s59,1671168
Mvaa 80,36,152
S_ m0,s52,896
Mvaa 80,224,192
Mvaa 88,36,156
L_ v6,28,s65,128
Mvaa 88,224,196
S_ s65,s59,1769472
Mvaa 92,24,196
S_ m0,s52,1920
Ta 36,%0,8200
Ta 38,%0,8712
Mvaa 92,20,156
S_ s55,s56,3932160
Mvaa 84,24,192
L_ v6,28,s65,128
Mvaa 84,20,152
S_ s65,s59,1867776
Ta 224,%0,12296
Ta 226,%0,12808
Mvaa 76,24,188
S_ m0,s52,2944
Mvaa 76,20,148
Mvaa 68,24,184
L_ v6,28,s65,128
Mvaa 68,20,144
Mvaa 60,24,180
Mvaa 60,20,140
W_ 4
Mvaa 96,28,140
s_nop 0
Ta 20,v1,8192
Ta 22,v1,8704
Mvaa 96,32,180
buffer_load_dword a254, v10, s[28:31], s55 offen
Mvaa 104,28,144
Mvaa 104,32,184
s_nop 0
Ta 24,v1,12288
Ta 26,v1,12800
Mvaa 112,28,148
Mvaa 112,32,188
buffer_load_dword a254, v12, s[24:27], s63 offen offset:640
Mvaa 120,28,152
Mvaa 120,32,192
Mvaa 128,28,156
Mvaa 128,32,196
W_ 4
Mvaa 132,224,196
s_nop 0
Ta 28,v1,8200
Ta 30,v1,8712
Mvaa 132,36,156
Mvaa 124,224,192
Mvaa 124,36,152
s_nop 0
Ta 32,v1,12296
Ta 34,v1,12808
Mvaa 116,224,188
Mvaa 116,36,148
Mvaa 108,224,184
Mvaa 108,36,144
Mvaa 100,224,180
s_waitcnt vmcnt(17) lgkmcnt(0)
s_barrier
Mvaa 100,36,140
S_ s65,s59,1572864
Da 60,v2,0
Mvaa 136,20,140
S_ m0,s52,7936
Ta 36,%0,16384
Ta 38,%0,16896
Mvaa 136,24,180
L_ v6,28,s65,256
Mvaa 144,20,144
S_ s65,s59,1671168
Da 100,v3,0
Mvaa 144,24,184
S_ m0,s52,8960
Ta 224,%0,20480
Ta 226,%0,20992
Mvaa 152,20,148
Mvaa 152,24,188
Da 64,v2,2048
Mvaa 160,20,152
L_ v6,28,s65,256
Mvaa 160,24,192
S_ s65,s59,1769472
Mvaa 168,20,156
S_ m0,s52,9984
Da 104,v3,2048
Mvaa 168,24,196
Mvaa 172,32,196
s_nop 0
Ta 20,%0,16392
Ta 22,%0,16904
Mvaa 172,28,156
Da 68,v2,4096
Mvaa 164,32,192
L_ v6,28,s65,256
Mvaa 164,28,152
S_ s65,s59,1867776
Ta 24,%0,20488
Ta 26,%0,21000
Mvaa 156,32,188
S_ m0,s52,11008
Da 108,v3,4096
Mvaa 156,28,148
Mvaa 148,32,184
L_ v6,28,s65,256
Mvaa 148,28,144
Da 72,v2,6144
Mvaa 140,32,180
Mvaa 140,28,140
W_ 9
Mvaa 176,36,140
s_nop 0
Ta 28,v1,16384
Ta 30,v1,16896
Mvaa 176,224,180
Da 112,v3,6144
Mvaa 184,36,144
Mvaa 184,224,184
s_nop 0
Ta 32,v1,20480
Ta 34,v1,20992
Mvaa 192,36,148
Da 76,v2,8192
Mvaa 192,224,188
Mvaa 200,36,152
Da 116,v3,8192
Mvaa 200,224,192
Mvaa 208,36,156
Mvaa 208,224,196
W_ 9
Mvaa 212,24,196
s_nop 0
Ta 36,v1,16392
Ta 38,v1,16904
Mvaa 212,20,156
Mvaa 204,24,192
Mvaa 204,20,152
s_nop 0
Ta 224,v1,20488
Ta 226,v1,21000
Mvaa 196,24,188
Mvaa 196,20,148
Mvaa 188,24,184
Mvaa 188,20,144
Mvaa 180,24,180
s_waitcnt vmcnt(15) lgkmcnt(0)
s_barrier
Mvaa 180,20,140
S_ s65,s59,2359296
Mvaa 216,28,140
S_ m0,s52,16384
Ta 20,%0,24576
Ta 22,%0,25088
Mvaa 216,32,180
L_ v6,28,s65,0
Mvaa 224,28,144
S_ s65,s59,2457600
Mvaa 224,32,184
S_ m0,s52,17408
Ta 24,%0,28672
Ta 26,%0,29184
Mvaa 232,28,148
Mvaa 232,32,188
L_ v6,28,s65,0
Mvaa 240,28,152
S_ s65,s59,2555904
Mvaa 240,32,192
S_ m0,s52,18432
Mvaa 248,28,156
Mvaa 248,32,196
Mvaa 252,224,196
s_nop 0
Ta 28,%0,24584
Ta 30,%0,25096
Mvaa 252,36,156
L_ v6,28,s65,0
Mvaa 244,224,192
S_ s65,s59,2654208
Mvaa 244,36,152
S_ m0,s52,19456
Ta 32,%0,28680
Ta 34,%0,29192
Mvaa 236,224,188
Mvaa 236,36,148
L_ v6,28,s65,0
Mvaa 228,224,184
S_ s65,s60,512
Mvaa 228,36,144
S_ m0,s50,0
Mvaa 220,224,180
Mvaa 220,36,140
W_ 4
Mvaa 16,20,60
s_nop 0
Ta 36,v1,24576
Ta 38,v1,25088
Mvaa 16,24,100
L_ v4,24,s65,0
Mvaa 24,20,64
S_ s65,s60,10752
Mvaa 24,24,104
S_ m0,s50,1024
Ta 224,v1,28672
Ta 226,v1,29184
Mvaa 32,20,68
Mvaa 32,24,108
L_ v5,24,s65,0
Mvaa 40,20,72
S_ s65,s60,20992
Mvaa 40,24,112
S_ m0,s50,2048
Mvaa 48,20,76
Mvaa 48,24,116
W_ 4
Mvaa 52,32,116
s_nop 0
Ta 20,v1,24584
Ta 22,v1,25096
Mvaa 52,28,76
L_ v4,24,s65,0
Mvaa 44,32,112
S_ s65,s60,31232
Mvaa 44,28,72
S_ m0,s50,3072
Ta 24,v1,28680
Ta 26,v1,29192
Mvaa 36,32,108
Mvaa 36,28,68
L_ v5,24,s65,0
Mvaa 28,32,104
S_ s65,s60,41472
Mvaa 28,28,64
S_ m0,s50,4096
Mvaa 20,32,100
s_waitcnt vmcnt(14) lgkmcnt(0)
s_barrier
Mvaa 20,28,60
L_ v4,24,s65,0
Mvaa 56,36,60
S_ s65,s59,2359296
Ta 28,%0,0
Ta 30,%0,512
Mvaa 56,224,100
S_ m0,s52,24448
Mvaa 64,36,64
Mvaa 64,224,104
s_nop 0
Ta 32,%0,4096
Ta 34,%0,4608
Mvaa 72,36,68
L_ v6,28,s65,128
Mvaa 72,224,108
S_ s65,s59,2457600
Mvaa 80,36,72
S_ m0,s52,25472
Mvaa 80,224,112
Mvaa 88,36,76
L_ v6,28,s65,128
Mvaa 88,224,116
S_ s65,s59,2555904
Mvaa 92,24,116
S_ m0,s52,26496
Ta 36,%0,8
Ta 38,%0,520
Mvaa 92,20,76
S_ s55,s56,4718592
Mvaa 84,24,112
L_ v6,28,s65,128
Mvaa 84,20,72
S_ s65,s59,2654208
Ta 224,%0,4104
Ta 226,%0,4616
Mvaa 76,24,108
S_ m0,s52,27520
Mvaa 76,20,68
Mvaa 68,24,104
L_ v6,28,s65,128
Mvaa 68,20,64
Mvaa 60,24,100
Mvaa 60,20,60
W_ 4
Mvaa 96,28,60
s_nop 0
Ta 20,v1,0
Ta 22,v1,512
Mvaa 96,32,100
buffer_load_dword a254, v10, s[28:31], s55 offen
Mvaa 104,28,64
Mvaa 104,32,104
s_nop 0
Ta 24,v1,4096
Ta 26,v1,4608
Mvaa 112,28,68
Mvaa 112,32,108
buffer_load_dword a254, v12, s[24:27], s63 offen offset:768
Mvaa 120,28,72
Mvaa 120,32,112
Mvaa 128,28,76
Mvaa 128,32,116
W_ 4
Mvaa 132,224,116
s_nop 0
Ta 28,v1,8
Ta 30,v1,520
Mvaa 132,36,76
Mvaa 124,224,112
Mvaa 124,36,72
s_nop 0
Ta 32,v1,4104
Ta 34,v1,4616
Mvaa 116,224,108
Mvaa 116,36,68
Mvaa 108,224,104
Mvaa 108,36,64
Mvaa 100,224,100
s_waitcnt vmcnt(15) lgkmcnt(0)
s_barrier
Mvaa 100,36,60
S_ s65,s59,2359296
Da 140,v2,20480
Mvaa 136,20,60
S_ m0,s52,-256
Ta 36,%0,8192
Ta 38,%0,8704
Mvaa 136,24,100
L_ v6,28,s65,256
Mvaa 144,20,64
S_ s65,s59,2457600
Da 180,v3,20480
Mvaa 144,24,104
S_ m0,s52,768
Ta 224,%0,12288
Ta 226,%0,12800
Mvaa 152,20,68
Mvaa 152,24,108
Da 144,v2,22528
Mvaa 160,20,72
L_ v6,28,s65,256
Mvaa 160,24,112
S_ s65,s59,2555904
Mvaa 168,20,76
S_ m0,s52,1792
Da 184,v3,22528
Mvaa 168,24,116
Mvaa 172,32,116
s_nop 0
Ta 20,%0,8200
Ta 22,%0,8712
Mvaa 172,28,76
Da 148,v2,24576
Mvaa 164,32,112
L_ v6,28,s65,256
Mvaa 164,28,72
S_ s65,s59,2654208
Ta 24,%0,12296
Ta 26,%0,12808
Mvaa 156,32,108
S_ m0,s52,2816
Da 188,v3,24576
Mvaa 156,28,68
Mvaa 148,32,104
L_ v6,28,s65,256
Mvaa 148,28,64
Da 152,v2,26624
Mvaa 140,32,100
Mvaa 140,28,60
W_ 9
Mvaa 176,36,60
s_nop 0
Ta 28,v1,8192
Ta 30,v1,8704
Mvaa 176,224,100
Da 192,v3,26624
Mvaa 184,36,64
Mvaa 184,224,104
s_nop 0
Ta 32,v1,12288
Ta 34,v1,12800
Mvaa 192,36,68
Da 156,v2,28672
Mvaa 192,224,108
Mvaa 200,36,72
Da 196,v3,28672
Mvaa 200,224,112
Mvaa 208,36,76
Mvaa 208,224,116
W_ 9
Mvaa 212,24,116
s_nop 0
Ta 36,v1,8200
Ta 38,v1,8712
Mvaa 212,20,76
Mvaa 204,24,112
Mvaa 204,20,72
s_nop 0
Ta 224,v1,12296
Ta 226,v1,12808
Mvaa 196,24,108
Mvaa 196,20,68
Mvaa 188,24,104
Mvaa 188,20,64
Mvaa 180,24,100
s_waitcnt vmcnt(15) lgkmcnt(0)
s_barrier
Mvaa 180,20,60
S_ s65,s59,3145728
Mvaa 216,28,60
S_ m0,s52,8192
Ta 20,%0,16384
Ta 22,%0,16896
Mvaa 216,32,100
L_ v6,28,s65,0
Mvaa 224,28,64
S_ s65,s59,3244032
Mvaa 224,32,104
S_ m0,s52,9216
Ta 24,%0,20480
Ta 26,%0,20992
Mvaa 232,28,68
Mvaa 232,32,108
L_ v6,28,s65,0
Mvaa 240,28,72
S_ s65,s59,3342336
Mvaa 240,32,112
S_ m0,s52,10240
Mvaa 248,28,76
Mvaa 248,32,116
Mvaa 252,224,116
s_nop 0
Ta 28,%0,16392
Ta 30,%0,16904
Mvaa 252,36,76
L_ v6,28,s65,0
Mvaa 244,224,112
S_ s65,s59,3440640
Mvaa 244,36,72
S_ m0,s52,11264
Ta 32,%0,20488
Ta 34,%0,21000
Mvaa 236,224,108
Mvaa 236,36,68
L_ v6,28,s65,0
Mvaa 228,224,104
S_ s65,s60,640
Mvaa 228,36,64
S_ m0,s50,20480
Mvaa 220,224,100
Mvaa 220,36,60
W_ 4
Mvaa 16,20,140
s_nop 0
Ta 36,v1,16384
Ta 38,v1,16896
Mvaa 16,24,180
L_ v4,24,s65,0
Mvaa 24,20,144
S_ s65,s60,10880
Mvaa 24,24,184
S_ m0,s50,21504
Ta 224,v1,20480
Ta 226,v1,20992
Mvaa 32,20,148
Mvaa 32,24,188
L_ v5,24,s65,0
Mvaa 40,20,152
S_ s65,s60,21120
Mvaa 40,24,192
S_ m0,s50,22528
Mvaa 48,20,156
Mvaa 48,24,196
W_ 4
Mvaa 52,32,196
s_nop 0
Ta 20,v1,16392
Ta 22,v1,16904
Mvaa 52,28,156
L_ v4,24,s65,0
Mvaa 44,32,192
S_ s65,s60,31360
Mvaa 44,28,152
S_ m0,s50,23552
Ta 24,v1,20488
Ta 26,v1,21000
Mvaa 36,32,188
Mvaa 36,28,148
L_ v5,24,s65,0
Mvaa 28,32,184
S_ s65,s60,41600
Mvaa 28,28,144
S_ m0,s50,24576
Mvaa 20,32,180
s_waitcnt vmcnt(14) lgkmcnt(0)
s_barrier
Mvaa 20,28,140
L_ v4,24,s65,0
Mvaa 56,36,140
S_ s65,s59,3145728
Ta 28,%0,24576
Ta 30,%0,25088
Mvaa 56,224,180
S_ m0,s52,16256
Mvaa 64,36,144
Mvaa 64,224,184
s_nop 0
Ta 32,%0,28672
Ta 34,%0,29184
Mvaa 72,36,148
L_ v6,28,s65,128
Mvaa 72,224,188
S_ s65,s59,3244032
Mvaa 80,36,152
S_ m0,s52,17280
Mvaa 80,224,192
Mvaa 88,36,156
L_ v6,28,s65,128
Mvaa 88,224,196
S_ s65,s59,3342336
Mvaa 92,24,196
S_ m0,s52,18304
Ta 36,%0,24584
Ta 38,%0,25096
Mvaa 92,20,156
S_ s55,s56,5505024
Mvaa 84,24,192
L_ v6,28,s65,128
Mvaa 84,20,152
S_ s65,s59,3440640
Ta 224,%0,28680
Ta 226,%0,29192
Mvaa 76,24,188
S_ m0,s52,19328
Mvaa 76,20,148
Mvaa 68,24,184
L_ v6,28,s65,128
Mvaa 68,20,144
Mvaa 60,24,180
Mvaa 60,20,140
W_ 4
Mvaa 96,28,140
s_nop 0
Ta 20,v1,24576
Ta 22,v1,25088
Mvaa 96,32,180
buffer_load_dword a254, v10, s[28:31], s55 offen
Mvaa 104,28,144
Mvaa 104,32,184
s_nop 0
Ta 24,v1,28672
Ta 26,v1,29184
Mvaa 112,28,148
Mvaa 112,32,188
buffer_load_dword a254, v12, s[24:27], s63 offen offset:896
Mvaa 120,28,152
Mvaa 120,32,192
Mvaa 128,28,156
Mvaa 128,32,196
W_ 4
Mvaa 132,224,196
s_nop 0
Ta 28,v1,24584
Ta 30,v1,25096
Mvaa 132,36,156
Mvaa 124,224,192
Mvaa 124,36,152
s_nop 0
Ta 32,v1,28680
Ta 34,v1,29192
Mvaa 116,224,188
Mvaa 116,36,148
Mvaa 108,224,184
Mvaa 108,36,144
Mvaa 100,224,180
s_waitcnt vmcnt(15) lgkmcnt(0)
s_barrier
Mvaa 100,36,140
S_ s65,s59,3145728
Da 60,v2,0
Mvaa 136,20,140
S_ m0,s52,24320
Ta 36,%0,0
Ta 38,%0,512
Mvaa 136,24,180
L_ v6,28,s65,256
Mvaa 144,20,144
S_ s65,s59,3244032
Da 100,v3,0
Mvaa 144,24,184
S_ m0,s52,25344
Ta 224,%0,4096
Ta 226,%0,4608
Mvaa 152,20,148
Mvaa 152,24,188
Da 64,v2,2048
Mvaa 160,20,152
L_ v6,28,s65,256
Mvaa 160,24,192
S_ s65,s59,3342336
Mvaa 168,20,156
S_ m0,s52,26368
Da 104,v3,2048
Mvaa 168,24,196
Mvaa 172,32,196
s_nop 0
Ta 20,%0,8
Ta 22,%0,520
Mvaa 172,28,156
Da 68,v2,4096
Mvaa 164,32,192
L_ v6,28,s65,256
Mvaa 164,28,152
S_ s65,s59,3440640
Ta 24,%0,4104
Ta 26,%0,4616
Mvaa 156,32,188
S_ m0,s52,27392
Da 108,v3,4096
Mvaa 156,28,148
Mvaa 148,32,184
L_ v6,28,s65,256
Mvaa 148,28,144
Da 72,v2,6144
Mvaa 140,32,180
Mvaa 140,28,140
W_ 9
Mvaa 176,36,140
s_nop 0
Ta 28,v1,0
Ta 30,v1,512
Mvaa 176,224,180
Da 112,v3,6144
Mvaa 184,36,144
Mvaa 184,224,184
s_nop 0
Ta 32,v1,4096
Ta 34,v1,4608
Mvaa 192,36,148
Da 76,v2,8192
Mvaa 192,224,188
Mvaa 200,36,152
Da 116,v3,8192
Mvaa 200,224,192
Mvaa 208,36,156
Mvaa 208,224,196
W_ 9
Mvaa 212,24,196
s_nop 0
Ta 36,v1,8
Ta 38,v1,520
Mvaa 212,20,156
Mvaa 204,24,192
Mvaa 204,20,152
s_nop 0
Ta 224,v1,4104
Ta 226,v1,4616
Mvaa 196,24,188
Mvaa 196,20,148
Mvaa 188,24,184
Mvaa 188,20,144
Mvaa 180,24,180
s_waitcnt vmcnt(15) lgkmcnt(0)
s_barrier
Mvaa 180,20,140
S_ s65,s59,3932160
Mvaa 216,28,140
S_ m0,s52,0
Ta 20,%0,8192
Ta 22,%0,8704
Mvaa 216,32,180
L_ v6,28,s65,0
Mvaa 224,28,144
S_ s65,s59,4030464
Mvaa 224,32,184
S_ m0,s52,1024
Ta 24,%0,12288
Ta 26,%0,12800
Mvaa 232,28,148
Mvaa 232,32,188
L_ v6,28,s65,0
Mvaa 240,28,152
S_ s65,s59,4128768
Mvaa 240,32,192
S_ m0,s52,2048
Mvaa 248,28,156
Mvaa 248,32,196
Mvaa 252,224,196
s_nop 0
Ta 28,%0,8200
Ta 30,%0,8712
Mvaa 252,36,156
L_ v6,28,s65,0
Mvaa 244,224,192
S_ s65,s59,4227072
Mvaa 244,36,152
S_ m0,s52,3072
Ta 32,%0,12296
Ta 34,%0,12808
Mvaa 236,224,188
Mvaa 236,36,148
L_ v6,28,s65,0
Mvaa 228,224,184
S_ s65,s60,768
Mvaa 228,36,144
S_ m0,s50,0
Mvaa 220,224,180
Mvaa 220,36,140
W_ 4
Mvaa 16,20,60
s_nop 0
Ta 36,v1,8192
Ta 38,v1,8704
Mvaa 16,24,100
L_ v4,24,s65,0
Mvaa 24,20,64
S_ s65,s60,11008
R_ v13,a248
Mvaa 24,24,104
S_ m0,s50,1024
Ta 224,v1,12288
Ta 226,v1,12800
Mvaa 32,20,68
v_min_u32 v13, 240, v13
Mvaa 32,24,108
L_ v5,24,s65,0
Mvaa 40,20,72
S_ s65,s60,21248
v_lshlrev_b32 v13, 3, v13
Mvaa 40,24,112
S_ m0,s50,2048
Mvaa 48,20,76
Mvaa 48,24,116
W_ 4
Mvaa 52,32,116
s_nop 0
Ta 20,v1,8200
Ta 22,v1,8712
Mvaa 52,28,76
L_ v4,24,s65,0
Mvaa 44,32,112
S_ s65,s60,31488
Mvaa 44,28,72
S_ m0,s50,3072
Ta 24,v1,12296
Ta 26,v1,12808
Mvaa 36,32,108
Mvaa 36,28,68
L_ v5,24,s65,0
Mvaa 28,32,104
S_ s65,s60,41728
Mvaa 28,28,64
S_ m0,s50,4096
Mvaa 20,32,100
s_waitcnt vmcnt(14) lgkmcnt(0)
s_barrier
Mvaa 20,28,60
L_ v4,24,s65,0
Mvaa 56,36,60
S_ s65,s59,3932160
Ta 28,%0,16384
Ta 30,%0,16896
Mvaa 56,224,100
S_ m0,s52,8064
Mvaa 64,36,64
Mvaa 64,224,104
s_nop 0
Ta 32,%0,20480
Ta 34,%0,20992
Mvaa 72,36,68
L_ v6,28,s65,128
Mvaa 72,224,108
S_ s65,s59,4030464
Mvaa 80,36,72
S_ m0,s52,9088
Mvaa 80,224,112
Mvaa 88,36,76
L_ v6,28,s65,128
Mvaa 88,224,116
S_ s65,s59,4128768
Mvaa 92,24,116
S_ m0,s52,10112
Ta 36,%0,16392
Ta 38,%0,16904
Mvaa 92,20,76
S_ s55,s56,6291456
Mvaa 84,24,112
L_ v6,28,s65,128
Mvaa 84,20,72
S_ s65,s59,4227072
Ta 224,%0,20488
Ta 226,%0,21000
Mvaa 76,24,108
S_ m0,s52,11136
Mvaa 76,20,68
Mvaa 68,24,104
L_ v6,28,s65,128
Mvaa 68,20,64
Mvaa 60,24,100
Mvaa 60,20,60
W_ 4
Mvaa 96,28,60
s_nop 0
Ta 20,v1,16384
Ta 22,v1,16896
Mvaa 96,32,100
buffer_load_dword a220, v13, s[44:47], s66 offen
Mvaa 104,28,64
Mvaa 104,32,104
s_nop 0
Ta 24,v1,20480
Ta 26,v1,20992
Mvaa 112,28,68
Mvaa 112,32,108
buffer_load_dword a254, v10, s[28:31], s55 offen
Mvaa 120,28,72
Mvaa 120,32,112
Mvaa 128,28,76
Mvaa 128,32,116
W_ 4
Mvaa 132,224,116
s_nop 0
Ta 28,v1,16392
Ta 30,v1,16904
Mvaa 132,36,76
buffer_load_dword a254, v12, s[24:27], s63 offen offset:1024
Mvaa 124,224,112
Mvaa 124,36,72
s_nop 0
Ta 32,v1,20488
Ta 34,v1,21000
Mvaa 116,224,108
Mvaa 116,36,68
Mvaa 108,224,104
Mvaa 108,36,64
Mvaa 100,224,100
s_waitcnt vmcnt(16) lgkmcnt(0)
s_barrier
Mvaa 100,36,60
S_ s65,s59,3932160
Da 140,v2,20480
Mvaa 136,20,60
S_ m0,s52,16128
Ta 36,%0,24576
Ta 38,%0,25088
Mvaa 136,24,100
L_ v6,28,s65,256
Mvaa 144,20,64
S_ s65,s59,4030464
Da 180,v3,20480
Mvaa 144,24,104
S_ m0,s52,17152
Ta 224,%0,28672
Ta 226,%0,29184
Mvaa 152,20,68
Mvaa 152,24,108
Da 144,v2,22528
Mvaa 160,20,72
L_ v6,28,s65,256
Mvaa 160,24,112
S_ s65,s59,4128768
Mvaa 168,20,76
S_ m0,s52,18176
Da 184,v3,22528
Mvaa 168,24,116
Mvaa 172,32,116
s_nop 0
Ta 20,%0,24584
Ta 22,%0,25096
Mvaa 172,28,76
Da 148,v2,24576
Mvaa 164,32,112
L_ v6,28,s65,256
Mvaa 164,28,72
S_ s65,s59,4227072
Ta 24,%0,28680
Ta 26,%0,29192
Mvaa 156,32,108
S_ m0,s52,19200
Da 188,v3,24576
Mvaa 156,28,68
Mvaa 148,32,104
L_ v6,28,s65,256
Mvaa 148,28,64
Da 152,v2,26624
Mvaa 140,32,100
Mvaa 140,28,60
W_ 9
Mvaa 176,36,60
s_nop 0
Ta 28,v1,24576
Ta 30,v1,25088
Mvaa 176,224,100
Da 192,v3,26624
Mvaa 184,36,64
Mvaa 184,224,104
s_nop 0
Ta 32,v1,28672
Ta 34,v1,29184
Mvaa 192,36,68
Da 156,v2,28672
Mvaa 192,224,108
Mvaa 200,36,72
Da 196,v3,28672
Mvaa 200,224,112
Mvaa 208,36,76
Mvaa 208,224,116
W_ 9
Mvaa 212,24,116
s_nop 0
Ta 36,v1,24584
Ta 38,v1,25096
Mvaa 212,20,76
Mvaa 204,24,112
Mvaa 204,20,72
s_nop 0
Ta 224,v1,28680
Ta 226,v1,29192
Mvaa 196,24,108
Mvaa 196,20,68
Mvaa 188,24,104
Mvaa 188,20,64
Mvaa 180,24,100
s_waitcnt vmcnt(16) lgkmcnt(0)
s_barrier
Mvaa 180,20,60
S_ s65,s59,4718592
Mvaa 216,28,60
S_ m0,s52,24576
Ta 20,%0,0
Ta 22,%0,512
Mvaa 216,32,100
L_ v6,28,s65,0
Mvaa 224,28,64
S_ s65,s59,4816896
Mvaa 224,32,104
S_ m0,s52,25600
Ta 24,%0,4096
Ta 26,%0,4608
Mvaa 232,28,68
Mvaa 232,32,108
L_ v6,28,s65,0
Mvaa 240,28,72
S_ s65,s59,4915200
Mvaa 240,32,112
S_ m0,s52,26624
Mvaa 248,28,76
Mvaa 248,32,116
Mvaa 252,224,116
s_nop 0
Ta 28,%0,8
Ta 30,%0,520
Mvaa 252,36,76
L_ v6,28,s65,0
Mvaa 244,224,112
S_ s65,s59,5013504
Mvaa 244,36,72
S_ m0,s52,27648
Ta 32,%0,4104
Ta 34,%0,4616
Mvaa 236,224,108
Mvaa 236,36,68
L_ v6,28,s65,0
Mvaa 228,224,104
S_ s65,s60,896
Mvaa 228,36,64
S_ m0,s50,20480
Mvaa 220,224,100
Mvaa 220,36,60
W_ 4
Mvaa 16,20,140
s_nop 0
Ta 36,v1,0
Ta 38,v1,512
Mvaa 16,24,180
L_ v4,24,s65,0
Mvaa 24,20,144
S_ s65,s60,11136
Mvaa 24,24,184
S_ m0,s50,21504
Ta 224,v1,4096
Ta 226,v1,4608
Mvaa 32,20,148
Mvaa 32,24,188
L_ v5,24,s65,0
Mvaa 40,20,152
S_ s65,s60,21376
Mvaa 40,24,192
S_ m0,s50,22528
Mvaa 48,20,156
Mvaa 48,24,196
W_ 4
Mvaa 52,32,196
s_nop 0
Ta 20,v1,8
Ta 22,v1,520
Mvaa 52,28,156
L_ v4,24,s65,0
Mvaa 44,32,192
S_ s65,s60,31616
Mvaa 44,28,152
S_ m0,s50,23552
Ta 24,v1,4104
Ta 26,v1,4616
Mvaa 36,32,188
Mvaa 36,28,148
L_ v5,24,s65,0
Mvaa 28,32,184
S_ s65,s60,41856
Mvaa 28,28,144
S_ m0,s50,24576
Mvaa 20,32,180
s_waitcnt vmcnt(15) lgkmcnt(0)
s_barrier
Mvaa 20,28,140
L_ v4,24,s65,0
Mvaa 56,36,140
S_ s65,s59,4718592
Ta 28,%0,8192
Ta 30,%0,8704
Mvaa 56,224,180
S_ m0,s52,-128
Mvaa 64,36,144
Mvaa 64,224,184
s_nop 0
Ta 32,%0,12288
Ta 34,%0,12800
Mvaa 72,36,148
L_ v6,28,s65,128
Mvaa 72,224,188
S_ s65,s59,4816896
Mvaa 80,36,152
S_ m0,s52,896
Mvaa 80,224,192
Mvaa 88,36,156
L_ v6,28,s65,128
Mvaa 88,224,196
S_ s65,s59,4915200
Mvaa 92,24,196
S_ m0,s52,1920
Ta 36,%0,8200
Ta 38,%0,8712
Mvaa 92,20,156
S_ s55,s56,7077888
Mvaa 84,24,192
L_ v6,28,s65,128
Mvaa 84,20,152
S_ s65,s59,5013504
Ta 224,%0,12296
Ta 226,%0,12808
Mvaa 76,24,188
S_ m0,s52,2944
Mvaa 76,20,148
Mvaa 68,24,184
L_ v6,28,s65,128
Mvaa 68,20,144
Mvaa 60,24,180
Mvaa 60,20,140
W_ 4
Mvaa 96,28,140
s_nop 0
Ta 20,v1,8192
Ta 22,v1,8704
Mvaa 96,32,180
buffer_load_dword a254, v10, s[28:31], s55 offen
Mvaa 104,28,144
Mvaa 104,32,184
s_nop 0
Ta 24,v1,12288
Ta 26,v1,12800
Mvaa 112,28,148
Mvaa 112,32,188
buffer_load_dword a254, v12, s[24:27], s63 offen offset:1152
Mvaa 120,28,152
Mvaa 120,32,192
Mvaa 128,28,156
Mvaa 128,32,196
W_ 4
Mvaa 132,224,196
s_nop 0
Ta 28,v1,8200
Ta 30,v1,8712
Mvaa 132,36,156
Mvaa 124,224,192
Mvaa 124,36,152
s_nop 0
Ta 32,v1,12296
Ta 34,v1,12808
Mvaa 116,224,188
Mvaa 116,36,148
Mvaa 108,224,184
Mvaa 108,36,144
Mvaa 100,224,180
s_waitcnt vmcnt(15) lgkmcnt(0)
s_barrier
Mvaa 100,36,140
S_ s65,s59,4718592
Da 60,v2,0
Mvaa 136,20,140
S_ m0,s52,7936
Ta 36,%0,16384
Ta 38,%0,16896
Mvaa 136,24,180
L_ v6,28,s65,256
Mvaa 144,20,144
S_ s65,s59,4816896
Da 100,v3,0
Mvaa 144,24,184
S_ m0,s52,8960
Ta 224,%0,20480
Ta 226,%0,20992
Mvaa 152,20,148
Mvaa 152,24,188
Da 64,v2,2048
Mvaa 160,20,152
L_ v6,28,s65,256
Mvaa 160,24,192
S_ s65,s59,4915200
Mvaa 168,20,156
S_ m0,s52,9984
Da 104,v3,2048
Mvaa 168,24,196
Mvaa 172,32,196
s_nop 0
Ta 20,%0,16392
Ta 22,%0,16904
Mvaa 172,28,156
Da 68,v2,4096
Mvaa 164,32,192
L_ v6,28,s65,256
Mvaa 164,28,152
S_ s65,s59,5013504
Ta 24,%0,20488
Ta 26,%0,21000
Mvaa 156,32,188
S_ m0,s52,11008
Da 108,v3,4096
Mvaa 156,28,148
Mvaa 148,32,184
L_ v6,28,s65,256
Mvaa 148,28,144
Da 72,v2,6144
Mvaa 140,32,180
Mvaa 140,28,140
W_ 9
Mvaa 176,36,140
s_nop 0
Ta 28,v1,16384
Ta 30,v1,16896
Mvaa 176,224,180
Da 112,v3,6144
Mvaa 184,36,144
Mvaa 184,224,184
s_nop 0
Ta 32,v1,20480
Ta 34,v1,20992
Mvaa 192,36,148
Da 76,v2,8192
Mvaa 192,224,188
Mvaa 200,36,152
Da 116,v3,8192
)A"
R"A(Mvaa 200,224,192
Mvaa 208,36,156
Mvaa 208,224,196
W_ 9
Mvaa 212,24,196
s_nop 0
Ta 36,v1,16392
Ta 38,v1,16904
Mvaa 212,20,156
Mvaa 204,24,192
Mvaa 204,20,152
s_nop 0
Ta 224,v1,20488
Ta 226,v1,21000
Mvaa 196,24,188
Mvaa 196,20,148
Mvaa 188,24,184
Mvaa 188,20,144
Mvaa 180,24,180
s_waitcnt vmcnt(15) lgkmcnt(0)
s_barrier
Mvaa 180,20,140
S_ s65,s59,5505024
Mvaa 216,28,140
S_ m0,s52,16384
Ta 20,%0,24576
Ta 22,%0,25088
Mvaa 216,32,180
L_ v6,28,s65,0
Mvaa 224,28,144
S_ s65,s59,5603328
Mvaa 224,32,184
S_ m0,s52,17408
Ta 24,%0,28672
Ta 26,%0,29184
Mvaa 232,28,148
Mvaa 232,32,188
L_ v6,28,s65,0
Mvaa 240,28,152
S_ s65,s59,5701632
Mvaa 240,32,192
S_ m0,s52,18432
Mvaa 248,28,156
Mvaa 248,32,196
Mvaa 252,224,196
s_nop 0
Ta 28,%0,24584
Ta 30,%0,25096
Mvaa 252,36,156
L_ v6,28,s65,0
Mvaa 244,224,192
S_ s65,s59,5799936
Mvaa 244,36,152
S_ m0,s52,19456
Ta 32,%0,28680
Ta 34,%0,29192
Mvaa 236,224,188
Mvaa 236,36,148
L_ v6,28,s65,0
Mvaa 228,224,184
S_ s65,s60,1024
Mvaa 228,36,144
S_ m0,s50,0
Mvaa 220,224,180
Mvaa 220,36,140
W_ 4
Mvaa 16,20,60
s_nop 0
Ta 36,v1,24576
Ta 38,v1,25088
Mvaa 16,24,100
L_ v4,24,s65,0
Mvaa 24,20,64
S_ s65,s60,11264
Mvaa 24,24,104
S_ m0,s50,1024
Ta 224,v1,28672
Ta 226,v1,29184
Mvaa 32,20,68
Mvaa 32,24,108
L_ v5,24,s65,0
Mvaa 40,20,72
S_ s65,s60,21504
Mvaa 40,24,112
S_ m0,s50,2048
Mvaa 48,20,76
Mvaa 48,24,116
W_ 4
Mvaa 52,32,116
s_nop 0
Ta 20,v1,24584
Ta 22,v1,25096
Mvaa 52,28,76
L_ v4,24,s65,0
Mvaa 44,32,112
S_ s65,s60,31744
Mvaa 44,28,72
S_ m0,s50,3072
Ta 24,v1,28680
Ta 26,v1,29192
Mvaa 36,32,108
Mvaa 36,28,68
L_ v5,24,s65,0
Mvaa 28,32,104
S_ s65,s60,41984
Mvaa 28,28,64
S_ m0,s50,4096
Mvaa 20,32,100
s_waitcnt vmcnt(14) lgkmcnt(0)
s_barrier
Mvaa 20,28,60
L_ v4,24,s65,0
Mvaa 56,36,60
S_ s65,s59,5505024
Ta 28,%0,0
Ta 30,%0,512
Mvaa 56,224,100
S_ m0,s52,24448
Mvaa 64,36,64
Mvaa 64,224,104
s_nop 0
Ta 32,%0,4096
Ta 34,%0,4608
Mvaa 72,36,68
L_ v6,28,s65,128
Mvaa 72,224,108
S_ s65,s59,5603328
Mvaa 80,36,72
S_ m0,s52,25472
Mvaa 80,224,112
Mvaa 88,36,76
L_ v6,28,s65,128
Mvaa 88,224,116
S_ s65,s59,5701632
Mvaa 92,24,116
S_ m0,s52,26496
Ta 36,%0,8
Ta 38,%0,520
Mvaa 92,20,76
Mvaa 84,24,112
L_ v6,28,s65,128
Mvaa 84,20,72
S_ s65,s59,5799936
Ta 224,%0,4104
Ta 226,%0,4616
Mvaa 76,24,108
S_ m0,s52,27520
Mvaa 76,20,68
Mvaa 68,24,104
L_ v6,28,s65,128
Mvaa 68,20,64
Mvaa 60,24,100
Mvaa 60,20,60
W_ 4
Mvaa 96,28,60
s_nop 0
Ta 20,v1,0
Ta 22,v1,512
Mvaa 96,32,100
Mvaa 104,28,64
Mvaa 104,32,104
s_nop 0
Ta 24,v1,4096
Ta 26,v1,4608
Mvaa 112,28,68
Mvaa 112,32,108
Mvaa 120,28,72
Mvaa 120,32,112
Mvaa 128,28,76
Mvaa 128,32,116
W_ 4
Mvaa 132,224,116
s_nop 0
Ta 28,v1,8
Ta 30,v1,520
Mvaa 132,36,76
Mvaa 124,224,112
Mvaa 124,36,72
s_nop 0
Ta 32,v1,4104
Ta 34,v1,4616
Mvaa 116,224,108
Mvaa 116,36,68
Mvaa 108,224,104
Mvaa 108,36,64
Mvaa 100,224,100
s_waitcnt vmcnt(13) lgkmcnt(0)
s_barrier
Mvaa 100,36,60
S_ s65,s59,5505024
Da 140,v2,20480
Mvaa 136,20,60
S_ m0,s52,-256
Ta 36,%0,8192
Ta 38,%0,8704
Mvaa 136,24,100
L_ v6,28,s65,256
Mvaa 144,20,64
S_ s65,s59,5603328
Da 180,v3,20480
Mvaa 144,24,104
S_ m0,s52,768
Ta 224,%0,12288
Ta 226,%0,12800
Mvaa 152,20,68
Mvaa 152,24,108
Da 144,v2,22528
Mvaa 160,20,72
L_ v6,28,s65,256
Mvaa 160,24,112
S_ s65,s59,5701632
Mvaa 168,20,76
S_ m0,s52,1792
Da 184,v3,22528
Mvaa 168,24,116
Mvaa 172,32,116
s_nop 0
Ta 20,%0,8200
Ta 22,%0,8712
Mvaa 172,28,76
Da 148,v2,24576
Mvaa 164,32,112
L_ v6,28,s65,256
Mvaa 164,28,72
S_ s65,s59,5799936
Ta 24,%0,12296
Ta 26,%0,12808
Mvaa 156,32,108
S_ m0,s52,2816
Da 188,v3,24576
Mvaa 156,28,68
Mvaa 148,32,104
L_ v6,28,s65,256
Mvaa 148,28,64
Da 152,v2,26624
Mvaa 140,32,100
Mvaa 140,28,60
W_ 9
Mvaa 176,36,60
s_nop 0
Ta 28,v1,8192
Ta 30,v1,8704
Mvaa 176,224,100
Da 192,v3,26624
Mvaa 184,36,64
Mvaa 184,224,104
s_nop 0
Ta 32,v1,12288
Ta 34,v1,12800
Mvaa 192,36,68
Da 156,v2,28672
Mvaa 192,224,108
Mvaa 200,36,72
Da 196,v3,28672
Mvaa 200,224,112
Mvaa 208,36,76
Mvaa 208,224,116
W_ 9
Mvaa 212,24,116
s_nop 0
Ta 36,v1,8200
Ta 38,v1,8712
Mvaa 212,20,76
Mvaa 204,24,112
Mvaa 204,20,72
s_nop 0
Ta 224,v1,12296
Ta 226,v1,12808
Mvaa 196,24,108
Mvaa 196,20,68
Mvaa 188,24,104
Mvaa 188,20,64
Mvaa 180,24,100
s_waitcnt vmcnt(13) lgkmcnt(0)
s_barrier
Mvaa 180,20,60
S_ s65,s59,6291456
Mvaa 216,28,60
S_ m0,s52,7936
Ta 20,%0,16384
Ta 22,%0,16896
Mvaa 216,32,100
L_ v6,28,s65,256
Mvaa 224,28,64
S_ s65,s59,6389760
Mvaa 224,32,104
S_ m0,s52,8960
Ta 24,%0,20480
Ta 26,%0,20992
Mvaa 232,28,68
Mvaa 232,32,108
L_ v6,28,s65,256
Mvaa 240,28,72
S_ s65,s59,6488064
Mvaa 240,32,112
S_ m0,s52,9984
Mvaa 248,28,76
Mvaa 248,32,116
Mvaa 252,224,116
s_nop 0
Ta 28,%0,16392
Ta 30,%0,16904
Mvaa 252,36,76
L_ v6,28,s65,256
Mvaa 244,224,112
S_ s65,s59,6586368
Mvaa 244,36,72
S_ m0,s52,11008
Ta 32,%0,20488
Ta 34,%0,21000
Mvaa 236,224,108
Mvaa 236,36,68
L_ v6,28,s65,256
Mvaa 228,224,104
S_ s65,s60,1152
Mvaa 228,36,64
S_ m0,s50,20480
Mvaa 220,224,100
Mvaa 220,36,60
W_ 4
Mvaa 16,20,140
s_nop 0
Ta 36,v1,16384
Ta 38,v1,16896
Mvaa 16,24,180
L_ v4,24,s65,0
Mvaa 24,20,144
S_ s65,s60,11392
Mvaa 24,24,184
S_ m0,s50,21504
Ta 224,v1,20480
Ta 226,v1,20992
Mvaa 32,20,148
Mvaa 32,24,188
L_ v5,24,s65,0
Mvaa 40,20,152
S_ s65,s60,21632
Mvaa 40,24,192
S_ m0,s50,22528
Mvaa 48,20,156
Mvaa 48,24,196
W_ 4
Mvaa 52,32,196
s_nop 0
Ta 20,v1,16392
Ta 22,v1,16904
Mvaa 52,28,156
L_ v4,24,s65,0
Mvaa 44,32,192
S_ s65,s60,31872
Mvaa 44,28,152
S_ m0,s50,23552
Ta 24,v1,20488
Ta 26,v1,21000
Mvaa 36,32,188
Mvaa 36,28,148
L_ v5,24,s65,0
Mvaa 28,32,184
S_ s65,s60,42112
Mvaa 28,28,144
S_ m0,s50,24576
Mvaa 20,32,180
s_waitcnt vmcnt(12) lgkmcnt(0)
s_barrier
Mvaa 20,28,140
L_ v4,24,s65,0
Mvaa 56,36,140
S_ s65,s59,7077888
Ta 28,%0,24576
Ta 30,%0,25088
Mvaa 56,224,180
S_ m0,s52,16128
Mvaa 64,36,144
Mvaa 64,224,184
s_nop 0
Ta 32,%0,28672
Ta 34,%0,29184
Mvaa 72,36,148
L_ v6,28,s65,256
Mvaa 72,224,188
S_ s65,s59,7176192
Mvaa 80,36,152
S_ m0,s52,17152
Mvaa 80,224,192
Mvaa 88,36,156
L_ v6,28,s65,256
Mvaa 88,224,196
S_ s65,s59,7274496
Mvaa 92,24,196
S_ m0,s52,18176
Ta 36,%0,24584
Ta 38,%0,25096
Mvaa 92,20,156
Mvaa 84,24,192
L_ v6,28,s65,256
Mvaa 84,20,152
S_ s65,s59,7372800
Ta 224,%0,28680
Ta 226,%0,29192
Mvaa 76,24,188
S_ m0,s52,19200
Mvaa 76,20,148
Mvaa 68,24,184
L_ v6,28,s65,256
Mvaa 68,20,144
Mvaa 60,24,180
Mvaa 60,20,140
W_ 4
Mvaa 96,28,140
s_nop 0
Ta 20,v1,24576
Ta 22,v1,25088
Mvaa 96,32,180
Mvaa 104,28,144
Mvaa 104,32,184
s_nop 0
Ta 24,v1,28672
Ta 26,v1,29184
Mvaa 112,28,148
Mvaa 112,32,188
Mvaa 120,28,152
Mvaa 120,32,192
Mvaa 128,28,156
Mvaa 128,32,196
W_ 4
Mvaa 132,224,196
s_nop 0
Ta 28,v1,24584
Ta 30,v1,25096
Mvaa 132,36,156
Mvaa 124,224,192
Mvaa 124,36,152
s_nop 0
Ta 32,v1,28680
Ta 34,v1,29192
Mvaa 116,224,188
Mvaa 116,36,148
Mvaa 108,224,184
Mvaa 108,36,144
Mvaa 100,224,180
s_waitcnt vmcnt(13) lgkmcnt(0)
s_barrier
Mvaa 100,36,140
S_ s65,s59,6291456
Da 60,v2,0
Mvaa 136,20,140
S_ m0,s52,24448
Ta 36,%0,0
Ta 38,%0,512
Mvaa 136,24,180
L_ v6,28,s65,128
Mvaa 144,20,144
S_ s65,s59,6389760
Da 100,v3,0
Mvaa 144,24,184
S_ m0,s52,25472
Ta 224,%0,4096
Ta 226,%0,4608
Mvaa 152,20,148
Mvaa 152,24,188
Da 64,v2,2048
Mvaa 160,20,152
L_ v6,28,s65,128
Mvaa 160,24,192
S_ s65,s59,6488064
Mvaa 168,20,156
S_ m0,s52,26496
Da 104,v3,2048
Mvaa 168,24,196
Mvaa 172,32,196
s_nop 0
Ta 20,%0,8
Ta 22,%0,520
Mvaa 172,28,156
Da 68,v2,4096
Mvaa 164,32,192
L_ v6,28,s65,128
Mvaa 164,28,152
S_ s65,s59,6586368
Ta 24,%0,4104
Ta 26,%0,4616
Mvaa 156,32,188
S_ m0,s52,27520
Da 108,v3,4096
Mvaa 156,28,148
Mvaa 148,32,184
L_ v6,28,s65,128
Mvaa 148,28,144
Da 72,v2,6144
Mvaa 140,32,180
Mvaa 140,28,140
W_ 9
Mvaa 176,36,140
s_nop 0
Ta 28,v1,0
Ta 30,v1,512
Mvaa 176,224,180
Da 112,v3,6144
Mvaa 184,36,144
Mvaa 184,224,184
s_nop 0
Ta 32,v1,4096
Ta 34,v1,4608
Mvaa 192,36,148
Da 76,v2,8192
Mvaa 192,224,188
Mvaa 200,36,152
Da 116,v3,8192
Mvaa 200,224,192
Mvaa 208,36,156
Mvaa 208,224,196
W_ 9
Mvaa 212,24,196
s_nop 0
Ta 36,v1,8
Ta 38,v1,520
Mvaa 212,20,156
Mvaa 204,24,192
Mvaa 204,20,152
s_nop 0
Ta 224,v1,4104
Ta 226,v1,4616
Mvaa 196,24,188
Mvaa 196,20,148
Mvaa 188,24,184
Mvaa 188,20,144
Mvaa 180,24,180
s_waitcnt vmcnt(8) lgkmcnt(0)
s_barrier
Mvaa 180,20,140
S_ s65,s59,7077888
Mvaa 216,28,140
S_ m0,s52,-128
Ta 20,v1,8200
Ta 22,v1,8712
Mvaa 216,32,180
L_ v6,28,s65,128
Mvaa 224,28,144
S_ s65,s59,7176192
Mvaa 224,32,184
S_ m0,s52,896
Ta 24,v1,12296
Ta 26,v1,12808
Mvaa 232,28,148
Mvaa 232,32,188
L_ v6,28,s65,128
Mvaa 240,28,152
S_ s65,s59,7274496
Mvaa 240,32,192
S_ m0,s52,1920
Mvaa 248,28,156
Mvaa 248,32,196
Mvaa 252,224,196
s_nop 0
Ta 28,v1,8192
Ta 30,v1,8704
Mvaa 252,36,156
L_ v6,28,s65,128
Mvaa 244,224,192
S_ s65,s59,7372800
Mvaa 244,36,152
S_ m0,s52,2944
Ta 32,v1,12288
Ta 34,v1,12800
Mvaa 236,224,188
Mvaa 236,36,148
L_ v6,28,s65,128
Mvaa 228,224,184
S_ s65,s60,0
Mvaa 228,36,144
S_ m0,s50,0
Mvaa 220,224,180
Mvaa 220,36,140
W_ 4
Mvaa 220,20,60
s_nop 0
Ta 36,%0,8200
Ta 38,%0,8712
Mvaa 220,24,100
Da 140,v2,20480
Mvaa 228,20,64
L_ v4,24,s65,0
Mvaa 228,24,104
S_ s65,s60,10240
Ta 224,%0,12296
Ta 226,%0,12808
Mvaa 236,20,68
S_ m0,s50,1024
Da 180,v3,20480
Mvaa 236,24,108
Mvaa 244,20,72
Da 144,v2,22528
Mvaa 244,24,112
L_ v5,24,s65,0
Mvaa 252,20,76
S_ s65,s60,20480
Mvaa 252,24,116
W_ 7
Da 184,v3,22528
S_ m0,s50,2048
Mvaa 248,32,116
s_nop 0
Ta 20,%0,8192
Ta 22,%0,8704
Mvaa 248,28,76
L_ v4,24,s65,0
Mvaa 240,32,112
S_ s65,s60,30720
Da 148,v2,24576
Mvaa 240,28,72
S_ m0,s50,3072
Ta 24,%0,12288
Ta 26,%0,12800
Mvaa 232,32,108
Mvaa 232,28,68
Da 188,v3,24576
Mvaa 224,32,104
L_ v5,24,s65,0
Mvaa 224,28,64
S_ s65,s60,40960
Mvaa 216,32,100
s_waitcnt vmcnt(12) lgkmcnt(0)
s_barrier
Da 152,v2,26624
Mvaa 216,28,60
S_ m0,s50,4096
Mvaa 180,36,60
s_nop 0
Ta 28,v1,16392
Ta 30,v1,16904
Mvaa 180,224,100
Da 192,v3,26624
Mvaa 188,36,64
L_ v4,24,s65,0
Mvaa 188,224,104
S_ s65,s59,6291456
Ta 32,v1,20488
Ta 34,v1,21000
Mvaa 196,36,68
S_ m0,s52,8192
Da 156,v2,28672
Mvaa 196,224,108
Mvaa 204,36,72
L_ v6,28,s65,0
Mvaa 204,224,112
S_ s65,s59,6389760
Da 196,v3,28672
Mvaa 212,36,76
S_ m0,s52,9216
Mvaa 212,224,116
Mvaa 208,24,116
s_nop 0
Ta 36,v1,16384
Ta 38,v1,16896
Mvaa 208,20,76
L_ v6,28,s65,0
Mvaa 200,24,112
S_ s65,s59,6488064
Mvaa 200,20,72
S_ m0,s52,10240
Ta 224,v1,20480
Ta 226,v1,20992
Mvaa 192,24,108
Mvaa 192,20,68
L_ v6,28,s65,0
Mvaa 184,24,104
S_ s65,s59,6586368
Mvaa 184,20,64
S_ m0,s52,11264
Mvaa 176,24,100
Mvaa 176,20,60
W_ 6
Mvaa 220,28,140
s_nop 0
Ta 20,%0,16392
Ta 22,%0,16904
Mvaa 220,32,180
L_ v6,28,s65,0
Mvaa 228,28,144
Mvaa 228,32,184
s_nop 0
Ta 24,%0,20488
Ta 26,%0,21000
Mvaa 236,28,148
Mvaa 236,32,188
Mvaa 244,28,152
Mvaa 244,32,192
W_ 9
Mvaa 252,28,156
W_ 8
Mvaa 252,32,196
W_ 4
Mvaa 248,224,196
s_nop 0
Ta 28,%0,16384
Ta 30,%0,16896
Mvaa 248,36,156
Mvaa 240,224,192
Mvaa 240,36,152
s_nop 0
Ta 32,%0,20480
Ta 34,%0,20992
Mvaa 232,224,188
Mvaa 232,36,148
Mvaa 224,224,184
Mvaa 224,36,144
Mvaa 216,224,180
s_waitcnt vmcnt(13) lgkmcnt(0)
s_barrier
Mvaa 216,36,140
S_ s65,s59,7077888
Mvaa 180,20,140
S_ m0,s52,16384
Ta 36,v1,24584
Ta 38,v1,25096
Mvaa 180,24,180
L_ v6,28,s65,0
Mvaa 188,20,144
S_ s65,s59,7176192
Mvaa 188,24,184
S_ m0,s52,17408
Ta 224,v1,28680
Ta 226,v1,29192
Mvaa 196,20,148
U_ v248,s48,v248
U_ v249,s48,v249
Mvaa 196,24,188
L_ v6,28,s65,0
Mvaa 204,20,152
S_ s65,s59,7274496
U_ v250,s48,v250
U_ v251,s48,v251
Mvaa 204,24,192
S_ m0,s52,18432
U_ v252,s48,v252
U_ v253,s48,v253
Mvaa 212,20,156
U_ v254,s48,v254
U_ v255,s48,v255
Mvaa 212,24,196
C_ 255,254,255
C_ 254,252,253
Mvaa 208,32,196
s_nop 0
Ta 20,v1,24576
Ta 22,v1,25088
Mvaa 208,28,156
L_ v6,28,s65,0
Mvaa 200,32,192
S_ s65,s59,7372800
C_ 253,250,251
C_ 252,248,249
Mvaa 200,28,152
S_ m0,s52,19456
Ta 24,v1,28672
Ta 26,v1,29184
Mvaa 192,32,188
X_ a216,v252
X_ a217,v253
Mvaa 192,28,148
L_ v6,28,s65,0
Mvaa 184,32,184
X_ a218,v254
X_ a219,v255
Mvaa 184,28,144
U_ v240,s48,v240
U_ v241,s48,v241
Mvaa 176,32,180
U_ v242,s48,v242
U_ v243,s48,v243
Mvaa 176,28,140
W_ 4
U_ v244,s48,v244
U_ v245,s48,v245
Mvaa 140,36,60
s_nop 0
Ta 28,%0,24584
Ta 30,%0,25096
Mvaa 140,224,100
U_ v246,s48,v246
U_ v247,s48,v247
Mvaa 148,36,64
C_ 251,246,247
C_ 250,244,245
Mvaa 148,224,104
s_nop 0
Ta 32,%0,28680
Ta 34,%0,29192
Mvaa 156,36,68
C_ 249,242,243
C_ 248,240,241
Mvaa 156,224,108
X_ a212,v248
X_ a213,v249
Mvaa 164,36,72
X_ a214,v250
X_ a215,v251
Mvaa 164,224,112
U_ v232,s48,v232
U_ v233,s48,v233
Mvaa 172,36,76
U_ v234,s48,v234
U_ v235,s48,v235
Mvaa 172,224,116
W_ 4
U_ v236,s48,v236
U_ v237,s48,v237
Mvaa 168,24,116
s_nop 0
Ta 36,%0,24576
Ta 38,%0,25088
Mvaa 168,20,76
U_ v238,s48,v238
U_ v239,s48,v239
Mvaa 160,24,112
C_ 247,238,239
C_ 246,236,237
Mvaa 160,20,72
s_nop 0
Ta 224,%0,28672
Ta 226,%0,29184
Mvaa 152,24,108
C_ 245,234,235
C_ 244,232,233
Mvaa 152,20,68
X_ a208,v244
X_ a209,v245
Mvaa 144,24,104
X_ a210,v246
X_ a211,v247
Mvaa 144,20,64
U_ v224,s48,v224
U_ v225,s48,v225
Mvaa 136,24,100
s_waitcnt vmcnt(13) lgkmcnt(0)
s_barrier
U_ v226,s48,v226
Mvaa 136,20,60
S_ s65,s59,-768
U_ v227,s48,v227
Mvaa 100,28,60
S_ m0,s52,24320
Ta 20,v1,8
Ta 22,v1,520
Mvaa 100,32,100
L_ v6,28,s65,256
Mvaa 108,28,64
S_ s65,s59,97536
U_ v228,s48,v228
U_ v229,s48,v229
Mvaa 108,32,104
S_ m0,s52,25344
Ta 24,v1,4104
Ta 26,v1,4616
Mvaa 116,28,68
U_ v230,s48,v230
U_ v231,s48,v231
Mvaa 116,32,108
L_ v6,28,s65,256
Mvaa 124,28,72
S_ s65,s59,195840
C_ 243,230,231
C_ 242,228,229
Mvaa 124,32,112
S_ m0,s52,26368
C_ 241,226,227
C_ 240,224,225
Mvaa 132,28,76
X_ a204,v240
X_ a205,v241
Mvaa 132,32,116
X_ a206,v242
X_ a207,v243
Mvaa 128,224,116
s_nop 0
Ta 28,v1,0
Ta 30,v1,512
Mvaa 128,36,76
L_ v6,28,s65,256
Mvaa 120,224,112
S_ s65,s59,294144
U_ v216,s48,v216
U_ v217,s48,v217
Mvaa 120,36,72
S_ m0,s52,27392
Ta 32,v1,4096
Ta 34,v1,4608
Mvaa 112,224,108
U_ v218,s48,v218
U_ v219,s48,v219
Mvaa 112,36,68
L_ v6,28,s65,256
Mvaa 104,224,104
S_ s65,s60,128
U_ v220,s48,v220
U_ v221,s48,v221
Mvaa 104,36,64
S_ m0,s50,20480
U_ v222,s48,v222
U_ v223,s48,v223
Mvaa 96,224,100
C_ 239,222,223
C_ 238,220,221
Mvaa 96,36,60
W_ 4
C_ 237,218,219
C_ 236,216,217
Mvaa 140,20,140
s_nop 0
Ta 36,%0,8
Ta 38,%0,520
Mvaa 140,24,180
L_ v4,24,s65,0
Mvaa 148,20,144
S_ s65,s60,10368
X_ a200,v236
X_ a201,v237
Mvaa 148,24,184
S_ m0,s50,21504
Ta 224,%0,4104
Ta 226,%0,4616
Mvaa 156,20,148
X_ a202,v238
X_ a203,v239
Mvaa 156,24,188
L_ v5,24,s65,0
Mvaa 164,20,152
S_ s65,s60,20608
U_ v208,s48,v208
U_ v209,s48,v209
Mvaa 164,24,192
S_ m0,s50,22528
U_ v210,s48,v210
U_ v211,s48,v211
Mvaa 172,20,156
U_ v212,s48,v212
U_ v213,s48,v213
Mvaa 172,24,196
W_ 4
U_ v214,s48,v214
U_ v215,s48,v215
Mvaa 168,32,196
s_nop 0
Ta 20,%0,0
Ta 22,%0,512
Mvaa 168,28,156
L_ v4,24,s65,0
Mvaa 160,32,192
S_ s65,s60,30848
C_ 235,214,215
C_ 234,212,213
Mvaa 160,28,152
S_ m0,s50,23552
Ta 24,%0,4096
Ta 26,%0,4608
Mvaa 152,32,188
C_ 233,210,211
C_ 232,208,209
Mvaa 152,28,148
L_ v5,24,s65,0
Mvaa 144,32,184
S_ s65,s60,41088
X_ a176,v232
X_ a177,v233
Mvaa 144,28,144
S_ m0,s50,24576
X_ a178,v234
X_ a179,v235
Mvaa 136,32,180
s_waitcnt vmcnt(12) lgkmcnt(0)
s_barrier
U_ v200,s48,v200
Mvaa 136,28,140
L_ v4,24,s65,0
Mvaa 100,36,140
S_ s65,s59,785664
Ta 28,v1,8200
Ta 30,v1,8712
Mvaa 100,224,180
S_ m0,s52,0
U_ v201,s48,v201
Mvaa 108,36,144
U_ v202,s48,v202
U_ v203,s48,v203
Mvaa 108,224,184
s_nop 0
Ta 32,v1,12296
Ta 34,v1,12808
Mvaa 116,36,148
L_ v6,28,s65,0
Mvaa 116,224,188
S_ s65,s59,883968
U_ v204,s48,v204
U_ v205,s48,v205
Mvaa 124,36,152
S_ m0,s52,1024
U_ v206,s48,v206
U_ v207,s48,v207
Mvaa 124,224,192
C_ 231,206,207
C_ 230,204,205
Mvaa 132,36,156
L_ v6,28,s65,0
Mvaa 132,224,196
C_ 229,202,203
S_ s65,s59,982272
Mvaa 128,24,196
S_ m0,s52,2048
Ta 36,v1,8192
Ta 38,v1,8704
Mvaa 128,20,156
C_ 228,200,201
Mvaa 120,24,192
L_ v6,28,s65,0
Mvaa 120,20,152
S_ s65,s59,1080576
Ta 224,v1,12288
Ta 226,v1,12800
Mvaa 112,24,188
S_ m0,s52,3072
X_ a172,v228
X_ a173,v229
Mvaa 112,20,148
X_ a174,v230
X_ a175,v231
Mvaa 104,24,184
L_ v6,28,s65,0
Mvaa 104,20,144
U_ v192,s48,v192
U_ v193,s48,v193
Mvaa 96,24,180
U_ v194,s48,v194
U_ v195,s48,v195
Mvaa 96,20,140
W_ 4
U_ v196,s48,v196
U_ v197,s48,v197
Mvaa 60,28,60
s_nop 0
Ta 20,%0,8200
Ta 22,%0,8712
Mvaa 60,32,100
U_ v198,s48,v198
U_ v199,s48,v199
Mvaa 68,28,64
C_ 227,198,199
C_ 226,196,197
Mvaa 68,32,104
s_nop 0
Ta 24,%0,12296
Ta 26,%0,12808
Mvaa 76,28,68
C_ 225,194,195
C_ 224,192,193
Mvaa 76,32,108
X_ a168,v224
X_ a169,v225
Mvaa 84,28,72
X_ a170,v226
X_ a171,v227
Mvaa 84,32,112
U_ v184,s48,v184
U_ v185,s48,v185
Mvaa 92,28,76
U_ v186,s48,v186
U_ v187,s48,v187
Mvaa 92,32,116
W_ 4
U_ v188,s48,v188
U_ v189,s48,v189
Mvaa 88,224,116
s_nop 0
Ta 28,%0,8192
Ta 30,%0,8704
Mvaa 88,36,76
U_ v190,s48,v190
U_ v191,s48,v191
Mvaa 80,224,112
C_ 223,190,191
C_ 222,188,189
Mvaa 80,36,72
s_nop 0
Ta 32,%0,12288
Ta 34,%0,12800
Mvaa 72,224,108
C_ 221,186,187
C_ 220,184,185
Mvaa 72,36,68
X_ a164,v220
X_ a165,v221
Mvaa 64,224,104
X_ a166,v222
X_ a167,v223
Mvaa 64,36,64
U_ v176,s48,v176
U_ v177,s48,v177
Mvaa 56,224,100
s_waitcnt vmcnt(13) lgkmcnt(0)
s_barrier
U_ v178,s48,v178
Mvaa 56,36,60
S_ s65,s59,-768
U_ v179,s48,v179
Mvaa 20,20,60
S_ m0,s52,8192
Ta 36,v1,16392
Ta 38,v1,16904
Mvaa 20,24,100
L_ v6,28,s65,0
Mvaa 28,20,64
S_ s65,s59,97536
U_ v180,s48,v180
U_ v181,s48,v181
Mvaa 28,24,104
S_ m0,s52,9216
Ta 224,v1,20488
Ta 226,v1,21000
Mvaa 36,20,68
U_ v182,s48,v182
U_ v183,s48,v183
Mvaa 36,24,108
L_ v6,28,s65,0
Mvaa 44,20,72
S_ s65,s59,195840
C_ 219,182,183
C_ 218,180,181
Mvaa 44,24,112
S_ m0,s52,10240
C_ 217,178,179
C_ 216,176,177
Mvaa 52,20,76
X_ a160,v216
X_ a161,v217
Mvaa 52,24,116
X_ a162,v218
X_ a163,v219
Mvaa 48,32,116
s_nop 0
Ta 20,v1,16384
Ta 22,v1,16896
Mvaa 48,28,76
L_ v6,28,s65,0
Mvaa 40,32,112
S_ s65,s59,294144
U_ v168,s48,v168
U_ v169,s48,v169
Mvaa 40,28,72
S_ m0,s52,11264
Ta 24,v1,20480
Ta 26,v1,20992
Mvaa 32,32,108
U_ v170,s48,v170
U_ v171,s48,v171
Mvaa 32,28,68
L_ v6,28,s65,0
Mvaa 24,32,104
U_ v172,s48,v172
U_ v173,s48,v173
Mvaa 24,28,64
U_ v174,s48,v174
U_ v175,s48,v175
Mvaa 16,32,100
C_ 215,174,175
C_ 214,172,173
Mvaa 16,28,60
W_ 4
C_ 213,170,171
C_ 212,168,169
Mvaa 60,36,140
s_nop 0
Ta 28,%0,16392
Ta 30,%0,16904
Mvaa 60,224,180
X_ a136,v212
X_ a137,v213
Mvaa 68,36,144
X_ a138,v214
X_ a139,v215
Mvaa 68,224,184
s_nop 0
Ta 32,%0,20488
Ta 34,%0,21000
Mvaa 76,36,148
U_ v160,s48,v160
U_ v161,s48,v161
Mvaa 76,224,188
U_ v162,s48,v162
U_ v163,s48,v163
Mvaa 84,36,152
U_ v164,s48,v164
U_ v165,s48,v165
Mvaa 84,224,192
U_ v166,s48,v166
U_ v167,s48,v167
Mvaa 92,36,156
C_ 211,166,167
C_ 210,164,165
Mvaa 92,224,196
W_ 4
C_ 209,162,163
C_ 208,160,161
Mvaa 88,24,196
s_nop 0
Ta 36,%0,16384
Ta 38,%0,16896
Mvaa 88,20,156
X_ a132,v208
X_ a133,v209
Mvaa 80,24,192
X_ a134,v210
X_ a135,v211
Mvaa 80,20,152
s_nop 0
Ta 224,%0,20480
Ta 226,%0,20992
Mvaa 72,24,188
U_ v152,s48,v152
U_ v153,s48,v153
Mvaa 72,20,148
U_ v154,s48,v154
U_ v155,s48,v155
Mvaa 64,24,184
U_ v156,s48,v156
U_ v157,s48,v157
Mvaa 64,20,144
U_ v158,s48,v158
U_ v159,s48,v159
Mvaa 56,24,180
s_waitcnt vmcnt(63) lgkmcnt(0)
s_barrier
C_ 207,158,159
Mvaa 56,20,140
S_ s65,s59,-768
C_ 206,156,157
Mvaa 20,28,140
S_ m0,s52,16256
C_ 205,154,155
C_ 204,152,153
Mvaa 20,32,180
L_ v6,28,s65,128
Mvaa 28,28,144
S_ s65,s59,97536
X_ a128,v204
X_ a129,v205
Mvaa 28,32,184
S_ m0,s52,17280
X_ a130,v206
X_ a131,v207
Mvaa 36,28,148
U_ v144,s48,v144
U_ v145,s48,v145
Mvaa 36,32,188
L_ v6,28,s65,128
Mvaa 44,28,152
S_ s65,s59,195840
U_ v146,s48,v146
U_ v147,s48,v147
Mvaa 44,32,192
S_ m0,s52,18304
U_ v148,s48,v148
U_ v149,s48,v149
Mvaa 52,28,156
U_ v150,s48,v150
U_ v151,s48,v151
Mvaa 52,32,196
C_ 203,150,151
C_ 202,148,149
Mvaa 48,224,196
L_ v6,28,s65,128
Mvaa 48,36,156
S_ s65,s59,294144
C_ 201,146,147
C_ 200,144,145
Mvaa 40,224,192
S_ m0,s52,19328
X_ a124,v200
X_ a125,v201
Mvaa 40,36,152
X_ a126,v202
X_ a127,v203
Mvaa 32,224,188
L_ v6,28,s65,128
Mvaa 32,36,148
U_ v136,s48,v136
U_ v137,s48,v137
Mvaa 24,224,184
U_ v138,s48,v138
U_ v139,s48,v139
Mvaa 24,36,144
U_ v140,s48,v140
U_ v141,s48,v141
Mvaa 16,224,180
U_ v142,s48,v142
U_ v143,s48,v143
Mvaa 16,36,140
C_ 199,142,143
C_ 198,140,141
C_ 197,138,139
C_ 196,136,137
X_ a120,v196
X_ a121,v197
X_ a122,v198
X_ a123,v199
U_ v128,s48,v128
U_ v129,s48,v129
U_ v130,s48,v130
U_ v131,s48,v131
U_ v132,s48,v132
U_ v133,s48,v133
U_ v134,s48,v134
U_ v135,s48,v135
C_ 195,134,135
C_ 194,132,133
C_ 193,130,131
C_ 192,128,129
X_ a96,v192
X_ a97,v193
X_ a98,v194
X_ a99,v195
U_ v120,s48,v120
U_ v121,s48,v121
U_ v122,s48,v122
U_ v123,s48,v123
U_ v124,s48,v124
U_ v125,s48,v125
U_ v126,s48,v126
U_ v127,s48,v127
C_ 191,126,127
C_ 190,124,125
C_ 189,122,123
C_ 188,120,121
X_ a92,v188
X_ a93,v189
X_ a94,v190
X_ a95,v191
U_ v112,s48,v112
U_ v113,s48,v113
U_ v114,s48,v114
U_ v115,s48,v115
U_ v116,s48,v116
U_ v117,s48,v117
U_ v118,s48,v118
U_ v119,s48,v119
C_ 187,118,119
C_ 186,116,117
C_ 185,114,115
C_ 184,112,113
X_ a88,v184
X_ a89,v185
X_ a90,v186
X_ a91,v187
U_ v104,s48,v104
U_ v105,s48,v105
U_ v106,s48,v106
U_ v107,s48,v107
U_ v108,s48,v108
U_ v109,s48,v109
U_ v110,s48,v110
U_ v111,s48,v111
C_ 183,110,111
C_ 182,108,109
C_ 181,106,107
C_ 180,104,105
X_ a84,v180
X_ a85,v181
X_ a86,v182
X_ a87,v183
U_ v96,s48,v96
U_ v97,s48,v97
U_ v98,s48,v98
U_ v99,s48,v99
U_ v100,s48,v100
U_ v101,s48,v101
U_ v102,s48,v102
U_ v103,s48,v103
C_ 179,102,103
C_ 178,100,101
C_ 177,98,99
C_ 176,96,97
X_ a80,v176
X_ a81,v177
X_ a82,v178
X_ a83,v179
v_pk_mul_f32 v[88:89], v[88:89], s[48:49]
v_pk_mul_f32 v[90:91], v[90:91], s[48:49]
v_pk_mul_f32 v[92:93], v[92:93], s[48:49]
v_pk_mul_f32 v[94:95], v[94:95], s[48:49]
C_ 175,94,95
C_ 174,92,93
C_ 173,90,91
C_ 172,88,89
X_ a56,v172
X_ a57,v173
X_ a58,v174
X_ a59,v175
v_pk_mul_f32 v[80:81], v[80:81], s[48:49]
v_pk_mul_f32 v[82:83], v[82:83], s[48:49]
v_pk_mul_f32 v[84:85], v[84:85], s[48:49]
v_pk_mul_f32 v[86:87], v[86:87], s[48:49]
C_ 171,86,87
C_ 170,84,85
C_ 169,82,83
C_ 168,80,81
X_ a52,v168
X_ a53,v169
X_ a54,v170
X_ a55,v171
v_pk_mul_f32 v[72:73], v[72:73], s[48:49]
v_pk_mul_f32 v[74:75], v[74:75], s[48:49]
v_pk_mul_f32 v[76:77], v[76:77], s[48:49]
v_pk_mul_f32 v[78:79], v[78:79], s[48:49]
C_ 167,78,79
C_ 166,76,77
C_ 165,74,75
C_ 164,72,73
X_ a48,v164
X_ a49,v165
X_ a50,v166
X_ a51,v167
v_pk_mul_f32 v[64:65], v[64:65], s[48:49]
v_pk_mul_f32 v[66:67], v[66:67], s[48:49]
v_pk_mul_f32 v[68:69], v[68:69], s[48:49]
v_pk_mul_f32 v[70:71], v[70:71], s[48:49]
C_ 163,70,71
C_ 162,68,69
C_ 161,66,67
C_ 160,64,65
X_ a44,v160
X_ a45,v161
X_ a46,v162
X_ a47,v163
v_pk_mul_f32 v[56:57], v[56:57], s[48:49]
v_pk_mul_f32 v[58:59], v[58:59], s[48:49]
v_pk_mul_f32 v[60:61], v[60:61], s[48:49]
v_pk_mul_f32 v[62:63], v[62:63], s[48:49]
C_ 159,62,63
C_ 158,60,61
C_ 157,58,59
C_ 156,56,57
X_ a40,v156
X_ a41,v157
X_ a42,v158
X_ a43,v159
v_pk_mul_f32 v[48:49], v[48:49], s[48:49]
v_pk_mul_f32 v[50:51], v[50:51], s[48:49]
v_pk_mul_f32 v[52:53], v[52:53], s[48:49]
v_pk_mul_f32 v[54:55], v[54:55], s[48:49]
C_ 155,54,55
C_ 154,52,53
C_ 153,50,51
C_ 152,48,49
X_ a16,v152
X_ a17,v153
X_ a18,v154
X_ a19,v155
v_pk_mul_f32 v[40:41], v[40:41], s[48:49]
v_pk_mul_f32 v[42:43], v[42:43], s[48:49]
v_pk_mul_f32 v[44:45], v[44:45], s[48:49]
v_pk_mul_f32 v[46:47], v[46:47], s[48:49]
C_ 151,46,47
C_ 150,44,45
C_ 149,42,43
C_ 148,40,41
X_ a12,v148
X_ a13,v149
X_ a14,v150
X_ a15,v151
s_sub_u32 s59, s59, 768
s_waitcnt vmcnt(0) lgkmcnt(0)
s_barrier
Ta 20,%0,8192
Ta 22,%0,8704
Ta 24,%0,12288
Ta 26,%0,12800
Da 60,v2,0
Da 100,v3,0
Da 64,v2,2048
Da 104,v3,2048
Da 68,v2,4096
Da 108,v3,4096
Da 72,v2,6144
Da 112,v3,6144
Da 76,v2,8192
Da 116,v3,8192
Ta 28,%0,8200
Ta 30,%0,8712
Ta 32,%0,12296
Ta 34,%0,12808
v_pk_mul_f32 v[32:33], v[32:33], s[48:49]
v_pk_mul_f32 v[34:35], v[34:35], s[48:49]
v_pk_mul_f32 v[36:37], v[36:37], s[48:49]
v_pk_mul_f32 v[38:39], v[38:39], s[48:49]
C_ 147,38,39
C_ 146,36,37
C_ 145,34,35
C_ 144,32,33
X_ a8,v144
X_ a9,v145
X_ a10,v146
X_ a11,v147
v_pk_mul_f32 v[24:25], v[24:25], s[48:49]
v_pk_mul_f32 v[26:27], v[26:27], s[48:49]
v_pk_mul_f32 v[28:29], v[28:29], s[48:49]
v_pk_mul_f32 v[30:31], v[30:31], s[48:49]
C_ 143,30,31
C_ 142,28,29
C_ 141,26,27
C_ 140,24,25
X_ a4,v140
X_ a5,v141
X_ a6,v142
X_ a7,v143
v_pk_mul_f32 v[16:17], v[16:17], s[48:49]
v_pk_mul_f32 v[18:19], v[18:19], s[48:49]
v_pk_mul_f32 v[20:21], v[20:21], s[48:49]
v_pk_mul_f32 v[22:23], v[22:23], s[48:49]
C_ 139,22,23
C_ 138,20,21
C_ 137,18,19
C_ 136,16,17
X_ a0,v136
X_ a1,v137
X_ a2,v138
X_ a3,v139
s_sub_u32 s90, s90, s88
s_cmp_gt_u32 s90, 4000
s_cselect_b32 s11, 1, 0
s_sub_u32 s12, s68, s98
S_ s12,s12,1252
s_mov_b32 s13, 109
s_nop 1
.p2align 3
.Lq:
W_ 13
Zvaa 16,20,60
s_nop 0
Ta 36,v1,8192
Ta 38,v1,8704
W_ 14
Mvaa 16,24,100
R_ v12,a248
W_ 13
Zvaa 24,20,64
v_lshrrev_b32 v12, 4, v12
W_ 12
Mvaa 24,24,104
s_nop 0
Ta 224,v1,12288
Ta 226,v1,12800
W_ 13
Zvaa 32,20,68
v_min_u32 v10, 11, v12
W_ 12
Mvaa 32,24,108
s_mul_i32 s56, s4, 12
W_ 11
Zvaa 40,20,72
v_add_u32 v10, s56, v10
W_ 10
Mvaa 40,24,112
v_mul_u32_u24 v11, 43691, v10
W_ 9
Zvaa 48,20,76
v_lshrrev_b32 v11, 18, v11
W_ 8
Mvaa 48,24,116
W_ 4
v_lshlrev_b32 v10, 7, v10
Zvaa 52,32,116
s_nop 0
Ta 20,v1,8200
Ta 22,v1,8712
Mvaa 52,28,76
v_mul_u32_u24 v11, 11520, v11
Zvaa 44,32,112
v_add_u32 v10, v10, v11
Mvaa 44,28,72
s_nop 0
Ta 24,v1,12296
Ta 26,v1,12808
Zvaa 36,32,108
v_min_u32 v12, 9, v12
Mvaa 36,28,68
s_and_b32 s56, s6, 3
Zvaa 28,32,104
s_lshl_b32 s56, s56, 2
Mvaa 28,28,64
S_ s56,s56,s4
Zvaa 20,32,100
s_waitcnt vmcnt(18) lgkmcnt(0)
s_barrier
s_mul_i32 s56, s56, 10
Mvaa 20,28,60
S_ s65,s59,786432
v_add_u32 v12, s56, v12
Zvaa 56,36,60
S_ m0,s52,8064
Ta 28,%0,16384
Ta 30,%0,16896
Mvaa 56,224,100
L_ v6,28,s65,128
Zvaa 64,36,64
S_ s65,s59,884736
v_mul_u32_u24 v12, 1280, v12
Mvaa 64,224,104
S_ m0,s52,9088
Ta 32,%0,20480
Ta 34,%0,20992
Zvaa 72,36,68
s_lshr_b32 s56, s7, 5
Mvaa 72,224,108
L_ v6,28,s65,128
Zvaa 80,36,72
S_ s65,s59,983040
s_mul_i32 s56, s56, 98304
Mvaa 80,224,112
S_ m0,s52,10112
s_and_b32 s55, s59, 4095
Zvaa 88,36,76
S_ s56,s56,s55
Mvaa 88,224,116
s_mul_i32 s55, s77, 384
Zvaa 92,24,116
s_sub_u32 s56, s56, s55
Ta 36,%0,16392
Ta 38,%0,16904
Mvaa 92,20,76
L_ v6,28,s65,128
Zvaa 84,24,112
S_ s65,s59,1081344
s_mov_b32 s63, 0
Mvaa 84,20,72
S_ m0,s52,11136
Ta 224,%0,20488
Ta 226,%0,21000
Zvaa 76,24,108
s_bitcmp1_b32 1, s58
s_cselect_b32 s63, 0, 4026531840
Mvaa 76,20,68
L_ v6,28,s65,128
Zvaa 68,24,104
S_ s65,s60,256
S_ s56,s56,s63
Mvaa 68,20,64
S_ m0,s50,0
S_ s55,s56,3145728
Zvaa 60,24,100
Mvaa 60,20,60
W_ 4
Zvaa 96,28,60
s_nop 0
Ta 20,v1,16384
Ta 22,v1,16896
Mvaa 96,32,100
L_ v4,24,s65,0
Zvaa 104,28,64
S_ s65,s60,10496
Mvaa 104,32,104
S_ m0,s50,1024
Ta 24,v1,20480
Ta 26,v1,20992
Zvaa 112,28,68
Mvaa 112,32,108
L_ v5,24,s65,0
Zvaa 120,28,72
S_ s65,s60,20736
Mvaa 120,32,112
S_ m0,s50,2048
Zvaa 128,28,76
Mvaa 128,32,116
W_ 4
Zvaa 132,224,116
s_nop 0
Ta 28,v1,16392
Ta 30,v1,16904
Mvaa 132,36,76
L_ v4,24,s65,0
Zvaa 124,224,112
S_ s65,s60,30976
Mvaa 124,36,72
S_ m0,s50,3072
Ta 32,v1,20488
Ta 34,v1,21000
Zvaa 116,224,108
Mvaa 116,36,68
L_ v5,24,s65,0
Zvaa 108,224,104
S_ s65,s60,41216
Mvaa 108,36,64
S_ m0,s50,4096
Zvaa 100,224,100
s_waitcnt vmcnt(17) lgkmcnt(0)
s_barrier
s_cmp_eq_u32 s11, 1
s_cbranch_scc1 .Lpace1
.Lpace1_back:
Mvaa 100,36,60
Da 140,v2,20480
Zvaa 136,20,60
s_nop 0
Ta 36,%0,24576
Ta 38,%0,25088
Mvaa 136,24,100
L_ v4,24,s65,0
Zvaa 144,20,64
S_ s65,s59,786432
Da 180,v3,20480
Mvaa 144,24,104
S_ m0,s52,16128
Ta 224,%0,28672
Ta 226,%0,29184
Zvaa 152,20,68
Mvaa 152,24,108
Da 144,v2,22528
Zvaa 160,20,72
L_ v6,28,s65,256
Mvaa 160,24,112
S_ s65,s59,884736
Zvaa 168,20,76
S_ m0,s52,17152
Da 184,v3,22528
Mvaa 168,24,116
Zvaa 172,32,116
s_nop 0
Ta 20,%0,24584
Ta 22,%0,25096
Mvaa 172,28,76
Da 148,v2,24576
Zvaa 164,32,112
L_ v6,28,s65,256
Mvaa 164,28,72
S_ s65,s59,983040
Ta 24,%0,28680
Ta 26,%0,29192
Zvaa 156,32,108
S_ m0,s52,18176
Da 188,v3,24576
Mvaa 156,28,68
Zvaa 148,32,104
L_ v6,28,s65,256
Mvaa 148,28,64
S_ s65,s59,1081344
Da 152,v2,26624
Zvaa 140,32,100
S_ m0,s52,19200
Mvaa 140,28,60
W_ 9
Zvaa 176,36,60
s_nop 0
Ta 28,v1,24576
Ta 30,v1,25088
Mvaa 176,224,100
Da 192,v3,26624
Zvaa 184,36,64
L_ v6,28,s65,256
Mvaa 184,224,104
s_nop 0
Ta 32,v1,28672
Ta 34,v1,29184
Zvaa 192,36,68
Da 156,v2,28672
Mvaa 192,224,108
Zvaa 200,36,72
Da 196,v3,28672
Mvaa 200,224,112
buffer_load_dword a254, v10, s[28:31], s55 offen
Zvaa 208,36,76
Mvaa 208,224,116
W_ 9
Zvaa 212,24,116
s_nop 0
Ta 36,v1,24584
Ta 38,v1,25096
Mvaa 212,20,76
buffer_load_dword a254, v12, s[24:27], s63 offen offset:512
Zvaa 204,24,112
Mvaa 204,20,72
s_nop 0
Ta 224,v1,28680
Ta 226,v1,29192
Zvaa 196,24,108
Mvaa 196,20,68
Zvaa 188,24,104
Mvaa 188,20,64
Zvaa 180,24,100
s_waitcnt vmcnt(20) lgkmcnt(0)
s_barrier
Mvaa 180,20,60
S_ s65,s59,1572864
Zvaa 216,28,60
S_ m0,s52,24576
Ta 20,%0,0
Ta 22,%0,512
Mvaa 216,32,100
L_ v6,28,s65,0
Zvaa 224,28,64
S_ s65,s59,1671168
Mvaa 224,32,104
S_ m0,s52,25600
Ta 24,%0,4096
Ta 26,%0,4608
Zvaa 232,28,68
Mvaa 232,32,108
L_ v6,28,s65,0
Zvaa 240,28,72
S_ s65,s59,1769472
Mvaa 240,32,112
S_ m0,s52,26624
Zvaa 248,28,76
Mvaa 248,32,116
Zvaa 252,224,116
s_nop 0
Ta 28,%0,8
Ta 30,%0,520
Mvaa 252,36,76
L_ v6,28,s65,0
Zvaa 244,224,112
S_ s65,s59,1867776
Mvaa 244,36,72
S_ m0,s52,27648
Ta 32,%0,4104
Ta 34,%0,4616
Zvaa 236,224,108
Mvaa 236,36,68
L_ v6,28,s65,0
Zvaa 228,224,104
S_ s65,s60,384
Mvaa 228,36,64
S_ m0,s50,20480
Zvaa 220,224,100
Mvaa 220,36,60
W_ 4
Mvaa 16,20,140
s_nop 0
Ta 36,v1,0
Ta 38,v1,512
Mvaa 16,24,180
L_ v4,24,s65,0
Mvaa 24,20,144
S_ s65,s60,10624
Mvaa 24,24,184
S_ m0,s50,21504
Ta 224,v1,4096
Ta 226,v1,4608
Mvaa 32,20,148
Mvaa 32,24,188
L_ v5,24,s65,0
Mvaa 40,20,152
S_ s65,s60,20864
Mvaa 40,24,192
S_ m0,s50,22528
Mvaa 48,20,156
Mvaa 48,24,196
W_ 4
Mvaa 52,32,196
s_nop 0
Ta 20,v1,8
Ta 22,v1,520
Mvaa 52,28,156
L_ v4,24,s65,0
Mvaa 44,32,192
S_ s65,s60,31104
Mvaa 44,28,152
S_ m0,s50,23552
Ta 24,v1,4104
Ta 26,v1,4616
Mvaa 36,32,188
Mvaa 36,28,148
L_ v5,24,s65,0
Mvaa 28,32,184
S_ s65,s60,41344
Mvaa 28,28,144
S_ m0,s50,24576
Mvaa 20,32,180
s_waitcnt vmcnt(19) lgkmcnt(0)
s_barrier
s_cmp_eq_u32 s11, 1
s_cbranch_scc1 .Lpace3
.Lpace3_back:
Mvaa 20,28,140
L_ v4,24,s65,0
Mvaa 56,36,140
S_ s65,s59,1572864
Ta 28,%0,8192
Ta 30,%0,8704
Mvaa 56,224,180
S_ m0,s52,-128
Mvaa 64,36,144
Mvaa 64,224,184
s_nop 0
Ta 32,%0,12288
Ta 34,%0,12800
Mvaa 72,36,148
L_ v6,28,s65,128
Mvaa 72,224,188
S_ s65,s59,1671168
Mvaa 80,36,152
S_ m0,s52,896
Mvaa 80,224,192
Mvaa 88,36,156
L_ v6,28,s65,128
Mvaa 88,224,196
S_ s65,s59,1769472
Mvaa 92,24,196
S_ m0,s52,1920
Ta 36,%0,8200
Ta 38,%0,8712
Mvaa 92,20,156
S_ s55,s56,3932160
Mvaa 84,24,192
L_ v6,28,s65,128
Mvaa 84,20,152
S_ s65,s59,1867776
Ta 224,%0,12296
Ta 226,%0,12808
Mvaa 76,24,188
S_ m0,s52,2944
Mvaa 76,20,148
Mvaa 68,24,184
L_ v6,28,s65,128
Mvaa 68,20,144
Mvaa 60,24,180
Mvaa 60,20,140
W_ 4
Mvaa 96,28,140
s_nop 0
Ta 20,v1,8192
Ta 22,v1,8704
Mvaa 96,32,180
buffer_load_dword a254, v10, s[28:31], s55 offen
Mvaa 104,28,144
Mvaa 104,32,184
s_nop 0
Ta 24,v1,12288
Ta 26,v1,12800
Mvaa 112,28,148
Mvaa 112,32,188
buffer_load_dword a254, v12, s[24:27], s63 offen offset:640
Mvaa 120,28,152
Mvaa 120,32,192
Mvaa 128,28,156
Mvaa 128,32,196
W_ 4
Mvaa 132,224,196
s_nop 0
Ta 28,v1,8200
Ta 30,v1,8712
Mvaa 132,36,156
Mvaa 124,224,192
Mvaa 124,36,152
s_nop 0
Ta 32,v1,12296
Ta 34,v1,12808
Mvaa 116,224,188
Mvaa 116,36,148
Mvaa 108,224,184
Mvaa 108,36,144
Mvaa 100,224,180
s_waitcnt vmcnt(17) lgkmcnt(0)
s_barrier
Mvaa 100,36,140
S_ s65,s59,1572864
Da 60,v2,0
Mvaa 136,20,140
S_ m0,s52,7936
Ta 36,%0,16384
Ta 38,%0,16896
Mvaa 136,24,180
L_ v6,28,s65,256
Mvaa 144,20,144
S_ s65,s59,1671168
Da 100,v3,0
Mvaa 144,24,184
S_ m0,s52,8960
Ta 224,%0,20480
Ta 226,%0,20992
Mvaa 152,20,148
Mvaa 152,24,188
Da 64,v2,2048
Mvaa 160,20,152
L_ v6,28,s65,256
Mvaa 160,24,192
S_ s65,s59,1769472
Mvaa 168,20,156
S_ m0,s52,9984
Da 104,v3,2048
Mvaa 168,24,196
Mvaa 172,32,196
s_nop 0
Ta 20,%0,16392
Ta 22,%0,16904
Mvaa 172,28,156
Da 68,v2,4096
Mvaa 164,32,192
L_ v6,28,s65,256
Mvaa 164,28,152
S_ s65,s59,1867776
Ta 24,%0,20488
Ta 26,%0,21000
Mvaa 156,32,188
)A"
R"A(S_ m0,s52,11008
Da 108,v3,4096
Mvaa 156,28,148
Mvaa 148,32,184
L_ v6,28,s65,256
Mvaa 148,28,144
Da 72,v2,6144
Mvaa 140,32,180
Mvaa 140,28,140
W_ 9
Mvaa 176,36,140
s_nop 0
Ta 28,v1,16384
Ta 30,v1,16896
Mvaa 176,224,180
Da 112,v3,6144
Mvaa 184,36,144
Mvaa 184,224,184
s_nop 0
Ta 32,v1,20480
Ta 34,v1,20992
Mvaa 192,36,148
Da 76,v2,8192
Mvaa 192,224,188
Mvaa 200,36,152
Da 116,v3,8192
Mvaa 200,224,192
Mvaa 208,36,156
Mvaa 208,224,196
W_ 9
Mvaa 212,24,196
s_nop 0
Ta 36,v1,16392
Ta 38,v1,16904
Mvaa 212,20,156
Mvaa 204,24,192
Mvaa 204,20,152
s_nop 0
Ta 224,v1,20488
Ta 226,v1,21000
Mvaa 196,24,188
Mvaa 196,20,148
Mvaa 188,24,184
Mvaa 188,20,144
Mvaa 180,24,180
s_waitcnt vmcnt(15) lgkmcnt(0)
s_barrier
s_cmp_eq_u32 s11, 1
s_cbranch_scc1 .Lpace5
.Lpace5_back:
Mvaa 180,20,140
S_ s65,s59,2359296
Mvaa 216,28,140
S_ m0,s52,16384
Ta 20,%0,24576
Ta 22,%0,25088
Mvaa 216,32,180
L_ v6,28,s65,0
Mvaa 224,28,144
S_ s65,s59,2457600
Mvaa 224,32,184
S_ m0,s52,17408
Ta 24,%0,28672
Ta 26,%0,29184
Mvaa 232,28,148
Mvaa 232,32,188
L_ v6,28,s65,0
Mvaa 240,28,152
S_ s65,s59,2555904
Mvaa 240,32,192
S_ m0,s52,18432
Mvaa 248,28,156
Mvaa 248,32,196
Mvaa 252,224,196
s_nop 0
Ta 28,%0,24584
Ta 30,%0,25096
Mvaa 252,36,156
L_ v6,28,s65,0
Mvaa 244,224,192
S_ s65,s59,2654208
Mvaa 244,36,152
S_ m0,s52,19456
Ta 32,%0,28680
Ta 34,%0,29192
Mvaa 236,224,188
Mvaa 236,36,148
L_ v6,28,s65,0
Mvaa 228,224,184
S_ s65,s60,512
Mvaa 228,36,144
S_ m0,s50,0
Mvaa 220,224,180
Mvaa 220,36,140
W_ 4
Mvaa 16,20,60
s_nop 0
Ta 36,v1,24576
Ta 38,v1,25088
Mvaa 16,24,100
L_ v4,24,s65,0
Mvaa 24,20,64
S_ s65,s60,10752
Mvaa 24,24,104
S_ m0,s50,1024
Ta 224,v1,28672
Ta 226,v1,29184
Mvaa 32,20,68
Mvaa 32,24,108
L_ v5,24,s65,0
Mvaa 40,20,72
S_ s65,s60,20992
Mvaa 40,24,112
S_ m0,s50,2048
Mvaa 48,20,76
Mvaa 48,24,116
W_ 4
Mvaa 52,32,116
s_nop 0
Ta 20,v1,24584
Ta 22,v1,25096
Mvaa 52,28,76
L_ v4,24,s65,0
Mvaa 44,32,112
S_ s65,s60,31232
Mvaa 44,28,72
S_ m0,s50,3072
Ta 24,v1,28680
Ta 26,v1,29192
Mvaa 36,32,108
Mvaa 36,28,68
L_ v5,24,s65,0
Mvaa 28,32,104
S_ s65,s60,41472
Mvaa 28,28,64
S_ m0,s50,4096
Mvaa 20,32,100
s_waitcnt vmcnt(14) lgkmcnt(0)
s_barrier
Mvaa 20,28,60
L_ v4,24,s65,0
Mvaa 56,36,60
S_ s65,s59,2359296
Ta 28,%0,0
Ta 30,%0,512
Mvaa 56,224,100
S_ m0,s52,24448
Mvaa 64,36,64
Mvaa 64,224,104
s_nop 0
Ta 32,%0,4096
Ta 34,%0,4608
Mvaa 72,36,68
L_ v6,28,s65,128
Mvaa 72,224,108
S_ s65,s59,2457600
Mvaa 80,36,72
S_ m0,s52,25472
Mvaa 80,224,112
Mvaa 88,36,76
L_ v6,28,s65,128
Mvaa 88,224,116
S_ s65,s59,2555904
Mvaa 92,24,116
S_ m0,s52,26496
Ta 36,%0,8
Ta 38,%0,520
Mvaa 92,20,76
S_ s55,s56,4718592
Mvaa 84,24,112
L_ v6,28,s65,128
Mvaa 84,20,72
S_ s65,s59,2654208
Ta 224,%0,4104
Ta 226,%0,4616
Mvaa 76,24,108
S_ m0,s52,27520
Mvaa 76,20,68
Mvaa 68,24,104
L_ v6,28,s65,128
Mvaa 68,20,64
Mvaa 60,24,100
Mvaa 60,20,60
W_ 4
Mvaa 96,28,60
s_nop 0
Ta 20,v1,0
Ta 22,v1,512
Mvaa 96,32,100
buffer_load_dword a254, v10, s[28:31], s55 offen
Mvaa 104,28,64
Mvaa 104,32,104
s_nop 0
Ta 24,v1,4096
Ta 26,v1,4608
Mvaa 112,28,68
Mvaa 112,32,108
buffer_load_dword a254, v12, s[24:27], s63 offen offset:768
Mvaa 120,28,72
Mvaa 120,32,112
Mvaa 128,28,76
Mvaa 128,32,116
W_ 4
Mvaa 132,224,116
s_nop 0
Ta 28,v1,8
Ta 30,v1,520
Mvaa 132,36,76
Mvaa 124,224,112
Mvaa 124,36,72
s_nop 0
Ta 32,v1,4104
Ta 34,v1,4616
Mvaa 116,224,108
Mvaa 116,36,68
Mvaa 108,224,104
Mvaa 108,36,64
Mvaa 100,224,100
s_waitcnt vmcnt(15) lgkmcnt(0)
s_barrier
s_cmp_eq_u32 s11, 1
s_cbranch_scc1 .Lpace7
.Lpace7_back:
Mvaa 100,36,60
S_ s65,s59,2359296
Da 140,v2,20480
Mvaa 136,20,60
S_ m0,s52,-256
Ta 36,%0,8192
Ta 38,%0,8704
Mvaa 136,24,100
L_ v6,28,s65,256
Mvaa 144,20,64
S_ s65,s59,2457600
Da 180,v3,20480
Mvaa 144,24,104
S_ m0,s52,768
Ta 224,%0,12288
Ta 226,%0,12800
Mvaa 152,20,68
Mvaa 152,24,108
Da 144,v2,22528
Mvaa 160,20,72
L_ v6,28,s65,256
Mvaa 160,24,112
S_ s65,s59,2555904
Mvaa 168,20,76
S_ m0,s52,1792
Da 184,v3,22528
Mvaa 168,24,116
Mvaa 172,32,116
s_nop 0
Ta 20,%0,8200
Ta 22,%0,8712
Mvaa 172,28,76
Da 148,v2,24576
Mvaa 164,32,112
L_ v6,28,s65,256
Mvaa 164,28,72
S_ s65,s59,2654208
Ta 24,%0,12296
Ta 26,%0,12808
Mvaa 156,32,108
S_ m0,s52,2816
Da 188,v3,24576
Mvaa 156,28,68
Mvaa 148,32,104
L_ v6,28,s65,256
Mvaa 148,28,64
Da 152,v2,26624
Mvaa 140,32,100
Mvaa 140,28,60
W_ 9
Mvaa 176,36,60
s_nop 0
Ta 28,v1,8192
Ta 30,v1,8704
Mvaa 176,224,100
Da 192,v3,26624
Mvaa 184,36,64
Mvaa 184,224,104
s_nop 0
Ta 32,v1,12288
Ta 34,v1,12800
Mvaa 192,36,68
Da 156,v2,28672
Mvaa 192,224,108
Mvaa 200,36,72
Da 196,v3,28672
Mvaa 200,224,112
Mvaa 208,36,76
Mvaa 208,224,116
W_ 9
Mvaa 212,24,116
s_nop 0
Ta 36,v1,8200
Ta 38,v1,8712
Mvaa 212,20,76
Mvaa 204,24,112
Mvaa 204,20,72
s_nop 0
Ta 224,v1,12296
Ta 226,v1,12808
Mvaa 196,24,108
Mvaa 196,20,68
Mvaa 188,24,104
Mvaa 188,20,64
Mvaa 180,24,100
s_waitcnt vmcnt(15) lgkmcnt(0)
s_barrier
Mvaa 180,20,60
S_ s65,s59,3145728
Mvaa 216,28,60
S_ m0,s52,8192
Ta 20,%0,16384
Ta 22,%0,16896
Mvaa 216,32,100
L_ v6,28,s65,0
Mvaa 224,28,64
S_ s65,s59,3244032
Mvaa 224,32,104
S_ m0,s52,9216
Ta 24,%0,20480
Ta 26,%0,20992
Mvaa 232,28,68
Mvaa 232,32,108
L_ v6,28,s65,0
Mvaa 240,28,72
S_ s65,s59,3342336
Mvaa 240,32,112
S_ m0,s52,10240
Mvaa 248,28,76
Mvaa 248,32,116
Mvaa 252,224,116
s_nop 0
Ta 28,%0,16392
Ta 30,%0,16904
Mvaa 252,36,76
L_ v6,28,s65,0
Mvaa 244,224,112
S_ s65,s59,3440640
Mvaa 244,36,72
S_ m0,s52,11264
Ta 32,%0,20488
Ta 34,%0,21000
Mvaa 236,224,108
Mvaa 236,36,68
L_ v6,28,s65,0
Mvaa 228,224,104
S_ s65,s60,640
Mvaa 228,36,64
S_ m0,s50,20480
Mvaa 220,224,100
Mvaa 220,36,60
W_ 4
Mvaa 16,20,140
s_nop 0
Ta 36,v1,16384
Ta 38,v1,16896
Mvaa 16,24,180
L_ v4,24,s65,0
Mvaa 24,20,144
S_ s65,s60,10880
Mvaa 24,24,184
S_ m0,s50,21504
Ta 224,v1,20480
Ta 226,v1,20992
Mvaa 32,20,148
Mvaa 32,24,188
L_ v5,24,s65,0
Mvaa 40,20,152
S_ s65,s60,21120
Mvaa 40,24,192
S_ m0,s50,22528
Mvaa 48,20,156
Mvaa 48,24,196
W_ 4
Mvaa 52,32,196
s_nop 0
Ta 20,v1,16392
Ta 22,v1,16904
Mvaa 52,28,156
L_ v4,24,s65,0
Mvaa 44,32,192
S_ s65,s60,31360
Mvaa 44,28,152
S_ m0,s50,23552
Ta 24,v1,20488
Ta 26,v1,21000
Mvaa 36,32,188
Mvaa 36,28,148
L_ v5,24,s65,0
Mvaa 28,32,184
S_ s65,s60,41600
Mvaa 28,28,144
S_ m0,s50,24576
Mvaa 20,32,180
s_waitcnt vmcnt(14) lgkmcnt(0)
s_barrier
s_cmp_eq_u32 s11, 1
s_cbranch_scc1 .Lpace9
.Lpace9_back:
Mvaa 20,28,140
L_ v4,24,s65,0
Mvaa 56,36,140
S_ s65,s59,3145728
Ta 28,%0,24576
Ta 30,%0,25088
Mvaa 56,224,180
S_ m0,s52,16256
Mvaa 64,36,144
Mvaa 64,224,184
s_nop 0
Ta 32,%0,28672
Ta 34,%0,29184
Mvaa 72,36,148
L_ v6,28,s65,128
Mvaa 72,224,188
S_ s65,s59,3244032
Mvaa 80,36,152
S_ m0,s52,17280
Mvaa 80,224,192
Mvaa 88,36,156
L_ v6,28,s65,128
Mvaa 88,224,196
S_ s65,s59,3342336
Mvaa 92,24,196
S_ m0,s52,18304
Ta 36,%0,24584
Ta 38,%0,25096
Mvaa 92,20,156
S_ s55,s56,5505024
Mvaa 84,24,192
L_ v6,28,s65,128
Mvaa 84,20,152
S_ s65,s59,3440640
Ta 224,%0,28680
Ta 226,%0,29192
Mvaa 76,24,188
S_ m0,s52,19328
Mvaa 76,20,148
Mvaa 68,24,184
L_ v6,28,s65,128
Mvaa 68,20,144
Mvaa 60,24,180
Mvaa 60,20,140
W_ 4
Mvaa 96,28,140
s_nop 0
Ta 20,v1,24576
Ta 22,v1,25088
Mvaa 96,32,180
buffer_load_dword a254, v10, s[28:31], s55 offen
Mvaa 104,28,144
Mvaa 104,32,184
s_nop 0
Ta 24,v1,28672
Ta 26,v1,29184
Mvaa 112,28,148
Mvaa 112,32,188
buffer_load_dword a254, v12, s[24:27], s63 offen offset:896
Mvaa 120,28,152
Mvaa 120,32,192
Mvaa 128,28,156
Mvaa 128,32,196
W_ 4
Mvaa 132,224,196
s_nop 0
Ta 28,v1,24584
Ta 30,v1,25096
Mvaa 132,36,156
Mvaa 124,224,192
Mvaa 124,36,152
s_nop 0
Ta 32,v1,28680
Ta 34,v1,29192
Mvaa 116,224,188
Mvaa 116,36,148
Mvaa 108,224,184
Mvaa 108,36,144
Mvaa 100,224,180
s_waitcnt vmcnt(15) lgkmcnt(0)
s_barrier
Mvaa 100,36,140
S_ s65,s59,3145728
Da 60,v2,0
Mvaa 136,20,140
S_ m0,s52,24320
Ta 36,%0,0
Ta 38,%0,512
Mvaa 136,24,180
L_ v6,28,s65,256
Mvaa 144,20,144
S_ s65,s59,3244032
Da 100,v3,0
Mvaa 144,24,184
S_ m0,s52,25344
Ta 224,%0,4096
Ta 226,%0,4608
Mvaa 152,20,148
Mvaa 152,24,188
Da 64,v2,2048
Mvaa 160,20,152
L_ v6,28,s65,256
Mvaa 160,24,192
S_ s65,s59,3342336
Mvaa 168,20,156
S_ m0,s52,26368
Da 104,v3,2048
Mvaa 168,24,196
Mvaa 172,32,196
s_nop 0
Ta 20,%0,8
Ta 22,%0,520
Mvaa 172,28,156
Da 68,v2,4096
Mvaa 164,32,192
L_ v6,28,s65,256
Mvaa 164,28,152
S_ s65,s59,3440640
Ta 24,%0,4104
Ta 26,%0,4616
Mvaa 156,32,188
S_ m0,s52,27392
Da 108,v3,4096
Mvaa 156,28,148
Mvaa 148,32,184
L_ v6,28,s65,256
Mvaa 148,28,144
Da 72,v2,6144
Mvaa 140,32,180
Mvaa 140,28,140
W_ 9
Mvaa 176,36,140
s_nop 0
Ta 28,v1,0
Ta 30,v1,512
Mvaa 176,224,180
Da 112,v3,6144
Mvaa 184,36,144
Mvaa 184,224,184
s_nop 0
Ta 32,v1,4096
Ta 34,v1,4608
Mvaa 192,36,148
Da 76,v2,8192
Mvaa 192,224,188
Mvaa 200,36,152
Da 116,v3,8192
Mvaa 200,224,192
Mvaa 208,36,156
Mvaa 208,224,196
W_ 9
Mvaa 212,24,196
s_nop 0
Ta 36,v1,8
Ta 38,v1,520
Mvaa 212,20,156
Mvaa 204,24,192
Mvaa 204,20,152
s_nop 0
Ta 224,v1,4104
Ta 226,v1,4616
Mvaa 196,24,188
Mvaa 196,20,148
Mvaa 188,24,184
Mvaa 188,20,144
Mvaa 180,24,180
s_waitcnt vmcnt(15) lgkmcnt(0)
s_barrier
s_cmp_eq_u32 s11, 1
s_cbranch_scc1 .Lpace11
.Lpace11_back:
Mvaa 180,20,140
S_ s65,s59,3932160
Mvaa 216,28,140
S_ m0,s52,0
Ta 20,%0,8192
Ta 22,%0,8704
Mvaa 216,32,180
L_ v6,28,s65,0
Mvaa 224,28,144
S_ s65,s59,4030464
Mvaa 224,32,184
S_ m0,s52,1024
Ta 24,%0,12288
Ta 26,%0,12800
Mvaa 232,28,148
Mvaa 232,32,188
L_ v6,28,s65,0
Mvaa 240,28,152
S_ s65,s59,4128768
Mvaa 240,32,192
S_ m0,s52,2048
Mvaa 248,28,156
Mvaa 248,32,196
Mvaa 252,224,196
s_nop 0
Ta 28,%0,8200
Ta 30,%0,8712
Mvaa 252,36,156
L_ v6,28,s65,0
Mvaa 244,224,192
S_ s65,s59,4227072
Mvaa 244,36,152
S_ m0,s52,3072
Ta 32,%0,12296
Ta 34,%0,12808
Mvaa 236,224,188
Mvaa 236,36,148
L_ v6,28,s65,0
Mvaa 228,224,184
S_ s65,s60,768
Mvaa 228,36,144
S_ m0,s50,0
Mvaa 220,224,180
Mvaa 220,36,140
W_ 4
Mvaa 16,20,60
s_nop 0
Ta 36,v1,8192
Ta 38,v1,8704
Mvaa 16,24,100
L_ v4,24,s65,0
Mvaa 24,20,64
S_ s65,s60,11008
R_ v13,a248
Mvaa 24,24,104
S_ m0,s50,1024
Ta 224,v1,12288
Ta 226,v1,12800
Mvaa 32,20,68
v_min_u32 v13, 240, v13
Mvaa 32,24,108
L_ v5,24,s65,0
Mvaa 40,20,72
S_ s65,s60,21248
v_lshlrev_b32 v13, 3, v13
Mvaa 40,24,112
S_ m0,s50,2048
Mvaa 48,20,76
Mvaa 48,24,116
W_ 4
Mvaa 52,32,116
s_nop 0
Ta 20,v1,8200
Ta 22,v1,8712
Mvaa 52,28,76
L_ v4,24,s65,0
Mvaa 44,32,112
S_ s65,s60,31488
Mvaa 44,28,72
S_ m0,s50,3072
Ta 24,v1,12296
Ta 26,v1,12808
Mvaa 36,32,108
Mvaa 36,28,68
L_ v5,24,s65,0
Mvaa 28,32,104
S_ s65,s60,41728
Mvaa 28,28,64
S_ m0,s50,4096
Mvaa 20,32,100
s_waitcnt vmcnt(14) lgkmcnt(0)
s_barrier
Mvaa 20,28,60
L_ v4,24,s65,0
Mvaa 56,36,60
S_ s65,s59,3932160
Ta 28,%0,16384
Ta 30,%0,16896
Mvaa 56,224,100
S_ m0,s52,8064
Mvaa 64,36,64
Mvaa 64,224,104
s_nop 0
Ta 32,%0,20480
Ta 34,%0,20992
Mvaa 72,36,68
L_ v6,28,s65,128
Mvaa 72,224,108
S_ s65,s59,4030464
Mvaa 80,36,72
S_ m0,s52,9088
Mvaa 80,224,112
Mvaa 88,36,76
L_ v6,28,s65,128
Mvaa 88,224,116
S_ s65,s59,4128768
Mvaa 92,24,116
S_ m0,s52,10112
Ta 36,%0,16392
Ta 38,%0,16904
Mvaa 92,20,76
S_ s55,s56,6291456
Mvaa 84,24,112
L_ v6,28,s65,128
Mvaa 84,20,72
S_ s65,s59,4227072
Ta 224,%0,20488
Ta 226,%0,21000
Mvaa 76,24,108
S_ m0,s52,11136
Mvaa 76,20,68
Mvaa 68,24,104
L_ v6,28,s65,128
Mvaa 68,20,64
Mvaa 60,24,100
Mvaa 60,20,60
W_ 4
Mvaa 96,28,60
s_nop 0
Ta 20,v1,16384
Ta 22,v1,16896
Mvaa 96,32,100
buffer_load_dword a220, v13, s[44:47], s66 offen
Mvaa 104,28,64
Mvaa 104,32,104
s_nop 0
Ta 24,v1,20480
Ta 26,v1,20992
Mvaa 112,28,68
Mvaa 112,32,108
buffer_load_dword a254, v10, s[28:31], s55 offen
Mvaa 120,28,72
Mvaa 120,32,112
Mvaa 128,28,76
Mvaa 128,32,116
W_ 4
Mvaa 132,224,116
s_nop 0
Ta 28,v1,16392
Ta 30,v1,16904
Mvaa 132,36,76
buffer_load_dword a254, v12, s[24:27], s63 offen offset:1024
Mvaa 124,224,112
Mvaa 124,36,72
s_nop 0
Ta 32,v1,20488
Ta 34,v1,21000
Mvaa 116,224,108
Mvaa 116,36,68
Mvaa 108,224,104
Mvaa 108,36,64
Mvaa 100,224,100
s_waitcnt vmcnt(16) lgkmcnt(0)
s_barrier
s_cmp_eq_u32 s11, 1
s_cbranch_scc1 .Lpace13
.Lpace13_back:
Mvaa 100,36,60
S_ s65,s59,3932160
Da 140,v2,20480
Mvaa 136,20,60
S_ m0,s52,16128
Ta 36,%0,24576
Ta 38,%0,25088
Mvaa 136,24,100
L_ v6,28,s65,256
Mvaa 144,20,64
S_ s65,s59,4030464
Da 180,v3,20480
Mvaa 144,24,104
S_ m0,s52,17152
Ta 224,%0,28672
Ta 226,%0,29184
Mvaa 152,20,68
Mvaa 152,24,108
Da 144,v2,22528
Mvaa 160,20,72
L_ v6,28,s65,256
Mvaa 160,24,112
S_ s65,s59,4128768
Mvaa 168,20,76
S_ m0,s52,18176
Da 184,v3,22528
Mvaa 168,24,116
Mvaa 172,32,116
s_nop 0
Ta 20,%0,24584
Ta 22,%0,25096
Mvaa 172,28,76
Da 148,v2,24576
Mvaa 164,32,112
L_ v6,28,s65,256
Mvaa 164,28,72
S_ s65,s59,4227072
Ta 24,%0,28680
Ta 26,%0,29192
Mvaa 156,32,108
S_ m0,s52,19200
Da 188,v3,24576
Mvaa 156,28,68
Mvaa 148,32,104
L_ v6,28,s65,256
Mvaa 148,28,64
Da 152,v2,26624
Mvaa 140,32,100
Mvaa 140,28,60
W_ 9
Mvaa 176,36,60
s_nop 0
Ta 28,v1,24576
Ta 30,v1,25088
Mvaa 176,224,100
Da 192,v3,26624
Mvaa 184,36,64
Mvaa 184,224,104
s_nop 0
Ta 32,v1,28672
Ta 34,v1,29184
Mvaa 192,36,68
Da 156,v2,28672
Mvaa 192,224,108
Mvaa 200,36,72
Da 196,v3,28672
Mvaa 200,224,112
Mvaa 208,36,76
Mvaa 208,224,116
W_ 9
Mvaa 212,24,116
s_nop 0
Ta 36,v1,24584
Ta 38,v1,25096
Mvaa 212,20,76
Mvaa 204,24,112
Mvaa 204,20,72
s_nop 0
Ta 224,v1,28680
Ta 226,v1,29192
Mvaa 196,24,108
Mvaa 196,20,68
Mvaa 188,24,104
Mvaa 188,20,64
Mvaa 180,24,100
s_waitcnt vmcnt(16) lgkmcnt(0)
s_barrier
Mvaa 180,20,60
S_ s65,s59,4718592
Mvaa 216,28,60
S_ m0,s52,24576
Ta 20,%0,0
Ta 22,%0,512
Mvaa 216,32,100
L_ v6,28,s65,0
Mvaa 224,28,64
S_ s65,s59,4816896
Mvaa 224,32,104
S_ m0,s52,25600
Ta 24,%0,4096
Ta 26,%0,4608
Mvaa 232,28,68
Mvaa 232,32,108
L_ v6,28,s65,0
Mvaa 240,28,72
S_ s65,s59,4915200
Mvaa 240,32,112
S_ m0,s52,26624
Mvaa 248,28,76
Mvaa 248,32,116
Mvaa 252,224,116
s_nop 0
Ta 28,%0,8
Ta 30,%0,520
Mvaa 252,36,76
L_ v6,28,s65,0
Mvaa 244,224,112
S_ s65,s59,5013504
Mvaa 244,36,72
S_ m0,s52,27648
Ta 32,%0,4104
Ta 34,%0,4616
Mvaa 236,224,108
Mvaa 236,36,68
L_ v6,28,s65,0
Mvaa 228,224,104
S_ s65,s60,896
Mvaa 228,36,64
S_ m0,s50,20480
Mvaa 220,224,100
Mvaa 220,36,60
W_ 4
Mvaa 16,20,140
s_nop 0
Ta 36,v1,0
Ta 38,v1,512
Mvaa 16,24,180
L_ v4,24,s65,0
Mvaa 24,20,144
S_ s65,s60,11136
Mvaa 24,24,184
S_ m0,s50,21504
Ta 224,v1,4096
Ta 226,v1,4608
Mvaa 32,20,148
Mvaa 32,24,188
L_ v5,24,s65,0
Mvaa 40,20,152
S_ s65,s60,21376
Mvaa 40,24,192
S_ m0,s50,22528
Mvaa 48,20,156
Mvaa 48,24,196
W_ 4
Mvaa 52,32,196
s_nop 0
Ta 20,v1,8
Ta 22,v1,520
Mvaa 52,28,156
L_ v4,24,s65,0
Mvaa 44,32,192
S_ s65,s60,31616
Mvaa 44,28,152
S_ m0,s50,23552
Ta 24,v1,4104
Ta 26,v1,4616
Mvaa 36,32,188
Mvaa 36,28,148
L_ v5,24,s65,0
Mvaa 28,32,184
S_ s65,s60,41856
Mvaa 28,28,144
S_ m0,s50,24576
Mvaa 20,32,180
s_waitcnt vmcnt(15) lgkmcnt(0)
s_barrier
s_cmp_eq_u32 s11, 1
s_cbranch_scc1 .Lpace15
.Lpace15_back:
Mvaa 20,28,140
L_ v4,24,s65,0
Mvaa 56,36,140
S_ s65,s59,4718592
Ta 28,%0,8192
Ta 30,%0,8704
Mvaa 56,224,180
S_ m0,s52,-128
Mvaa 64,36,144
Mvaa 64,224,184
s_nop 0
Ta 32,%0,12288
Ta 34,%0,12800
Mvaa 72,36,148
L_ v6,28,s65,128
Mvaa 72,224,188
S_ s65,s59,4816896
Mvaa 80,36,152
S_ m0,s52,896
Mvaa 80,224,192
Mvaa 88,36,156
L_ v6,28,s65,128
Mvaa 88,224,196
S_ s65,s59,4915200
Mvaa 92,24,196
S_ m0,s52,1920
Ta 36,%0,8200
Ta 38,%0,8712
Mvaa 92,20,156
S_ s55,s56,7077888
Mvaa 84,24,192
L_ v6,28,s65,128
Mvaa 84,20,152
S_ s65,s59,5013504
Ta 224,%0,12296
Ta 226,%0,12808
Mvaa 76,24,188
S_ m0,s52,2944
Mvaa 76,20,148
Mvaa 68,24,184
L_ v6,28,s65,128
Mvaa 68,20,144
Mvaa 60,24,180
Mvaa 60,20,140
W_ 4
Mvaa 96,28,140
s_nop 0
Ta 20,v1,8192
Ta 22,v1,8704
Mvaa 96,32,180
buffer_load_dword a254, v10, s[28:31], s55 offen
Mvaa 104,28,144
Mvaa 104,32,184
s_nop 0
Ta 24,v1,12288
Ta 26,v1,12800
Mvaa 112,28,148
Mvaa 112,32,188
buffer_load_dword a254, v12, s[24:27], s63 offen offset:1152
Mvaa 120,28,152
Mvaa 120,32,192
Mvaa 128,28,156
Mvaa 128,32,196
W_ 4
Mvaa 132,224,196
s_nop 0
Ta 28,v1,8200
Ta 30,v1,8712
Mvaa 132,36,156
Mvaa 124,224,192
Mvaa 124,36,152
s_nop 0
Ta 32,v1,12296
Ta 34,v1,12808
Mvaa 116,224,188
Mvaa 116,36,148
Mvaa 108,224,184
Mvaa 108,36,144
Mvaa 100,224,180
s_waitcnt vmcnt(15) lgkmcnt(0)
s_barrier
Mvaa 100,36,140
S_ s65,s59,4718592
Da 60,v2,0
Mvaa 136,20,140
S_ m0,s52,7936
Ta 36,%0,16384
Ta 38,%0,16896
Mvaa 136,24,180
L_ v6,28,s65,256
Mvaa 144,20,144
S_ s65,s59,4816896
Da 100,v3,0
Mvaa 144,24,184
S_ m0,s52,8960
Ta 224,%0,20480
Ta 226,%0,20992
Mvaa 152,20,148
Mvaa 152,24,188
Da 64,v2,2048
Mvaa 160,20,152
L_ v6,28,s65,256
Mvaa 160,24,192
S_ s65,s59,4915200
Mvaa 168,20,156
S_ m0,s52,9984
Da 104,v3,2048
Mvaa 168,24,196
Mvaa 172,32,196
s_nop 0
Ta 20,%0,16392
Ta 22,%0,16904
Mvaa 172,28,156
Da 68,v2,4096
Mvaa 164,32,192
L_ v6,28,s65,256
Mvaa 164,28,152
S_ s65,s59,5013504
Ta 24,%0,20488
Ta 26,%0,21000
Mvaa 156,32,188
S_ m0,s52,11008
Da 108,v3,4096
Mvaa 156,28,148
Mvaa 148,32,184
L_ v6,28,s65,256
Mvaa 148,28,144
Da 72,v2,6144
Mvaa 140,32,180
Mvaa 140,28,140
W_ 9
Mvaa 176,36,140
s_nop 0
Ta 28,v1,16384
Ta 30,v1,16896
Mvaa 176,224,180
Da 112,v3,6144
Mvaa 184,36,144
Mvaa 184,224,184
s_nop 0
Ta 32,v1,20480
Ta 34,v1,20992
Mvaa 192,36,148
Da 76,v2,8192
Mvaa 192,224,188
Mvaa 200,36,152
Da 116,v3,8192
Mvaa 200,224,192
Mvaa 208,36,156
Mvaa 208,224,196
W_ 9
Mvaa 212,24,196
s_nop 0
Ta 36,v1,16392
Ta 38,v1,16904
Mvaa 212,20,156
Mvaa 204,24,192
Mvaa 204,20,152
s_nop 0
Ta 224,v1,20488
Ta 226,v1,21000
Mvaa 196,24,188
Mvaa 196,20,148
Mvaa 188,24,184
Mvaa 188,20,144
Mvaa 180,24,180
s_waitcnt vmcnt(15) lgkmcnt(0)
s_barrier
s_cmp_eq_u32 s11, 1
s_cbranch_scc1 .Lpace17
.Lpace17_back:
Mvaa 180,20,140
S_ s65,s59,5505024
Mvaa 216,28,140
S_ m0,s52,16384
Ta 20,%0,24576
Ta 22,%0,25088
Mvaa 216,32,180
L_ v6,28,s65,0
Mvaa 224,28,144
S_ s65,s59,5603328
Mvaa 224,32,184
S_ m0,s52,17408
Ta 24,%0,28672
Ta 26,%0,29184
Mvaa 232,28,148
Mvaa 232,32,188
L_ v6,28,s65,0
Mvaa 240,28,152
S_ s65,s59,5701632
Mvaa 240,32,192
S_ m0,s52,18432
Mvaa 248,28,156
Mvaa 248,32,196
Mvaa 252,224,196
s_nop 0
Ta 28,%0,24584
Ta 30,%0,25096
Mvaa 252,36,156
L_ v6,28,s65,0
Mvaa 244,224,192
S_ s65,s59,5799936
Mvaa 244,36,152
S_ m0,s52,19456
Ta 32,%0,28680
Ta 34,%0,29192
Mvaa 236,224,188
Mvaa 236,36,148
L_ v6,28,s65,0
Mvaa 228,224,184
S_ s65,s60,1024
Mvaa 228,36,144
S_ m0,s50,0
Mvaa 220,224,180
Mvaa 220,36,140
W_ 4
Mvaa 16,20,60
s_nop 0
Ta 36,v1,24576
Ta 38,v1,25088
Mvaa 16,24,100
L_ v4,24,s65,0
Mvaa 24,20,64
S_ s65,s60,11264
Mvaa 24,24,104
S_ m0,s50,1024
Ta 224,v1,28672
Ta 226,v1,29184
Mvaa 32,20,68
Mvaa 32,24,108
L_ v5,24,s65,0
Mvaa 40,20,72
S_ s65,s60,21504
Mvaa 40,24,112
S_ m0,s50,2048
Mvaa 48,20,76
Mvaa 48,24,116
W_ 4
Mvaa 52,32,116
s_nop 0
Ta 20,v1,24584
Ta 22,v1,25096
Mvaa 52,28,76
L_ v4,24,s65,0
Mvaa 44,32,112
S_ s65,s60,31744
Mvaa 44,28,72
S_ m0,s50,3072
Ta 24,v1,28680
Ta 26,v1,29192
Mvaa 36,32,108
Mvaa 36,28,68
L_ v5,24,s65,0
Mvaa 28,32,104
S_ s65,s60,41984
Mvaa 28,28,64
S_ m0,s50,4096
Mvaa 20,32,100
s_waitcnt vmcnt(14) lgkmcnt(0)
s_barrier
Mvaa 20,28,60
L_ v4,24,s65,0
Mvaa 56,36,60
S_ s65,s59,5505024
Ta 28,%0,0
Ta 30,%0,512
Mvaa 56,224,100
S_ m0,s52,24448
Mvaa 64,36,64
Mvaa 64,224,104
s_nop 0
Ta 32,%0,4096
Ta 34,%0,4608
Mvaa 72,36,68
L_ v6,28,s65,128
Mvaa 72,224,108
S_ s65,s59,5603328
Mvaa 80,36,72
S_ m0,s52,25472
Mvaa 80,224,112
Mvaa 88,36,76
L_ v6,28,s65,128
Mvaa 88,224,116
S_ s65,s59,5701632
Mvaa 92,24,116
S_ m0,s52,26496
Ta 36,%0,8
Ta 38,%0,520
Mvaa 92,20,76
Mvaa 84,24,112
L_ v6,28,s65,128
Mvaa 84,20,72
S_ s65,s59,5799936
Ta 224,%0,4104
Ta 226,%0,4616
Mvaa 76,24,108
S_ m0,s52,27520
Mvaa 76,20,68
Mvaa 68,24,104
L_ v6,28,s65,128
Mvaa 68,20,64
S_ s65,s61,0
Mvaa 60,24,100
S_ m0,s51,36864
Mvaa 60,20,60
W_ 4
Mvaa 96,28,60
s_nop 0
Ta 20,v1,0
Ta 22,v1,512
Mvaa 96,32,100
Ln v7,32,s65,0
Mvaa 104,28,64
Mvaa 104,32,104
s_nop 0
Ta 24,v1,4096
Ta 26,v1,4608
Mvaa 112,28,68
Mvaa 112,32,108
Ln v8,32,s65,3072
Mvaa 120,28,72
S_ m0,s51,37760
Mvaa 120,32,112
Mvaa 128,28,76
Mvaa 128,32,116
W_ 4
Mvaa 132,224,116
s_nop 0
Ta 28,v1,8
Ta 30,v1,520
Mvaa 132,36,76
Ln v7,32,s65,128
Mvaa 124,224,112
Mvaa 124,36,72
s_nop 0
Ta 32,v1,4104
Ta 34,v1,4616
Mvaa 116,224,108
Mvaa 116,36,68
Ln v8,32,s65,3200
Mvaa 108,224,104
S_ m0,s51,38656
Mvaa 108,36,64
Mvaa 100,224,100
s_waitcnt vmcnt(17) lgkmcnt(0)
s_barrier
s_cmp_eq_u32 s11, 1
s_cbranch_scc1 .Lpace19
.Lpace19_back:
Mvaa 100,36,60
Da 140,v2,20480
Mvaa 136,20,60
s_nop 0
Ta 36,%0,8192
Ta 38,%0,8704
Mvaa 136,24,100
Ln v7,32,s65,256
Mvaa 144,20,64
Da 180,v3,20480
Mvaa 144,24,104
s_nop 0
Ta 224,%0,12288
Ta 226,%0,12800
Mvaa 152,20,68
Mvaa 152,24,108
Da 144,v2,22528
Mvaa 160,20,72
Ln v8,32,s65,3328
Mvaa 160,24,112
S_ s65,s59,5505024
Mvaa 168,20,76
S_ m0,s52,-256
Da 184,v3,22528
Mvaa 168,24,116
Mvaa 172,32,116
s_nop 0
Ta 20,%0,8200
Ta 22,%0,8712
Mvaa 172,28,76
Da 148,v2,24576
Mvaa 164,32,112
L_ v6,28,s65,256
Mvaa 164,28,72
S_ s65,s59,5603328
Ta 24,%0,12296
Ta 26,%0,12808
Mvaa 156,32,108
S_ m0,s52,768
Da 188,v3,24576
Mvaa 156,28,68
Mvaa 148,32,104
L_ v6,28,s65,256
Mvaa 148,28,64
S_ s65,s59,5701632
Da 152,v2,26624
Mvaa 140,32,100
S_ m0,s52,1792
Mvaa 140,28,60
W_ 9
Mvaa 176,36,60
s_nop 0
Ta 28,v1,8192
Ta 30,v1,8704
Mvaa 176,224,100
Da 192,v3,26624
Mvaa 184,36,64
L_ v6,28,s65,256
Mvaa 184,224,104
S_ s65,s59,5799936
Ta 32,v1,12288
Ta 34,v1,12800
Mvaa 192,36,68
S_ m0,s52,2816
Da 156,v2,28672
Mvaa 192,224,108
Mvaa 200,36,72
Da 196,v3,28672
Mvaa 200,224,112
L_ v6,28,s65,256
Mvaa 208,36,76
Mvaa 208,224,116
W_ 9
Mvaa 212,24,116
s_nop 0
Ta 36,v1,8200
Ta 38,v1,8712
Mvaa 212,20,76
Mvaa 204,24,112
Mvaa 204,20,72
s_nop 0
Ta 224,v1,12296
Ta 226,v1,12808
Mvaa 196,24,108
Mvaa 196,20,68
Mvaa 188,24,104
Mvaa 188,20,64
Mvaa 180,24,100
s_waitcnt vmcnt(19) lgkmcnt(0)
s_barrier
Mvaa 180,20,60
S_ s65,s59,6291456
Mvaa 216,28,60
S_ m0,s52,7936
Ta 20,%0,16384
Ta 22,%0,16896
Mvaa 216,32,100
L_ v6,28,s65,256
Mvaa 224,28,64
S_ s65,s59,6389760
Mvaa 224,32,104
S_ m0,s52,8960
Ta 24,%0,20480
Ta 26,%0,20992
Mvaa 232,28,68
Mvaa 232,32,108
L_ v6,28,s65,256
Mvaa 240,28,72
S_ s65,s59,6488064
Mvaa 240,32,112
S_ m0,s52,9984
Mvaa 248,28,76
Mvaa 248,32,116
Mvaa 252,224,116
s_nop 0
Ta 28,%0,16392
Ta 30,%0,16904
Mvaa 252,36,76
L_ v6,28,s65,256
Mvaa 244,224,112
S_ s65,s59,6586368
Mvaa 244,36,72
S_ m0,s52,11008
Ta 32,%0,20488
Ta 34,%0,21000
Mvaa 236,224,108
Mvaa 236,36,68
L_ v6,28,s65,256
Mvaa 228,224,104
S_ s65,s60,1152
Mvaa 228,36,64
S_ m0,s50,20480
Mvaa 220,224,100
Mvaa 220,36,60
W_ 4
Mvaa 16,20,140
s_nop 0
Ta 36,v1,16384
Ta 38,v1,16896
Mvaa 16,24,180
L_ v4,24,s65,0
Mvaa 24,20,144
S_ s65,s60,11392
Mvaa 24,24,184
S_ m0,s50,21504
Ta 224,v1,20480
Ta 226,v1,20992
Mvaa 32,20,148
Mvaa 32,24,188
L_ v5,24,s65,0
Mvaa 40,20,152
S_ s65,s60,21632
Mvaa 40,24,192
S_ m0,s50,22528
Mvaa 48,20,156
Mvaa 48,24,196
W_ 4
Mvaa 52,32,196
s_nop 0
Ta 20,v1,16392
Ta 22,v1,16904
Mvaa 52,28,156
L_ v4,24,s65,0
Mvaa 44,32,192
S_ s65,s60,31872
Mvaa 44,28,152
S_ m0,s50,23552
Ta 24,v1,20488
Ta 26,v1,21000
Mvaa 36,32,188
Mvaa 36,28,148
L_ v5,24,s65,0
Mvaa 28,32,184
S_ s65,s60,42112
Mvaa 28,28,144
S_ m0,s50,24576
Mvaa 20,32,180
s_waitcnt vmcnt(18) lgkmcnt(0)
s_barrier
s_cmp_eq_u32 s11, 1
s_cbranch_scc1 .Lpace21
.Lpace21_back:
Mvaa 20,28,140
L_ v4,24,s65,0
Mvaa 56,36,140
S_ s65,s59,7077888
Ta 28,%0,24576
Ta 30,%0,25088
Mvaa 56,224,180
S_ m0,s52,16128
Mvaa 64,36,144
Mvaa 64,224,184
s_nop 0
Ta 32,%0,28672
Ta 34,%0,29184
Mvaa 72,36,148
L_ v6,28,s65,256
Mvaa 72,224,188
S_ s65,s59,7176192
Mvaa 80,36,152
S_ m0,s52,17152
Mvaa 80,224,192
Mvaa 88,36,156
L_ v6,28,s65,256
Mvaa 88,224,196
S_ s65,s59,7274496
Mvaa 92,24,196
S_ m0,s52,18176
Ta 36,%0,24584
Ta 38,%0,25096
Mvaa 92,20,156
Mvaa 84,24,192
L_ v6,28,s65,256
Mvaa 84,20,152
S_ s65,s59,7372800
Ta 224,%0,28680
Ta 226,%0,29192
Mvaa 76,24,188
S_ m0,s52,19200
Mvaa 76,20,148
Mvaa 68,24,184
L_ v6,28,s65,256
Mvaa 68,20,144
S_ s65,s61,12288
Mvaa 60,24,180
S_ m0,s51,49152
Mvaa 60,20,140
W_ 4
Mvaa 96,28,140
s_nop 0
Ta 20,v1,24576
Ta 22,v1,25088
Mvaa 96,32,180
Ln v7,32,s65,0
Mvaa 104,28,144
Mvaa 104,32,184
s_nop 0
Ta 24,v1,28672
Ta 26,v1,29184
Mvaa 112,28,148
Mvaa 112,32,188
Ln v8,32,s65,3072
Mvaa 120,28,152
S_ m0,s51,50048
Mvaa 120,32,192
Mvaa 128,28,156
Mvaa 128,32,196
W_ 4
Mvaa 132,224,196
s_nop 0
Ta 28,v1,24584
Ta 30,v1,25096
Mvaa 132,36,156
Ln v7,32,s65,128
Mvaa 124,224,192
Mvaa 124,36,152
s_nop 0
Ta 32,v1,28680
Ta 34,v1,29192
Mvaa 116,224,188
Mvaa 116,36,148
Ln v8,32,s65,3200
Mvaa 108,224,184
S_ m0,s51,50944
Mvaa 108,36,144
Mvaa 100,224,180
s_waitcnt vmcnt(17) lgkmcnt(0)
s_barrier
Mvaa 100,36,140
Da 60,v2,0
Mvaa 136,20,140
s_nop 0
Ta 36,%0,0
Ta 38,%0,512
Mvaa 136,24,180
Ln v7,32,s65,256
Mvaa 144,20,144
Da 100,v3,0
Mvaa 144,24,184
s_nop 0
Ta 224,%0,4096
Ta 226,%0,4608
Mvaa 152,20,148
Mvaa 152,24,188
Da 64,v2,2048
Mvaa 160,20,152
Ln v8,32,s65,3328
Mvaa 160,24,192
S_ s65,s59,6291456
Mvaa 168,20,156
S_ m0,s52,24448
Da 104,v3,2048
Mvaa 168,24,196
Mvaa 172,32,196
s_nop 0
Ta 20,%0,8
Ta 22,%0,520
Mvaa 172,28,156
Da 68,v2,4096
Mvaa 164,32,192
L_ v6,28,s65,128
Mvaa 164,28,152
S_ s65,s59,6389760
Ta 24,%0,4104
Ta 26,%0,4616
Mvaa 156,32,188
S_ m0,s52,25472
Da 108,v3,4096
Mvaa 156,28,148
Mvaa 148,32,184
L_ v6,28,s65,128
Mvaa 148,28,144
S_ s65,s59,6488064
Da 72,v2,6144
Mvaa 140,32,180
S_ m0,s52,26496
Mvaa 140,28,140
W_ 9
Mvaa 176,36,140
s_nop 0
Ta 28,v1,0
Ta 30,v1,512
Mvaa 176,224,180
Da 112,v3,6144
Mvaa 184,36,144
L_ v6,28,s65,128
Mvaa 184,224,184
S_ s65,s59,6586368
Ta 32,v1,4096
Ta 34,v1,4608
Mvaa 192,36,148
S_ m0,s52,27520
Da 76,v2,8192
Mvaa 192,224,188
Mvaa 200,36,152
Da 116,v3,8192
Mvaa 200,224,192
L_ v6,28,s65,128
Mvaa 208,36,156
Mvaa 208,224,196
W_ 9
Mvaa 212,24,196
s_nop 0
Ta 36,v1,8
Ta 38,v1,520
Mvaa 212,20,156
Mvaa 204,24,192
Mvaa 204,20,152
s_nop 0
Ta 224,v1,4104
Ta 226,v1,4616
Mvaa 196,24,188
Mvaa 196,20,148
Mvaa 188,24,184
Mvaa 188,20,144
Mvaa 180,24,180
s_waitcnt vmcnt(14) lgkmcnt(0)
s_barrier
s_cmp_eq_u32 s11, 1
s_cbranch_scc1 .Lpace23
.Lpace23_back:
Mvaa 180,20,140
S_ s65,s59,7077888
Mvaa 216,28,140
S_ m0,s52,-128
Ta 20,v1,8200
Ta 22,v1,8712
Mvaa 216,32,180
L_ v6,28,s65,128
Mvaa 224,28,144
S_ s65,s59,7176192
Mvaa 224,32,184
S_ m0,s52,896
Ta 24,v1,12296
Ta 26,v1,12808
Mvaa 232,28,148
Mvaa 232,32,188
L_ v6,28,s65,128
Mvaa 240,28,152
S_ s65,s59,7274496
Mvaa 240,32,192
S_ m0,s52,1920
Mvaa 248,28,156
Mvaa 248,32,196
Mvaa 252,224,196
s_nop 0
Ta 28,v1,8192
Ta 30,v1,8704
Mvaa 252,36,156
L_ v6,28,s65,128
Mvaa 244,224,192
S_ s65,s59,7372800
Mvaa 244,36,152
S_ m0,s52,2944
Ta 32,v1,12288
Ta 34,v1,12800
Mvaa 236,224,188
Mvaa 236,36,148
L_ v6,28,s65,128
Mvaa 228,224,184
Mvaa 228,36,144
Mvaa 220,224,180
Mvaa 220,36,140
W_ 4
Mvaa 220,20,60
s_nop 0
Ta 36,%0,8200
Ta 38,%0,8712
Mvaa 220,24,100
Da 140,v2,20480
Mvaa 228,20,64
Mvaa 228,24,104
s_nop 0
Ta 224,%0,12296
Ta 226,%0,12808
Mvaa 236,20,68
Da 180,v3,20480
Mvaa 236,24,108
Mvaa 244,20,72
Da 144,v2,22528
Mvaa 244,24,112
Mvaa 252,20,76
Mvaa 252,24,116
W_ 7
Da 184,v3,22528
Mvaa 248,32,116
s_nop 0
Ta 20,%0,8192
Ta 22,%0,8704
Mvaa 248,28,76
Mvaa 240,32,112
Da 148,v2,24576
Mvaa 240,28,72
s_nop 0
Ta 24,%0,12288
Ta 26,%0,12800
Mvaa 232,32,108
Mvaa 232,28,68
Da 188,v3,24576
Mvaa 224,32,104
Mvaa 224,28,64
Mvaa 216,32,100
s_waitcnt vmcnt(14) lgkmcnt(0)
s_barrier
Da 152,v2,26624
Mvaa 216,28,60
S_ s65,s59,6291456
Mvaa 180,36,60
S_ m0,s52,8192
Ta 28,v1,16392
Ta 30,v1,16904
Mvaa 180,224,100
Da 192,v3,26624
Mvaa 188,36,64
L_ v6,28,s65,0
Mvaa 188,224,104
S_ s65,s59,6389760
Ta 32,v1,20488
Ta 34,v1,21000
Mvaa 196,36,68
S_ m0,s52,9216
Da 156,v2,28672
Mvaa 196,224,108
Mvaa 204,36,72
L_ v6,28,s65,0
Mvaa 204,224,112
S_ s65,s59,6488064
Da 196,v3,28672
Mvaa 212,36,76
S_ m0,s52,10240
Mvaa 212,224,116
Mvaa 208,24,116
s_nop 0
Ta 36,v1,16384
Ta 38,v1,16896
Mvaa 208,20,76
L_ v6,28,s65,0
Mvaa 200,24,112
S_ s65,s59,6586368
Mvaa 200,20,72
S_ m0,s52,11264
Ta 224,v1,20480
Ta 226,v1,20992
Mvaa 192,24,108
Mvaa 192,20,68
L_ v6,28,s65,0
Mvaa 184,24,104
Mvaa 184,20,64
Mvaa 176,24,100
Mvaa 176,20,60
W_ 6
Mvaa 220,28,140
s_nop 0
Ta 20,%0,16392
Ta 22,%0,16904
Mvaa 220,32,180
Mvaa 228,28,144
Mvaa 228,32,184
s_nop 0
Ta 24,%0,20488
Ta 26,%0,21000
Mvaa 236,28,148
Mvaa 236,32,188
Mvaa 244,28,152
Mvaa 244,32,192
W_ 9
Mvaa 252,28,156
W_ 8
Mvaa 252,32,196
W_ 4
Mvaa 248,224,196
s_nop 0
Ta 28,%0,16384
Ta 30,%0,16896
Mvaa 248,36,156
Mvaa 240,224,192
Mvaa 240,36,152
s_nop 0
Ta 32,%0,20480
Ta 34,%0,20992
Mvaa 232,224,188
Mvaa 232,36,148
Mvaa 224,224,184
Mvaa 224,36,144
Mvaa 216,224,180
s_waitcnt vmcnt(8) lgkmcnt(0)
s_barrier
s_cmp_eq_u32 s11, 1
s_cbranch_scc1 .Lpace25
.Lpace25_back:
Mvaa 216,36,140
S_ s65,s59,7077888
Mvaa 180,20,140
S_ m0,s52,16384
Ta 36,v1,24584
Ta 38,v1,25096
Mvaa 180,24,180
L_ v6,28,s65,0
Mvaa 188,20,144
S_ s65,s59,7176192
Mvaa 188,24,184
S_ m0,s52,17408
Ta 224,v1,28680
Ta 226,v1,29192
Mvaa 196,20,148
U_ v248,s48,v248
U_ v249,s48,v249
Mvaa 196,24,188
L_ v6,28,s65,0
Mvaa 204,20,152
S_ s65,s59,7274496
U_ v250,s48,v250
U_ v251,s48,v251
Mvaa 204,24,192
S_ m0,s52,18432
U_ v252,s48,v252
U_ v253,s48,v253
Mvaa 212,20,156
U_ v254,s48,v254
U_ v255,s48,v255
Mvaa 212,24,196
C_ 255,254,255
C_ 254,252,253
Mvaa 208,32,196
s_nop 0
Ta 20,v1,24576
Ta 22,v1,25088
Mvaa 208,28,156
L_ v6,28,s65,0
Mvaa 200,32,192
S_ s65,s59,7372800
C_ 253,250,251
C_ 252,248,249
Mvaa 200,28,152
S_ m0,s52,19456
Ta 24,v1,28672
Ta 26,v1,29184
Mvaa 192,32,188
U_ v240,s48,v240
U_ v241,s48,v241
Mvaa 192,28,148
L_ v6,28,s65,0
Mvaa 184,32,184
U_ v242,s48,v242
U_ v243,s48,v243
Mvaa 184,28,144
U_ v244,s48,v244
U_ v245,s48,v245
Mvaa 176,32,180
U_ v246,s48,v246
U_ v247,s48,v247
Mvaa 176,28,140
W_ 4
C_ 251,246,247
C_ 250,244,245
Mvaa 140,36,60
s_nop 0
Ta 28,%0,24584
Ta 30,%0,25096
Mvaa 140,224,100
C_ 249,242,243
C_ 248,240,241
Mvaa 148,36,64
U_ v232,s48,v232
U_ v233,s48,v233
Mvaa 148,224,104
s_nop 0
Ta 32,%0,28680
Ta 34,%0,29192
Mvaa 156,36,68
U_ v234,s48,v234
U_ v235,s48,v235
Mvaa 156,224,108
U_ v236,s48,v236
U_ v237,s48,v237
Mvaa 164,36,72
U_ v238,s48,v238
U_ v239,s48,v239
Mvaa 164,224,112
C_ 247,238,239
C_ 246,236,237
Mvaa 172,36,76
C_ 245,234,235
C_ 244,232,233
Mvaa 172,224,116
W_ 4
U_ v224,s48,v224
U_ v225,s48,v225
Mvaa 168,24,116
s_nop 0
Ta 36,%0,24576
Ta 38,%0,25088
Mvaa 168,20,76
U_ v226,s48,v226
U_ v227,s48,v227
Mvaa 160,24,112
U_ v228,s48,v228
U_ v229,s48,v229
Mvaa 160,20,72
s_nop 0
Ta 224,%0,28672
Ta 226,%0,29184
Mvaa 152,24,108
U_ v230,s48,v230
U_ v231,s48,v231
Mvaa 152,20,68
C_ 243,230,231
C_ 242,228,229
Mvaa 144,24,104
C_ 241,226,227
C_ 240,224,225
Mvaa 144,20,64
U_ v216,s48,v216
U_ v217,s48,v217
Mvaa 136,24,100
s_waitcnt vmcnt(8) lgkmcnt(0)
s_barrier
U_ v218,s48,v218
Mvaa 136,20,60
S_ s65,s61,0
U_ v219,s48,v219
Mvaa 100,28,60
S_ m0,s51,24576
Ta 20,v1,8
Ta 22,v1,520
Mvaa 100,32,100
Ln v9,36,s65,0
Mvaa 108,28,64
U_ v220,s48,v220
U_ v221,s48,v221
Mvaa 108,32,104
s_nop 0
Ta 24,v1,4104
Ta 26,v1,4616
Mvaa 116,28,68
U_ v222,s48,v222
U_ v223,s48,v223
Mvaa 116,32,108
Ln v9,36,s65,3072
Mvaa 124,28,72
S_ m0,s51,25472
C_ 239,222,223
C_ 238,220,221
Mvaa 124,32,112
C_ 237,218,219
C_ 236,216,217
Mvaa 132,28,76
U_ v208,s48,v208
U_ v209,s48,v209
Mvaa 132,32,116
U_ v210,s48,v210
U_ v211,s48,v211
Mvaa 128,224,116
s_nop 0
Ta 28,v1,0
Ta 30,v1,512
Mvaa 128,36,76
Ln v9,36,s65,128
Mvaa 120,224,112
U_ v212,s48,v212
U_ v213,s48,v213
Mvaa 120,36,72
s_nop 0
Ta 32,v1,4096
Ta 34,v1,4608
Mvaa 112,224,108
U_ v214,s48,v214
U_ v215,s48,v215
Mvaa 112,36,68
Ln v9,36,s65,3200
Mvaa 104,224,104
S_ m0,s51,26368
C_ 235,214,215
C_ 234,212,213
Mvaa 104,36,64
C_ 233,210,211
C_ 232,208,209
Mvaa 96,224,100
U_ v200,s48,v200
U_ v201,s48,v201
Mvaa 96,36,60
W_ 4
)A"
R"A(U_ v202,s48,v202
U_ v203,s48,v203
Mvaa 140,20,140
s_nop 0
Ta 36,%0,8
Ta 38,%0,520
Mvaa 140,24,180
Ln v9,36,s65,256
Mvaa 148,20,144
U_ v204,s48,v204
U_ v205,s48,v205
Mvaa 148,24,184
s_nop 0
Ta 224,%0,4104
Ta 226,%0,4616
Mvaa 156,20,148
U_ v206,s48,v206
U_ v207,s48,v207
Mvaa 156,24,188
Ln v9,36,s65,3328
Mvaa 164,20,152
C_ 231,206,207
C_ 230,204,205
Mvaa 164,24,192
C_ 229,202,203
C_ 228,200,201
Mvaa 172,20,156
U_ v192,s48,v192
U_ v193,s48,v193
Mvaa 172,24,196
W_ 4
U_ v194,s48,v194
U_ v195,s48,v195
Mvaa 168,32,196
s_nop 0
Ta 20,%0,0
Ta 22,%0,512
Mvaa 168,28,156
U_ v196,s48,v196
U_ v197,s48,v197
Mvaa 160,32,192
U_ v198,s48,v198
U_ v199,s48,v199
Mvaa 160,28,152
s_nop 0
Ta 24,%0,4096
Ta 26,%0,4608
Mvaa 152,32,188
C_ 227,198,199
C_ 226,196,197
Mvaa 152,28,148
C_ 225,194,195
C_ 224,192,193
Mvaa 144,32,184
U_ v184,s48,v184
U_ v185,s48,v185
Mvaa 144,28,144
U_ v186,s48,v186
U_ v187,s48,v187
Mvaa 136,32,180
s_waitcnt vmcnt(10) lgkmcnt(0)
s_barrier
s_cmp_eq_u32 s11, 1
s_cbranch_scc1 .Lpace27
.Lpace27_back:
U_ v188,s48,v188
Mvaa 136,28,140
U_ v189,s48,v189
Mvaa 100,36,140
s_nop 0
Ta 28,v1,8200
Ta 30,v1,8712
Mvaa 100,224,180
U_ v190,s48,v190
U_ v191,s48,v191
Mvaa 108,36,144
C_ 223,190,191
C_ 222,188,189
Mvaa 108,224,184
s_nop 0
Ta 32,v1,12296
Ta 34,v1,12808
Mvaa 116,36,148
C_ 221,186,187
C_ 220,184,185
Mvaa 116,224,188
U_ v176,s48,v176
U_ v177,s48,v177
Mvaa 124,36,152
U_ v178,s48,v178
U_ v179,s48,v179
Mvaa 124,224,192
U_ v180,s48,v180
U_ v181,s48,v181
Mvaa 132,36,156
U_ v182,s48,v182
U_ v183,s48,v183
Mvaa 132,224,196
C_ 219,182,183
C_ 218,180,181
Mvaa 128,24,196
s_nop 0
Ta 36,v1,8192
Ta 38,v1,8704
Mvaa 128,20,156
C_ 217,178,179
C_ 216,176,177
Mvaa 120,24,192
U_ v168,s48,v168
U_ v169,s48,v169
Mvaa 120,20,152
s_nop 0
Ta 224,v1,12288
Ta 226,v1,12800
Mvaa 112,24,188
U_ v170,s48,v170
U_ v171,s48,v171
Mvaa 112,20,148
U_ v172,s48,v172
U_ v173,s48,v173
Mvaa 104,24,184
U_ v174,s48,v174
U_ v175,s48,v175
Mvaa 104,20,144
C_ 215,174,175
C_ 214,172,173
Mvaa 96,24,180
C_ 213,170,171
C_ 212,168,169
Mvaa 96,20,140
W_ 4
U_ v160,s48,v160
U_ v161,s48,v161
Mvaa 60,28,60
s_nop 0
Ta 20,%0,8200
Ta 22,%0,8712
Mvaa 60,32,100
U_ v162,s48,v162
U_ v163,s48,v163
Mvaa 68,28,64
U_ v164,s48,v164
U_ v165,s48,v165
Mvaa 68,32,104
s_nop 0
Ta 24,%0,12296
Ta 26,%0,12808
Mvaa 76,28,68
U_ v166,s48,v166
U_ v167,s48,v167
Mvaa 76,32,108
C_ 211,166,167
C_ 210,164,165
Mvaa 84,28,72
C_ 209,162,163
C_ 208,160,161
Mvaa 84,32,112
U_ v152,s48,v152
U_ v153,s48,v153
Mvaa 92,28,76
U_ v154,s48,v154
U_ v155,s48,v155
Mvaa 92,32,116
W_ 4
U_ v156,s48,v156
U_ v157,s48,v157
Mvaa 88,224,116
s_nop 0
Ta 28,%0,8192
Ta 30,%0,8704
Mvaa 88,36,76
U_ v158,s48,v158
U_ v159,s48,v159
Mvaa 80,224,112
C_ 207,158,159
C_ 206,156,157
Mvaa 80,36,72
s_nop 0
Ta 32,%0,12288
Ta 34,%0,12800
Mvaa 72,224,108
C_ 205,154,155
C_ 204,152,153
Mvaa 72,36,68
U_ v144,s48,v144
U_ v145,s48,v145
Mvaa 64,224,104
U_ v146,s48,v146
U_ v147,s48,v147
Mvaa 64,36,64
U_ v148,s48,v148
U_ v149,s48,v149
Mvaa 56,224,100
s_waitcnt vmcnt(6) lgkmcnt(0)
s_barrier
U_ v150,s48,v150
Mvaa 56,36,60
S_ s65,s61,24576
U_ v151,s48,v151
Mvaa 20,20,60
S_ m0,s51,0
Ta 36,v1,16392
Ta 38,v1,16904
Mvaa 20,24,100
Ln v7,32,s65,0
Mvaa 28,20,64
C_ 203,150,151
C_ 202,148,149
Mvaa 28,24,104
s_nop 0
Ta 224,v1,20488
Ta 226,v1,21000
Mvaa 36,20,68
C_ 201,146,147
C_ 200,144,145
Mvaa 36,24,108
Ln v8,32,s65,3072
Mvaa 44,20,72
S_ m0,s51,896
U_ v136,s48,v136
U_ v137,s48,v137
Mvaa 44,24,112
U_ v138,s48,v138
U_ v139,s48,v139
Mvaa 52,20,76
U_ v140,s48,v140
U_ v141,s48,v141
Mvaa 52,24,116
U_ v142,s48,v142
U_ v143,s48,v143
Mvaa 48,32,116
s_nop 0
Ta 20,v1,16384
Ta 22,v1,16896
Mvaa 48,28,76
Ln v7,32,s65,128
Mvaa 40,32,112
C_ 199,142,143
C_ 198,140,141
Mvaa 40,28,72
s_nop 0
Ta 24,v1,20480
Ta 26,v1,20992
Mvaa 32,32,108
C_ 197,138,139
C_ 196,136,137
Mvaa 32,28,68
Ln v8,32,s65,3200
Mvaa 24,32,104
S_ m0,s51,1792
U_ v128,s48,v128
U_ v129,s48,v129
Mvaa 24,28,64
U_ v130,s48,v130
U_ v131,s48,v131
Mvaa 16,32,100
U_ v132,s48,v132
U_ v133,s48,v133
Mvaa 16,28,60
W_ 4
U_ v134,s48,v134
U_ v135,s48,v135
Mvaa 60,36,140
s_nop 0
Ta 28,%0,16392
Ta 30,%0,16904
Mvaa 60,224,180
Ln v7,32,s65,256
Mvaa 68,36,144
C_ 195,134,135
C_ 194,132,133
Mvaa 68,224,184
s_nop 0
Ta 32,%0,20488
Ta 34,%0,21000
Mvaa 76,36,148
C_ 193,130,131
C_ 192,128,129
Mvaa 76,224,188
Ln v8,32,s65,3328
Mvaa 84,36,152
U_ v120,s48,v120
U_ v121,s48,v121
Mvaa 84,224,192
U_ v122,s48,v122
U_ v123,s48,v123
Mvaa 92,36,156
U_ v124,s48,v124
U_ v125,s48,v125
Mvaa 92,224,196
W_ 4
U_ v126,s48,v126
U_ v127,s48,v127
Mvaa 88,24,196
s_nop 0
Ta 36,%0,16384
Ta 38,%0,16896
Mvaa 88,20,156
C_ 191,126,127
C_ 190,124,125
Mvaa 80,24,192
C_ 189,122,123
C_ 188,120,121
Mvaa 80,20,152
s_nop 0
Ta 224,%0,20480
Ta 226,%0,20992
Mvaa 72,24,188
U_ v112,s48,v112
U_ v113,s48,v113
Mvaa 72,20,148
U_ v114,s48,v114
U_ v115,s48,v115
Mvaa 64,24,184
U_ v116,s48,v116
U_ v117,s48,v117
Mvaa 64,20,144
U_ v118,s48,v118
U_ v119,s48,v119
Mvaa 56,24,180
s_waitcnt vmcnt(51) lgkmcnt(0)
s_barrier
s_cmp_eq_u32 s11, 1
s_cbranch_scc1 .Lpace29
.Lpace29_back:
C_ 187,118,119
Mvaa 56,20,140
S_ s65,s61,12288
C_ 186,116,117
Mvaa 20,28,140
S_ m0,s51,12288
C_ 185,114,115
C_ 184,112,113
Mvaa 20,32,180
Ln v9,36,s65,0
Mvaa 28,28,144
U_ v104,s48,v104
U_ v105,s48,v105
Mvaa 28,32,184
U_ v106,s48,v106
U_ v107,s48,v107
Mvaa 36,28,148
U_ v108,s48,v108
U_ v109,s48,v109
Mvaa 36,32,188
Ln v9,36,s65,3072
Mvaa 44,28,152
S_ m0,s51,13184
U_ v110,s48,v110
U_ v111,s48,v111
Mvaa 44,32,192
C_ 183,110,111
C_ 182,108,109
Mvaa 52,28,156
C_ 181,106,107
C_ 180,104,105
Mvaa 52,32,196
U_ v96,s48,v96
U_ v97,s48,v97
Mvaa 48,224,196
Ln v9,36,s65,128
Mvaa 48,36,156
U_ v98,s48,v98
U_ v99,s48,v99
Mvaa 40,224,192
U_ v100,s48,v100
U_ v101,s48,v101
Mvaa 40,36,152
U_ v102,s48,v102
U_ v103,s48,v103
Mvaa 32,224,188
Ln v9,36,s65,3200
Mvaa 32,36,148
S_ m0,s51,14080
C_ 179,102,103
C_ 178,100,101
Mvaa 24,224,184
C_ 177,98,99
C_ 176,96,97
Mvaa 24,36,144
v_pk_mul_f32 v[88:89], v[88:89], s[48:49]
v_pk_mul_f32 v[90:91], v[90:91], s[48:49]
Mvaa 16,224,180
Ln v9,36,s65,256
Mvaa 16,36,140
v_pk_mul_f32 v[92:93], v[92:93], s[48:49]
v_pk_mul_f32 v[94:95], v[94:95], s[48:49]
Ln v9,36,s65,3328
C_ 175,94,95
C_ 174,92,93
C_ 173,90,91
C_ 172,88,89
v_pk_mul_f32 v[80:81], v[80:81], s[48:49]
v_pk_mul_f32 v[82:83], v[82:83], s[48:49]
v_pk_mul_f32 v[84:85], v[84:85], s[48:49]
v_pk_mul_f32 v[86:87], v[86:87], s[48:49]
C_ 171,86,87
C_ 170,84,85
C_ 169,82,83
C_ 168,80,81
v_pk_mul_f32 v[72:73], v[72:73], s[48:49]
v_pk_mul_f32 v[74:75], v[74:75], s[48:49]
v_pk_mul_f32 v[76:77], v[76:77], s[48:49]
v_pk_mul_f32 v[78:79], v[78:79], s[48:49]
C_ 167,78,79
C_ 166,76,77
C_ 165,74,75
C_ 164,72,73
v_pk_mul_f32 v[40:41], v[40:41], s[48:49]
v_pk_mul_f32 v[42:43], v[42:43], s[48:49]
v_pk_mul_f32 v[44:45], v[44:45], s[48:49]
v_pk_mul_f32 v[46:47], v[46:47], s[48:49]
C_ 151,46,47
C_ 150,44,45
C_ 149,42,43
C_ 148,40,41
v_pk_mul_f32 v[32:33], v[32:33], s[48:49]
v_pk_mul_f32 v[34:35], v[34:35], s[48:49]
v_pk_mul_f32 v[36:37], v[36:37], s[48:49]
v_pk_mul_f32 v[38:39], v[38:39], s[48:49]
C_ 147,38,39
C_ 146,36,37
C_ 145,34,35
C_ 144,32,33
v_pk_mul_f32 v[24:25], v[24:25], s[48:49]
v_pk_mul_f32 v[26:27], v[26:27], s[48:49]
v_pk_mul_f32 v[28:29], v[28:29], s[48:49]
v_pk_mul_f32 v[30:31], v[30:31], s[48:49]
C_ 143,30,31
C_ 142,28,29
C_ 141,26,27
C_ 140,24,25
R_ %0,a244
v_xor_b32 v1, 64, %0
Dv 30,%0,43008
Dv 34,v1,43008
Dv 38,%0,44032
v_pk_mul_f32 v[64:65], v[64:65], s[48:49]
v_pk_mul_f32 v[66:67], v[66:67], s[48:49]
v_pk_mul_f32 v[68:69], v[68:69], s[48:49]
v_pk_mul_f32 v[70:71], v[70:71], s[48:49]
C_ 163,70,71
C_ 162,68,69
C_ 161,66,67
C_ 160,64,65
v_pk_mul_f32 v[56:57], v[56:57], s[48:49]
v_pk_mul_f32 v[58:59], v[58:59], s[48:49]
v_pk_mul_f32 v[60:61], v[60:61], s[48:49]
v_pk_mul_f32 v[62:63], v[62:63], s[48:49]
C_ 159,62,63
C_ 158,60,61
C_ 157,58,59
C_ 156,56,57
v_pk_mul_f32 v[48:49], v[48:49], s[48:49]
v_pk_mul_f32 v[50:51], v[50:51], s[48:49]
v_pk_mul_f32 v[52:53], v[52:53], s[48:49]
v_pk_mul_f32 v[54:55], v[54:55], s[48:49]
C_ 155,54,55
C_ 154,52,53
C_ 153,50,51
C_ 152,48,49
v_pk_mul_f32 v[16:17], v[16:17], s[48:49]
v_pk_mul_f32 v[18:19], v[18:19], s[48:49]
v_pk_mul_f32 v[20:21], v[20:21], s[48:49]
v_pk_mul_f32 v[22:23], v[22:23], s[48:49]
C_ 139,22,23
C_ 138,20,21
C_ 137,18,19
C_ 136,16,17
s_cmp_eq_u32 s58, 1
s_cbranch_scc1 .Latt1
s_cmp_eq_u32 s58, 2
s_cbranch_scc1 .Latt2
.Latt0:
s_cmp_eq_u32 s4, 0
s_cbranch_scc0 .Lhs_tok0
R_ v11,a241
buffer_load_dword a255, v11, s[92:95], 0 offen offset:0 sc1
.Lhs_tok0:
S_ s59,s59,768
W_ 2
Zvvv 96,30,136
Dv 42,v1,44032
R_ v2,a245
Zvvv 100,30,140
v_xor_b32 v3, 32, v2
v_xor_b32 v4, 64, v2
Zvvv 104,30,144
v_xor_b32 v5, 96, v2
Zvvv 108,30,148
s_cmp_eq_u32 s4, 0
s_cselect_b32 s67, 13, 0
Zvvv 112,30,152
W_ 2
s_or_b32 s67, s67, 8192
Mvvv 96,34,156
Dv 30,%0,45056
Mvvv 100,34,160
Mvvv 104,34,164
Mvvv 108,34,168
Mvvv 112,34,172
W_ 2
Mvvv 96,38,176
Dv 34,v1,45056
Mvvv 100,38,180
Mvvv 104,38,184
Mvvv 108,38,188
Mvvv 112,38,192
W_ 2
Mvvv 96,42,196
Dv 38,%0,36864
Mvvv 100,42,200
Mvvv 104,42,204
Mvvv 108,42,208
Mvvv 112,42,212
W_ 2
Mvvv 96,30,216
Dv 42,v1,36864
Mvvv 100,30,220
Mvvv 104,30,224
Mvvv 108,30,228
Mvvv 112,30,232
W_ 2
Mvvv 96,34,236
Dv 30,%0,37888
Mvvv 100,34,240
Mvvv 104,34,244
Mvvv 108,34,248
Mvvv 112,34,252
W_ 2
s_nop 1
v_max3_f32 v56, v96, v97, v98
v_max_f32 v56, v56, v99
Zvvv 76,38,136
s_memtime s[12:13]
Dv 34,v1,37888
v_max3_f32 v60, v100, v101, v102
Zvvv 80,38,140
s_memrealtime s[100:101]
v_max_f32 v60, v60, v103
Zvvv 84,38,144
v_max3_f32 v64, v104, v105, v106
v_max_f32 v64, v64, v107
Zvvv 88,38,148
v_max3_f32 v68, v108, v109, v110
v_max_f32 v68, v68, v111
Zvvv 92,38,152
W_ 2
v_max3_f32 v72, v112, v113, v114
v_max_f32 v72, v72, v115
Mvvv 76,42,156
Dv 38,%0,38912
Mvvv 80,42,160
Mvvv 84,42,164
Mvvv 88,42,168
Mvvv 92,42,172
W_ 2
Mvvv 76,30,176
Dv 42,v1,38912
Mvvv 80,30,180
Mvvv 84,30,184
Mvvv 88,30,188
Mvvv 92,30,192
s_waitcnt vmcnt(34) lgkmcnt(0)
s_barrier
Mvvv 76,34,196
S_ s65,s61,184320
Dv 30,%0,55296
Mvvv 76,38,216
S_ m0,s51,36864
s_sub_u32 s12, s12, s90
Mvvv 76,42,236
L_ v9,36,s65,0
Mvvv 80,34,200
s_sub_u32 s100, s100, s68
Mvvv 80,38,220
L_ v9,36,s65,3072
Mvvv 80,42,240
S_ m0,s51,37760
s_sub_u32 s101, s68, s98
Mvvv 84,34,204
L_ v9,36,s65,128
Mvvv 84,38,224
s_mul_i32 s12, s12, s101
v_max3_f32 v56, v56, v76, v77
v_max3_f32 v56, v56, v78, v79
Mvvv 84,42,244
L_ v9,36,s65,3200
Mvvv 88,34,208
S_ m0,s51,38656
v_max3_f32 v60, v60, v80, v81
s_sub_u32 s101, s90, s88
Mvvv 88,38,228
L_ v9,36,s65,256
Mvvv 88,42,248
s_mul_i32 s100, s100, s101
v_max3_f32 v60, v60, v82, v83
Mvvv 92,34,212
L_ v9,36,s65,3328
Mvvv 92,38,232
s_lshr_b32 s100, s100, 6
v_max3_f32 v64, v64, v84, v85
v_max3_f32 v64, v64, v86, v87
Mvvv 92,42,252
s_mul_i32 s100, s100, 47
s_cmp_gt_u32 s101, 4000
s_cselect_b32 s13, 2, 0
s_cmp_gt_u32 s12, s100
s_cselect_b32 s13, s13, 0
Dv 34,v1,55296
.p2align 3
v_mov_b32 v11, 0x3f803f80
v_permlane32_swap_b32 v56, v60
v_max3_f32 v68, v68, v88, v89
v_max3_f32 v68, v68, v90, v91
v_max_f32 v56, v56, v60
v_max3_f32 v72, v72, v92, v93
v_max3_f32 v72, v72, v94, v95
v_mov_b32 v73, v72
v_permlane32_swap_b32 v64, v68
v_max_f32 v64, v64, v68
v_permlane32_swap_b32 v72, v73
v_max_f32 v72, v72, v73
v_mov_b32 v73, v72
v_permlane16_swap_b32 v56, v64
v_max_f32 v56, v56, v64
v_permlane16_swap_b32 v72, v73
B_ v56,0xc0e00000,v56
v_max_f32 v72, v72, v73
v_mov_b32 v64, v56
B_ v72,0xc0e00000,v72
v_mov_b32 v73, v72
v_permlane16_swap_b32 v56, v64
v_mov_b32 v60, v56
v_mov_b32 v68, v64
v_mov_b64 v[74:75], v[72:73]
v_permlane32_swap_b32 v56, v60
v_permlane32_swap_b32 v64, v68
v_mov_b32 v57, v56
v_mov_b32 v61, v60
v_mov_b32 v65, v64
v_mov_b32 v69, v68
v_pk_add_f32 v[96:97], v[96:97], v[56:57]
v_pk_add_f32 v[98:99], v[98:99], v[56:57]
v_pk_add_f32 v[76:77], v[76:77], v[56:57]
v_pk_add_f32 v[78:79], v[78:79], v[56:57]
v_mov_b64 v[58:59], v[56:57]
v_pk_add_f32 v[100:101], v[100:101], v[60:61]
v_pk_add_f32 v[102:103], v[102:103], v[60:61]
v_pk_add_f32 v[80:81], v[80:81], v[60:61]
v_pk_add_f32 v[82:83], v[82:83], v[60:61]
v_mov_b64 v[62:63], v[60:61]
v_pk_add_f32 v[104:105], v[104:105], v[64:65]
v_pk_add_f32 v[106:107], v[106:107], v[64:65]
v_pk_add_f32 v[84:85], v[84:85], v[64:65]
v_pk_add_f32 v[86:87], v[86:87], v[64:65]
v_mov_b64 v[66:67], v[64:65]
v_pk_add_f32 v[108:109], v[108:109], v[68:69]
v_pk_add_f32 v[110:111], v[110:111], v[68:69]
v_pk_add_f32 v[88:89], v[88:89], v[68:69]
v_pk_add_f32 v[90:91], v[90:91], v[68:69]
v_mov_b64 v[70:71], v[68:69]
v_pk_add_f32 v[112:113], v[112:113], v[72:73]
v_pk_add_f32 v[114:115], v[114:115], v[72:73]
v_pk_add_f32 v[92:93], v[92:93], v[72:73]
v_pk_add_f32 v[94:95], v[94:95], v[72:73]
E_ 96
E_ 97
E_ 98
E_ 99
E_ 76
E_ 77
E_ 78
E_ 79
C_ 99,98,99
C_ 98,96,97
C_ 96,76,77
C_ 97,78,79
E_ 100
E_ 101
E_ 102
E_ 103
E_ 80
E_ 81
E_ 82
E_ 83
C_ 103,102,103
C_ 102,100,101
C_ 100,80,81
C_ 101,82,83
.Lstep0_1:
W_ 1
Nvvv 116,30,136,56
s_nop 0
Dv 38,%0,56320
Dv 42,v1,56320
W_ 2
Mvvv 116,34,156
E_ 104
Nvvv 120,30,140,60
E_ 105
Mvvv 120,34,160
E_ 106
Nvvv 124,30,144,64
E_ 107
Mvvv 124,34,164
E_ 84
Nvvv 128,30,148,68
E_ 85
Mvvv 128,34,168
E_ 86
Nvvv 132,30,152,72
E_ 87
Mvvv 132,34,172
W_ 0
C_ 107,106,107
C_ 106,104,105
Mvvv 132,38,192
s_nop 0
Dv 30,%0,57344
Dv 34,v1,57344
Mvvv 132,42,212
C_ 104,84,85
C_ 105,86,87
Mvvv 128,38,188
E_ 108
Mvvv 128,42,208
E_ 109
Mvvv 124,38,184
E_ 110
Mvvv 124,42,204
E_ 111
Mvvv 120,38,180
E_ 88
Mvvv 120,42,200
E_ 89
Mvvv 116,38,176
E_ 90
Mvvv 116,42,196
W_ 0
E_ 91
Mvvv 116,30,216
s_nop 0
Dv 38,%0,49152
Dv 42,v1,49152
Mvvv 116,34,236
C_ 111,110,111
C_ 110,108,109
Mvvv 120,30,220
C_ 108,88,89
C_ 109,90,91
Mvvv 120,34,240
E_ 112
Mvvv 124,30,224
E_ 113
Mvvv 124,34,244
E_ 114
Mvvv 128,30,228
E_ 115
Mvvv 128,34,248
E_ 92
Mvvv 132,30,232
E_ 93
Mvvv 132,34,252
W_ 0
E_ 94
Nvvv 76,38,136,56
s_nop 0
Dv 30,%0,50176
Dv 34,v1,50176
Mvvv 76,42,156
E_ 95
Nvvv 80,38,140,60
C_ 115,114,115
C_ 114,112,113
Mvvv 80,42,160
C_ 112,92,93
C_ 113,94,95
Nvvv 84,38,144,64
E_ 116
Mvvv 84,42,164
E_ 117
Nvvv 88,38,148,68
E_ 118
Mvvv 88,42,168
E_ 119
Nvvv 92,38,152,72
C_ 119,118,119
C_ 118,116,117
Mvvv 92,42,172
W_ 0
E_ 120
Mvvv 92,30,192
s_nop 0
Dv 38,%0,51200
Dv 42,v1,51200
Mvvv 92,34,212
X_ a180,v11
X_ a181,v11
Mvvv 88,30,188
X_ a182,v11
X_ a183,v11
Mvvv 88,34,208
E_ 121
Mvvv 84,30,184
E_ 122
Mvvv 84,34,204
E_ 123
Mvvv 80,30,180
C_ 123,122,123
C_ 122,120,121
Mvvv 80,34,200
E_ 124
Mvvv 76,30,176
E_ 125
Mvvv 76,34,196
S_ s65,s61,36864
E_ 126
W_ 1
Mvvv 76,38,216
S_ m0,s51,49152
E_ 127
W_ 0
Mvvv 76,42,236
s_waitcnt vmcnt(18) lgkmcnt(0)
s_barrier
C_ 127,126,127
Mvvv 80,38,220
Ln v7,32,s65,0
Mvvv 80,42,240
C_ 126,124,125
Mvvv 84,38,224
Ln v8,32,s65,3072
S_ m0,s51,50048
Mvvv 84,42,244
s_nop 0
Tv 30,v2,24576
Tv 32,v2,30720
Mvvv 88,38,228
Ln v7,32,s65,128
Mvvv 88,42,248
s_nop 0
Tv 34,v3,24576
Tv 36,v3,30720
Mvvv 92,38,232
Ln v8,32,s65,3200
S_ m0,s51,50944
Mvvv 92,42,252
E_ 128
Zaav 220,180,96
Ln v7,32,s65,256
Zaav 224,180,100
E_ 129
Zaav 228,180,104
Ln v8,32,s65,3328
Zaav 232,180,108
E_ 130
Zaav 236,180,112
W_ 0
E_ 131
Zavv 0,30,96
s_nop 0
Tv 38,v4,24576
Tv 40,v4,30720
Zavv 20,34,96
C_ 131,130,131
C_ 130,128,129
Zavv 24,34,100
s_nop 0
Tv 42,v5,24576
Tv 44,v5,30720
Zavv 4,30,100
E_ 132
Zavv 8,30,104
E_ 133
Zavv 28,34,104
R_ v46,a220
R_ v47,a224
Zavv 32,34,108
R_ v48,a228
R_ v49,a232
Zavv 12,30,108
R_ v50,a236
Zavv 16,30,112
E_ 134
Zavv 36,34,112
W_ 0
E_ 135
Zavv 56,38,112
s_nop 0
Tv 30,v2,25600
Tv 32,v2,31744
Zavv 76,42,112
C_ 135,134,135
C_ 134,132,133
Zavv 72,42,108
s_nop 0
Tv 34,v3,25600
Tv 36,v3,31744
Zavv 52,38,108
E_ 76
Zavv 48,38,104
E_ 77
Zavv 68,42,104
E_ 78
Zavv 64,42,100
E_ 79
Zavv 44,38,100
C_ 116,76,77
C_ 117,78,79
Zavv 40,38,96
E_ 80
Zavv 60,42,96
W_ 0
E_ 81
Zavv 80,30,96
s_nop 0
Tv 38,v4,25600
Tv 40,v4,31744
Zavv 100,34,96
E_ 82
Zavv 104,34,100
s_nop 0
Tv 42,v5,25600
Tv 44,v5,31744
Zavv 84,30,100
E_ 83
Zavv 88,30,104
C_ 120,80,81
C_ 121,82,83
Zavv 108,34,104
E_ 84
Zavv 112,34,108
E_ 85
Zavv 92,30,108
E_ 86
Zavv 96,30,112
E_ 87
Zavv 116,34,112
W_ 0
C_ 124,84,85
C_ 125,86,87
Zavv 136,38,112
s_nop 0
Tv 30,v2,26624
Tv 32,v2,32768
Zavv 156,42,112
E_ 88
Zavv 152,42,108
s_nop 0
Tv 34,v3,26624
Tv 36,v3,32768
Zavv 132,38,108
E_ 89
Zavv 128,38,104
E_ 90
Zavv 148,42,104
E_ 91
Zavv 144,42,100
C_ 128,88,89
C_ 129,90,91
Zavv 124,38,100
E_ 92
Zavv 120,38,96
E_ 93
Zavv 140,42,96
W_ 0
E_ 94
Zavv 160,30,96
s_nop 0
Tv 38,v4,26624
Tv 40,v4,32768
Zavv 180,34,96
E_ 95
Zavv 184,34,100
s_nop 0
Tv 42,v5,26624
Tv 44,v5,32768
Zavv 164,30,100
C_ 132,92,93
C_ 133,94,95
Zavv 168,30,104
v_mov_b32 v92, 0x3f803f80
v_mov_b32 v93, 0x3f803f80
Zavv 188,34,104
S_ s65,s61,24576
v_mov_b32 v94, 0x3f803f80
v_mov_b32 v95, 0x3f803f80
Zavv 192,34,108
S_ m0,s51,24576
Zavv 172,30,108
s_waitcnt vmcnt(18) lgkmcnt(0)
s_barrier
Zavv 176,30,112
L_ v9,36,s65,0
Zavv 196,34,112
Zavv 216,38,112
Dv 30,%0,6144
Zavv 236,42,112
Dv 34,v1,6144
Zavv 232,42,108
L_ v9,36,s65,3072
S_ m0,s51,25472
Zavv 212,38,108
Zavv 208,38,104
L_ v9,36,s65,128
Zavv 228,42,104
Zavv 224,42,100
L_ v9,36,s65,3200
S_ m0,s51,26368
Zavv 204,38,100
Zavv 200,38,96
L_ v9,36,s65,256
Zavv 220,42,96
.Lstep0_2:
Zvvv 76,92,116
Dv 38,%0,7168
Zvvv 80,92,120
Dv 42,v1,7168
Zvvv 84,92,124
Zvvv 88,92,128
Zvvv 92,92,132
W_ 2
Nvvv 96,30,136,56
Mvvv 96,34,156
Nvvv 100,30,140,60
s_nop 1
v_max3_f32 v10, v76, v80, v84
s_nop 0
v_max3_f32 v10, v10, v88, v92
Mvvv 100,34,160
v_cmp_lt_f32 vcc, 0x42000000, v10
s_cbranch_vccnz .Ltramp0_1
Nvvv 104,30,144,64
A_ v46,v46,v76
A_ v47,v47,v80
Mvvv 104,34,164
A_ v48,v48,v84
A_ v49,v49,v88
Nvvv 108,30,148,68
A_ v50,v50,v92
Mvvv 108,34,168
L_ v9,36,s65,3328
Nvvv 112,30,152,72
R_ v11,a248
Mvvv 112,34,172
W_ 0
v_min_u32 v11, 240, v11
Mvvv 112,38,192
Dv 30,%0,8192
v_lshlrev_b32 v11, 3, v11
Mvvv 112,42,212
Dv 34,v1,8192
Mvvv 108,38,188
buffer_load_dword a246, v11, s[44:47], s66 offen
Mvvv 108,42,208
Mvvv 104,38,184
Mvvv 104,42,204
Mvvv 100,38,180
Mvvv 100,42,200
Mvvv 96,38,176
Mvvv 96,42,196
W_ 0
Mvvv 96,30,216
Dv 38,%0,0
Mvvv 96,34,236
Dv 42,v1,0
Mvvv 100,30,220
Mvvv 100,34,240
Mvvv 104,30,224
Mvvv 104,34,244
Mvvv 108,30,228
Mvvv 108,34,248
s_nop 0
E_ 96
Mvvv 112,30,232
E_ 97
Mvvv 112,34,252
W_ 0
E_ 98
Nvvv 76,38,136,56
Dv 30,%0,1024
Mvvv 76,42,156
Dv 34,v1,1024
Nvvv 80,38,140,60
E_ 99
Mvvv 80,42,160
C_ 99,98,99
C_ 98,96,97
Nvvv 84,38,144,64
E_ 100
Mvvv 84,42,164
E_ 101
Nvvv 88,38,148,68
E_ 102
Mvvv 88,42,168
E_ 103
Nvvv 92,38,152,72
C_ 103,102,103
C_ 102,100,101
Mvvv 92,42,172
W_ 0
E_ 104
Mvvv 92,30,192
Dv 38,%0,2048
Mvvv 92,34,212
Dv 42,v1,2048
Mvvv 88,30,188
E_ 105
Mvvv 88,34,208
E_ 106
Mvvv 84,30,184
S_ s65,s61,49152
E_ 107
Mvvv 84,34,204
S_ m0,s51,0
C_ 107,106,107
C_ 106,104,105
Mvvv 80,30,180
s_waitcnt vmcnt(13) lgkmcnt(0)
s_barrier
s_cmp_eq_u32 s11, 2
s_cbranch_scc1 .Lxs2m
.Lxs2m_back:
Mvvv 80,34,200
Ln v7,32,s65,0
Mvvv 76,30,176
E_ 108
Mvvv 76,34,196
Ln v8,32,s65,3072
S_ m0,s51,896
Mvvv 76,38,216
Tv 30,v2,12288
Tv 32,v2,18432
Mvvv 76,42,236
Ln v7,32,s65,128
Mvvv 80,38,220
s_nop 0
Tv 34,v3,12288
Tv 36,v3,18432
Mvvv 80,42,240
Ln v8,32,s65,3200
S_ m0,s51,1792
Mvvv 84,38,224
E_ 109
Mvvv 84,42,244
Ln v7,32,s65,256
Mvvv 88,38,228
E_ 110
Mvvv 88,42,248
Ln v8,32,s65,3328
Mvvv 92,38,232
E_ 111
Mvvv 92,42,252
W_ 0
C_ 111,110,111
C_ 110,108,109
Mavv 0,30,116
s_nop 0
Tv 38,v4,12288
Tv 40,v4,18432
Mavv 20,34,116
E_ 112
Mavv 24,34,120
s_nop 0
Tv 42,v5,12288
Tv 44,v5,18432
Mavv 4,30,120
E_ 113
Mavv 8,30,124
E_ 114
Mavv 28,34,124
E_ 115
Mavv 32,34,128
C_ 115,114,115
C_ 114,112,113
Mavv 12,30,128
E_ 76
Mavv 16,30,132
E_ 77
Mavv 36,34,132
W_ 0
E_ 78
Mavv 56,38,132
s_nop 0
Tv 30,v2,13312
Tv 32,v2,19456
Mavv 76,42,132
E_ 79
Mavv 72,42,128
s_nop 0
Tv 34,v3,13312
Tv 36,v3,19456
Mavv 52,38,128
C_ 96,76,77
C_ 97,78,79
Mavv 48,38,124
E_ 80
Mavv 68,42,124
E_ 81
Mavv 64,42,120
E_ 82
Mavv 44,38,120
E_ 83
Mavv 40,38,116
C_ 100,80,81
C_ 101,82,83
Mavv 60,42,116
W_ 0
E_ 84
Mavv 80,30,116
s_nop 0
Tv 38,v4,13312
Tv 40,v4,19456
Mavv 100,34,116
E_ 85
Mavv 104,34,120
s_nop 0
Tv 42,v5,13312
Tv 44,v5,19456
Mavv 84,30,120
E_ 86
Mavv 88,30,124
E_ 87
Mavv 108,34,124
C_ 104,84,85
C_ 105,86,87
Mavv 112,34,128
E_ 88
Mavv 92,30,128
E_ 89
Mavv 96,30,132
E_ 90
Mavv 116,34,132
W_ 0
E_ 91
Mavv 136,38,132
s_nop 0
Tv 30,v2,14336
Tv 32,v2,20480
Mavv 156,42,132
C_ 108,88,89
C_ 109,90,91
Mavv 152,42,128
s_nop 0
Tv 34,v3,14336
Tv 36,v3,20480
Mavv 132,38,128
E_ 92
Mavv 128,38,124
E_ 93
Mavv 148,42,124
E_ 94
Mavv 144,42,120
E_ 95
Mavv 124,38,120
C_ 112,92,93
C_ 113,94,95
Mavv 120,38,116
v_mov_b32 v92, 0x3f803f80
v_mov_b32 v93, 0x3f803f80
Mavv 140,42,116
W_ 0
v_mov_b32 v94, 0x3f803f80
v_mov_b32 v95, 0x3f803f80
Mavv 160,30,116
s_nop 0
Tv 38,v4,14336
Tv 40,v4,20480
Mavv 180,34,116
Mavv 184,34,120
s_nop 0
Tv 42,v5,14336
Tv 44,v5,20480
Mavv 164,30,120
Mavv 168,30,124
Mavv 188,34,124
S_ s65,s61,36864
Mavv 192,34,128
S_ m0,s51,12288
Mavv 172,30,128
s_waitcnt vmcnt(13) lgkmcnt(0)
s_barrier
s_cmp_eq_u32 s11, 2
s_cbranch_scc1 .Lxs2e
.Lxs2e_back:
Mavv 176,30,132
L_ v9,36,s65,0
Mavv 196,34,132
Mavv 216,38,132
Dv 30,%0,55296
Mavv 236,42,132
Dv 34,v1,55296
Mavv 232,42,128
L_ v9,36,s65,3072
S_ m0,s51,13184
Mavv 212,38,128
Mavv 208,38,124
L_ v9,36,s65,128
Mavv 228,42,124
Mavv 224,42,120
L_ v9,36,s65,3200
S_ m0,s51,14080
Mavv 204,38,120
Mavv 200,38,116
L_ v9,36,s65,256
Mavv 220,42,116
s_bitcmp1_b32 s67, 0
s_cbranch_scc1 .Lhsd0_0
.Lhsd0_0_back:
.Lstep0_3:
Zvvv 76,92,96
Dv 38,%0,56320
Zvvv 80,92,100
Dv 42,v1,56320
Zvvv 84,92,104
Zvvv 88,92,108
Zvvv 92,92,112
W_ 2
Nvvv 116,30,136,56
Mvvv 116,34,156
Nvvv 120,30,140,60
s_nop 1
v_max3_f32 v10, v76, v80, v84
s_nop 0
v_max3_f32 v10, v10, v88, v92
Mvvv 120,34,160
v_cmp_lt_f32 vcc, 0x42000000, v10
s_cbranch_vccnz .Ltramp0_2
Nvvv 124,30,144,64
A_ v46,v46,v76
A_ v47,v47,v80
Mvvv 124,34,164
A_ v48,v48,v84
A_ v49,v49,v88
Nvvv 128,30,148,68
A_ v50,v50,v92
Mvvv 128,34,168
L_ v9,36,s65,3328
Nvvv 132,30,152,72
Mvvv 132,34,172
W_ 0
Mvvv 132,38,192
Dv 30,%0,57344
Mvvv 132,42,212
Dv 34,v1,57344
Mvvv 128,38,188
Mvvv 128,42,208
Mvvv 124,38,184
Mvvv 124,42,204
Mvvv 120,38,180
Mvvv 120,42,200
Mvvv 116,38,176
Mvvv 116,42,196
W_ 0
Mvvv 116,30,216
Dv 38,%0,49152
Mvvv 116,34,236
Dv 42,v1,49152
Mvvv 120,30,220
Mvvv 120,34,240
Mvvv 124,30,224
Mvvv 124,34,244
Mvvv 128,30,228
Mvvv 128,34,248
s_nop 0
E_ 116
Mvvv 132,30,232
E_ 117
Mvvv 132,34,252
W_ 0
E_ 118
Nvvv 76,38,136,56
Dv 30,%0,50176
Mvvv 76,42,156
Dv 34,v1,50176
Nvvv 80,38,140,60
E_ 119
Mvvv 80,42,160
C_ 119,118,119
C_ 118,116,117
Nvvv 84,38,144,64
E_ 120
Mvvv 84,42,164
E_ 121
Nvvv 88,38,148,68
E_ 122
Mvvv 88,42,168
E_ 123
Nvvv 92,38,152,72
C_ 123,122,123
C_ 122,120,121
Mvvv 92,42,172
W_ 0
E_ 124
Mvvv 92,30,192
Dv 38,%0,51200
Mvvv 92,34,212
Dv 42,v1,51200
Mvvv 88,30,188
E_ 125
Mvvv 88,34,208
E_ 126
Mvvv 84,30,184
S_ s65,s61,61440
E_ 127
Mvvv 84,34,204
S_ m0,s51,49152
C_ 127,126,127
C_ 126,124,125
Mvvv 80,30,180
s_waitcnt vmcnt(13) lgkmcnt(0)
s_barrier
s_cmp_eq_u32 s11, 2
s_cbranch_scc1 .Lxs3m
.Lxs3m_back:
Mvvv 80,34,200
Ln v7,32,s65,0
Mvvv 76,30,176
E_ 128
Mvvv 76,34,196
Ln v8,32,s65,3072
S_ m0,s51,50048
Mvvv 76,38,216
Tv 30,v2,24576
Tv 32,v2,30720
Mvvv 76,42,236
Ln v7,32,s65,128
Mvvv 80,38,220
s_nop 0
Tv 34,v3,24576
Tv 36,v3,30720
Mvvv 80,42,240
Ln v8,32,s65,3200
S_ m0,s51,50944
Mvvv 84,38,224
E_ 129
Mvvv 84,42,244
Ln v7,32,s65,256
Mvvv 88,38,228
E_ 130
Mvvv 88,42,248
Ln v8,32,s65,3328
Mvvv 92,38,232
E_ 131
Mvvv 92,42,252
W_ 0
C_ 131,130,131
C_ 130,128,129
Mavv 0,30,96
s_nop 0
Tv 38,v4,24576
Tv 40,v4,30720
Mavv 20,34,96
E_ 132
Mavv 24,34,100
s_nop 0
Tv 42,v5,24576
Tv 44,v5,30720
Mavv 4,30,100
E_ 133
Mavv 8,30,104
E_ 134
Mavv 28,34,104
E_ 135
Mavv 32,34,108
C_ 135,134,135
C_ 134,132,133
Mavv 12,30,108
E_ 76
Mavv 16,30,112
E_ 77
Mavv 36,34,112
W_ 0
E_ 78
Mavv 56,38,112
s_nop 0
Tv 30,v2,25600
Tv 32,v2,31744
Mavv 76,42,112
E_ 79
Mavv 72,42,108
s_nop 0
Tv 34,v3,25600
Tv 36,v3,31744
Mavv 52,38,108
C_ 116,76,77
C_ 117,78,79
Mavv 48,38,104
E_ 80
Mavv 68,42,104
E_ 81
Mavv 64,42,100
E_ 82
Mavv 44,38,100
E_ 83
Mavv 40,38,96
C_ 120,80,81
C_ 121,82,83
Mavv 60,42,96
W_ 0
E_ 84
Mavv 80,30,96
s_nop 0
Tv 38,v4,25600
Tv 40,v4,31744
Mavv 100,34,96
E_ 85
Mavv 104,34,100
s_nop 0
Tv 42,v5,25600
Tv 44,v5,31744
Mavv 84,30,100
E_ 86
Mavv 88,30,104
E_ 87
Mavv 108,34,104
C_ 124,84,85
C_ 125,86,87
Mavv 112,34,108
E_ 88
Mavv 92,30,108
E_ 89
Mavv 96,30,112
E_ 90
Mavv 116,34,112
W_ 0
E_ 91
Mavv 136,38,112
s_nop 0
Tv 30,v2,26624
Tv 32,v2,32768
Mavv 156,42,112
C_ 128,88,89
C_ 129,90,91
Mavv 152,42,108
s_nop 0
Tv 34,v3,26624
Tv 36,v3,32768
Mavv 132,38,108
E_ 92
Mavv 128,38,104
E_ 93
Mavv 148,42,104
E_ 94
Mavv 144,42,100
E_ 95
Mavv 124,38,100
C_ 132,92,93
C_ 133,94,95
Mavv 120,38,96
v_mov_b32 v92, 0x3f803f80
v_mov_b32 v93, 0x3f803f80
Mavv 140,42,96
W_ 0
v_mov_b32 v94, 0x3f803f80
v_mov_b32 v95, 0x3f803f80
Mavv 160,30,96
s_nop 0
Tv 38,v4,26624
Tv 40,v4,32768
Mavv 180,34,96
Mavv 184,34,100
s_nop 0
Tv 42,v5,26624
Tv 44,v5,32768
Mavv 164,30,100
Mavv 168,30,104
Mavv 188,34,104
S_ s65,s61,49152
Mavv 192,34,108
S_ m0,s51,24576
Mavv 172,30,108
s_waitcnt vmcnt(12) lgkmcnt(0)
s_barrier
s_cmp_eq_u32 s11, 2
s_cbranch_scc1 .Lxs3e
.Lxs3e_back:
Mavv 176,30,112
L_ v9,36,s65,0
Mavv 196,34,112
S_ s61,s61,24576
Mavv 216,38,112
s_lshr_b32 s67, s67, 1
Dv 30,%0,6144
Mavv 236,42,112
s_bitcmp1_b32 s67, 9
s_cselect_b32 s11, s13, s11
Dv 34,v1,6144
Mavv 232,42,108
L_ v9,36,s65,3072
S_ m0,s51,25472
Mavv 212,38,108
Mavv 208,38,104
L_ v9,36,s65,128
Mavv 228,42,104
Mavv 224,42,100
L_ v9,36,s65,3200
S_ m0,s51,26368
Mavv 204,38,100
Mavv 200,38,96
L_ v9,36,s65,256
Mavv 220,42,96
s_cmp_ge_u32 s67, 256
s_cbranch_scc1 .Lstep0_2
s_sub_u32 s61, s61, 147456
.Lstep0_14:
Zvvv 76,92,116
Dv 38,%0,7168
Zvvv 80,92,120
Dv 42,v1,7168
Zvvv 84,92,124
Zvvv 88,92,128
Zvvv 92,92,132
W_ 2
Nvvv 96,30,136,56
Mvvv 96,34,156
Nvvv 100,30,140,60
s_nop 1
v_max3_f32 v10, v76, v80, v84
s_nop 0
v_max3_f32 v10, v10, v88, v92
Mvvv 100,34,160
v_cmp_lt_f32 vcc, 0x42000000, v10
s_cbranch_vccnz .Ltramp0_13
Nvvv 104,30,144,64
A_ v46,v46,v76
A_ v47,v47,v80
Mvvv 104,34,164
A_ v48,v48,v84
A_ v49,v49,v88
Nvvv 108,30,148,68
A_ v50,v50,v92
Mvvv 108,34,168
L_ v9,36,s65,3328
Nvvv 112,30,152,72
R_ v20,a240
Mvvv 112,34,172
W_ 0
v_xor_b32 v21, 64, v20
Mvvv 112,38,192
S_ s65,s60,0
Dv 30,%0,8192
Mvvv 112,42,212
S_ m0,s50,0
Dv 34,v1,8192
Mvvv 108,38,188
L_ v20,24,s65,0
Mvvv 108,42,208
S_ s65,s60,10240
Mvvv 104,38,184
S_ m0,s50,1024
Mvvv 104,42,204
L_ v21,24,s65,0
Mvvv 100,38,180
S_ s65,s60,20480
Mvvv 100,42,200
S_ m0,s50,2048
Mvvv 96,38,176
L_ v20,24,s65,0
Mvvv 96,42,196
S_ s65,s60,30720
W_ 0
Mvvv 96,30,216
S_ m0,s50,3072
Dv 38,%0,0
Mvvv 96,34,236
Dv 42,v1,0
Mvvv 100,30,220
L_ v21,24,s65,0
Mvvv 100,34,240
S_ s65,s60,40960
Mvvv 104,30,224
S_ m0,s50,4096
Mvvv 104,34,244
L_ v20,24,s65,0
Mvvv 108,30,228
Mvvv 108,34,248
E_ 96
Mvvv 112,30,232
E_ 97
Mvvv 112,34,252
W_ 0
E_ 98
Nvvv 76,38,136,56
Dv 30,%0,1024
Mvvv 76,42,156
Dv 34,v1,1024
Nvvv 80,38,140,60
E_ 99
Mvvv 80,42,160
C_ 99,98,99
C_ 98,96,97
Nvvv 84,38,144,64
E_ 100
Mvvv 84,42,164
E_ 101
Nvvv 88,38,148,68
E_ 102
Mvvv 88,42,168
E_ 103
Nvvv 92,38,152,72
C_ 103,102,103
C_ 102,100,101
Mvvv 92,42,172
W_ 0
E_ 104
Mvvv 92,30,192
Dv 38,%0,2048
Mvvv 92,34,212
Dv 42,v1,2048
Mvvv 88,30,188
E_ 105
Mvvv 88,34,208
E_ 106
Mvvv 84,30,184
E_ 107
Mvvv 84,34,204
C_ 107,106,107
C_ 106,104,105
Mvvv 80,30,180
s_waitcnt vmcnt(17) lgkmcnt(0)
s_barrier
s_cmp_eq_u32 s11, 2
s_cbranch_scc1 .Lxs14m
.Lxs14m_back:
Mvvv 80,34,200
E_ 108
Mvvv 76,30,176
E_ 109
Mvvv 76,34,196
E_ 110
Mvvv 76,38,216
s_nop 0
Tv 30,v2,12288
Tv 32,v2,18432
Mvvv 76,42,236
E_ 111
Mvvv 80,38,220
s_nop 0
Tv 34,v3,12288
Tv 36,v3,18432
Mvvv 80,42,240
C_ 111,110,111
C_ 110,108,109
Mvvv 84,38,224
E_ 112
Mvvv 84,42,244
E_ 113
Mvvv 88,38,228
E_ 114
Mvvv 88,42,248
E_ 115
Mvvv 92,38,232
C_ 115,114,115
C_ 114,112,113
Mvvv 92,42,252
W_ 0
E_ 76
Mavv 0,30,116
s_nop 0
Tv 38,v4,12288
Tv 40,v4,18432
Mavv 20,34,116
E_ 77
Mavv 24,34,120
s_nop 0
Tv 42,v5,12288
Tv 44,v5,18432
Mavv 4,30,120
E_ 78
Mavv 8,30,124
E_ 79
Mavv 28,34,124
C_ 96,76,77
C_ 97,78,79
Mavv 32,34,128
E_ 80
Mavv 12,30,128
E_ 81
Mavv 16,30,132
E_ 82
Mavv 36,34,132
W_ 0
E_ 83
Mavv 56,38,132
s_nop 0
Tv 30,v2,13312
Tv 32,v2,19456
Mavv 76,42,132
C_ 100,80,81
C_ 101,82,83
Mavv 72,42,128
s_nop 0
Tv 34,v3,13312
Tv 36,v3,19456
Mavv 52,38,128
E_ 84
Mavv 48,38,124
E_ 85
Mavv 68,42,124
E_ 86
Mavv 64,42,120
E_ 87
Mavv 44,38,120
C_ 104,84,85
C_ 105,86,87
Mavv 40,38,116
E_ 88
Mavv 60,42,116
W_ 0
E_ 89
Mavv 80,30,116
s_nop 0
Tv 38,v4,13312
Tv 40,v4,19456
Mavv 100,34,116
E_ 90
Mavv 104,34,120
s_nop 0
Tv 42,v5,13312
Tv 44,v5,19456
Mavv 84,30,120
E_ 91
Mavv 88,30,124
C_ 108,88,89
C_ 109,90,91
Mavv 108,34,124
E_ 92
Mavv 112,34,128
E_ 93
Mavv 92,30,128
E_ 94
Mavv 96,30,132
E_ 95
Mavv 116,34,132
W_ 0
C_ 112,92,93
C_ 113,94,95
Mavv 136,38,132
s_nop 0
Tv 30,v2,14336
Tv 32,v2,20480
Mavv 156,42,132
v_mov_b32 v92, 0x3f803f80
v_mov_b32 v93, 0x3f803f80
Mavv 152,42,128
s_nop 0
Tv 34,v3,14336
Tv 36,v3,20480
Mavv 132,38,128
v_mov_b32 v94, 0x3f803f80
v_mov_b32 v95, 0x3f803f80
Mavv 128,38,124
Mavv 148,42,124
Mavv 144,42,120
Mavv 124,38,120
Mavv 120,38,116
Mavv 140,42,116
W_ 0
Mavv 160,30,116
s_nop 0
Tv 38,v4,14336
Tv 40,v4,20480
Mavv 180,34,116
Mavv 184,34,120
s_nop 0
Tv 42,v5,14336
Tv 44,v5,20480
Mavv 164,30,120
Mavv 168,30,124
Mavv 188,34,124
Mavv 192,34,128
Mavv 172,30,128
s_waitcnt vmcnt(11) lgkmcnt(0)
s_barrier
s_cmp_eq_u32 s11, 2
s_cbranch_scc1 .Lxs14e
.Lxs14e_back:
Mavv 176,30,132
S_ s65,s59,0
Mavv 196,34,132
S_ m0,s52,8192
Mavv 216,38,132
Dv 30,%0,55296
Mavv 236,42,132
Dv 34,v1,55296
Mavv 232,42,128
L_ v6,28,s65,0
Mavv 212,38,128
S_ s65,s59,98304
Mavv 208,38,124
S_ m0,s52,9216
Mavv 228,42,124
L_ v6,28,s65,0
Mavv 224,42,120
S_ s65,s59,196608
Mavv 204,38,120
S_ m0,s52,10240
Mavv 200,38,116
L_ v6,28,s65,0
Mavv 220,42,116
S_ s65,s59,294912
.Lstep0_15:
Zvvv 76,92,96
Dv 38,%0,56320
Zvvv 80,92,100
Dv 42,v1,56320
Zvvv 84,92,104
Zvvv 88,92,108
Zvvv 92,92,112
W_ 2
Nvvv 116,30,136,56
Mvvv 116,34,156
Nvvv 120,30,140,60
s_nop 1
v_max3_f32 v10, v76, v80, v84
s_nop 0
v_max3_f32 v10, v10, v88, v92
Mvvv 120,34,160
v_cmp_lt_f32 vcc, 0x42000000, v10
s_cbranch_vccnz .Ltramp0_14
Nvvv 124,30,144,64
S_ m0,s52,11264
A_ v46,v46,v76
A_ v47,v47,v80
Mvvv 124,34,164
A_ v48,v48,v84
A_ v49,v49,v88
Nvvv 128,30,148,68
A_ v50,v50,v92
Mvvv 128,34,168
L_ v6,28,s65,0
Nvvv 132,30,152,72
S_ s65,s59,0
Mvvv 132,34,172
S_ m0,s52,16256
W_ 0
Mvvv 132,38,192
Dv 30,%0,57344
Mvvv 132,42,212
Dv 34,v1,57344
Mvvv 128,38,188
L_ v6,28,s65,128
Mvvv 128,42,208
S_ s65,s59,98304
Mvvv 124,38,184
S_ m0,s52,17280
Mvvv 124,42,204
L_ v6,28,s65,128
Mvvv 120,38,180
S_ s65,s59,196608
Mvvv 120,42,200
S_ m0,s52,18304
Mvvv 116,38,176
L_ v6,28,s65,128
Mvvv 116,42,196
S_ s65,s59,294912
W_ 0
Mvvv 116,30,216
S_ m0,s52,19328
Dv 38,%0,49152
Mvvv 116,34,236
Dv 42,v1,49152
Mvvv 120,30,220
L_ v6,28,s65,128
Mvvv 120,34,240
S_ s65,s59,786432
Mvvv 124,30,224
S_ m0,s52,0
Mvvv 124,34,244
L_ v6,28,s65,0
Mvvv 128,30,228
S_ s65,s59,884736
Mvvv 128,34,248
S_ m0,s52,1024
E_ 116
Mvvv 132,30,232
L_ v6,28,s65,0
Mvvv 132,34,252
S_ s65,s59,983040
E_ 117
W_ 1
Nvvv 76,38,136,56
S_ m0,s52,2048
Dv 30,%0,50176
W_ 1
Mvvv 76,42,156
Dv 34,v1,50176
Nvvv 80,38,140,60
L_ v6,28,s65,0
Mvvv 80,42,160
S_ s65,s59,1081344
E_ 118
Nvvv 84,38,144,64
S_ m0,s52,3072
E_ 119
Mvvv 84,42,164
L_ v6,28,s65,0
Nvvv 88,38,148,68
)A"
R"A(S_ s65,s60,128
C_ 119,118,119
C_ 118,116,117
Mvvv 88,42,168
S_ m0,s50,20480
E_ 120
Nvvv 92,38,152,72
L_ v20,24,s65,0
Mvvv 92,42,172
S_ s65,s60,10368
E_ 121
W_ 1
Mvvv 92,30,192
S_ m0,s50,21504
Dv 38,%0,51200
W_ 1
Mvvv 92,34,212
Dv 42,v1,51200
Mvvv 88,30,188
L_ v21,24,s65,0
Mvvv 88,34,208
S_ s65,s60,20608
E_ 122
Mvvv 84,30,184
S_ m0,s50,22528
E_ 123
Mvvv 84,34,204
L_ v20,24,s65,0
Mvvv 80,30,180
s_waitcnt vmcnt(20) lgkmcnt(0)
s_barrier
s_cmp_eq_u32 s11, 2
s_cbranch_scc1 .Lxs15m
.Lxs15m_back:
S_ s65,s60,30848
Mvvv 80,34,200
S_ m0,s50,23552
C_ 123,122,123
C_ 122,120,121
Mvvv 76,30,176
L_ v21,24,s65,0
Mvvv 76,34,196
S_ s65,s60,41088
E_ 124
Mvvv 76,38,216
S_ m0,s50,24576
Tv 30,v2,24576
Tv 32,v2,30720
Mvvv 76,42,236
L_ v20,24,s65,0
Mvvv 80,38,220
Tv 34,v3,24576
Tv 36,v3,30720
Mvvv 80,42,240
E_ 125
Mvvv 84,38,224
E_ 126
Mvvv 84,42,244
E_ 127
Mvvv 88,38,228
C_ 127,126,127
C_ 126,124,125
Mvvv 88,42,248
E_ 128
Mvvv 92,38,232
E_ 129
Mvvv 92,42,252
W_ 0
E_ 130
Mavv 0,30,96
s_nop 0
Tv 38,v4,24576
Tv 40,v4,30720
Mavv 20,34,96
E_ 131
Mavv 24,34,100
s_nop 0
Tv 42,v5,24576
Tv 44,v5,30720
Mavv 4,30,100
C_ 131,130,131
C_ 130,128,129
Mavv 8,30,104
E_ 132
Mavv 28,34,104
E_ 133
Mavv 32,34,108
E_ 134
Mavv 12,30,108
E_ 135
Mavv 16,30,112
C_ 135,134,135
C_ 134,132,133
Mavv 36,34,112
W_ 0
E_ 76
Mavv 56,38,112
s_nop 0
Tv 30,v2,25600
Tv 32,v2,31744
Mavv 76,42,112
E_ 77
Mavv 72,42,108
s_nop 0
Tv 34,v3,25600
Tv 36,v3,31744
Mavv 52,38,108
E_ 78
Mavv 48,38,104
E_ 79
Mavv 68,42,104
C_ 116,76,77
C_ 117,78,79
Mavv 64,42,100
E_ 80
Mavv 44,38,100
E_ 81
Mavv 40,38,96
E_ 82
Mavv 60,42,96
W_ 0
E_ 83
Mavv 80,30,96
s_nop 0
Tv 38,v4,25600
Tv 40,v4,31744
Mavv 100,34,96
C_ 120,80,81
C_ 121,82,83
Mavv 104,34,100
s_nop 0
Tv 42,v5,25600
Tv 44,v5,31744
Mavv 84,30,100
E_ 84
Mavv 88,30,104
E_ 85
Mavv 108,34,104
E_ 86
Mavv 112,34,108
E_ 87
Mavv 92,30,108
C_ 124,84,85
C_ 125,86,87
Mavv 96,30,112
E_ 88
Mavv 116,34,112
W_ 0
E_ 89
Mavv 136,38,112
s_nop 0
Tv 30,v2,26624
Tv 32,v2,32768
Mavv 156,42,112
E_ 90
Mavv 152,42,108
s_nop 0
Tv 34,v3,26624
Tv 36,v3,32768
Mavv 132,38,108
E_ 91
Mavv 128,38,104
C_ 128,88,89
C_ 129,90,91
Mavv 148,42,104
E_ 92
Mavv 144,42,100
E_ 93
Mavv 124,38,100
E_ 94
Mavv 120,38,96
E_ 95
Mavv 140,42,96
W_ 0
C_ 132,92,93
C_ 133,94,95
Mavv 160,30,96
s_nop 0
Tv 38,v4,26624
Tv 40,v4,32768
Mavv 180,34,96
v_mov_b32 v92, 0x3f803f80
v_mov_b32 v93, 0x3f803f80
Mavv 184,34,100
s_nop 0
Tv 42,v5,26624
Tv 44,v5,32768
Mavv 164,30,100
v_mov_b32 v94, 0x3f803f80
v_mov_b32 v95, 0x3f803f80
Mavv 168,30,104
Mavv 188,34,104
Mavv 192,34,108
Mavv 172,30,108
s_waitcnt vmcnt(63) lgkmcnt(0)
s_barrier
s_cmp_eq_u32 s11, 2
s_cbranch_scc1 .Lxs15e
.Lxs15e_back:
Mavv 176,30,112
S_ s65,s59,0
Mavv 196,34,112
S_ m0,s52,24320
Mavv 216,38,112
s_nop 0
Tv 30,v2,36864
Tv 32,v2,43008
Zvvv 76,92,116
L_ v6,28,s65,256
Zvvv 80,92,120
S_ s65,s59,98304
Tv 34,v3,36864
Tv 36,v3,43008
Zvvv 84,92,124
S_ m0,s52,25344
Zvvv 88,92,128
L_ v6,28,s65,256
Zvvv 92,92,132
S_ s65,s59,196608
Mavv 236,42,112
S_ m0,s52,26368
Mavv 232,42,108
L_ v6,28,s65,256
Mavv 212,38,108
S_ s65,s59,294912
Mavv 208,38,104
S_ m0,s52,27392
v_max3_f32 v10, v76, v80, v84
Mavv 228,42,104
v_max3_f32 v10, v10, v88, v92
v_cmp_lt_f32 vcc, 0x42000000, v10
Mavv 224,42,100
L_ v6,28,s65,256
Mavv 204,38,100
Mavv 200,38,96
Mavv 220,42,96
s_cbranch_vccnz .Ltramp0_15
.Ljoin0:
W_ 2
Mavv 0,30,116
s_mov_b32 s58, 1
Tv 38,v4,36864
Tv 40,v4,43008
W_ 2
Mavv 20,34,116
S_ s61,s61,393216
R_ v12,a248
s_mov_b32 s85, 4026531840
Mavv 24,34,120
s_nop 0
Tv 42,v5,36864
Tv 44,v5,43008
Mavv 4,30,120
A_ v46,v46,v76
A_ v47,v47,v80
Mavv 8,30,124
A_ v48,v48,v84
A_ v49,v49,v88
Mavv 28,34,124
A_ v50,v50,v92
Mavv 32,34,128
v_rcp_f32 v46, v46
Mavv 12,30,128
v_rcp_f32 v47, v47
Mavv 16,30,132
v_rcp_f32 v48, v48
Mavv 36,34,132
W_ 0
v_rcp_f32 v49, v49
Mavv 56,38,132
s_nop 0
Tv 30,v2,37888
Tv 32,v2,44032
Mavv 76,42,132
v_rcp_f32 v50, v50
Mavv 72,42,128
s_nop 0
Tv 34,v3,37888
Tv 36,v3,44032
Mavv 52,38,128
R_ v96,a0
R_ v97,a1
Mavv 48,38,124
R_ v98,a2
R_ v99,a3
Mavv 68,42,124
R_ v100,a20
R_ v101,a21
Mavv 64,42,120
R_ v102,a22
R_ v103,a23
Mavv 44,38,120
U_ v96,v96,v46
U_ v97,v97,v46
Mavv 40,38,116
U_ v98,v98,v46
U_ v99,v99,v46
Mavv 60,42,116
W_ 0
U_ v100,v100,v46
U_ v101,v101,v46
Mavv 80,30,116
s_nop 0
Tv 38,v4,37888
Tv 40,v4,44032
Mavv 100,34,116
U_ v102,v102,v46
U_ v103,v103,v46
Mavv 104,34,120
s_nop 0
Tv 42,v5,37888
Tv 44,v5,44032
Mavv 84,30,120
C_ 76,96,97
C_ 77,98,99
Mavv 88,30,124
C_ 78,100,101
C_ 79,102,103
Mavv 108,34,124
X_ a0,v76
X_ a1,v77
Mavv 112,34,128
X_ a2,v78
X_ a3,v79
Mavv 92,30,128
R_ v104,a4
R_ v105,a5
Mavv 96,30,132
R_ v106,a6
R_ v107,a7
Mavv 116,34,132
W_ 0
R_ v108,a24
R_ v109,a25
Mavv 136,38,132
s_nop 0
Tv 30,v2,38912
Tv 32,v2,45056
Mavv 156,42,132
R_ v110,a26
R_ v111,a27
Mavv 152,42,128
s_nop 0
Tv 34,v3,38912
Tv 36,v3,45056
Mavv 132,38,128
U_ v104,v104,v47
U_ v105,v105,v47
Mavv 128,38,124
U_ v106,v106,v47
U_ v107,v107,v47
Mavv 148,42,124
U_ v108,v108,v47
U_ v109,v109,v47
Mavv 144,42,120
U_ v110,v110,v47
U_ v111,v111,v47
Mavv 124,38,120
C_ 80,104,105
C_ 81,106,107
Mavv 120,38,116
C_ 82,108,109
C_ 83,110,111
Mavv 140,42,116
W_ 0
X_ a4,v80
X_ a5,v81
Mavv 160,30,116
s_nop 0
Tv 38,v4,38912
Tv 40,v4,45056
Mavv 180,34,116
X_ a6,v82
X_ a7,v83
Mavv 184,34,120
s_nop 0
Tv 42,v5,38912
Tv 44,v5,45056
Mavv 164,30,120
R_ v96,a8
R_ v97,a9
Mavv 168,30,124
R_ v98,a10
R_ v99,a11
Mavv 188,34,124
R_ v100,a28
R_ v101,a29
Mavv 192,34,128
R_ v102,a30
R_ v103,a31
Mavv 172,30,128
U_ v96,v96,v48
U_ v97,v97,v48
Mavv 176,30,132
U_ v98,v98,v48
U_ v99,v99,v48
Mavv 196,34,132
W_ 0
U_ v100,v100,v48
U_ v101,v101,v48
Mavv 216,38,132
U_ v102,v102,v48
U_ v103,v103,v48
Mavv 236,42,132
C_ 84,96,97
C_ 85,98,99
Mavv 232,42,128
C_ 86,100,101
C_ 87,102,103
Mavv 212,38,128
X_ a8,v84
X_ a9,v85
Mavv 208,38,124
X_ a10,v86
X_ a11,v87
Mavv 228,42,124
Mavv 224,42,120
Mavv 204,38,120
Mavv 200,38,116
Mavv 220,42,116
.p2align 3
R_ v104,a12
R_ v105,a13
R_ v106,a14
R_ v107,a15
R_ v108,a32
R_ v109,a33
R_ v110,a34
R_ v111,a35
v_pk_mul_f32 v[104:105], v[104:105], v[48:49] op_sel:[0,1]
v_pk_mul_f32 v[106:107], v[106:107], v[48:49] op_sel:[0,1]
v_pk_mul_f32 v[108:109], v[108:109], v[48:49] op_sel:[0,1]
v_pk_mul_f32 v[110:111], v[110:111], v[48:49] op_sel:[0,1]
C_ 88,104,105
C_ 89,106,107
C_ 90,108,109
C_ 91,110,111
X_ a12,v88
X_ a13,v89
X_ a14,v90
X_ a15,v91
.p2align 3
R_ v96,a16
R_ v97,a17
R_ v98,a18
R_ v99,a19
R_ v100,a36
R_ v101,a37
R_ v102,a38
R_ v103,a39
v_pk_mul_f32 v[96:97], v[96:97], v[50:51] op_sel_hi:[1,0]
v_pk_mul_f32 v[98:99], v[98:99], v[50:51] op_sel_hi:[1,0]
v_pk_mul_f32 v[100:101], v[100:101], v[50:51] op_sel_hi:[1,0]
v_pk_mul_f32 v[102:103], v[102:103], v[50:51] op_sel_hi:[1,0]
C_ 92,96,97
C_ 93,98,99
C_ 94,100,101
C_ 95,102,103
X_ a16,v92
X_ a17,v93
X_ a18,v94
X_ a19,v95
.p2align 3
R_ v104,a40
R_ v105,a41
R_ v106,a42
R_ v107,a43
R_ v108,a60
R_ v109,a61
R_ v110,a62
R_ v111,a63
v_pk_mul_f32 v[104:105], v[104:105], v[46:47] op_sel_hi:[1,0]
v_pk_mul_f32 v[106:107], v[106:107], v[46:47] op_sel_hi:[1,0]
v_pk_mul_f32 v[108:109], v[108:109], v[46:47] op_sel_hi:[1,0]
v_pk_mul_f32 v[110:111], v[110:111], v[46:47] op_sel_hi:[1,0]
C_ 76,104,105
C_ 77,106,107
C_ 78,108,109
C_ 79,110,111
X_ a40,v76
X_ a41,v77
X_ a42,v78
X_ a43,v79
.p2align 3
R_ v96,a44
R_ v97,a45
R_ v98,a46
R_ v99,a47
R_ v100,a64
R_ v101,a65
R_ v102,a66
R_ v103,a67
v_pk_mul_f32 v[96:97], v[96:97], v[46:47] op_sel:[0,1]
v_pk_mul_f32 v[98:99], v[98:99], v[46:47] op_sel:[0,1]
v_pk_mul_f32 v[100:101], v[100:101], v[46:47] op_sel:[0,1]
v_pk_mul_f32 v[102:103], v[102:103], v[46:47] op_sel:[0,1]
C_ 80,96,97
C_ 81,98,99
C_ 82,100,101
C_ 83,102,103
X_ a44,v80
X_ a45,v81
X_ a46,v82
X_ a47,v83
.p2align 3
R_ v104,a48
R_ v105,a49
R_ v106,a50
R_ v107,a51
R_ v108,a68
R_ v109,a69
R_ v110,a70
R_ v111,a71
v_pk_mul_f32 v[104:105], v[104:105], v[48:49] op_sel_hi:[1,0]
v_pk_mul_f32 v[106:107], v[106:107], v[48:49] op_sel_hi:[1,0]
v_pk_mul_f32 v[108:109], v[108:109], v[48:49] op_sel_hi:[1,0]
v_pk_mul_f32 v[110:111], v[110:111], v[48:49] op_sel_hi:[1,0]
C_ 84,104,105
C_ 85,106,107
C_ 86,108,109
C_ 87,110,111
X_ a48,v84
X_ a49,v85
X_ a50,v86
X_ a51,v87
.p2align 3
R_ v96,a52
R_ v97,a53
R_ v98,a54
R_ v99,a55
R_ v100,a72
R_ v101,a73
R_ v102,a74
R_ v103,a75
v_pk_mul_f32 v[96:97], v[96:97], v[48:49] op_sel:[0,1]
v_pk_mul_f32 v[98:99], v[98:99], v[48:49] op_sel:[0,1]
v_pk_mul_f32 v[100:101], v[100:101], v[48:49] op_sel:[0,1]
v_pk_mul_f32 v[102:103], v[102:103], v[48:49] op_sel:[0,1]
C_ 88,96,97
C_ 89,98,99
C_ 90,100,101
C_ 91,102,103
X_ a52,v88
X_ a53,v89
X_ a54,v90
X_ a55,v91
.p2align 3
R_ v104,a56
R_ v105,a57
R_ v106,a58
R_ v107,a59
R_ v108,a76
R_ v109,a77
R_ v110,a78
R_ v111,a79
v_pk_mul_f32 v[104:105], v[104:105], v[50:51] op_sel_hi:[1,0]
v_pk_mul_f32 v[106:107], v[106:107], v[50:51] op_sel_hi:[1,0]
v_pk_mul_f32 v[108:109], v[108:109], v[50:51] op_sel_hi:[1,0]
v_pk_mul_f32 v[110:111], v[110:111], v[50:51] op_sel_hi:[1,0]
C_ 92,104,105
C_ 93,106,107
C_ 94,108,109
C_ 95,110,111
X_ a56,v92
X_ a57,v93
X_ a58,v94
X_ a59,v95
.p2align 3
R_ v96,a80
R_ v97,a81
R_ v98,a82
R_ v99,a83
R_ v100,a100
R_ v101,a101
R_ v102,a102
R_ v103,a103
v_pk_mul_f32 v[96:97], v[96:97], v[46:47] op_sel_hi:[1,0]
v_pk_mul_f32 v[98:99], v[98:99], v[46:47] op_sel_hi:[1,0]
v_pk_mul_f32 v[100:101], v[100:101], v[46:47] op_sel_hi:[1,0]
v_pk_mul_f32 v[102:103], v[102:103], v[46:47] op_sel_hi:[1,0]
C_ 76,96,97
C_ 77,98,99
C_ 78,100,101
C_ 79,102,103
X_ a80,v76
X_ a81,v77
X_ a82,v78
X_ a83,v79
.p2align 3
R_ v104,a84
R_ v105,a85
R_ v106,a86
R_ v107,a87
R_ v108,a104
R_ v109,a105
R_ v110,a106
R_ v111,a107
v_pk_mul_f32 v[104:105], v[104:105], v[46:47] op_sel:[0,1]
v_pk_mul_f32 v[106:107], v[106:107], v[46:47] op_sel:[0,1]
v_pk_mul_f32 v[108:109], v[108:109], v[46:47] op_sel:[0,1]
v_pk_mul_f32 v[110:111], v[110:111], v[46:47] op_sel:[0,1]
C_ 80,104,105
C_ 81,106,107
C_ 82,108,109
C_ 83,110,111
X_ a84,v80
X_ a85,v81
X_ a86,v82
X_ a87,v83
.p2align 3
R_ v96,a88
R_ v97,a89
R_ v98,a90
R_ v99,a91
R_ v100,a108
R_ v101,a109
R_ v102,a110
R_ v103,a111
v_pk_mul_f32 v[96:97], v[96:97], v[48:49] op_sel_hi:[1,0]
v_pk_mul_f32 v[98:99], v[98:99], v[48:49] op_sel_hi:[1,0]
v_pk_mul_f32 v[100:101], v[100:101], v[48:49] op_sel_hi:[1,0]
v_pk_mul_f32 v[102:103], v[102:103], v[48:49] op_sel_hi:[1,0]
C_ 84,96,97
C_ 85,98,99
C_ 86,100,101
C_ 87,102,103
X_ a88,v84
X_ a89,v85
X_ a90,v86
X_ a91,v87
.p2align 3
R_ v104,a92
R_ v105,a93
R_ v106,a94
R_ v107,a95
R_ v108,a112
R_ v109,a113
R_ v110,a114
R_ v111,a115
v_pk_mul_f32 v[104:105], v[104:105], v[48:49] op_sel:[0,1]
v_pk_mul_f32 v[106:107], v[106:107], v[48:49] op_sel:[0,1]
v_pk_mul_f32 v[108:109], v[108:109], v[48:49] op_sel:[0,1]
v_pk_mul_f32 v[110:111], v[110:111], v[48:49] op_sel:[0,1]
C_ 88,104,105
C_ 89,106,107
C_ 90,108,109
C_ 91,110,111
X_ a92,v88
X_ a93,v89
X_ a94,v90
X_ a95,v91
.p2align 3
R_ v96,a96
R_ v97,a97
R_ v98,a98
R_ v99,a99
R_ v100,a116
R_ v101,a117
R_ v102,a118
R_ v103,a119
v_pk_mul_f32 v[96:97], v[96:97], v[50:51] op_sel_hi:[1,0]
v_pk_mul_f32 v[98:99], v[98:99], v[50:51] op_sel_hi:[1,0]
v_pk_mul_f32 v[100:101], v[100:101], v[50:51] op_sel_hi:[1,0]
v_pk_mul_f32 v[102:103], v[102:103], v[50:51] op_sel_hi:[1,0]
C_ 92,96,97
C_ 93,98,99
C_ 94,100,101
C_ 95,102,103
X_ a96,v92
X_ a97,v93
X_ a98,v94
X_ a99,v95
.p2align 3
R_ v104,a120
R_ v105,a121
R_ v106,a122
R_ v107,a123
R_ v108,a140
R_ v109,a141
R_ v110,a142
R_ v111,a143
v_pk_mul_f32 v[104:105], v[104:105], v[46:47] op_sel_hi:[1,0]
v_pk_mul_f32 v[106:107], v[106:107], v[46:47] op_sel_hi:[1,0]
v_pk_mul_f32 v[108:109], v[108:109], v[46:47] op_sel_hi:[1,0]
v_pk_mul_f32 v[110:111], v[110:111], v[46:47] op_sel_hi:[1,0]
C_ 76,104,105
C_ 77,106,107
C_ 78,108,109
C_ 79,110,111
X_ a120,v76
X_ a121,v77
X_ a122,v78
X_ a123,v79
.p2align 3
R_ v96,a124
R_ v97,a125
R_ v98,a126
R_ v99,a127
R_ v100,a144
R_ v101,a145
R_ v102,a146
R_ v103,a147
v_pk_mul_f32 v[96:97], v[96:97], v[46:47] op_sel:[0,1]
v_pk_mul_f32 v[98:99], v[98:99], v[46:47] op_sel:[0,1]
v_pk_mul_f32 v[100:101], v[100:101], v[46:47] op_sel:[0,1]
v_pk_mul_f32 v[102:103], v[102:103], v[46:47] op_sel:[0,1]
C_ 80,96,97
C_ 81,98,99
C_ 82,100,101
C_ 83,102,103
X_ a124,v80
X_ a125,v81
X_ a126,v82
X_ a127,v83
.p2align 3
R_ v104,a128
R_ v105,a129
R_ v106,a130
R_ v107,a131
R_ v108,a148
R_ v109,a149
R_ v110,a150
R_ v111,a151
v_pk_mul_f32 v[104:105], v[104:105], v[48:49] op_sel_hi:[1,0]
v_pk_mul_f32 v[106:107], v[106:107], v[48:49] op_sel_hi:[1,0]
v_pk_mul_f32 v[108:109], v[108:109], v[48:49] op_sel_hi:[1,0]
v_pk_mul_f32 v[110:111], v[110:111], v[48:49] op_sel_hi:[1,0]
C_ 84,104,105
C_ 85,106,107
C_ 86,108,109
C_ 87,110,111
X_ a128,v84
X_ a129,v85
X_ a130,v86
X_ a131,v87
.p2align 3
R_ v96,a132
R_ v97,a133
R_ v98,a134
R_ v99,a135
R_ v100,a152
R_ v101,a153
R_ v102,a154
R_ v103,a155
v_pk_mul_f32 v[96:97], v[96:97], v[48:49] op_sel:[0,1]
v_pk_mul_f32 v[98:99], v[98:99], v[48:49] op_sel:[0,1]
v_pk_mul_f32 v[100:101], v[100:101], v[48:49] op_sel:[0,1]
v_pk_mul_f32 v[102:103], v[102:103], v[48:49] op_sel:[0,1]
C_ 88,96,97
C_ 89,98,99
C_ 90,100,101
C_ 91,102,103
X_ a132,v88
X_ a133,v89
X_ a134,v90
X_ a135,v91
.p2align 3
R_ v104,a136
R_ v105,a137
R_ v106,a138
R_ v107,a139
R_ v108,a156
R_ v109,a157
R_ v110,a158
R_ v111,a159
v_pk_mul_f32 v[104:105], v[104:105], v[50:51] op_sel_hi:[1,0]
v_pk_mul_f32 v[106:107], v[106:107], v[50:51] op_sel_hi:[1,0]
v_pk_mul_f32 v[108:109], v[108:109], v[50:51] op_sel_hi:[1,0]
v_pk_mul_f32 v[110:111], v[110:111], v[50:51] op_sel_hi:[1,0]
C_ 92,104,105
C_ 93,106,107
C_ 94,108,109
C_ 95,110,111
X_ a136,v92
X_ a137,v93
X_ a138,v94
X_ a139,v95
.p2align 3
R_ v96,a160
R_ v97,a161
R_ v98,a162
R_ v99,a163
R_ v100,a180
R_ v101,a181
R_ v102,a182
R_ v103,a183
v_pk_mul_f32 v[96:97], v[96:97], v[46:47] op_sel_hi:[1,0]
v_pk_mul_f32 v[98:99], v[98:99], v[46:47] op_sel_hi:[1,0]
v_pk_mul_f32 v[100:101], v[100:101], v[46:47] op_sel_hi:[1,0]
v_pk_mul_f32 v[102:103], v[102:103], v[46:47] op_sel_hi:[1,0]
C_ 76,96,97
C_ 77,98,99
C_ 78,100,101
C_ 79,102,103
X_ a160,v76
X_ a161,v77
X_ a162,v78
X_ a163,v79
.p2align 3
R_ v104,a164
R_ v105,a165
R_ v106,a166
R_ v107,a167
R_ v108,a184
R_ v109,a185
R_ v110,a186
R_ v111,a187
v_pk_mul_f32 v[104:105], v[104:105], v[46:47] op_sel:[0,1]
v_pk_mul_f32 v[106:107], v[106:107], v[46:47] op_sel:[0,1]
v_pk_mul_f32 v[108:109], v[108:109], v[46:47] op_sel:[0,1]
v_pk_mul_f32 v[110:111], v[110:111], v[46:47] op_sel:[0,1]
C_ 80,104,105
C_ 81,106,107
C_ 82,108,109
C_ 83,110,111
X_ a164,v80
X_ a165,v81
X_ a166,v82
X_ a167,v83
.p2align 3
R_ v96,a168
R_ v97,a169
R_ v98,a170
R_ v99,a171
R_ v100,a188
R_ v101,a189
R_ v102,a190
R_ v103,a191
v_pk_mul_f32 v[96:97], v[96:97], v[48:49] op_sel_hi:[1,0]
v_pk_mul_f32 v[98:99], v[98:99], v[48:49] op_sel_hi:[1,0]
v_pk_mul_f32 v[100:101], v[100:101], v[48:49] op_sel_hi:[1,0]
v_pk_mul_f32 v[102:103], v[102:103], v[48:49] op_sel_hi:[1,0]
C_ 84,96,97
C_ 85,98,99
C_ 86,100,101
C_ 87,102,103
X_ a168,v84
X_ a169,v85
X_ a170,v86
X_ a171,v87
.p2align 3
R_ v104,a172
R_ v105,a173
R_ v106,a174
R_ v107,a175
R_ v108,a192
R_ v109,a193
R_ v110,a194
R_ v111,a195
v_pk_mul_f32 v[104:105], v[104:105], v[48:49] op_sel:[0,1]
v_pk_mul_f32 v[106:107], v[106:107], v[48:49] op_sel:[0,1]
v_pk_mul_f32 v[108:109], v[108:109], v[48:49] op_sel:[0,1]
v_pk_mul_f32 v[110:111], v[110:111], v[48:49] op_sel:[0,1]
C_ 88,104,105
C_ 89,106,107
C_ 90,108,109
C_ 91,110,111
X_ a172,v88
X_ a173,v89
X_ a174,v90
X_ a175,v91
.p2align 3
R_ v96,a176
R_ v97,a177
R_ v98,a178
R_ v99,a179
R_ v100,a196
R_ v101,a197
R_ v102,a198
R_ v103,a199
v_pk_mul_f32 v[96:97], v[96:97], v[50:51] op_sel_hi:[1,0]
v_pk_mul_f32 v[98:99], v[98:99], v[50:51] op_sel_hi:[1,0]
v_pk_mul_f32 v[100:101], v[100:101], v[50:51] op_sel_hi:[1,0]
v_pk_mul_f32 v[102:103], v[102:103], v[50:51] op_sel_hi:[1,0]
C_ 92,96,97
C_ 93,98,99
C_ 94,100,101
C_ 95,102,103
X_ a176,v92
X_ a177,v93
X_ a178,v94
X_ a179,v95
.p2align 3
R_ v104,a200
R_ v105,a201
R_ v106,a202
R_ v107,a203
R_ v108,a220
R_ v109,a221
R_ v110,a222
R_ v111,a223
v_pk_mul_f32 v[104:105], v[104:105], v[46:47] op_sel_hi:[1,0]
v_pk_mul_f32 v[106:107], v[106:107], v[46:47] op_sel_hi:[1,0]
v_pk_mul_f32 v[108:109], v[108:109], v[46:47] op_sel_hi:[1,0]
v_pk_mul_f32 v[110:111], v[110:111], v[46:47] op_sel_hi:[1,0]
C_ 76,104,105
C_ 77,106,107
C_ 78,108,109
C_ 79,110,111
X_ a200,v76
X_ a201,v77
X_ a202,v78
X_ a203,v79
.p2align 3
R_ v96,a204
R_ v97,a205
R_ v98,a206
R_ v99,a207
R_ v100,a224
R_ v101,a225
R_ v102,a226
R_ v103,a227
v_pk_mul_f32 v[96:97], v[96:97], v[46:47] op_sel:[0,1]
v_pk_mul_f32 v[98:99], v[98:99], v[46:47] op_sel:[0,1]
v_pk_mul_f32 v[100:101], v[100:101], v[46:47] op_sel:[0,1]
v_pk_mul_f32 v[102:103], v[102:103], v[46:47] op_sel:[0,1]
C_ 80,96,97
C_ 81,98,99
C_ 82,100,101
C_ 83,102,103
X_ a204,v80
X_ a205,v81
X_ a206,v82
X_ a207,v83
v_mov_b32 v4, v20
v_mov_b32 v5, v21
R_ %0,a242
R_ v2,a243
v_xor_b32 v1, 64, %0
v_xor_b32 v3, 64, v2
s_waitcnt vmcnt(0) lgkmcnt(0)
s_barrier
Ta 20,%0,8192
Ta 22,%0,8704
Ta 24,%0,12288
Ta 26,%0,12800
Da 60,v2,0
Da 100,v3,0
Da 64,v2,2048
Da 104,v3,2048
Da 68,v2,4096
Da 108,v3,4096
Da 72,v2,6144
Da 112,v3,6144
Da 76,v2,8192
Da 116,v3,8192
Ta 28,%0,8200
Ta 30,%0,8712
Ta 32,%0,12296
Ta 34,%0,12808
.p2align 3
R_ v104,a208
R_ v105,a209
R_ v106,a210
R_ v107,a211
R_ v108,a228
R_ v109,a229
R_ v110,a230
R_ v111,a231
v_pk_mul_f32 v[104:105], v[104:105], v[48:49] op_sel_hi:[1,0]
v_pk_mul_f32 v[106:107], v[106:107], v[48:49] op_sel_hi:[1,0]
v_pk_mul_f32 v[108:109], v[108:109], v[48:49] op_sel_hi:[1,0]
v_pk_mul_f32 v[110:111], v[110:111], v[48:49] op_sel_hi:[1,0]
C_ 84,104,105
C_ 85,106,107
C_ 86,108,109
C_ 87,110,111
X_ a208,v84
X_ a209,v85
X_ a210,v86
X_ a211,v87
.p2align 3
R_ v96,a212
R_ v97,a213
R_ v98,a214
R_ v99,a215
R_ v100,a232
R_ v101,a233
R_ v102,a234
R_ v103,a235
v_pk_mul_f32 v[96:97], v[96:97], v[48:49] op_sel:[0,1]
v_pk_mul_f32 v[98:99], v[98:99], v[48:49] op_sel:[0,1]
v_pk_mul_f32 v[100:101], v[100:101], v[48:49] op_sel:[0,1]
v_pk_mul_f32 v[102:103], v[102:103], v[48:49] op_sel:[0,1]
C_ 88,96,97
C_ 89,98,99
C_ 90,100,101
C_ 91,102,103
X_ a212,v88
X_ a213,v89
X_ a214,v90
X_ a215,v91
.p2align 3
R_ v104,a216
R_ v105,a217
R_ v106,a218
R_ v107,a219
R_ v108,a236
R_ v109,a237
R_ v110,a238
R_ v111,a239
v_pk_mul_f32 v[104:105], v[104:105], v[50:51] op_sel_hi:[1,0]
v_pk_mul_f32 v[106:107], v[106:107], v[50:51] op_sel_hi:[1,0]
v_pk_mul_f32 v[108:109], v[108:109], v[50:51] op_sel_hi:[1,0]
v_pk_mul_f32 v[110:111], v[110:111], v[50:51] op_sel_hi:[1,0]
C_ 92,104,105
C_ 93,106,107
C_ 94,108,109
C_ 95,110,111
X_ a216,v92
X_ a217,v93
X_ a218,v94
X_ a219,v95
s_branch .Lq
.Ltramp0_1:
s_mov_b32 s63, 1
s_branch .Lslow1
.Ltramp0_2:
s_mov_b32 s63, 2
s_branch .Lslow0
.Ltramp0_13:
s_mov_b32 s63, 13
s_branch .Lslow1
.Ltramp0_14:
s_mov_b32 s63, 14
s_branch .Lslow0
.Ltramp0_15:
s_mov_b32 s63, 15
s_branch .Lslow1
.Lstep0_16:
Zvvv 76,92,116
Zvvv 80,92,120
Zvvv 84,92,124
Zvvv 88,92,128
Zvvv 92,92,132
s_nop 5
v_max3_f32 v10, v76, v80, v84
s_nop 0
v_max3_f32 v10, v10, v88, v92
v_cmp_lt_f32 vcc, 0x42000000, v10
s_cbranch_vccnz .Ltramp0_15
s_branch .Ljoin0
.Ltramp1_1:
s_mov_b32 s63, 1
s_branch .Lslow1
.Ltramp1_2:
s_mov_b32 s63, 2
s_branch .Lslow0
.Ltramp1_3:
s_mov_b32 s63, 3
s_branch .Lslow1
.Ltramp1_4:
s_mov_b32 s63, 4
s_branch .Lslow0
.Ltramp1_5:
s_mov_b32 s63, 5
s_branch .Lslow1
.Ltramp1_6:
s_mov_b32 s63, 6
s_branch .Lslow0
.Ltramp1_7:
s_mov_b32 s63, 7
s_branch .Lslow1
.Ltramp1_8:
s_mov_b32 s63, 8
s_branch .Lslow0
.Ltramp1_9:
s_mov_b32 s63, 9
s_branch .Lslow1
.Ltramp1_10:
s_mov_b32 s63, 10
s_branch .Lslow0
.Ltramp1_11:
s_mov_b32 s63, 11
s_branch .Lslow1
.Ltramp1_12:
s_mov_b32 s63, 12
s_branch .Lslow0
.Ltramp1_13:
s_mov_b32 s63, 13
s_branch .Lslow1
.Ltramp1_14:
s_mov_b32 s63, 14
s_branch .Lslow0
.Ltramp1_15:
s_mov_b32 s63, 15
s_branch .Lslow1
.Lstep1_16:
Zvvv 76,92,116
Zvvv 80,92,120
Zvvv 84,92,124
Zvvv 88,92,128
Zvvv 92,92,132
s_nop 5
v_max3_f32 v10, v76, v80, v84
s_nop 0
v_max3_f32 v10, v10, v88, v92
v_cmp_lt_f32 vcc, 0x42000000, v10
s_cbranch_vccnz .Ltramp1_15
s_branch .Ljoin1
.Ltramp2_1:
s_mov_b32 s63, 1
s_branch .Lslow1
.Ltramp2_2:
s_mov_b32 s63, 2
s_branch .Lslow0
.Ltramp2_3:
s_mov_b32 s63, 3
s_branch .Lslow1
.Ltramp2_4:
s_mov_b32 s63, 4
s_branch .Lslow0
.Ltramp2_5:
s_mov_b32 s63, 5
s_branch .Lslow1
.Ltramp2_6:
s_mov_b32 s63, 6
s_branch .Lslow0
.Ltramp2_7:
s_mov_b32 s63, 7
s_branch .Lslow1
.Ltramp2_8:
s_mov_b32 s63, 8
s_branch .Lslow0
.Ltramp2_9:
s_mov_b32 s63, 9
s_branch .Lslow1
.Ltramp2_10:
s_mov_b32 s63, 10
s_branch .Lslow0
.Ltramp2_11:
s_mov_b32 s63, 11
s_branch .Lslow1
.Ltramp2_12:
s_mov_b32 s63, 12
s_branch .Lslow0
.Ltramp2_13:
s_mov_b32 s63, 13
s_branch .Lslow1
.Ltramp2_14:
s_mov_b32 s63, 14
s_branch .Lslow0
.Ltramp2_15:
s_mov_b32 s63, 15
s_branch .Lslow1
.Lstep2_16:
Zvvv 76,92,116
Zvvv 80,92,120
Zvvv 84,92,124
Zvvv 88,92,128
Zvvv 92,92,132
s_nop 5
v_max3_f32 v10, v76, v80, v84
s_nop 0
v_max3_f32 v10, v10, v88, v92
v_cmp_lt_f32 vcc, 0x42000000, v10
s_cbranch_vccnz .Ltramp2_15
s_branch .Ljoin2
.Lslow0:
W_ 0
s_nop 7
s_nop 7
s_and_b32 s64, s61, 4294959104
s_mul_i32 s53, s63, 12288
S_ s64,s64,s53
R_ v11,a248
v_and_b32 v10, 0xf0, v11
v_mul_u32_u24 v10, 24, v10
v_lshrrev_b32 v11, 8, v11
v_lshl_add_u32 v10, v11, 4, v10
S_ s53,s64,6144
buffer_load_dwordx4 v[116:119], v10, s[32:35], s53 offen offset:0
S_ s53,s64,6144
buffer_load_dwordx4 v[120:123], v10, s[32:35], s53 offen offset:64
S_ s53,s64,6144
buffer_load_dwordx4 v[124:127], v10, s[32:35], s53 offen offset:128
S_ s53,s64,6144
buffer_load_dwordx4 v[128:131], v10, s[32:35], s53 offen offset:192
s_waitcnt vmcnt(0)
Nvvv 96,116,136,56
Nvvv 100,116,140,60
Nvvv 104,116,144,64
Nvvv 108,116,148,68
Nvvv 112,116,152,72
Mvvv 96,120,156
Mvvv 100,120,160
Mvvv 104,120,164
Mvvv 108,120,168
Mvvv 112,120,172
Mvvv 96,124,176
Mvvv 100,124,180
Mvvv 104,124,184
Mvvv 108,124,188
Mvvv 112,124,192
Mvvv 96,128,196
Mvvv 100,128,200
Mvvv 104,128,204
Mvvv 108,128,208
Mvvv 112,128,212
s_nop 7
S_ s53,s64,6144
buffer_load_dwordx4 v[116:119], v10, s[32:35], s53 offen offset:256
S_ s53,s64,6144
buffer_load_dwordx4 v[120:123], v10, s[32:35], s53 offen offset:320
S_ s53,s64,0
buffer_load_dwordx4 v[124:127], v10, s[32:35], s53 offen offset:0
S_ s53,s64,0
buffer_load_dwordx4 v[128:131], v10, s[32:35], s53 offen offset:64
s_waitcnt vmcnt(0)
Mvvv 96,116,216
Mvvv 100,116,220
Mvvv 104,116,224
Mvvv 108,116,228
Mvvv 112,116,232
Mvvv 96,120,236
Mvvv 100,120,240
Mvvv 104,120,244
Mvvv 108,120,248
Mvvv 112,120,252
Nvvv 76,124,136,56
Nvvv 80,124,140,60
Nvvv 84,124,144,64
Nvvv 88,124,148,68
Nvvv 92,124,152,72
Mvvv 76,128,156
Mvvv 80,128,160
Mvvv 84,128,164
Mvvv 88,128,168
Mvvv 92,128,172
s_nop 7
S_ s53,s64,0
buffer_load_dwordx4 v[116:119], v10, s[32:35], s53 offen offset:128
S_ s53,s64,0
buffer_load_dwordx4 v[120:123], v10, s[32:35], s53 offen offset:192
S_ s53,s64,0
buffer_load_dwordx4 v[124:127], v10, s[32:35], s53 offen offset:256
S_ s53,s64,0
buffer_load_dwordx4 v[128:131], v10, s[32:35], s53 offen offset:320
s_waitcnt vmcnt(0)
Mvvv 76,116,176
Mvvv 80,116,180
Mvvv 84,116,184
Mvvv 88,116,188
Mvvv 92,116,192
Mvvv 76,120,196
Mvvv 80,120,200
Mvvv 84,120,204
Mvvv 88,120,208
Mvvv 92,120,212
Mvvv 76,124,216
Mvvv 80,124,220
Mvvv 84,124,224
Mvvv 88,124,228
Mvvv 92,124,232
Mvvv 76,128,236
Mvvv 80,128,240
Mvvv 84,128,244
Mvvv 88,128,248
Mvvv 92,128,252
s_nop 7
s_nop 2
v_max3_f32 v10, v76, v77, v78
v_max3_f32 v10, v10, v79, v96
v_max3_f32 v10, v10, v97, v98
v_max_f32 v10, v10, v99
v_mov_b32 v11, v10
s_nop 1
v_permlane16_swap_b32 v10, v11
v_max_f32 v10, v10, v11
v_mov_b32 v11, v10
s_nop 1
v_permlane32_swap_b32 v10, v11
v_max_f32 v10, v10, v11
A_ v10,0x40e00000,v10
v_max_f32 v10, 0, v10
v_exp_f32 v11, -v10
s_nop 0
U_ v46,v46,v11
R_ v12,a0
U_ v12,v12,v11
X_ a0,v12
R_ v13,a1
U_ v13,v13,v11
X_ a1,v13
R_ v12,a2
U_ v12,v12,v11
X_ a2,v12
R_ v13,a3
U_ v13,v13,v11
X_ a3,v13
R_ v12,a20
U_ v12,v12,v11
X_ a20,v12
R_ v13,a21
U_ v13,v13,v11
X_ a21,v13
R_ v12,a22
U_ v12,v12,v11
X_ a22,v12
R_ v13,a23
U_ v13,v13,v11
X_ a23,v13
R_ v12,a40
U_ v12,v12,v11
X_ a40,v12
R_ v13,a41
U_ v13,v13,v11
X_ a41,v13
R_ v12,a42
U_ v12,v12,v11
X_ a42,v12
R_ v13,a43
U_ v13,v13,v11
X_ a43,v13
R_ v12,a60
U_ v12,v12,v11
X_ a60,v12
R_ v13,a61
U_ v13,v13,v11
X_ a61,v13
R_ v12,a62
U_ v12,v12,v11
X_ a62,v12
R_ v13,a63
U_ v13,v13,v11
X_ a63,v13
R_ v12,a80
U_ v12,v12,v11
X_ a80,v12
R_ v13,a81
U_ v13,v13,v11
X_ a81,v13
R_ v12,a82
U_ v12,v12,v11
X_ a82,v12
R_ v13,a83
U_ v13,v13,v11
X_ a83,v13
R_ v12,a100
U_ v12,v12,v11
X_ a100,v12
R_ v13,a101
U_ v13,v13,v11
X_ a101,v13
R_ v12,a102
U_ v12,v12,v11
X_ a102,v12
R_ v13,a103
U_ v13,v13,v11
X_ a103,v13
R_ v12,a120
U_ v12,v12,v11
X_ a120,v12
R_ v13,a121
U_ v13,v13,v11
X_ a121,v13
R_ v12,a122
U_ v12,v12,v11
X_ a122,v12
R_ v13,a123
U_ v13,v13,v11
X_ a123,v13
R_ v12,a140
U_ v12,v12,v11
X_ a140,v12
R_ v13,a141
U_ v13,v13,v11
X_ a141,v13
R_ v12,a142
U_ v12,v12,v11
X_ a142,v12
R_ v13,a143
U_ v13,v13,v11
X_ a143,v13
R_ v12,a160
U_ v12,v12,v11
X_ a160,v12
R_ v13,a161
U_ v13,v13,v11
X_ a161,v13
R_ v12,a162
U_ v12,v12,v11
X_ a162,v12
R_ v13,a163
U_ v13,v13,v11
X_ a163,v13
R_ v12,a180
U_ v12,v12,v11
X_ a180,v12
R_ v13,a181
U_ v13,v13,v11
X_ a181,v13
R_ v12,a182
U_ v12,v12,v11
X_ a182,v12
R_ v13,a183
U_ v13,v13,v11
X_ a183,v13
R_ v12,a200
U_ v12,v12,v11
X_ a200,v12
R_ v13,a201
U_ v13,v13,v11
X_ a201,v13
R_ v12,a202
U_ v12,v12,v11
X_ a202,v12
R_ v13,a203
U_ v13,v13,v11
X_ a203,v13
R_ v12,a220
U_ v12,v12,v11
X_ a220,v12
R_ v13,a221
U_ v13,v13,v11
X_ a221,v13
R_ v12,a222
U_ v12,v12,v11
X_ a222,v12
R_ v13,a223
U_ v13,v13,v11
X_ a223,v13
B_ v56,v56,v10
B_ v57,v57,v10
B_ v58,v58,v10
B_ v59,v59,v10
B_ v76,v76,v10
B_ v77,v77,v10
B_ v78,v78,v10
B_ v79,v79,v10
B_ v96,v96,v10
B_ v97,v97,v10
B_ v98,v98,v10
B_ v99,v99,v10
E_ 76
E_ 77
E_ 78
E_ 79
E_ 96
E_ 97
E_ 98
E_ 99
s_nop 0
C_ 99,98,99
C_ 98,96,97
C_ 96,76,77
C_ 97,78,79
v_max3_f32 v10, v80, v81, v82
v_max3_f32 v10, v10, v83, v100
v_max3_f32 v10, v10, v101, v102
v_max_f32 v10, v10, v103
v_mov_b32 v11, v10
s_nop 1
v_permlane16_swap_b32 v10, v11
v_max_f32 v10, v10, v11
v_mov_b32 v11, v10
s_nop 1
v_permlane32_swap_b32 v10, v11
v_max_f32 v10, v10, v11
A_ v10,0x40e00000,v10
v_max_f32 v10, 0, v10
v_exp_f32 v11, -v10
s_nop 0
U_ v47,v47,v11
R_ v12,a4
U_ v12,v12,v11
X_ a4,v12
R_ v13,a5
U_ v13,v13,v11
X_ a5,v13
R_ v12,a6
U_ v12,v12,v11
X_ a6,v12
R_ v13,a7
U_ v13,v13,v11
X_ a7,v13
R_ v12,a24
U_ v12,v12,v11
X_ a24,v12
R_ v13,a25
U_ v13,v13,v11
X_ a25,v13
R_ v12,a26
U_ v12,v12,v11
X_ a26,v12
R_ v13,a27
U_ v13,v13,v11
X_ a27,v13
R_ v12,a44
U_ v12,v12,v11
X_ a44,v12
R_ v13,a45
U_ v13,v13,v11
X_ a45,v13
R_ v12,a46
U_ v12,v12,v11
X_ a46,v12
R_ v13,a47
U_ v13,v13,v11
X_ a47,v13
R_ v12,a64
U_ v12,v12,v11
X_ a64,v12
R_ v13,a65
U_ v13,v13,v11
X_ a65,v13
R_ v12,a66
U_ v12,v12,v11
X_ a66,v12
R_ v13,a67
U_ v13,v13,v11
X_ a67,v13
R_ v12,a84
U_ v12,v12,v11
X_ a84,v12
R_ v13,a85
U_ v13,v13,v11
X_ a85,v13
R_ v12,a86
U_ v12,v12,v11
X_ a86,v12
R_ v13,a87
U_ v13,v13,v11
X_ a87,v13
R_ v12,a104
U_ v12,v12,v11
X_ a104,v12
R_ v13,a105
U_ v13,v13,v11
X_ a105,v13
R_ v12,a106
U_ v12,v12,v11
X_ a106,v12
R_ v13,a107
U_ v13,v13,v11
X_ a107,v13
R_ v12,a124
U_ v12,v12,v11
X_ a124,v12
R_ v13,a125
U_ v13,v13,v11
X_ a125,v13
R_ v12,a126
U_ v12,v12,v11
X_ a126,v12
R_ v13,a127
U_ v13,v13,v11
X_ a127,v13
R_ v12,a144
U_ v12,v12,v11
X_ a144,v12
R_ v13,a145
U_ v13,v13,v11
X_ a145,v13
R_ v12,a146
U_ v12,v12,v11
X_ a146,v12
R_ v13,a147
U_ v13,v13,v11
X_ a147,v13
R_ v12,a164
U_ v12,v12,v11
X_ a164,v12
R_ v13,a165
U_ v13,v13,v11
X_ a165,v13
R_ v12,a166
U_ v12,v12,v11
X_ a166,v12
R_ v13,a167
U_ v13,v13,v11
X_ a167,v13
R_ v12,a184
U_ v12,v12,v11
X_ a184,v12
R_ v13,a185
U_ v13,v13,v11
X_ a185,v13
R_ v12,a186
U_ v12,v12,v11
X_ a186,v12
R_ v13,a187
U_ v13,v13,v11
X_ a187,v13
R_ v12,a204
U_ v12,v12,v11
X_ a204,v12
R_ v13,a205
U_ v13,v13,v11
X_ a205,v13
R_ v12,a206
U_ v12,v12,v11
X_ a206,v12
R_ v13,a207
U_ v13,v13,v11
X_ a207,v13
R_ v12,a224
U_ v12,v12,v11
X_ a224,v12
R_ v13,a225
U_ v13,v13,v11
X_ a225,v13
R_ v12,a226
U_ v12,v12,v11
X_ a226,v12
R_ v13,a227
U_ v13,v13,v11
X_ a227,v13
B_ v60,v60,v10
B_ v61,v61,v10
B_ v62,v62,v10
B_ v63,v63,v10
B_ v80,v80,v10
B_ v81,v81,v10
B_ v82,v82,v10
B_ v83,v83,v10
B_ v100,v100,v10
B_ v101,v101,v10
B_ v102,v102,v10
B_ v103,v103,v10
E_ 80
E_ 81
E_ 82
E_ 83
E_ 100
E_ 101
E_ 102
E_ 103
s_nop 0
C_ 103,102,103
C_ 102,100,101
C_ 100,80,81
C_ 101,82,83
v_max3_f32 v10, v84, v85, v86
v_max3_f32 v10, v10, v87, v104
v_max3_f32 v10, v10, v105, v106
v_max_f32 v10, v10, v107
v_mov_b32 v11, v10
s_nop 1
v_permlane16_swap_b32 v10, v11
v_max_f32 v10, v10, v11
v_mov_b32 v11, v10
s_nop 1
v_permlane32_swap_b32 v10, v11
v_max_f32 v10, v10, v11
A_ v10,0x40e00000,v10
v_max_f32 v10, 0, v10
v_exp_f32 v11, -v10
s_nop 0
U_ v48,v48,v11
R_ v12,a8
U_ v12,v12,v11
X_ a8,v12
R_ v13,a9
U_ v13,v13,v11
X_ a9,v13
R_ v12,a10
U_ v12,v12,v11
X_ a10,v12
R_ v13,a11
U_ v13,v13,v11
X_ a11,v13
R_ v12,a28
U_ v12,v12,v11
X_ a28,v12
R_ v13,a29
U_ v13,v13,v11
X_ a29,v13
R_ v12,a30
U_ v12,v12,v11
X_ a30,v12
R_ v13,a31
U_ v13,v13,v11
X_ a31,v13
R_ v12,a48
U_ v12,v12,v11
X_ a48,v12
R_ v13,a49
U_ v13,v13,v11
X_ a49,v13
R_ v12,a50
U_ v12,v12,v11
X_ a50,v12
R_ v13,a51
U_ v13,v13,v11
X_ a51,v13
R_ v12,a68
U_ v12,v12,v11
X_ a68,v12
R_ v13,a69
U_ v13,v13,v11
X_ a69,v13
R_ v12,a70
U_ v12,v12,v11
X_ a70,v12
R_ v13,a71
U_ v13,v13,v11
X_ a71,v13
R_ v12,a88
U_ v12,v12,v11
X_ a88,v12
R_ v13,a89
U_ v13,v13,v11
X_ a89,v13
R_ v12,a90
U_ v12,v12,v11
X_ a90,v12
R_ v13,a91
U_ v13,v13,v11
X_ a91,v13
R_ v12,a108
U_ v12,v12,v11
X_ a108,v12
R_ v13,a109
U_ v13,v13,v11
X_ a109,v13
R_ v12,a110
U_ v12,v12,v11
X_ a110,v12
R_ v13,a111
U_ v13,v13,v11
X_ a111,v13
R_ v12,a128
U_ v12,v12,v11
X_ a128,v12
R_ v13,a129
U_ v13,v13,v11
X_ a129,v13
R_ v12,a130
U_ v12,v12,v11
X_ a130,v12
R_ v13,a131
U_ v13,v13,v11
X_ a131,v13
R_ v12,a148
U_ v12,v12,v11
X_ a148,v12
R_ v13,a149
U_ v13,v13,v11
X_ a149,v13
R_ v12,a150
U_ v12,v12,v11
X_ a150,v12
R_ v13,a151
U_ v13,v13,v11
X_ a151,v13
R_ v12,a168
U_ v12,v12,v11
X_ a168,v12
R_ v13,a169
U_ v13,v13,v11
X_ a169,v13
R_ v12,a170
U_ v12,v12,v11
X_ a170,v12
R_ v13,a171
U_ v13,v13,v11
X_ a171,v13
R_ v12,a188
U_ v12,v12,v11
X_ a188,v12
R_ v13,a189
U_ v13,v13,v11
X_ a189,v13
R_ v12,a190
U_ v12,v12,v11
X_ a190,v12
R_ v13,a191
U_ v13,v13,v11
X_ a191,v13
R_ v12,a208
U_ v12,v12,v11
X_ a208,v12
R_ v13,a209
U_ v13,v13,v11
X_ a209,v13
R_ v12,a210
U_ v12,v12,v11
X_ a210,v12
R_ v13,a211
U_ v13,v13,v11
X_ a211,v13
R_ v12,a228
U_ v12,v12,v11
X_ a228,v12
R_ v13,a229
U_ v13,v13,v11
X_ a229,v13
R_ v12,a230
U_ v12,v12,v11
X_ a230,v12
R_ v13,a231
U_ v13,v13,v11
X_ a231,v13
B_ v64,v64,v10
B_ v65,v65,v10
B_ v66,v66,v10
B_ v67,v67,v10
B_ v84,v84,v10
B_ v85,v85,v10
B_ v86,v86,v10
B_ v87,v87,v10
B_ v104,v104,v10
B_ v105,v105,v10
B_ v106,v106,v10
B_ v107,v107,v10
E_ 84
E_ 85
E_ 86
E_ 87
E_ 104
E_ 105
E_ 106
E_ 107
s_nop 0
C_ 107,106,107
C_ 106,104,105
C_ 104,84,85
C_ 105,86,87
v_max3_f32 v10, v88, v89, v90
v_max3_f32 v10, v10, v91, v108
v_max3_f32 v10, v10, v109, v110
v_max_f32 v10, v10, v111
v_mov_b32 v11, v10
s_nop 1
v_permlane16_swap_b32 v10, v11
v_max_f32 v10, v10, v11
v_mov_b32 v11, v10
s_nop 1
v_permlane32_swap_b32 v10, v11
v_max_f32 v10, v10, v11
A_ v10,0x40e00000,v10
v_max_f32 v10, 0, v10
v_exp_f32 v11, -v10
s_nop 0
U_ v49,v49,v11
R_ v12,a12
U_ v12,v12,v11
X_ a12,v12
R_ v13,a13
U_ v13,v13,v11
X_ a13,v13
R_ v12,a14
U_ v12,v12,v11
X_ a14,v12
R_ v13,a15
U_ v13,v13,v11
X_ a15,v13
R_ v12,a32
U_ v12,v12,v11
X_ a32,v12
R_ v13,a33
U_ v13,v13,v11
X_ a33,v13
R_ v12,a34
U_ v12,v12,v11
X_ a34,v12
R_ v13,a35
U_ v13,v13,v11
X_ a35,v13
R_ v12,a52
U_ v12,v12,v11
X_ a52,v12
R_ v13,a53
U_ v13,v13,v11
X_ a53,v13
R_ v12,a54
U_ v12,v12,v11
X_ a54,v12
R_ v13,a55
U_ v13,v13,v11
X_ a55,v13
R_ v12,a72
U_ v12,v12,v11
X_ a72,v12
R_ v13,a73
U_ v13,v13,v11
X_ a73,v13
R_ v12,a74
U_ v12,v12,v11
X_ a74,v12
R_ v13,a75
U_ v13,v13,v11
X_ a75,v13
R_ v12,a92
U_ v12,v12,v11
X_ a92,v12
R_ v13,a93
U_ v13,v13,v11
X_ a93,v13
R_ v12,a94
U_ v12,v12,v11
X_ a94,v12
R_ v13,a95
U_ v13,v13,v11
X_ a95,v13
R_ v12,a112
U_ v12,v12,v11
X_ a112,v12
R_ v13,a113
U_ v13,v13,v11
X_ a113,v13
R_ v12,a114
U_ v12,v12,v11
X_ a114,v12
R_ v13,a115
U_ v13,v13,v11
X_ a115,v13
R_ v12,a132
U_ v12,v12,v11
X_ a132,v12
R_ v13,a133
U_ v13,v13,v11
X_ a133,v13
R_ v12,a134
U_ v12,v12,v11
X_ a134,v12
R_ v13,a135
U_ v13,v13,v11
X_ a135,v13
R_ v12,a152
U_ v12,v12,v11
X_ a152,v12
R_ v13,a153
U_ v13,v13,v11
X_ a153,v13
R_ v12,a154
U_ v12,v12,v11
X_ a154,v12
R_ v13,a155
U_ v13,v13,v11
X_ a155,v13
R_ v12,a172
U_ v12,v12,v11
X_ a172,v12
R_ v13,a173
U_ v13,v13,v11
X_ a173,v13
R_ v12,a174
U_ v12,v12,v11
X_ a174,v12
R_ v13,a175
U_ v13,v13,v11
X_ a175,v13
R_ v12,a192
U_ v12,v12,v11
X_ a192,v12
R_ v13,a193
U_ v13,v13,v11
X_ a193,v13
R_ v12,a194
U_ v12,v12,v11
X_ a194,v12
R_ v13,a195
U_ v13,v13,v11
X_ a195,v13
R_ v12,a212
U_ v12,v12,v11
X_ a212,v12
R_ v13,a213
U_ v13,v13,v11
X_ a213,v13
R_ v12,a214
U_ v12,v12,v11
X_ a214,v12
R_ v13,a215
U_ v13,v13,v11
X_ a215,v13
R_ v12,a232
U_ v12,v12,v11
X_ a232,v12
R_ v13,a233
U_ v13,v13,v11
X_ a233,v13
R_ v12,a234
U_ v12,v12,v11
X_ a234,v12
R_ v13,a235
U_ v13,v13,v11
X_ a235,v13
B_ v68,v68,v10
B_ v69,v69,v10
B_ v70,v70,v10
B_ v71,v71,v10
B_ v88,v88,v10
B_ v89,v89,v10
B_ v90,v90,v10
B_ v91,v91,v10
B_ v108,v108,v10
B_ v109,v109,v10
B_ v110,v110,v10
B_ v111,v111,v10
E_ 88
E_ 89
E_ 90
E_ 91
E_ 108
E_ 109
E_ 110
E_ 111
)A"
R"A(s_nop 0
C_ 111,110,111
C_ 110,108,109
C_ 108,88,89
C_ 109,90,91
v_max3_f32 v10, v92, v93, v94
v_max3_f32 v10, v10, v95, v112
v_max3_f32 v10, v10, v113, v114
v_max_f32 v10, v10, v115
v_mov_b32 v11, v10
s_nop 1
v_permlane16_swap_b32 v10, v11
v_max_f32 v10, v10, v11
v_mov_b32 v11, v10
s_nop 1
v_permlane32_swap_b32 v10, v11
v_max_f32 v10, v10, v11
A_ v10,0x40e00000,v10
v_max_f32 v10, 0, v10
v_exp_f32 v11, -v10
s_nop 0
U_ v50,v50,v11
R_ v12,a16
U_ v12,v12,v11
X_ a16,v12
R_ v13,a17
U_ v13,v13,v11
X_ a17,v13
R_ v12,a18
U_ v12,v12,v11
X_ a18,v12
R_ v13,a19
U_ v13,v13,v11
X_ a19,v13
R_ v12,a36
U_ v12,v12,v11
X_ a36,v12
R_ v13,a37
U_ v13,v13,v11
X_ a37,v13
R_ v12,a38
U_ v12,v12,v11
X_ a38,v12
R_ v13,a39
U_ v13,v13,v11
X_ a39,v13
R_ v12,a56
U_ v12,v12,v11
X_ a56,v12
R_ v13,a57
U_ v13,v13,v11
X_ a57,v13
R_ v12,a58
U_ v12,v12,v11
X_ a58,v12
R_ v13,a59
U_ v13,v13,v11
X_ a59,v13
R_ v12,a76
U_ v12,v12,v11
X_ a76,v12
R_ v13,a77
U_ v13,v13,v11
X_ a77,v13
R_ v12,a78
U_ v12,v12,v11
X_ a78,v12
R_ v13,a79
U_ v13,v13,v11
X_ a79,v13
R_ v12,a96
U_ v12,v12,v11
X_ a96,v12
R_ v13,a97
U_ v13,v13,v11
X_ a97,v13
R_ v12,a98
U_ v12,v12,v11
X_ a98,v12
R_ v13,a99
U_ v13,v13,v11
X_ a99,v13
R_ v12,a116
U_ v12,v12,v11
X_ a116,v12
R_ v13,a117
U_ v13,v13,v11
X_ a117,v13
R_ v12,a118
U_ v12,v12,v11
X_ a118,v12
R_ v13,a119
U_ v13,v13,v11
X_ a119,v13
R_ v12,a136
U_ v12,v12,v11
X_ a136,v12
R_ v13,a137
U_ v13,v13,v11
X_ a137,v13
R_ v12,a138
U_ v12,v12,v11
X_ a138,v12
R_ v13,a139
U_ v13,v13,v11
X_ a139,v13
R_ v12,a156
U_ v12,v12,v11
X_ a156,v12
R_ v13,a157
U_ v13,v13,v11
X_ a157,v13
R_ v12,a158
U_ v12,v12,v11
X_ a158,v12
R_ v13,a159
U_ v13,v13,v11
X_ a159,v13
R_ v12,a176
U_ v12,v12,v11
X_ a176,v12
R_ v13,a177
U_ v13,v13,v11
X_ a177,v13
R_ v12,a178
U_ v12,v12,v11
X_ a178,v12
R_ v13,a179
U_ v13,v13,v11
X_ a179,v13
R_ v12,a196
U_ v12,v12,v11
X_ a196,v12
R_ v13,a197
U_ v13,v13,v11
X_ a197,v13
R_ v12,a198
U_ v12,v12,v11
X_ a198,v12
R_ v13,a199
U_ v13,v13,v11
X_ a199,v13
R_ v12,a216
U_ v12,v12,v11
X_ a216,v12
R_ v13,a217
U_ v13,v13,v11
X_ a217,v13
R_ v12,a218
U_ v12,v12,v11
X_ a218,v12
R_ v13,a219
U_ v13,v13,v11
X_ a219,v13
R_ v12,a236
U_ v12,v12,v11
X_ a236,v12
R_ v13,a237
U_ v13,v13,v11
X_ a237,v13
R_ v12,a238
U_ v12,v12,v11
X_ a238,v12
R_ v13,a239
U_ v13,v13,v11
X_ a239,v13
B_ v72,v72,v10
B_ v73,v73,v10
B_ v74,v74,v10
B_ v75,v75,v10
B_ v92,v92,v10
B_ v93,v93,v10
B_ v94,v94,v10
B_ v95,v95,v10
B_ v112,v112,v10
B_ v113,v113,v10
B_ v114,v114,v10
B_ v115,v115,v10
E_ 92
E_ 93
E_ 94
E_ 95
E_ 112
E_ 113
E_ 114
E_ 115
s_nop 0
C_ 115,114,115
C_ 114,112,113
C_ 112,92,93
C_ 113,94,95
v_mov_b32 v92, 0x3f803f80
v_mov_b32 v93, 0x3f803f80
v_mov_b32 v94, 0x3f803f80
v_mov_b32 v95, 0x3f803f80
s_nop 7
s_lshl_b32 s53, s58, 4
S_ s53,s53,s63
s_cmp_eq_u32 s53, 2
s_cbranch_scc1 .Lstep0_3
s_cmp_eq_u32 s53, 14
s_cbranch_scc1 .Lstep0_15
s_cmp_eq_u32 s53, 18
s_cbranch_scc1 .Lstep1_3
s_cmp_eq_u32 s53, 20
s_cbranch_scc1 .Lstep1_5
s_cmp_eq_u32 s53, 22
s_cbranch_scc1 .Lstep1_7
s_cmp_eq_u32 s53, 24
s_cbranch_scc1 .Lstep1_9
s_cmp_eq_u32 s53, 26
s_cbranch_scc1 .Lstep1_11
s_cmp_eq_u32 s53, 28
s_cbranch_scc1 .Lstep1_13
s_cmp_eq_u32 s53, 30
s_cbranch_scc1 .Lstep1_15
s_cmp_eq_u32 s53, 34
s_cbranch_scc1 .Lstep2_3
s_cmp_eq_u32 s53, 36
s_cbranch_scc1 .Lstep2_5
s_cmp_eq_u32 s53, 38
s_cbranch_scc1 .Lstep2_7
s_cmp_eq_u32 s53, 40
s_cbranch_scc1 .Lstep2_9
s_cmp_eq_u32 s53, 42
s_cbranch_scc1 .Lstep2_11
s_cmp_eq_u32 s53, 44
s_cbranch_scc1 .Lstep2_13
s_cmp_eq_u32 s53, 46
s_cbranch_scc1 .Lstep2_15
s_endpgm
.Lslow1:
W_ 0
s_nop 7
s_nop 7
s_and_b32 s64, s61, 4294959104
s_mul_i32 s53, s63, 12288
S_ s64,s64,s53
R_ v11,a248
v_and_b32 v10, 0xf0, v11
v_mul_u32_u24 v10, 24, v10
v_lshrrev_b32 v11, 8, v11
v_lshl_add_u32 v10, v11, 4, v10
S_ s53,s64,6144
buffer_load_dwordx4 v[96:99], v10, s[32:35], s53 offen offset:0
S_ s53,s64,6144
buffer_load_dwordx4 v[100:103], v10, s[32:35], s53 offen offset:64
S_ s53,s64,6144
buffer_load_dwordx4 v[104:107], v10, s[32:35], s53 offen offset:128
S_ s53,s64,6144
buffer_load_dwordx4 v[108:111], v10, s[32:35], s53 offen offset:192
s_waitcnt vmcnt(0)
Nvvv 116,96,136,56
Nvvv 120,96,140,60
Nvvv 124,96,144,64
Nvvv 128,96,148,68
Nvvv 132,96,152,72
Mvvv 116,100,156
Mvvv 120,100,160
Mvvv 124,100,164
Mvvv 128,100,168
Mvvv 132,100,172
Mvvv 116,104,176
Mvvv 120,104,180
Mvvv 124,104,184
Mvvv 128,104,188
Mvvv 132,104,192
Mvvv 116,108,196
Mvvv 120,108,200
Mvvv 124,108,204
Mvvv 128,108,208
Mvvv 132,108,212
s_nop 7
S_ s53,s64,6144
buffer_load_dwordx4 v[96:99], v10, s[32:35], s53 offen offset:256
S_ s53,s64,6144
buffer_load_dwordx4 v[100:103], v10, s[32:35], s53 offen offset:320
S_ s53,s64,0
buffer_load_dwordx4 v[104:107], v10, s[32:35], s53 offen offset:0
S_ s53,s64,0
buffer_load_dwordx4 v[108:111], v10, s[32:35], s53 offen offset:64
s_waitcnt vmcnt(0)
Mvvv 116,96,216
Mvvv 120,96,220
Mvvv 124,96,224
Mvvv 128,96,228
Mvvv 132,96,232
Mvvv 116,100,236
Mvvv 120,100,240
Mvvv 124,100,244
Mvvv 128,100,248
Mvvv 132,100,252
Nvvv 76,104,136,56
Nvvv 80,104,140,60
Nvvv 84,104,144,64
Nvvv 88,104,148,68
Nvvv 92,104,152,72
Mvvv 76,108,156
Mvvv 80,108,160
Mvvv 84,108,164
Mvvv 88,108,168
Mvvv 92,108,172
s_nop 7
S_ s53,s64,0
buffer_load_dwordx4 v[96:99], v10, s[32:35], s53 offen offset:128
S_ s53,s64,0
buffer_load_dwordx4 v[100:103], v10, s[32:35], s53 offen offset:192
S_ s53,s64,0
buffer_load_dwordx4 v[104:107], v10, s[32:35], s53 offen offset:256
S_ s53,s64,0
buffer_load_dwordx4 v[108:111], v10, s[32:35], s53 offen offset:320
s_waitcnt vmcnt(0)
Mvvv 76,96,176
Mvvv 80,96,180
Mvvv 84,96,184
Mvvv 88,96,188
Mvvv 92,96,192
Mvvv 76,100,196
Mvvv 80,100,200
Mvvv 84,100,204
Mvvv 88,100,208
Mvvv 92,100,212
Mvvv 76,104,216
Mvvv 80,104,220
Mvvv 84,104,224
Mvvv 88,104,228
Mvvv 92,104,232
Mvvv 76,108,236
Mvvv 80,108,240
Mvvv 84,108,244
Mvvv 88,108,248
Mvvv 92,108,252
s_nop 7
s_nop 2
v_max3_f32 v10, v76, v77, v78
v_max3_f32 v10, v10, v79, v116
v_max3_f32 v10, v10, v117, v118
v_max_f32 v10, v10, v119
v_mov_b32 v11, v10
s_nop 1
v_permlane16_swap_b32 v10, v11
v_max_f32 v10, v10, v11
v_mov_b32 v11, v10
s_nop 1
v_permlane32_swap_b32 v10, v11
v_max_f32 v10, v10, v11
A_ v10,0x40e00000,v10
v_max_f32 v10, 0, v10
v_exp_f32 v11, -v10
s_nop 0
U_ v46,v46,v11
R_ v12,a0
U_ v12,v12,v11
X_ a0,v12
R_ v13,a1
U_ v13,v13,v11
X_ a1,v13
R_ v12,a2
U_ v12,v12,v11
X_ a2,v12
R_ v13,a3
U_ v13,v13,v11
X_ a3,v13
R_ v12,a20
U_ v12,v12,v11
X_ a20,v12
R_ v13,a21
U_ v13,v13,v11
X_ a21,v13
R_ v12,a22
U_ v12,v12,v11
X_ a22,v12
R_ v13,a23
U_ v13,v13,v11
X_ a23,v13
R_ v12,a40
U_ v12,v12,v11
X_ a40,v12
R_ v13,a41
U_ v13,v13,v11
X_ a41,v13
R_ v12,a42
U_ v12,v12,v11
X_ a42,v12
R_ v13,a43
U_ v13,v13,v11
X_ a43,v13
R_ v12,a60
U_ v12,v12,v11
X_ a60,v12
R_ v13,a61
U_ v13,v13,v11
X_ a61,v13
R_ v12,a62
U_ v12,v12,v11
X_ a62,v12
R_ v13,a63
U_ v13,v13,v11
X_ a63,v13
R_ v12,a80
U_ v12,v12,v11
X_ a80,v12
R_ v13,a81
U_ v13,v13,v11
X_ a81,v13
R_ v12,a82
U_ v12,v12,v11
X_ a82,v12
R_ v13,a83
U_ v13,v13,v11
X_ a83,v13
R_ v12,a100
U_ v12,v12,v11
X_ a100,v12
R_ v13,a101
U_ v13,v13,v11
X_ a101,v13
R_ v12,a102
U_ v12,v12,v11
X_ a102,v12
R_ v13,a103
U_ v13,v13,v11
X_ a103,v13
R_ v12,a120
U_ v12,v12,v11
X_ a120,v12
R_ v13,a121
U_ v13,v13,v11
X_ a121,v13
R_ v12,a122
U_ v12,v12,v11
X_ a122,v12
R_ v13,a123
U_ v13,v13,v11
X_ a123,v13
R_ v12,a140
U_ v12,v12,v11
X_ a140,v12
R_ v13,a141
U_ v13,v13,v11
X_ a141,v13
R_ v12,a142
U_ v12,v12,v11
X_ a142,v12
R_ v13,a143
U_ v13,v13,v11
X_ a143,v13
R_ v12,a160
U_ v12,v12,v11
X_ a160,v12
R_ v13,a161
U_ v13,v13,v11
X_ a161,v13
R_ v12,a162
U_ v12,v12,v11
X_ a162,v12
R_ v13,a163
U_ v13,v13,v11
X_ a163,v13
R_ v12,a180
U_ v12,v12,v11
X_ a180,v12
R_ v13,a181
U_ v13,v13,v11
X_ a181,v13
R_ v12,a182
U_ v12,v12,v11
X_ a182,v12
R_ v13,a183
U_ v13,v13,v11
X_ a183,v13
R_ v12,a200
U_ v12,v12,v11
X_ a200,v12
R_ v13,a201
U_ v13,v13,v11
X_ a201,v13
R_ v12,a202
U_ v12,v12,v11
X_ a202,v12
R_ v13,a203
U_ v13,v13,v11
X_ a203,v13
R_ v12,a220
U_ v12,v12,v11
X_ a220,v12
R_ v13,a221
U_ v13,v13,v11
X_ a221,v13
R_ v12,a222
U_ v12,v12,v11
X_ a222,v12
R_ v13,a223
U_ v13,v13,v11
X_ a223,v13
B_ v56,v56,v10
B_ v57,v57,v10
B_ v58,v58,v10
B_ v59,v59,v10
B_ v76,v76,v10
B_ v77,v77,v10
B_ v78,v78,v10
B_ v79,v79,v10
B_ v116,v116,v10
B_ v117,v117,v10
B_ v118,v118,v10
B_ v119,v119,v10
E_ 76
E_ 77
E_ 78
E_ 79
E_ 116
E_ 117
E_ 118
E_ 119
s_nop 0
C_ 119,118,119
C_ 118,116,117
C_ 116,76,77
C_ 117,78,79
v_max3_f32 v10, v80, v81, v82
v_max3_f32 v10, v10, v83, v120
v_max3_f32 v10, v10, v121, v122
v_max_f32 v10, v10, v123
v_mov_b32 v11, v10
s_nop 1
v_permlane16_swap_b32 v10, v11
v_max_f32 v10, v10, v11
v_mov_b32 v11, v10
s_nop 1
v_permlane32_swap_b32 v10, v11
v_max_f32 v10, v10, v11
A_ v10,0x40e00000,v10
v_max_f32 v10, 0, v10
v_exp_f32 v11, -v10
s_nop 0
U_ v47,v47,v11
R_ v12,a4
U_ v12,v12,v11
X_ a4,v12
R_ v13,a5
U_ v13,v13,v11
X_ a5,v13
R_ v12,a6
U_ v12,v12,v11
X_ a6,v12
R_ v13,a7
U_ v13,v13,v11
X_ a7,v13
R_ v12,a24
U_ v12,v12,v11
X_ a24,v12
R_ v13,a25
U_ v13,v13,v11
X_ a25,v13
R_ v12,a26
U_ v12,v12,v11
X_ a26,v12
R_ v13,a27
U_ v13,v13,v11
X_ a27,v13
R_ v12,a44
U_ v12,v12,v11
X_ a44,v12
R_ v13,a45
U_ v13,v13,v11
X_ a45,v13
R_ v12,a46
U_ v12,v12,v11
X_ a46,v12
R_ v13,a47
U_ v13,v13,v11
X_ a47,v13
R_ v12,a64
U_ v12,v12,v11
X_ a64,v12
R_ v13,a65
U_ v13,v13,v11
X_ a65,v13
R_ v12,a66
U_ v12,v12,v11
X_ a66,v12
R_ v13,a67
U_ v13,v13,v11
X_ a67,v13
R_ v12,a84
U_ v12,v12,v11
X_ a84,v12
R_ v13,a85
U_ v13,v13,v11
X_ a85,v13
R_ v12,a86
U_ v12,v12,v11
X_ a86,v12
R_ v13,a87
U_ v13,v13,v11
X_ a87,v13
R_ v12,a104
U_ v12,v12,v11
X_ a104,v12
R_ v13,a105
U_ v13,v13,v11
X_ a105,v13
R_ v12,a106
U_ v12,v12,v11
X_ a106,v12
R_ v13,a107
U_ v13,v13,v11
X_ a107,v13
R_ v12,a124
U_ v12,v12,v11
X_ a124,v12
R_ v13,a125
U_ v13,v13,v11
X_ a125,v13
R_ v12,a126
U_ v12,v12,v11
X_ a126,v12
R_ v13,a127
U_ v13,v13,v11
X_ a127,v13
R_ v12,a144
U_ v12,v12,v11
X_ a144,v12
R_ v13,a145
U_ v13,v13,v11
X_ a145,v13
R_ v12,a146
U_ v12,v12,v11
X_ a146,v12
R_ v13,a147
U_ v13,v13,v11
X_ a147,v13
R_ v12,a164
U_ v12,v12,v11
X_ a164,v12
R_ v13,a165
U_ v13,v13,v11
X_ a165,v13
R_ v12,a166
U_ v12,v12,v11
X_ a166,v12
R_ v13,a167
U_ v13,v13,v11
X_ a167,v13
R_ v12,a184
U_ v12,v12,v11
X_ a184,v12
R_ v13,a185
U_ v13,v13,v11
X_ a185,v13
R_ v12,a186
U_ v12,v12,v11
X_ a186,v12
R_ v13,a187
U_ v13,v13,v11
X_ a187,v13
R_ v12,a204
U_ v12,v12,v11
X_ a204,v12
R_ v13,a205
U_ v13,v13,v11
X_ a205,v13
R_ v12,a206
U_ v12,v12,v11
X_ a206,v12
R_ v13,a207
U_ v13,v13,v11
X_ a207,v13
R_ v12,a224
U_ v12,v12,v11
X_ a224,v12
R_ v13,a225
U_ v13,v13,v11
X_ a225,v13
R_ v12,a226
U_ v12,v12,v11
X_ a226,v12
R_ v13,a227
U_ v13,v13,v11
X_ a227,v13
B_ v60,v60,v10
B_ v61,v61,v10
B_ v62,v62,v10
B_ v63,v63,v10
B_ v80,v80,v10
B_ v81,v81,v10
B_ v82,v82,v10
B_ v83,v83,v10
B_ v120,v120,v10
B_ v121,v121,v10
B_ v122,v122,v10
B_ v123,v123,v10
E_ 80
E_ 81
E_ 82
E_ 83
E_ 120
E_ 121
E_ 122
E_ 123
s_nop 0
C_ 123,122,123
C_ 122,120,121
C_ 120,80,81
C_ 121,82,83
v_max3_f32 v10, v84, v85, v86
v_max3_f32 v10, v10, v87, v124
v_max3_f32 v10, v10, v125, v126
v_max_f32 v10, v10, v127
v_mov_b32 v11, v10
s_nop 1
v_permlane16_swap_b32 v10, v11
v_max_f32 v10, v10, v11
v_mov_b32 v11, v10
s_nop 1
v_permlane32_swap_b32 v10, v11
v_max_f32 v10, v10, v11
A_ v10,0x40e00000,v10
v_max_f32 v10, 0, v10
v_exp_f32 v11, -v10
s_nop 0
U_ v48,v48,v11
R_ v12,a8
U_ v12,v12,v11
X_ a8,v12
R_ v13,a9
U_ v13,v13,v11
X_ a9,v13
R_ v12,a10
U_ v12,v12,v11
X_ a10,v12
R_ v13,a11
U_ v13,v13,v11
X_ a11,v13
R_ v12,a28
U_ v12,v12,v11
X_ a28,v12
R_ v13,a29
U_ v13,v13,v11
X_ a29,v13
R_ v12,a30
U_ v12,v12,v11
X_ a30,v12
R_ v13,a31
U_ v13,v13,v11
X_ a31,v13
R_ v12,a48
U_ v12,v12,v11
X_ a48,v12
R_ v13,a49
U_ v13,v13,v11
X_ a49,v13
R_ v12,a50
U_ v12,v12,v11
X_ a50,v12
R_ v13,a51
U_ v13,v13,v11
X_ a51,v13
R_ v12,a68
U_ v12,v12,v11
X_ a68,v12
R_ v13,a69
U_ v13,v13,v11
X_ a69,v13
R_ v12,a70
U_ v12,v12,v11
X_ a70,v12
R_ v13,a71
U_ v13,v13,v11
X_ a71,v13
R_ v12,a88
U_ v12,v12,v11
X_ a88,v12
R_ v13,a89
U_ v13,v13,v11
X_ a89,v13
R_ v12,a90
U_ v12,v12,v11
X_ a90,v12
R_ v13,a91
U_ v13,v13,v11
X_ a91,v13
R_ v12,a108
U_ v12,v12,v11
X_ a108,v12
R_ v13,a109
U_ v13,v13,v11
X_ a109,v13
R_ v12,a110
U_ v12,v12,v11
X_ a110,v12
R_ v13,a111
U_ v13,v13,v11
X_ a111,v13
R_ v12,a128
U_ v12,v12,v11
X_ a128,v12
R_ v13,a129
U_ v13,v13,v11
X_ a129,v13
R_ v12,a130
U_ v12,v12,v11
X_ a130,v12
R_ v13,a131
U_ v13,v13,v11
X_ a131,v13
R_ v12,a148
U_ v12,v12,v11
X_ a148,v12
R_ v13,a149
U_ v13,v13,v11
X_ a149,v13
R_ v12,a150
U_ v12,v12,v11
X_ a150,v12
R_ v13,a151
U_ v13,v13,v11
X_ a151,v13
R_ v12,a168
U_ v12,v12,v11
X_ a168,v12
R_ v13,a169
U_ v13,v13,v11
X_ a169,v13
R_ v12,a170
U_ v12,v12,v11
X_ a170,v12
R_ v13,a171
U_ v13,v13,v11
X_ a171,v13
R_ v12,a188
U_ v12,v12,v11
X_ a188,v12
R_ v13,a189
U_ v13,v13,v11
X_ a189,v13
R_ v12,a190
U_ v12,v12,v11
X_ a190,v12
R_ v13,a191
U_ v13,v13,v11
X_ a191,v13
R_ v12,a208
U_ v12,v12,v11
X_ a208,v12
R_ v13,a209
U_ v13,v13,v11
X_ a209,v13
R_ v12,a210
U_ v12,v12,v11
X_ a210,v12
R_ v13,a211
U_ v13,v13,v11
X_ a211,v13
R_ v12,a228
U_ v12,v12,v11
X_ a228,v12
R_ v13,a229
U_ v13,v13,v11
X_ a229,v13
R_ v12,a230
U_ v12,v12,v11
X_ a230,v12
R_ v13,a231
U_ v13,v13,v11
X_ a231,v13
B_ v64,v64,v10
B_ v65,v65,v10
B_ v66,v66,v10
B_ v67,v67,v10
B_ v84,v84,v10
B_ v85,v85,v10
B_ v86,v86,v10
B_ v87,v87,v10
B_ v124,v124,v10
B_ v125,v125,v10
B_ v126,v126,v10
B_ v127,v127,v10
E_ 84
E_ 85
E_ 86
E_ 87
E_ 124
E_ 125
E_ 126
E_ 127
s_nop 0
C_ 127,126,127
C_ 126,124,125
C_ 124,84,85
C_ 125,86,87
v_max3_f32 v10, v88, v89, v90
v_max3_f32 v10, v10, v91, v128
v_max3_f32 v10, v10, v129, v130
v_max_f32 v10, v10, v131
v_mov_b32 v11, v10
s_nop 1
v_permlane16_swap_b32 v10, v11
v_max_f32 v10, v10, v11
v_mov_b32 v11, v10
s_nop 1
v_permlane32_swap_b32 v10, v11
v_max_f32 v10, v10, v11
A_ v10,0x40e00000,v10
v_max_f32 v10, 0, v10
v_exp_f32 v11, -v10
s_nop 0
U_ v49,v49,v11
R_ v12,a12
U_ v12,v12,v11
X_ a12,v12
R_ v13,a13
U_ v13,v13,v11
X_ a13,v13
R_ v12,a14
U_ v12,v12,v11
X_ a14,v12
R_ v13,a15
U_ v13,v13,v11
X_ a15,v13
R_ v12,a32
U_ v12,v12,v11
X_ a32,v12
R_ v13,a33
U_ v13,v13,v11
X_ a33,v13
R_ v12,a34
U_ v12,v12,v11
X_ a34,v12
R_ v13,a35
U_ v13,v13,v11
X_ a35,v13
R_ v12,a52
U_ v12,v12,v11
X_ a52,v12
R_ v13,a53
U_ v13,v13,v11
X_ a53,v13
R_ v12,a54
U_ v12,v12,v11
X_ a54,v12
R_ v13,a55
U_ v13,v13,v11
X_ a55,v13
R_ v12,a72
U_ v12,v12,v11
X_ a72,v12
R_ v13,a73
U_ v13,v13,v11
X_ a73,v13
R_ v12,a74
U_ v12,v12,v11
X_ a74,v12
R_ v13,a75
U_ v13,v13,v11
X_ a75,v13
R_ v12,a92
U_ v12,v12,v11
X_ a92,v12
R_ v13,a93
U_ v13,v13,v11
X_ a93,v13
R_ v12,a94
U_ v12,v12,v11
X_ a94,v12
R_ v13,a95
U_ v13,v13,v11
X_ a95,v13
R_ v12,a112
U_ v12,v12,v11
X_ a112,v12
R_ v13,a113
U_ v13,v13,v11
X_ a113,v13
R_ v12,a114
U_ v12,v12,v11
X_ a114,v12
R_ v13,a115
U_ v13,v13,v11
X_ a115,v13
R_ v12,a132
U_ v12,v12,v11
X_ a132,v12
R_ v13,a133
U_ v13,v13,v11
X_ a133,v13
R_ v12,a134
U_ v12,v12,v11
X_ a134,v12
R_ v13,a135
U_ v13,v13,v11
X_ a135,v13
R_ v12,a152
U_ v12,v12,v11
X_ a152,v12
R_ v13,a153
U_ v13,v13,v11
X_ a153,v13
R_ v12,a154
U_ v12,v12,v11
X_ a154,v12
R_ v13,a155
U_ v13,v13,v11
X_ a155,v13
R_ v12,a172
U_ v12,v12,v11
X_ a172,v12
R_ v13,a173
U_ v13,v13,v11
X_ a173,v13
R_ v12,a174
U_ v12,v12,v11
X_ a174,v12
R_ v13,a175
U_ v13,v13,v11
X_ a175,v13
R_ v12,a192
U_ v12,v12,v11
X_ a192,v12
R_ v13,a193
U_ v13,v13,v11
X_ a193,v13
R_ v12,a194
U_ v12,v12,v11
X_ a194,v12
R_ v13,a195
U_ v13,v13,v11
X_ a195,v13
R_ v12,a212
U_ v12,v12,v11
X_ a212,v12
R_ v13,a213
U_ v13,v13,v11
X_ a213,v13
R_ v12,a214
U_ v12,v12,v11
X_ a214,v12
R_ v13,a215
U_ v13,v13,v11
X_ a215,v13
R_ v12,a232
U_ v12,v12,v11
X_ a232,v12
R_ v13,a233
U_ v13,v13,v11
X_ a233,v13
R_ v12,a234
U_ v12,v12,v11
X_ a234,v12
R_ v13,a235
U_ v13,v13,v11
X_ a235,v13
B_ v68,v68,v10
B_ v69,v69,v10
B_ v70,v70,v10
B_ v71,v71,v10
B_ v88,v88,v10
B_ v89,v89,v10
B_ v90,v90,v10
B_ v91,v91,v10
B_ v128,v128,v10
B_ v129,v129,v10
B_ v130,v130,v10
B_ v131,v131,v10
E_ 88
E_ 89
E_ 90
E_ 91
E_ 128
E_ 129
E_ 130
E_ 131
s_nop 0
C_ 131,130,131
C_ 130,128,129
C_ 128,88,89
C_ 129,90,91
v_max3_f32 v10, v92, v93, v94
v_max3_f32 v10, v10, v95, v132
v_max3_f32 v10, v10, v133, v134
v_max_f32 v10, v10, v135
v_mov_b32 v11, v10
s_nop 1
v_permlane16_swap_b32 v10, v11
v_max_f32 v10, v10, v11
v_mov_b32 v11, v10
s_nop 1
v_permlane32_swap_b32 v10, v11
v_max_f32 v10, v10, v11
A_ v10,0x40e00000,v10
v_max_f32 v10, 0, v10
v_exp_f32 v11, -v10
s_nop 0
U_ v50,v50,v11
R_ v12,a16
U_ v12,v12,v11
X_ a16,v12
R_ v13,a17
U_ v13,v13,v11
X_ a17,v13
R_ v12,a18
U_ v12,v12,v11
X_ a18,v12
R_ v13,a19
U_ v13,v13,v11
X_ a19,v13
R_ v12,a36
U_ v12,v12,v11
X_ a36,v12
R_ v13,a37
U_ v13,v13,v11
X_ a37,v13
R_ v12,a38
U_ v12,v12,v11
X_ a38,v12
R_ v13,a39
U_ v13,v13,v11
X_ a39,v13
R_ v12,a56
U_ v12,v12,v11
X_ a56,v12
R_ v13,a57
U_ v13,v13,v11
X_ a57,v13
R_ v12,a58
U_ v12,v12,v11
X_ a58,v12
R_ v13,a59
U_ v13,v13,v11
X_ a59,v13
R_ v12,a76
U_ v12,v12,v11
X_ a76,v12
R_ v13,a77
U_ v13,v13,v11
X_ a77,v13
R_ v12,a78
U_ v12,v12,v11
X_ a78,v12
R_ v13,a79
U_ v13,v13,v11
X_ a79,v13
R_ v12,a96
U_ v12,v12,v11
X_ a96,v12
R_ v13,a97
U_ v13,v13,v11
X_ a97,v13
R_ v12,a98
U_ v12,v12,v11
X_ a98,v12
R_ v13,a99
U_ v13,v13,v11
X_ a99,v13
R_ v12,a116
U_ v12,v12,v11
X_ a116,v12
R_ v13,a117
U_ v13,v13,v11
X_ a117,v13
R_ v12,a118
U_ v12,v12,v11
X_ a118,v12
R_ v13,a119
U_ v13,v13,v11
X_ a119,v13
R_ v12,a136
U_ v12,v12,v11
X_ a136,v12
R_ v13,a137
U_ v13,v13,v11
X_ a137,v13
R_ v12,a138
U_ v12,v12,v11
X_ a138,v12
R_ v13,a139
U_ v13,v13,v11
X_ a139,v13
R_ v12,a156
U_ v12,v12,v11
X_ a156,v12
R_ v13,a157
U_ v13,v13,v11
X_ a157,v13
R_ v12,a158
U_ v12,v12,v11
X_ a158,v12
R_ v13,a159
U_ v13,v13,v11
X_ a159,v13
R_ v12,a176
U_ v12,v12,v11
X_ a176,v12
R_ v13,a177
U_ v13,v13,v11
X_ a177,v13
R_ v12,a178
U_ v12,v12,v11
X_ a178,v12
R_ v13,a179
U_ v13,v13,v11
X_ a179,v13
R_ v12,a196
U_ v12,v12,v11
X_ a196,v12
R_ v13,a197
U_ v13,v13,v11
X_ a197,v13
R_ v12,a198
U_ v12,v12,v11
X_ a198,v12
R_ v13,a199
U_ v13,v13,v11
X_ a199,v13
R_ v12,a216
U_ v12,v12,v11
X_ a216,v12
R_ v13,a217
U_ v13,v13,v11
X_ a217,v13
R_ v12,a218
U_ v12,v12,v11
X_ a218,v12
R_ v13,a219
U_ v13,v13,v11
X_ a219,v13
R_ v12,a236
U_ v12,v12,v11
X_ a236,v12
R_ v13,a237
U_ v13,v13,v11
X_ a237,v13
R_ v12,a238
U_ v12,v12,v11
X_ a238,v12
R_ v13,a239
U_ v13,v13,v11
X_ a239,v13
B_ v72,v72,v10
B_ v73,v73,v10
B_ v74,v74,v10
B_ v75,v75,v10
B_ v92,v92,v10
B_ v93,v93,v10
B_ v94,v94,v10
B_ v95,v95,v10
B_ v132,v132,v10
B_ v133,v133,v10
B_ v134,v134,v10
B_ v135,v135,v10
E_ 92
E_ 93
E_ 94
E_ 95
E_ 132
E_ 133
E_ 134
E_ 135
s_nop 0
C_ 135,134,135
C_ 134,132,133
C_ 132,92,93
C_ 133,94,95
v_mov_b32 v92, 0x3f803f80
v_mov_b32 v93, 0x3f803f80
v_mov_b32 v94, 0x3f803f80
v_mov_b32 v95, 0x3f803f80
s_nop 7
s_lshl_b32 s53, s58, 4
S_ s53,s53,s63
s_cmp_eq_u32 s53, 1
s_cbranch_scc1 .Lstep0_2
s_cmp_eq_u32 s53, 13
s_cbranch_scc1 .Lstep0_14
s_cmp_eq_u32 s53, 15
s_cbranch_scc1 .Lstep0_16
s_cmp_eq_u32 s53, 17
s_cbranch_scc1 .Lstep1_2
s_cmp_eq_u32 s53, 19
s_cbranch_scc1 .Lstep1_4
s_cmp_eq_u32 s53, 21
s_cbranch_scc1 .Lstep1_6
s_cmp_eq_u32 s53, 23
s_cbranch_scc1 .Lstep1_8
s_cmp_eq_u32 s53, 25
s_cbranch_scc1 .Lstep1_10
s_cmp_eq_u32 s53, 27
s_cbranch_scc1 .Lstep1_12
s_cmp_eq_u32 s53, 29
s_cbranch_scc1 .Lstep1_14
s_cmp_eq_u32 s53, 31
s_cbranch_scc1 .Lstep1_16
s_cmp_eq_u32 s53, 33
s_cbranch_scc1 .Lstep2_2
s_cmp_eq_u32 s53, 35
s_cbranch_scc1 .Lstep2_4
s_cmp_eq_u32 s53, 37
s_cbranch_scc1 .Lstep2_6
s_cmp_eq_u32 s53, 39
s_cbranch_scc1 .Lstep2_8
s_cmp_eq_u32 s53, 41
s_cbranch_scc1 .Lstep2_10
s_cmp_eq_u32 s53, 43
s_cbranch_scc1 .Lstep2_12
s_cmp_eq_u32 s53, 45
s_cbranch_scc1 .Lstep2_14
s_cmp_eq_u32 s53, 47
s_cbranch_scc1 .Lstep2_16
s_endpgm
.Lxs2m:
s_sleep 5
s_branch .Lxs2m_back
.Lxs2e:
s_sleep 5
s_branch .Lxs2e_back
.Lxs3m:
s_sleep 5
s_branch .Lxs3m_back
.Lxs3e:
s_sleep 5
s_branch .Lxs3e_back
.Lxs14m:
s_sleep 7
s_branch .Lxs14m_back
.Lxs14e:
s_sleep 7
s_branch .Lxs14e_back
.Lxs15m:
s_sleep 7
s_branch .Lxs15m_back
.Lxs15e:
s_sleep 7
s_branch .Lxs15e_back
.Lpace1:
S_ s12,s12,s13
.Lpace1_l:
s_memrealtime s[100:101]
W_ 0
s_sub_u32 s100, s100, s98
s_cmp_ge_u32 s100, s12
s_cbranch_scc1 .Lpace1_back
s_sleep 2
s_branch .Lpace1_l
.Lpace3:
S_ s12,s12,s13
.Lpace3_l:
s_memrealtime s[100:101]
W_ 0
s_sub_u32 s100, s100, s98
s_cmp_ge_u32 s100, s12
s_cbranch_scc1 .Lpace3_back
s_sleep 2
s_branch .Lpace3_l
.Lpace5:
S_ s12,s12,s13
.Lpace5_l:
s_memrealtime s[100:101]
W_ 0
s_sub_u32 s100, s100, s98
s_cmp_ge_u32 s100, s12
s_cbranch_scc1 .Lpace5_back
s_sleep 2
s_branch .Lpace5_l
.Lpace7:
S_ s12,s12,s13
.Lpace7_l:
s_memrealtime s[100:101]
W_ 0
s_sub_u32 s100, s100, s98
s_cmp_ge_u32 s100, s12
s_cbranch_scc1 .Lpace7_back
s_sleep 2
s_branch .Lpace7_l
.Lpace9:
S_ s12,s12,s13
.Lpace9_l:
s_memrealtime s[100:101]
W_ 0
s_sub_u32 s100, s100, s98
s_cmp_ge_u32 s100, s12
s_cbranch_scc1 .Lpace9_back
s_sleep 2
s_branch .Lpace9_l
.Lpace11:
S_ s12,s12,s13
.Lpace11_l:
s_memrealtime s[100:101]
W_ 0
s_sub_u32 s100, s100, s98
s_cmp_ge_u32 s100, s12
s_cbranch_scc1 .Lpace11_back
s_sleep 2
s_branch .Lpace11_l
.Lpace13:
S_ s12,s12,s13
.Lpace13_l:
s_memrealtime s[100:101]
W_ 0
s_sub_u32 s100, s100, s98
s_cmp_ge_u32 s100, s12
s_cbranch_scc1 .Lpace13_back
s_sleep 2
s_branch .Lpace13_l
.Lpace15:
S_ s12,s12,s13
.Lpace15_l:
s_memrealtime s[100:101]
W_ 0
s_sub_u32 s100, s100, s98
s_cmp_ge_u32 s100, s12
s_cbranch_scc1 .Lpace15_back
s_sleep 2
s_branch .Lpace15_l
.Lpace17:
S_ s12,s12,s13
.Lpace17_l:
s_memrealtime s[100:101]
W_ 0
s_sub_u32 s100, s100, s98
s_cmp_ge_u32 s100, s12
s_cbranch_scc1 .Lpace17_back
s_sleep 2
s_branch .Lpace17_l
.Lpace19:
S_ s12,s12,s13
.Lpace19_l:
s_memrealtime s[100:101]
W_ 0
s_sub_u32 s100, s100, s98
s_cmp_ge_u32 s100, s12
s_cbranch_scc1 .Lpace19_back
s_sleep 2
s_branch .Lpace19_l
.Lpace21:
S_ s12,s12,s13
.Lpace21_l:
s_memrealtime s[100:101]
W_ 0
s_sub_u32 s100, s100, s98
s_cmp_ge_u32 s100, s12
s_cbranch_scc1 .Lpace21_back
s_sleep 2
s_branch .Lpace21_l
.Lpace23:
S_ s12,s12,s13
.Lpace23_l:
s_memrealtime s[100:101]
W_ 0
s_sub_u32 s100, s100, s98
s_cmp_ge_u32 s100, s12
s_cbranch_scc1 .Lpace23_back
s_sleep 2
s_branch .Lpace23_l
.Lpace25:
S_ s12,s12,s13
.Lpace25_l:
s_memrealtime s[100:101]
W_ 0
s_sub_u32 s100, s100, s98
s_cmp_ge_u32 s100, s12
s_cbranch_scc1 .Lpace25_back
s_sleep 2
s_branch .Lpace25_l
.Lpace27:
S_ s12,s12,s13
.Lpace27_l:
s_memrealtime s[100:101]
W_ 0
s_sub_u32 s100, s100, s98
s_cmp_ge_u32 s100, s12
s_cbranch_scc1 .Lpace27_back
s_sleep 2
s_branch .Lpace27_l
.Lpace29:
S_ s12,s12,s13
.Lpace29_l:
s_memrealtime s[100:101]
W_ 0
s_sub_u32 s100, s100, s98
s_cmp_ge_u32 s100, s12
s_cbranch_scc1 .Lpace29_back
s_sleep 2
s_branch .Lpace29_l
.Lhsd0_0:
s_bitcmp1_b32 s67, 13
s_cbranch_scc1 .Lhs_a0
s_bitcmp1_b32 s67, 11
s_cbranch_scc1 .Lhs_c0
s_branch .Lhs_d0
.Lhs_a0:
s_nop 4
s_waitcnt vmcnt(12)
R_ v10,a251
buffer_store_dword a255, v10, s[92:95], 0 offen offset:128 sc1
s_branch .Lhsd0_0_back
.Lhs_a1:
s_nop 4
R_ v11,a248
v_lshrrev_b32 v11, 2, v11
buffer_load_dword a254, v11, s[92:95], s96 offen offset:128 sc1
s_branch .Lhs_a1_back
.Lhs_a2:
s_nop 4
s_waitcnt vmcnt(12)
R_ v10,a251
buffer_store_dword a255, v10, s[92:95], 0 offen offset:128 sc1
s_branch .Lhs_a2_back
.Lhs_b1:
s_nop 4
s_waitcnt vmcnt(12)
R_ v11,a254
v_cmp_eq_u32 vcc, s62, v11
R_ v10,a251
v_mov_b32 v11, 0x7fff0000
v_cndmask_b32 v10, v11, v10, vcc
R_ v11,a253
v_add_u32 v11, 1, v11
buffer_store_dword v11, v10, s[92:95], 0 offen offset:256 sc1
s_and_b32 s53, vcc_lo, 255
s_cmp_eq_u32 s53, 0xFF
s_cselect_b32 s97, 1, 0
s_branch .Lhs_b1_back
.Lhs_c0:
s_nop 4
R_ v11,a248
v_lshrrev_b32 v11, 2, v11
buffer_load_dword a254, v11, s[92:95], s96 offen offset:128 sc1
s_branch .Lhsd0_0_back
.Lhs_c1:
s_nop 4
R_ v11,a248
v_lshrrev_b32 v11, 2, v11
buffer_load_dword a254, v11, s[92:95], s96 offen offset:256 sc1
s_branch .Lhs_c1_back
.Lhs_c2:
s_nop 4
R_ v11,a248
v_lshrrev_b32 v11, 2, v11
buffer_load_dword a254, v11, s[92:95], s96 offen offset:128 sc1
s_branch .Lhs_c2_back
.Lhs_d0:
s_nop 4
s_waitcnt vmcnt(12)
R_ v11,a254
v_cmp_eq_u32 vcc, s62, v11
R_ v10,a251
v_mov_b32 v11, 0x7fff0000
v_cndmask_b32 v10, v11, v10, vcc
R_ v11,a253
v_add_u32 v11, 1, v11
buffer_store_dword v11, v10, s[92:95], 0 offen offset:256 sc1
s_and_b32 s53, vcc_lo, 255
s_cmp_eq_u32 s53, 0xFF
s_cselect_b32 s97, 1, 0
s_branch .Lhsd0_0_back
.Lhs_d2:
s_nop 4
s_waitcnt vmcnt(12)
R_ v11,a254
v_cmp_eq_u32 vcc, s62, v11
R_ v10,a251
v_mov_b32 v11, 0x7fff0000
v_cndmask_b32 v10, v11, v10, vcc
R_ v11,a253
v_add_u32 v11, 1, v11
buffer_store_dword v11, v10, s[92:95], 0 offen offset:256 sc1
s_and_b32 s53, vcc_lo, 255
s_cmp_eq_u32 s53, 0xFF
s_cselect_b32 s97, 1, 0
s_branch .Lhs_d2_back
.Lhs_d1:
s_nop 4
s_waitcnt vmcnt(12)
R_ v11,a254
R_ v10,a252
v_cmp_ne_u32 vcc, v11, v10
s_nop 1
s_and_b32 s53, vcc_lo, 255
s_cmp_eq_u32 s53, 0xFF
s_cselect_b32 s53, s97, 0
s_cmp_eq_u32 s53, 1
s_cbranch_scc1 .Lhs_ok
.Lhs_spin:
s_cmp_eq_u32 s57, 0
s_cbranch_scc1 .Lhs_ok
s_sub_u32 s57, s57, 1
s_sleep 8
R_ v10,a251
s_lshl_b32 s53, s6, 2
v_subrev_u32 v10, s53, v10
buffer_load_dword v11, v10, s[92:95], 0 offen offset:0 sc1
R_ v10,a248
v_lshrrev_b32 v10, 2, v10
buffer_load_dword v12, v10, s[92:95], s96 offen offset:128 sc1
buffer_load_dword v13, v10, s[92:95], s96 offen offset:256 sc1
s_waitcnt vmcnt(0)
X_ a255,v11
R_ v10,a251
buffer_store_dword v11, v10, s[92:95], 0 offen offset:128 sc1
s_nop 1
v_cmp_eq_u32 vcc, s62, v12
R_ v10,a251
v_mov_b32 v12, 0x7fff0000
v_cndmask_b32 v10, v12, v10, vcc
R_ v12,a253
v_add_u32 v12, 1, v12
buffer_store_dword v12, v10, s[92:95], 0 offen offset:256 sc1
s_and_b32 s53, vcc_lo, 255
s_cmp_eq_u32 s53, 0xFF
s_cselect_b32 s97, 1, 0
s_nop 1
R_ v10,a252
v_cmp_ne_u32 vcc, v13, v10
s_nop 1
s_and_b32 s53, vcc_lo, 255
s_cmp_eq_u32 s53, 0xFF
s_cselect_b32 s53, s97, 0
s_cmp_eq_u32 s53, 1
s_cbranch_scc0 .Lhs_spin
.Lhs_ok:
s_branch .Lhs_d1_back
.Latt2:
R_ v13,a248
S_ s53,s51,36864
v_add_u32 v12, s53, v13
s_mul_i32 s53, s4, 10240
S_ s53,s53,0
v_add_u32 v13, s53, v13
s_cmp_eq_u32 s4, 0
s_cbranch_scc0 .Lhs_tok2
R_ v11,a241
buffer_load_dword a255, v11, s[92:95], 0 offen offset:0 sc1
.Lhs_tok2:
W_ 2
Zvvv 96,30,136
Dv 42,v1,44032
R_ v2,a245
Zvvv 100,30,140
ds_write_b128 v13, a[0:3] offset:0
Zvvv 104,30,144
v_xor_b32 v3, 32, v2
v_xor_b32 v4, 64, v2
Zvvv 108,30,148
ds_write_b128 v13, a[4:7] offset:1024
Zvvv 112,30,152
W_ 4
v_xor_b32 v5, 96, v2
s_mov_b64 s[24:25], s[16:17]
Mvvv 96,34,156
s_mov_b32 s26, 7864320
Dv 30,%0,45056
s_mov_b64 s[28:29], s[22:23]
Mvvv 100,34,160
ds_write_b128 v13, a[8:11] offset:2048
Mvvv 104,34,164
s_mov_b32 s30, 1638400
R_ v20,a80
s_mul_i32 s52, s4, 10240
Mvvv 108,34,168
ds_write_b128 v13, a[12:15] offset:3072
Mvvv 112,34,172
W_ 6
R_ v21,a81
R_ v22,a82
Mvvv 96,38,176
Dv 34,v1,45056
R_ v23,a83
Mvvv 100,38,180
ds_write_b128 v13, a[16:19] offset:4096
Mvvv 104,38,184
R_ v24,a84
R_ v25,a85
Mvvv 108,38,188
ds_write_b128 v13, a[40:43] offset:5120
Mvvv 112,38,192
W_ 8
R_ v26,a86
R_ v27,a87
Mvvv 96,42,196
Dv 38,%0,36864
R_ v52,a88
Mvvv 100,42,200
ds_write_b128 v13, a[44:47] offset:6144
Mvvv 104,42,204
R_ v53,a89
R_ v54,a90
Mvvv 108,42,208
ds_write_b128 v13, a[48:51] offset:7168
Mvvv 112,42,212
W_ 8
R_ v55,a91
v_accvgpr_mov_b32 a240, a92
Mvvv 96,30,216
Dv 42,v1,36864
v_accvgpr_mov_b32 a241, a93
Mvvv 100,30,220
ds_write_b128 v13, a[52:55] offset:8192
Mvvv 104,30,224
v_accvgpr_mov_b32 a242, a94
v_accvgpr_mov_b32 a243, a95
Mvvv 108,30,228
ds_write_b128 v13, a[56:59] offset:9216
Mvvv 112,30,232
W_ 8
v_accvgpr_mov_b32 a244, a96
v_accvgpr_mov_b32 a245, a97
Mvvv 96,34,236
Dv 30,%0,37888
v_accvgpr_mov_b32 a246, a98
Mvvv 100,34,240
v_accvgpr_mov_b32 a247, a99
R_ v16,a120
Mvvv 104,34,244
R_ v17,a121
R_ v18,a122
Mvvv 108,34,248
R_ v19,a123
R_ v28,a124
Mvvv 112,34,252
W_ 6
R_ v29,a125
R_ v51,a126
Zvvv 76,38,136
Dv 34,v1,37888
R_ v6,a127
Zvvv 80,38,140
R_ v14,a128
R_ v15,a129
Zvvv 84,38,144
v_accvgpr_mov_b32 a249, a130
v_accvgpr_mov_b32 a250, a131
Zvvv 88,38,148
v_max3_f32 v56, v96, v97, v98
v_max_f32 v56, v56, v99
Zvvv 92,38,152
W_ 4
v_max3_f32 v60, v100, v101, v102
v_max_f32 v60, v60, v103
Mvvv 76,42,156
Dv 38,%0,38912
v_max3_f32 v64, v104, v105, v106
Mvvv 80,42,160
v_max_f32 v64, v64, v107
Mvvv 84,42,164
v_max3_f32 v68, v108, v109, v110
v_max_f32 v68, v68, v111
Mvvv 88,42,168
v_max3_f32 v72, v112, v113, v114
v_max_f32 v72, v72, v115
Mvvv 92,42,172
W_ 2
Mvvv 76,30,176
Dv 42,v1,38912
Mvvv 80,30,180
Mvvv 84,30,184
Mvvv 88,30,188
Mvvv 92,30,192
s_waitcnt vmcnt(34) lgkmcnt(0)
s_barrier
Mvvv 76,34,196
Dv 30,%0,55296
Mvvv 76,38,216
Mvvv 76,42,236
Mvvv 80,34,200
Mvvv 80,38,220
Mvvv 80,42,240
Mvvv 84,34,204
s_nop 3
v_max3_f32 v56, v56, v76, v77
v_max3_f32 v56, v56, v78, v79
Mvvv 84,38,224
Mvvv 84,42,244
Mvvv 88,34,208
v_max3_f32 v60, v60, v80, v81
v_max3_f32 v60, v60, v82, v83
Mvvv 88,38,228
Mvvv 88,42,248
Mvvv 92,34,212
s_nop 1
v_max3_f32 v64, v64, v84, v85
v_max3_f32 v64, v64, v86, v87
Mvvv 92,38,232
Mvvv 92,42,252
Dv 34,v1,55296
.p2align 3
v_mov_b32 v11, 0x3f803f80
v_permlane32_swap_b32 v56, v60
v_max3_f32 v68, v68, v88, v89
v_max3_f32 v68, v68, v90, v91
v_max_f32 v56, v56, v60
s_nop 1
v_max3_f32 v72, v72, v92, v93
v_max3_f32 v72, v72, v94, v95
v_mov_b32 v73, v72
v_permlane32_swap_b32 v64, v68
v_max_f32 v64, v64, v68
v_permlane32_swap_b32 v72, v73
v_max_f32 v72, v72, v73
v_mov_b32 v73, v72
v_permlane16_swap_b32 v56, v64
v_max_f32 v56, v56, v64
v_permlane16_swap_b32 v72, v73
B_ v56,0xc0e00000,v56
v_max_f32 v72, v72, v73
v_mov_b32 v64, v56
B_ v72,0xc0e00000,v72
v_mov_b32 v73, v72
v_permlane16_swap_b32 v56, v64
v_mov_b32 v60, v56
v_mov_b32 v68, v64
v_mov_b64 v[74:75], v[72:73]
v_permlane32_swap_b32 v56, v60
v_permlane32_swap_b32 v64, v68
v_mov_b32 v57, v56
v_mov_b32 v61, v60
v_mov_b32 v65, v64
v_mov_b32 v69, v68
v_pk_add_f32 v[96:97], v[96:97], v[56:57]
v_pk_add_f32 v[98:99], v[98:99], v[56:57]
v_pk_add_f32 v[76:77], v[76:77], v[56:57]
v_pk_add_f32 v[78:79], v[78:79], v[56:57]
v_mov_b64 v[58:59], v[56:57]
v_pk_add_f32 v[100:101], v[100:101], v[60:61]
v_pk_add_f32 v[102:103], v[102:103], v[60:61]
v_pk_add_f32 v[80:81], v[80:81], v[60:61]
v_pk_add_f32 v[82:83], v[82:83], v[60:61]
v_mov_b64 v[62:63], v[60:61]
v_pk_add_f32 v[104:105], v[104:105], v[64:65]
v_pk_add_f32 v[106:107], v[106:107], v[64:65]
v_pk_add_f32 v[84:85], v[84:85], v[64:65]
v_pk_add_f32 v[86:87], v[86:87], v[64:65]
v_mov_b64 v[66:67], v[64:65]
v_pk_add_f32 v[108:109], v[108:109], v[68:69]
v_pk_add_f32 v[110:111], v[110:111], v[68:69]
v_pk_add_f32 v[88:89], v[88:89], v[68:69]
v_pk_add_f32 v[90:91], v[90:91], v[68:69]
v_mov_b64 v[70:71], v[68:69]
v_pk_add_f32 v[112:113], v[112:113], v[72:73]
v_pk_add_f32 v[114:115], v[114:115], v[72:73]
v_pk_add_f32 v[92:93], v[92:93], v[72:73]
v_pk_add_f32 v[94:95], v[94:95], v[72:73]
E_ 96
E_ 97
E_ 98
E_ 99
E_ 76
E_ 77
E_ 78
E_ 79
C_ 99,98,99
C_ 98,96,97
C_ 96,76,77
C_ 97,78,79
E_ 100
E_ 101
E_ 102
E_ 103
E_ 80
E_ 81
E_ 82
E_ 83
C_ 103,102,103
C_ 102,100,101
C_ 100,80,81
C_ 101,82,83
.Lstep2_1:
W_ 1
Nvvv 116,30,136,56
s_nop 0
Dv 38,%0,56320
Dv 42,v1,56320
W_ 2
Mvvv 116,34,156
E_ 104
Nvvv 120,30,140,60
E_ 105
Mvvv 120,34,160
E_ 106
Nvvv 124,30,144,64
E_ 107
Mvvv 124,34,164
E_ 84
Nvvv 128,30,148,68
E_ 85
Mvvv 128,34,168
E_ 86
Nvvv 132,30,152,72
E_ 87
Mvvv 132,34,172
W_ 0
C_ 107,106,107
C_ 106,104,105
Mvvv 132,38,192
s_nop 0
Dv 30,%0,57344
Dv 34,v1,57344
Mvvv 132,42,212
C_ 104,84,85
C_ 105,86,87
Mvvv 128,38,188
E_ 108
Mvvv 128,42,208
E_ 109
Mvvv 124,38,184
E_ 110
Mvvv 124,42,204
E_ 111
Mvvv 120,38,180
E_ 88
Mvvv 120,42,200
E_ 89
Mvvv 116,38,176
E_ 90
Mvvv 116,42,196
W_ 0
E_ 91
Mvvv 116,30,216
s_nop 0
Dv 38,%0,49152
Dv 42,v1,49152
Mvvv 116,34,236
C_ 111,110,111
C_ 110,108,109
Mvvv 120,30,220
C_ 108,88,89
C_ 109,90,91
Mvvv 120,34,240
E_ 112
Mvvv 124,30,224
E_ 113
Mvvv 124,34,244
E_ 114
Mvvv 128,30,228
E_ 115
Mvvv 128,34,248
E_ 92
Mvvv 132,30,232
E_ 93
Mvvv 132,34,252
W_ 0
E_ 94
Nvvv 76,38,136,56
s_nop 0
Dv 30,%0,50176
Dv 34,v1,50176
Mvvv 76,42,156
E_ 95
Nvvv 80,38,140,60
C_ 115,114,115
C_ 114,112,113
Mvvv 80,42,160
C_ 112,92,93
C_ 113,94,95
Nvvv 84,38,144,64
E_ 116
Mvvv 84,42,164
E_ 117
Nvvv 88,38,148,68
E_ 118
Mvvv 88,42,168
E_ 119
Nvvv 92,38,152,72
C_ 119,118,119
C_ 118,116,117
Mvvv 92,42,172
W_ 0
E_ 120
Mvvv 92,30,192
s_nop 0
Dv 38,%0,51200
Dv 42,v1,51200
Mvvv 92,34,212
X_ a180,v11
X_ a181,v11
Mvvv 88,30,188
X_ a182,v11
X_ a183,v11
Mvvv 88,34,208
ds_write_b128 v12, a[132:135] offset:0
Mvvv 84,30,184
E_ 121
Mvvv 84,34,204
E_ 122
Mvvv 80,30,180
ds_write_b128 v12, a[136:139] offset:1024
Mvvv 80,34,200
E_ 123
Mvvv 76,30,176
C_ 123,122,123
C_ 122,120,121
Mvvv 76,34,196
ds_write_b128 v12, a[160:163] offset:2048
W_ 4
Mvvv 76,38,216
W_ 3
E_ 124
Mvvv 76,42,236
s_waitcnt vmcnt(12) lgkmcnt(0)
s_barrier
)A"
R"A(Mvvv 80,38,220
ds_write_b128 v12, a[164:167] offset:3072
Mvvv 80,42,240
E_ 125
Mvvv 84,38,224
E_ 126
Mvvv 84,42,244
s_nop 0
Tv 30,v2,24576
Tv 32,v2,30720
Mvvv 88,38,228
ds_write_b128 v12, a[168:171] offset:4096
Mvvv 88,42,248
s_nop 0
Tv 34,v3,24576
Tv 36,v3,30720
Mvvv 92,38,232
E_ 127
Mvvv 92,42,252
ds_write_b128 v12, a[172:175] offset:5120
Zaav 220,180,96
C_ 127,126,127
C_ 126,124,125
Zaav 224,180,100
E_ 128
Zaav 228,180,104
ds_write_b128 v12, a[176:179] offset:12288
Zaav 232,180,108
E_ 129
Zaav 236,180,112
W_ 2
E_ 130
Zavv 0,30,96
s_nop 0
Tv 38,v4,24576
Tv 40,v4,30720
Zavv 20,34,96
ds_write_b128 v12, a[200:203] offset:13312
Zavv 24,34,100
s_nop 0
Tv 42,v5,24576
Tv 44,v5,30720
Zavv 4,30,100
E_ 131
Zavv 8,30,104
ds_write_b128 v12, a[204:207] offset:14336
Zavv 28,34,104
R_ v46,a220
R_ v47,a224
Zavv 32,34,108
R_ v48,a228
R_ v49,a232
Zavv 12,30,108
R_ v50,a236
C_ 131,130,131
Zavv 16,30,112
ds_write_b128 v12, a[208:211] offset:15360
Zavv 36,34,112
W_ 2
C_ 130,128,129
Zavv 56,38,112
s_nop 0
Tv 30,v2,25600
Tv 32,v2,31744
Zavv 76,42,112
ds_write_b128 v12, a[212:215] offset:16384
Zavv 72,42,108
s_nop 0
Tv 34,v3,25600
Tv 36,v3,31744
Zavv 52,38,108
E_ 132
Zavv 48,38,104
ds_write_b128 v12, a[216:219] offset:17408
Zavv 68,42,104
E_ 133
Zavv 64,42,100
E_ 134
Zavv 44,38,100
E_ 135
Zavv 40,38,96
C_ 135,134,135
C_ 134,132,133
Zavv 60,42,96
W_ 1
E_ 76
Zavv 80,30,96
s_nop 0
Tv 38,v4,25600
Tv 40,v4,31744
Zavv 100,34,96
E_ 77
Zavv 104,34,100
s_nop 0
Tv 42,v5,25600
Tv 44,v5,31744
Zavv 84,30,100
E_ 78
Zavv 88,30,104
E_ 79
Zavv 108,34,104
C_ 116,76,77
C_ 117,78,79
Zavv 112,34,108
E_ 80
Zavv 92,30,108
E_ 81
Zavv 96,30,112
E_ 82
Zavv 116,34,112
W_ 0
E_ 83
Zavv 136,38,112
s_nop 0
Tv 30,v2,26624
Tv 32,v2,32768
Zavv 156,42,112
C_ 120,80,81
C_ 121,82,83
Zavv 152,42,108
s_nop 0
Tv 34,v3,26624
Tv 36,v3,32768
Zavv 132,38,108
E_ 84
Zavv 128,38,104
E_ 85
Zavv 148,42,104
E_ 86
Zavv 144,42,100
E_ 87
Zavv 124,38,100
C_ 124,84,85
C_ 125,86,87
Zavv 120,38,96
E_ 88
Zavv 140,42,96
W_ 0
E_ 89
Zavv 160,30,96
s_nop 0
Tv 38,v4,26624
Tv 40,v4,32768
Zavv 180,34,96
E_ 90
Zavv 184,34,100
s_nop 0
Tv 42,v5,26624
Tv 44,v5,32768
Zavv 164,30,100
E_ 91
Zavv 168,30,104
C_ 128,88,89
C_ 129,90,91
Zavv 188,34,104
S_ s65,s61,36864
E_ 92
Zavv 192,34,108
S_ m0,s51,24576
E_ 93
Zavv 172,30,108
s_waitcnt vmcnt(6) lgkmcnt(0)
s_barrier
Zavv 176,30,112
Ln v7,32,s65,0
Zavv 196,34,112
E_ 94
Zavv 216,38,112
Dv 30,%0,6144
Zavv 236,42,112
Dv 34,v1,6144
Zavv 232,42,108
Ln v8,32,s65,3072
S_ m0,s51,25472
Zavv 212,38,108
E_ 95
Zavv 208,38,104
Ln v7,32,s65,128
Zavv 228,42,104
C_ 132,92,93
C_ 133,94,95
Zavv 224,42,100
Ln v8,32,s65,3200
S_ m0,s51,26368
Zavv 204,38,100
v_mov_b32 v92, 0x3f803f80
v_mov_b32 v93, 0x3f803f80
Zavv 200,38,96
Ln v7,32,s65,256
Zavv 220,42,96
v_mov_b32 v94, 0x3f803f80
v_mov_b32 v95, 0x3f803f80
Ln v8,32,s65,3328
.Lstep2_2:
s_nop 0
Zvvv 76,92,116
Dv 38,%0,7168
Zvvv 80,92,120
Dv 42,v1,7168
Zvvv 84,92,124
Zvvv 88,92,128
Zvvv 92,92,132
W_ 2
Nvvv 96,30,136,56
Mvvv 96,34,156
Nvvv 100,30,140,60
s_nop 1
v_max3_f32 v10, v76, v80, v84
s_nop 0
v_max3_f32 v10, v10, v88, v92
Mvvv 100,34,160
v_cmp_lt_f32 vcc, 0x42000000, v10
s_cbranch_vccnz .Ltramp2_1
Nvvv 104,30,144,64
A_ v46,v46,v76
A_ v47,v47,v80
Mvvv 104,34,164
A_ v48,v48,v84
A_ v49,v49,v88
Nvvv 108,30,148,68
A_ v50,v50,v92
Mvvv 108,34,168
Nvvv 112,30,152,72
Mvvv 112,34,172
W_ 0
Mvvv 112,38,192
Dv 30,%0,8192
Mvvv 112,42,212
Dv 34,v1,8192
Mvvv 108,38,188
Mvvv 108,42,208
Mvvv 104,38,184
Mvvv 104,42,204
Mvvv 100,38,180
Mvvv 100,42,200
Mvvv 96,38,176
Mvvv 96,42,196
W_ 0
Mvvv 96,30,216
Dv 38,%0,0
Mvvv 96,34,236
Dv 42,v1,0
Mvvv 100,30,220
Mvvv 100,34,240
Mvvv 104,30,224
Mvvv 104,34,244
Mvvv 108,30,228
Mvvv 108,34,248
s_nop 0
E_ 96
Mvvv 112,30,232
E_ 97
Mvvv 112,34,252
W_ 0
E_ 98
Nvvv 76,38,136,56
Dv 30,%0,1024
Mvvv 76,42,156
Dv 34,v1,1024
Nvvv 80,38,140,60
E_ 99
Mvvv 80,42,160
C_ 99,98,99
C_ 98,96,97
Nvvv 84,38,144,64
E_ 100
Mvvv 84,42,164
E_ 101
Nvvv 88,38,148,68
E_ 102
Mvvv 88,42,168
E_ 103
Nvvv 92,38,152,72
C_ 103,102,103
C_ 102,100,101
Mvvv 92,42,172
W_ 0
E_ 104
Mvvv 92,30,192
Dv 38,%0,2048
Mvvv 92,34,212
Dv 42,v1,2048
Mvvv 88,30,188
E_ 105
Mvvv 88,34,208
E_ 106
Mvvv 84,30,184
S_ s65,s61,24576
E_ 107
Mvvv 84,34,204
S_ m0,s51,0
C_ 107,106,107
C_ 106,104,105
Mvvv 80,30,180
s_waitcnt vmcnt(6) lgkmcnt(0)
s_barrier
Mvvv 80,34,200
L_ v9,36,s65,0
Mvvv 76,30,176
E_ 108
Mvvv 76,34,196
L_ v9,36,s65,3072
S_ m0,s51,896
Mvvv 76,38,216
Tv 30,v2,12288
Tv 32,v2,18432
Mvvv 76,42,236
L_ v9,36,s65,128
Mvvv 80,38,220
s_nop 0
Tv 34,v3,12288
Tv 36,v3,18432
Mvvv 80,42,240
L_ v9,36,s65,3200
S_ m0,s51,1792
Mvvv 84,38,224
E_ 109
Mvvv 84,42,244
L_ v9,36,s65,256
Mvvv 88,38,228
E_ 110
Mvvv 88,42,248
L_ v9,36,s65,3328
Mvvv 92,38,232
E_ 111
Mvvv 92,42,252
W_ 0
C_ 111,110,111
C_ 110,108,109
Mavv 0,30,116
s_nop 0
Tv 38,v4,12288
Tv 40,v4,18432
Mavv 20,34,116
E_ 112
Mavv 24,34,120
s_nop 0
Tv 42,v5,12288
Tv 44,v5,18432
Mavv 4,30,120
E_ 113
Mavv 8,30,124
E_ 114
Mavv 28,34,124
E_ 115
Mavv 32,34,128
C_ 115,114,115
C_ 114,112,113
Mavv 12,30,128
E_ 76
Mavv 16,30,132
E_ 77
Mavv 36,34,132
W_ 0
E_ 78
Mavv 56,38,132
s_nop 0
Tv 30,v2,13312
Tv 32,v2,19456
Mavv 76,42,132
E_ 79
Mavv 72,42,128
s_nop 0
Tv 34,v3,13312
Tv 36,v3,19456
Mavv 52,38,128
C_ 96,76,77
C_ 97,78,79
Mavv 48,38,124
E_ 80
Mavv 68,42,124
E_ 81
Mavv 64,42,120
E_ 82
Mavv 44,38,120
E_ 83
Mavv 40,38,116
C_ 100,80,81
C_ 101,82,83
Mavv 60,42,116
W_ 0
E_ 84
Mavv 80,30,116
s_nop 0
Tv 38,v4,13312
Tv 40,v4,19456
Mavv 100,34,116
E_ 85
Mavv 104,34,120
s_nop 0
Tv 42,v5,13312
Tv 44,v5,19456
Mavv 84,30,120
E_ 86
Mavv 88,30,124
E_ 87
Mavv 108,34,124
C_ 104,84,85
C_ 105,86,87
Mavv 112,34,128
E_ 88
Mavv 92,30,128
E_ 89
Mavv 96,30,132
E_ 90
Mavv 116,34,132
W_ 0
E_ 91
Mavv 136,38,132
s_nop 0
Tv 30,v2,14336
Tv 32,v2,20480
Mavv 156,42,132
C_ 108,88,89
C_ 109,90,91
Mavv 152,42,128
s_nop 0
Tv 34,v3,14336
Tv 36,v3,20480
Mavv 132,38,128
E_ 92
Mavv 128,38,124
E_ 93
Mavv 148,42,124
E_ 94
Mavv 144,42,120
E_ 95
Mavv 124,38,120
C_ 112,92,93
C_ 113,94,95
Mavv 120,38,116
v_mov_b32 v92, 0x3f803f80
v_mov_b32 v93, 0x3f803f80
Mavv 140,42,116
W_ 0
v_mov_b32 v94, 0x3f803f80
v_mov_b32 v95, 0x3f803f80
Mavv 160,30,116
s_nop 0
Tv 38,v4,14336
Tv 40,v4,20480
Mavv 180,34,116
Mavv 184,34,120
s_nop 0
Tv 42,v5,14336
Tv 44,v5,20480
Mavv 164,30,120
Mavv 168,30,124
Mavv 188,34,124
S_ s65,s61,49152
Mavv 192,34,128
S_ m0,s51,12288
Mavv 172,30,128
s_waitcnt vmcnt(6) lgkmcnt(0)
s_barrier
Mavv 176,30,132
Ln v7,32,s65,0
Mavv 196,34,132
Mavv 216,38,132
Dv 30,%0,30720
Mavv 236,42,132
Dv 34,v1,30720
Mavv 232,42,128
Ln v8,32,s65,3072
S_ m0,s51,13184
Mavv 212,38,128
Mavv 208,38,124
Ln v7,32,s65,128
Mavv 228,42,124
Mavv 224,42,120
Ln v8,32,s65,3200
S_ m0,s51,14080
Mavv 204,38,120
Mavv 200,38,116
Ln v7,32,s65,256
Mavv 220,42,116
s_cmp_eq_u32 s4, 0
s_cbranch_scc1 .Lhs_a2
.Lhs_a2_back:
.Lstep2_3:
Zvvv 76,92,96
Dv 38,%0,31744
Zvvv 80,92,100
Dv 42,v1,31744
Zvvv 84,92,104
Zvvv 88,92,108
Zvvv 92,92,112
W_ 2
Nvvv 116,30,136,56
Mvvv 116,34,156
Nvvv 120,30,140,60
s_nop 1
v_max3_f32 v10, v76, v80, v84
s_nop 0
v_max3_f32 v10, v10, v88, v92
Mvvv 120,34,160
v_cmp_lt_f32 vcc, 0x42000000, v10
s_cbranch_vccnz .Ltramp2_2
Nvvv 124,30,144,64
A_ v46,v46,v76
A_ v47,v47,v80
Mvvv 124,34,164
A_ v48,v48,v84
A_ v49,v49,v88
Nvvv 128,30,148,68
A_ v50,v50,v92
Mvvv 128,34,168
Ln v8,32,s65,3328
Nvvv 132,30,152,72
Mvvv 132,34,172
W_ 0
Mvvv 132,38,192
Dv 30,%0,32768
Mvvv 132,42,212
Dv 34,v1,32768
Mvvv 128,38,188
Mvvv 128,42,208
Mvvv 124,38,184
Mvvv 124,42,204
Mvvv 120,38,180
Mvvv 120,42,200
Mvvv 116,38,176
Mvvv 116,42,196
W_ 0
Mvvv 116,30,216
Dv 38,%0,24576
Mvvv 116,34,236
Dv 42,v1,24576
Mvvv 120,30,220
Mvvv 120,34,240
Mvvv 124,30,224
Mvvv 124,34,244
Mvvv 128,30,228
Mvvv 128,34,248
s_nop 0
E_ 116
Mvvv 132,30,232
E_ 117
Mvvv 132,34,252
W_ 0
E_ 118
Nvvv 76,38,136,56
Dv 30,%0,25600
Mvvv 76,42,156
Dv 34,v1,25600
Nvvv 80,38,140,60
E_ 119
Mvvv 80,42,160
C_ 119,118,119
C_ 118,116,117
Nvvv 84,38,144,64
E_ 120
Mvvv 84,42,164
E_ 121
Nvvv 88,38,148,68
E_ 122
Mvvv 88,42,168
E_ 123
Nvvv 92,38,152,72
C_ 123,122,123
C_ 122,120,121
Mvvv 92,42,172
W_ 0
E_ 124
Mvvv 92,30,192
Dv 38,%0,26624
Mvvv 92,34,212
Dv 42,v1,26624
Mvvv 88,30,188
E_ 125
Mvvv 88,34,208
E_ 126
Mvvv 84,30,184
S_ s65,s61,36864
E_ 127
Mvvv 84,34,204
S_ m0,s51,24576
C_ 127,126,127
C_ 126,124,125
Mvvv 80,30,180
s_waitcnt vmcnt(6) lgkmcnt(0)
s_barrier
Mvvv 80,34,200
L_ v9,36,s65,0
Mvvv 76,30,176
E_ 128
Mvvv 76,34,196
L_ v9,36,s65,3072
S_ m0,s51,25472
Mvvv 76,38,216
Tv 30,v2,0
Tv 32,v2,6144
Mvvv 76,42,236
L_ v9,36,s65,128
Mvvv 80,38,220
s_nop 0
Tv 34,v3,0
Tv 36,v3,6144
Mvvv 80,42,240
L_ v9,36,s65,3200
S_ m0,s51,26368
Mvvv 84,38,224
E_ 129
Mvvv 84,42,244
L_ v9,36,s65,256
Mvvv 88,38,228
E_ 130
Mvvv 88,42,248
L_ v9,36,s65,3328
Mvvv 92,38,232
E_ 131
Mvvv 92,42,252
W_ 0
C_ 131,130,131
C_ 130,128,129
Mavv 0,30,96
s_nop 0
Tv 38,v4,0
Tv 40,v4,6144
Mavv 20,34,96
E_ 132
Mavv 24,34,100
s_nop 0
Tv 42,v5,0
Tv 44,v5,6144
Mavv 4,30,100
E_ 133
Mavv 8,30,104
E_ 134
Mavv 28,34,104
E_ 135
Mavv 32,34,108
C_ 135,134,135
C_ 134,132,133
Mavv 12,30,108
E_ 76
Mavv 16,30,112
E_ 77
Mavv 36,34,112
W_ 0
E_ 78
Mavv 56,38,112
s_nop 0
Tv 30,v2,1024
Tv 32,v2,7168
Mavv 76,42,112
E_ 79
Mavv 72,42,108
s_nop 0
Tv 34,v3,1024
Tv 36,v3,7168
Mavv 52,38,108
C_ 116,76,77
C_ 117,78,79
Mavv 48,38,104
E_ 80
Mavv 68,42,104
E_ 81
Mavv 64,42,100
E_ 82
Mavv 44,38,100
E_ 83
Mavv 40,38,96
C_ 120,80,81
C_ 121,82,83
Mavv 60,42,96
W_ 0
E_ 84
Mavv 80,30,96
s_nop 0
Tv 38,v4,1024
Tv 40,v4,7168
Mavv 100,34,96
E_ 85
Mavv 104,34,100
s_nop 0
Tv 42,v5,1024
Tv 44,v5,7168
Mavv 84,30,100
E_ 86
Mavv 88,30,104
E_ 87
Mavv 108,34,104
C_ 124,84,85
C_ 125,86,87
Mavv 112,34,108
E_ 88
Mavv 92,30,108
E_ 89
Mavv 96,30,112
E_ 90
Mavv 116,34,112
W_ 0
E_ 91
Mavv 136,38,112
s_nop 0
Tv 30,v2,2048
Tv 32,v2,8192
Mavv 156,42,112
C_ 128,88,89
C_ 129,90,91
Mavv 152,42,108
s_nop 0
Tv 34,v3,2048
Tv 36,v3,8192
Mavv 132,38,108
E_ 92
Mavv 128,38,104
E_ 93
Mavv 148,42,104
E_ 94
Mavv 144,42,100
E_ 95
Mavv 124,38,100
C_ 132,92,93
C_ 133,94,95
Mavv 120,38,96
v_mov_b32 v92, 0x3f803f80
v_mov_b32 v93, 0x3f803f80
Mavv 140,42,96
W_ 0
v_mov_b32 v94, 0x3f803f80
v_mov_b32 v95, 0x3f803f80
Mavv 160,30,96
s_nop 0
Tv 38,v4,2048
Tv 40,v4,8192
Mavv 180,34,96
Mavv 184,34,100
s_nop 0
Tv 42,v5,2048
Tv 44,v5,8192
Mavv 164,30,100
Mavv 168,30,104
Mavv 188,34,104
S_ s65,s61,61440
Mavv 192,34,108
S_ m0,s51,0
Mavv 172,30,108
s_waitcnt vmcnt(6) lgkmcnt(0)
s_barrier
Mavv 176,30,112
Ln v7,32,s65,0
Mavv 196,34,112
Mavv 216,38,112
Dv 30,%0,18432
Mavv 236,42,112
Dv 34,v1,18432
Mavv 232,42,108
Ln v8,32,s65,3072
S_ m0,s51,896
Mavv 212,38,108
Mavv 208,38,104
Ln v7,32,s65,128
Mavv 228,42,104
Mavv 224,42,100
Ln v8,32,s65,3200
S_ m0,s51,1792
Mavv 204,38,100
Mavv 200,38,96
Ln v7,32,s65,256
Mavv 220,42,96
.Lstep2_4:
Zvvv 76,92,116
Dv 38,%0,19456
Zvvv 80,92,120
Dv 42,v1,19456
Zvvv 84,92,124
Zvvv 88,92,128
Zvvv 92,92,132
W_ 2
Nvvv 96,30,136,56
Mvvv 96,34,156
Nvvv 100,30,140,60
s_nop 1
v_max3_f32 v10, v76, v80, v84
s_nop 0
v_max3_f32 v10, v10, v88, v92
Mvvv 100,34,160
v_cmp_lt_f32 vcc, 0x42000000, v10
s_cbranch_vccnz .Ltramp2_3
Nvvv 104,30,144,64
A_ v46,v46,v76
A_ v47,v47,v80
Mvvv 104,34,164
A_ v48,v48,v84
A_ v49,v49,v88
Nvvv 108,30,148,68
A_ v50,v50,v92
Mvvv 108,34,168
Ln v8,32,s65,3328
Nvvv 112,30,152,72
Mvvv 112,34,172
W_ 0
Mvvv 112,38,192
Dv 30,%0,20480
Mvvv 112,42,212
Dv 34,v1,20480
Mvvv 108,38,188
Mvvv 108,42,208
Mvvv 104,38,184
Mvvv 104,42,204
Mvvv 100,38,180
Mvvv 100,42,200
Mvvv 96,38,176
Mvvv 96,42,196
W_ 0
Mvvv 96,30,216
Dv 38,%0,12288
Mvvv 96,34,236
Dv 42,v1,12288
Mvvv 100,30,220
Mvvv 100,34,240
Mvvv 104,30,224
Mvvv 104,34,244
Mvvv 108,30,228
Mvvv 108,34,248
s_nop 0
E_ 96
Mvvv 112,30,232
E_ 97
Mvvv 112,34,252
W_ 0
E_ 98
Nvvv 76,38,136,56
Dv 30,%0,13312
Mvvv 76,42,156
Dv 34,v1,13312
Nvvv 80,38,140,60
E_ 99
Mvvv 80,42,160
C_ 99,98,99
C_ 98,96,97
Nvvv 84,38,144,64
E_ 100
Mvvv 84,42,164
E_ 101
Nvvv 88,38,148,68
E_ 102
Mvvv 88,42,168
E_ 103
Nvvv 92,38,152,72
C_ 103,102,103
C_ 102,100,101
Mvvv 92,42,172
W_ 0
E_ 104
Mvvv 92,30,192
Dv 38,%0,14336
Mvvv 92,34,212
Dv 42,v1,14336
Mvvv 88,30,188
E_ 105
Mvvv 88,34,208
E_ 106
Mvvv 84,30,184
S_ s65,s61,49152
E_ 107
Mvvv 84,34,204
S_ m0,s51,12288
C_ 107,106,107
C_ 106,104,105
Mvvv 80,30,180
s_waitcnt vmcnt(6) lgkmcnt(0)
s_barrier
Mvvv 80,34,200
L_ v9,36,s65,0
Mvvv 76,30,176
E_ 108
Mvvv 76,34,196
L_ v9,36,s65,3072
S_ m0,s51,13184
Mvvv 76,38,216
Tv 30,v2,24576
Tv 32,v2,30720
Mvvv 76,42,236
L_ v9,36,s65,128
Mvvv 80,38,220
s_nop 0
Tv 34,v3,24576
Tv 36,v3,30720
Mvvv 80,42,240
L_ v9,36,s65,3200
S_ m0,s51,14080
Mvvv 84,38,224
E_ 109
Mvvv 84,42,244
L_ v9,36,s65,256
Mvvv 88,38,228
E_ 110
Mvvv 88,42,248
L_ v9,36,s65,3328
Mvvv 92,38,232
E_ 111
Mvvv 92,42,252
W_ 0
C_ 111,110,111
C_ 110,108,109
Mavv 0,30,116
s_nop 0
Tv 38,v4,24576
Tv 40,v4,30720
Mavv 20,34,116
E_ 112
Mavv 24,34,120
s_nop 0
Tv 42,v5,24576
Tv 44,v5,30720
Mavv 4,30,120
E_ 113
Mavv 8,30,124
E_ 114
Mavv 28,34,124
E_ 115
Mavv 32,34,128
C_ 115,114,115
C_ 114,112,113
Mavv 12,30,128
E_ 76
Mavv 16,30,132
E_ 77
Mavv 36,34,132
W_ 0
E_ 78
Mavv 56,38,132
s_nop 0
Tv 30,v2,25600
Tv 32,v2,31744
Mavv 76,42,132
E_ 79
Mavv 72,42,128
s_nop 0
Tv 34,v3,25600
Tv 36,v3,31744
Mavv 52,38,128
C_ 96,76,77
C_ 97,78,79
Mavv 48,38,124
E_ 80
Mavv 68,42,124
E_ 81
Mavv 64,42,120
E_ 82
Mavv 44,38,120
E_ 83
Mavv 40,38,116
C_ 100,80,81
C_ 101,82,83
Mavv 60,42,116
W_ 0
E_ 84
Mavv 80,30,116
s_nop 0
Tv 38,v4,25600
Tv 40,v4,31744
Mavv 100,34,116
E_ 85
Mavv 104,34,120
s_nop 0
Tv 42,v5,25600
Tv 44,v5,31744
Mavv 84,30,120
E_ 86
Mavv 88,30,124
E_ 87
Mavv 108,34,124
C_ 104,84,85
C_ 105,86,87
Mavv 112,34,128
E_ 88
Mavv 92,30,128
E_ 89
Mavv 96,30,132
E_ 90
Mavv 116,34,132
W_ 0
E_ 91
Mavv 136,38,132
s_nop 0
Tv 30,v2,26624
Tv 32,v2,32768
Mavv 156,42,132
C_ 108,88,89
C_ 109,90,91
Mavv 152,42,128
s_nop 0
Tv 34,v3,26624
Tv 36,v3,32768
Mavv 132,38,128
E_ 92
Mavv 128,38,124
E_ 93
Mavv 148,42,124
E_ 94
Mavv 144,42,120
E_ 95
Mavv 124,38,120
C_ 112,92,93
C_ 113,94,95
Mavv 120,38,116
v_mov_b32 v92, 0x3f803f80
v_mov_b32 v93, 0x3f803f80
Mavv 140,42,116
W_ 0
v_mov_b32 v94, 0x3f803f80
v_mov_b32 v95, 0x3f803f80
Mavv 160,30,116
s_nop 0
Tv 38,v4,26624
Tv 40,v4,32768
Mavv 180,34,116
Mavv 184,34,120
s_nop 0
Tv 42,v5,26624
Tv 44,v5,32768
Mavv 164,30,120
Mavv 168,30,124
Mavv 188,34,124
S_ s65,s61,73728
Mavv 192,34,128
S_ m0,s51,24576
Mavv 172,30,128
s_waitcnt vmcnt(6) lgkmcnt(0)
s_barrier
Mavv 176,30,132
Ln v7,32,s65,0
Mavv 196,34,132
Mavv 216,38,132
Dv 30,%0,6144
Mavv 236,42,132
Dv 34,v1,6144
Mavv 232,42,128
Ln v8,32,s65,3072
S_ m0,s51,25472
Mavv 212,38,128
Mavv 208,38,124
Ln v7,32,s65,128
Mavv 228,42,124
Mavv 224,42,120
Ln v8,32,s65,3200
S_ m0,s51,26368
Mavv 204,38,120
Mavv 200,38,116
Ln v7,32,s65,256
Mavv 220,42,116
.Lstep2_5:
Zvvv 76,92,96
Dv 38,%0,7168
Zvvv 80,92,100
Dv 42,v1,7168
Zvvv 84,92,104
Zvvv 88,92,108
Zvvv 92,92,112
W_ 2
Nvvv 116,30,136,56
Mvvv 116,34,156
Nvvv 120,30,140,60
s_nop 1
v_max3_f32 v10, v76, v80, v84
s_nop 0
v_max3_f32 v10, v10, v88, v92
Mvvv 120,34,160
v_cmp_lt_f32 vcc, 0x42000000, v10
s_cbranch_vccnz .Ltramp2_4
Nvvv 124,30,144,64
A_ v46,v46,v76
A_ v47,v47,v80
Mvvv 124,34,164
A_ v48,v48,v84
A_ v49,v49,v88
Nvvv 128,30,148,68
A_ v50,v50,v92
Mvvv 128,34,168
Ln v8,32,s65,3328
Nvvv 132,30,152,72
Mvvv 132,34,172
W_ 0
Mvvv 132,38,192
Dv 30,%0,8192
Mvvv 132,42,212
Dv 34,v1,8192
Mvvv 128,38,188
Mvvv 128,42,208
Mvvv 124,38,184
Mvvv 124,42,204
Mvvv 120,38,180
Mvvv 120,42,200
Mvvv 116,38,176
Mvvv 116,42,196
W_ 0
Mvvv 116,30,216
Dv 38,%0,0
Mvvv 116,34,236
Dv 42,v1,0
Mvvv 120,30,220
Mvvv 120,34,240
Mvvv 124,30,224
Mvvv 124,34,244
Mvvv 128,30,228
Mvvv 128,34,248
s_nop 0
E_ 116
Mvvv 132,30,232
E_ 117
Mvvv 132,34,252
W_ 0
E_ 118
Nvvv 76,38,136,56
Dv 30,%0,1024
Mvvv 76,42,156
Dv 34,v1,1024
Nvvv 80,38,140,60
E_ 119
Mvvv 80,42,160
C_ 119,118,119
C_ 118,116,117
Nvvv 84,38,144,64
E_ 120
Mvvv 84,42,164
E_ 121
Nvvv 88,38,148,68
E_ 122
Mvvv 88,42,168
E_ 123
Nvvv 92,38,152,72
C_ 123,122,123
C_ 122,120,121
Mvvv 92,42,172
W_ 0
E_ 124
Mvvv 92,30,192
Dv 38,%0,2048
Mvvv 92,34,212
Dv 42,v1,2048
Mvvv 88,30,188
E_ 125
Mvvv 88,34,208
E_ 126
Mvvv 84,30,184
S_ s65,s61,61440
E_ 127
Mvvv 84,34,204
S_ m0,s51,0
C_ 127,126,127
C_ 126,124,125
Mvvv 80,30,180
s_waitcnt vmcnt(6) lgkmcnt(0)
s_barrier
Mvvv 80,34,200
L_ v9,36,s65,0
Mvvv 76,30,176
E_ 128
Mvvv 76,34,196
L_ v9,36,s65,3072
S_ m0,s51,896
Mvvv 76,38,216
Tv 30,v2,12288
Tv 32,v2,18432
Mvvv 76,42,236
L_ v9,36,s65,128
Mvvv 80,38,220
s_nop 0
Tv 34,v3,12288
Tv 36,v3,18432
Mvvv 80,42,240
L_ v9,36,s65,3200
S_ m0,s51,1792
Mvvv 84,38,224
E_ 129
Mvvv 84,42,244
L_ v9,36,s65,256
Mvvv 88,38,228
E_ 130
Mvvv 88,42,248
L_ v9,36,s65,3328
Mvvv 92,38,232
E_ 131
Mvvv 92,42,252
W_ 0
C_ 131,130,131
C_ 130,128,129
Mavv 0,30,96
s_nop 0
Tv 38,v4,12288
Tv 40,v4,18432
Mavv 20,34,96
E_ 132
Mavv 24,34,100
s_nop 0
Tv 42,v5,12288
Tv 44,v5,18432
Mavv 4,30,100
E_ 133
Mavv 8,30,104
E_ 134
Mavv 28,34,104
E_ 135
Mavv 32,34,108
C_ 135,134,135
C_ 134,132,133
Mavv 12,30,108
E_ 76
Mavv 16,30,112
E_ 77
Mavv 36,34,112
W_ 0
E_ 78
Mavv 56,38,112
s_nop 0
Tv 30,v2,13312
Tv 32,v2,19456
Mavv 76,42,112
E_ 79
Mavv 72,42,108
s_nop 0
Tv 34,v3,13312
Tv 36,v3,19456
Mavv 52,38,108
C_ 116,76,77
C_ 117,78,79
Mavv 48,38,104
E_ 80
Mavv 68,42,104
E_ 81
Mavv 64,42,100
E_ 82
Mavv 44,38,100
E_ 83
Mavv 40,38,96
C_ 120,80,81
C_ 121,82,83
Mavv 60,42,96
W_ 0
E_ 84
Mavv 80,30,96
s_nop 0
Tv 38,v4,13312
Tv 40,v4,19456
Mavv 100,34,96
E_ 85
Mavv 104,34,100
s_nop 0
Tv 42,v5,13312
Tv 44,v5,19456
Mavv 84,30,100
E_ 86
Mavv 88,30,104
E_ 87
Mavv 108,34,104
C_ 124,84,85
C_ 125,86,87
Mavv 112,34,108
E_ 88
Mavv 92,30,108
E_ 89
Mavv 96,30,112
E_ 90
Mavv 116,34,112
W_ 0
E_ 91
Mavv 136,38,112
s_nop 0
Tv 30,v2,14336
Tv 32,v2,20480
Mavv 156,42,112
C_ 128,88,89
C_ 129,90,91
Mavv 152,42,108
s_nop 0
Tv 34,v3,14336
Tv 36,v3,20480
Mavv 132,38,108
E_ 92
Mavv 128,38,104
E_ 93
Mavv 148,42,104
E_ 94
Mavv 144,42,100
E_ 95
Mavv 124,38,100
C_ 132,92,93
C_ 133,94,95
Mavv 120,38,96
v_mov_b32 v92, 0x3f803f80
v_mov_b32 v93, 0x3f803f80
Mavv 140,42,96
W_ 0
v_mov_b32 v94, 0x3f803f80
v_mov_b32 v95, 0x3f803f80
Mavv 160,30,96
s_nop 0
Tv 38,v4,14336
Tv 40,v4,20480
Mavv 180,34,96
Mavv 184,34,100
s_nop 0
Tv 42,v5,14336
Tv 44,v5,20480
Mavv 164,30,100
Mavv 168,30,104
Mavv 188,34,104
S_ s65,s61,86016
Mavv 192,34,108
S_ m0,s51,12288
Mavv 172,30,108
s_waitcnt vmcnt(6) lgkmcnt(0)
s_barrier
Mavv 176,30,112
Ln v7,32,s65,0
Mavv 196,34,112
Mavv 216,38,112
Dv 30,%0,30720
Mavv 236,42,112
Dv 34,v1,30720
Mavv 232,42,108
Ln v8,32,s65,3072
S_ m0,s51,13184
Mavv 212,38,108
Mavv 208,38,104
Ln v7,32,s65,128
Mavv 228,42,104
Mavv 224,42,100
Ln v8,32,s65,3200
S_ m0,s51,14080
Mavv 204,38,100
Mavv 200,38,96
Ln v7,32,s65,256
Mavv 220,42,96
.Lstep2_6:
Zvvv 76,92,116
Dv 38,%0,31744
Zvvv 80,92,120
Dv 42,v1,31744
Zvvv 84,92,124
Zvvv 88,92,128
Zvvv 92,92,132
W_ 2
Nvvv 96,30,136,56
Mvvv 96,34,156
Nvvv 100,30,140,60
s_nop 1
v_max3_f32 v10, v76, v80, v84
s_nop 0
v_max3_f32 v10, v10, v88, v92
Mvvv 100,34,160
v_cmp_lt_f32 vcc, 0x42000000, v10
s_cbranch_vccnz .Ltramp2_5
Nvvv 104,30,144,64
A_ v46,v46,v76
A_ v47,v47,v80
Mvvv 104,34,164
A_ v48,v48,v84
A_ v49,v49,v88
Nvvv 108,30,148,68
A_ v50,v50,v92
Mvvv 108,34,168
Ln v8,32,s65,3328
Nvvv 112,30,152,72
R_ v11,a248
Mvvv 112,34,172
W_ 0
v_min_u32 v11, 240, v11
Mvvv 112,38,192
Dv 30,%0,32768
v_lshlrev_b32 v11, 3, v11
Mvvv 112,42,212
Dv 34,v1,32768
Mvvv 108,38,188
buffer_load_dword a254, v11, s[44:47], s66 offen
Mvvv 108,42,208
Mvvv 104,38,184
Mvvv 104,42,204
Mvvv 100,38,180
Mvvv 100,42,200
Mvvv 96,38,176
Mvvv 96,42,196
W_ 0
Mvvv 96,30,216
Dv 38,%0,24576
Mvvv 96,34,236
Dv 42,v1,24576
Mvvv 100,30,220
Mvvv 100,34,240
Mvvv 104,30,224
Mvvv 104,34,244
Mvvv 108,30,228
Mvvv 108,34,248
s_nop 0
E_ 96
Mvvv 112,30,232
E_ 97
Mvvv 112,34,252
W_ 0
E_ 98
Nvvv 76,38,136,56
Dv 30,%0,25600
Mvvv 76,42,156
Dv 34,v1,25600
Nvvv 80,38,140,60
E_ 99
Mvvv 80,42,160
C_ 99,98,99
C_ 98,96,97
Nvvv 84,38,144,64
E_ 100
Mvvv 84,42,164
E_ 101
Nvvv 88,38,148,68
E_ 102
Mvvv 88,42,168
E_ 103
Nvvv 92,38,152,72
C_ 103,102,103
C_ 102,100,101
Mvvv 92,42,172
W_ 0
E_ 104
Mvvv 92,30,192
Dv 38,%0,26624
Mvvv 92,34,212
Dv 42,v1,26624
Mvvv 88,30,188
E_ 105
Mvvv 88,34,208
E_ 106
Mvvv 84,30,184
S_ s65,s61,73728
E_ 107
Mvvv 84,34,204
S_ m0,s51,24576
C_ 107,106,107
C_ 106,104,105
Mvvv 80,30,180
s_waitcnt vmcnt(7) lgkmcnt(0)
s_barrier
Mvvv 80,34,200
L_ v9,36,s65,0
Mvvv 76,30,176
E_ 108
Mvvv 76,34,196
L_ v9,36,s65,3072
S_ m0,s51,25472
Mvvv 76,38,216
Tv 30,v2,0
Tv 32,v2,6144
Mvvv 76,42,236
L_ v9,36,s65,128
Mvvv 80,38,220
s_nop 0
Tv 34,v3,0
Tv 36,v3,6144
Mvvv 80,42,240
L_ v9,36,s65,3200
S_ m0,s51,26368
Mvvv 84,38,224
E_ 109
Mvvv 84,42,244
L_ v9,36,s65,256
Mvvv 88,38,228
E_ 110
Mvvv 88,42,248
L_ v9,36,s65,3328
Mvvv 92,38,232
E_ 111
Mvvv 92,42,252
W_ 0
C_ 111,110,111
C_ 110,108,109
Mavv 0,30,116
s_nop 0
Tv 38,v4,0
Tv 40,v4,6144
Mavv 20,34,116
E_ 112
Mavv 24,34,120
s_nop 0
Tv 42,v5,0
Tv 44,v5,6144
Mavv 4,30,120
E_ 113
Mavv 8,30,124
E_ 114
Mavv 28,34,124
E_ 115
Mavv 32,34,128
C_ 115,114,115
C_ 114,112,113
Mavv 12,30,128
E_ 76
Mavv 16,30,132
E_ 77
Mavv 36,34,132
W_ 0
E_ 78
Mavv 56,38,132
s_nop 0
Tv 30,v2,1024
Tv 32,v2,7168
Mavv 76,42,132
E_ 79
Mavv 72,42,128
s_nop 0
Tv 34,v3,1024
Tv 36,v3,7168
Mavv 52,38,128
C_ 96,76,77
C_ 97,78,79
Mavv 48,38,124
E_ 80
Mavv 68,42,124
E_ 81
Mavv 64,42,120
E_ 82
Mavv 44,38,120
E_ 83
Mavv 40,38,116
C_ 100,80,81
C_ 101,82,83
Mavv 60,42,116
W_ 0
E_ 84
Mavv 80,30,116
s_nop 0
Tv 38,v4,1024
Tv 40,v4,7168
Mavv 100,34,116
E_ 85
Mavv 104,34,120
s_nop 0
Tv 42,v5,1024
Tv 44,v5,7168
Mavv 84,30,120
E_ 86
Mavv 88,30,124
E_ 87
Mavv 108,34,124
C_ 104,84,85
C_ 105,86,87
Mavv 112,34,128
E_ 88
Mavv 92,30,128
E_ 89
Mavv 96,30,132
E_ 90
Mavv 116,34,132
W_ 0
E_ 91
Mavv 136,38,132
s_nop 0
Tv 30,v2,2048
Tv 32,v2,8192
Mavv 156,42,132
C_ 108,88,89
C_ 109,90,91
Mavv 152,42,128
s_nop 0
Tv 34,v3,2048
Tv 36,v3,8192
Mavv 132,38,128
E_ 92
Mavv 128,38,124
E_ 93
Mavv 148,42,124
E_ 94
Mavv 144,42,120
E_ 95
Mavv 124,38,120
C_ 112,92,93
C_ 113,94,95
Mavv 120,38,116
v_mov_b32 v92, 0x3f803f80
v_mov_b32 v93, 0x3f803f80
Mavv 140,42,116
W_ 0
v_mov_b32 v94, 0x3f803f80
v_mov_b32 v95, 0x3f803f80
Mavv 160,30,116
s_nop 0
Tv 38,v4,2048
Tv 40,v4,8192
Mavv 180,34,116
Mavv 184,34,120
s_nop 0
Tv 42,v5,2048
Tv 44,v5,8192
Mavv 164,30,120
Mavv 168,30,124
Mavv 188,34,124
S_ s65,s61,98304
Mavv 192,34,128
S_ m0,s51,0
Mavv 172,30,128
s_waitcnt vmcnt(7) lgkmcnt(0)
s_barrier
Mavv 176,30,132
Ln v7,32,s65,0
Mavv 196,34,132
Mavv 216,38,132
Dv 30,%0,18432
Mavv 236,42,132
Dv 34,v1,18432
Mavv 232,42,128
Ln v8,32,s65,3072
S_ m0,s51,896
Mavv 212,38,128
Mavv 208,38,124
Ln v7,32,s65,128
Mavv 228,42,124
Mavv 224,42,120
Ln v8,32,s65,3200
S_ m0,s51,1792
Mavv 204,38,120
Mavv 200,38,116
Ln v7,32,s65,256
Mavv 220,42,116
s_cmp_eq_u32 s4, 0
s_cbranch_scc1 .Lhs_c2
.Lhs_c2_back:
.Lstep2_7:
Zvvv 76,92,96
Dv 38,%0,19456
Zvvv 80,92,100
Dv 42,v1,19456
Zvvv 84,92,104
Zvvv 88,92,108
Zvvv 92,92,112
W_ 2
Nvvv 116,30,136,56
Mvvv 116,34,156
Nvvv 120,30,140,60
s_nop 1
v_max3_f32 v10, v76, v80, v84
s_nop 0
v_max3_f32 v10, v10, v88, v92
Mvvv 120,34,160
v_cmp_lt_f32 vcc, 0x42000000, v10
s_cbranch_vccnz .Ltramp2_6
Nvvv 124,30,144,64
A_ v46,v46,v76
A_ v47,v47,v80
Mvvv 124,34,164
A_ v48,v48,v84
A_ v49,v49,v88
Nvvv 128,30,148,68
A_ v50,v50,v92
Mvvv 128,34,168
Ln v8,32,s65,3328
Nvvv 132,30,152,72
Mvvv 132,34,172
W_ 0
Mvvv 132,38,192
Dv 30,%0,20480
Mvvv 132,42,212
Dv 34,v1,20480
Mvvv 128,38,188
Mvvv 128,42,208
Mvvv 124,38,184
Mvvv 124,42,204
Mvvv 120,38,180
Mvvv 120,42,200
Mvvv 116,38,176
Mvvv 116,42,196
W_ 0
Mvvv 116,30,216
Dv 38,%0,12288
Mvvv 116,34,236
Dv 42,v1,12288
Mvvv 120,30,220
Mvvv 120,34,240
Mvvv 124,30,224
Mvvv 124,34,244
Mvvv 128,30,228
Mvvv 128,34,248
s_nop 0
E_ 116
Mvvv 132,30,232
E_ 117
Mvvv 132,34,252
W_ 0
E_ 118
Nvvv 76,38,136,56
Dv 30,%0,13312
Mvvv 76,42,156
Dv 34,v1,13312
Nvvv 80,38,140,60
E_ 119
Mvvv 80,42,160
C_ 119,118,119
C_ 118,116,117
Nvvv 84,38,144,64
E_ 120
Mvvv 84,42,164
E_ 121
Nvvv 88,38,148,68
E_ 122
Mvvv 88,42,168
E_ 123
Nvvv 92,38,152,72
C_ 123,122,123
C_ 122,120,121
Mvvv 92,42,172
W_ 0
E_ 124
Mvvv 92,30,192
Dv 38,%0,14336
Mvvv 92,34,212
Dv 42,v1,14336
Mvvv 88,30,188
E_ 125
Mvvv 88,34,208
E_ 126
Mvvv 84,30,184
S_ s65,s61,86016
E_ 127
Mvvv 84,34,204
S_ m0,s51,12288
C_ 127,126,127
C_ 126,124,125
Mvvv 80,30,180
s_waitcnt vmcnt(6) lgkmcnt(0)
s_barrier
Mvvv 80,34,200
L_ v9,36,s65,0
Mvvv 76,30,176
E_ 128
Mvvv 76,34,196
L_ v9,36,s65,3072
S_ m0,s51,13184
Mvvv 76,38,216
Tv 30,v2,24576
Tv 32,v2,30720
Mvvv 76,42,236
L_ v9,36,s65,128
Mvvv 80,38,220
s_nop 0
Tv 34,v3,24576
Tv 36,v3,30720
Mvvv 80,42,240
L_ v9,36,s65,3200
S_ m0,s51,14080
Mvvv 84,38,224
E_ 129
Mvvv 84,42,244
L_ v9,36,s65,256
Mvvv 88,38,228
E_ 130
Mvvv 88,42,248
L_ v9,36,s65,3328
Mvvv 92,38,232
E_ 131
Mvvv 92,42,252
W_ 0
C_ 131,130,131
C_ 130,128,129
Mavv 0,30,96
s_nop 0
Tv 38,v4,24576
Tv 40,v4,30720
Mavv 20,34,96
E_ 132
Mavv 24,34,100
s_nop 0
Tv 42,v5,24576
Tv 44,v5,30720
Mavv 4,30,100
E_ 133
Mavv 8,30,104
E_ 134
Mavv 28,34,104
E_ 135
Mavv 32,34,108
C_ 135,134,135
C_ 134,132,133
Mavv 12,30,108
E_ 76
Mavv 16,30,112
E_ 77
Mavv 36,34,112
W_ 0
E_ 78
Mavv 56,38,112
s_nop 0
Tv 30,v2,25600
Tv 32,v2,31744
Mavv 76,42,112
E_ 79
Mavv 72,42,108
s_nop 0
Tv 34,v3,25600
Tv 36,v3,31744
Mavv 52,38,108
C_ 116,76,77
C_ 117,78,79
Mavv 48,38,104
E_ 80
Mavv 68,42,104
E_ 81
Mavv 64,42,100
E_ 82
Mavv 44,38,100
E_ 83
Mavv 40,38,96
C_ 120,80,81
C_ 121,82,83
Mavv 60,42,96
W_ 0
E_ 84
Mavv 80,30,96
s_nop 0
Tv 38,v4,25600
Tv 40,v4,31744
Mavv 100,34,96
E_ 85
Mavv 104,34,100
s_nop 0
Tv 42,v5,25600
Tv 44,v5,31744
Mavv 84,30,100
E_ 86
Mavv 88,30,104
E_ 87
Mavv 108,34,104
C_ 124,84,85
C_ 125,86,87
Mavv 112,34,108
E_ 88
Mavv 92,30,108
E_ 89
Mavv 96,30,112
E_ 90
Mavv 116,34,112
W_ 0
E_ 91
Mavv 136,38,112
s_nop 0
Tv 30,v2,26624
Tv 32,v2,32768
Mavv 156,42,112
C_ 128,88,89
C_ 129,90,91
Mavv 152,42,108
s_nop 0
Tv 34,v3,26624
Tv 36,v3,32768
Mavv 132,38,108
E_ 92
Mavv 128,38,104
E_ 93
Mavv 148,42,104
E_ 94
Mavv 144,42,100
E_ 95
Mavv 124,38,100
C_ 132,92,93
C_ 133,94,95
Mavv 120,38,96
v_mov_b32 v92, 0x3f803f80
v_mov_b32 v93, 0x3f803f80
Mavv 140,42,96
W_ 0
v_mov_b32 v94, 0x3f803f80
v_mov_b32 v95, 0x3f803f80
Mavv 160,30,96
s_nop 0
Tv 38,v4,26624
Tv 40,v4,32768
Mavv 180,34,96
Mavv 184,34,100
s_nop 0
Tv 42,v5,26624
Tv 44,v5,32768
Mavv 164,30,100
Mavv 168,30,104
Mavv 188,34,104
S_ s65,s61,110592
Mavv 192,34,108
S_ m0,s51,24576
Mavv 172,30,108
s_waitcnt vmcnt(6) lgkmcnt(0)
s_barrier
Mavv 176,30,112
Ln v7,32,s65,0
Mavv 196,34,112
Mavv 216,38,112
Dv 30,%0,6144
Mavv 236,42,112
Dv 34,v1,6144
Mavv 232,42,108
Ln v8,32,s65,3072
S_ m0,s51,25472
Mavv 212,38,108
Mavv 208,38,104
Ln v7,32,s65,128
Mavv 228,42,104
Mavv 224,42,100
Ln v8,32,s65,3200
S_ m0,s51,26368
Mavv 204,38,100
Mavv 200,38,96
Ln v7,32,s65,256
Mavv 220,42,96
.Lstep2_8:
Zvvv 76,92,116
Dv 38,%0,7168
Zvvv 80,92,120
Dv 42,v1,7168
Zvvv 84,92,124
Zvvv 88,92,128
Zvvv 92,92,132
W_ 2
Nvvv 96,30,136,56
Mvvv 96,34,156
Nvvv 100,30,140,60
s_nop 1
v_max3_f32 v10, v76, v80, v84
s_nop 0
v_max3_f32 v10, v10, v88, v92
Mvvv 100,34,160
v_cmp_lt_f32 vcc, 0x42000000, v10
s_cbranch_vccnz .Ltramp2_7
Nvvv 104,30,144,64
A_ v46,v46,v76
A_ v47,v47,v80
Mvvv 104,34,164
A_ v48,v48,v84
A_ v49,v49,v88
Nvvv 108,30,148,68
A_ v50,v50,v92
Mvvv 108,34,168
Ln v8,32,s65,3328
Nvvv 112,30,152,72
Mvvv 112,34,172
W_ 0
Mvvv 112,38,192
Dv 30,%0,8192
Mvvv 112,42,212
Dv 34,v1,8192
Mvvv 108,38,188
Mvvv 108,42,208
Mvvv 104,38,184
Mvvv 104,42,204
Mvvv 100,38,180
Mvvv 100,42,200
Mvvv 96,38,176
Mvvv 96,42,196
W_ 0
Mvvv 96,30,216
Dv 38,%0,0
Mvvv 96,34,236
Dv 42,v1,0
Mvvv 100,30,220
Mvvv 100,34,240
Mvvv 104,30,224
Mvvv 104,34,244
Mvvv 108,30,228
Mvvv 108,34,248
s_nop 0
E_ 96
Mvvv 112,30,232
E_ 97
Mvvv 112,34,252
W_ 0
E_ 98
Nvvv 76,38,136,56
Dv 30,%0,1024
Mvvv 76,42,156
Dv 34,v1,1024
Nvvv 80,38,140,60
E_ 99
Mvvv 80,42,160
C_ 99,98,99
C_ 98,96,97
Nvvv 84,38,144,64
E_ 100
Mvvv 84,42,164
E_ 101
Nvvv 88,38,148,68
E_ 102
Mvvv 88,42,168
E_ 103
Nvvv 92,38,152,72
C_ 103,102,103
C_ 102,100,101
Mvvv 92,42,172
W_ 0
E_ 104
Mvvv 92,30,192
Dv 38,%0,2048
Mvvv 92,34,212
Dv 42,v1,2048
Mvvv 88,30,188
E_ 105
Mvvv 88,34,208
E_ 106
Mvvv 84,30,184
S_ s65,s61,98304
E_ 107
Mvvv 84,34,204
S_ m0,s51,0
C_ 107,106,107
)A"
R"A(C_ 106,104,105
Mvvv 80,30,180
s_waitcnt vmcnt(6) lgkmcnt(0)
s_barrier
Mvvv 80,34,200
L_ v9,36,s65,0
Mvvv 76,30,176
E_ 108
Mvvv 76,34,196
L_ v9,36,s65,3072
S_ m0,s51,896
Mvvv 76,38,216
Tv 30,v2,12288
Tv 32,v2,18432
Mvvv 76,42,236
L_ v9,36,s65,128
Mvvv 80,38,220
s_nop 0
Tv 34,v3,12288
Tv 36,v3,18432
Mvvv 80,42,240
L_ v9,36,s65,3200
S_ m0,s51,1792
Mvvv 84,38,224
E_ 109
Mvvv 84,42,244
L_ v9,36,s65,256
Mvvv 88,38,228
E_ 110
Mvvv 88,42,248
L_ v9,36,s65,3328
Mvvv 92,38,232
E_ 111
Mvvv 92,42,252
W_ 0
C_ 111,110,111
C_ 110,108,109
Mavv 0,30,116
s_nop 0
Tv 38,v4,12288
Tv 40,v4,18432
Mavv 20,34,116
E_ 112
Mavv 24,34,120
s_nop 0
Tv 42,v5,12288
Tv 44,v5,18432
Mavv 4,30,120
E_ 113
Mavv 8,30,124
E_ 114
Mavv 28,34,124
E_ 115
Mavv 32,34,128
C_ 115,114,115
C_ 114,112,113
Mavv 12,30,128
E_ 76
Mavv 16,30,132
E_ 77
Mavv 36,34,132
W_ 0
E_ 78
Mavv 56,38,132
s_nop 0
Tv 30,v2,13312
Tv 32,v2,19456
Mavv 76,42,132
E_ 79
Mavv 72,42,128
s_nop 0
Tv 34,v3,13312
Tv 36,v3,19456
Mavv 52,38,128
C_ 96,76,77
C_ 97,78,79
Mavv 48,38,124
E_ 80
Mavv 68,42,124
E_ 81
Mavv 64,42,120
E_ 82
Mavv 44,38,120
E_ 83
Mavv 40,38,116
C_ 100,80,81
C_ 101,82,83
Mavv 60,42,116
W_ 0
E_ 84
Mavv 80,30,116
s_nop 0
Tv 38,v4,13312
Tv 40,v4,19456
Mavv 100,34,116
E_ 85
Mavv 104,34,120
s_nop 0
Tv 42,v5,13312
Tv 44,v5,19456
Mavv 84,30,120
E_ 86
Mavv 88,30,124
E_ 87
Mavv 108,34,124
C_ 104,84,85
C_ 105,86,87
Mavv 112,34,128
E_ 88
Mavv 92,30,128
E_ 89
Mavv 96,30,132
E_ 90
Mavv 116,34,132
W_ 0
E_ 91
Mavv 136,38,132
s_nop 0
Tv 30,v2,14336
Tv 32,v2,20480
Mavv 156,42,132
C_ 108,88,89
C_ 109,90,91
Mavv 152,42,128
s_nop 0
Tv 34,v3,14336
Tv 36,v3,20480
Mavv 132,38,128
E_ 92
Mavv 128,38,124
E_ 93
Mavv 148,42,124
E_ 94
Mavv 144,42,120
E_ 95
Mavv 124,38,120
C_ 112,92,93
C_ 113,94,95
Mavv 120,38,116
v_mov_b32 v92, 0x3f803f80
v_mov_b32 v93, 0x3f803f80
Mavv 140,42,116
W_ 0
v_mov_b32 v94, 0x3f803f80
v_mov_b32 v95, 0x3f803f80
Mavv 160,30,116
s_nop 0
Tv 38,v4,14336
Tv 40,v4,20480
Mavv 180,34,116
Mavv 184,34,120
s_nop 0
Tv 42,v5,14336
Tv 44,v5,20480
Mavv 164,30,120
Mavv 168,30,124
Mavv 188,34,124
S_ s65,s61,122880
Mavv 192,34,128
S_ m0,s51,12288
Mavv 172,30,128
s_waitcnt vmcnt(6) lgkmcnt(0)
s_barrier
Mavv 176,30,132
Ln v7,32,s65,0
Mavv 196,34,132
Mavv 216,38,132
Dv 30,%0,30720
Mavv 236,42,132
Dv 34,v1,30720
Mavv 232,42,128
Ln v8,32,s65,3072
S_ m0,s51,13184
Mavv 212,38,128
Mavv 208,38,124
Ln v7,32,s65,128
Mavv 228,42,124
Mavv 224,42,120
Ln v8,32,s65,3200
S_ m0,s51,14080
Mavv 204,38,120
Mavv 200,38,116
Ln v7,32,s65,256
Mavv 220,42,116
s_cmp_eq_u32 s4, 0
s_cbranch_scc1 .Lhs_d2
.Lhs_d2_back:
.Lstep2_9:
Zvvv 76,92,96
Dv 38,%0,31744
Zvvv 80,92,100
Dv 42,v1,31744
Zvvv 84,92,104
Zvvv 88,92,108
Zvvv 92,92,112
W_ 2
Nvvv 116,30,136,56
Mvvv 116,34,156
Nvvv 120,30,140,60
s_nop 1
v_max3_f32 v10, v76, v80, v84
s_nop 0
v_max3_f32 v10, v10, v88, v92
Mvvv 120,34,160
v_cmp_lt_f32 vcc, 0x42000000, v10
s_cbranch_vccnz .Ltramp2_8
Nvvv 124,30,144,64
A_ v46,v46,v76
A_ v47,v47,v80
Mvvv 124,34,164
A_ v48,v48,v84
A_ v49,v49,v88
Nvvv 128,30,148,68
A_ v50,v50,v92
Mvvv 128,34,168
Ln v8,32,s65,3328
Nvvv 132,30,152,72
Mvvv 132,34,172
W_ 0
Mvvv 132,38,192
Dv 30,%0,32768
Mvvv 132,42,212
Dv 34,v1,32768
Mvvv 128,38,188
Mvvv 128,42,208
Mvvv 124,38,184
Mvvv 124,42,204
Mvvv 120,38,180
Mvvv 120,42,200
Mvvv 116,38,176
Mvvv 116,42,196
W_ 0
Mvvv 116,30,216
Dv 38,%0,24576
Mvvv 116,34,236
Dv 42,v1,24576
Mvvv 120,30,220
Mvvv 120,34,240
Mvvv 124,30,224
Mvvv 124,34,244
Mvvv 128,30,228
Mvvv 128,34,248
s_nop 0
E_ 116
Mvvv 132,30,232
E_ 117
Mvvv 132,34,252
W_ 0
E_ 118
Nvvv 76,38,136,56
Dv 30,%0,25600
Mvvv 76,42,156
Dv 34,v1,25600
Nvvv 80,38,140,60
E_ 119
Mvvv 80,42,160
C_ 119,118,119
C_ 118,116,117
Nvvv 84,38,144,64
E_ 120
Mvvv 84,42,164
E_ 121
Nvvv 88,38,148,68
E_ 122
Mvvv 88,42,168
E_ 123
Nvvv 92,38,152,72
C_ 123,122,123
C_ 122,120,121
Mvvv 92,42,172
W_ 0
E_ 124
Mvvv 92,30,192
Dv 38,%0,26624
Mvvv 92,34,212
Dv 42,v1,26624
Mvvv 88,30,188
E_ 125
Mvvv 88,34,208
E_ 126
Mvvv 84,30,184
S_ s65,s61,110592
E_ 127
Mvvv 84,34,204
S_ m0,s51,24576
C_ 127,126,127
C_ 126,124,125
Mvvv 80,30,180
s_waitcnt vmcnt(6) lgkmcnt(0)
s_barrier
Mvvv 80,34,200
Ln v9,36,s65,0
Mvvv 76,30,176
E_ 128
Mvvv 76,34,196
Ln v9,36,s65,3072
S_ m0,s51,25472
Mvvv 76,38,216
Tv 30,v2,0
Tv 32,v2,6144
Mvvv 76,42,236
Ln v9,36,s65,128
Mvvv 80,38,220
s_nop 0
Tv 34,v3,0
Tv 36,v3,6144
Mvvv 80,42,240
Ln v9,36,s65,3200
S_ m0,s51,26368
Mvvv 84,38,224
E_ 129
Mvvv 84,42,244
Ln v9,36,s65,256
Mvvv 88,38,228
E_ 130
Mvvv 88,42,248
Ln v9,36,s65,3328
Mvvv 92,38,232
E_ 131
Mvvv 92,42,252
W_ 0
C_ 131,130,131
C_ 130,128,129
Mavv 0,30,96
s_nop 0
Tv 38,v4,0
Tv 40,v4,6144
Mavv 20,34,96
E_ 132
Mavv 24,34,100
s_nop 0
Tv 42,v5,0
Tv 44,v5,6144
Mavv 4,30,100
E_ 133
Mavv 8,30,104
E_ 134
Mavv 28,34,104
E_ 135
Mavv 32,34,108
C_ 135,134,135
C_ 134,132,133
Mavv 12,30,108
E_ 76
Mavv 16,30,112
E_ 77
Mavv 36,34,112
W_ 0
E_ 78
Mavv 56,38,112
s_nop 0
Tv 30,v2,1024
Tv 32,v2,7168
Mavv 76,42,112
E_ 79
Mavv 72,42,108
s_nop 0
Tv 34,v3,1024
Tv 36,v3,7168
Mavv 52,38,108
C_ 116,76,77
C_ 117,78,79
Mavv 48,38,104
E_ 80
Mavv 68,42,104
E_ 81
Mavv 64,42,100
E_ 82
Mavv 44,38,100
E_ 83
Mavv 40,38,96
C_ 120,80,81
C_ 121,82,83
Mavv 60,42,96
W_ 0
E_ 84
Mavv 80,30,96
s_nop 0
Tv 38,v4,1024
Tv 40,v4,7168
Mavv 100,34,96
E_ 85
Mavv 104,34,100
s_nop 0
Tv 42,v5,1024
Tv 44,v5,7168
Mavv 84,30,100
E_ 86
Mavv 88,30,104
E_ 87
Mavv 108,34,104
C_ 124,84,85
C_ 125,86,87
Mavv 112,34,108
E_ 88
Mavv 92,30,108
E_ 89
Mavv 96,30,112
E_ 90
Mavv 116,34,112
W_ 0
E_ 91
Mavv 136,38,112
s_nop 0
Tv 30,v2,2048
Tv 32,v2,8192
Mavv 156,42,112
C_ 128,88,89
C_ 129,90,91
Mavv 152,42,108
s_nop 0
Tv 34,v3,2048
Tv 36,v3,8192
Mavv 132,38,108
E_ 92
Mavv 128,38,104
E_ 93
Mavv 148,42,104
E_ 94
Mavv 144,42,100
E_ 95
Mavv 124,38,100
C_ 132,92,93
C_ 133,94,95
Mavv 120,38,96
v_mov_b32 v92, 0x3f803f80
v_mov_b32 v93, 0x3f803f80
Mavv 140,42,96
W_ 0
v_mov_b32 v94, 0x3f803f80
v_mov_b32 v95, 0x3f803f80
Mavv 160,30,96
s_nop 0
Tv 38,v4,2048
Tv 40,v4,8192
Mavv 180,34,96
Mavv 184,34,100
s_nop 0
Tv 42,v5,2048
Tv 44,v5,8192
Mavv 164,30,100
Mavv 168,30,104
Mavv 188,34,104
S_ s65,s61,135168
Mavv 192,34,108
S_ m0,s51,0
Mavv 172,30,108
s_waitcnt vmcnt(6) lgkmcnt(0)
s_barrier
Mavv 176,30,112
Ln v7,32,s65,0
Mavv 196,34,112
Mavv 216,38,112
Dv 30,%0,18432
Mavv 236,42,112
Dv 34,v1,18432
Mavv 232,42,108
Ln v8,32,s65,3072
S_ m0,s51,896
Mavv 212,38,108
Mavv 208,38,104
Ln v7,32,s65,128
Mavv 228,42,104
Mavv 224,42,100
Ln v8,32,s65,3200
S_ m0,s51,1792
Mavv 204,38,100
Mavv 200,38,96
Ln v7,32,s65,256
Mavv 220,42,96
.Lstep2_10:
Zvvv 76,92,116
Dv 38,%0,19456
Zvvv 80,92,120
Dv 42,v1,19456
Zvvv 84,92,124
Zvvv 88,92,128
Zvvv 92,92,132
W_ 2
Nvvv 96,30,136,56
Mvvv 96,34,156
Nvvv 100,30,140,60
s_nop 1
v_max3_f32 v10, v76, v80, v84
s_nop 0
v_max3_f32 v10, v10, v88, v92
Mvvv 100,34,160
v_cmp_lt_f32 vcc, 0x42000000, v10
s_cbranch_vccnz .Ltramp2_9
Nvvv 104,30,144,64
A_ v46,v46,v76
A_ v47,v47,v80
Mvvv 104,34,164
A_ v48,v48,v84
A_ v49,v49,v88
Nvvv 108,30,148,68
A_ v50,v50,v92
Mvvv 108,34,168
Ln v8,32,s65,3328
Nvvv 112,30,152,72
Mvvv 112,34,172
W_ 0
Mvvv 112,38,192
Dv 30,%0,20480
Mvvv 112,42,212
Dv 34,v1,20480
Mvvv 108,38,188
Mvvv 108,42,208
Mvvv 104,38,184
Mvvv 104,42,204
Mvvv 100,38,180
Mvvv 100,42,200
Mvvv 96,38,176
Mvvv 96,42,196
W_ 0
Mvvv 96,30,216
Dv 38,%0,12288
Mvvv 96,34,236
Dv 42,v1,12288
Mvvv 100,30,220
Mvvv 100,34,240
Mvvv 104,30,224
Mvvv 104,34,244
Mvvv 108,30,228
Mvvv 108,34,248
s_nop 0
E_ 96
Mvvv 112,30,232
E_ 97
Mvvv 112,34,252
W_ 0
E_ 98
Nvvv 76,38,136,56
Dv 30,%0,13312
Mvvv 76,42,156
Dv 34,v1,13312
Nvvv 80,38,140,60
E_ 99
Mvvv 80,42,160
C_ 99,98,99
C_ 98,96,97
Nvvv 84,38,144,64
E_ 100
Mvvv 84,42,164
E_ 101
Nvvv 88,38,148,68
E_ 102
Mvvv 88,42,168
E_ 103
Nvvv 92,38,152,72
C_ 103,102,103
C_ 102,100,101
Mvvv 92,42,172
W_ 0
E_ 104
Mvvv 92,30,192
Dv 38,%0,14336
Mvvv 92,34,212
Dv 42,v1,14336
Mvvv 88,30,188
E_ 105
Mvvv 88,34,208
E_ 106
Mvvv 84,30,184
S_ s65,s61,122880
E_ 107
Mvvv 84,34,204
S_ m0,s51,12288
C_ 107,106,107
C_ 106,104,105
Mvvv 80,30,180
s_waitcnt vmcnt(6) lgkmcnt(0)
s_barrier
Mvvv 80,34,200
Ln v9,36,s65,0
Mvvv 76,30,176
E_ 108
Mvvv 76,34,196
Ln v9,36,s65,3072
S_ m0,s51,13184
Mvvv 76,38,216
Tv 30,v2,24576
Tv 32,v2,30720
Mvvv 76,42,236
Ln v9,36,s65,128
Mvvv 80,38,220
s_nop 0
Tv 34,v3,24576
Tv 36,v3,30720
Mvvv 80,42,240
Ln v9,36,s65,3200
S_ m0,s51,14080
Mvvv 84,38,224
E_ 109
Mvvv 84,42,244
Ln v9,36,s65,256
Mvvv 88,38,228
E_ 110
Mvvv 88,42,248
Ln v9,36,s65,3328
Mvvv 92,38,232
E_ 111
Mvvv 92,42,252
W_ 0
C_ 111,110,111
C_ 110,108,109
Mavv 0,30,116
s_nop 0
Tv 38,v4,24576
Tv 40,v4,30720
Mavv 20,34,116
E_ 112
Mavv 24,34,120
s_nop 0
Tv 42,v5,24576
Tv 44,v5,30720
Mavv 4,30,120
E_ 113
Mavv 8,30,124
E_ 114
Mavv 28,34,124
E_ 115
Mavv 32,34,128
C_ 115,114,115
C_ 114,112,113
Mavv 12,30,128
E_ 76
Mavv 16,30,132
E_ 77
Mavv 36,34,132
W_ 0
E_ 78
Mavv 56,38,132
s_nop 0
Tv 30,v2,25600
Tv 32,v2,31744
Mavv 76,42,132
E_ 79
Mavv 72,42,128
s_nop 0
Tv 34,v3,25600
Tv 36,v3,31744
Mavv 52,38,128
C_ 96,76,77
C_ 97,78,79
Mavv 48,38,124
E_ 80
Mavv 68,42,124
E_ 81
Mavv 64,42,120
E_ 82
Mavv 44,38,120
E_ 83
Mavv 40,38,116
C_ 100,80,81
C_ 101,82,83
Mavv 60,42,116
W_ 0
E_ 84
Mavv 80,30,116
s_nop 0
Tv 38,v4,25600
Tv 40,v4,31744
Mavv 100,34,116
E_ 85
Mavv 104,34,120
s_nop 0
Tv 42,v5,25600
Tv 44,v5,31744
Mavv 84,30,120
E_ 86
Mavv 88,30,124
E_ 87
Mavv 108,34,124
C_ 104,84,85
C_ 105,86,87
Mavv 112,34,128
E_ 88
Mavv 92,30,128
E_ 89
Mavv 96,30,132
E_ 90
Mavv 116,34,132
W_ 0
E_ 91
Mavv 136,38,132
s_nop 0
Tv 30,v2,26624
Tv 32,v2,32768
Mavv 156,42,132
C_ 108,88,89
C_ 109,90,91
Mavv 152,42,128
s_nop 0
Tv 34,v3,26624
Tv 36,v3,32768
Mavv 132,38,128
E_ 92
Mavv 128,38,124
E_ 93
Mavv 148,42,124
E_ 94
Mavv 144,42,120
E_ 95
Mavv 124,38,120
C_ 112,92,93
C_ 113,94,95
Mavv 120,38,116
v_mov_b32 v92, 0x3f803f80
v_mov_b32 v93, 0x3f803f80
Mavv 140,42,116
W_ 0
v_mov_b32 v94, 0x3f803f80
v_mov_b32 v95, 0x3f803f80
Mavv 160,30,116
s_nop 0
Tv 38,v4,26624
Tv 40,v4,32768
Mavv 180,34,116
Mavv 184,34,120
s_nop 0
Tv 42,v5,26624
Tv 44,v5,32768
Mavv 164,30,120
Mavv 168,30,124
Mavv 188,34,124
S_ s65,s61,147456
Mavv 192,34,128
S_ m0,s51,24576
Mavv 172,30,128
s_waitcnt vmcnt(6) lgkmcnt(0)
s_barrier
Mavv 176,30,132
Ln v7,32,s65,0
Mavv 196,34,132
Mavv 216,38,132
Dv 30,%0,6144
Mavv 236,42,132
Dv 34,v1,6144
Mavv 232,42,128
Ln v8,32,s65,3072
S_ m0,s51,25472
Mavv 212,38,128
Mavv 208,38,124
Ln v7,32,s65,128
Mavv 228,42,124
Mavv 224,42,120
Ln v8,32,s65,3200
S_ m0,s51,26368
Mavv 204,38,120
Mavv 200,38,116
Ln v7,32,s65,256
Mavv 220,42,116
.Lstep2_11:
Zvvv 76,92,96
Dv 38,%0,7168
Zvvv 80,92,100
Dv 42,v1,7168
Zvvv 84,92,104
Zvvv 88,92,108
Zvvv 92,92,112
W_ 2
Nvvv 116,30,136,56
Mvvv 116,34,156
Nvvv 120,30,140,60
s_nop 1
v_max3_f32 v10, v76, v80, v84
s_nop 0
v_max3_f32 v10, v10, v88, v92
Mvvv 120,34,160
v_cmp_lt_f32 vcc, 0x42000000, v10
s_cbranch_vccnz .Ltramp2_10
Nvvv 124,30,144,64
A_ v46,v46,v76
A_ v47,v47,v80
Mvvv 124,34,164
A_ v48,v48,v84
A_ v49,v49,v88
Nvvv 128,30,148,68
A_ v50,v50,v92
Mvvv 128,34,168
Ln v8,32,s65,3328
Nvvv 132,30,152,72
Mvvv 132,34,172
W_ 0
Mvvv 132,38,192
Dv 30,%0,8192
Mvvv 132,42,212
Dv 34,v1,8192
Mvvv 128,38,188
Mvvv 128,42,208
Mvvv 124,38,184
Mvvv 124,42,204
Mvvv 120,38,180
Mvvv 120,42,200
Mvvv 116,38,176
Mvvv 116,42,196
W_ 0
Mvvv 116,30,216
Dv 38,%0,0
Mvvv 116,34,236
Dv 42,v1,0
Mvvv 120,30,220
Mvvv 120,34,240
Mvvv 124,30,224
Mvvv 124,34,244
Mvvv 128,30,228
Mvvv 128,34,248
s_nop 0
E_ 116
Mvvv 132,30,232
E_ 117
Mvvv 132,34,252
W_ 0
E_ 118
Nvvv 76,38,136,56
Dv 30,%0,1024
Mvvv 76,42,156
Dv 34,v1,1024
Nvvv 80,38,140,60
E_ 119
Mvvv 80,42,160
C_ 119,118,119
C_ 118,116,117
Nvvv 84,38,144,64
E_ 120
Mvvv 84,42,164
E_ 121
Nvvv 88,38,148,68
E_ 122
Mvvv 88,42,168
E_ 123
Nvvv 92,38,152,72
C_ 123,122,123
C_ 122,120,121
Mvvv 92,42,172
W_ 0
E_ 124
Mvvv 92,30,192
Dv 38,%0,2048
Mvvv 92,34,212
Dv 42,v1,2048
Mvvv 88,30,188
E_ 125
Mvvv 88,34,208
E_ 126
Mvvv 84,30,184
S_ s65,s61,135168
E_ 127
Mvvv 84,34,204
S_ m0,s51,0
C_ 127,126,127
C_ 126,124,125
Mvvv 80,30,180
s_waitcnt vmcnt(6) lgkmcnt(0)
s_barrier
Mvvv 80,34,200
Ln v9,36,s65,0
Mvvv 76,30,176
E_ 128
Mvvv 76,34,196
Ln v9,36,s65,3072
S_ m0,s51,896
Mvvv 76,38,216
Tv 30,v2,12288
Tv 32,v2,18432
Mvvv 76,42,236
Ln v9,36,s65,128
Mvvv 80,38,220
s_nop 0
Tv 34,v3,12288
Tv 36,v3,18432
Mvvv 80,42,240
Ln v9,36,s65,3200
S_ m0,s51,1792
Mvvv 84,38,224
E_ 129
Mvvv 84,42,244
Ln v9,36,s65,256
Mvvv 88,38,228
E_ 130
Mvvv 88,42,248
Ln v9,36,s65,3328
Mvvv 92,38,232
E_ 131
Mvvv 92,42,252
W_ 0
C_ 131,130,131
C_ 130,128,129
Mavv 0,30,96
s_nop 0
Tv 38,v4,12288
Tv 40,v4,18432
Mavv 20,34,96
E_ 132
Mavv 24,34,100
s_nop 0
Tv 42,v5,12288
Tv 44,v5,18432
Mavv 4,30,100
E_ 133
Mavv 8,30,104
E_ 134
Mavv 28,34,104
E_ 135
Mavv 32,34,108
C_ 135,134,135
C_ 134,132,133
Mavv 12,30,108
E_ 76
Mavv 16,30,112
E_ 77
Mavv 36,34,112
W_ 0
E_ 78
Mavv 56,38,112
s_nop 0
Tv 30,v2,13312
Tv 32,v2,19456
Mavv 76,42,112
E_ 79
Mavv 72,42,108
s_nop 0
Tv 34,v3,13312
Tv 36,v3,19456
Mavv 52,38,108
C_ 116,76,77
C_ 117,78,79
Mavv 48,38,104
E_ 80
Mavv 68,42,104
E_ 81
Mavv 64,42,100
E_ 82
Mavv 44,38,100
E_ 83
Mavv 40,38,96
C_ 120,80,81
C_ 121,82,83
Mavv 60,42,96
W_ 0
E_ 84
Mavv 80,30,96
s_nop 0
Tv 38,v4,13312
Tv 40,v4,19456
Mavv 100,34,96
E_ 85
Mavv 104,34,100
s_nop 0
Tv 42,v5,13312
Tv 44,v5,19456
Mavv 84,30,100
E_ 86
Mavv 88,30,104
E_ 87
Mavv 108,34,104
C_ 124,84,85
C_ 125,86,87
Mavv 112,34,108
E_ 88
Mavv 92,30,108
E_ 89
Mavv 96,30,112
E_ 90
Mavv 116,34,112
W_ 0
E_ 91
Mavv 136,38,112
s_nop 0
Tv 30,v2,14336
Tv 32,v2,20480
Mavv 156,42,112
C_ 128,88,89
C_ 129,90,91
Mavv 152,42,108
s_nop 0
Tv 34,v3,14336
Tv 36,v3,20480
Mavv 132,38,108
E_ 92
Mavv 128,38,104
E_ 93
Mavv 148,42,104
E_ 94
Mavv 144,42,100
E_ 95
Mavv 124,38,100
C_ 132,92,93
C_ 133,94,95
Mavv 120,38,96
v_mov_b32 v92, 0x3f803f80
v_mov_b32 v93, 0x3f803f80
Mavv 140,42,96
W_ 0
v_mov_b32 v94, 0x3f803f80
v_mov_b32 v95, 0x3f803f80
Mavv 160,30,96
s_nop 0
Tv 38,v4,14336
Tv 40,v4,20480
Mavv 180,34,96
Mavv 184,34,100
s_nop 0
Tv 42,v5,14336
Tv 44,v5,20480
Mavv 164,30,100
Mavv 168,30,104
Mavv 188,34,104
S_ s65,s61,159744
Mavv 192,34,108
S_ m0,s51,12288
Mavv 172,30,108
s_waitcnt vmcnt(6) lgkmcnt(0)
s_barrier
Mavv 176,30,112
Ln v7,32,s65,0
Mavv 196,34,112
Mavv 216,38,112
Dv 30,%0,30720
Mavv 236,42,112
Dv 34,v1,30720
Mavv 232,42,108
Ln v8,32,s65,3072
S_ m0,s51,13184
Mavv 212,38,108
Mavv 208,38,104
Ln v7,32,s65,128
Mavv 228,42,104
Mavv 224,42,100
Ln v8,32,s65,3200
S_ m0,s51,14080
Mavv 204,38,100
Mavv 200,38,96
Ln v7,32,s65,256
Mavv 220,42,96
.Lstep2_12:
Zvvv 76,92,116
Dv 38,%0,31744
Zvvv 80,92,120
Dv 42,v1,31744
Zvvv 84,92,124
Zvvv 88,92,128
Zvvv 92,92,132
W_ 2
Nvvv 96,30,136,56
Mvvv 96,34,156
Nvvv 100,30,140,60
s_nop 1
v_max3_f32 v10, v76, v80, v84
s_nop 0
v_max3_f32 v10, v10, v88, v92
Mvvv 100,34,160
v_cmp_lt_f32 vcc, 0x42000000, v10
s_cbranch_vccnz .Ltramp2_11
Nvvv 104,30,144,64
A_ v46,v46,v76
A_ v47,v47,v80
Mvvv 104,34,164
A_ v48,v48,v84
A_ v49,v49,v88
Nvvv 108,30,148,68
A_ v50,v50,v92
Mvvv 108,34,168
Ln v8,32,s65,3328
Nvvv 112,30,152,72
R_ v11,a248
Mvvv 112,34,172
W_ 0
v_min_u32 v11, 240, v11
Mvvv 112,38,192
Dv 30,%0,32768
v_lshlrev_b32 v11, 3, v11
Mvvv 112,42,212
Dv 34,v1,32768
Mvvv 108,38,188
buffer_load_dword a254, v11, s[44:47], s66 offen
Mvvv 108,42,208
Mvvv 104,38,184
Mvvv 104,42,204
Mvvv 100,38,180
Mvvv 100,42,200
Mvvv 96,38,176
Mvvv 96,42,196
W_ 0
Mvvv 96,30,216
Dv 38,%0,24576
Mvvv 96,34,236
Dv 42,v1,24576
Mvvv 100,30,220
Mvvv 100,34,240
Mvvv 104,30,224
Mvvv 104,34,244
Mvvv 108,30,228
Mvvv 108,34,248
s_nop 0
E_ 96
Mvvv 112,30,232
E_ 97
Mvvv 112,34,252
W_ 0
E_ 98
Nvvv 76,38,136,56
Dv 30,%0,25600
Mvvv 76,42,156
Dv 34,v1,25600
Nvvv 80,38,140,60
E_ 99
Mvvv 80,42,160
C_ 99,98,99
C_ 98,96,97
Nvvv 84,38,144,64
E_ 100
Mvvv 84,42,164
E_ 101
Nvvv 88,38,148,68
E_ 102
Mvvv 88,42,168
E_ 103
Nvvv 92,38,152,72
C_ 103,102,103
C_ 102,100,101
Mvvv 92,42,172
W_ 0
E_ 104
Mvvv 92,30,192
Dv 38,%0,26624
Mvvv 92,34,212
Dv 42,v1,26624
Mvvv 88,30,188
E_ 105
Mvvv 88,34,208
E_ 106
Mvvv 84,30,184
S_ s65,s61,147456
E_ 107
Mvvv 84,34,204
S_ m0,s51,24576
C_ 107,106,107
C_ 106,104,105
Mvvv 80,30,180
s_waitcnt vmcnt(7) lgkmcnt(0)
s_barrier
Mvvv 80,34,200
Ln v9,36,s65,0
Mvvv 76,30,176
E_ 108
Mvvv 76,34,196
Ln v9,36,s65,3072
S_ m0,s51,25472
Mvvv 76,38,216
Tv 30,v2,0
Tv 32,v2,6144
Mvvv 76,42,236
Ln v9,36,s65,128
Mvvv 80,38,220
s_nop 0
Tv 34,v3,0
Tv 36,v3,6144
Mvvv 80,42,240
Ln v9,36,s65,3200
S_ m0,s51,26368
Mvvv 84,38,224
E_ 109
Mvvv 84,42,244
Ln v9,36,s65,256
Mvvv 88,38,228
E_ 110
Mvvv 88,42,248
Ln v9,36,s65,3328
Mvvv 92,38,232
E_ 111
Mvvv 92,42,252
W_ 0
C_ 111,110,111
C_ 110,108,109
Mavv 0,30,116
s_nop 0
Tv 38,v4,0
Tv 40,v4,6144
Mavv 20,34,116
E_ 112
Mavv 24,34,120
s_nop 0
Tv 42,v5,0
Tv 44,v5,6144
Mavv 4,30,120
E_ 113
Mavv 8,30,124
E_ 114
Mavv 28,34,124
E_ 115
Mavv 32,34,128
C_ 115,114,115
C_ 114,112,113
Mavv 12,30,128
E_ 76
Mavv 16,30,132
E_ 77
Mavv 36,34,132
W_ 0
E_ 78
Mavv 56,38,132
s_nop 0
Tv 30,v2,1024
Tv 32,v2,7168
Mavv 76,42,132
E_ 79
Mavv 72,42,128
s_nop 0
Tv 34,v3,1024
Tv 36,v3,7168
Mavv 52,38,128
C_ 96,76,77
C_ 97,78,79
Mavv 48,38,124
E_ 80
Mavv 68,42,124
E_ 81
Mavv 64,42,120
E_ 82
Mavv 44,38,120
E_ 83
Mavv 40,38,116
C_ 100,80,81
C_ 101,82,83
Mavv 60,42,116
W_ 0
E_ 84
Mavv 80,30,116
s_nop 0
Tv 38,v4,1024
Tv 40,v4,7168
Mavv 100,34,116
E_ 85
Mavv 104,34,120
s_nop 0
Tv 42,v5,1024
Tv 44,v5,7168
Mavv 84,30,120
E_ 86
Mavv 88,30,124
E_ 87
Mavv 108,34,124
C_ 104,84,85
C_ 105,86,87
Mavv 112,34,128
E_ 88
Mavv 92,30,128
E_ 89
Mavv 96,30,132
E_ 90
Mavv 116,34,132
W_ 0
E_ 91
Mavv 136,38,132
s_nop 0
Tv 30,v2,2048
Tv 32,v2,8192
Mavv 156,42,132
C_ 108,88,89
C_ 109,90,91
Mavv 152,42,128
s_nop 0
Tv 34,v3,2048
Tv 36,v3,8192
Mavv 132,38,128
E_ 92
Mavv 128,38,124
E_ 93
Mavv 148,42,124
E_ 94
Mavv 144,42,120
E_ 95
Mavv 124,38,120
C_ 112,92,93
C_ 113,94,95
Mavv 120,38,116
v_mov_b32 v92, 0x3f803f80
v_mov_b32 v93, 0x3f803f80
Mavv 140,42,116
W_ 0
v_mov_b32 v94, 0x3f803f80
v_mov_b32 v95, 0x3f803f80
Mavv 160,30,116
s_nop 0
Tv 38,v4,2048
Tv 40,v4,8192
Mavv 180,34,116
Mavv 184,34,120
s_nop 0
Tv 42,v5,2048
Tv 44,v5,8192
Mavv 164,30,120
Mavv 168,30,124
Mavv 188,34,124
S_ s65,s61,172032
Mavv 192,34,128
S_ m0,s51,0
Mavv 172,30,128
s_waitcnt vmcnt(7) lgkmcnt(0)
s_barrier
Mavv 176,30,132
Ln v7,32,s65,0
Mavv 196,34,132
Mavv 216,38,132
Dv 30,%0,18432
Mavv 236,42,132
Dv 34,v1,18432
Mavv 232,42,128
Ln v8,32,s65,3072
S_ m0,s51,896
Mavv 212,38,128
Mavv 208,38,124
Ln v7,32,s65,128
Mavv 228,42,124
Mavv 224,42,120
Ln v8,32,s65,3200
S_ m0,s51,1792
Mavv 204,38,120
Mavv 200,38,116
Ln v7,32,s65,256
Mavv 220,42,116
.Lstep2_13:
Zvvv 76,92,96
Dv 38,%0,19456
Zvvv 80,92,100
Dv 42,v1,19456
Zvvv 84,92,104
Zvvv 88,92,108
Zvvv 92,92,112
W_ 2
Nvvv 116,30,136,56
Mvvv 116,34,156
Nvvv 120,30,140,60
s_nop 1
v_max3_f32 v10, v76, v80, v84
s_nop 0
v_max3_f32 v10, v10, v88, v92
Mvvv 120,34,160
v_cmp_lt_f32 vcc, 0x42000000, v10
s_cbranch_vccnz .Ltramp2_12
Nvvv 124,30,144,64
A_ v46,v46,v76
A_ v47,v47,v80
Mvvv 124,34,164
A_ v48,v48,v84
A_ v49,v49,v88
Nvvv 128,30,148,68
A_ v50,v50,v92
Mvvv 128,34,168
Ln v8,32,s65,3328
Nvvv 132,30,152,72
Mvvv 132,34,172
W_ 0
Mvvv 132,38,192
Dv 30,%0,20480
Mvvv 132,42,212
Dv 34,v1,20480
Mvvv 128,38,188
Mvvv 128,42,208
Mvvv 124,38,184
Mvvv 124,42,204
Mvvv 120,38,180
Mvvv 120,42,200
Mvvv 116,38,176
Mvvv 116,42,196
W_ 0
Mvvv 116,30,216
Dv 38,%0,12288
Mvvv 116,34,236
Dv 42,v1,12288
Mvvv 120,30,220
Mvvv 120,34,240
Mvvv 124,30,224
Mvvv 124,34,244
Mvvv 128,30,228
Mvvv 128,34,248
s_nop 0
E_ 116
Mvvv 132,30,232
E_ 117
Mvvv 132,34,252
W_ 0
E_ 118
Nvvv 76,38,136,56
Dv 30,%0,13312
Mvvv 76,42,156
Dv 34,v1,13312
Nvvv 80,38,140,60
E_ 119
Mvvv 80,42,160
C_ 119,118,119
C_ 118,116,117
Nvvv 84,38,144,64
E_ 120
Mvvv 84,42,164
E_ 121
Nvvv 88,38,148,68
E_ 122
Mvvv 88,42,168
E_ 123
Nvvv 92,38,152,72
C_ 123,122,123
C_ 122,120,121
Mvvv 92,42,172
W_ 0
E_ 124
Mvvv 92,30,192
Dv 38,%0,14336
Mvvv 92,34,212
Dv 42,v1,14336
Mvvv 88,30,188
E_ 125
Mvvv 88,34,208
E_ 126
Mvvv 84,30,184
S_ s65,s61,159744
E_ 127
Mvvv 84,34,204
S_ m0,s51,12288
C_ 127,126,127
C_ 126,124,125
Mvvv 80,30,180
s_waitcnt vmcnt(6) lgkmcnt(0)
s_barrier
Mvvv 80,34,200
Ln v9,36,s65,0
Mvvv 76,30,176
E_ 128
Mvvv 76,34,196
Ln v9,36,s65,3072
S_ m0,s51,13184
Mvvv 76,38,216
Tv 30,v2,24576
Tv 32,v2,30720
Mvvv 76,42,236
Ln v9,36,s65,128
Mvvv 80,38,220
s_nop 0
Tv 34,v3,24576
Tv 36,v3,30720
Mvvv 80,42,240
Ln v9,36,s65,3200
S_ m0,s51,14080
Mvvv 84,38,224
E_ 129
Mvvv 84,42,244
Ln v9,36,s65,256
Mvvv 88,38,228
E_ 130
Mvvv 88,42,248
Ln v9,36,s65,3328
Mvvv 92,38,232
E_ 131
Mvvv 92,42,252
W_ 0
C_ 131,130,131
C_ 130,128,129
Mavv 0,30,96
s_nop 0
Tv 38,v4,24576
Tv 40,v4,30720
Mavv 20,34,96
E_ 132
Mavv 24,34,100
s_nop 0
Tv 42,v5,24576
Tv 44,v5,30720
Mavv 4,30,100
E_ 133
Mavv 8,30,104
E_ 134
Mavv 28,34,104
E_ 135
Mavv 32,34,108
C_ 135,134,135
C_ 134,132,133
Mavv 12,30,108
E_ 76
Mavv 16,30,112
E_ 77
Mavv 36,34,112
W_ 0
E_ 78
Mavv 56,38,112
s_nop 0
Tv 30,v2,25600
Tv 32,v2,31744
Mavv 76,42,112
E_ 79
Mavv 72,42,108
s_nop 0
Tv 34,v3,25600
Tv 36,v3,31744
Mavv 52,38,108
C_ 116,76,77
C_ 117,78,79
Mavv 48,38,104
E_ 80
Mavv 68,42,104
E_ 81
Mavv 64,42,100
E_ 82
Mavv 44,38,100
E_ 83
Mavv 40,38,96
C_ 120,80,81
C_ 121,82,83
Mavv 60,42,96
W_ 0
E_ 84
Mavv 80,30,96
s_nop 0
Tv 38,v4,25600
Tv 40,v4,31744
Mavv 100,34,96
E_ 85
Mavv 104,34,100
s_nop 0
Tv 42,v5,25600
Tv 44,v5,31744
Mavv 84,30,100
E_ 86
Mavv 88,30,104
E_ 87
Mavv 108,34,104
C_ 124,84,85
C_ 125,86,87
Mavv 112,34,108
E_ 88
Mavv 92,30,108
E_ 89
Mavv 96,30,112
E_ 90
Mavv 116,34,112
W_ 0
E_ 91
Mavv 136,38,112
s_nop 0
Tv 30,v2,26624
Tv 32,v2,32768
Mavv 156,42,112
C_ 128,88,89
C_ 129,90,91
Mavv 152,42,108
s_nop 0
Tv 34,v3,26624
Tv 36,v3,32768
Mavv 132,38,108
E_ 92
Mavv 128,38,104
E_ 93
Mavv 148,42,104
E_ 94
Mavv 144,42,100
E_ 95
Mavv 124,38,100
C_ 132,92,93
C_ 133,94,95
Mavv 120,38,96
v_mov_b32 v92, 0x3f803f80
v_mov_b32 v93, 0x3f803f80
Mavv 140,42,96
W_ 0
v_mov_b32 v94, 0x3f803f80
v_mov_b32 v95, 0x3f803f80
Mavv 160,30,96
s_nop 0
Tv 38,v4,26624
Tv 40,v4,32768
Mavv 180,34,96
Mavv 184,34,100
s_nop 0
Tv 42,v5,26624
Tv 44,v5,32768
Mavv 164,30,100
Mavv 168,30,104
Mavv 188,34,104
S_ s65,s61,184320
Mavv 192,34,108
S_ m0,s51,24576
Mavv 172,30,108
s_waitcnt vmcnt(6) lgkmcnt(0)
s_barrier
Mavv 176,30,112
Ln v7,32,s65,0
Mavv 196,34,112
Mavv 216,38,112
Dv 30,%0,6144
Mavv 236,42,112
Dv 34,v1,6144
Mavv 232,42,108
Ln v8,32,s65,3072
S_ m0,s51,25472
Mavv 212,38,108
Mavv 208,38,104
Ln v7,32,s65,128
Mavv 228,42,104
Mavv 224,42,100
Ln v8,32,s65,3200
S_ m0,s51,26368
Mavv 204,38,100
Mavv 200,38,96
Ln v7,32,s65,256
Mavv 220,42,96
.Lstep2_14:
Zvvv 76,92,116
Dv 38,%0,7168
Zvvv 80,92,120
Dv 42,v1,7168
Zvvv 84,92,124
Zvvv 88,92,128
Zvvv 92,92,132
W_ 2
Nvvv 96,30,136,56
Mvvv 96,34,156
Nvvv 100,30,140,60
s_nop 1
v_max3_f32 v10, v76, v80, v84
s_nop 0
v_max3_f32 v10, v10, v88, v92
Mvvv 100,34,160
v_cmp_lt_f32 vcc, 0x42000000, v10
s_cbranch_vccnz .Ltramp2_13
Nvvv 104,30,144,64
A_ v46,v46,v76
A_ v47,v47,v80
Mvvv 104,34,164
A_ v48,v48,v84
A_ v49,v49,v88
Nvvv 108,30,148,68
A_ v50,v50,v92
Mvvv 108,34,168
Ln v8,32,s65,3328
Nvvv 112,30,152,72
Mvvv 112,34,172
W_ 0
Mvvv 112,38,192
Dv 30,%0,8192
Mvvv 112,42,212
Dv 34,v1,8192
Mvvv 108,38,188
Mvvv 108,42,208
Mvvv 104,38,184
Mvvv 104,42,204
Mvvv 100,38,180
Mvvv 100,42,200
Mvvv 96,38,176
Mvvv 96,42,196
W_ 0
Mvvv 96,30,216
Dv 38,%0,0
Mvvv 96,34,236
Dv 42,v1,0
Mvvv 100,30,220
Mvvv 100,34,240
Mvvv 104,30,224
Mvvv 104,34,244
Mvvv 108,30,228
Mvvv 108,34,248
s_nop 0
E_ 96
Mvvv 112,30,232
E_ 97
Mvvv 112,34,252
W_ 0
E_ 98
Nvvv 76,38,136,56
Dv 30,%0,1024
Mvvv 76,42,156
Dv 34,v1,1024
Nvvv 80,38,140,60
E_ 99
Mvvv 80,42,160
C_ 99,98,99
C_ 98,96,97
Nvvv 84,38,144,64
E_ 100
Mvvv 84,42,164
E_ 101
Nvvv 88,38,148,68
E_ 102
Mvvv 88,42,168
E_ 103
Nvvv 92,38,152,72
C_ 103,102,103
C_ 102,100,101
Mvvv 92,42,172
W_ 0
E_ 104
Mvvv 92,30,192
Dv 38,%0,2048
Mvvv 92,34,212
Dv 42,v1,2048
Mvvv 88,30,188
E_ 105
Mvvv 88,34,208
E_ 106
Mvvv 84,30,184
S_ s65,s61,172032
E_ 107
Mvvv 84,34,204
S_ m0,s51,0
C_ 107,106,107
C_ 106,104,105
Mvvv 80,30,180
s_waitcnt vmcnt(6) lgkmcnt(0)
s_barrier
Mvvv 80,34,200
Ln v9,36,s65,0
Mvvv 76,30,176
E_ 108
Mvvv 76,34,196
Ln v9,36,s65,3072
S_ m0,s51,896
Mvvv 76,38,216
Tv 30,v2,12288
Tv 32,v2,18432
Mvvv 76,42,236
Ln v9,36,s65,128
Mvvv 80,38,220
s_nop 0
Tv 34,v3,12288
Tv 36,v3,18432
Mvvv 80,42,240
Ln v9,36,s65,3200
S_ m0,s51,1792
Mvvv 84,38,224
E_ 109
Mvvv 84,42,244
Ln v9,36,s65,256
Mvvv 88,38,228
E_ 110
Mvvv 88,42,248
Ln v9,36,s65,3328
Mvvv 92,38,232
E_ 111
Mvvv 92,42,252
W_ 0
C_ 111,110,111
C_ 110,108,109
Mavv 0,30,116
s_nop 0
Tv 38,v4,12288
Tv 40,v4,18432
Mavv 20,34,116
E_ 112
Mavv 24,34,120
s_nop 0
Tv 42,v5,12288
Tv 44,v5,18432
Mavv 4,30,120
E_ 113
Mavv 8,30,124
E_ 114
Mavv 28,34,124
E_ 115
Mavv 32,34,128
C_ 115,114,115
C_ 114,112,113
Mavv 12,30,128
E_ 76
Mavv 16,30,132
E_ 77
Mavv 36,34,132
W_ 0
E_ 78
Mavv 56,38,132
s_nop 0
Tv 30,v2,13312
Tv 32,v2,19456
Mavv 76,42,132
E_ 79
Mavv 72,42,128
s_nop 0
Tv 34,v3,13312
Tv 36,v3,19456
Mavv 52,38,128
C_ 96,76,77
C_ 97,78,79
Mavv 48,38,124
E_ 80
Mavv 68,42,124
E_ 81
Mavv 64,42,120
E_ 82
Mavv 44,38,120
E_ 83
Mavv 40,38,116
C_ 100,80,81
C_ 101,82,83
Mavv 60,42,116
W_ 0
E_ 84
Mavv 80,30,116
s_nop 0
Tv 38,v4,13312
Tv 40,v4,19456
Mavv 100,34,116
E_ 85
Mavv 104,34,120
s_nop 0
Tv 42,v5,13312
Tv 44,v5,19456
Mavv 84,30,120
E_ 86
Mavv 88,30,124
E_ 87
Mavv 108,34,124
C_ 104,84,85
C_ 105,86,87
Mavv 112,34,128
E_ 88
Mavv 92,30,128
E_ 89
Mavv 96,30,132
E_ 90
Mavv 116,34,132
W_ 0
E_ 91
Mavv 136,38,132
s_nop 0
Tv 30,v2,14336
Tv 32,v2,20480
Mavv 156,42,132
C_ 108,88,89
C_ 109,90,91
Mavv 152,42,128
s_nop 0
Tv 34,v3,14336
Tv 36,v3,20480
Mavv 132,38,128
E_ 92
Mavv 128,38,124
E_ 93
Mavv 148,42,124
E_ 94
Mavv 144,42,120
E_ 95
Mavv 124,38,120
C_ 112,92,93
C_ 113,94,95
Mavv 120,38,116
v_mov_b32 v92, 0x3f803f80
v_mov_b32 v93, 0x3f803f80
Mavv 140,42,116
W_ 0
v_mov_b32 v94, 0x3f803f80
v_mov_b32 v95, 0x3f803f80
Mavv 160,30,116
s_nop 0
Tv 38,v4,14336
Tv 40,v4,20480
Mavv 180,34,116
Mavv 184,34,120
s_nop 0
Tv 42,v5,14336
Tv 44,v5,20480
Mavv 164,30,120
Mavv 168,30,124
Mavv 188,34,124
S_ s65,s61,184320
Mavv 192,34,128
S_ m0,s51,12288
Mavv 172,30,128
s_waitcnt vmcnt(6) lgkmcnt(0)
s_barrier
Mavv 176,30,132
Ln v9,36,s65,0
Mavv 196,34,132
Mavv 216,38,132
Dv 30,%0,30720
Mavv 236,42,132
Dv 34,v1,30720
Mavv 232,42,128
Ln v9,36,s65,3072
S_ m0,s51,13184
Mavv 212,38,128
Mavv 208,38,124
Ln v9,36,s65,128
Mavv 228,42,124
Mavv 224,42,120
Ln v9,36,s65,3200
S_ m0,s51,14080
Mavv 204,38,120
Mavv 200,38,116
Ln v9,36,s65,256
Mavv 220,42,116
.Lstep2_15:
Zvvv 76,92,96
Dv 38,%0,31744
Zvvv 80,92,100
Dv 42,v1,31744
Zvvv 84,92,104
Zvvv 88,92,108
Zvvv 92,92,112
W_ 2
Nvvv 116,30,136,56
Mvvv 116,34,156
Nvvv 120,30,140,60
s_nop 1
v_max3_f32 v10, v76, v80, v84
s_nop 0
v_max3_f32 v10, v10, v88, v92
Mvvv 120,34,160
v_cmp_lt_f32 vcc, 0x42000000, v10
s_cbranch_vccnz .Ltramp2_14
Nvvv 124,30,144,64
A_ v46,v46,v76
A_ v47,v47,v80
Mvvv 124,34,164
A_ v48,v48,v84
A_ v49,v49,v88
Nvvv 128,30,148,68
A_ v50,v50,v92
Mvvv 128,34,168
Ln v9,36,s65,3328
Nvvv 132,30,152,72
Mvvv 132,34,172
W_ 0
Mvvv 132,38,192
Dv 30,%0,32768
Mvvv 132,42,212
Dv 34,v1,32768
Mvvv 128,38,188
Mvvv 128,42,208
Mvvv 124,38,184
Mvvv 124,42,204
Mvvv 120,38,180
Mvvv 120,42,200
Mvvv 116,38,176
Mvvv 116,42,196
W_ 0
Mvvv 116,30,216
Dv 38,%0,24576
Mvvv 116,34,236
Dv 42,v1,24576
Mvvv 120,30,220
Mvvv 120,34,240
Mvvv 124,30,224
Mvvv 124,34,244
Mvvv 128,30,228
Mvvv 128,34,248
s_nop 0
E_ 116
Mvvv 132,30,232
E_ 117
Mvvv 132,34,252
W_ 0
E_ 118
Nvvv 76,38,136,56
Dv 30,%0,25600
Mvvv 76,42,156
Dv 34,v1,25600
Nvvv 80,38,140,60
E_ 119
Mvvv 80,42,160
C_ 119,118,119
C_ 118,116,117
Nvvv 84,38,144,64
E_ 120
Mvvv 84,42,164
E_ 121
Nvvv 88,38,148,68
E_ 122
Mvvv 88,42,168
E_ 123
Nvvv 92,38,152,72
C_ 123,122,123
C_ 122,120,121
Mvvv 92,42,172
W_ 0
E_ 124
Mvvv 92,30,192
Dv 38,%0,26624
Mvvv 92,34,212
Dv 42,v1,26624
Mvvv 88,30,188
E_ 125
Mvvv 88,34,208
E_ 126
Mvvv 84,30,184
S_ s65,s61,393216
E_ 127
Mvvv 84,34,204
S_ m0,s51,24576
C_ 127,126,127
C_ 126,124,125
Mvvv 80,30,180
s_waitcnt vmcnt(6) lgkmcnt(0)
s_barrier
Mvvv 80,34,200
L_ v9,36,s65,0
Mvvv 76,30,176
E_ 128
Mvvv 76,34,196
L_ v9,36,s65,3072
S_ m0,s51,25472
Mvvv 76,38,216
)A"
R"A(Tv 30,v2,0
Tv 32,v2,6144
Mvvv 76,42,236
L_ v9,36,s65,128
Mvvv 80,38,220
s_nop 0
Tv 34,v3,0
Tv 36,v3,6144
Mvvv 80,42,240
L_ v9,36,s65,3200
S_ m0,s51,26368
Mvvv 84,38,224
E_ 129
Mvvv 84,42,244
L_ v9,36,s65,256
Mvvv 88,38,228
E_ 130
Mvvv 88,42,248
L_ v9,36,s65,3328
Mvvv 92,38,232
E_ 131
Mvvv 92,42,252
W_ 0
C_ 131,130,131
C_ 130,128,129
Mavv 0,30,96
s_nop 0
Tv 38,v4,0
Tv 40,v4,6144
Mavv 20,34,96
E_ 132
Mavv 24,34,100
s_nop 0
Tv 42,v5,0
Tv 44,v5,6144
Mavv 4,30,100
E_ 133
Mavv 8,30,104
E_ 134
Mavv 28,34,104
E_ 135
Mavv 32,34,108
C_ 135,134,135
C_ 134,132,133
Mavv 12,30,108
E_ 76
Mavv 16,30,112
E_ 77
Mavv 36,34,112
W_ 0
E_ 78
Mavv 56,38,112
s_nop 0
Tv 30,v2,1024
Tv 32,v2,7168
Mavv 76,42,112
E_ 79
Mavv 72,42,108
s_nop 0
Tv 34,v3,1024
Tv 36,v3,7168
Mavv 52,38,108
C_ 116,76,77
C_ 117,78,79
Mavv 48,38,104
E_ 80
Mavv 68,42,104
E_ 81
Mavv 64,42,100
E_ 82
Mavv 44,38,100
E_ 83
Mavv 40,38,96
C_ 120,80,81
C_ 121,82,83
Mavv 60,42,96
W_ 0
E_ 84
Mavv 80,30,96
s_nop 0
Tv 38,v4,1024
Tv 40,v4,7168
Mavv 100,34,96
E_ 85
Mavv 104,34,100
s_nop 0
Tv 42,v5,1024
Tv 44,v5,7168
Mavv 84,30,100
E_ 86
Mavv 88,30,104
E_ 87
Mavv 108,34,104
C_ 124,84,85
C_ 125,86,87
Mavv 112,34,108
E_ 88
Mavv 92,30,108
E_ 89
Mavv 96,30,112
E_ 90
Mavv 116,34,112
W_ 0
E_ 91
Mavv 136,38,112
s_nop 0
Tv 30,v2,2048
Tv 32,v2,8192
Mavv 156,42,112
C_ 128,88,89
C_ 129,90,91
Mavv 152,42,108
s_nop 0
Tv 34,v3,2048
Tv 36,v3,8192
Mavv 132,38,108
E_ 92
Mavv 128,38,104
E_ 93
Mavv 148,42,104
E_ 94
Mavv 144,42,100
E_ 95
Mavv 124,38,100
C_ 132,92,93
C_ 133,94,95
Mavv 120,38,96
v_mov_b32 v92, 0x3f803f80
v_mov_b32 v93, 0x3f803f80
Mavv 140,42,96
W_ 0
v_mov_b32 v94, 0x3f803f80
v_mov_b32 v95, 0x3f803f80
Mavv 160,30,96
s_nop 0
Tv 38,v4,2048
Tv 40,v4,8192
Mavv 180,34,96
Mavv 184,34,100
s_nop 0
Tv 42,v5,2048
Tv 44,v5,8192
Mavv 164,30,100
Mavv 168,30,104
Mavv 188,34,104
S_ s65,s61,393216
Mavv 192,34,108
S_ m0,s51,0
Mavv 172,30,108
s_waitcnt vmcnt(6) lgkmcnt(0)
s_barrier
Mavv 176,30,112
Ln v7,32,s65,0
Mavv 196,34,112
Mavv 216,38,112
s_nop 0
Tv 30,v2,12288
Tv 32,v2,18432
Zvvv 76,92,116
Ln v8,32,s65,3072
S_ m0,s51,896
Zvvv 80,92,120
s_nop 0
Tv 34,v3,12288
Tv 36,v3,18432
Zvvv 84,92,124
Ln v7,32,s65,128
Zvvv 88,92,128
Zvvv 92,92,132
Ln v8,32,s65,3200
S_ m0,s51,1792
Mavv 236,42,112
Mavv 232,42,108
Ln v7,32,s65,256
Mavv 212,38,108
Mavv 208,38,104
v_max3_f32 v10, v76, v80, v84
Mavv 228,42,104
v_max3_f32 v10, v10, v88, v92
v_cmp_lt_f32 vcc, 0x42000000, v10
Mavv 224,42,100
Ln v8,32,s65,3328
Mavv 204,38,100
Mavv 200,38,96
Mavv 220,42,96
s_cbranch_vccnz .Ltramp2_15
.Ljoin2:
W_ 2
Mavv 0,30,116
s_mov_b32 s58, 1
Tv 38,v4,12288
Tv 40,v4,18432
W_ 2
Mavv 20,34,116
R_ v11,a248
R_ v13,a248
Mavv 24,34,120
S_ s53,s51,36864
Tv 42,v5,12288
Tv 44,v5,18432
Mavv 4,30,120
v_add_u32 v12, s53, v13
s_mul_i32 s53, s4, 10240
Mavv 8,30,124
v_add_u32 v13, s53, v13
A_ v46,v46,v76
Mavv 28,34,124
A_ v47,v47,v80
Mavv 32,34,128
A_ v48,v48,v84
A_ v49,v49,v88
Mavv 12,30,128
A_ v50,v50,v92
Mavv 16,30,132
v_rcp_f32 v46, v46
Mavv 36,34,132
W_ 0
v_rcp_f32 v47, v47
Mavv 56,38,132
s_nop 0
Tv 30,v2,13312
Tv 32,v2,19456
Mavv 76,42,132
v_rcp_f32 v48, v48
Mavv 72,42,128
s_nop 0
Tv 34,v3,13312
Tv 36,v3,19456
Mavv 52,38,128
v_rcp_f32 v49, v49
Mavv 48,38,124
v_rcp_f32 v50, v50
Mavv 68,42,124
s_mov_b32 s56, 12288
v_mov_b32 v176, v20
v_mov_b32 v177, v21
Mavv 64,42,120
Dv 136,v13,0
v_mov_b32 v178, v22
Mavv 44,38,120
Dv 140,v13,1024
v_mov_b32 v179, v23
Mavv 40,38,116
Dv 144,v13,2048
v_mov_b32 v180, v24
Mavv 60,42,116
W_ 3
Dv 148,v13,3072
v_mov_b32 v181, v25
Mavv 80,30,116
s_nop 0
Tv 38,v4,13312
Tv 40,v4,19456
Mavv 100,34,116
Dv 152,v13,4096
v_mov_b32 v182, v26
Mavv 104,34,120
s_nop 0
Tv 42,v5,13312
Tv 44,v5,19456
Mavv 84,30,120
Dv 156,v13,5120
v_mov_b32 v183, v27
Mavv 88,30,124
Dv 160,v13,6144
v_mov_b32 v184, v52
Mavv 108,34,124
Dv 164,v13,7168
v_mov_b32 v185, v53
Mavv 112,34,128
Dv 168,v13,8192
v_mov_b32 v186, v54
Mavv 92,30,128
Dv 172,v13,9216
v_mov_b32 v187, v55
Mavv 96,30,132
Dv 208,v12,0
R_ v188,a240
Mavv 116,34,132
W_ 6
Dv 212,v12,1024
R_ v189,a241
Mavv 136,38,132
s_nop 0
Tv 30,v2,14336
Tv 32,v2,20480
Mavv 156,42,132
Dv 216,v12,2048
R_ v190,a242
Mavv 152,42,128
s_nop 0
Tv 34,v3,14336
Tv 36,v3,20480
Mavv 132,38,128
Dv 220,v12,3072
R_ v191,a243
Mavv 128,38,124
Dv 224,v12,4096
R_ v192,a244
Mavv 148,42,124
Dv 228,v12,5120
R_ v193,a245
Mavv 144,42,120
Dv 232,v12,12288
R_ v194,a246
Mavv 124,38,120
Dv 236,v12,13312
R_ v195,a247
Mavv 120,38,116
Dv 240,v12,14336
v_mov_b32 v196, v16
Mavv 140,42,116
W_ 6
Dv 244,v12,15360
v_mov_b32 v197, v17
Mavv 160,30,116
s_nop 0
Tv 38,v4,14336
Tv 40,v4,20480
Mavv 180,34,116
Dv 248,v12,16384
v_mov_b32 v198, v18
Mavv 184,34,120
s_nop 0
Tv 42,v5,14336
Tv 44,v5,20480
Mavv 164,30,120
Dv 252,v12,17408
v_mov_b32 v199, v19
Mavv 168,30,124
v_mov_b32 v200, v28
v_mov_b32 v201, v29
Mavv 188,34,124
v_mov_b32 v202, v51
v_mov_b32 v203, v6
Mavv 192,34,128
v_mov_b32 v204, v14
v_mov_b32 v205, v15
Mavv 172,30,128
S_ s65,s61,405504
R_ v206,a249
R_ v207,a250
Mavv 176,30,132
S_ m0,s51,12288
R_ v96,a0
R_ v97,a1
Mavv 196,34,132
s_waitcnt vmcnt(63) lgkmcnt(0)
s_barrier
R_ v98,a2
Mavv 216,38,132
Ln v7,32,s65,0
Mavv 236,42,132
R_ v99,a3
Mavv 232,42,128
Ln v8,32,s65,3072
S_ m0,s51,13184
Mavv 212,38,128
R_ v100,a20
R_ v101,a21
Mavv 208,38,124
Ln v7,32,s65,128
Mavv 228,42,124
R_ v102,a22
R_ v103,a23
Mavv 224,42,120
Ln v8,32,s65,3200
S_ m0,s51,14080
Mavv 204,38,120
U_ v96,v96,v46
U_ v97,v97,v46
Mavv 200,38,116
Ln v7,32,s65,256
Mavv 220,42,116
U_ v98,v98,v46
U_ v99,v99,v46
U_ v100,v100,v46
U_ v101,v101,v46
U_ v102,v102,v46
U_ v103,v103,v46
C_ 76,96,97
C_ 77,98,99
C_ 78,100,101
C_ 79,102,103
ds_write_b128 v13, v[76:79] offset:0
Ln v8,32,s65,3328
S_ s61,s61,393216
.p2align 3
R_ v104,a4
R_ v105,a5
R_ v106,a6
R_ v107,a7
R_ v108,a24
R_ v109,a25
R_ v110,a26
R_ v111,a27
v_pk_mul_f32 v[104:105], v[104:105], v[46:47] op_sel:[0,1]
v_pk_mul_f32 v[106:107], v[106:107], v[46:47] op_sel:[0,1]
v_pk_mul_f32 v[108:109], v[108:109], v[46:47] op_sel:[0,1]
v_pk_mul_f32 v[110:111], v[110:111], v[46:47] op_sel:[0,1]
C_ 80,104,105
C_ 81,106,107
C_ 82,108,109
C_ 83,110,111
ds_write_b128 v13, v[80:83] offset:1024
.p2align 3
R_ v96,a8
R_ v97,a9
R_ v98,a10
R_ v99,a11
R_ v100,a28
R_ v101,a29
R_ v102,a30
R_ v103,a31
v_pk_mul_f32 v[96:97], v[96:97], v[48:49] op_sel_hi:[1,0]
v_pk_mul_f32 v[98:99], v[98:99], v[48:49] op_sel_hi:[1,0]
v_pk_mul_f32 v[100:101], v[100:101], v[48:49] op_sel_hi:[1,0]
v_pk_mul_f32 v[102:103], v[102:103], v[48:49] op_sel_hi:[1,0]
C_ 84,96,97
C_ 85,98,99
C_ 86,100,101
C_ 87,102,103
ds_write_b128 v13, v[84:87] offset:2048
.p2align 3
R_ v104,a12
R_ v105,a13
R_ v106,a14
R_ v107,a15
R_ v108,a32
R_ v109,a33
R_ v110,a34
R_ v111,a35
v_pk_mul_f32 v[104:105], v[104:105], v[48:49] op_sel:[0,1]
v_pk_mul_f32 v[106:107], v[106:107], v[48:49] op_sel:[0,1]
v_pk_mul_f32 v[108:109], v[108:109], v[48:49] op_sel:[0,1]
v_pk_mul_f32 v[110:111], v[110:111], v[48:49] op_sel:[0,1]
C_ 88,104,105
C_ 89,106,107
C_ 90,108,109
C_ 91,110,111
ds_write_b128 v13, v[88:91] offset:3072
.p2align 3
R_ v96,a16
R_ v97,a17
R_ v98,a18
R_ v99,a19
R_ v100,a36
R_ v101,a37
R_ v102,a38
R_ v103,a39
v_pk_mul_f32 v[96:97], v[96:97], v[50:51] op_sel_hi:[1,0]
v_pk_mul_f32 v[98:99], v[98:99], v[50:51] op_sel_hi:[1,0]
v_pk_mul_f32 v[100:101], v[100:101], v[50:51] op_sel_hi:[1,0]
v_pk_mul_f32 v[102:103], v[102:103], v[50:51] op_sel_hi:[1,0]
C_ 92,96,97
C_ 93,98,99
C_ 94,100,101
C_ 95,102,103
ds_write_b128 v13, v[92:95] offset:4096
.p2align 3
R_ v104,a40
R_ v105,a41
R_ v106,a42
R_ v107,a43
R_ v108,a60
R_ v109,a61
R_ v110,a62
R_ v111,a63
v_pk_mul_f32 v[104:105], v[104:105], v[46:47] op_sel_hi:[1,0]
v_pk_mul_f32 v[106:107], v[106:107], v[46:47] op_sel_hi:[1,0]
v_pk_mul_f32 v[108:109], v[108:109], v[46:47] op_sel_hi:[1,0]
v_pk_mul_f32 v[110:111], v[110:111], v[46:47] op_sel_hi:[1,0]
C_ 76,104,105
C_ 77,106,107
C_ 78,108,109
C_ 79,110,111
ds_write_b128 v13, v[76:79] offset:5120
.p2align 3
R_ v96,a44
R_ v97,a45
R_ v98,a46
R_ v99,a47
R_ v100,a64
R_ v101,a65
R_ v102,a66
R_ v103,a67
v_pk_mul_f32 v[96:97], v[96:97], v[46:47] op_sel:[0,1]
v_pk_mul_f32 v[98:99], v[98:99], v[46:47] op_sel:[0,1]
v_pk_mul_f32 v[100:101], v[100:101], v[46:47] op_sel:[0,1]
v_pk_mul_f32 v[102:103], v[102:103], v[46:47] op_sel:[0,1]
C_ 80,96,97
C_ 81,98,99
C_ 82,100,101
C_ 83,102,103
ds_write_b128 v13, v[80:83] offset:6144
.p2align 3
R_ v104,a48
R_ v105,a49
R_ v106,a50
R_ v107,a51
R_ v108,a68
R_ v109,a69
R_ v110,a70
R_ v111,a71
v_pk_mul_f32 v[104:105], v[104:105], v[48:49] op_sel_hi:[1,0]
v_pk_mul_f32 v[106:107], v[106:107], v[48:49] op_sel_hi:[1,0]
v_pk_mul_f32 v[108:109], v[108:109], v[48:49] op_sel_hi:[1,0]
v_pk_mul_f32 v[110:111], v[110:111], v[48:49] op_sel_hi:[1,0]
C_ 84,104,105
C_ 85,106,107
C_ 86,108,109
C_ 87,110,111
ds_write_b128 v13, v[84:87] offset:7168
.p2align 3
R_ v96,a52
R_ v97,a53
R_ v98,a54
R_ v99,a55
R_ v100,a72
R_ v101,a73
R_ v102,a74
R_ v103,a75
v_pk_mul_f32 v[96:97], v[96:97], v[48:49] op_sel:[0,1]
v_pk_mul_f32 v[98:99], v[98:99], v[48:49] op_sel:[0,1]
v_pk_mul_f32 v[100:101], v[100:101], v[48:49] op_sel:[0,1]
v_pk_mul_f32 v[102:103], v[102:103], v[48:49] op_sel:[0,1]
C_ 88,96,97
C_ 89,98,99
C_ 90,100,101
C_ 91,102,103
ds_write_b128 v13, v[88:91] offset:8192
.p2align 3
R_ v104,a56
R_ v105,a57
R_ v106,a58
R_ v107,a59
R_ v108,a76
R_ v109,a77
R_ v110,a78
R_ v111,a79
v_pk_mul_f32 v[104:105], v[104:105], v[50:51] op_sel_hi:[1,0]
v_pk_mul_f32 v[106:107], v[106:107], v[50:51] op_sel_hi:[1,0]
v_pk_mul_f32 v[108:109], v[108:109], v[50:51] op_sel_hi:[1,0]
v_pk_mul_f32 v[110:111], v[110:111], v[50:51] op_sel_hi:[1,0]
C_ 92,104,105
C_ 93,106,107
C_ 94,108,109
C_ 95,110,111
ds_write_b128 v13, v[92:95] offset:9216
.p2align 3
R_ v104,a92
R_ v105,a93
R_ v106,a94
R_ v107,a95
R_ v108,a112
R_ v109,a113
R_ v110,a114
R_ v111,a115
v_pk_mul_f32 v[104:105], v[104:105], v[48:49] op_sel:[0,1]
v_pk_mul_f32 v[106:107], v[106:107], v[48:49] op_sel:[0,1]
v_pk_mul_f32 v[108:109], v[108:109], v[48:49] op_sel:[0,1]
v_pk_mul_f32 v[110:111], v[110:111], v[48:49] op_sel:[0,1]
C_ 88,104,105
C_ 89,106,107
C_ 90,108,109
C_ 91,110,111
X_ a240,v88
X_ a241,v89
X_ a242,v90
X_ a243,v91
.p2align 3
R_ v96,a96
R_ v97,a97
R_ v98,a98
R_ v99,a99
R_ v100,a116
R_ v101,a117
R_ v102,a118
R_ v103,a119
v_pk_mul_f32 v[96:97], v[96:97], v[50:51] op_sel_hi:[1,0]
v_pk_mul_f32 v[98:99], v[98:99], v[50:51] op_sel_hi:[1,0]
v_pk_mul_f32 v[100:101], v[100:101], v[50:51] op_sel_hi:[1,0]
v_pk_mul_f32 v[102:103], v[102:103], v[50:51] op_sel_hi:[1,0]
C_ 92,96,97
C_ 93,98,99
C_ 94,100,101
C_ 95,102,103
X_ a244,v92
X_ a245,v93
X_ a246,v94
X_ a247,v95
.p2align 3
R_ v96,a124
R_ v97,a125
R_ v98,a126
R_ v99,a127
R_ v100,a144
R_ v101,a145
R_ v102,a146
R_ v103,a147
v_pk_mul_f32 v[96:97], v[96:97], v[46:47] op_sel:[0,1]
v_pk_mul_f32 v[98:99], v[98:99], v[46:47] op_sel:[0,1]
v_pk_mul_f32 v[100:101], v[100:101], v[46:47] op_sel:[0,1]
v_pk_mul_f32 v[102:103], v[102:103], v[46:47] op_sel:[0,1]
C_ 80,96,97
C_ 81,98,99
C_ 82,100,101
C_ 83,102,103
v_mov_b32 v28, v80
v_mov_b32 v29, v81
v_mov_b32 v51, v82
v_mov_b32 v6, v83
.p2align 3
R_ v104,a128
R_ v105,a129
R_ v106,a130
R_ v107,a131
R_ v108,a148
R_ v109,a149
R_ v110,a150
R_ v111,a151
v_pk_mul_f32 v[104:105], v[104:105], v[48:49] op_sel_hi:[1,0]
v_pk_mul_f32 v[106:107], v[106:107], v[48:49] op_sel_hi:[1,0]
v_pk_mul_f32 v[108:109], v[108:109], v[48:49] op_sel_hi:[1,0]
v_pk_mul_f32 v[110:111], v[110:111], v[48:49] op_sel_hi:[1,0]
C_ 84,104,105
C_ 85,106,107
C_ 86,108,109
C_ 87,110,111
v_mov_b32 v14, v84
v_mov_b32 v15, v85
X_ a249,v86
X_ a250,v87
.p2align 3
R_ v96,a132
R_ v97,a133
R_ v98,a134
R_ v99,a135
R_ v100,a152
R_ v101,a153
R_ v102,a154
R_ v103,a155
v_pk_mul_f32 v[96:97], v[96:97], v[48:49] op_sel:[0,1]
v_pk_mul_f32 v[98:99], v[98:99], v[48:49] op_sel:[0,1]
v_pk_mul_f32 v[100:101], v[100:101], v[48:49] op_sel:[0,1]
v_pk_mul_f32 v[102:103], v[102:103], v[48:49] op_sel:[0,1]
C_ 88,96,97
C_ 89,98,99
C_ 90,100,101
C_ 91,102,103
ds_write_b128 v12, v[88:91] offset:0
.p2align 3
R_ v104,a136
R_ v105,a137
R_ v106,a138
R_ v107,a139
R_ v108,a156
R_ v109,a157
R_ v110,a158
R_ v111,a159
v_pk_mul_f32 v[104:105], v[104:105], v[50:51] op_sel_hi:[1,0]
v_pk_mul_f32 v[106:107], v[106:107], v[50:51] op_sel_hi:[1,0]
v_pk_mul_f32 v[108:109], v[108:109], v[50:51] op_sel_hi:[1,0]
v_pk_mul_f32 v[110:111], v[110:111], v[50:51] op_sel_hi:[1,0]
C_ 92,104,105
C_ 93,106,107
C_ 94,108,109
C_ 95,110,111
ds_write_b128 v12, v[92:95] offset:1024
.p2align 3
R_ v96,a160
R_ v97,a161
R_ v98,a162
R_ v99,a163
R_ v100,a180
R_ v101,a181
R_ v102,a182
R_ v103,a183
v_pk_mul_f32 v[96:97], v[96:97], v[46:47] op_sel_hi:[1,0]
v_pk_mul_f32 v[98:99], v[98:99], v[46:47] op_sel_hi:[1,0]
v_pk_mul_f32 v[100:101], v[100:101], v[46:47] op_sel_hi:[1,0]
v_pk_mul_f32 v[102:103], v[102:103], v[46:47] op_sel_hi:[1,0]
C_ 76,96,97
C_ 77,98,99
C_ 78,100,101
C_ 79,102,103
ds_write_b128 v12, v[76:79] offset:2048
.p2align 3
R_ v104,a164
R_ v105,a165
R_ v106,a166
R_ v107,a167
R_ v108,a184
R_ v109,a185
R_ v110,a186
R_ v111,a187
v_pk_mul_f32 v[104:105], v[104:105], v[46:47] op_sel:[0,1]
v_pk_mul_f32 v[106:107], v[106:107], v[46:47] op_sel:[0,1]
v_pk_mul_f32 v[108:109], v[108:109], v[46:47] op_sel:[0,1]
v_pk_mul_f32 v[110:111], v[110:111], v[46:47] op_sel:[0,1]
C_ 80,104,105
C_ 81,106,107
C_ 82,108,109
C_ 83,110,111
ds_write_b128 v12, v[80:83] offset:3072
.p2align 3
R_ v96,a168
R_ v97,a169
R_ v98,a170
R_ v99,a171
R_ v100,a188
R_ v101,a189
R_ v102,a190
R_ v103,a191
v_pk_mul_f32 v[96:97], v[96:97], v[48:49] op_sel_hi:[1,0]
v_pk_mul_f32 v[98:99], v[98:99], v[48:49] op_sel_hi:[1,0]
v_pk_mul_f32 v[100:101], v[100:101], v[48:49] op_sel_hi:[1,0]
v_pk_mul_f32 v[102:103], v[102:103], v[48:49] op_sel_hi:[1,0]
C_ 84,96,97
C_ 85,98,99
C_ 86,100,101
C_ 87,102,103
ds_write_b128 v12, v[84:87] offset:4096
.p2align 3
R_ v104,a172
R_ v105,a173
R_ v106,a174
R_ v107,a175
R_ v108,a192
R_ v109,a193
R_ v110,a194
R_ v111,a195
v_pk_mul_f32 v[104:105], v[104:105], v[48:49] op_sel:[0,1]
v_pk_mul_f32 v[106:107], v[106:107], v[48:49] op_sel:[0,1]
v_pk_mul_f32 v[108:109], v[108:109], v[48:49] op_sel:[0,1]
v_pk_mul_f32 v[110:111], v[110:111], v[48:49] op_sel:[0,1]
C_ 88,104,105
C_ 89,106,107
C_ 90,108,109
C_ 91,110,111
ds_write_b128 v12, v[88:91] offset:5120
.p2align 3
R_ v96,a176
R_ v97,a177
R_ v98,a178
R_ v99,a179
R_ v100,a196
R_ v101,a197
R_ v102,a198
R_ v103,a199
v_pk_mul_f32 v[96:97], v[96:97], v[50:51] op_sel_hi:[1,0]
v_pk_mul_f32 v[98:99], v[98:99], v[50:51] op_sel_hi:[1,0]
v_pk_mul_f32 v[100:101], v[100:101], v[50:51] op_sel_hi:[1,0]
v_pk_mul_f32 v[102:103], v[102:103], v[50:51] op_sel_hi:[1,0]
C_ 92,96,97
C_ 93,98,99
C_ 94,100,101
C_ 95,102,103
ds_write_b128 v12, v[92:95] offset:12288
.p2align 3
R_ v104,a200
R_ v105,a201
R_ v106,a202
R_ v107,a203
R_ v108,a220
R_ v109,a221
R_ v110,a222
R_ v111,a223
v_pk_mul_f32 v[104:105], v[104:105], v[46:47] op_sel_hi:[1,0]
v_pk_mul_f32 v[106:107], v[106:107], v[46:47] op_sel_hi:[1,0]
v_pk_mul_f32 v[108:109], v[108:109], v[46:47] op_sel_hi:[1,0]
v_pk_mul_f32 v[110:111], v[110:111], v[46:47] op_sel_hi:[1,0]
C_ 76,104,105
C_ 77,106,107
C_ 78,108,109
C_ 79,110,111
ds_write_b128 v12, v[76:79] offset:13312
.p2align 3
R_ v96,a204
R_ v97,a205
R_ v98,a206
R_ v99,a207
R_ v100,a224
R_ v101,a225
R_ v102,a226
R_ v103,a227
v_pk_mul_f32 v[96:97], v[96:97], v[46:47] op_sel:[0,1]
v_pk_mul_f32 v[98:99], v[98:99], v[46:47] op_sel:[0,1]
v_pk_mul_f32 v[100:101], v[100:101], v[46:47] op_sel:[0,1]
v_pk_mul_f32 v[102:103], v[102:103], v[46:47] op_sel:[0,1]
C_ 80,96,97
C_ 81,98,99
C_ 82,100,101
C_ 83,102,103
ds_write_b128 v12, v[80:83] offset:14336
.p2align 3
R_ v104,a208
R_ v105,a209
R_ v106,a210
R_ v107,a211
R_ v108,a228
R_ v109,a229
R_ v110,a230
R_ v111,a231
v_pk_mul_f32 v[104:105], v[104:105], v[48:49] op_sel_hi:[1,0]
v_pk_mul_f32 v[106:107], v[106:107], v[48:49] op_sel_hi:[1,0]
v_pk_mul_f32 v[108:109], v[108:109], v[48:49] op_sel_hi:[1,0]
v_pk_mul_f32 v[110:111], v[110:111], v[48:49] op_sel_hi:[1,0]
C_ 84,104,105
C_ 85,106,107
C_ 86,108,109
C_ 87,110,111
ds_write_b128 v12, v[84:87] offset:15360
s_waitcnt vmcnt(6) lgkmcnt(0)
s_barrier
Dv 30,%0,6144
Dv 34,v1,6144
Dv 38,%0,7168
.p2align 3
R_ v96,a212
R_ v97,a213
R_ v98,a214
R_ v99,a215
R_ v100,a232
R_ v101,a233
R_ v102,a234
R_ v103,a235
v_pk_mul_f32 v[96:97], v[96:97], v[48:49] op_sel:[0,1]
v_pk_mul_f32 v[98:99], v[98:99], v[48:49] op_sel:[0,1]
v_pk_mul_f32 v[100:101], v[100:101], v[48:49] op_sel:[0,1]
v_pk_mul_f32 v[102:103], v[102:103], v[48:49] op_sel:[0,1]
C_ 88,96,97
C_ 89,98,99
C_ 90,100,101
C_ 91,102,103
ds_write_b128 v12, v[88:91] offset:16384
.p2align 3
R_ v104,a216
R_ v105,a217
R_ v106,a218
R_ v107,a219
R_ v108,a236
R_ v109,a237
R_ v110,a238
R_ v111,a239
v_pk_mul_f32 v[104:105], v[104:105], v[50:51] op_sel_hi:[1,0]
v_pk_mul_f32 v[106:107], v[106:107], v[50:51] op_sel_hi:[1,0]
v_pk_mul_f32 v[108:109], v[108:109], v[50:51] op_sel_hi:[1,0]
v_pk_mul_f32 v[110:111], v[110:111], v[50:51] op_sel_hi:[1,0]
C_ 92,104,105
C_ 93,106,107
C_ 94,108,109
C_ 95,110,111
ds_write_b128 v12, v[92:95] offset:17408
.Latt3:
W_ 4
Zvvv 96,30,136
Dv 42,v1,7168
R_ v116,a80
Zvvv 100,30,140
R_ v117,a81
R_ v118,a82
Zvvv 104,30,144
R_ v119,a83
R_ v120,a100
Zvvv 108,30,148
R_ v121,a101
R_ v122,a102
Zvvv 112,30,152
W_ 4
R_ v123,a103
U_ v116,v116,v46
Mvvv 96,34,156
Dv 30,%0,8192
U_ v117,v117,v46
Mvvv 100,34,160
U_ v118,v118,v46
U_ v119,v119,v46
Mvvv 104,34,164
U_ v120,v120,v46
U_ v121,v121,v46
Mvvv 108,34,168
U_ v122,v122,v46
U_ v123,v123,v46
Mvvv 112,34,172
W_ 4
C_ 20,116,117
C_ 21,118,119
Mvvv 96,38,176
Dv 34,v1,8192
C_ 22,120,121
Mvvv 100,38,180
C_ 23,122,123
R_ v124,a84
Mvvv 104,38,184
R_ v125,a85
R_ v126,a86
Mvvv 108,38,188
R_ v127,a87
R_ v128,a104
Mvvv 112,38,192
W_ 2
R_ v129,a105
R_ v130,a106
Mvvv 96,42,196
Dv 38,%0,0
R_ v131,a107
Mvvv 100,42,200
U_ v124,v124,v47
U_ v125,v125,v47
Mvvv 104,42,204
U_ v126,v126,v47
U_ v127,v127,v47
Mvvv 108,42,208
U_ v128,v128,v47
U_ v129,v129,v47
Mvvv 112,42,212
W_ 2
U_ v130,v130,v47
U_ v131,v131,v47
Mvvv 96,30,216
Dv 42,v1,0
C_ 24,124,125
Mvvv 100,30,220
C_ 25,126,127
C_ 26,128,129
Mvvv 104,30,224
C_ 27,130,131
R_ v116,a88
Mvvv 108,30,228
R_ v117,a89
R_ v118,a90
Mvvv 112,30,232
W_ 2
R_ v119,a91
R_ v120,a108
Mvvv 96,34,236
Dv 30,%0,1024
R_ v121,a109
Mvvv 100,34,240
R_ v122,a110
R_ v123,a111
Mvvv 104,34,244
U_ v116,v116,v48
U_ v117,v117,v48
Mvvv 108,34,248
U_ v118,v118,v48
U_ v119,v119,v48
Mvvv 112,34,252
W_ 2
U_ v120,v120,v48
U_ v121,v121,v48
Zvvv 76,38,136
Dv 34,v1,1024
U_ v122,v122,v48
Zvvv 80,38,140
U_ v123,v123,v48
C_ 52,116,117
Zvvv 84,38,144
C_ 53,118,119
C_ 54,120,121
Zvvv 88,38,148
C_ 55,122,123
R_ v124,a120
Zvvv 92,38,152
W_ 2
R_ v125,a121
R_ v126,a122
Mvvv 76,42,156
Dv 38,%0,2048
R_ v127,a123
Mvvv 80,42,160
R_ v128,a140
R_ v129,a141
Mvvv 84,42,164
R_ v130,a142
R_ v131,a143
Mvvv 88,42,168
U_ v124,v124,v46
U_ v125,v125,v46
Mvvv 92,42,172
W_ 2
U_ v126,v126,v46
U_ v127,v127,v46
Mvvv 76,30,176
Dv 42,v1,2048
U_ v128,v128,v46
Mvvv 80,30,180
U_ v129,v129,v46
U_ v130,v130,v46
Mvvv 84,30,184
U_ v131,v131,v46
C_ 16,124,125
Mvvv 88,30,188
C_ 17,126,127
C_ 18,128,129
Mvvv 92,30,192
s_waitcnt vmcnt(0) lgkmcnt(0)
s_barrier
Mvvv 76,34,196
S_ s65,s61,24576
Dv 30,%0,18432
C_ 19,130,131
Mvvv 76,38,216
S_ m0,s51,0
v_max3_f32 v56, v96, v97, v98
v_max_f32 v56, v56, v99
Mvvv 76,42,236
Ln v7,32,s65,0
Mvvv 80,34,200
v_max3_f32 v60, v100, v101, v102
v_max_f32 v60, v60, v103
Mvvv 80,38,220
Ln v8,32,s65,3072
Mvvv 80,42,240
S_ m0,s51,896
v_max3_f32 v64, v104, v105, v106
v_max_f32 v64, v64, v107
Mvvv 84,34,204
Ln v7,32,s65,128
Mvvv 84,38,224
v_max3_f32 v68, v108, v109, v110
v_max_f32 v68, v68, v111
Mvvv 84,42,244
Ln v8,32,s65,3200
Mvvv 88,34,208
S_ m0,s51,1792
v_max3_f32 v72, v112, v113, v114
v_max_f32 v72, v72, v115
Mvvv 88,38,228
Ln v7,32,s65,256
Mvvv 88,42,248
v_max3_f32 v56, v56, v76, v77
v_max3_f32 v56, v56, v78, v79
Mvvv 92,34,212
Ln v8,32,s65,3328
Mvvv 92,38,232
v_max3_f32 v60, v60, v80, v81
v_max3_f32 v60, v60, v82, v83
Mvvv 92,42,252
v_max3_f32 v64, v64, v84, v85
v_max3_f32 v64, v64, v86, v87
Dv 34,v1,18432
.p2align 3
v_mov_b32 v11, 0x3f803f80
v_permlane32_swap_b32 v56, v60
v_max3_f32 v68, v68, v88, v89
v_max3_f32 v68, v68, v90, v91
v_max_f32 v56, v56, v60
v_max3_f32 v72, v72, v92, v93
v_max3_f32 v72, v72, v94, v95
v_mov_b32 v73, v72
v_permlane32_swap_b32 v64, v68
v_max_f32 v64, v64, v68
v_permlane32_swap_b32 v72, v73
v_max_f32 v72, v72, v73
v_mov_b32 v73, v72
v_permlane16_swap_b32 v56, v64
v_max_f32 v56, v56, v64
v_permlane16_swap_b32 v72, v73
B_ v56,0xc0e00000,v56
v_max_f32 v72, v72, v73
v_mov_b32 v64, v56
B_ v72,0xc0e00000,v72
v_mov_b32 v73, v72
v_permlane16_swap_b32 v56, v64
v_mov_b32 v60, v56
v_mov_b32 v68, v64
v_mov_b64 v[74:75], v[72:73]
v_permlane32_swap_b32 v56, v60
v_permlane32_swap_b32 v64, v68
v_mov_b32 v57, v56
v_mov_b32 v61, v60
v_mov_b32 v65, v64
v_mov_b32 v69, v68
v_pk_add_f32 v[96:97], v[96:97], v[56:57]
v_pk_add_f32 v[98:99], v[98:99], v[56:57]
v_pk_add_f32 v[76:77], v[76:77], v[56:57]
v_pk_add_f32 v[78:79], v[78:79], v[56:57]
v_mov_b64 v[58:59], v[56:57]
v_pk_add_f32 v[100:101], v[100:101], v[60:61]
v_pk_add_f32 v[102:103], v[102:103], v[60:61]
v_pk_add_f32 v[80:81], v[80:81], v[60:61]
v_pk_add_f32 v[82:83], v[82:83], v[60:61]
v_mov_b64 v[62:63], v[60:61]
v_pk_add_f32 v[104:105], v[104:105], v[64:65]
v_pk_add_f32 v[106:107], v[106:107], v[64:65]
v_pk_add_f32 v[84:85], v[84:85], v[64:65]
v_pk_add_f32 v[86:87], v[86:87], v[64:65]
v_mov_b64 v[66:67], v[64:65]
v_pk_add_f32 v[108:109], v[108:109], v[68:69]
v_pk_add_f32 v[110:111], v[110:111], v[68:69]
v_pk_add_f32 v[88:89], v[88:89], v[68:69]
v_pk_add_f32 v[90:91], v[90:91], v[68:69]
v_mov_b64 v[70:71], v[68:69]
v_pk_add_f32 v[112:113], v[112:113], v[72:73]
v_pk_add_f32 v[114:115], v[114:115], v[72:73]
v_pk_add_f32 v[92:93], v[92:93], v[72:73]
v_pk_add_f32 v[94:95], v[94:95], v[72:73]
E_ 96
E_ 97
E_ 98
E_ 99
E_ 76
E_ 77
E_ 78
E_ 79
C_ 99,98,99
C_ 98,96,97
C_ 96,76,77
C_ 97,78,79
E_ 100
E_ 101
E_ 102
E_ 103
E_ 80
E_ 81
E_ 82
E_ 83
C_ 103,102,103
C_ 102,100,101
C_ 100,80,81
C_ 101,82,83
.Lstep3_1:
W_ 1
Nvvv 116,30,136,56
s_nop 0
Dv 38,%0,19456
Dv 42,v1,19456
W_ 2
Mvvv 116,34,156
E_ 104
Nvvv 120,30,140,60
E_ 105
Mvvv 120,34,160
E_ 106
Nvvv 124,30,144,64
E_ 107
Mvvv 124,34,164
E_ 84
Nvvv 128,30,148,68
E_ 85
Mvvv 128,34,168
E_ 86
Nvvv 132,30,152,72
E_ 87
Mvvv 132,34,172
W_ 0
C_ 107,106,107
C_ 106,104,105
Mvvv 132,38,192
s_nop 0
Dv 30,%0,20480
Dv 34,v1,20480
Mvvv 132,42,212
C_ 104,84,85
C_ 105,86,87
Mvvv 128,38,188
E_ 108
Mvvv 128,42,208
E_ 109
Mvvv 124,38,184
E_ 110
Mvvv 124,42,204
E_ 111
Mvvv 120,38,180
E_ 88
Mvvv 120,42,200
E_ 89
Mvvv 116,38,176
E_ 90
Mvvv 116,42,196
W_ 0
E_ 91
Mvvv 116,30,216
s_nop 0
Dv 38,%0,12288
Dv 42,v1,12288
Mvvv 116,34,236
C_ 111,110,111
C_ 110,108,109
Mvvv 120,30,220
C_ 108,88,89
C_ 109,90,91
Mvvv 120,34,240
E_ 112
Mvvv 124,30,224
E_ 113
Mvvv 124,34,244
E_ 114
Mvvv 128,30,228
E_ 115
Mvvv 128,34,248
E_ 92
Mvvv 132,30,232
E_ 93
Mvvv 132,34,252
W_ 0
E_ 94
Nvvv 76,38,136,56
s_nop 0
Dv 30,%0,13312
Dv 34,v1,13312
Mvvv 76,42,156
E_ 95
Nvvv 80,38,140,60
C_ 115,114,115
C_ 114,112,113
Mvvv 80,42,160
C_ 112,92,93
C_ 113,94,95
Nvvv 84,38,144,64
E_ 116
Mvvv 84,42,164
E_ 117
Nvvv 88,38,148,68
E_ 118
Mvvv 88,42,168
E_ 119
Nvvv 92,38,152,72
C_ 119,118,119
C_ 118,116,117
Mvvv 92,42,172
W_ 0
E_ 120
Mvvv 92,30,192
s_nop 0
Dv 38,%0,14336
Dv 42,v1,14336
Mvvv 92,34,212
X_ a180,v11
X_ a181,v11
Mvvv 88,30,188
X_ a182,v11
X_ a183,v11
Mvvv 88,34,208
E_ 121
Mvvv 84,30,184
E_ 122
Mvvv 84,34,204
E_ 123
Mvvv 80,30,180
C_ 123,122,123
C_ 122,120,121
Mvvv 80,34,200
E_ 124
Mvvv 76,30,176
E_ 125
Mvvv 76,34,196
S_ s65,s61,12288
E_ 126
W_ 1
Mvvv 76,38,216
S_ m0,s51,12288
E_ 127
W_ 0
Mvvv 76,42,236
s_waitcnt vmcnt(18) lgkmcnt(0)
s_barrier
C_ 127,126,127
Mvvv 80,38,220
L_ v9,36,s65,0
Mvvv 80,42,240
C_ 126,124,125
Mvvv 84,38,224
L_ v9,36,s65,3072
S_ m0,s51,13184
Mvvv 84,42,244
s_nop 0
Tv 30,v2,24576
Tv 32,v2,30720
Mvvv 88,38,228
L_ v9,36,s65,128
Mvvv 88,42,248
s_nop 0
Tv 34,v3,24576
Tv 36,v3,30720
Mvvv 92,38,232
L_ v9,36,s65,3200
S_ m0,s51,14080
Mvvv 92,42,252
E_ 128
Zaav 220,180,96
L_ v9,36,s65,256
Zaav 224,180,100
E_ 129
Zaav 228,180,104
L_ v9,36,s65,3328
Zaav 232,180,108
E_ 130
Zaav 236,180,112
W_ 0
E_ 131
Zavv 0,30,96
s_nop 0
Tv 38,v4,24576
Tv 40,v4,30720
Zavv 20,34,96
C_ 131,130,131
C_ 130,128,129
Zavv 24,34,100
s_nop 0
Tv 42,v5,24576
Tv 44,v5,30720
Zavv 4,30,100
E_ 132
Zavv 8,30,104
E_ 133
Zavv 28,34,104
R_ v46,a220
R_ v47,a224
Zavv 32,34,108
R_ v48,a228
R_ v49,a232
Zavv 12,30,108
R_ v50,a236
Zavv 16,30,112
E_ 134
Zavv 36,34,112
W_ 0
E_ 135
Zavv 56,38,112
s_nop 0
Tv 30,v2,25600
Tv 32,v2,31744
Zavv 76,42,112
C_ 135,134,135
C_ 134,132,133
Zavv 72,42,108
s_nop 0
Tv 34,v3,25600
Tv 36,v3,31744
Zavv 52,38,108
E_ 76
Zavv 48,38,104
E_ 77
Zavv 68,42,104
E_ 78
Zavv 64,42,100
E_ 79
Zavv 44,38,100
C_ 116,76,77
C_ 117,78,79
Zavv 40,38,96
E_ 80
Zavv 60,42,96
W_ 0
E_ 81
Zavv 80,30,96
s_nop 0
Tv 38,v4,25600
Tv 40,v4,31744
Zavv 100,34,96
E_ 82
Zavv 104,34,100
s_nop 0
Tv 42,v5,25600
Tv 44,v5,31744
Zavv 84,30,100
E_ 83
Zavv 88,30,104
C_ 120,80,81
C_ 121,82,83
Zavv 108,34,104
E_ 84
Zavv 112,34,108
E_ 85
Zavv 92,30,108
E_ 86
Zavv 96,30,112
E_ 87
Zavv 116,34,112
W_ 0
C_ 124,84,85
C_ 125,86,87
Zavv 136,38,112
s_nop 0
Tv 30,v2,26624
Tv 32,v2,32768
Zavv 156,42,112
E_ 88
Zavv 152,42,108
s_nop 0
Tv 34,v3,26624
Tv 36,v3,32768
Zavv 132,38,108
E_ 89
Zavv 128,38,104
E_ 90
Zavv 148,42,104
E_ 91
Zavv 144,42,100
C_ 128,88,89
C_ 129,90,91
Zavv 124,38,100
E_ 92
Zavv 120,38,96
E_ 93
Zavv 140,42,96
W_ 0
E_ 94
Zavv 160,30,96
s_nop 0
Tv 38,v4,26624
Tv 40,v4,32768
Zavv 180,34,96
E_ 95
Zavv 184,34,100
s_nop 0
Tv 42,v5,26624
Tv 44,v5,32768
Zavv 164,30,100
C_ 132,92,93
C_ 133,94,95
Zavv 168,30,104
v_mov_b32 v92, 0x3f803f80
v_mov_b32 v93, 0x3f803f80
Zavv 188,34,104
S_ s65,s61,36864
v_mov_b32 v94, 0x3f803f80
v_mov_b32 v95, 0x3f803f80
Zavv 192,34,108
S_ m0,s51,24576
Zavv 172,30,108
s_waitcnt vmcnt(6) lgkmcnt(0)
s_barrier
Zavv 176,30,112
Ln v7,32,s65,0
Zavv 196,34,112
Zavv 216,38,112
Dv 30,%0,6144
Zavv 236,42,112
Dv 34,v1,6144
Zavv 232,42,108
Ln v8,32,s65,3072
S_ m0,s51,25472
Zavv 212,38,108
Zavv 208,38,104
Ln v7,32,s65,128
Zavv 228,42,104
Zavv 224,42,100
Ln v8,32,s65,3200
S_ m0,s51,26368
Zavv 204,38,100
Zavv 200,38,96
Ln v7,32,s65,256
Zavv 220,42,96
Ln v8,32,s65,3328
W_ 0
s_nop 7
s_branch .Lstep1_2
.Latt1:
R_ v13,a248
S_ s53,s51,36864
v_add_u32 v12, s53, v13
s_mul_i32 s53, s4, 10240
S_ s53,s53,0
v_add_u32 v13, s53, v13
W_ 2
Zvvv 96,30,136
Dv 42,v1,44032
R_ v2,a245
Zvvv 100,30,140
ds_write_b128 v13, a[0:3] offset:0
Zvvv 104,30,144
v_xor_b32 v3, 32, v2
v_xor_b32 v4, 64, v2
Zvvv 108,30,148
ds_write_b128 v13, a[4:7] offset:1024
Zvvv 112,30,152
W_ 4
v_xor_b32 v5, 96, v2
s_mov_b64 s[24:25], s[16:17]
Mvvv 96,34,156
s_mov_b32 s26, 7864320
Dv 30,%0,45056
s_mov_b64 s[28:29], s[22:23]
Mvvv 100,34,160
ds_write_b128 v13, a[8:11] offset:2048
Mvvv 104,34,164
s_mov_b32 s30, 1638400
R_ v20,a80
s_mul_i32 s52, s4, 10240
Mvvv 108,34,168
ds_write_b128 v13, a[12:15] offset:3072
Mvvv 112,34,172
W_ 6
R_ v21,a81
R_ v22,a82
Mvvv 96,38,176
Dv 34,v1,45056
R_ v23,a83
Mvvv 100,38,180
ds_write_b128 v13, a[16:19] offset:4096
Mvvv 104,38,184
R_ v24,a84
R_ v25,a85
Mvvv 108,38,188
ds_write_b128 v13, a[40:43] offset:5120
Mvvv 112,38,192
W_ 8
R_ v26,a86
R_ v27,a87
Mvvv 96,42,196
Dv 38,%0,36864
R_ v52,a88
Mvvv 100,42,200
ds_write_b128 v13, a[44:47] offset:6144
Mvvv 104,42,204
R_ v53,a89
R_ v54,a90
Mvvv 108,42,208
ds_write_b128 v13, a[48:51] offset:7168
Mvvv 112,42,212
W_ 8
R_ v55,a91
v_accvgpr_mov_b32 a240, a92
Mvvv 96,30,216
Dv 42,v1,36864
v_accvgpr_mov_b32 a241, a93
Mvvv 100,30,220
ds_write_b128 v13, a[52:55] offset:8192
Mvvv 104,30,224
v_accvgpr_mov_b32 a242, a94
v_accvgpr_mov_b32 a243, a95
Mvvv 108,30,228
ds_write_b128 v13, a[56:59] offset:9216
Mvvv 112,30,232
W_ 8
v_accvgpr_mov_b32 a244, a96
v_accvgpr_mov_b32 a245, a97
Mvvv 96,34,236
Dv 30,%0,37888
v_accvgpr_mov_b32 a246, a98
Mvvv 100,34,240
v_accvgpr_mov_b32 a247, a99
R_ v16,a120
Mvvv 104,34,244
R_ v17,a121
R_ v18,a122
Mvvv 108,34,248
R_ v19,a123
R_ v28,a124
Mvvv 112,34,252
W_ 6
R_ v29,a125
R_ v51,a126
Zvvv 76,38,136
Dv 34,v1,37888
R_ v6,a127
Zvvv 80,38,140
R_ v14,a128
R_ v15,a129
Zvvv 84,38,144
v_accvgpr_mov_b32 a249, a130
v_accvgpr_mov_b32 a250, a131
Zvvv 88,38,148
v_max3_f32 v56, v96, v97, v98
v_max_f32 v56, v56, v99
Zvvv 92,38,152
W_ 4
v_max3_f32 v60, v100, v101, v102
v_max_f32 v60, v60, v103
Mvvv 76,42,156
Dv 38,%0,38912
v_max3_f32 v64, v104, v105, v106
Mvvv 80,42,160
v_max_f32 v64, v64, v107
Mvvv 84,42,164
v_max3_f32 v68, v108, v109, v110
v_max_f32 v68, v68, v111
Mvvv 88,42,168
v_max3_f32 v72, v112, v113, v114
v_max_f32 v72, v72, v115
Mvvv 92,42,172
W_ 2
Mvvv 76,30,176
Dv 42,v1,38912
Mvvv 80,30,180
Mvvv 84,30,184
Mvvv 88,30,188
Mvvv 92,30,192
s_waitcnt vmcnt(34) lgkmcnt(0)
s_barrier
Mvvv 76,34,196
Dv 30,%0,55296
Mvvv 76,38,216
Mvvv 76,42,236
Mvvv 80,34,200
Mvvv 80,38,220
Mvvv 80,42,240
Mvvv 84,34,204
s_nop 3
v_max3_f32 v56, v56, v76, v77
v_max3_f32 v56, v56, v78, v79
Mvvv 84,38,224
Mvvv 84,42,244
Mvvv 88,34,208
v_max3_f32 v60, v60, v80, v81
v_max3_f32 v60, v60, v82, v83
Mvvv 88,38,228
Mvvv 88,42,248
Mvvv 92,34,212
s_nop 1
v_max3_f32 v64, v64, v84, v85
v_max3_f32 v64, v64, v86, v87
Mvvv 92,38,232
Mvvv 92,42,252
Dv 34,v1,55296
.p2align 3
v_mov_b32 v11, 0x3f803f80
v_permlane32_swap_b32 v56, v60
v_max3_f32 v68, v68, v88, v89
v_max3_f32 v68, v68, v90, v91
v_max_f32 v56, v56, v60
s_nop 1
v_max3_f32 v72, v72, v92, v93
v_max3_f32 v72, v72, v94, v95
v_mov_b32 v73, v72
v_permlane32_swap_b32 v64, v68
v_max_f32 v64, v64, v68
v_permlane32_swap_b32 v72, v73
v_max_f32 v72, v72, v73
v_mov_b32 v73, v72
v_permlane16_swap_b32 v56, v64
v_max_f32 v56, v56, v64
v_permlane16_swap_b32 v72, v73
B_ v56,0xc0e00000,v56
v_max_f32 v72, v72, v73
v_mov_b32 v64, v56
B_ v72,0xc0e00000,v72
v_mov_b32 v73, v72
v_permlane16_swap_b32 v56, v64
v_mov_b32 v60, v56
v_mov_b32 v68, v64
v_mov_b64 v[74:75], v[72:73]
v_permlane32_swap_b32 v56, v60
v_permlane32_swap_b32 v64, v68
v_mov_b32 v57, v56
v_mov_b32 v61, v60
v_mov_b32 v65, v64
v_mov_b32 v69, v68
v_pk_add_f32 v[96:97], v[96:97], v[56:57]
v_pk_add_f32 v[98:99], v[98:99], v[56:57]
v_pk_add_f32 v[76:77], v[76:77], v[56:57]
v_pk_add_f32 v[78:79], v[78:79], v[56:57]
v_mov_b64 v[58:59], v[56:57]
v_pk_add_f32 v[100:101], v[100:101], v[60:61]
v_pk_add_f32 v[102:103], v[102:103], v[60:61]
v_pk_add_f32 v[80:81], v[80:81], v[60:61]
v_pk_add_f32 v[82:83], v[82:83], v[60:61]
v_mov_b64 v[62:63], v[60:61]
v_pk_add_f32 v[104:105], v[104:105], v[64:65]
v_pk_add_f32 v[106:107], v[106:107], v[64:65]
v_pk_add_f32 v[84:85], v[84:85], v[64:65]
v_pk_add_f32 v[86:87], v[86:87], v[64:65]
v_mov_b64 v[66:67], v[64:65]
v_pk_add_f32 v[108:109], v[108:109], v[68:69]
v_pk_add_f32 v[110:111], v[110:111], v[68:69]
v_pk_add_f32 v[88:89], v[88:89], v[68:69]
v_pk_add_f32 v[90:91], v[90:91], v[68:69]
v_mov_b64 v[70:71], v[68:69]
v_pk_add_f32 v[112:113], v[112:113], v[72:73]
v_pk_add_f32 v[114:115], v[114:115], v[72:73]
v_pk_add_f32 v[92:93], v[92:93], v[72:73]
v_pk_add_f32 v[94:95], v[94:95], v[72:73]
E_ 96
E_ 97
E_ 98
E_ 99
E_ 76
E_ 77
E_ 78
E_ 79
C_ 99,98,99
C_ 98,96,97
C_ 96,76,77
C_ 97,78,79
E_ 100
E_ 101
E_ 102
E_ 103
E_ 80
E_ 81
E_ 82
E_ 83
C_ 103,102,103
C_ 102,100,101
C_ 100,80,81
C_ 101,82,83
.Lstep1_1:
W_ 1
Nvvv 116,30,136,56
s_nop 0
Dv 38,%0,56320
Dv 42,v1,56320
W_ 2
Mvvv 116,34,156
E_ 104
Nvvv 120,30,140,60
E_ 105
Mvvv 120,34,160
E_ 106
Nvvv 124,30,144,64
E_ 107
Mvvv 124,34,164
E_ 84
Nvvv 128,30,148,68
E_ 85
Mvvv 128,34,168
E_ 86
Nvvv 132,30,152,72
E_ 87
Mvvv 132,34,172
W_ 0
C_ 107,106,107
C_ 106,104,105
Mvvv 132,38,192
s_nop 0
Dv 30,%0,57344
Dv 34,v1,57344
Mvvv 132,42,212
C_ 104,84,85
C_ 105,86,87
Mvvv 128,38,188
E_ 108
Mvvv 128,42,208
E_ 109
Mvvv 124,38,184
E_ 110
Mvvv 124,42,204
E_ 111
Mvvv 120,38,180
E_ 88
Mvvv 120,42,200
E_ 89
Mvvv 116,38,176
E_ 90
Mvvv 116,42,196
W_ 0
E_ 91
Mvvv 116,30,216
s_nop 0
Dv 38,%0,49152
Dv 42,v1,49152
Mvvv 116,34,236
C_ 111,110,111
C_ 110,108,109
Mvvv 120,30,220
C_ 108,88,89
C_ 109,90,91
Mvvv 120,34,240
E_ 112
Mvvv 124,30,224
E_ 113
Mvvv 124,34,244
E_ 114
Mvvv 128,30,228
E_ 115
Mvvv 128,34,248
E_ 92
Mvvv 132,30,232
E_ 93
Mvvv 132,34,252
W_ 0
E_ 94
Nvvv 76,38,136,56
s_nop 0
Dv 30,%0,50176
Dv 34,v1,50176
Mvvv 76,42,156
E_ 95
Nvvv 80,38,140,60
C_ 115,114,115
C_ 114,112,113
Mvvv 80,42,160
C_ 112,92,93
C_ 113,94,95
Nvvv 84,38,144,64
E_ 116
Mvvv 84,42,164
E_ 117
Nvvv 88,38,148,68
E_ 118
Mvvv 88,42,168
E_ 119
Nvvv 92,38,152,72
C_ 119,118,119
C_ 118,116,117
Mvvv 92,42,172
W_ 0
E_ 120
Mvvv 92,30,192
s_nop 0
Dv 38,%0,51200
Dv 42,v1,51200
Mvvv 92,34,212
X_ a180,v11
X_ a181,v11
Mvvv 88,30,188
X_ a182,v11
X_ a183,v11
Mvvv 88,34,208
ds_write_b128 v12, a[132:135] offset:0
Mvvv 84,30,184
E_ 121
Mvvv 84,34,204
E_ 122
Mvvv 80,30,180
ds_write_b128 v12, a[136:139] offset:1024
Mvvv 80,34,200
E_ 123
Mvvv 76,30,176
C_ 123,122,123
C_ 122,120,121
Mvvv 76,34,196
ds_write_b128 v12, a[160:163] offset:2048
W_ 4
Mvvv 76,38,216
W_ 3
E_ 124
Mvvv 76,42,236
s_waitcnt vmcnt(12) lgkmcnt(0)
s_barrier
Mvvv 80,38,220
ds_write_b128 v12, a[164:167] offset:3072
Mvvv 80,42,240
E_ 125
Mvvv 84,38,224
E_ 126
Mvvv 84,42,244
s_nop 0
Tv 30,v2,24576
Tv 32,v2,30720
Mvvv 88,38,228
ds_write_b128 v12, a[168:171] offset:4096
Mvvv 88,42,248
s_nop 0
Tv 34,v3,24576
Tv 36,v3,30720
Mvvv 92,38,232
E_ 127
Mvvv 92,42,252
ds_write_b128 v12, a[172:175] offset:5120
Zaav 220,180,96
C_ 127,126,127
C_ 126,124,125
Zaav 224,180,100
E_ 128
Zaav 228,180,104
ds_write_b128 v12, a[176:179] offset:12288
Zaav 232,180,108
E_ 129
Zaav 236,180,112
W_ 2
E_ 130
Zavv 0,30,96
s_nop 0
Tv 38,v4,24576
Tv 40,v4,30720
Zavv 20,34,96
ds_write_b128 v12, a[200:203] offset:13312
Zavv 24,34,100
s_nop 0
Tv 42,v5,24576
Tv 44,v5,30720
Zavv 4,30,100
E_ 131
Zavv 8,30,104
ds_write_b128 v12, a[204:207] offset:14336
Zavv 28,34,104
R_ v46,a220
R_ v47,a224
Zavv 32,34,108
R_ v48,a228
R_ v49,a232
Zavv 12,30,108
R_ v50,a236
C_ 131,130,131
Zavv 16,30,112
ds_write_b128 v12, a[208:211] offset:15360
Zavv 36,34,112
W_ 2
C_ 130,128,129
Zavv 56,38,112
s_nop 0
Tv 30,v2,25600
Tv 32,v2,31744
Zavv 76,42,112
ds_write_b128 v12, a[212:215] offset:16384
Zavv 72,42,108
s_nop 0
Tv 34,v3,25600
Tv 36,v3,31744
Zavv 52,38,108
E_ 132
Zavv 48,38,104
ds_write_b128 v12, a[216:219] offset:17408
Zavv 68,42,104
E_ 133
Zavv 64,42,100
E_ 134
Zavv 44,38,100
E_ 135
Zavv 40,38,96
C_ 135,134,135
C_ 134,132,133
Zavv 60,42,96
W_ 1
E_ 76
Zavv 80,30,96
s_nop 0
Tv 38,v4,25600
Tv 40,v4,31744
Zavv 100,34,96
E_ 77
Zavv 104,34,100
s_nop 0
Tv 42,v5,25600
Tv 44,v5,31744
Zavv 84,30,100
E_ 78
Zavv 88,30,104
E_ 79
Zavv 108,34,104
C_ 116,76,77
C_ 117,78,79
Zavv 112,34,108
E_ 80
Zavv 92,30,108
E_ 81
Zavv 96,30,112
E_ 82
Zavv 116,34,112
W_ 0
E_ 83
Zavv 136,38,112
s_nop 0
Tv 30,v2,26624
Tv 32,v2,32768
Zavv 156,42,112
C_ 120,80,81
C_ 121,82,83
Zavv 152,42,108
s_nop 0
Tv 34,v3,26624
Tv 36,v3,32768
Zavv 132,38,108
E_ 84
Zavv 128,38,104
E_ 85
Zavv 148,42,104
E_ 86
Zavv 144,42,100
E_ 87
Zavv 124,38,100
C_ 124,84,85
C_ 125,86,87
Zavv 120,38,96
E_ 88
Zavv 140,42,96
W_ 0
E_ 89
Zavv 160,30,96
s_nop 0
Tv 38,v4,26624
Tv 40,v4,32768
Zavv 180,34,96
E_ 90
Zavv 184,34,100
s_nop 0
Tv 42,v5,26624
Tv 44,v5,32768
Zavv 164,30,100
E_ 91
Zavv 168,30,104
C_ 128,88,89
C_ 129,90,91
Zavv 188,34,104
S_ s65,s61,36864
E_ 92
Zavv 192,34,108
S_ m0,s51,24576
E_ 93
Zavv 172,30,108
s_waitcnt vmcnt(6) lgkmcnt(0)
s_barrier
Zavv 176,30,112
Ln v7,32,s65,0
Zavv 196,34,112
E_ 94
Zavv 216,38,112
Dv 30,%0,6144
Zavv 236,42,112
Dv 34,v1,6144
Zavv 232,42,108
Ln v8,32,s65,3072
S_ m0,s51,25472
Zavv 212,38,108
E_ 95
)A"
R"A(Zavv 208,38,104
Ln v7,32,s65,128
Zavv 228,42,104
C_ 132,92,93
C_ 133,94,95
Zavv 224,42,100
Ln v8,32,s65,3200
S_ m0,s51,26368
Zavv 204,38,100
v_mov_b32 v92, 0x3f803f80
v_mov_b32 v93, 0x3f803f80
Zavv 200,38,96
Ln v7,32,s65,256
Zavv 220,42,96
v_mov_b32 v94, 0x3f803f80
v_mov_b32 v95, 0x3f803f80
Ln v8,32,s65,3328
.Lstep1_2:
s_nop 0
Zvvv 76,92,116
Dv 38,%0,7168
Zvvv 80,92,120
Dv 42,v1,7168
Zvvv 84,92,124
Zvvv 88,92,128
Zvvv 92,92,132
W_ 2
Nvvv 96,30,136,56
Mvvv 96,34,156
Nvvv 100,30,140,60
s_nop 1
v_max3_f32 v10, v76, v80, v84
s_nop 0
v_max3_f32 v10, v10, v88, v92
Mvvv 100,34,160
v_cmp_lt_f32 vcc, 0x42000000, v10
s_cbranch_vccnz .Ltramp1_1
Nvvv 104,30,144,64
A_ v46,v46,v76
A_ v47,v47,v80
Mvvv 104,34,164
A_ v48,v48,v84
A_ v49,v49,v88
Nvvv 108,30,148,68
A_ v50,v50,v92
Mvvv 108,34,168
Nvvv 112,30,152,72
Mvvv 112,34,172
W_ 0
Mvvv 112,38,192
Dv 30,%0,8192
Mvvv 112,42,212
Dv 34,v1,8192
Mvvv 108,38,188
Mvvv 108,42,208
Mvvv 104,38,184
Mvvv 104,42,204
Mvvv 100,38,180
Mvvv 100,42,200
Mvvv 96,38,176
Mvvv 96,42,196
W_ 0
Mvvv 96,30,216
Dv 38,%0,0
Mvvv 96,34,236
Dv 42,v1,0
Mvvv 100,30,220
Mvvv 100,34,240
Mvvv 104,30,224
Mvvv 104,34,244
Mvvv 108,30,228
Mvvv 108,34,248
s_nop 0
E_ 96
Mvvv 112,30,232
E_ 97
Mvvv 112,34,252
W_ 0
E_ 98
Nvvv 76,38,136,56
Dv 30,%0,1024
Mvvv 76,42,156
Dv 34,v1,1024
Nvvv 80,38,140,60
E_ 99
Mvvv 80,42,160
C_ 99,98,99
C_ 98,96,97
Nvvv 84,38,144,64
E_ 100
Mvvv 84,42,164
E_ 101
Nvvv 88,38,148,68
E_ 102
Mvvv 88,42,168
E_ 103
Nvvv 92,38,152,72
C_ 103,102,103
C_ 102,100,101
Mvvv 92,42,172
W_ 0
E_ 104
Mvvv 92,30,192
Dv 38,%0,2048
Mvvv 92,34,212
Dv 42,v1,2048
Mvvv 88,30,188
E_ 105
Mvvv 88,34,208
E_ 106
Mvvv 84,30,184
S_ s65,s61,24576
E_ 107
Mvvv 84,34,204
S_ m0,s51,0
C_ 107,106,107
C_ 106,104,105
Mvvv 80,30,180
s_waitcnt vmcnt(6) lgkmcnt(0)
s_barrier
Mvvv 80,34,200
L_ v9,36,s65,0
Mvvv 76,30,176
E_ 108
Mvvv 76,34,196
L_ v9,36,s65,3072
S_ m0,s51,896
Mvvv 76,38,216
Tv 30,v2,12288
Tv 32,v2,18432
Mvvv 76,42,236
L_ v9,36,s65,128
Mvvv 80,38,220
s_nop 0
Tv 34,v3,12288
Tv 36,v3,18432
Mvvv 80,42,240
L_ v9,36,s65,3200
S_ m0,s51,1792
Mvvv 84,38,224
E_ 109
Mvvv 84,42,244
L_ v9,36,s65,256
Mvvv 88,38,228
E_ 110
Mvvv 88,42,248
L_ v9,36,s65,3328
Mvvv 92,38,232
E_ 111
Mvvv 92,42,252
W_ 0
C_ 111,110,111
C_ 110,108,109
Mavv 0,30,116
s_nop 0
Tv 38,v4,12288
Tv 40,v4,18432
Mavv 20,34,116
E_ 112
Mavv 24,34,120
s_nop 0
Tv 42,v5,12288
Tv 44,v5,18432
Mavv 4,30,120
E_ 113
Mavv 8,30,124
E_ 114
Mavv 28,34,124
E_ 115
Mavv 32,34,128
C_ 115,114,115
C_ 114,112,113
Mavv 12,30,128
E_ 76
Mavv 16,30,132
E_ 77
Mavv 36,34,132
W_ 0
E_ 78
Mavv 56,38,132
s_nop 0
Tv 30,v2,13312
Tv 32,v2,19456
Mavv 76,42,132
E_ 79
Mavv 72,42,128
s_nop 0
Tv 34,v3,13312
Tv 36,v3,19456
Mavv 52,38,128
C_ 96,76,77
C_ 97,78,79
Mavv 48,38,124
E_ 80
Mavv 68,42,124
E_ 81
Mavv 64,42,120
E_ 82
Mavv 44,38,120
E_ 83
Mavv 40,38,116
C_ 100,80,81
C_ 101,82,83
Mavv 60,42,116
W_ 0
E_ 84
Mavv 80,30,116
s_nop 0
Tv 38,v4,13312
Tv 40,v4,19456
Mavv 100,34,116
E_ 85
Mavv 104,34,120
s_nop 0
Tv 42,v5,13312
Tv 44,v5,19456
Mavv 84,30,120
E_ 86
Mavv 88,30,124
E_ 87
Mavv 108,34,124
C_ 104,84,85
C_ 105,86,87
Mavv 112,34,128
E_ 88
Mavv 92,30,128
E_ 89
Mavv 96,30,132
E_ 90
Mavv 116,34,132
W_ 0
E_ 91
Mavv 136,38,132
s_nop 0
Tv 30,v2,14336
Tv 32,v2,20480
Mavv 156,42,132
C_ 108,88,89
C_ 109,90,91
Mavv 152,42,128
s_nop 0
Tv 34,v3,14336
Tv 36,v3,20480
Mavv 132,38,128
E_ 92
Mavv 128,38,124
E_ 93
Mavv 148,42,124
E_ 94
Mavv 144,42,120
E_ 95
Mavv 124,38,120
C_ 112,92,93
C_ 113,94,95
Mavv 120,38,116
v_mov_b32 v92, 0x3f803f80
v_mov_b32 v93, 0x3f803f80
Mavv 140,42,116
W_ 0
v_mov_b32 v94, 0x3f803f80
v_mov_b32 v95, 0x3f803f80
Mavv 160,30,116
s_nop 0
Tv 38,v4,14336
Tv 40,v4,20480
Mavv 180,34,116
Mavv 184,34,120
s_nop 0
Tv 42,v5,14336
Tv 44,v5,20480
Mavv 164,30,120
Mavv 168,30,124
Mavv 188,34,124
S_ s65,s61,49152
Mavv 192,34,128
S_ m0,s51,12288
Mavv 172,30,128
s_waitcnt vmcnt(6) lgkmcnt(0)
s_barrier
Mavv 176,30,132
Ln v7,32,s65,0
Mavv 196,34,132
Mavv 216,38,132
Dv 30,%0,30720
Mavv 236,42,132
Dv 34,v1,30720
Mavv 232,42,128
Ln v8,32,s65,3072
S_ m0,s51,13184
Mavv 212,38,128
Mavv 208,38,124
Ln v7,32,s65,128
Mavv 228,42,124
Mavv 224,42,120
Ln v8,32,s65,3200
S_ m0,s51,14080
Mavv 204,38,120
Mavv 200,38,116
Ln v7,32,s65,256
Mavv 220,42,116
s_cmp_eq_u32 s4, 0
s_cbranch_scc1 .Lhs_a1
.Lhs_a1_back:
.Lstep1_3:
Zvvv 76,92,96
Dv 38,%0,31744
Zvvv 80,92,100
Dv 42,v1,31744
Zvvv 84,92,104
Zvvv 88,92,108
Zvvv 92,92,112
W_ 2
Nvvv 116,30,136,56
Mvvv 116,34,156
Nvvv 120,30,140,60
s_nop 1
v_max3_f32 v10, v76, v80, v84
s_nop 0
v_max3_f32 v10, v10, v88, v92
Mvvv 120,34,160
v_cmp_lt_f32 vcc, 0x42000000, v10
s_cbranch_vccnz .Ltramp1_2
Nvvv 124,30,144,64
A_ v46,v46,v76
A_ v47,v47,v80
Mvvv 124,34,164
A_ v48,v48,v84
A_ v49,v49,v88
Nvvv 128,30,148,68
A_ v50,v50,v92
Mvvv 128,34,168
Ln v8,32,s65,3328
Nvvv 132,30,152,72
Mvvv 132,34,172
W_ 0
Mvvv 132,38,192
Dv 30,%0,32768
Mvvv 132,42,212
Dv 34,v1,32768
Mvvv 128,38,188
Mvvv 128,42,208
Mvvv 124,38,184
Mvvv 124,42,204
Mvvv 120,38,180
Mvvv 120,42,200
Mvvv 116,38,176
Mvvv 116,42,196
W_ 0
Mvvv 116,30,216
Dv 38,%0,24576
Mvvv 116,34,236
Dv 42,v1,24576
Mvvv 120,30,220
Mvvv 120,34,240
Mvvv 124,30,224
Mvvv 124,34,244
Mvvv 128,30,228
Mvvv 128,34,248
s_nop 0
E_ 116
Mvvv 132,30,232
E_ 117
Mvvv 132,34,252
W_ 0
E_ 118
Nvvv 76,38,136,56
Dv 30,%0,25600
Mvvv 76,42,156
Dv 34,v1,25600
Nvvv 80,38,140,60
E_ 119
Mvvv 80,42,160
C_ 119,118,119
C_ 118,116,117
Nvvv 84,38,144,64
E_ 120
Mvvv 84,42,164
E_ 121
Nvvv 88,38,148,68
E_ 122
Mvvv 88,42,168
E_ 123
Nvvv 92,38,152,72
C_ 123,122,123
C_ 122,120,121
Mvvv 92,42,172
W_ 0
E_ 124
Mvvv 92,30,192
Dv 38,%0,26624
Mvvv 92,34,212
Dv 42,v1,26624
Mvvv 88,30,188
E_ 125
Mvvv 88,34,208
E_ 126
Mvvv 84,30,184
S_ s65,s61,36864
E_ 127
Mvvv 84,34,204
S_ m0,s51,24576
C_ 127,126,127
C_ 126,124,125
Mvvv 80,30,180
s_waitcnt vmcnt(6) lgkmcnt(0)
s_barrier
Mvvv 80,34,200
L_ v9,36,s65,0
Mvvv 76,30,176
E_ 128
Mvvv 76,34,196
L_ v9,36,s65,3072
S_ m0,s51,25472
Mvvv 76,38,216
Tv 30,v2,0
Tv 32,v2,6144
Mvvv 76,42,236
L_ v9,36,s65,128
Mvvv 80,38,220
s_nop 0
Tv 34,v3,0
Tv 36,v3,6144
Mvvv 80,42,240
L_ v9,36,s65,3200
S_ m0,s51,26368
Mvvv 84,38,224
E_ 129
Mvvv 84,42,244
L_ v9,36,s65,256
Mvvv 88,38,228
E_ 130
Mvvv 88,42,248
L_ v9,36,s65,3328
Mvvv 92,38,232
E_ 131
Mvvv 92,42,252
W_ 0
C_ 131,130,131
C_ 130,128,129
Mavv 0,30,96
s_nop 0
Tv 38,v4,0
Tv 40,v4,6144
Mavv 20,34,96
E_ 132
Mavv 24,34,100
s_nop 0
Tv 42,v5,0
Tv 44,v5,6144
Mavv 4,30,100
E_ 133
Mavv 8,30,104
E_ 134
Mavv 28,34,104
E_ 135
Mavv 32,34,108
C_ 135,134,135
C_ 134,132,133
Mavv 12,30,108
E_ 76
Mavv 16,30,112
E_ 77
Mavv 36,34,112
W_ 0
E_ 78
Mavv 56,38,112
s_nop 0
Tv 30,v2,1024
Tv 32,v2,7168
Mavv 76,42,112
E_ 79
Mavv 72,42,108
s_nop 0
Tv 34,v3,1024
Tv 36,v3,7168
Mavv 52,38,108
C_ 116,76,77
C_ 117,78,79
Mavv 48,38,104
E_ 80
Mavv 68,42,104
E_ 81
Mavv 64,42,100
E_ 82
Mavv 44,38,100
E_ 83
Mavv 40,38,96
C_ 120,80,81
C_ 121,82,83
Mavv 60,42,96
W_ 0
E_ 84
Mavv 80,30,96
s_nop 0
Tv 38,v4,1024
Tv 40,v4,7168
Mavv 100,34,96
E_ 85
Mavv 104,34,100
s_nop 0
Tv 42,v5,1024
Tv 44,v5,7168
Mavv 84,30,100
E_ 86
Mavv 88,30,104
E_ 87
Mavv 108,34,104
C_ 124,84,85
C_ 125,86,87
Mavv 112,34,108
E_ 88
Mavv 92,30,108
E_ 89
Mavv 96,30,112
E_ 90
Mavv 116,34,112
W_ 0
E_ 91
Mavv 136,38,112
s_nop 0
Tv 30,v2,2048
Tv 32,v2,8192
Mavv 156,42,112
C_ 128,88,89
C_ 129,90,91
Mavv 152,42,108
s_nop 0
Tv 34,v3,2048
Tv 36,v3,8192
Mavv 132,38,108
E_ 92
Mavv 128,38,104
E_ 93
Mavv 148,42,104
E_ 94
Mavv 144,42,100
E_ 95
Mavv 124,38,100
C_ 132,92,93
C_ 133,94,95
Mavv 120,38,96
v_mov_b32 v92, 0x3f803f80
v_mov_b32 v93, 0x3f803f80
Mavv 140,42,96
W_ 0
v_mov_b32 v94, 0x3f803f80
v_mov_b32 v95, 0x3f803f80
Mavv 160,30,96
s_nop 0
Tv 38,v4,2048
Tv 40,v4,8192
Mavv 180,34,96
Mavv 184,34,100
s_nop 0
Tv 42,v5,2048
Tv 44,v5,8192
Mavv 164,30,100
Mavv 168,30,104
Mavv 188,34,104
S_ s65,s61,61440
Mavv 192,34,108
S_ m0,s51,0
Mavv 172,30,108
s_waitcnt vmcnt(6) lgkmcnt(0)
s_barrier
Mavv 176,30,112
Ln v7,32,s65,0
Mavv 196,34,112
Mavv 216,38,112
Dv 30,%0,18432
Mavv 236,42,112
Dv 34,v1,18432
Mavv 232,42,108
Ln v8,32,s65,3072
S_ m0,s51,896
Mavv 212,38,108
Mavv 208,38,104
Ln v7,32,s65,128
Mavv 228,42,104
Mavv 224,42,100
Ln v8,32,s65,3200
S_ m0,s51,1792
Mavv 204,38,100
Mavv 200,38,96
Ln v7,32,s65,256
Mavv 220,42,96
.Lstep1_4:
Zvvv 76,92,116
Dv 38,%0,19456
Zvvv 80,92,120
Dv 42,v1,19456
Zvvv 84,92,124
Zvvv 88,92,128
Zvvv 92,92,132
W_ 2
Nvvv 96,30,136,56
Mvvv 96,34,156
Nvvv 100,30,140,60
s_nop 1
v_max3_f32 v10, v76, v80, v84
s_nop 0
v_max3_f32 v10, v10, v88, v92
Mvvv 100,34,160
v_cmp_lt_f32 vcc, 0x42000000, v10
s_cbranch_vccnz .Ltramp1_3
Nvvv 104,30,144,64
A_ v46,v46,v76
A_ v47,v47,v80
Mvvv 104,34,164
A_ v48,v48,v84
A_ v49,v49,v88
Nvvv 108,30,148,68
A_ v50,v50,v92
Mvvv 108,34,168
Ln v8,32,s65,3328
Nvvv 112,30,152,72
Mvvv 112,34,172
W_ 0
Mvvv 112,38,192
Dv 30,%0,20480
Mvvv 112,42,212
Dv 34,v1,20480
Mvvv 108,38,188
Mvvv 108,42,208
Mvvv 104,38,184
Mvvv 104,42,204
Mvvv 100,38,180
Mvvv 100,42,200
Mvvv 96,38,176
Mvvv 96,42,196
W_ 0
Mvvv 96,30,216
Dv 38,%0,12288
Mvvv 96,34,236
Dv 42,v1,12288
Mvvv 100,30,220
Mvvv 100,34,240
Mvvv 104,30,224
Mvvv 104,34,244
Mvvv 108,30,228
Mvvv 108,34,248
s_nop 0
E_ 96
Mvvv 112,30,232
E_ 97
Mvvv 112,34,252
W_ 0
E_ 98
Nvvv 76,38,136,56
Dv 30,%0,13312
Mvvv 76,42,156
Dv 34,v1,13312
Nvvv 80,38,140,60
E_ 99
Mvvv 80,42,160
C_ 99,98,99
C_ 98,96,97
Nvvv 84,38,144,64
E_ 100
Mvvv 84,42,164
E_ 101
Nvvv 88,38,148,68
E_ 102
Mvvv 88,42,168
E_ 103
Nvvv 92,38,152,72
C_ 103,102,103
C_ 102,100,101
Mvvv 92,42,172
W_ 0
E_ 104
Mvvv 92,30,192
Dv 38,%0,14336
Mvvv 92,34,212
Dv 42,v1,14336
Mvvv 88,30,188
E_ 105
Mvvv 88,34,208
E_ 106
Mvvv 84,30,184
S_ s65,s61,49152
E_ 107
Mvvv 84,34,204
S_ m0,s51,12288
C_ 107,106,107
C_ 106,104,105
Mvvv 80,30,180
s_waitcnt vmcnt(6) lgkmcnt(0)
s_barrier
Mvvv 80,34,200
L_ v9,36,s65,0
Mvvv 76,30,176
E_ 108
Mvvv 76,34,196
L_ v9,36,s65,3072
S_ m0,s51,13184
Mvvv 76,38,216
Tv 30,v2,24576
Tv 32,v2,30720
Mvvv 76,42,236
L_ v9,36,s65,128
Mvvv 80,38,220
s_nop 0
Tv 34,v3,24576
Tv 36,v3,30720
Mvvv 80,42,240
L_ v9,36,s65,3200
S_ m0,s51,14080
Mvvv 84,38,224
E_ 109
Mvvv 84,42,244
L_ v9,36,s65,256
Mvvv 88,38,228
E_ 110
Mvvv 88,42,248
L_ v9,36,s65,3328
Mvvv 92,38,232
E_ 111
Mvvv 92,42,252
W_ 0
C_ 111,110,111
C_ 110,108,109
Mavv 0,30,116
s_nop 0
Tv 38,v4,24576
Tv 40,v4,30720
Mavv 20,34,116
E_ 112
Mavv 24,34,120
s_nop 0
Tv 42,v5,24576
Tv 44,v5,30720
Mavv 4,30,120
E_ 113
Mavv 8,30,124
E_ 114
Mavv 28,34,124
E_ 115
Mavv 32,34,128
C_ 115,114,115
C_ 114,112,113
Mavv 12,30,128
E_ 76
Mavv 16,30,132
E_ 77
Mavv 36,34,132
W_ 0
E_ 78
Mavv 56,38,132
s_nop 0
Tv 30,v2,25600
Tv 32,v2,31744
Mavv 76,42,132
E_ 79
Mavv 72,42,128
s_nop 0
Tv 34,v3,25600
Tv 36,v3,31744
Mavv 52,38,128
C_ 96,76,77
C_ 97,78,79
Mavv 48,38,124
E_ 80
Mavv 68,42,124
E_ 81
Mavv 64,42,120
E_ 82
Mavv 44,38,120
E_ 83
Mavv 40,38,116
C_ 100,80,81
C_ 101,82,83
Mavv 60,42,116
W_ 0
E_ 84
Mavv 80,30,116
s_nop 0
Tv 38,v4,25600
Tv 40,v4,31744
Mavv 100,34,116
E_ 85
Mavv 104,34,120
s_nop 0
Tv 42,v5,25600
Tv 44,v5,31744
Mavv 84,30,120
E_ 86
Mavv 88,30,124
E_ 87
Mavv 108,34,124
C_ 104,84,85
C_ 105,86,87
Mavv 112,34,128
E_ 88
Mavv 92,30,128
E_ 89
Mavv 96,30,132
E_ 90
Mavv 116,34,132
W_ 0
E_ 91
Mavv 136,38,132
s_nop 0
Tv 30,v2,26624
Tv 32,v2,32768
Mavv 156,42,132
C_ 108,88,89
C_ 109,90,91
Mavv 152,42,128
s_nop 0
Tv 34,v3,26624
Tv 36,v3,32768
Mavv 132,38,128
E_ 92
Mavv 128,38,124
E_ 93
Mavv 148,42,124
E_ 94
Mavv 144,42,120
E_ 95
Mavv 124,38,120
C_ 112,92,93
C_ 113,94,95
Mavv 120,38,116
v_mov_b32 v92, 0x3f803f80
v_mov_b32 v93, 0x3f803f80
Mavv 140,42,116
W_ 0
v_mov_b32 v94, 0x3f803f80
v_mov_b32 v95, 0x3f803f80
Mavv 160,30,116
s_nop 0
Tv 38,v4,26624
Tv 40,v4,32768
Mavv 180,34,116
Mavv 184,34,120
s_nop 0
Tv 42,v5,26624
Tv 44,v5,32768
Mavv 164,30,120
Mavv 168,30,124
Mavv 188,34,124
S_ s65,s61,73728
Mavv 192,34,128
S_ m0,s51,24576
Mavv 172,30,128
s_waitcnt vmcnt(6) lgkmcnt(0)
s_barrier
Mavv 176,30,132
Ln v7,32,s65,0
Mavv 196,34,132
Mavv 216,38,132
Dv 30,%0,6144
Mavv 236,42,132
Dv 34,v1,6144
Mavv 232,42,128
Ln v8,32,s65,3072
S_ m0,s51,25472
Mavv 212,38,128
Mavv 208,38,124
Ln v7,32,s65,128
Mavv 228,42,124
Mavv 224,42,120
Ln v8,32,s65,3200
S_ m0,s51,26368
Mavv 204,38,120
Mavv 200,38,116
Ln v7,32,s65,256
Mavv 220,42,116
s_cmp_eq_u32 s4, 0
s_cbranch_scc1 .Lhs_b1
.Lhs_b1_back:
.Lstep1_5:
Zvvv 76,92,96
Dv 38,%0,7168
Zvvv 80,92,100
Dv 42,v1,7168
Zvvv 84,92,104
Zvvv 88,92,108
Zvvv 92,92,112
W_ 2
Nvvv 116,30,136,56
Mvvv 116,34,156
Nvvv 120,30,140,60
s_nop 1
v_max3_f32 v10, v76, v80, v84
s_nop 0
v_max3_f32 v10, v10, v88, v92
Mvvv 120,34,160
v_cmp_lt_f32 vcc, 0x42000000, v10
s_cbranch_vccnz .Ltramp1_4
Nvvv 124,30,144,64
A_ v46,v46,v76
A_ v47,v47,v80
Mvvv 124,34,164
A_ v48,v48,v84
A_ v49,v49,v88
Nvvv 128,30,148,68
A_ v50,v50,v92
Mvvv 128,34,168
Ln v8,32,s65,3328
Nvvv 132,30,152,72
Mvvv 132,34,172
W_ 0
Mvvv 132,38,192
Dv 30,%0,8192
Mvvv 132,42,212
Dv 34,v1,8192
Mvvv 128,38,188
Mvvv 128,42,208
Mvvv 124,38,184
Mvvv 124,42,204
Mvvv 120,38,180
Mvvv 120,42,200
Mvvv 116,38,176
Mvvv 116,42,196
W_ 0
Mvvv 116,30,216
Dv 38,%0,0
Mvvv 116,34,236
Dv 42,v1,0
Mvvv 120,30,220
Mvvv 120,34,240
Mvvv 124,30,224
Mvvv 124,34,244
Mvvv 128,30,228
Mvvv 128,34,248
s_nop 0
E_ 116
Mvvv 132,30,232
E_ 117
Mvvv 132,34,252
W_ 0
E_ 118
Nvvv 76,38,136,56
Dv 30,%0,1024
Mvvv 76,42,156
Dv 34,v1,1024
Nvvv 80,38,140,60
E_ 119
Mvvv 80,42,160
C_ 119,118,119
C_ 118,116,117
Nvvv 84,38,144,64
E_ 120
Mvvv 84,42,164
E_ 121
Nvvv 88,38,148,68
E_ 122
Mvvv 88,42,168
E_ 123
Nvvv 92,38,152,72
C_ 123,122,123
C_ 122,120,121
Mvvv 92,42,172
W_ 0
E_ 124
Mvvv 92,30,192
Dv 38,%0,2048
Mvvv 92,34,212
Dv 42,v1,2048
Mvvv 88,30,188
E_ 125
Mvvv 88,34,208
E_ 126
Mvvv 84,30,184
S_ s65,s61,61440
E_ 127
Mvvv 84,34,204
S_ m0,s51,0
C_ 127,126,127
C_ 126,124,125
Mvvv 80,30,180
s_waitcnt vmcnt(6) lgkmcnt(0)
s_barrier
Mvvv 80,34,200
L_ v9,36,s65,0
Mvvv 76,30,176
E_ 128
Mvvv 76,34,196
L_ v9,36,s65,3072
S_ m0,s51,896
Mvvv 76,38,216
Tv 30,v2,12288
Tv 32,v2,18432
Mvvv 76,42,236
L_ v9,36,s65,128
Mvvv 80,38,220
s_nop 0
Tv 34,v3,12288
Tv 36,v3,18432
Mvvv 80,42,240
L_ v9,36,s65,3200
S_ m0,s51,1792
Mvvv 84,38,224
E_ 129
Mvvv 84,42,244
L_ v9,36,s65,256
Mvvv 88,38,228
E_ 130
Mvvv 88,42,248
L_ v9,36,s65,3328
Mvvv 92,38,232
E_ 131
Mvvv 92,42,252
W_ 0
C_ 131,130,131
C_ 130,128,129
Mavv 0,30,96
s_nop 0
Tv 38,v4,12288
Tv 40,v4,18432
Mavv 20,34,96
E_ 132
Mavv 24,34,100
s_nop 0
Tv 42,v5,12288
Tv 44,v5,18432
Mavv 4,30,100
E_ 133
Mavv 8,30,104
E_ 134
Mavv 28,34,104
E_ 135
Mavv 32,34,108
C_ 135,134,135
C_ 134,132,133
Mavv 12,30,108
E_ 76
Mavv 16,30,112
E_ 77
Mavv 36,34,112
W_ 0
E_ 78
Mavv 56,38,112
s_nop 0
Tv 30,v2,13312
Tv 32,v2,19456
Mavv 76,42,112
E_ 79
Mavv 72,42,108
s_nop 0
Tv 34,v3,13312
Tv 36,v3,19456
Mavv 52,38,108
C_ 116,76,77
C_ 117,78,79
Mavv 48,38,104
E_ 80
Mavv 68,42,104
E_ 81
Mavv 64,42,100
E_ 82
Mavv 44,38,100
E_ 83
Mavv 40,38,96
C_ 120,80,81
C_ 121,82,83
Mavv 60,42,96
W_ 0
E_ 84
Mavv 80,30,96
s_nop 0
Tv 38,v4,13312
Tv 40,v4,19456
Mavv 100,34,96
E_ 85
Mavv 104,34,100
s_nop 0
Tv 42,v5,13312
Tv 44,v5,19456
Mavv 84,30,100
E_ 86
Mavv 88,30,104
E_ 87
Mavv 108,34,104
C_ 124,84,85
C_ 125,86,87
Mavv 112,34,108
E_ 88
Mavv 92,30,108
E_ 89
Mavv 96,30,112
E_ 90
Mavv 116,34,112
W_ 0
E_ 91
Mavv 136,38,112
s_nop 0
Tv 30,v2,14336
Tv 32,v2,20480
Mavv 156,42,112
C_ 128,88,89
C_ 129,90,91
Mavv 152,42,108
s_nop 0
Tv 34,v3,14336
Tv 36,v3,20480
Mavv 132,38,108
E_ 92
Mavv 128,38,104
E_ 93
Mavv 148,42,104
E_ 94
Mavv 144,42,100
E_ 95
Mavv 124,38,100
C_ 132,92,93
C_ 133,94,95
Mavv 120,38,96
v_mov_b32 v92, 0x3f803f80
v_mov_b32 v93, 0x3f803f80
Mavv 140,42,96
W_ 0
v_mov_b32 v94, 0x3f803f80
v_mov_b32 v95, 0x3f803f80
Mavv 160,30,96
s_nop 0
Tv 38,v4,14336
Tv 40,v4,20480
Mavv 180,34,96
Mavv 184,34,100
s_nop 0
Tv 42,v5,14336
Tv 44,v5,20480
Mavv 164,30,100
Mavv 168,30,104
Mavv 188,34,104
S_ s65,s61,86016
Mavv 192,34,108
S_ m0,s51,12288
Mavv 172,30,108
s_waitcnt vmcnt(6) lgkmcnt(0)
s_barrier
Mavv 176,30,112
Ln v7,32,s65,0
Mavv 196,34,112
Mavv 216,38,112
Dv 30,%0,30720
Mavv 236,42,112
Dv 34,v1,30720
Mavv 232,42,108
Ln v8,32,s65,3072
S_ m0,s51,13184
Mavv 212,38,108
Mavv 208,38,104
Ln v7,32,s65,128
Mavv 228,42,104
Mavv 224,42,100
Ln v8,32,s65,3200
S_ m0,s51,14080
Mavv 204,38,100
Mavv 200,38,96
Ln v7,32,s65,256
Mavv 220,42,96
.Lstep1_6:
Zvvv 76,92,116
Dv 38,%0,31744
Zvvv 80,92,120
Dv 42,v1,31744
Zvvv 84,92,124
Zvvv 88,92,128
Zvvv 92,92,132
W_ 2
Nvvv 96,30,136,56
Mvvv 96,34,156
Nvvv 100,30,140,60
s_nop 1
v_max3_f32 v10, v76, v80, v84
s_nop 0
v_max3_f32 v10, v10, v88, v92
Mvvv 100,34,160
v_cmp_lt_f32 vcc, 0x42000000, v10
s_cbranch_vccnz .Ltramp1_5
Nvvv 104,30,144,64
A_ v46,v46,v76
A_ v47,v47,v80
Mvvv 104,34,164
A_ v48,v48,v84
A_ v49,v49,v88
Nvvv 108,30,148,68
A_ v50,v50,v92
Mvvv 108,34,168
Ln v8,32,s65,3328
Nvvv 112,30,152,72
R_ v11,a248
Mvvv 112,34,172
W_ 0
v_min_u32 v11, 240, v11
Mvvv 112,38,192
Dv 30,%0,32768
v_lshlrev_b32 v11, 3, v11
Mvvv 112,42,212
Dv 34,v1,32768
Mvvv 108,38,188
buffer_load_dword a254, v11, s[44:47], s66 offen
Mvvv 108,42,208
Mvvv 104,38,184
Mvvv 104,42,204
Mvvv 100,38,180
Mvvv 100,42,200
Mvvv 96,38,176
Mvvv 96,42,196
W_ 0
Mvvv 96,30,216
Dv 38,%0,24576
Mvvv 96,34,236
Dv 42,v1,24576
Mvvv 100,30,220
Mvvv 100,34,240
Mvvv 104,30,224
Mvvv 104,34,244
Mvvv 108,30,228
Mvvv 108,34,248
s_nop 0
E_ 96
Mvvv 112,30,232
E_ 97
Mvvv 112,34,252
W_ 0
E_ 98
Nvvv 76,38,136,56
Dv 30,%0,25600
Mvvv 76,42,156
Dv 34,v1,25600
Nvvv 80,38,140,60
E_ 99
Mvvv 80,42,160
C_ 99,98,99
C_ 98,96,97
Nvvv 84,38,144,64
E_ 100
Mvvv 84,42,164
E_ 101
Nvvv 88,38,148,68
E_ 102
Mvvv 88,42,168
E_ 103
Nvvv 92,38,152,72
C_ 103,102,103
C_ 102,100,101
Mvvv 92,42,172
W_ 0
E_ 104
Mvvv 92,30,192
Dv 38,%0,26624
Mvvv 92,34,212
Dv 42,v1,26624
Mvvv 88,30,188
E_ 105
Mvvv 88,34,208
E_ 106
Mvvv 84,30,184
S_ s65,s61,73728
E_ 107
Mvvv 84,34,204
S_ m0,s51,24576
C_ 107,106,107
C_ 106,104,105
Mvvv 80,30,180
s_waitcnt vmcnt(7) lgkmcnt(0)
s_barrier
Mvvv 80,34,200
L_ v9,36,s65,0
Mvvv 76,30,176
E_ 108
Mvvv 76,34,196
L_ v9,36,s65,3072
S_ m0,s51,25472
Mvvv 76,38,216
Tv 30,v2,0
Tv 32,v2,6144
Mvvv 76,42,236
L_ v9,36,s65,128
Mvvv 80,38,220
s_nop 0
Tv 34,v3,0
Tv 36,v3,6144
Mvvv 80,42,240
L_ v9,36,s65,3200
S_ m0,s51,26368
Mvvv 84,38,224
E_ 109
Mvvv 84,42,244
L_ v9,36,s65,256
Mvvv 88,38,228
E_ 110
Mvvv 88,42,248
L_ v9,36,s65,3328
Mvvv 92,38,232
E_ 111
Mvvv 92,42,252
W_ 0
C_ 111,110,111
C_ 110,108,109
Mavv 0,30,116
s_nop 0
Tv 38,v4,0
Tv 40,v4,6144
Mavv 20,34,116
E_ 112
Mavv 24,34,120
s_nop 0
Tv 42,v5,0
Tv 44,v5,6144
Mavv 4,30,120
E_ 113
Mavv 8,30,124
E_ 114
Mavv 28,34,124
E_ 115
Mavv 32,34,128
C_ 115,114,115
C_ 114,112,113
Mavv 12,30,128
E_ 76
Mavv 16,30,132
E_ 77
Mavv 36,34,132
W_ 0
E_ 78
Mavv 56,38,132
s_nop 0
Tv 30,v2,1024
Tv 32,v2,7168
Mavv 76,42,132
E_ 79
Mavv 72,42,128
s_nop 0
Tv 34,v3,1024
Tv 36,v3,7168
Mavv 52,38,128
C_ 96,76,77
C_ 97,78,79
Mavv 48,38,124
E_ 80
Mavv 68,42,124
E_ 81
Mavv 64,42,120
E_ 82
Mavv 44,38,120
E_ 83
Mavv 40,38,116
C_ 100,80,81
C_ 101,82,83
Mavv 60,42,116
W_ 0
E_ 84
Mavv 80,30,116
s_nop 0
Tv 38,v4,1024
Tv 40,v4,7168
Mavv 100,34,116
E_ 85
Mavv 104,34,120
s_nop 0
Tv 42,v5,1024
Tv 44,v5,7168
Mavv 84,30,120
E_ 86
Mavv 88,30,124
E_ 87
Mavv 108,34,124
C_ 104,84,85
C_ 105,86,87
Mavv 112,34,128
E_ 88
Mavv 92,30,128
E_ 89
Mavv 96,30,132
E_ 90
Mavv 116,34,132
W_ 0
E_ 91
Mavv 136,38,132
s_nop 0
Tv 30,v2,2048
Tv 32,v2,8192
Mavv 156,42,132
C_ 108,88,89
C_ 109,90,91
Mavv 152,42,128
s_nop 0
Tv 34,v3,2048
Tv 36,v3,8192
Mavv 132,38,128
E_ 92
Mavv 128,38,124
E_ 93
Mavv 148,42,124
E_ 94
Mavv 144,42,120
E_ 95
Mavv 124,38,120
C_ 112,92,93
C_ 113,94,95
Mavv 120,38,116
v_mov_b32 v92, 0x3f803f80
v_mov_b32 v93, 0x3f803f80
Mavv 140,42,116
W_ 0
v_mov_b32 v94, 0x3f803f80
v_mov_b32 v95, 0x3f803f80
Mavv 160,30,116
s_nop 0
Tv 38,v4,2048
Tv 40,v4,8192
Mavv 180,34,116
Mavv 184,34,120
s_nop 0
Tv 42,v5,2048
Tv 44,v5,8192
Mavv 164,30,120
Mavv 168,30,124
Mavv 188,34,124
S_ s65,s61,98304
Mavv 192,34,128
S_ m0,s51,0
Mavv 172,30,128
s_waitcnt vmcnt(7) lgkmcnt(0)
s_barrier
Mavv 176,30,132
Ln v7,32,s65,0
Mavv 196,34,132
Mavv 216,38,132
Dv 30,%0,18432
Mavv 236,42,132
Dv 34,v1,18432
Mavv 232,42,128
Ln v8,32,s65,3072
S_ m0,s51,896
Mavv 212,38,128
Mavv 208,38,124
Ln v7,32,s65,128
Mavv 228,42,124
Mavv 224,42,120
Ln v8,32,s65,3200
S_ m0,s51,1792
Mavv 204,38,120
Mavv 200,38,116
Ln v7,32,s65,256
Mavv 220,42,116
s_cmp_eq_u32 s4, 0
s_cbranch_scc1 .Lhs_c1
.Lhs_c1_back:
.Lstep1_7:
Zvvv 76,92,96
Dv 38,%0,19456
Zvvv 80,92,100
Dv 42,v1,19456
Zvvv 84,92,104
Zvvv 88,92,108
Zvvv 92,92,112
W_ 2
Nvvv 116,30,136,56
Mvvv 116,34,156
Nvvv 120,30,140,60
s_nop 1
v_max3_f32 v10, v76, v80, v84
s_nop 0
v_max3_f32 v10, v10, v88, v92
Mvvv 120,34,160
v_cmp_lt_f32 vcc, 0x42000000, v10
s_cbranch_vccnz .Ltramp1_6
Nvvv 124,30,144,64
A_ v46,v46,v76
A_ v47,v47,v80
Mvvv 124,34,164
A_ v48,v48,v84
A_ v49,v49,v88
Nvvv 128,30,148,68
A_ v50,v50,v92
Mvvv 128,34,168
Ln v8,32,s65,3328
Nvvv 132,30,152,72
Mvvv 132,34,172
W_ 0
Mvvv 132,38,192
Dv 30,%0,20480
Mvvv 132,42,212
Dv 34,v1,20480
Mvvv 128,38,188
Mvvv 128,42,208
Mvvv 124,38,184
Mvvv 124,42,204
Mvvv 120,38,180
Mvvv 120,42,200
Mvvv 116,38,176
Mvvv 116,42,196
W_ 0
Mvvv 116,30,216
Dv 38,%0,12288
Mvvv 116,34,236
Dv 42,v1,12288
Mvvv 120,30,220
Mvvv 120,34,240
Mvvv 124,30,224
Mvvv 124,34,244
Mvvv 128,30,228
Mvvv 128,34,248
s_nop 0
E_ 116
Mvvv 132,30,232
E_ 117
Mvvv 132,34,252
W_ 0
E_ 118
Nvvv 76,38,136,56
Dv 30,%0,13312
Mvvv 76,42,156
Dv 34,v1,13312
Nvvv 80,38,140,60
E_ 119
Mvvv 80,42,160
C_ 119,118,119
C_ 118,116,117
Nvvv 84,38,144,64
E_ 120
Mvvv 84,42,164
E_ 121
Nvvv 88,38,148,68
E_ 122
Mvvv 88,42,168
E_ 123
Nvvv 92,38,152,72
C_ 123,122,123
C_ 122,120,121
Mvvv 92,42,172
W_ 0
E_ 124
Mvvv 92,30,192
Dv 38,%0,14336
Mvvv 92,34,212
Dv 42,v1,14336
Mvvv 88,30,188
E_ 125
Mvvv 88,34,208
E_ 126
Mvvv 84,30,184
S_ s65,s61,86016
E_ 127
Mvvv 84,34,204
S_ m0,s51,12288
C_ 127,126,127
C_ 126,124,125
Mvvv 80,30,180
s_waitcnt vmcnt(6) lgkmcnt(0)
s_barrier
Mvvv 80,34,200
L_ v9,36,s65,0
Mvvv 76,30,176
E_ 128
Mvvv 76,34,196
L_ v9,36,s65,3072
S_ m0,s51,13184
Mvvv 76,38,216
Tv 30,v2,24576
Tv 32,v2,30720
Mvvv 76,42,236
L_ v9,36,s65,128
Mvvv 80,38,220
s_nop 0
Tv 34,v3,24576
Tv 36,v3,30720
Mvvv 80,42,240
L_ v9,36,s65,3200
S_ m0,s51,14080
Mvvv 84,38,224
E_ 129
Mvvv 84,42,244
L_ v9,36,s65,256
Mvvv 88,38,228
E_ 130
Mvvv 88,42,248
L_ v9,36,s65,3328
Mvvv 92,38,232
E_ 131
Mvvv 92,42,252
W_ 0
C_ 131,130,131
C_ 130,128,129
Mavv 0,30,96
s_nop 0
Tv 38,v4,24576
Tv 40,v4,30720
Mavv 20,34,96
E_ 132
Mavv 24,34,100
s_nop 0
Tv 42,v5,24576
Tv 44,v5,30720
Mavv 4,30,100
E_ 133
Mavv 8,30,104
E_ 134
Mavv 28,34,104
E_ 135
Mavv 32,34,108
C_ 135,134,135
C_ 134,132,133
Mavv 12,30,108
E_ 76
Mavv 16,30,112
E_ 77
Mavv 36,34,112
W_ 0
E_ 78
Mavv 56,38,112
s_nop 0
Tv 30,v2,25600
Tv 32,v2,31744
Mavv 76,42,112
E_ 79
Mavv 72,42,108
s_nop 0
Tv 34,v3,25600
Tv 36,v3,31744
Mavv 52,38,108
C_ 116,76,77
C_ 117,78,79
Mavv 48,38,104
E_ 80
Mavv 68,42,104
E_ 81
Mavv 64,42,100
E_ 82
Mavv 44,38,100
E_ 83
Mavv 40,38,96
C_ 120,80,81
C_ 121,82,83
Mavv 60,42,96
W_ 0
E_ 84
Mavv 80,30,96
s_nop 0
Tv 38,v4,25600
Tv 40,v4,31744
Mavv 100,34,96
E_ 85
Mavv 104,34,100
s_nop 0
Tv 42,v5,25600
Tv 44,v5,31744
Mavv 84,30,100
E_ 86
Mavv 88,30,104
E_ 87
Mavv 108,34,104
C_ 124,84,85
C_ 125,86,87
Mavv 112,34,108
E_ 88
Mavv 92,30,108
E_ 89
Mavv 96,30,112
E_ 90
Mavv 116,34,112
W_ 0
E_ 91
Mavv 136,38,112
s_nop 0
Tv 30,v2,26624
Tv 32,v2,32768
Mavv 156,42,112
C_ 128,88,89
C_ 129,90,91
Mavv 152,42,108
s_nop 0
Tv 34,v3,26624
Tv 36,v3,32768
Mavv 132,38,108
E_ 92
Mavv 128,38,104
E_ 93
Mavv 148,42,104
E_ 94
Mavv 144,42,100
E_ 95
Mavv 124,38,100
C_ 132,92,93
C_ 133,94,95
Mavv 120,38,96
v_mov_b32 v92, 0x3f803f80
v_mov_b32 v93, 0x3f803f80
Mavv 140,42,96
W_ 0
v_mov_b32 v94, 0x3f803f80
v_mov_b32 v95, 0x3f803f80
Mavv 160,30,96
s_nop 0
Tv 38,v4,26624
Tv 40,v4,32768
Mavv 180,34,96
Mavv 184,34,100
s_nop 0
Tv 42,v5,26624
Tv 44,v5,32768
Mavv 164,30,100
Mavv 168,30,104
Mavv 188,34,104
S_ s65,s61,110592
Mavv 192,34,108
S_ m0,s51,24576
Mavv 172,30,108
s_waitcnt vmcnt(6) lgkmcnt(0)
s_barrier
Mavv 176,30,112
Ln v7,32,s65,0
Mavv 196,34,112
Mavv 216,38,112
Dv 30,%0,6144
Mavv 236,42,112
Dv 34,v1,6144
Mavv 232,42,108
Ln v8,32,s65,3072
S_ m0,s51,25472
Mavv 212,38,108
Mavv 208,38,104
Ln v7,32,s65,128
Mavv 228,42,104
Mavv 224,42,100
Ln v8,32,s65,3200
S_ m0,s51,26368
Mavv 204,38,100
Mavv 200,38,96
Ln v7,32,s65,256
Mavv 220,42,96
.Lstep1_8:
Zvvv 76,92,116
Dv 38,%0,7168
Zvvv 80,92,120
Dv 42,v1,7168
Zvvv 84,92,124
Zvvv 88,92,128
Zvvv 92,92,132
W_ 2
Nvvv 96,30,136,56
Mvvv 96,34,156
Nvvv 100,30,140,60
s_nop 1
v_max3_f32 v10, v76, v80, v84
s_nop 0
v_max3_f32 v10, v10, v88, v92
Mvvv 100,34,160
v_cmp_lt_f32 vcc, 0x42000000, v10
s_cbranch_vccnz .Ltramp1_7
Nvvv 104,30,144,64
A_ v46,v46,v76
A_ v47,v47,v80
Mvvv 104,34,164
A_ v48,v48,v84
A_ v49,v49,v88
Nvvv 108,30,148,68
A_ v50,v50,v92
Mvvv 108,34,168
Ln v8,32,s65,3328
Nvvv 112,30,152,72
Mvvv 112,34,172
W_ 0
Mvvv 112,38,192
Dv 30,%0,8192
Mvvv 112,42,212
Dv 34,v1,8192
Mvvv 108,38,188
Mvvv 108,42,208
Mvvv 104,38,184
Mvvv 104,42,204
Mvvv 100,38,180
Mvvv 100,42,200
Mvvv 96,38,176
Mvvv 96,42,196
W_ 0
Mvvv 96,30,216
Dv 38,%0,0
Mvvv 96,34,236
Dv 42,v1,0
Mvvv 100,30,220
Mvvv 100,34,240
Mvvv 104,30,224
Mvvv 104,34,244
Mvvv 108,30,228
Mvvv 108,34,248
s_nop 0
E_ 96
Mvvv 112,30,232
E_ 97
Mvvv 112,34,252
W_ 0
E_ 98
Nvvv 76,38,136,56
Dv 30,%0,1024
Mvvv 76,42,156
Dv 34,v1,1024
Nvvv 80,38,140,60
E_ 99
Mvvv 80,42,160
C_ 99,98,99
C_ 98,96,97
Nvvv 84,38,144,64
E_ 100
Mvvv 84,42,164
E_ 101
Nvvv 88,38,148,68
E_ 102
Mvvv 88,42,168
E_ 103
Nvvv 92,38,152,72
C_ 103,102,103
C_ 102,100,101
Mvvv 92,42,172
W_ 0
E_ 104
Mvvv 92,30,192
Dv 38,%0,2048
Mvvv 92,34,212
Dv 42,v1,2048
Mvvv 88,30,188
E_ 105
Mvvv 88,34,208
E_ 106
Mvvv 84,30,184
S_ s65,s61,98304
E_ 107
Mvvv 84,34,204
S_ m0,s51,0
C_ 107,106,107
C_ 106,104,105
Mvvv 80,30,180
s_waitcnt vmcnt(6) lgkmcnt(0)
s_barrier
Mvvv 80,34,200
L_ v9,36,s65,0
Mvvv 76,30,176
E_ 108
Mvvv 76,34,196
L_ v9,36,s65,3072
S_ m0,s51,896
Mvvv 76,38,216
Tv 30,v2,12288
Tv 32,v2,18432
Mvvv 76,42,236
L_ v9,36,s65,128
Mvvv 80,38,220
s_nop 0
Tv 34,v3,12288
Tv 36,v3,18432
Mvvv 80,42,240
L_ v9,36,s65,3200
S_ m0,s51,1792
Mvvv 84,38,224
E_ 109
Mvvv 84,42,244
L_ v9,36,s65,256
Mvvv 88,38,228
E_ 110
Mvvv 88,42,248
L_ v9,36,s65,3328
Mvvv 92,38,232
E_ 111
Mvvv 92,42,252
W_ 0
C_ 111,110,111
C_ 110,108,109
Mavv 0,30,116
s_nop 0
Tv 38,v4,12288
Tv 40,v4,18432
Mavv 20,34,116
E_ 112
Mavv 24,34,120
s_nop 0
Tv 42,v5,12288
Tv 44,v5,18432
Mavv 4,30,120
E_ 113
Mavv 8,30,124
E_ 114
Mavv 28,34,124
E_ 115
Mavv 32,34,128
C_ 115,114,115
C_ 114,112,113
Mavv 12,30,128
E_ 76
Mavv 16,30,132
E_ 77
Mavv 36,34,132
W_ 0
E_ 78
Mavv 56,38,132
s_nop 0
Tv 30,v2,13312
Tv 32,v2,19456
Mavv 76,42,132
E_ 79
Mavv 72,42,128
s_nop 0
Tv 34,v3,13312
Tv 36,v3,19456
Mavv 52,38,128
C_ 96,76,77
C_ 97,78,79
Mavv 48,38,124
E_ 80
Mavv 68,42,124
E_ 81
Mavv 64,42,120
E_ 82
Mavv 44,38,120
E_ 83
Mavv 40,38,116
C_ 100,80,81
C_ 101,82,83
Mavv 60,42,116
W_ 0
E_ 84
Mavv 80,30,116
s_nop 0
Tv 38,v4,13312
Tv 40,v4,19456
Mavv 100,34,116
E_ 85
Mavv 104,34,120
s_nop 0
Tv 42,v5,13312
Tv 44,v5,19456
Mavv 84,30,120
E_ 86
Mavv 88,30,124
E_ 87
Mavv 108,34,124
C_ 104,84,85
C_ 105,86,87
Mavv 112,34,128
E_ 88
Mavv 92,30,128
E_ 89
Mavv 96,30,132
E_ 90
Mavv 116,34,132
W_ 0
E_ 91
Mavv 136,38,132
s_nop 0
Tv 30,v2,14336
Tv 32,v2,20480
Mavv 156,42,132
C_ 108,88,89
C_ 109,90,91
Mavv 152,42,128
s_nop 0
Tv 34,v3,14336
Tv 36,v3,20480
Mavv 132,38,128
E_ 92
Mavv 128,38,124
E_ 93
Mavv 148,42,124
E_ 94
Mavv 144,42,120
E_ 95
Mavv 124,38,120
C_ 112,92,93
C_ 113,94,95
Mavv 120,38,116
v_mov_b32 v92, 0x3f803f80
v_mov_b32 v93, 0x3f803f80
Mavv 140,42,116
W_ 0
v_mov_b32 v94, 0x3f803f80
v_mov_b32 v95, 0x3f803f80
Mavv 160,30,116
s_nop 0
Tv 38,v4,14336
Tv 40,v4,20480
Mavv 180,34,116
Mavv 184,34,120
s_nop 0
Tv 42,v5,14336
Tv 44,v5,20480
Mavv 164,30,120
Mavv 168,30,124
Mavv 188,34,124
S_ s65,s61,122880
Mavv 192,34,128
S_ m0,s51,12288
Mavv 172,30,128
s_waitcnt vmcnt(6) lgkmcnt(0)
s_barrier
Mavv 176,30,132
Ln v7,32,s65,0
Mavv 196,34,132
Mavv 216,38,132
Dv 30,%0,30720
Mavv 236,42,132
Dv 34,v1,30720
Mavv 232,42,128
Ln v8,32,s65,3072
S_ m0,s51,13184
)A"
R"A(Mavv 212,38,128
Mavv 208,38,124
Ln v7,32,s65,128
Mavv 228,42,124
Mavv 224,42,120
Ln v8,32,s65,3200
S_ m0,s51,14080
Mavv 204,38,120
Mavv 200,38,116
Ln v7,32,s65,256
Mavv 220,42,116
s_cmp_eq_u32 s4, 0
s_cbranch_scc1 .Lhs_d1
.Lhs_d1_back:
.Lstep1_9:
Zvvv 76,92,96
Dv 38,%0,31744
Zvvv 80,92,100
Dv 42,v1,31744
Zvvv 84,92,104
Zvvv 88,92,108
Zvvv 92,92,112
W_ 2
Nvvv 116,30,136,56
Mvvv 116,34,156
Nvvv 120,30,140,60
s_nop 1
v_max3_f32 v10, v76, v80, v84
s_nop 0
v_max3_f32 v10, v10, v88, v92
Mvvv 120,34,160
v_cmp_lt_f32 vcc, 0x42000000, v10
s_cbranch_vccnz .Ltramp1_8
Nvvv 124,30,144,64
A_ v46,v46,v76
A_ v47,v47,v80
Mvvv 124,34,164
A_ v48,v48,v84
A_ v49,v49,v88
Nvvv 128,30,148,68
A_ v50,v50,v92
Mvvv 128,34,168
Ln v8,32,s65,3328
Nvvv 132,30,152,72
Mvvv 132,34,172
W_ 0
Mvvv 132,38,192
Dv 30,%0,32768
Mvvv 132,42,212
Dv 34,v1,32768
Mvvv 128,38,188
Mvvv 128,42,208
Mvvv 124,38,184
Mvvv 124,42,204
Mvvv 120,38,180
Mvvv 120,42,200
Mvvv 116,38,176
Mvvv 116,42,196
W_ 0
Mvvv 116,30,216
Dv 38,%0,24576
Mvvv 116,34,236
Dv 42,v1,24576
Mvvv 120,30,220
Mvvv 120,34,240
Mvvv 124,30,224
Mvvv 124,34,244
Mvvv 128,30,228
Mvvv 128,34,248
s_nop 0
E_ 116
Mvvv 132,30,232
E_ 117
Mvvv 132,34,252
W_ 0
E_ 118
Nvvv 76,38,136,56
Dv 30,%0,25600
Mvvv 76,42,156
Dv 34,v1,25600
Nvvv 80,38,140,60
E_ 119
Mvvv 80,42,160
C_ 119,118,119
C_ 118,116,117
Nvvv 84,38,144,64
E_ 120
Mvvv 84,42,164
E_ 121
Nvvv 88,38,148,68
E_ 122
Mvvv 88,42,168
E_ 123
Nvvv 92,38,152,72
C_ 123,122,123
C_ 122,120,121
Mvvv 92,42,172
W_ 0
E_ 124
Mvvv 92,30,192
Dv 38,%0,26624
Mvvv 92,34,212
Dv 42,v1,26624
Mvvv 88,30,188
E_ 125
Mvvv 88,34,208
E_ 126
Mvvv 84,30,184
S_ s65,s61,110592
E_ 127
Mvvv 84,34,204
S_ m0,s51,24576
C_ 127,126,127
C_ 126,124,125
Mvvv 80,30,180
s_waitcnt vmcnt(6) lgkmcnt(0)
s_barrier
Mvvv 80,34,200
L_ v9,36,s65,0
Mvvv 76,30,176
E_ 128
Mvvv 76,34,196
L_ v9,36,s65,3072
S_ m0,s51,25472
Mvvv 76,38,216
Tv 30,v2,0
Tv 32,v2,6144
Mvvv 76,42,236
L_ v9,36,s65,128
Mvvv 80,38,220
s_nop 0
Tv 34,v3,0
Tv 36,v3,6144
Mvvv 80,42,240
L_ v9,36,s65,3200
S_ m0,s51,26368
Mvvv 84,38,224
E_ 129
Mvvv 84,42,244
L_ v9,36,s65,256
Mvvv 88,38,228
E_ 130
Mvvv 88,42,248
L_ v9,36,s65,3328
Mvvv 92,38,232
E_ 131
Mvvv 92,42,252
W_ 0
C_ 131,130,131
C_ 130,128,129
Mavv 0,30,96
s_nop 0
Tv 38,v4,0
Tv 40,v4,6144
Mavv 20,34,96
E_ 132
Mavv 24,34,100
s_nop 0
Tv 42,v5,0
Tv 44,v5,6144
Mavv 4,30,100
E_ 133
Mavv 8,30,104
E_ 134
Mavv 28,34,104
E_ 135
Mavv 32,34,108
C_ 135,134,135
C_ 134,132,133
Mavv 12,30,108
E_ 76
Mavv 16,30,112
E_ 77
Mavv 36,34,112
W_ 0
E_ 78
Mavv 56,38,112
s_nop 0
Tv 30,v2,1024
Tv 32,v2,7168
Mavv 76,42,112
E_ 79
Mavv 72,42,108
s_nop 0
Tv 34,v3,1024
Tv 36,v3,7168
Mavv 52,38,108
C_ 116,76,77
C_ 117,78,79
Mavv 48,38,104
E_ 80
Mavv 68,42,104
E_ 81
Mavv 64,42,100
E_ 82
Mavv 44,38,100
E_ 83
Mavv 40,38,96
C_ 120,80,81
C_ 121,82,83
Mavv 60,42,96
W_ 0
E_ 84
Mavv 80,30,96
s_nop 0
Tv 38,v4,1024
Tv 40,v4,7168
Mavv 100,34,96
E_ 85
Mavv 104,34,100
s_nop 0
Tv 42,v5,1024
Tv 44,v5,7168
Mavv 84,30,100
E_ 86
Mavv 88,30,104
E_ 87
Mavv 108,34,104
C_ 124,84,85
C_ 125,86,87
Mavv 112,34,108
E_ 88
Mavv 92,30,108
E_ 89
Mavv 96,30,112
E_ 90
Mavv 116,34,112
W_ 0
E_ 91
Mavv 136,38,112
s_nop 0
Tv 30,v2,2048
Tv 32,v2,8192
Mavv 156,42,112
C_ 128,88,89
C_ 129,90,91
Mavv 152,42,108
s_nop 0
Tv 34,v3,2048
Tv 36,v3,8192
Mavv 132,38,108
E_ 92
Mavv 128,38,104
E_ 93
Mavv 148,42,104
E_ 94
Mavv 144,42,100
E_ 95
Mavv 124,38,100
C_ 132,92,93
C_ 133,94,95
Mavv 120,38,96
v_mov_b32 v92, 0x3f803f80
v_mov_b32 v93, 0x3f803f80
Mavv 140,42,96
W_ 0
v_mov_b32 v94, 0x3f803f80
v_mov_b32 v95, 0x3f803f80
Mavv 160,30,96
s_nop 0
Tv 38,v4,2048
Tv 40,v4,8192
Mavv 180,34,96
Mavv 184,34,100
s_nop 0
Tv 42,v5,2048
Tv 44,v5,8192
Mavv 164,30,100
Mavv 168,30,104
Mavv 188,34,104
S_ s65,s61,135168
Mavv 192,34,108
S_ m0,s51,0
Mavv 172,30,108
s_waitcnt vmcnt(6) lgkmcnt(0)
s_barrier
Mavv 176,30,112
Ln v7,32,s65,0
Mavv 196,34,112
Mavv 216,38,112
Dv 30,%0,18432
Mavv 236,42,112
Dv 34,v1,18432
Mavv 232,42,108
Ln v8,32,s65,3072
S_ m0,s51,896
Mavv 212,38,108
Mavv 208,38,104
Ln v7,32,s65,128
Mavv 228,42,104
Mavv 224,42,100
Ln v8,32,s65,3200
S_ m0,s51,1792
Mavv 204,38,100
Mavv 200,38,96
Ln v7,32,s65,256
Mavv 220,42,96
.Lstep1_10:
Zvvv 76,92,116
Dv 38,%0,19456
Zvvv 80,92,120
Dv 42,v1,19456
Zvvv 84,92,124
Zvvv 88,92,128
Zvvv 92,92,132
W_ 2
Nvvv 96,30,136,56
Mvvv 96,34,156
Nvvv 100,30,140,60
s_nop 1
v_max3_f32 v10, v76, v80, v84
s_nop 0
v_max3_f32 v10, v10, v88, v92
Mvvv 100,34,160
v_cmp_lt_f32 vcc, 0x42000000, v10
s_cbranch_vccnz .Ltramp1_9
Nvvv 104,30,144,64
A_ v46,v46,v76
A_ v47,v47,v80
Mvvv 104,34,164
A_ v48,v48,v84
A_ v49,v49,v88
Nvvv 108,30,148,68
A_ v50,v50,v92
Mvvv 108,34,168
Ln v8,32,s65,3328
Nvvv 112,30,152,72
Mvvv 112,34,172
W_ 0
Mvvv 112,38,192
Dv 30,%0,20480
Mvvv 112,42,212
Dv 34,v1,20480
Mvvv 108,38,188
Mvvv 108,42,208
Mvvv 104,38,184
Mvvv 104,42,204
Mvvv 100,38,180
Mvvv 100,42,200
Mvvv 96,38,176
Mvvv 96,42,196
W_ 0
Mvvv 96,30,216
Dv 38,%0,12288
Mvvv 96,34,236
Dv 42,v1,12288
Mvvv 100,30,220
Mvvv 100,34,240
Mvvv 104,30,224
Mvvv 104,34,244
Mvvv 108,30,228
Mvvv 108,34,248
s_nop 0
E_ 96
Mvvv 112,30,232
E_ 97
Mvvv 112,34,252
W_ 0
E_ 98
Nvvv 76,38,136,56
Dv 30,%0,13312
Mvvv 76,42,156
Dv 34,v1,13312
Nvvv 80,38,140,60
E_ 99
Mvvv 80,42,160
C_ 99,98,99
C_ 98,96,97
Nvvv 84,38,144,64
E_ 100
Mvvv 84,42,164
E_ 101
Nvvv 88,38,148,68
E_ 102
Mvvv 88,42,168
E_ 103
Nvvv 92,38,152,72
C_ 103,102,103
C_ 102,100,101
Mvvv 92,42,172
W_ 0
E_ 104
Mvvv 92,30,192
Dv 38,%0,14336
Mvvv 92,34,212
Dv 42,v1,14336
Mvvv 88,30,188
E_ 105
Mvvv 88,34,208
E_ 106
Mvvv 84,30,184
S_ s65,s61,122880
E_ 107
Mvvv 84,34,204
S_ m0,s51,12288
C_ 107,106,107
C_ 106,104,105
Mvvv 80,30,180
s_waitcnt vmcnt(6) lgkmcnt(0)
s_barrier
Mvvv 80,34,200
L_ v9,36,s65,0
Mvvv 76,30,176
E_ 108
Mvvv 76,34,196
L_ v9,36,s65,3072
S_ m0,s51,13184
Mvvv 76,38,216
Tv 30,v2,24576
Tv 32,v2,30720
Mvvv 76,42,236
L_ v9,36,s65,128
Mvvv 80,38,220
s_nop 0
Tv 34,v3,24576
Tv 36,v3,30720
Mvvv 80,42,240
L_ v9,36,s65,3200
S_ m0,s51,14080
Mvvv 84,38,224
E_ 109
Mvvv 84,42,244
L_ v9,36,s65,256
Mvvv 88,38,228
E_ 110
Mvvv 88,42,248
L_ v9,36,s65,3328
Mvvv 92,38,232
E_ 111
Mvvv 92,42,252
W_ 0
C_ 111,110,111
C_ 110,108,109
Mavv 0,30,116
s_nop 0
Tv 38,v4,24576
Tv 40,v4,30720
Mavv 20,34,116
E_ 112
Mavv 24,34,120
s_nop 0
Tv 42,v5,24576
Tv 44,v5,30720
Mavv 4,30,120
E_ 113
Mavv 8,30,124
E_ 114
Mavv 28,34,124
E_ 115
Mavv 32,34,128
C_ 115,114,115
C_ 114,112,113
Mavv 12,30,128
E_ 76
Mavv 16,30,132
E_ 77
Mavv 36,34,132
W_ 0
E_ 78
Mavv 56,38,132
s_nop 0
Tv 30,v2,25600
Tv 32,v2,31744
Mavv 76,42,132
E_ 79
Mavv 72,42,128
s_nop 0
Tv 34,v3,25600
Tv 36,v3,31744
Mavv 52,38,128
C_ 96,76,77
C_ 97,78,79
Mavv 48,38,124
E_ 80
Mavv 68,42,124
E_ 81
Mavv 64,42,120
E_ 82
Mavv 44,38,120
E_ 83
Mavv 40,38,116
C_ 100,80,81
C_ 101,82,83
Mavv 60,42,116
W_ 0
E_ 84
Mavv 80,30,116
s_nop 0
Tv 38,v4,25600
Tv 40,v4,31744
Mavv 100,34,116
E_ 85
Mavv 104,34,120
s_nop 0
Tv 42,v5,25600
Tv 44,v5,31744
Mavv 84,30,120
E_ 86
Mavv 88,30,124
E_ 87
Mavv 108,34,124
C_ 104,84,85
C_ 105,86,87
Mavv 112,34,128
E_ 88
Mavv 92,30,128
E_ 89
Mavv 96,30,132
E_ 90
Mavv 116,34,132
W_ 0
E_ 91
Mavv 136,38,132
s_nop 0
Tv 30,v2,26624
Tv 32,v2,32768
Mavv 156,42,132
C_ 108,88,89
C_ 109,90,91
Mavv 152,42,128
s_nop 0
Tv 34,v3,26624
Tv 36,v3,32768
Mavv 132,38,128
E_ 92
Mavv 128,38,124
E_ 93
Mavv 148,42,124
E_ 94
Mavv 144,42,120
E_ 95
Mavv 124,38,120
C_ 112,92,93
C_ 113,94,95
Mavv 120,38,116
v_mov_b32 v92, 0x3f803f80
v_mov_b32 v93, 0x3f803f80
Mavv 140,42,116
W_ 0
v_mov_b32 v94, 0x3f803f80
v_mov_b32 v95, 0x3f803f80
Mavv 160,30,116
s_nop 0
Tv 38,v4,26624
Tv 40,v4,32768
Mavv 180,34,116
Mavv 184,34,120
s_nop 0
Tv 42,v5,26624
Tv 44,v5,32768
Mavv 164,30,120
Mavv 168,30,124
Mavv 188,34,124
S_ s65,s61,147456
Mavv 192,34,128
S_ m0,s51,24576
Mavv 172,30,128
s_waitcnt vmcnt(6) lgkmcnt(0)
s_barrier
Mavv 176,30,132
Ln v7,32,s65,0
Mavv 196,34,132
Mavv 216,38,132
Dv 30,%0,6144
Mavv 236,42,132
Dv 34,v1,6144
Mavv 232,42,128
Ln v8,32,s65,3072
S_ m0,s51,25472
Mavv 212,38,128
Mavv 208,38,124
Ln v7,32,s65,128
Mavv 228,42,124
Mavv 224,42,120
Ln v8,32,s65,3200
S_ m0,s51,26368
Mavv 204,38,120
Mavv 200,38,116
Ln v7,32,s65,256
Mavv 220,42,116
.Lstep1_11:
Zvvv 76,92,96
Dv 38,%0,7168
Zvvv 80,92,100
Dv 42,v1,7168
Zvvv 84,92,104
Zvvv 88,92,108
Zvvv 92,92,112
W_ 2
Nvvv 116,30,136,56
Mvvv 116,34,156
Nvvv 120,30,140,60
s_nop 1
v_max3_f32 v10, v76, v80, v84
s_nop 0
v_max3_f32 v10, v10, v88, v92
Mvvv 120,34,160
v_cmp_lt_f32 vcc, 0x42000000, v10
s_cbranch_vccnz .Ltramp1_10
Nvvv 124,30,144,64
A_ v46,v46,v76
A_ v47,v47,v80
Mvvv 124,34,164
A_ v48,v48,v84
A_ v49,v49,v88
Nvvv 128,30,148,68
A_ v50,v50,v92
Mvvv 128,34,168
Ln v8,32,s65,3328
Nvvv 132,30,152,72
Mvvv 132,34,172
W_ 0
Mvvv 132,38,192
Dv 30,%0,8192
Mvvv 132,42,212
Dv 34,v1,8192
Mvvv 128,38,188
Mvvv 128,42,208
Mvvv 124,38,184
Mvvv 124,42,204
Mvvv 120,38,180
Mvvv 120,42,200
Mvvv 116,38,176
Mvvv 116,42,196
W_ 0
Mvvv 116,30,216
Dv 38,%0,0
Mvvv 116,34,236
Dv 42,v1,0
Mvvv 120,30,220
Mvvv 120,34,240
Mvvv 124,30,224
Mvvv 124,34,244
Mvvv 128,30,228
Mvvv 128,34,248
s_nop 0
E_ 116
Mvvv 132,30,232
E_ 117
Mvvv 132,34,252
W_ 0
E_ 118
Nvvv 76,38,136,56
Dv 30,%0,1024
Mvvv 76,42,156
Dv 34,v1,1024
Nvvv 80,38,140,60
E_ 119
Mvvv 80,42,160
C_ 119,118,119
C_ 118,116,117
Nvvv 84,38,144,64
E_ 120
Mvvv 84,42,164
E_ 121
Nvvv 88,38,148,68
E_ 122
Mvvv 88,42,168
E_ 123
Nvvv 92,38,152,72
C_ 123,122,123
C_ 122,120,121
Mvvv 92,42,172
W_ 0
E_ 124
Mvvv 92,30,192
Dv 38,%0,2048
Mvvv 92,34,212
Dv 42,v1,2048
Mvvv 88,30,188
E_ 125
Mvvv 88,34,208
E_ 126
Mvvv 84,30,184
S_ s65,s61,135168
E_ 127
Mvvv 84,34,204
S_ m0,s51,0
C_ 127,126,127
C_ 126,124,125
Mvvv 80,30,180
s_waitcnt vmcnt(6) lgkmcnt(0)
s_barrier
Mvvv 80,34,200
L_ v9,36,s65,0
Mvvv 76,30,176
E_ 128
Mvvv 76,34,196
L_ v9,36,s65,3072
S_ m0,s51,896
Mvvv 76,38,216
Tv 30,v2,12288
Tv 32,v2,18432
Mvvv 76,42,236
L_ v9,36,s65,128
Mvvv 80,38,220
s_nop 0
Tv 34,v3,12288
Tv 36,v3,18432
Mvvv 80,42,240
L_ v9,36,s65,3200
S_ m0,s51,1792
Mvvv 84,38,224
E_ 129
Mvvv 84,42,244
L_ v9,36,s65,256
Mvvv 88,38,228
E_ 130
Mvvv 88,42,248
L_ v9,36,s65,3328
Mvvv 92,38,232
E_ 131
Mvvv 92,42,252
W_ 0
C_ 131,130,131
C_ 130,128,129
Mavv 0,30,96
s_nop 0
Tv 38,v4,12288
Tv 40,v4,18432
Mavv 20,34,96
E_ 132
Mavv 24,34,100
s_nop 0
Tv 42,v5,12288
Tv 44,v5,18432
Mavv 4,30,100
E_ 133
Mavv 8,30,104
E_ 134
Mavv 28,34,104
E_ 135
Mavv 32,34,108
C_ 135,134,135
C_ 134,132,133
Mavv 12,30,108
E_ 76
Mavv 16,30,112
E_ 77
Mavv 36,34,112
W_ 0
E_ 78
Mavv 56,38,112
s_nop 0
Tv 30,v2,13312
Tv 32,v2,19456
Mavv 76,42,112
E_ 79
Mavv 72,42,108
s_nop 0
Tv 34,v3,13312
Tv 36,v3,19456
Mavv 52,38,108
C_ 116,76,77
C_ 117,78,79
Mavv 48,38,104
E_ 80
Mavv 68,42,104
E_ 81
Mavv 64,42,100
E_ 82
Mavv 44,38,100
E_ 83
Mavv 40,38,96
C_ 120,80,81
C_ 121,82,83
Mavv 60,42,96
W_ 0
E_ 84
Mavv 80,30,96
s_nop 0
Tv 38,v4,13312
Tv 40,v4,19456
Mavv 100,34,96
E_ 85
Mavv 104,34,100
s_nop 0
Tv 42,v5,13312
Tv 44,v5,19456
Mavv 84,30,100
E_ 86
Mavv 88,30,104
E_ 87
Mavv 108,34,104
C_ 124,84,85
C_ 125,86,87
Mavv 112,34,108
E_ 88
Mavv 92,30,108
E_ 89
Mavv 96,30,112
E_ 90
Mavv 116,34,112
W_ 0
E_ 91
Mavv 136,38,112
s_nop 0
Tv 30,v2,14336
Tv 32,v2,20480
Mavv 156,42,112
C_ 128,88,89
C_ 129,90,91
Mavv 152,42,108
s_nop 0
Tv 34,v3,14336
Tv 36,v3,20480
Mavv 132,38,108
E_ 92
Mavv 128,38,104
E_ 93
Mavv 148,42,104
E_ 94
Mavv 144,42,100
E_ 95
Mavv 124,38,100
C_ 132,92,93
C_ 133,94,95
Mavv 120,38,96
v_mov_b32 v92, 0x3f803f80
v_mov_b32 v93, 0x3f803f80
Mavv 140,42,96
W_ 0
v_mov_b32 v94, 0x3f803f80
v_mov_b32 v95, 0x3f803f80
Mavv 160,30,96
s_nop 0
Tv 38,v4,14336
Tv 40,v4,20480
Mavv 180,34,96
Mavv 184,34,100
s_nop 0
Tv 42,v5,14336
Tv 44,v5,20480
Mavv 164,30,100
Mavv 168,30,104
Mavv 188,34,104
S_ s65,s61,159744
Mavv 192,34,108
S_ m0,s51,12288
Mavv 172,30,108
s_waitcnt vmcnt(6) lgkmcnt(0)
s_barrier
Mavv 176,30,112
Ln v7,32,s65,0
Mavv 196,34,112
Mavv 216,38,112
Dv 30,%0,30720
Mavv 236,42,112
Dv 34,v1,30720
Mavv 232,42,108
Ln v8,32,s65,3072
S_ m0,s51,13184
Mavv 212,38,108
Mavv 208,38,104
Ln v7,32,s65,128
Mavv 228,42,104
Mavv 224,42,100
Ln v8,32,s65,3200
S_ m0,s51,14080
Mavv 204,38,100
Mavv 200,38,96
Ln v7,32,s65,256
Mavv 220,42,96
.Lstep1_12:
Zvvv 76,92,116
Dv 38,%0,31744
Zvvv 80,92,120
Dv 42,v1,31744
Zvvv 84,92,124
Zvvv 88,92,128
Zvvv 92,92,132
W_ 2
Nvvv 96,30,136,56
Mvvv 96,34,156
Nvvv 100,30,140,60
s_nop 1
v_max3_f32 v10, v76, v80, v84
s_nop 0
v_max3_f32 v10, v10, v88, v92
Mvvv 100,34,160
v_cmp_lt_f32 vcc, 0x42000000, v10
s_cbranch_vccnz .Ltramp1_11
Nvvv 104,30,144,64
A_ v46,v46,v76
A_ v47,v47,v80
Mvvv 104,34,164
A_ v48,v48,v84
A_ v49,v49,v88
Nvvv 108,30,148,68
A_ v50,v50,v92
Mvvv 108,34,168
Ln v8,32,s65,3328
Nvvv 112,30,152,72
R_ v11,a248
Mvvv 112,34,172
W_ 0
v_min_u32 v11, 240, v11
Mvvv 112,38,192
Dv 30,%0,32768
v_lshlrev_b32 v11, 3, v11
Mvvv 112,42,212
Dv 34,v1,32768
Mvvv 108,38,188
buffer_load_dword a254, v11, s[44:47], s66 offen
Mvvv 108,42,208
Mvvv 104,38,184
Mvvv 104,42,204
Mvvv 100,38,180
Mvvv 100,42,200
Mvvv 96,38,176
Mvvv 96,42,196
W_ 0
Mvvv 96,30,216
Dv 38,%0,24576
Mvvv 96,34,236
Dv 42,v1,24576
Mvvv 100,30,220
Mvvv 100,34,240
Mvvv 104,30,224
Mvvv 104,34,244
Mvvv 108,30,228
Mvvv 108,34,248
s_nop 0
E_ 96
Mvvv 112,30,232
E_ 97
Mvvv 112,34,252
W_ 0
E_ 98
Nvvv 76,38,136,56
Dv 30,%0,25600
Mvvv 76,42,156
Dv 34,v1,25600
Nvvv 80,38,140,60
E_ 99
Mvvv 80,42,160
C_ 99,98,99
C_ 98,96,97
Nvvv 84,38,144,64
E_ 100
Mvvv 84,42,164
E_ 101
Nvvv 88,38,148,68
E_ 102
Mvvv 88,42,168
E_ 103
Nvvv 92,38,152,72
C_ 103,102,103
C_ 102,100,101
Mvvv 92,42,172
W_ 0
E_ 104
Mvvv 92,30,192
Dv 38,%0,26624
Mvvv 92,34,212
Dv 42,v1,26624
Mvvv 88,30,188
E_ 105
Mvvv 88,34,208
E_ 106
Mvvv 84,30,184
S_ s65,s61,147456
E_ 107
Mvvv 84,34,204
S_ m0,s51,24576
C_ 107,106,107
C_ 106,104,105
Mvvv 80,30,180
s_waitcnt vmcnt(7) lgkmcnt(0)
s_barrier
Mvvv 80,34,200
Ln v9,36,s65,0
Mvvv 76,30,176
E_ 108
Mvvv 76,34,196
Ln v9,36,s65,3072
S_ m0,s51,25472
Mvvv 76,38,216
Tv 30,v2,0
Tv 32,v2,6144
Mvvv 76,42,236
Ln v9,36,s65,128
Mvvv 80,38,220
s_nop 0
Tv 34,v3,0
Tv 36,v3,6144
Mvvv 80,42,240
Ln v9,36,s65,3200
S_ m0,s51,26368
Mvvv 84,38,224
E_ 109
Mvvv 84,42,244
Ln v9,36,s65,256
Mvvv 88,38,228
E_ 110
Mvvv 88,42,248
Ln v9,36,s65,3328
Mvvv 92,38,232
E_ 111
Mvvv 92,42,252
W_ 0
C_ 111,110,111
C_ 110,108,109
Mavv 0,30,116
s_nop 0
Tv 38,v4,0
Tv 40,v4,6144
Mavv 20,34,116
E_ 112
Mavv 24,34,120
s_nop 0
Tv 42,v5,0
Tv 44,v5,6144
Mavv 4,30,120
E_ 113
Mavv 8,30,124
E_ 114
Mavv 28,34,124
E_ 115
Mavv 32,34,128
C_ 115,114,115
C_ 114,112,113
Mavv 12,30,128
E_ 76
Mavv 16,30,132
E_ 77
Mavv 36,34,132
W_ 0
E_ 78
Mavv 56,38,132
s_nop 0
Tv 30,v2,1024
Tv 32,v2,7168
Mavv 76,42,132
E_ 79
Mavv 72,42,128
s_nop 0
Tv 34,v3,1024
Tv 36,v3,7168
Mavv 52,38,128
C_ 96,76,77
C_ 97,78,79
Mavv 48,38,124
E_ 80
Mavv 68,42,124
E_ 81
Mavv 64,42,120
E_ 82
Mavv 44,38,120
E_ 83
Mavv 40,38,116
C_ 100,80,81
C_ 101,82,83
Mavv 60,42,116
W_ 0
E_ 84
Mavv 80,30,116
s_nop 0
Tv 38,v4,1024
Tv 40,v4,7168
Mavv 100,34,116
E_ 85
Mavv 104,34,120
s_nop 0
Tv 42,v5,1024
Tv 44,v5,7168
Mavv 84,30,120
E_ 86
Mavv 88,30,124
E_ 87
Mavv 108,34,124
C_ 104,84,85
C_ 105,86,87
Mavv 112,34,128
E_ 88
Mavv 92,30,128
E_ 89
Mavv 96,30,132
E_ 90
Mavv 116,34,132
W_ 0
E_ 91
Mavv 136,38,132
s_nop 0
Tv 30,v2,2048
Tv 32,v2,8192
Mavv 156,42,132
C_ 108,88,89
C_ 109,90,91
Mavv 152,42,128
s_nop 0
Tv 34,v3,2048
Tv 36,v3,8192
Mavv 132,38,128
E_ 92
Mavv 128,38,124
E_ 93
Mavv 148,42,124
E_ 94
Mavv 144,42,120
E_ 95
Mavv 124,38,120
C_ 112,92,93
C_ 113,94,95
Mavv 120,38,116
v_mov_b32 v92, 0x3f803f80
v_mov_b32 v93, 0x3f803f80
Mavv 140,42,116
W_ 0
v_mov_b32 v94, 0x3f803f80
v_mov_b32 v95, 0x3f803f80
Mavv 160,30,116
s_nop 0
Tv 38,v4,2048
Tv 40,v4,8192
Mavv 180,34,116
Mavv 184,34,120
s_nop 0
Tv 42,v5,2048
Tv 44,v5,8192
Mavv 164,30,120
Mavv 168,30,124
Mavv 188,34,124
S_ s65,s61,172032
Mavv 192,34,128
S_ m0,s51,0
Mavv 172,30,128
s_waitcnt vmcnt(7) lgkmcnt(0)
s_barrier
Mavv 176,30,132
Ln v7,32,s65,0
Mavv 196,34,132
Mavv 216,38,132
Dv 30,%0,18432
Mavv 236,42,132
Dv 34,v1,18432
Mavv 232,42,128
Ln v8,32,s65,3072
S_ m0,s51,896
Mavv 212,38,128
Mavv 208,38,124
Ln v7,32,s65,128
Mavv 228,42,124
Mavv 224,42,120
Ln v8,32,s65,3200
S_ m0,s51,1792
Mavv 204,38,120
Mavv 200,38,116
Ln v7,32,s65,256
Mavv 220,42,116
.Lstep1_13:
Zvvv 76,92,96
Dv 38,%0,19456
Zvvv 80,92,100
Dv 42,v1,19456
Zvvv 84,92,104
Zvvv 88,92,108
Zvvv 92,92,112
W_ 2
Nvvv 116,30,136,56
Mvvv 116,34,156
Nvvv 120,30,140,60
s_nop 1
v_max3_f32 v10, v76, v80, v84
s_nop 0
v_max3_f32 v10, v10, v88, v92
Mvvv 120,34,160
v_cmp_lt_f32 vcc, 0x42000000, v10
s_cbranch_vccnz .Ltramp1_12
Nvvv 124,30,144,64
A_ v46,v46,v76
A_ v47,v47,v80
Mvvv 124,34,164
A_ v48,v48,v84
A_ v49,v49,v88
Nvvv 128,30,148,68
A_ v50,v50,v92
Mvvv 128,34,168
Ln v8,32,s65,3328
Nvvv 132,30,152,72
Mvvv 132,34,172
W_ 0
Mvvv 132,38,192
Dv 30,%0,20480
Mvvv 132,42,212
Dv 34,v1,20480
Mvvv 128,38,188
Mvvv 128,42,208
Mvvv 124,38,184
Mvvv 124,42,204
Mvvv 120,38,180
Mvvv 120,42,200
Mvvv 116,38,176
Mvvv 116,42,196
W_ 0
Mvvv 116,30,216
Dv 38,%0,12288
Mvvv 116,34,236
Dv 42,v1,12288
Mvvv 120,30,220
Mvvv 120,34,240
Mvvv 124,30,224
Mvvv 124,34,244
Mvvv 128,30,228
Mvvv 128,34,248
s_nop 0
E_ 116
Mvvv 132,30,232
E_ 117
Mvvv 132,34,252
W_ 0
E_ 118
Nvvv 76,38,136,56
Dv 30,%0,13312
Mvvv 76,42,156
Dv 34,v1,13312
Nvvv 80,38,140,60
E_ 119
Mvvv 80,42,160
C_ 119,118,119
C_ 118,116,117
Nvvv 84,38,144,64
E_ 120
Mvvv 84,42,164
E_ 121
Nvvv 88,38,148,68
E_ 122
Mvvv 88,42,168
E_ 123
Nvvv 92,38,152,72
C_ 123,122,123
C_ 122,120,121
Mvvv 92,42,172
W_ 0
E_ 124
Mvvv 92,30,192
Dv 38,%0,14336
Mvvv 92,34,212
Dv 42,v1,14336
Mvvv 88,30,188
E_ 125
Mvvv 88,34,208
E_ 126
Mvvv 84,30,184
S_ s65,s61,159744
E_ 127
Mvvv 84,34,204
S_ m0,s51,12288
C_ 127,126,127
C_ 126,124,125
Mvvv 80,30,180
s_waitcnt vmcnt(6) lgkmcnt(0)
s_barrier
Mvvv 80,34,200
Ln v9,36,s65,0
Mvvv 76,30,176
E_ 128
Mvvv 76,34,196
Ln v9,36,s65,3072
S_ m0,s51,13184
Mvvv 76,38,216
Tv 30,v2,24576
Tv 32,v2,30720
Mvvv 76,42,236
Ln v9,36,s65,128
Mvvv 80,38,220
s_nop 0
Tv 34,v3,24576
Tv 36,v3,30720
Mvvv 80,42,240
Ln v9,36,s65,3200
S_ m0,s51,14080
Mvvv 84,38,224
E_ 129
Mvvv 84,42,244
Ln v9,36,s65,256
Mvvv 88,38,228
E_ 130
Mvvv 88,42,248
Ln v9,36,s65,3328
Mvvv 92,38,232
E_ 131
Mvvv 92,42,252
W_ 0
C_ 131,130,131
C_ 130,128,129
Mavv 0,30,96
s_nop 0
Tv 38,v4,24576
Tv 40,v4,30720
Mavv 20,34,96
E_ 132
Mavv 24,34,100
s_nop 0
Tv 42,v5,24576
Tv 44,v5,30720
Mavv 4,30,100
E_ 133
Mavv 8,30,104
E_ 134
Mavv 28,34,104
E_ 135
Mavv 32,34,108
C_ 135,134,135
C_ 134,132,133
Mavv 12,30,108
E_ 76
Mavv 16,30,112
E_ 77
Mavv 36,34,112
W_ 0
E_ 78
Mavv 56,38,112
s_nop 0
Tv 30,v2,25600
Tv 32,v2,31744
Mavv 76,42,112
E_ 79
Mavv 72,42,108
s_nop 0
Tv 34,v3,25600
Tv 36,v3,31744
Mavv 52,38,108
C_ 116,76,77
C_ 117,78,79
Mavv 48,38,104
E_ 80
Mavv 68,42,104
E_ 81
Mavv 64,42,100
E_ 82
Mavv 44,38,100
E_ 83
Mavv 40,38,96
C_ 120,80,81
C_ 121,82,83
Mavv 60,42,96
W_ 0
E_ 84
Mavv 80,30,96
s_nop 0
Tv 38,v4,25600
Tv 40,v4,31744
Mavv 100,34,96
E_ 85
Mavv 104,34,100
s_nop 0
Tv 42,v5,25600
Tv 44,v5,31744
Mavv 84,30,100
E_ 86
Mavv 88,30,104
E_ 87
Mavv 108,34,104
C_ 124,84,85
C_ 125,86,87
Mavv 112,34,108
E_ 88
Mavv 92,30,108
E_ 89
Mavv 96,30,112
E_ 90
Mavv 116,34,112
W_ 0
E_ 91
Mavv 136,38,112
s_nop 0
Tv 30,v2,26624
Tv 32,v2,32768
Mavv 156,42,112
C_ 128,88,89
C_ 129,90,91
Mavv 152,42,108
s_nop 0
Tv 34,v3,26624
Tv 36,v3,32768
Mavv 132,38,108
E_ 92
Mavv 128,38,104
E_ 93
Mavv 148,42,104
E_ 94
Mavv 144,42,100
E_ 95
Mavv 124,38,100
C_ 132,92,93
C_ 133,94,95
Mavv 120,38,96
v_mov_b32 v92, 0x3f803f80
v_mov_b32 v93, 0x3f803f80
Mavv 140,42,96
W_ 0
v_mov_b32 v94, 0x3f803f80
v_mov_b32 v95, 0x3f803f80
Mavv 160,30,96
s_nop 0
Tv 38,v4,26624
Tv 40,v4,32768
Mavv 180,34,96
Mavv 184,34,100
s_nop 0
Tv 42,v5,26624
Tv 44,v5,32768
Mavv 164,30,100
Mavv 168,30,104
Mavv 188,34,104
S_ s65,s61,184320
Mavv 192,34,108
S_ m0,s51,24576
Mavv 172,30,108
s_waitcnt vmcnt(6) lgkmcnt(0)
s_barrier
Mavv 176,30,112
Ln v7,32,s65,0
Mavv 196,34,112
Mavv 216,38,112
Dv 30,%0,6144
Mavv 236,42,112
Dv 34,v1,6144
Mavv 232,42,108
Ln v8,32,s65,3072
S_ m0,s51,25472
Mavv 212,38,108
Mavv 208,38,104
Ln v7,32,s65,128
Mavv 228,42,104
Mavv 224,42,100
Ln v8,32,s65,3200
S_ m0,s51,26368
Mavv 204,38,100
Mavv 200,38,96
Ln v7,32,s65,256
Mavv 220,42,96
.Lstep1_14:
Zvvv 76,92,116
Dv 38,%0,7168
Zvvv 80,92,120
Dv 42,v1,7168
Zvvv 84,92,124
Zvvv 88,92,128
Zvvv 92,92,132
W_ 2
Nvvv 96,30,136,56
Mvvv 96,34,156
Nvvv 100,30,140,60
s_nop 1
v_max3_f32 v10, v76, v80, v84
s_nop 0
v_max3_f32 v10, v10, v88, v92
Mvvv 100,34,160
v_cmp_lt_f32 vcc, 0x42000000, v10
s_cbranch_vccnz .Ltramp1_13
Nvvv 104,30,144,64
A_ v46,v46,v76
A_ v47,v47,v80
Mvvv 104,34,164
A_ v48,v48,v84
A_ v49,v49,v88
Nvvv 108,30,148,68
A_ v50,v50,v92
Mvvv 108,34,168
Ln v8,32,s65,3328
Nvvv 112,30,152,72
Mvvv 112,34,172
W_ 0
Mvvv 112,38,192
Dv 30,%0,8192
Mvvv 112,42,212
Dv 34,v1,8192
Mvvv 108,38,188
Mvvv 108,42,208
Mvvv 104,38,184
Mvvv 104,42,204
Mvvv 100,38,180
Mvvv 100,42,200
Mvvv 96,38,176
Mvvv 96,42,196
W_ 0
Mvvv 96,30,216
Dv 38,%0,0
Mvvv 96,34,236
Dv 42,v1,0
Mvvv 100,30,220
Mvvv 100,34,240
Mvvv 104,30,224
Mvvv 104,34,244
Mvvv 108,30,228
Mvvv 108,34,248
s_nop 0
E_ 96
Mvvv 112,30,232
E_ 97
Mvvv 112,34,252
W_ 0
E_ 98
Nvvv 76,38,136,56
Dv 30,%0,1024
Mvvv 76,42,156
Dv 34,v1,1024
Nvvv 80,38,140,60
E_ 99
Mvvv 80,42,160
C_ 99,98,99
C_ 98,96,97
Nvvv 84,38,144,64
E_ 100
Mvvv 84,42,164
E_ 101
Nvvv 88,38,148,68
E_ 102
Mvvv 88,42,168
E_ 103
Nvvv 92,38,152,72
C_ 103,102,103
C_ 102,100,101
Mvvv 92,42,172
W_ 0
E_ 104
Mvvv 92,30,192
Dv 38,%0,2048
Mvvv 92,34,212
Dv 42,v1,2048
Mvvv 88,30,188
E_ 105
Mvvv 88,34,208
E_ 106
Mvvv 84,30,184
S_ s65,s61,172032
E_ 107
Mvvv 84,34,204
S_ m0,s51,0
C_ 107,106,107
C_ 106,104,105
Mvvv 80,30,180
s_waitcnt vmcnt(6) lgkmcnt(0)
s_barrier
Mvvv 80,34,200
Ln v9,36,s65,0
Mvvv 76,30,176
E_ 108
Mvvv 76,34,196
Ln v9,36,s65,3072
S_ m0,s51,896
Mvvv 76,38,216
Tv 30,v2,12288
Tv 32,v2,18432
Mvvv 76,42,236
Ln v9,36,s65,128
Mvvv 80,38,220
s_nop 0
Tv 34,v3,12288
Tv 36,v3,18432
Mvvv 80,42,240
Ln v9,36,s65,3200
S_ m0,s51,1792
Mvvv 84,38,224
E_ 109
Mvvv 84,42,244
Ln v9,36,s65,256
Mvvv 88,38,228
E_ 110
Mvvv 88,42,248
Ln v9,36,s65,3328
Mvvv 92,38,232
E_ 111
Mvvv 92,42,252
W_ 0
C_ 111,110,111
C_ 110,108,109
Mavv 0,30,116
s_nop 0
Tv 38,v4,12288
Tv 40,v4,18432
Mavv 20,34,116
E_ 112
Mavv 24,34,120
s_nop 0
Tv 42,v5,12288
Tv 44,v5,18432
Mavv 4,30,120
E_ 113
Mavv 8,30,124
E_ 114
Mavv 28,34,124
E_ 115
Mavv 32,34,128
C_ 115,114,115
C_ 114,112,113
Mavv 12,30,128
E_ 76
Mavv 16,30,132
E_ 77
Mavv 36,34,132
W_ 0
E_ 78
Mavv 56,38,132
s_nop 0
Tv 30,v2,13312
Tv 32,v2,19456
Mavv 76,42,132
E_ 79
Mavv 72,42,128
s_nop 0
Tv 34,v3,13312
Tv 36,v3,19456
Mavv 52,38,128
C_ 96,76,77
C_ 97,78,79
Mavv 48,38,124
E_ 80
Mavv 68,42,124
E_ 81
Mavv 64,42,120
E_ 82
Mavv 44,38,120
E_ 83
Mavv 40,38,116
C_ 100,80,81
C_ 101,82,83
Mavv 60,42,116
W_ 0
E_ 84
Mavv 80,30,116
s_nop 0
Tv 38,v4,13312
Tv 40,v4,19456
Mavv 100,34,116
E_ 85
Mavv 104,34,120
s_nop 0
Tv 42,v5,13312
Tv 44,v5,19456
Mavv 84,30,120
E_ 86
Mavv 88,30,124
E_ 87
Mavv 108,34,124
C_ 104,84,85
C_ 105,86,87
Mavv 112,34,128
E_ 88
Mavv 92,30,128
E_ 89
Mavv 96,30,132
E_ 90
Mavv 116,34,132
W_ 0
E_ 91
Mavv 136,38,132
s_nop 0
Tv 30,v2,14336
Tv 32,v2,20480
Mavv 156,42,132
C_ 108,88,89
C_ 109,90,91
Mavv 152,42,128
s_nop 0
Tv 34,v3,14336
Tv 36,v3,20480
Mavv 132,38,128
E_ 92
Mavv 128,38,124
E_ 93
Mavv 148,42,124
E_ 94
Mavv 144,42,120
E_ 95
Mavv 124,38,120
C_ 112,92,93
C_ 113,94,95
Mavv 120,38,116
v_mov_b32 v92, 0x3f803f80
v_mov_b32 v93, 0x3f803f80
Mavv 140,42,116
W_ 0
v_mov_b32 v94, 0x3f803f80
v_mov_b32 v95, 0x3f803f80
Mavv 160,30,116
s_nop 0
Tv 38,v4,14336
Tv 40,v4,20480
Mavv 180,34,116
Mavv 184,34,120
s_nop 0
Tv 42,v5,14336
Tv 44,v5,20480
Mavv 164,30,120
Mavv 168,30,124
Mavv 188,34,124
S_ s65,s61,184320
Mavv 192,34,128
S_ m0,s51,12288
Mavv 172,30,128
s_waitcnt vmcnt(6) lgkmcnt(0)
s_barrier
Mavv 176,30,132
Ln v9,36,s65,0
Mavv 196,34,132
Mavv 216,38,132
Dv 30,%0,30720
Mavv 236,42,132
Dv 34,v1,30720
Mavv 232,42,128
Ln v9,36,s65,3072
S_ m0,s51,13184
Mavv 212,38,128
Mavv 208,38,124
Ln v9,36,s65,128
Mavv 228,42,124
Mavv 224,42,120
Ln v9,36,s65,3200
S_ m0,s51,14080
Mavv 204,38,120
Mavv 200,38,116
Ln v9,36,s65,256
Mavv 220,42,116
.Lstep1_15:
Zvvv 76,92,96
Dv 38,%0,31744
Zvvv 80,92,100
Dv 42,v1,31744
Zvvv 84,92,104
Zvvv 88,92,108
Zvvv 92,92,112
W_ 2
Nvvv 116,30,136,56
Mvvv 116,34,156
Nvvv 120,30,140,60
s_nop 1
v_max3_f32 v10, v76, v80, v84
s_nop 0
v_max3_f32 v10, v10, v88, v92
Mvvv 120,34,160
v_cmp_lt_f32 vcc, 0x42000000, v10
s_cbranch_vccnz .Ltramp1_14
Nvvv 124,30,144,64
A_ v46,v46,v76
A_ v47,v47,v80
Mvvv 124,34,164
A_ v48,v48,v84
A_ v49,v49,v88
Nvvv 128,30,148,68
A_ v50,v50,v92
Mvvv 128,34,168
Ln v9,36,s65,3328
Nvvv 132,30,152,72
Mvvv 132,34,172
W_ 0
Mvvv 132,38,192
Dv 30,%0,32768
Mvvv 132,42,212
Dv 34,v1,32768
Mvvv 128,38,188
Mvvv 128,42,208
Mvvv 124,38,184
Mvvv 124,42,204
Mvvv 120,38,180
Mvvv 120,42,200
Mvvv 116,38,176
Mvvv 116,42,196
W_ 0
Mvvv 116,30,216
Dv 38,%0,24576
Mvvv 116,34,236
Dv 42,v1,24576
Mvvv 120,30,220
Mvvv 120,34,240
Mvvv 124,30,224
Mvvv 124,34,244
Mvvv 128,30,228
Mvvv 128,34,248
s_nop 0
E_ 116
Mvvv 132,30,232
E_ 117
Mvvv 132,34,252
W_ 0
E_ 118
Nvvv 76,38,136,56
Dv 30,%0,25600
Mvvv 76,42,156
Dv 34,v1,25600
Nvvv 80,38,140,60
E_ 119
Mvvv 80,42,160
C_ 119,118,119
C_ 118,116,117
Nvvv 84,38,144,64
E_ 120
Mvvv 84,42,164
E_ 121
Nvvv 88,38,148,68
E_ 122
Mvvv 88,42,168
E_ 123
Nvvv 92,38,152,72
C_ 123,122,123
C_ 122,120,121
Mvvv 92,42,172
W_ 0
E_ 124
Mvvv 92,30,192
Dv 38,%0,26624
Mvvv 92,34,212
Dv 42,v1,26624
Mvvv 88,30,188
E_ 125
Mvvv 88,34,208
E_ 126
Mvvv 84,30,184
E_ 127
Mvvv 84,34,204
C_ 127,126,127
C_ 126,124,125
Mvvv 80,30,180
s_waitcnt vmcnt(6) lgkmcnt(0)
s_barrier
Mvvv 80,34,200
R_ v7,a248
Mvvv 76,30,176
v_lshrrev_b32 v7, 7, v7
Mvvv 76,34,196
v_mul_u32_u24 v7, 896, v7
Mvvv 76,38,216
s_nop 0
Tv 30,v2,0
Tv 32,v2,6144
Mvvv 76,42,236
v_add_u32 v7, v7, v9
Mvvv 80,38,220
S_ s71,s72,1024
Tv 34,v3,0
Tv 36,v3,6144
Mvvv 80,42,240
S_ s55,s71,491520
E_ 128
Mvvv 84,38,224
S_ m0,s51,24576
E_ 129
Mvvv 84,42,244
L_ v7,24,s55,0
Mvvv 88,38,228
S_ s55,s71,501760
E_ 130
Mvvv 88,42,248
S_ m0,s51,25600
E_ 131
Mvvv 92,38,232
L_ v7,24,s55,0
Mvvv 92,42,252
S_ s55,s71,512000
C_ 131,130,131
C_ 130,128,129
W_ 2
Mavv 0,30,96
S_ m0,s51,26624
Tv 38,v4,0
Tv 40,v4,6144
W_ 2
Mavv 20,34,96
L_ v7,24,s55,0
Mavv 24,34,100
S_ s55,s71,522240
Tv 42,v5,0
Tv 44,v5,6144
Mavv 4,30,100
S_ m0,s51,27648
E_ 132
Mavv 8,30,104
L_ v7,24,s55,0
Mavv 28,34,104
S_ s55,s71,532480
E_ 133
Mavv 32,34,108
S_ m0,s51,28672
E_ 134
Mavv 12,30,108
L_ v7,24,s55,0
Mavv 16,30,112
S_ s55,s71,542720
E_ 135
Mavv 36,34,112
S_ m0,s51,29696
C_ 135,134,135
C_ 134,132,133
W_ 2
Mavv 56,38,112
Tv 30,v2,1024
Tv 32,v2,7168
W_ 2
Mavv 76,42,112
L_ v7,24,s55,0
Mavv 72,42,108
Tv 34,v3,1024
Tv 36,v3,7168
Mavv 52,38,108
E_ 76
Mavv 48,38,104
E_ 77
Mavv 68,42,104
E_ 78
Mavv 64,42,100
E_ 79
Mavv 44,38,100
C_ 116,76,77
C_ 117,78,79
Mavv 40,38,96
E_ 80
Mavv 60,42,96
W_ 0
E_ 81
Mavv 80,30,96
s_nop 0
Tv 38,v4,1024
Tv 40,v4,7168
Mavv 100,34,96
E_ 82
Mavv 104,34,100
s_nop 0
Tv 42,v5,1024
Tv 44,v5,7168
Mavv 84,30,100
E_ 83
Mavv 88,30,104
C_ 120,80,81
C_ 121,82,83
Mavv 108,34,104
E_ 84
Mavv 112,34,108
E_ 85
Mavv 92,30,108
E_ 86
Mavv 96,30,112
E_ 87
Mavv 116,34,112
W_ 0
C_ 124,84,85
C_ 125,86,87
Mavv 136,38,112
s_nop 0
Tv 30,v2,2048
Tv 32,v2,8192
Mavv 156,42,112
E_ 88
Mavv 152,42,108
s_nop 0
Tv 34,v3,2048
Tv 36,v3,8192
Mavv 132,38,108
E_ 89
Mavv 128,38,104
E_ 90
Mavv 148,42,104
E_ 91
Mavv 144,42,100
C_ 128,88,89
C_ 129,90,91
Mavv 124,38,100
E_ 92
Mavv 120,38,96
E_ 93
Mavv 140,42,96
W_ 0
E_ 94
Mavv 160,30,96
s_nop 0
Tv 38,v4,2048
Tv 40,v4,8192
Mavv 180,34,96
E_ 95
Mavv 184,34,100
s_nop 0
Tv 42,v5,2048
Tv 44,v5,8192
Mavv 164,30,100
C_ 132,92,93
C_ 133,94,95
Mavv 168,30,104
v_mov_b32 v92, 0x3f803f80
v_mov_b32 v93, 0x3f803f80
Mavv 188,34,104
v_mov_b32 v94, 0x3f803f80
v_mov_b32 v95, 0x3f803f80
Mavv 192,34,108
R_ v11,a248
Mavv 172,30,108
s_waitcnt vmcnt(6) lgkmcnt(0)
s_barrier
v_mul_u32_u24 v11, 80, v11
Mavv 176,30,112
S_ s55,s71,614400
v_min_u32 v11, 29440, v11
Mavv 196,34,112
S_ m0,s51,0
S_ s85,s87,1024
Mavv 216,38,112
s_nop 0
Tv 30,v2,12288
Tv 32,v2,18432
Zvvv 76,92,116
)A"
R"A(L_ v7,24,s55,0
Zvvv 80,92,120
S_ s55,s71,624640
Tv 34,v3,12288
Tv 36,v3,18432
Zvvv 84,92,124
buffer_load_dword a254, v11, s[24:27], s85 offen
Zvvv 88,92,128
S_ m0,s51,1024
S_ s85,s87,1152
Zvvv 92,92,132
L_ v7,24,s55,0
Mavv 236,42,112
S_ s55,s71,634880
Mavv 232,42,108
buffer_load_dword a254, v11, s[24:27], s85 offen
Mavv 212,38,108
S_ m0,s51,2048
S_ s85,s6,1
Mavv 208,38,104
s_and_b32 s85, s85, 7
v_max3_f32 v10, v76, v80, v84
Mavv 228,42,104
s_lshl_b32 s85, s85, 7
v_max3_f32 v10, v10, v88, v92
v_cmp_lt_f32 vcc, 0x42000000, v10
Mavv 224,42,100
L_ v7,24,s55,0
Mavv 204,38,100
S_ s55,s71,645120
S_ s85,s85,s87
Mavv 200,38,96
buffer_load_dword a254, v11, s[24:27], s85 offen
Mavv 220,42,96
S_ m0,s51,3072
s_cbranch_vccnz .Ltramp1_15
.Ljoin1:
W_ 2
Mavv 0,30,116
s_nop 0
Tv 38,v4,12288
Tv 40,v4,18432
W_ 2
Mavv 20,34,116
L_ v7,24,s55,0
Mavv 24,34,120
S_ s55,s71,655360
Tv 42,v5,12288
Tv 44,v5,18432
Mavv 4,30,120
S_ m0,s51,4096
R_ v13,a248
S_ s53,s51,36864
Mavv 8,30,124
L_ v7,24,s55,0
Mavv 28,34,124
S_ s55,s71,665600
v_add_u32 v12, s53, v13
s_mul_i32 s53, s4, 10240
Mavv 32,34,128
S_ m0,s51,5120
v_add_u32 v13, s53, v13
R_ v10,a248
Mavv 12,30,128
L_ v7,24,s55,0
Mavv 16,30,132
A_ v46,v46,v76
A_ v47,v47,v80
Mavv 36,34,132
buffer_load_dword v11, off, s[92:95], s96 offset:0 sc1
W_ 2
Mavv 56,38,132
W_ 0
Tv 30,v2,13312
Tv 32,v2,19456
Mavv 76,42,132
A_ v48,v48,v84
A_ v49,v49,v88
Mavv 72,42,128
s_nop 0
Tv 34,v3,13312
Tv 36,v3,19456
Mavv 52,38,128
A_ v50,v50,v92
Mavv 48,38,124
v_rcp_f32 v46, v46
Mavv 68,42,124
v_rcp_f32 v47, v47
Mavv 64,42,120
v_rcp_f32 v48, v48
Mavv 44,38,120
v_rcp_f32 v49, v49
Mavv 40,38,116
v_rcp_f32 v50, v50
Mavv 60,42,116
W_ 0
R_ v96,a0
R_ v97,a1
Mavv 80,30,116
s_nop 0
Tv 38,v4,13312
Tv 40,v4,19456
Mavv 100,34,116
R_ v98,a2
R_ v99,a3
Mavv 104,34,120
s_nop 0
Tv 42,v5,13312
Tv 44,v5,19456
Mavv 84,30,120
R_ v100,a20
R_ v101,a21
Mavv 88,30,124
R_ v102,a22
R_ v103,a23
Mavv 108,34,124
U_ v96,v96,v46
U_ v97,v97,v46
Mavv 112,34,128
U_ v98,v98,v46
U_ v99,v99,v46
Mavv 92,30,128
U_ v100,v100,v46
U_ v101,v101,v46
Mavv 96,30,132
U_ v102,v102,v46
U_ v103,v103,v46
Mavv 116,34,132
W_ 0
C_ 136,96,97
C_ 137,98,99
Mavv 136,38,132
s_nop 0
Tv 30,v2,14336
Tv 32,v2,20480
Mavv 156,42,132
C_ 138,100,101
C_ 139,102,103
Mavv 152,42,128
s_nop 0
Tv 34,v3,14336
Tv 36,v3,20480
Mavv 132,38,128
Da 0,v13,0
Mavv 128,38,124
R_ v104,a4
R_ v105,a5
Mavv 148,42,124
R_ v106,a6
R_ v107,a7
Mavv 144,42,120
R_ v108,a24
R_ v109,a25
Mavv 124,38,120
R_ v110,a26
R_ v111,a27
Mavv 120,38,116
U_ v104,v104,v47
U_ v105,v105,v47
Mavv 140,42,116
W_ 1
U_ v106,v106,v47
U_ v107,v107,v47
Mavv 160,30,116
s_nop 0
Tv 38,v4,14336
Tv 40,v4,20480
Mavv 180,34,116
U_ v108,v108,v47
U_ v109,v109,v47
Mavv 184,34,120
s_nop 0
Tv 42,v5,14336
Tv 44,v5,20480
Mavv 164,30,120
U_ v110,v110,v47
U_ v111,v111,v47
Mavv 168,30,124
C_ 140,104,105
C_ 141,106,107
Mavv 188,34,124
C_ 142,108,109
C_ 143,110,111
Mavv 192,34,128
Da 4,v13,1024
Mavv 172,30,128
S_ s55,s71,0
R_ v96,a8
R_ v97,a9
Mavv 176,30,132
S_ m0,s51,12288
R_ v98,a10
R_ v99,a11
Mavv 196,34,132
s_waitcnt vmcnt(63) lgkmcnt(0)
s_barrier
R_ v100,a28
Mavv 216,38,132
L_ v7,24,s55,0
S_ s55,s71,10240
S_ m0,s51,13312
Mavv 236,42,132
R_ v101,a29
Mavv 232,42,128
L_ v7,24,s55,0
S_ s55,s71,20480
S_ m0,s51,14336
Mavv 212,38,128
R_ v102,a30
R_ v103,a31
Mavv 208,38,124
L_ v7,24,s55,0
S_ s55,s71,30720
S_ m0,s51,15360
Mavv 228,42,124
U_ v96,v96,v48
U_ v97,v97,v48
Mavv 224,42,120
L_ v7,24,s55,0
S_ s55,s71,40960
S_ m0,s51,16384
Mavv 204,38,120
U_ v98,v98,v48
U_ v99,v99,v48
Mavv 200,38,116
L_ v7,24,s55,0
S_ s55,s71,51200
S_ m0,s51,17408
Mavv 220,42,116
U_ v100,v100,v48
U_ v101,v101,v48
U_ v102,v102,v48
U_ v103,v103,v48
C_ 144,96,97
C_ 145,98,99
C_ 146,100,101
C_ 147,102,103
Da 8,v13,2048
L_ v7,24,s55,0
.p2align 3
R_ v104,a12
R_ v105,a13
R_ v106,a14
R_ v107,a15
R_ v108,a32
R_ v109,a33
R_ v110,a34
R_ v111,a35
v_pk_mul_f32 v[104:105], v[104:105], v[48:49] op_sel:[0,1]
v_pk_mul_f32 v[106:107], v[106:107], v[48:49] op_sel:[0,1]
v_pk_mul_f32 v[108:109], v[108:109], v[48:49] op_sel:[0,1]
v_pk_mul_f32 v[110:111], v[110:111], v[48:49] op_sel:[0,1]
C_ 148,104,105
C_ 149,106,107
C_ 150,108,109
C_ 151,110,111
Da 12,v13,3072
.p2align 3
R_ v96,a16
R_ v97,a17
R_ v98,a18
R_ v99,a19
R_ v100,a36
R_ v101,a37
R_ v102,a38
R_ v103,a39
v_pk_mul_f32 v[96:97], v[96:97], v[50:51] op_sel_hi:[1,0]
v_pk_mul_f32 v[98:99], v[98:99], v[50:51] op_sel_hi:[1,0]
v_pk_mul_f32 v[100:101], v[100:101], v[50:51] op_sel_hi:[1,0]
v_pk_mul_f32 v[102:103], v[102:103], v[50:51] op_sel_hi:[1,0]
C_ 152,96,97
C_ 153,98,99
C_ 154,100,101
C_ 155,102,103
Da 16,v13,4096
.p2align 3
R_ v104,a40
R_ v105,a41
R_ v106,a42
R_ v107,a43
R_ v108,a60
R_ v109,a61
R_ v110,a62
R_ v111,a63
v_pk_mul_f32 v[104:105], v[104:105], v[46:47] op_sel_hi:[1,0]
v_pk_mul_f32 v[106:107], v[106:107], v[46:47] op_sel_hi:[1,0]
v_pk_mul_f32 v[108:109], v[108:109], v[46:47] op_sel_hi:[1,0]
v_pk_mul_f32 v[110:111], v[110:111], v[46:47] op_sel_hi:[1,0]
C_ 156,104,105
C_ 157,106,107
C_ 158,108,109
C_ 159,110,111
Da 40,v13,5120
.p2align 3
R_ v96,a44
R_ v97,a45
R_ v98,a46
R_ v99,a47
R_ v100,a64
R_ v101,a65
R_ v102,a66
R_ v103,a67
v_pk_mul_f32 v[96:97], v[96:97], v[46:47] op_sel:[0,1]
v_pk_mul_f32 v[98:99], v[98:99], v[46:47] op_sel:[0,1]
v_pk_mul_f32 v[100:101], v[100:101], v[46:47] op_sel:[0,1]
v_pk_mul_f32 v[102:103], v[102:103], v[46:47] op_sel:[0,1]
C_ 160,96,97
C_ 161,98,99
C_ 162,100,101
C_ 163,102,103
Da 44,v13,6144
.p2align 3
R_ v104,a48
R_ v105,a49
R_ v106,a50
R_ v107,a51
R_ v108,a68
R_ v109,a69
R_ v110,a70
R_ v111,a71
v_pk_mul_f32 v[104:105], v[104:105], v[48:49] op_sel_hi:[1,0]
v_pk_mul_f32 v[106:107], v[106:107], v[48:49] op_sel_hi:[1,0]
v_pk_mul_f32 v[108:109], v[108:109], v[48:49] op_sel_hi:[1,0]
v_pk_mul_f32 v[110:111], v[110:111], v[48:49] op_sel_hi:[1,0]
C_ 164,104,105
C_ 165,106,107
C_ 166,108,109
C_ 167,110,111
Da 48,v13,7168
.p2align 3
R_ v96,a52
R_ v97,a53
R_ v98,a54
R_ v99,a55
R_ v100,a72
R_ v101,a73
R_ v102,a74
R_ v103,a75
v_pk_mul_f32 v[96:97], v[96:97], v[48:49] op_sel:[0,1]
v_pk_mul_f32 v[98:99], v[98:99], v[48:49] op_sel:[0,1]
v_pk_mul_f32 v[100:101], v[100:101], v[48:49] op_sel:[0,1]
v_pk_mul_f32 v[102:103], v[102:103], v[48:49] op_sel:[0,1]
C_ 168,96,97
C_ 169,98,99
C_ 170,100,101
C_ 171,102,103
Da 52,v13,8192
.p2align 3
R_ v104,a56
R_ v105,a57
R_ v106,a58
R_ v107,a59
R_ v108,a76
R_ v109,a77
R_ v110,a78
R_ v111,a79
v_pk_mul_f32 v[104:105], v[104:105], v[50:51] op_sel_hi:[1,0]
v_pk_mul_f32 v[106:107], v[106:107], v[50:51] op_sel_hi:[1,0]
v_pk_mul_f32 v[108:109], v[108:109], v[50:51] op_sel_hi:[1,0]
v_pk_mul_f32 v[110:111], v[110:111], v[50:51] op_sel_hi:[1,0]
C_ 172,104,105
C_ 173,106,107
C_ 174,108,109
C_ 175,110,111
Da 56,v13,9216
.p2align 3
R_ v96,a80
R_ v97,a81
R_ v98,a82
R_ v99,a83
R_ v100,a100
R_ v101,a101
R_ v102,a102
R_ v103,a103
v_pk_mul_f32 v[96:97], v[96:97], v[46:47] op_sel_hi:[1,0]
v_pk_mul_f32 v[98:99], v[98:99], v[46:47] op_sel_hi:[1,0]
v_pk_mul_f32 v[100:101], v[100:101], v[46:47] op_sel_hi:[1,0]
v_pk_mul_f32 v[102:103], v[102:103], v[46:47] op_sel_hi:[1,0]
C_ 176,96,97
C_ 177,98,99
C_ 178,100,101
C_ 179,102,103
X_ a80,v20
X_ a81,v21
X_ a82,v22
X_ a83,v23
.p2align 3
R_ v104,a84
R_ v105,a85
R_ v106,a86
R_ v107,a87
R_ v108,a104
R_ v109,a105
R_ v110,a106
R_ v111,a107
v_pk_mul_f32 v[104:105], v[104:105], v[46:47] op_sel:[0,1]
v_pk_mul_f32 v[106:107], v[106:107], v[46:47] op_sel:[0,1]
v_pk_mul_f32 v[108:109], v[108:109], v[46:47] op_sel:[0,1]
v_pk_mul_f32 v[110:111], v[110:111], v[46:47] op_sel:[0,1]
C_ 180,104,105
C_ 181,106,107
C_ 182,108,109
C_ 183,110,111
X_ a84,v24
X_ a85,v25
X_ a86,v26
X_ a87,v27
.p2align 3
R_ v96,a88
R_ v97,a89
R_ v98,a90
R_ v99,a91
R_ v100,a108
R_ v101,a109
R_ v102,a110
R_ v103,a111
v_pk_mul_f32 v[96:97], v[96:97], v[48:49] op_sel_hi:[1,0]
v_pk_mul_f32 v[98:99], v[98:99], v[48:49] op_sel_hi:[1,0]
v_pk_mul_f32 v[100:101], v[100:101], v[48:49] op_sel_hi:[1,0]
v_pk_mul_f32 v[102:103], v[102:103], v[48:49] op_sel_hi:[1,0]
C_ 184,96,97
C_ 185,98,99
C_ 186,100,101
C_ 187,102,103
X_ a88,v52
X_ a89,v53
X_ a90,v54
X_ a91,v55
.p2align 3
R_ v104,a92
R_ v105,a93
R_ v106,a94
R_ v107,a95
R_ v108,a112
R_ v109,a113
R_ v110,a114
R_ v111,a115
v_pk_mul_f32 v[104:105], v[104:105], v[48:49] op_sel:[0,1]
v_pk_mul_f32 v[106:107], v[106:107], v[48:49] op_sel:[0,1]
v_pk_mul_f32 v[108:109], v[108:109], v[48:49] op_sel:[0,1]
v_pk_mul_f32 v[110:111], v[110:111], v[48:49] op_sel:[0,1]
C_ 188,104,105
C_ 189,106,107
C_ 190,108,109
C_ 191,110,111
.p2align 3
R_ v96,a96
R_ v97,a97
R_ v98,a98
R_ v99,a99
R_ v100,a116
R_ v101,a117
R_ v102,a118
R_ v103,a119
v_pk_mul_f32 v[96:97], v[96:97], v[50:51] op_sel_hi:[1,0]
v_pk_mul_f32 v[98:99], v[98:99], v[50:51] op_sel_hi:[1,0]
v_pk_mul_f32 v[100:101], v[100:101], v[50:51] op_sel_hi:[1,0]
v_pk_mul_f32 v[102:103], v[102:103], v[50:51] op_sel_hi:[1,0]
C_ 192,96,97
C_ 193,98,99
C_ 194,100,101
C_ 195,102,103
.p2align 3
R_ v104,a120
R_ v105,a121
R_ v106,a122
R_ v107,a123
R_ v108,a140
R_ v109,a141
R_ v110,a142
R_ v111,a143
v_pk_mul_f32 v[104:105], v[104:105], v[46:47] op_sel_hi:[1,0]
v_pk_mul_f32 v[106:107], v[106:107], v[46:47] op_sel_hi:[1,0]
v_pk_mul_f32 v[108:109], v[108:109], v[46:47] op_sel_hi:[1,0]
v_pk_mul_f32 v[110:111], v[110:111], v[46:47] op_sel_hi:[1,0]
C_ 196,104,105
C_ 197,106,107
C_ 198,108,109
C_ 199,110,111
X_ a120,v16
X_ a121,v17
X_ a122,v18
X_ a123,v19
.p2align 3
R_ v96,a124
R_ v97,a125
R_ v98,a126
R_ v99,a127
R_ v100,a144
R_ v101,a145
R_ v102,a146
R_ v103,a147
v_pk_mul_f32 v[96:97], v[96:97], v[46:47] op_sel:[0,1]
v_pk_mul_f32 v[98:99], v[98:99], v[46:47] op_sel:[0,1]
v_pk_mul_f32 v[100:101], v[100:101], v[46:47] op_sel:[0,1]
v_pk_mul_f32 v[102:103], v[102:103], v[46:47] op_sel:[0,1]
C_ 200,96,97
C_ 201,98,99
C_ 202,100,101
C_ 203,102,103
X_ a124,v28
X_ a125,v29
X_ a126,v51
X_ a127,v6
.p2align 3
R_ v104,a128
R_ v105,a129
R_ v106,a130
R_ v107,a131
R_ v108,a148
R_ v109,a149
R_ v110,a150
R_ v111,a151
v_pk_mul_f32 v[104:105], v[104:105], v[48:49] op_sel_hi:[1,0]
v_pk_mul_f32 v[106:107], v[106:107], v[48:49] op_sel_hi:[1,0]
v_pk_mul_f32 v[108:109], v[108:109], v[48:49] op_sel_hi:[1,0]
v_pk_mul_f32 v[110:111], v[110:111], v[48:49] op_sel_hi:[1,0]
C_ 204,104,105
C_ 205,106,107
C_ 206,108,109
C_ 207,110,111
X_ a128,v14
X_ a129,v15
v_accvgpr_mov_b32 a130, a249
v_accvgpr_mov_b32 a131, a250
.p2align 3
R_ v96,a132
R_ v97,a133
R_ v98,a134
R_ v99,a135
R_ v100,a152
R_ v101,a153
R_ v102,a154
R_ v103,a155
v_pk_mul_f32 v[96:97], v[96:97], v[48:49] op_sel:[0,1]
v_pk_mul_f32 v[98:99], v[98:99], v[48:49] op_sel:[0,1]
v_pk_mul_f32 v[100:101], v[100:101], v[48:49] op_sel:[0,1]
v_pk_mul_f32 v[102:103], v[102:103], v[48:49] op_sel:[0,1]
C_ 208,96,97
C_ 209,98,99
C_ 210,100,101
C_ 211,102,103
Da 132,v12,0
.p2align 3
R_ v104,a136
R_ v105,a137
R_ v106,a138
R_ v107,a139
R_ v108,a156
R_ v109,a157
R_ v110,a158
R_ v111,a159
v_pk_mul_f32 v[104:105], v[104:105], v[50:51] op_sel_hi:[1,0]
v_pk_mul_f32 v[106:107], v[106:107], v[50:51] op_sel_hi:[1,0]
v_pk_mul_f32 v[108:109], v[108:109], v[50:51] op_sel_hi:[1,0]
v_pk_mul_f32 v[110:111], v[110:111], v[50:51] op_sel_hi:[1,0]
C_ 212,104,105
C_ 213,106,107
C_ 214,108,109
C_ 215,110,111
Da 136,v12,1024
.p2align 3
R_ v96,a160
R_ v97,a161
R_ v98,a162
R_ v99,a163
R_ v100,a180
R_ v101,a181
R_ v102,a182
R_ v103,a183
v_pk_mul_f32 v[96:97], v[96:97], v[46:47] op_sel_hi:[1,0]
v_pk_mul_f32 v[98:99], v[98:99], v[46:47] op_sel_hi:[1,0]
v_pk_mul_f32 v[100:101], v[100:101], v[46:47] op_sel_hi:[1,0]
v_pk_mul_f32 v[102:103], v[102:103], v[46:47] op_sel_hi:[1,0]
C_ 216,96,97
C_ 217,98,99
C_ 218,100,101
C_ 219,102,103
Da 160,v12,2048
.p2align 3
R_ v104,a164
R_ v105,a165
R_ v106,a166
R_ v107,a167
R_ v108,a184
R_ v109,a185
R_ v110,a186
R_ v111,a187
v_pk_mul_f32 v[104:105], v[104:105], v[46:47] op_sel:[0,1]
v_pk_mul_f32 v[106:107], v[106:107], v[46:47] op_sel:[0,1]
v_pk_mul_f32 v[108:109], v[108:109], v[46:47] op_sel:[0,1]
v_pk_mul_f32 v[110:111], v[110:111], v[46:47] op_sel:[0,1]
C_ 220,104,105
C_ 221,106,107
C_ 222,108,109
C_ 223,110,111
Da 164,v12,3072
.p2align 3
R_ v96,a168
R_ v97,a169
R_ v98,a170
R_ v99,a171
R_ v100,a188
R_ v101,a189
R_ v102,a190
R_ v103,a191
v_pk_mul_f32 v[96:97], v[96:97], v[48:49] op_sel_hi:[1,0]
v_pk_mul_f32 v[98:99], v[98:99], v[48:49] op_sel_hi:[1,0]
v_pk_mul_f32 v[100:101], v[100:101], v[48:49] op_sel_hi:[1,0]
v_pk_mul_f32 v[102:103], v[102:103], v[48:49] op_sel_hi:[1,0]
C_ 224,96,97
C_ 225,98,99
C_ 226,100,101
C_ 227,102,103
Da 168,v12,4096
.p2align 3
R_ v104,a172
R_ v105,a173
R_ v106,a174
R_ v107,a175
R_ v108,a192
R_ v109,a193
R_ v110,a194
R_ v111,a195
v_pk_mul_f32 v[104:105], v[104:105], v[48:49] op_sel:[0,1]
v_pk_mul_f32 v[106:107], v[106:107], v[48:49] op_sel:[0,1]
v_pk_mul_f32 v[108:109], v[108:109], v[48:49] op_sel:[0,1]
v_pk_mul_f32 v[110:111], v[110:111], v[48:49] op_sel:[0,1]
C_ 228,104,105
C_ 229,106,107
C_ 230,108,109
C_ 231,110,111
Da 172,v12,5120
.p2align 3
R_ v96,a176
R_ v97,a177
R_ v98,a178
R_ v99,a179
R_ v100,a196
R_ v101,a197
R_ v102,a198
R_ v103,a199
v_pk_mul_f32 v[96:97], v[96:97], v[50:51] op_sel_hi:[1,0]
v_pk_mul_f32 v[98:99], v[98:99], v[50:51] op_sel_hi:[1,0]
v_pk_mul_f32 v[100:101], v[100:101], v[50:51] op_sel_hi:[1,0]
v_pk_mul_f32 v[102:103], v[102:103], v[50:51] op_sel_hi:[1,0]
C_ 232,96,97
C_ 233,98,99
C_ 234,100,101
C_ 235,102,103
Da 176,v12,12288
.p2align 3
R_ v104,a200
R_ v105,a201
R_ v106,a202
R_ v107,a203
R_ v108,a220
R_ v109,a221
R_ v110,a222
R_ v111,a223
v_pk_mul_f32 v[104:105], v[104:105], v[46:47] op_sel_hi:[1,0]
v_pk_mul_f32 v[106:107], v[106:107], v[46:47] op_sel_hi:[1,0]
v_pk_mul_f32 v[108:109], v[108:109], v[46:47] op_sel_hi:[1,0]
v_pk_mul_f32 v[110:111], v[110:111], v[46:47] op_sel_hi:[1,0]
C_ 236,104,105
C_ 237,106,107
C_ 238,108,109
C_ 239,110,111
Da 200,v12,13312
.p2align 3
R_ v96,a204
R_ v97,a205
R_ v98,a206
R_ v99,a207
R_ v100,a224
R_ v101,a225
R_ v102,a226
R_ v103,a227
v_pk_mul_f32 v[96:97], v[96:97], v[46:47] op_sel:[0,1]
v_pk_mul_f32 v[98:99], v[98:99], v[46:47] op_sel:[0,1]
v_pk_mul_f32 v[100:101], v[100:101], v[46:47] op_sel:[0,1]
v_pk_mul_f32 v[102:103], v[102:103], v[46:47] op_sel:[0,1]
C_ 240,96,97
C_ 241,98,99
C_ 242,100,101
C_ 243,102,103
Da 204,v12,14336
v_mov_b32 v6, v10
v_lshrrev_b32 v10, 9, v6
v_lshlrev_b32 v10, 11, v10
v_sub_u32 %0, v2, v10
v_xor_b32 %0, s83, %0
s_waitcnt vmcnt(16) lgkmcnt(0)
s_barrier
Ta 180,%0,24576
Ta 182,%0,26624
Ta 184,%0,28672
Ta 186,%0,30720
Ta 188,%0,32768
Ta 190,%0,34816
.p2align 3
R_ v104,a208
R_ v105,a209
R_ v106,a210
R_ v107,a211
R_ v108,a228
R_ v109,a229
R_ v110,a230
R_ v111,a231
v_pk_mul_f32 v[104:105], v[104:105], v[48:49] op_sel_hi:[1,0]
v_pk_mul_f32 v[106:107], v[106:107], v[48:49] op_sel_hi:[1,0]
v_pk_mul_f32 v[108:109], v[108:109], v[48:49] op_sel_hi:[1,0]
v_pk_mul_f32 v[110:111], v[110:111], v[48:49] op_sel_hi:[1,0]
C_ 244,104,105
C_ 245,106,107
C_ 246,108,109
C_ 247,110,111
Da 208,v12,15360
.p2align 3
R_ v96,a212
R_ v97,a213
R_ v98,a214
R_ v99,a215
R_ v100,a232
R_ v101,a233
R_ v102,a234
R_ v103,a235
v_pk_mul_f32 v[96:97], v[96:97], v[48:49] op_sel:[0,1]
v_pk_mul_f32 v[98:99], v[98:99], v[48:49] op_sel:[0,1]
v_pk_mul_f32 v[100:101], v[100:101], v[48:49] op_sel:[0,1]
v_pk_mul_f32 v[102:103], v[102:103], v[48:49] op_sel:[0,1]
C_ 248,96,97
C_ 249,98,99
C_ 250,100,101
C_ 251,102,103
Da 212,v12,16384
.p2align 3
R_ v104,a216
R_ v105,a217
R_ v106,a218
R_ v107,a219
R_ v108,a236
R_ v109,a237
R_ v110,a238
R_ v111,a239
v_pk_mul_f32 v[104:105], v[104:105], v[50:51] op_sel_hi:[1,0]
v_pk_mul_f32 v[106:107], v[106:107], v[50:51] op_sel_hi:[1,0]
v_pk_mul_f32 v[108:109], v[108:109], v[50:51] op_sel_hi:[1,0]
v_pk_mul_f32 v[110:111], v[110:111], v[50:51] op_sel_hi:[1,0]
C_ 252,104,105
C_ 253,106,107
C_ 254,108,109
C_ 255,110,111
Da 216,v12,17408
v_xor_b32 v1, 32, %0
W_ 7
Zvav 56,180,136
s_mul_i32 s76, s6, 204800
Ta 192,v1,24576
Ta 194,v1,26624
W_ 7
Mvav 56,184,156
v_xor_b32 v2, 64, %0
v_xor_b32 v3, 96, %0
W_ 5
Mvav 56,188,176
s_nop 0
Ta 196,v1,28672
Ta 198,v1,30720
Zvav 60,180,140
W_ 4
R_ v4,a248
v_mul_u32_u24 v4, 80, v4
Mvav 60,184,160
S_ s55,s71,122880
Ta 220,v1,32768
Ta 222,v1,34816
Mvav 60,188,180
S_ m0,s51,49152
v_min_u32 v4, 29440, v4
s_waitcnt vmcnt(6)
Zvav 64,180,144
v_readfirstlane_b32 s62, v11
v_lshrrev_b32 v8, 2, v6
Mvav 64,184,164
v_add_u32 v5, s75, v8
R_ v9,a251
Mvav 64,188,184
v_or_b32 v9, s73, v9
v_lshrrev_b32 v10, 8, v6
Zvav 68,180,148
v_bfe_u32 v12, v6, 6, 2
v_cmp_eq_u32 vcc, v10, v12
Mvav 68,184,168
v_and_b32 v10, 16, v6
v_mov_b32 v12, 0x3f80
Mvav 68,188,188
v_lshlrev_b32 v12, v10, v12
v_cndmask_b32 v12, 0, v12, vcc
Zvav 72,180,152
v_and_b32 v10, 32, v6
v_cmp_eq_u32 vcc, 0, v10
Mvav 72,184,172
X_ a68,0
X_ a69,0
Mvav 72,188,192
W_ 0
X_ a72,0
X_ a73,0
Zvav 92,220,192
s_nop 0
Ta 180,v2,24576
Ta 182,v2,26624
Mvav 92,196,172
v_cndmask_b32 v10, 0, v12, vcc
v_sub_u32 v12, v12, v10
Mvav 92,192,152
s_nop 0
Ta 184,v2,28672
Ta 186,v2,30720
Zvav 88,220,188
X_ a70,v10
X_ a71,v12
Mvav 88,196,168
s_mov_b64 s[32:33], s[20:21]
Ta 188,v2,32768
Ta 190,v2,34816
Mvav 88,192,148
s_mov_b32 s34, 409600
S_ s85,s6,2
Zvav 84,220,184
L_ v7,24,s55,0
Mvav 84,196,164
S_ s55,s71,133120
s_and_b32 s85, s85, 7
Mvav 84,192,144
S_ m0,s51,50176
s_lshl_b32 s85, s85, 7
Zvav 80,220,180
S_ s85,s85,s87
Mvav 80,196,160
L_ v7,24,s55,0
Mvav 80,192,140
S_ s55,s71,143360
Zvav 76,220,176
S_ m0,s51,51200
Mvav 76,196,156
Mvav 76,192,136
W_ 0
Zvav 16,180,136
s_nop 0
Ta 192,v3,24576
Ta 194,v3,26624
Mvav 16,184,156
L_ v7,24,s55,0
Mvav 16,188,176
S_ s55,s71,153600
Ta 196,v3,28672
Ta 198,v3,30720
Zvav 20,180,140
S_ m0,s51,52224
Mvav 20,184,160
s_nop 0
Ta 220,v3,32768
Ta 222,v3,34816
Mvav 20,188,180
L_ v7,24,s55,0
Zvav 24,180,144
S_ s55,s71,163840
Mvav 24,184,164
S_ m0,s51,53248
Mvav 24,188,184
Zvav 28,180,148
L_ v7,24,s55,0
Mvav 28,184,168
S_ s55,s71,174080
Mvav 28,188,188
S_ m0,s51,54272
Zvav 32,180,152
Mvav 32,184,172
L_ v7,24,s55,0
Mvav 32,188,192
s_waitcnt vmcnt(13) lgkmcnt(0)
s_barrier
S_ s71,s72,1152
Zvav 52,220,192
S_ s55,s71,491520
Ta 180,%0,0
Ta 182,%0,2048
Mvav 52,196,172
S_ m0,s51,36864
Mvav 52,192,152
s_nop 0
Ta 184,%0,4096
Ta 186,%0,6144
Zvav 48,220,188
L_ v7,24,s55,0
Mvav 48,196,168
S_ s55,s71,501760
Ta 188,%0,8192
Ta 190,%0,10240
Mvav 48,192,148
S_ m0,s51,37888
Zvav 44,220,184
Mvav 44,196,164
L_ v7,24,s55,0
Mvav 44,192,144
S_ s55,s71,512000
Zvav 40,220,180
S_ m0,s51,38912
Mvav 40,196,160
Mvav 40,192,140
L_ v7,24,s55,0
Zvav 36,220,176
S_ s55,s71,522240
Mvav 36,196,156
S_ m0,s51,39936
Mvav 36,192,136
W_ 0
Mvav 56,180,196
s_nop 0
Ta 192,v1,0
Ta 194,v1,2048
Mvav 56,184,216
L_ v7,24,s55,0
Mvav 56,188,236
S_ s55,s71,532480
Ta 196,v1,4096
Ta 198,v1,6144
Mvav 60,180,200
S_ m0,s51,40960
Mvav 60,184,220
s_nop 0
Ta 220,v1,8192
Ta 222,v1,10240
Mvav 60,188,240
L_ v7,24,s55,0
Mvav 64,180,204
S_ s55,s71,542720
Mvav 64,184,224
S_ m0,s51,41984
Mvav 64,188,244
Mvav 68,180,208
L_ v7,24,s55,0
Mvav 68,184,228
S_ s55,s71,614400
Mvav 68,188,248
S_ m0,s51,24576
Mvav 72,180,212
Mvav 72,184,232
L_ v7,24,s55,0
Mvav 72,188,252
W_ 0
S_ s55,s71,624640
Mvav 92,220,252
S_ m0,s51,25600
Ta 180,v2,0
Ta 182,v2,2048
Mvav 92,196,232
Mvav 92,192,212
s_nop 0
Ta 184,v2,4096
Ta 186,v2,6144
Mvav 88,220,248
L_ v7,24,s55,0
Mvav 88,196,228
S_ s55,s71,634880
Ta 188,v2,8192
Ta 190,v2,10240
Mvav 88,192,208
S_ m0,s51,26624
Mvav 84,220,244
Mvav 84,196,224
L_ v7,24,s55,0
Mvav 84,192,204
S_ s55,s71,645120
Mvav 80,220,240
S_ m0,s51,27648
Mvav 80,196,220
Mvav 80,192,200
L_ v7,24,s55,0
Mvav 76,220,236
S_ s55,s71,655360
Mvav 76,196,216
S_ m0,s51,28672
Mvav 76,192,196
W_ 0
Mvav 16,180,196
s_nop 0
Ta 192,v3,0
Ta 194,v3,2048
Mvav 16,184,216
L_ v7,24,s55,0
Mvav 16,188,236
S_ s55,s71,665600
Ta 196,v3,4096
Ta 198,v3,6144
Mvav 20,180,200
S_ m0,s51,29696
Mvav 20,184,220
s_nop 0
Ta 220,v3,8192
Ta 222,v3,10240
Mvav 20,188,240
L_ v7,24,s55,0
Mvav 24,180,204
Mvav 24,184,224
Mvav 24,188,244
Mvav 28,180,208
buffer_load_dword a74, v4, s[24:27], s85 offen
Mvav 28,184,228
Mvav 28,188,248
Mvav 32,180,212
Mvav 32,184,232
Mvav 32,188,252
s_waitcnt vmcnt(19) lgkmcnt(0)
s_barrier
Mvav 52,220,252
S_ s55,s71,0
Ta 180,%0,12288
Ta 182,%0,14336
Mvav 52,196,232
S_ m0,s51,0
Mvav 52,192,212
s_nop 0
Ta 184,%0,16384
Ta 186,%0,18432
Mvav 48,220,248
L_ v7,24,s55,0
Mvav 48,196,228
S_ s55,s71,10240
Ta 188,%0,20480
Ta 190,%0,22528
Mvav 48,192,208
S_ m0,s51,1024
Mvav 44,220,244
Mvav 44,196,224
L_ v7,24,s55,0
Mvav 44,192,204
S_ s55,s71,20480
Mvav 40,220,240
S_ m0,s51,2048
Mvav 40,196,220
Mvav 40,192,200
L_ v7,24,s55,0
Mvav 36,220,236
S_ s55,s71,30720
Mvav 36,196,216
S_ m0,s51,3072
Mvav 36,192,196
W_ 0
Mvaa 56,180,0
s_nop 0
Ta 192,v1,12288
Ta 194,v1,14336
Mvaa 56,184,40
L_ v7,24,s55,0
Mvaa 56,188,80
S_ s55,s71,40960
Ta 196,v1,16384
Ta 198,v1,18432
Mvaa 60,180,4
S_ m0,s51,4096
Mvaa 60,184,44
s_nop 0
Ta 220,v1,20480
Ta 222,v1,22528
Mvaa 60,188,84
L_ v7,24,s55,0
Mvaa 64,180,8
S_ s55,s71,51200
Mvaa 64,184,48
S_ m0,s51,5120
Mvaa 64,188,88
Mvaa 68,180,12
L_ v7,24,s55,0
Mvaa 68,184,52
Mvaa 68,188,240
Mvaa 72,180,16
Mvaa 72,184,56
Mvaa 72,188,244
W_ 0
Mvaa 92,220,244
s_nop 0
Ta 180,v2,12288
Ta 182,v2,14336
Mvaa 92,196,56
Mvaa 92,192,16
s_nop 0
Ta 184,v2,16384
Ta 186,v2,18432
Mvaa 88,220,240
Mvaa 88,196,52
s_nop 0
Ta 188,v2,20480
Ta 190,v2,22528
Mvaa 88,192,12
Mvaa 84,220,88
Mvaa 84,196,48
Mvaa 84,192,8
Mvaa 80,220,84
Mvaa 80,196,44
Mvaa 80,192,4
Mvaa 76,220,80
Mvaa 76,196,40
Mvaa 76,192,0
W_ 0
Mvaa 16,180,0
s_nop 0
Ta 192,v3,12288
Ta 194,v3,14336
Mvaa 16,184,40
Mvaa 16,188,80
s_nop 0
Ta 196,v3,16384
Ta 198,v3,18432
Mvaa 20,180,4
Mvaa 20,184,44
s_nop 0
Ta 220,v3,20480
Ta 222,v3,22528
Mvaa 20,188,84
Mvaa 24,180,8
Mvaa 24,184,48
Mvaa 24,188,88
Mvaa 28,180,12
Mvaa 28,184,52
Mvaa 28,188,240
Mvaa 32,180,16
Mvaa 32,184,56
Mvaa 32,188,244
s_waitcnt vmcnt(19) lgkmcnt(0)
s_barrier
Mvaa 52,220,244
s_nop 0
Ta 180,%0,49152
Ta 182,%0,51200
Mvaa 52,196,56
Mvaa 52,192,16
s_nop 0
Ta 184,%0,53248
Ta 186,%0,55296
Mvaa 48,220,240
Mvaa 48,196,52
s_nop 0
Ta 188,%0,57344
Ta 190,%0,59392
Mvaa 48,192,12
Mvaa 44,220,88
Mvaa 44,196,48
Mvaa 44,192,8
Mvaa 40,220,84
Mvaa 40,196,44
Mvaa 40,192,4
Mvaa 36,220,80
Mvaa 36,196,40
Mvaa 36,192,0
W_ 0
Mvaa 56,180,120
S_ s73,s78,163840
Ta 192,v1,49152
Ta 194,v1,51200
Mvaa 56,184,160
S_ s73,s73,4096
Mvaa 56,188,200
s_nop 0
Ta 196,v1,53248
Ta 198,v1,55296
Mvaa 60,180,124
s_mov_b32 m0, s52
Mvaa 60,184,164
s_nop 0
Ta 220,v1,57344
Ta 222,v1,59392
Mvaa 60,188,204
s_nop 0
Y_ 56,0
Y_ 57,256
Mvaa 64,180,128
s_nop 0
Y_ 58,512
Y_ 59,768
Mvaa 64,184,168
Mvaa 64,188,208
s_nop 0
Y_ 60,1024
Y_ 61,1280
Mvaa 68,180,132
s_nop 0
Y_ 62,1536
Y_ 63,1792
Mvaa 68,184,172
Mvaa 68,188,212
s_nop 0
Y_ 64,2048
Y_ 65,2304
Mvaa 72,180,136
s_nop 0
Y_ 66,2560
Y_ 67,2816
Mvaa 72,184,176
Mvaa 72,188,216
W_ 12
Y_ 68,3072
Y_ 69,3328
Mvaa 92,220,216
s_nop 0
Ta 180,v2,49152
Ta 182,v2,51200
Mvaa 92,196,176
s_nop 0
Y_ 70,3584
Y_ 71,3840
Mvaa 92,192,136
s_nop 0
Ta 184,v2,53248
Ta 186,v2,55296
Mvaa 88,220,212
s_nop 0
Y_ 72,4096
Y_ 73,4352
Mvaa 88,196,172
s_nop 0
Ta 188,v2,57344
Ta 190,v2,59392
Mvaa 88,192,132
s_nop 0
Y_ 74,4608
Y_ 75,4864
Mvaa 84,220,208
s_nop 0
Y_ 92,9216
Y_ 93,9472
Mvaa 84,196,168
s_nop 0
Y_ 94,9728
Y_ 95,9984
Mvaa 84,192,128
s_nop 0
Y_ 88,8192
Y_ 89,8448
Mvaa 80,220,204
s_nop 0
Y_ 90,8704
Y_ 91,8960
Mvaa 80,196,164
Mvaa 80,192,124
s_nop 0
Y_ 84,7168
Y_ 85,7424
Mvaa 76,220,200
s_nop 0
Y_ 86,7680
Y_ 87,7936
Mvaa 76,196,160
Mvaa 76,192,120
W_ 14
Y_ 80,6144
Y_ 81,6400
Mvaa 16,180,120
s_nop 0
Ta 192,v3,49152
Ta 194,v3,51200
Mvaa 16,184,160
s_nop 0
Y_ 82,6656
Y_ 83,6912
Mvaa 16,188,200
s_nop 0
Ta 196,v3,53248
Ta 198,v3,55296
Mvaa 20,180,124
s_nop 0
Y_ 76,5120
Y_ 77,5376
Mvaa 20,184,164
s_nop 0
Ta 220,v3,57344
Ta 222,v3,59392
Mvaa 20,188,204
s_nop 0
Y_ 78,5632
Y_ 79,5888
Mvaa 24,180,128
Mvaa 24,184,168
Mvaa 24,188,208
s_waitcnt vmcnt(13) lgkmcnt(0)
s_barrier
Mvaa 28,180,132
S_ s55,s71,122880
ds_read2st64_b32 v[96:97], v5 offset0:0 offset1:1
Mvaa 28,184,172
S_ m0,s51,12288
ds_read2st64_b32 v[98:99], v5 offset0:2 offset1:3
Mvaa 28,188,212
L_ v7,24,s55,0
Mvaa 32,180,136
S_ s55,s71,133120
ds_read2st64_b32 v[104:105], v5 offset0:4 offset1:5
Mvaa 32,184,176
S_ m0,s51,13312
ds_read2st64_b32 v[106:107], v5 offset0:6 offset1:7
Mvaa 32,188,216
ds_read2st64_b32 v[112:113], v5 offset0:8 offset1:9
Mvaa 52,220,216
s_nop 0
Ta 180,%0,36864
Ta 182,%0,38912
Mvaa 52,196,176
L_ v7,24,s55,0
Mvaa 52,192,136
W_ 5
Ta 184,%0,40960
Ta 186,%0,43008
Mvaa 48,220,212
S_ s55,s71,143360
ds_read2st64_b32 v[114:115], v5 offset0:10 offset1:11
A_ v96,v16,v96
Mvaa 48,196,172
S_ m0,s51,14336
Ta 188,%0,45056
Ta 190,%0,47104
Mvaa 48,192,132
L_ v7,24,s55,0
Mvaa 44,220,208
S_ s55,s71,153600
ds_read2st64_b32 v[120:121], v5 offset0:12 offset1:13
A_ v97,v17,v97
Mvaa 44,196,168
S_ m0,s51,15360
ds_read2st64_b32 v[122:123], v5 offset0:14 offset1:15
A_ v98,v18,v98
Mvaa 44,192,128
ds_read2st64_b32 v[128:129], v5 offset0:16 offset1:17
A_ v99,v19,v99
Mvaa 40,220,204
L_ v7,24,s55,0
Mvaa 40,196,164
W_ 11
ds_read2st64_b32 v[130:131], v5 offset0:18 offset1:19
A_ v104,v20,v104
Mvaa 40,192,124
S_ s55,s71,163840
ds_read2st64_b32 v[132:133], v5 offset0:36 offset1:37
A_ v105,v21,v105
Mvaa 36,220,200
S_ m0,s51,16384
ds_read2st64_b32 v[134:135], v5 offset0:38 offset1:39
A_ v106,v22,v106
Mvaa 36,196,160
L_ v7,24,s55,0
Mvaa 36,192,120
W_ 6
ds_read2st64_b32 v[124:125], v5 offset0:32 offset1:33
A_ v107,v23,v107
Zvav 56,180,136
Ta 192,v1,36864
Ta 194,v1,38912
Mvav 56,184,156
S_ s55,s71,174080
ds_read2st64_b32 v[126:127], v5 offset0:34 offset1:35
A_ v112,v24,v112
Mvav 56,188,176
S_ m0,s51,17408
Ta 196,v1,40960
Ta 198,v1,43008
Zvav 60,180,140
L_ v7,24,s55,0
Mvav 60,184,160
S_ s79,s6,1
Ta 220,v1,45056
Ta 222,v1,47104
Mvav 60,188,180
s_and_b32 s79, s79, 7
ds_read2st64_b32 v[116:117], v5 offset0:28 offset1:29
A_ v113,v25,v113
Zvav 64,180,144
s_lshl_b32 s79, s79, 7
ds_read2st64_b32 v[118:119], v5 offset0:30 offset1:31
A_ v114,v26,v114
Mvav 64,184,164
S_ s71,s72,s79
ds_read2st64_b32 v[108:109], v5 offset0:24 offset1:25
A_ v115,v27,v115
Mvav 64,188,184
W_ 14
ds_read2st64_b32 v[110:111], v5 offset0:26 offset1:27
A_ v120,v28,v120
Zvav 68,180,148
S_ s55,s71,491520
ds_read2st64_b32 v[100:101], v5 offset0:20 offset1:21
A_ v121,v29,v121
Mvav 68,184,168
S_ m0,s51,49152
ds_read2st64_b32 v[102:103], v5 offset0:22 offset1:23
A_ v122,v30,v122
Mvav 68,188,188
L_ v7,24,s55,0
Zvav 72,180,152
S_ s55,s71,501760
A_ v123,v31,v123
R_ v12,a248
Mvav 72,184,172
W_ 14
A_ v128,v32,v128
A_ v129,v33,v129
Mvav 72,188,192
W_ 6
A_ v130,v34,v130
A_ v131,v35,v131
Zvav 92,220,192
Ta 180,v2,36864
Ta 182,v2,38912
Mvav 92,196,172
S_ m0,s51,50176
A_ v132,v52,v132
A_ v133,v53,v133
Mvav 92,192,152
s_nop 0
Ta 184,v2,40960
Ta 186,v2,43008
Zvav 88,220,188
L_ v7,24,s55,0
Mvav 88,196,168
S_ s55,s71,512000
Ta 188,v2,45056
Ta 190,v2,47104
Mvav 88,192,148
S_ m0,s51,51200
A_ v134,v54,v134
A_ v135,v55,v135
Zvav 84,220,184
A_ v124,v48,v124
A_ v125,v49,v125
Mvav 84,196,164
L_ v7,24,s55,0
Mvav 84,192,144
S_ s55,s71,522240
A_ v126,v50,v126
A_ v127,v51,v127
Zvav 80,220,180
W_ 10
A_ v116,v44,v116
A_ v117,v45,v117
Mvav 80,196,160
S_ m0,s51,52224
A_ v118,v46,v118
A_ v119,v47,v119
Mvav 80,192,140
L_ v7,24,s55,0
Zvav 76,220,176
W_ 8
A_ v108,v40,v108
A_ v109,v41,v109
Mvav 76,196,156
S_ s55,s71,532480
A_ v110,v42,v110
A_ v111,v43,v111
Mvav 76,192,136
W_ 0
A_ v100,v36,v100
Zvav 16,180,136
S_ m0,s51,53248
Ta 192,v3,36864
Ta 194,v3,38912
Mvav 16,184,156
L_ v7,24,s55,0
Mvav 16,188,176
S_ s55,s71,542720
Ta 196,v3,40960
Ta 198,v3,43008
Zvav 20,180,140
S_ m0,s51,54272
A_ v101,v37,v101
A_ v102,v38,v102
Mvav 20,184,160
S_ s85,s6,3
Ta 220,v3,45056
Ta 222,v3,47104
Mvav 20,188,180
L_ v7,24,s55,0
Zvav 24,180,144
s_and_b32 s85, s85, 7
A_ v103,v39,v103
C_ 131,130,131
Mvav 24,184,164
s_lshl_b32 s85, s85, 7
C_ 130,128,129
C_ 132,132,133
Mvav 24,188,184
S_ s85,s85,s87
C_ 133,134,135
v_min_u32 v12, 240, v12
Zvav 28,180,148
buffer_store_dwordx4 v[130:133], v6, s[28:31], s73 offen offset:0 sc1
Mvav 28,184,168
s_sub_u32 s73, s73, 4096
v_lshlrev_b32 v12, 3, v12
Mvav 28,188,188
C_ 123,122,123
C_ 122,120,121
Zvav 32,180,152
C_ 124,124,125
C_ 125,126,127
Mvav 32,184,172
buffer_store_dwordx4 v[122:125], v6, s[28:31], s73 offen offset:3072 sc1
Mvav 32,188,192
s_waitcnt vmcnt(21) lgkmcnt(0)
s_barrier
C_ 115,114,115
Zvav 52,220,192
S_ s55,s71,614400
Ta 180,%0,24576
Ta 182,%0,26624
Mvav 52,196,172
S_ m0,s51,36864
C_ 114,112,113
C_ 116,116,117
Mvav 52,192,152
s_nop 0
Ta 184,%0,28672
Ta 186,%0,30720
Zvav 48,220,188
L_ v7,24,s55,0
Mvav 48,196,168
S_ s55,s71,624640
Ta 188,%0,32768
Ta 190,%0,34816
Mvav 48,192,148
S_ m0,s51,37888
C_ 117,118,119
Zvav 44,220,184
Mvav 44,196,164
buffer_store_dwordx4 v[114:117], v6, s[28:31], s73 offen offset:2048 sc1
Mvav 44,192,144
C_ 107,106,107
C_ 106,104,105
Zvav 40,220,180
C_ 108,108,109
C_ 109,110,111
Mvav 40,196,160
Mvav 40,192,140
buffer_store_dwordx4 v[106:109], v6, s[28:31], s73 offen offset:1024 sc1
Zvav 36,220,176
C_ 99,98,99
C_ 98,96,97
Mvav 36,196,156
C_ 100,100,101
C_ 101,102,103
Mvav 36,192,136
W_ 0
Mvav 56,180,196
s_nop 0
Ta 192,v1,24576
Ta 194,v1,26624
Mvav 56,184,216
buffer_store_dwordx4 v[98:101], v6, s[28:31], s73 offen offset:0 sc1
Mvav 56,188,236
s_nop 0
Ta 196,v1,28672
Ta 198,v1,30720
Mvav 60,180,200
Mvav 60,184,220
s_nop 0
Ta 220,v1,32768
Ta 222,v1,34816
Mvav 60,188,240
L_ v7,24,s55,0
Mvav 64,180,204
S_ s55,s71,634880
Mvav 64,184,224
S_ m0,s51,38912
Mvav 64,188,244
Mvav 68,180,208
L_ v7,24,s55,0
Mvav 68,184,228
S_ s55,s71,645120
Mvav 68,188,248
S_ m0,s51,39936
Mvav 72,180,212
Mvav 72,184,232
L_ v7,24,s55,0
Mvav 72,188,252
W_ 0
S_ s55,s71,655360
Mvav 92,220,252
S_ m0,s51,40960
Ta 180,v2,24576
Ta 182,v2,26624
Mvav 92,196,232
Mvav 92,192,212
s_nop 0
Ta 184,v2,28672
Ta 186,v2,30720
Mvav 88,220,248
L_ v7,24,s55,0
Mvav 88,196,228
S_ s55,s71,665600
Ta 188,v2,32768
Ta 190,v2,34816
Mvav 88,192,208
S_ m0,s51,41984
Mvav 84,220,244
Mvav 84,196,224
L_ v7,24,s55,0
Mvav 84,192,204
Mvav 80,220,240
Mvav 80,196,220
Mvav 80,192,200
buffer_load_dword a74, v12, s[44:47], s66 offen
Mvav 76,220,236
Mvav 76,196,216
Mvav 76,192,196
W_ 0
Mvav 16,180,196
s_nop 0
Ta 192,v3,24576
Ta 194,v3,26624
Mvav 16,184,216
buffer_load_dword a74, v4, s[24:27], s85 offen
Mvav 16,188,236
s_nop 0
Ta 196,v3,28672
Ta 198,v3,30720
Mvav 20,180,200
Mvav 20,184,220
s_nop 0
Ta 220,v3,32768
Ta 222,v3,34816
Mvav 20,188,240
Mvav 24,180,204
Mvav 24,184,224
Mvav 24,188,244
Mvav 28,180,208
Mvav 28,184,228
Mvav 28,188,248
Mvav 32,180,212
Mvav 32,184,232
Mvav 32,188,252
s_waitcnt vmcnt(25) lgkmcnt(0)
s_barrier
Mvav 52,220,252
S_ s55,s71,0
Ta 180,%0,0
Ta 182,%0,2048
Mvav 52,196,232
S_ m0,s51,24576
Mvav 52,192,212
s_nop 0
Ta 184,%0,4096
Ta 186,%0,6144
Mvav 48,220,248
L_ v7,24,s55,0
Mvav 48,196,228
S_ s55,s71,10240
Ta 188,%0,8192
Ta 190,%0,10240
Mvav 48,192,208
S_ m0,s51,25600
Mvav 44,220,244
Mvav 44,196,224
L_ v7,24,s55,0
Mvav 44,192,204
S_ s55,s71,20480
Mvav 40,220,240
S_ m0,s51,26624
Mvav 40,196,220
Mvav 40,192,200
L_ v7,24,s55,0
Mvav 36,220,236
S_ s55,s71,30720
Mvav 36,196,216
S_ m0,s51,27648
Mvav 36,192,196
W_ 0
Mvaa 56,180,0
s_nop 0
Ta 192,v1,0
Ta 194,v1,2048
Mvaa 56,184,40
L_ v7,24,s55,0
Mvaa 56,188,80
S_ s55,s71,40960
Ta 196,v1,4096
Ta 198,v1,6144
Mvaa 60,180,4
S_ m0,s51,28672
Mvaa 60,184,44
s_nop 0
Ta 220,v1,8192
Ta 222,v1,10240
Mvaa 60,188,84
L_ v7,24,s55,0
Mvaa 64,180,8
S_ s55,s71,51200
Mvaa 64,184,48
S_ m0,s51,29696
Mvaa 64,188,88
Mvaa 68,180,12
L_ v7,24,s55,0
Mvaa 68,184,52
Mvaa 68,188,240
Mvaa 72,180,16
Mvaa 72,184,56
Mvaa 72,188,244
W_ 0
Mvaa 92,220,244
s_nop 0
Ta 180,v2,0
Ta 182,v2,2048
Mvaa 92,196,56
Mvaa 92,192,16
s_nop 0
Ta 184,v2,4096
Ta 186,v2,6144
Mvaa 88,220,240
Mvaa 88,196,52
s_nop 0
Ta 188,v2,8192
Ta 190,v2,10240
Mvaa 88,192,12
Mvaa 84,220,88
Mvaa 84,196,48
Mvaa 84,192,8
Mvaa 80,220,84
Mvaa 80,196,44
Mvaa 80,192,4
Mvaa 76,220,80
Mvaa 76,196,40
Mvaa 76,192,0
W_ 0
Mvaa 16,180,0
s_nop 0
Ta 192,v3,0
Ta 194,v3,2048
Mvaa 16,184,40
Mvaa 16,188,80
s_nop 0
Ta 196,v3,4096
Ta 198,v3,6144
Mvaa 20,180,4
Mvaa 20,184,44
s_nop 0
Ta 220,v3,8192
Ta 222,v3,10240
Mvaa 20,188,84
Mvaa 24,180,8
Mvaa 24,184,48
Mvaa 24,188,88
Mvaa 28,180,12
Mvaa 28,184,52
Mvaa 28,188,240
Mvaa 32,180,16
Mvaa 32,184,56
Mvaa 32,188,244
s_waitcnt vmcnt(25) lgkmcnt(0)
s_barrier
Mvaa 52,220,244
s_nop 0
Ta 180,%0,12288
Ta 182,%0,14336
Mvaa 52,196,56
Mvaa 52,192,16
s_nop 0
Ta 184,%0,16384
Ta 186,%0,18432
Mvaa 48,220,240
Mvaa 48,196,52
s_nop 0
Ta 188,%0,20480
Ta 190,%0,22528
Mvaa 48,192,12
Mvaa 44,220,88
Mvaa 44,196,48
Mvaa 44,192,8
Mvaa 40,220,84
Mvaa 40,196,44
Mvaa 40,192,4
Mvaa 36,220,80
Mvaa 36,196,40
Mvaa 36,192,0
W_ 0
Mvaa 56,180,120
S_ s73,s78,184320
Ta 192,v1,12288
Ta 194,v1,14336
Mvaa 56,184,160
S_ s73,s73,4096
Mvaa 56,188,200
s_nop 0
Ta 196,v1,16384
Ta 198,v1,18432
Mvaa 60,180,124
s_mov_b32 m0, s52
Mvaa 60,184,164
s_nop 0
Ta 220,v1,20480
Ta 222,v1,22528
Mvaa 60,188,204
s_nop 0
Y_ 56,0
Y_ 57,256
Mvaa 64,180,128
s_nop 0
Y_ 58,512
Y_ 59,768
Mvaa 64,184,168
Mvaa 64,188,208
s_nop 0
Y_ 60,1024
Y_ 61,1280
Mvaa 68,180,132
s_nop 0
Y_ 62,1536
Y_ 63,1792
Mvaa 68,184,172
Mvaa 68,188,212
s_nop 0
Y_ 64,2048
Y_ 65,2304
Mvaa 72,180,136
s_nop 0
Y_ 66,2560
Y_ 67,2816
Mvaa 72,184,176
Mvaa 72,188,216
W_ 12
Y_ 68,3072
Y_ 69,3328
Mvaa 92,220,216
s_nop 0
Ta 180,v2,12288
Ta 182,v2,14336
Mvaa 92,196,176
s_nop 0
Y_ 70,3584
Y_ 71,3840
Mvaa 92,192,136
s_nop 0
Ta 184,v2,16384
Ta 186,v2,18432
Mvaa 88,220,212
s_nop 0
Y_ 72,4096
Y_ 73,4352
Mvaa 88,196,172
s_nop 0
Ta 188,v2,20480
Ta 190,v2,22528
Mvaa 88,192,132
s_nop 0
Y_ 74,4608
Y_ 75,4864
Mvaa 84,220,208
s_nop 0
Y_ 92,9216
Y_ 93,9472
Mvaa 84,196,168
s_nop 0
Y_ 94,9728
Y_ 95,9984
Mvaa 84,192,128
s_nop 0
Y_ 88,8192
Y_ 89,8448
Mvaa 80,220,204
s_nop 0
Y_ 90,8704
Y_ 91,8960
Mvaa 80,196,164
Mvaa 80,192,124
s_nop 0
Y_ 84,7168
Y_ 85,7424
Mvaa 76,220,200
s_nop 0
Y_ 86,7680
Y_ 87,7936
Mvaa 76,196,160
Mvaa 76,192,120
W_ 14
Y_ 80,6144
Y_ 81,6400
Mvaa 16,180,120
s_nop 0
Ta 192,v3,12288
Ta 194,v3,14336
Mvaa 16,184,160
s_nop 0
Y_ 82,6656
Y_ 83,6912
Mvaa 16,188,200
s_nop 0
Ta 196,v3,16384
Ta 198,v3,18432
Mvaa 20,180,124
s_nop 0
Y_ 76,5120
Y_ 77,5376
Mvaa 20,184,164
s_nop 0
Ta 220,v3,20480
Ta 222,v3,22528
Mvaa 20,188,204
s_nop 0
Y_ 78,5632
Y_ 79,5888
Mvaa 24,180,128
Mvaa 24,184,168
Mvaa 24,188,208
s_waitcnt vmcnt(19) lgkmcnt(0)
s_barrier
Mvaa 28,180,132
S_ s55,s71,122880
ds_read2st64_b32 v[96:97], v5 offset0:0 offset1:1
Mvaa 28,184,172
S_ m0,s51,0
ds_read2st64_b32 v[98:99], v5 offset0:2 offset1:3
Mvaa 28,188,212
L_ v7,24,s55,0
Mvaa 32,180,136
S_ s55,s71,133120
ds_read2st64_b32 v[104:105], v5 offset0:4 offset1:5
Mvaa 32,184,176
S_ m0,s51,1024
ds_read2st64_b32 v[106:107], v5 offset0:6 offset1:7
Mvaa 32,188,216
ds_read2st64_b32 v[112:113], v5 offset0:8 offset1:9
Mvaa 52,220,216
s_nop 0
Ta 180,%0,49152
Ta 182,%0,51200
Mvaa 52,196,176
L_ v7,24,s55,0
Mvaa 52,192,136
W_ 5
Ta 184,%0,53248
Ta 186,%0,55296
Mvaa 48,220,212
S_ s55,s71,143360
ds_read2st64_b32 v[114:115], v5 offset0:10 offset1:11
A_ v96,v16,v96
Mvaa 48,196,172
S_ m0,s51,2048
Ta 188,%0,57344
Ta 190,%0,59392
Mvaa 48,192,132
L_ v7,24,s55,0
Mvaa 44,220,208
S_ s55,s71,153600
ds_read2st64_b32 v[120:121], v5 offset0:12 offset1:13
A_ v97,v17,v97
Mvaa 44,196,168
S_ m0,s51,3072
ds_read2st64_b32 v[122:123], v5 offset0:14 offset1:15
A_ v98,v18,v98
Mvaa 44,192,128
ds_read2st64_b32 v[128:129], v5 offset0:16 offset1:17
A_ v99,v19,v99
Mvaa 40,220,204
L_ v7,24,s55,0
Mvaa 40,196,164
W_ 11
ds_read2st64_b32 v[130:131], v5 offset0:18 offset1:19
A_ v104,v20,v104
Mvaa 40,192,124
S_ s55,s71,163840
ds_read2st64_b32 v[132:133], v5 offset0:36 offset1:37
A_ v105,v21,v105
Mvaa 36,220,200
S_ m0,s51,4096
ds_read2st64_b32 v[134:135], v5 offset0:38 offset1:39
A_ v106,v22,v106
Mvaa 36,196,160
L_ v7,24,s55,0
Mvaa 36,192,120
W_ 6
ds_read2st64_b32 v[124:125], v5 offset0:32 offset1:33
A_ v107,v23,v107
Zvav 56,180,136
Ta 192,v1,49152
Ta 194,v1,51200
Mvav 56,184,156
S_ s55,s71,174080
ds_read2st64_b32 v[126:127], v5 offset0:34 offset1:35
A_ v112,v24,v112
Mvav 56,188,176
S_ m0,s51,5120
Ta 196,v1,53248
Ta 198,v1,55296
Zvav 60,180,140
L_ v7,24,s55,0
Mvav 60,184,160
S_ s79,s6,2
Ta 220,v1,57344
Ta 222,v1,59392
Mvav 60,188,180
s_and_b32 s79, s79, 7
ds_read2st64_b32 v[116:117], v5 offset0:28 offset1:29
A_ v113,v25,v113
)A"
R"A(Zvav 64,180,144
s_lshl_b32 s79, s79, 7
ds_read2st64_b32 v[118:119], v5 offset0:30 offset1:31
A_ v114,v26,v114
Mvav 64,184,164
S_ s71,s72,s79
ds_read2st64_b32 v[108:109], v5 offset0:24 offset1:25
A_ v115,v27,v115
Mvav 64,188,184
W_ 14
ds_read2st64_b32 v[110:111], v5 offset0:26 offset1:27
A_ v120,v28,v120
Zvav 68,180,148
S_ s55,s71,491520
ds_read2st64_b32 v[100:101], v5 offset0:20 offset1:21
A_ v121,v29,v121
Mvav 68,184,168
S_ m0,s51,12288
ds_read2st64_b32 v[102:103], v5 offset0:22 offset1:23
A_ v122,v30,v122
Mvav 68,188,188
L_ v7,24,s55,0
Zvav 72,180,152
S_ s55,s71,501760
A_ v123,v31,v123
Mvav 72,184,172
W_ 14
A_ v128,v32,v128
A_ v129,v33,v129
Mvav 72,188,192
W_ 6
A_ v130,v34,v130
A_ v131,v35,v131
Zvav 92,220,192
Ta 180,v2,49152
Ta 182,v2,51200
Mvav 92,196,172
S_ m0,s51,13312
A_ v132,v52,v132
A_ v133,v53,v133
Mvav 92,192,152
s_nop 0
Ta 184,v2,53248
Ta 186,v2,55296
Zvav 88,220,188
L_ v7,24,s55,0
Mvav 88,196,168
S_ s55,s71,512000
Ta 188,v2,57344
Ta 190,v2,59392
Mvav 88,192,148
S_ m0,s51,14336
A_ v134,v54,v134
A_ v135,v55,v135
Zvav 84,220,184
A_ v124,v48,v124
A_ v125,v49,v125
Mvav 84,196,164
L_ v7,24,s55,0
Mvav 84,192,144
S_ s55,s71,522240
A_ v126,v50,v126
A_ v127,v51,v127
Zvav 80,220,180
W_ 10
A_ v116,v44,v116
A_ v117,v45,v117
Mvav 80,196,160
S_ m0,s51,15360
A_ v118,v46,v118
A_ v119,v47,v119
Mvav 80,192,140
L_ v7,24,s55,0
Zvav 76,220,176
W_ 8
A_ v108,v40,v108
A_ v109,v41,v109
Mvav 76,196,156
S_ s55,s71,532480
A_ v110,v42,v110
A_ v111,v43,v111
Mvav 76,192,136
W_ 0
A_ v100,v36,v100
Zvav 16,180,136
S_ m0,s51,16384
Ta 192,v3,49152
Ta 194,v3,51200
Mvav 16,184,156
L_ v7,24,s55,0
Mvav 16,188,176
S_ s55,s71,542720
Ta 196,v3,53248
Ta 198,v3,55296
Zvav 20,180,140
S_ m0,s51,17408
A_ v101,v37,v101
A_ v102,v38,v102
Mvav 20,184,160
S_ s85,s6,4
Ta 220,v3,57344
Ta 222,v3,59392
Mvav 20,188,180
L_ v7,24,s55,0
Zvav 24,180,144
s_and_b32 s85, s85, 7
A_ v103,v39,v103
C_ 131,130,131
Mvav 24,184,164
s_lshl_b32 s85, s85, 7
C_ 130,128,129
C_ 132,132,133
Mvav 24,188,184
S_ s85,s85,s87
C_ 133,134,135
Zvav 28,180,148
buffer_store_dwordx4 v[130:133], v6, s[28:31], s73 offen offset:0 sc1
Mvav 28,184,168
s_sub_u32 s73, s73, 4096
Mvav 28,188,188
C_ 123,122,123
C_ 122,120,121
Zvav 32,180,152
C_ 124,124,125
C_ 125,126,127
Mvav 32,184,172
buffer_store_dwordx4 v[122:125], v6, s[28:31], s73 offen offset:3072 sc1
Mvav 32,188,192
s_waitcnt vmcnt(22) lgkmcnt(0)
s_barrier
C_ 115,114,115
Zvav 52,220,192
S_ s55,s71,614400
Ta 180,%0,36864
Ta 182,%0,38912
Mvav 52,196,172
S_ m0,s51,49152
C_ 114,112,113
C_ 116,116,117
Mvav 52,192,152
s_nop 0
Ta 184,%0,40960
Ta 186,%0,43008
Zvav 48,220,188
L_ v7,24,s55,0
Mvav 48,196,168
S_ s55,s71,624640
Ta 188,%0,45056
Ta 190,%0,47104
Mvav 48,192,148
S_ m0,s51,50176
C_ 117,118,119
Zvav 44,220,184
Mvav 44,196,164
buffer_store_dwordx4 v[114:117], v6, s[28:31], s73 offen offset:2048 sc1
Mvav 44,192,144
C_ 107,106,107
C_ 106,104,105
Zvav 40,220,180
C_ 108,108,109
C_ 109,110,111
Mvav 40,196,160
Mvav 40,192,140
buffer_store_dwordx4 v[106:109], v6, s[28:31], s73 offen offset:1024 sc1
Zvav 36,220,176
C_ 99,98,99
C_ 98,96,97
Mvav 36,196,156
C_ 100,100,101
C_ 101,102,103
Mvav 36,192,136
W_ 0
Mvav 56,180,196
s_nop 0
Ta 192,v1,36864
Ta 194,v1,38912
Mvav 56,184,216
buffer_store_dwordx4 v[98:101], v6, s[28:31], s73 offen offset:0 sc1
Mvav 56,188,236
s_nop 0
Ta 196,v1,40960
Ta 198,v1,43008
Mvav 60,180,200
X_ a20,-1
X_ a21,-1
Mvav 60,184,220
s_nop 0
Ta 220,v1,45056
Ta 222,v1,47104
Mvav 60,188,240
L_ v7,24,s55,0
Mvav 64,180,204
S_ s55,s71,634880
X_ a22,-1
X_ a23,-1
Mvav 64,184,224
S_ s81,s6,7
S_ m0,s51,51200
Mvav 64,188,244
s_and_b32 s81, s81, 7
Mvav 68,180,208
L_ v7,24,s55,0
Mvav 68,184,228
s_mul_i32 s81, s81, 20480
S_ s55,s71,645120
Mvav 68,188,248
S_ s81,s81,s78
S_ m0,s51,52224
Mvav 72,180,212
Mvav 72,184,232
buffer_store_dwordx4 a[20:23], v6, s[28:31], s81 offen offset:0 sc1
Mvav 72,188,252
W_ 0
Mvav 92,220,252
s_nop 0
Ta 180,v2,36864
Ta 182,v2,38912
Mvav 92,196,232
Mvav 92,192,212
s_nop 0
Ta 184,v2,40960
Ta 186,v2,43008
Mvav 88,220,248
buffer_store_dwordx4 a[20:23], v6, s[28:31], s81 offen offset:1024 sc1
Mvav 88,196,228
s_nop 0
Ta 188,v2,45056
Ta 190,v2,47104
Mvav 88,192,208
Mvav 84,220,244
Mvav 84,196,224
buffer_store_dwordx4 a[20:23], v6, s[28:31], s81 offen offset:2048 sc1
Mvav 84,192,204
Mvav 80,220,240
Mvav 80,196,220
Mvav 80,192,200
buffer_store_dwordx4 a[20:23], v6, s[28:31], s81 offen offset:3072 sc1
Mvav 76,220,236
S_ s81,s81,4096
Mvav 76,196,216
Mvav 76,192,196
W_ 0
Mvav 16,180,196
s_nop 0
Ta 192,v3,36864
Ta 194,v3,38912
Mvav 16,184,216
buffer_store_dwordx4 a[20:23], v6, s[28:31], s81 offen offset:0 sc1
Mvav 16,188,236
s_nop 0
Ta 196,v3,40960
Ta 198,v3,43008
Mvav 20,180,200
Mvav 20,184,220
s_nop 0
Ta 220,v3,45056
Ta 222,v3,47104
Mvav 20,188,240
L_ v7,24,s55,0
Mvav 24,180,204
S_ s55,s71,655360
Mvav 24,184,224
S_ m0,s51,53248
Mvav 24,188,244
Mvav 28,180,208
L_ v7,24,s55,0
Mvav 28,184,228
S_ s55,s71,665600
Mvav 28,188,248
S_ m0,s51,54272
Mvav 32,180,212
Mvav 32,184,232
L_ v7,24,s55,0
Mvav 32,188,252
s_waitcnt vmcnt(28) lgkmcnt(0)
s_barrier
Mvav 52,220,252
S_ s55,s71,0
Ta 180,%0,24576
Ta 182,%0,26624
Mvav 52,196,232
S_ m0,s51,36864
Mvav 52,192,212
s_nop 0
Ta 184,%0,28672
Ta 186,%0,30720
Mvav 48,220,248
L_ v7,24,s55,0
Mvav 48,196,228
S_ s55,s71,10240
Ta 188,%0,32768
Ta 190,%0,34816
Mvav 48,192,208
S_ m0,s51,37888
Mvav 44,220,244
Mvav 44,196,224
L_ v7,24,s55,0
Mvav 44,192,204
S_ s55,s71,20480
Mvav 40,220,240
S_ m0,s51,38912
Mvav 40,196,220
Mvav 40,192,200
L_ v7,24,s55,0
Mvav 36,220,236
S_ s55,s71,30720
Mvav 36,196,216
S_ m0,s51,39936
Mvav 36,192,196
W_ 0
Mvaa 56,180,0
s_nop 0
Ta 192,v1,24576
Ta 194,v1,26624
Mvaa 56,184,40
L_ v7,24,s55,0
Mvaa 56,188,80
S_ s55,s71,40960
Ta 196,v1,28672
Ta 198,v1,30720
Mvaa 60,180,4
S_ m0,s51,40960
Mvaa 60,184,44
s_nop 0
Ta 220,v1,32768
Ta 222,v1,34816
Mvaa 60,188,84
L_ v7,24,s55,0
Mvaa 64,180,8
S_ s55,s71,51200
Mvaa 64,184,48
S_ m0,s51,41984
Mvaa 64,188,88
Mvaa 68,180,12
L_ v7,24,s55,0
Mvaa 68,184,52
Mvaa 68,188,240
Mvaa 72,180,16
Mvaa 72,184,56
buffer_load_dword a74, v4, s[24:27], s85 offen
Mvaa 72,188,244
W_ 0
Mvaa 92,220,244
s_nop 0
Ta 180,v2,24576
Ta 182,v2,26624
Mvaa 92,196,56
Mvaa 92,192,16
s_nop 0
Ta 184,v2,28672
Ta 186,v2,30720
Mvaa 88,220,240
Mvaa 88,196,52
s_nop 0
Ta 188,v2,32768
Ta 190,v2,34816
Mvaa 88,192,12
Mvaa 84,220,88
Mvaa 84,196,48
Mvaa 84,192,8
Mvaa 80,220,84
Mvaa 80,196,44
Mvaa 80,192,4
Mvaa 76,220,80
Mvaa 76,196,40
Mvaa 76,192,0
W_ 0
Mvaa 16,180,0
s_nop 0
Ta 192,v3,24576
Ta 194,v3,26624
Mvaa 16,184,40
Mvaa 16,188,80
s_nop 0
Ta 196,v3,28672
Ta 198,v3,30720
Mvaa 20,180,4
Mvaa 20,184,44
s_nop 0
Ta 220,v3,32768
Ta 222,v3,34816
Mvaa 20,188,84
Mvaa 24,180,8
Mvaa 24,184,48
Mvaa 24,188,88
Mvaa 28,180,12
Mvaa 28,184,52
Mvaa 28,188,240
Mvaa 32,180,16
Mvaa 32,184,56
Mvaa 32,188,244
s_waitcnt vmcnt(29) lgkmcnt(0)
s_barrier
Mvaa 52,220,244
s_nop 0
Ta 180,%0,0
Ta 182,%0,2048
Mvaa 52,196,56
Mvaa 52,192,16
s_nop 0
Ta 184,%0,4096
Ta 186,%0,6144
Mvaa 48,220,240
Mvaa 48,196,52
s_nop 0
Ta 188,%0,8192
Ta 190,%0,10240
Mvaa 48,192,12
Mvaa 44,220,88
Mvaa 44,196,48
Mvaa 44,192,8
Mvaa 40,220,84
Mvaa 40,196,44
Mvaa 40,192,4
Mvaa 36,220,80
Mvaa 36,196,40
Mvaa 36,192,0
W_ 0
Mvaa 56,180,120
S_ s81,s6,1
Ta 192,v1,0
Ta 194,v1,2048
Mvaa 56,184,160
s_and_b32 s81, s81, 7
Mvaa 56,188,200
s_mul_i32 s81, s81, 20480
Ta 196,v1,4096
Ta 198,v1,6144
Mvaa 60,180,124
s_mov_b32 m0, s52
S_ s73,s81,s78
Mvaa 60,184,164
S_ s73,s73,4096
Ta 220,v1,8192
Ta 222,v1,10240
Mvaa 60,188,204
s_nop 0
Y_ 56,0
Y_ 57,256
Mvaa 64,180,128
s_nop 0
Y_ 58,512
Y_ 59,768
Mvaa 64,184,168
Mvaa 64,188,208
s_nop 0
Y_ 60,1024
Y_ 61,1280
Mvaa 68,180,132
s_nop 0
Y_ 62,1536
Y_ 63,1792
Mvaa 68,184,172
Mvaa 68,188,212
s_nop 0
Y_ 64,2048
Y_ 65,2304
Mvaa 72,180,136
s_nop 0
Y_ 66,2560
Y_ 67,2816
Mvaa 72,184,176
Mvaa 72,188,216
W_ 12
Y_ 68,3072
Y_ 69,3328
Mvaa 92,220,216
s_nop 0
Ta 180,v2,0
Ta 182,v2,2048
Mvaa 92,196,176
s_nop 0
Y_ 70,3584
Y_ 71,3840
Mvaa 92,192,136
s_nop 0
Ta 184,v2,4096
Ta 186,v2,6144
Mvaa 88,220,212
s_nop 0
Y_ 72,4096
Y_ 73,4352
Mvaa 88,196,172
s_nop 0
Ta 188,v2,8192
Ta 190,v2,10240
Mvaa 88,192,132
s_nop 0
Y_ 74,4608
Y_ 75,4864
Mvaa 84,220,208
s_nop 0
Y_ 92,9216
Y_ 93,9472
Mvaa 84,196,168
s_nop 0
Y_ 94,9728
Y_ 95,9984
Mvaa 84,192,128
s_nop 0
Y_ 88,8192
Y_ 89,8448
Mvaa 80,220,204
s_nop 0
Y_ 90,8704
Y_ 91,8960
Mvaa 80,196,164
Mvaa 80,192,124
s_nop 0
Y_ 84,7168
Y_ 85,7424
Mvaa 76,220,200
s_nop 0
Y_ 86,7680
Y_ 87,7936
Mvaa 76,196,160
Mvaa 76,192,120
W_ 14
Y_ 80,6144
Y_ 81,6400
Mvaa 16,180,120
s_nop 0
Ta 192,v3,0
Ta 194,v3,2048
Mvaa 16,184,160
s_nop 0
Y_ 82,6656
Y_ 83,6912
Mvaa 16,188,200
s_nop 0
Ta 196,v3,4096
Ta 198,v3,6144
Mvaa 20,180,124
s_nop 0
Y_ 76,5120
Y_ 77,5376
Mvaa 20,184,164
s_nop 0
Ta 220,v3,8192
Ta 222,v3,10240
Mvaa 20,188,204
s_nop 0
Y_ 78,5632
Y_ 79,5888
Mvaa 24,180,128
Mvaa 24,184,168
Mvaa 24,188,208
s_waitcnt vmcnt(23) lgkmcnt(0)
s_barrier
Mvaa 28,180,132
S_ s55,s71,122880
ds_read2st64_b32 v[96:97], v5 offset0:0 offset1:1
Mvaa 28,184,172
S_ m0,s51,24576
ds_read2st64_b32 v[98:99], v5 offset0:2 offset1:3
Mvaa 28,188,212
L_ v7,24,s55,0
Mvaa 32,180,136
S_ s55,s71,133120
ds_read2st64_b32 v[104:105], v5 offset0:4 offset1:5
Mvaa 32,184,176
S_ m0,s51,25600
ds_read2st64_b32 v[106:107], v5 offset0:6 offset1:7
Mvaa 32,188,216
ds_read2st64_b32 v[112:113], v5 offset0:8 offset1:9
Mvaa 52,220,216
s_nop 0
Ta 180,%0,12288
Ta 182,%0,14336
Mvaa 52,196,176
L_ v7,24,s55,0
Mvaa 52,192,136
W_ 5
Ta 184,%0,16384
Ta 186,%0,18432
Mvaa 48,220,212
S_ s55,s71,143360
ds_read2st64_b32 v[114:115], v5 offset0:10 offset1:11
A_ v96,v16,v96
Mvaa 48,196,172
S_ m0,s51,26624
Ta 188,%0,20480
Ta 190,%0,22528
Mvaa 48,192,132
L_ v7,24,s55,0
Mvaa 44,220,208
S_ s55,s71,153600
ds_read2st64_b32 v[120:121], v5 offset0:12 offset1:13
A_ v97,v17,v97
Mvaa 44,196,168
S_ m0,s51,27648
ds_read2st64_b32 v[122:123], v5 offset0:14 offset1:15
A_ v98,v18,v98
Mvaa 44,192,128
ds_read2st64_b32 v[128:129], v5 offset0:16 offset1:17
A_ v99,v19,v99
Mvaa 40,220,204
L_ v7,24,s55,0
Mvaa 40,196,164
W_ 11
ds_read2st64_b32 v[130:131], v5 offset0:18 offset1:19
A_ v104,v20,v104
Mvaa 40,192,124
S_ s55,s71,163840
ds_read2st64_b32 v[132:133], v5 offset0:36 offset1:37
A_ v105,v21,v105
Mvaa 36,220,200
S_ m0,s51,28672
ds_read2st64_b32 v[134:135], v5 offset0:38 offset1:39
A_ v106,v22,v106
Mvaa 36,196,160
L_ v7,24,s55,0
Mvaa 36,192,120
W_ 6
ds_read2st64_b32 v[124:125], v5 offset0:32 offset1:33
A_ v107,v23,v107
Zvav 56,180,136
Ta 192,v1,12288
Ta 194,v1,14336
Mvav 56,184,156
S_ s55,s71,174080
ds_read2st64_b32 v[126:127], v5 offset0:34 offset1:35
A_ v112,v24,v112
Mvav 56,188,176
S_ m0,s51,29696
Ta 196,v1,16384
Ta 198,v1,18432
Zvav 60,180,140
L_ v7,24,s55,0
Mvav 60,184,160
S_ s79,s6,3
Ta 220,v1,20480
Ta 222,v1,22528
Mvav 60,188,180
s_and_b32 s79, s79, 7
ds_read2st64_b32 v[116:117], v5 offset0:28 offset1:29
A_ v113,v25,v113
Zvav 64,180,144
s_lshl_b32 s79, s79, 7
ds_read2st64_b32 v[118:119], v5 offset0:30 offset1:31
A_ v114,v26,v114
Mvav 64,184,164
S_ s71,s72,s79
ds_read2st64_b32 v[108:109], v5 offset0:24 offset1:25
A_ v115,v27,v115
Mvav 64,188,184
W_ 14
ds_read2st64_b32 v[110:111], v5 offset0:26 offset1:27
A_ v120,v28,v120
Zvav 68,180,148
S_ s55,s71,491520
ds_read2st64_b32 v[100:101], v5 offset0:20 offset1:21
A_ v121,v29,v121
Mvav 68,184,168
S_ m0,s51,0
ds_read2st64_b32 v[102:103], v5 offset0:22 offset1:23
A_ v122,v30,v122
Mvav 68,188,188
L_ v7,24,s55,0
Zvav 72,180,152
S_ s55,s71,501760
A_ v123,v31,v123
Mvav 72,184,172
W_ 14
A_ v128,v32,v128
A_ v129,v33,v129
Mvav 72,188,192
W_ 6
A_ v130,v34,v130
A_ v131,v35,v131
Zvav 92,220,192
Ta 180,v2,12288
Ta 182,v2,14336
Mvav 92,196,172
S_ m0,s51,1024
A_ v132,v52,v132
A_ v133,v53,v133
Mvav 92,192,152
s_nop 0
Ta 184,v2,16384
Ta 186,v2,18432
Zvav 88,220,188
L_ v7,24,s55,0
Mvav 88,196,168
S_ s55,s71,512000
Ta 188,v2,20480
Ta 190,v2,22528
Mvav 88,192,148
S_ m0,s51,2048
A_ v134,v54,v134
A_ v135,v55,v135
Zvav 84,220,184
A_ v124,v48,v124
A_ v125,v49,v125
Mvav 84,196,164
L_ v7,24,s55,0
Mvav 84,192,144
S_ s55,s71,522240
A_ v126,v50,v126
A_ v127,v51,v127
Zvav 80,220,180
W_ 10
A_ v116,v44,v116
A_ v117,v45,v117
Mvav 80,196,160
S_ m0,s51,3072
A_ v118,v46,v118
A_ v119,v47,v119
Mvav 80,192,140
L_ v7,24,s55,0
Zvav 76,220,176
W_ 8
A_ v108,v40,v108
A_ v109,v41,v109
Mvav 76,196,156
S_ s55,s71,532480
A_ v110,v42,v110
A_ v111,v43,v111
Mvav 76,192,136
W_ 0
A_ v100,v36,v100
Zvav 16,180,136
S_ m0,s51,4096
Ta 192,v3,12288
Ta 194,v3,14336
Mvav 16,184,156
L_ v7,24,s55,0
Mvav 16,188,176
S_ s55,s71,542720
Ta 196,v3,16384
Ta 198,v3,18432
Zvav 20,180,140
S_ m0,s51,5120
A_ v101,v37,v101
A_ v102,v38,v102
Mvav 20,184,160
S_ s85,s6,5
Ta 220,v3,20480
Ta 222,v3,22528
Mvav 20,188,180
L_ v7,24,s55,0
Zvav 24,180,144
s_and_b32 s85, s85, 7
A_ v103,v39,v103
C_ 131,130,131
Mvav 24,184,164
s_lshl_b32 s85, s85, 7
C_ 130,128,129
C_ 132,132,133
Mvav 24,188,184
S_ s85,s85,s87
C_ 133,134,135
Zvav 28,180,148
buffer_store_dwordx4 v[130:133], v6, s[28:31], s73 offen offset:0 sc1
Mvav 28,184,168
s_sub_u32 s73, s73, 4096
Mvav 28,188,188
C_ 123,122,123
C_ 122,120,121
Zvav 32,180,152
C_ 124,124,125
C_ 125,126,127
Mvav 32,184,172
buffer_store_dwordx4 v[122:125], v6, s[28:31], s73 offen offset:3072 sc1
Mvav 32,188,192
s_waitcnt vmcnt(21) lgkmcnt(0)
s_barrier
C_ 115,114,115
Zvav 52,220,192
S_ s56,s76,s74
Ta 180,%0,49152
Ta 182,%0,51200
Mvav 52,196,172
S_ s55,s71,614400
C_ 114,112,113
C_ 116,116,117
Mvav 52,192,152
S_ m0,s51,12288
Ta 184,%0,53248
Ta 186,%0,55296
Zvav 48,220,188
buffer_load_dwordx4 a[60:63], v6, s[28:31], s56 offen offset:0 sc1
Mvav 48,196,168
S_ s56,s76,s82
Ta 188,%0,57344
Ta 190,%0,59392
Mvav 48,192,148
C_ 117,118,119
Zvav 44,220,184
Mvav 44,196,164
buffer_load_dwordx4 a[64:67], v6, s[28:31], s56 offen offset:0 sc1
Mvav 44,192,144
Zvav 40,220,180
Mvav 40,196,160
Mvav 40,192,140
buffer_store_dwordx4 v[114:117], v6, s[28:31], s73 offen offset:2048 sc1
Zvav 36,220,176
C_ 107,106,107
C_ 106,104,105
Mvav 36,196,156
C_ 108,108,109
C_ 109,110,111
Mvav 36,192,136
W_ 0
Mvav 56,180,196
s_nop 0
Ta 192,v1,49152
Ta 194,v1,51200
Mvav 56,184,216
buffer_store_dwordx4 v[106:109], v6, s[28:31], s73 offen offset:1024 sc1
Mvav 56,188,236
s_nop 0
Ta 196,v1,53248
Ta 198,v1,55296
Mvav 60,180,200
C_ 99,98,99
C_ 98,96,97
Mvav 60,184,220
s_nop 0
Ta 220,v1,57344
Ta 222,v1,59392
Mvav 60,188,240
L_ v7,24,s55,0
Mvav 64,180,204
S_ s55,s71,624640
C_ 100,100,101
C_ 101,102,103
Mvav 64,184,224
S_ m0,s51,13312
Mvav 64,188,244
Mvav 68,180,208
buffer_store_dwordx4 v[98:101], v6, s[28:31], s73 offen offset:0 sc1
Mvav 68,184,228
Mvav 68,188,248
Mvav 72,180,212
Mvav 72,184,232
L_ v7,24,s55,0
Mvav 72,188,252
W_ 0
S_ s55,s71,634880
Mvav 92,220,252
S_ m0,s51,14336
Ta 180,v2,49152
Ta 182,v2,51200
Mvav 92,196,232
Mvav 92,192,212
s_nop 0
Ta 184,v2,53248
Ta 186,v2,55296
Mvav 88,220,248
L_ v7,24,s55,0
Mvav 88,196,228
S_ s55,s71,645120
Ta 188,v2,57344
Ta 190,v2,59392
Mvav 88,192,208
S_ m0,s51,15360
Mvav 84,220,244
Mvav 84,196,224
L_ v7,24,s55,0
Mvav 84,192,204
S_ s55,s71,655360
Mvav 80,220,240
S_ m0,s51,16384
Mvav 80,196,220
S_ s83,s6,7
Mvav 80,192,200
L_ v7,24,s55,0
Mvav 76,220,236
s_and_b32 s83, s83, 7
S_ s55,s71,665600
Mvav 76,196,216
s_lshl_b32 s83, s83, 2
S_ m0,s51,17408
Mvav 76,192,196
W_ 0
v_cmp_eq_u32_e64 s[36:37], s83, v8
Mvav 16,180,196
s_nop 0
Ta 192,v3,49152
Ta 194,v3,51200
Mvav 16,184,216
L_ v7,24,s55,0
Mvav 16,188,236
s_nop 0
Ta 196,v3,53248
Ta 198,v3,55296
Mvav 20,180,200
v_mov_b32 v10, 0x7fff0000
Mvav 20,184,220
s_nop 0
Ta 220,v3,57344
Ta 222,v3,59392
Mvav 20,188,240
buffer_load_dword a74, v4, s[24:27], s85 offen
Mvav 24,180,204
v_cndmask_b32_e64 v10, v10, v9, s[36:37]
Mvav 24,184,224
Mvav 24,188,244
Mvav 28,180,208
Mvav 28,184,228
Mvav 28,188,248
Mvav 32,180,212
Mvav 32,184,232
Mvav 32,188,252
s_waitcnt vmcnt(27) lgkmcnt(0)
s_barrier
Mvav 52,220,252
S_ s55,s71,0
Ta 180,%0,36864
Ta 182,%0,38912
Mvav 52,196,232
buffer_store_dword a255, v10, s[92:95], 0 offen offset:384 sc1
Mvav 52,192,212
S_ m0,s51,49152
Ta 184,%0,40960
Ta 186,%0,43008
Mvav 48,220,248
Mvav 48,196,228
s_nop 0
Ta 188,%0,45056
Ta 190,%0,47104
Mvav 48,192,208
L_ v7,24,s55,0
Mvav 44,220,244
S_ s55,s71,10240
Mvav 44,196,224
S_ m0,s51,50176
Mvav 44,192,204
Mvav 40,220,240
L_ v7,24,s55,0
Mvav 40,196,220
S_ s55,s71,20480
Mvav 40,192,200
S_ m0,s51,51200
Mvav 36,220,236
Mvav 36,196,216
L_ v7,24,s55,0
Mvav 36,192,196
W_ 0
S_ s55,s71,30720
Mvaa 56,180,0
S_ m0,s51,52224
Ta 192,v1,36864
Ta 194,v1,38912
Mvaa 56,184,40
Mvaa 56,188,80
s_nop 0
Ta 196,v1,40960
Ta 198,v1,43008
Mvaa 60,180,4
L_ v7,24,s55,0
Mvaa 60,184,44
S_ s55,s71,40960
Ta 220,v1,45056
Ta 222,v1,47104
Mvaa 60,188,84
S_ m0,s51,53248
Mvaa 64,180,8
Mvaa 64,184,48
L_ v7,24,s55,0
Mvaa 64,188,88
S_ s55,s71,51200
Mvaa 68,180,12
S_ m0,s51,54272
Mvaa 68,184,52
Mvaa 68,188,240
L_ v7,24,s55,0
Mvaa 72,180,16
Mvaa 72,184,56
Mvaa 72,188,244
W_ 0
Mvaa 92,220,244
s_nop 0
Ta 180,v2,36864
Ta 182,v2,38912
Mvaa 92,196,56
Mvaa 92,192,16
s_nop 0
Ta 184,v2,40960
Ta 186,v2,43008
Mvaa 88,220,240
Mvaa 88,196,52
s_nop 0
Ta 188,v2,45056
Ta 190,v2,47104
Mvaa 88,192,12
Mvaa 84,220,88
Mvaa 84,196,48
Mvaa 84,192,8
Mvaa 80,220,84
Mvaa 80,196,44
Mvaa 80,192,4
Mvaa 76,220,80
Mvaa 76,196,40
Mvaa 76,192,0
W_ 0
Mvaa 16,180,0
s_nop 0
Ta 192,v3,36864
Ta 194,v3,38912
Mvaa 16,184,40
Mvaa 16,188,80
s_nop 0
Ta 196,v3,40960
Ta 198,v3,43008
Mvaa 20,180,4
Mvaa 20,184,44
s_nop 0
Ta 220,v3,45056
Ta 222,v3,47104
Mvaa 20,188,84
Mvaa 24,180,8
Mvaa 24,184,48
Mvaa 24,188,88
Mvaa 28,180,12
Mvaa 28,184,52
Mvaa 28,188,240
Mvaa 32,180,16
Mvaa 32,184,56
Mvaa 32,188,244
s_waitcnt vmcnt(27) lgkmcnt(0)
s_barrier
Mvaa 52,220,244
s_nop 0
Ta 180,%0,24576
Ta 182,%0,26624
Mvaa 52,196,56
Mvaa 52,192,16
s_nop 0
Ta 184,%0,28672
Ta 186,%0,30720
Mvaa 48,220,240
Mvaa 48,196,52
s_nop 0
Ta 188,%0,32768
Ta 190,%0,34816
Mvaa 48,192,12
Mvaa 44,220,88
Mvaa 44,196,48
Mvaa 44,192,8
Mvaa 40,220,84
Mvaa 40,196,44
Mvaa 40,192,4
Mvaa 36,220,80
Mvaa 36,196,40
Mvaa 36,192,0
W_ 0
Mvaa 56,180,120
S_ s81,s6,2
Ta 192,v1,24576
Ta 194,v1,26624
Mvaa 56,184,160
s_and_b32 s81, s81, 7
Mvaa 56,188,200
s_mul_i32 s81, s81, 20480
Ta 196,v1,28672
Ta 198,v1,30720
Mvaa 60,180,124
s_mov_b32 m0, s52
S_ s73,s81,s78
Mvaa 60,184,164
S_ s73,s73,4096
Ta 220,v1,32768
Ta 222,v1,34816
Mvaa 60,188,204
s_nop 0
Y_ 56,0
Y_ 57,256
Mvaa 64,180,128
s_nop 0
Y_ 58,512
Y_ 59,768
Mvaa 64,184,168
Mvaa 64,188,208
s_nop 0
Y_ 60,1024
Y_ 61,1280
Mvaa 68,180,132
s_nop 0
Y_ 62,1536
Y_ 63,1792
Mvaa 68,184,172
Mvaa 68,188,212
s_nop 0
Y_ 64,2048
Y_ 65,2304
Mvaa 72,180,136
s_nop 0
Y_ 66,2560
Y_ 67,2816
Mvaa 72,184,176
Mvaa 72,188,216
W_ 12
Y_ 68,3072
Y_ 69,3328
Mvaa 92,220,216
s_nop 0
Ta 180,v2,24576
Ta 182,v2,26624
Mvaa 92,196,176
s_nop 0
Y_ 70,3584
Y_ 71,3840
Mvaa 92,192,136
s_nop 0
Ta 184,v2,28672
Ta 186,v2,30720
Mvaa 88,220,212
s_nop 0
Y_ 72,4096
Y_ 73,4352
Mvaa 88,196,172
S_ s83,s6,1
Ta 188,v2,32768
Ta 190,v2,34816
Mvaa 88,192,132
s_and_b32 s83, s83, 7
Y_ 74,4608
Y_ 75,4864
Mvaa 84,220,208
s_lshl_b32 s83, s83, 2
Y_ 92,9216
Y_ 93,9472
Mvaa 84,196,168
s_nop 0
Y_ 94,9728
Y_ 95,9984
Mvaa 84,192,128
s_nop 0
Y_ 88,8192
Y_ 89,8448
Mvaa 80,220,204
s_nop 0
Y_ 90,8704
Y_ 91,8960
Mvaa 80,196,164
v_cmp_eq_u32_e64 s[36:37], s83, v8
Mvaa 80,192,124
s_nop 0
Y_ 84,7168
Y_ 85,7424
Mvaa 76,220,200
s_nop 0
Y_ 86,7680
Y_ 87,7936
Mvaa 76,196,160
v_mov_b32 v10, 0x7fff0000
Mvaa 76,192,120
W_ 14
Y_ 80,6144
Y_ 81,6400
Mvaa 16,180,120
s_nop 0
Ta 192,v3,24576
Ta 194,v3,26624
Mvaa 16,184,160
s_nop 0
Y_ 82,6656
Y_ 83,6912
Mvaa 16,188,200
s_nop 0
Ta 196,v3,28672
Ta 198,v3,30720
Mvaa 20,180,124
s_nop 0
Y_ 76,5120
Y_ 77,5376
Mvaa 20,184,164
s_nop 0
Ta 220,v3,32768
Ta 222,v3,34816
Mvaa 20,188,204
s_nop 0
Y_ 78,5632
Y_ 79,5888
Mvaa 24,180,128
v_cndmask_b32_e64 v10, v10, v9, s[36:37]
Mvaa 24,184,168
Mvaa 24,188,208
s_waitcnt vmcnt(13) lgkmcnt(0)
s_barrier
Mvaa 28,180,132
buffer_store_dword a255, v10, s[92:95], 0 offen offset:384 sc1
Mvaa 28,184,172
S_ s55,s71,122880
ds_read2st64_b32 v[96:97], v5 offset0:0 offset1:1
Mvaa 28,188,212
S_ m0,s51,36864
ds_read2st64_b32 v[98:99], v5 offset0:2 offset1:3
Mvaa 32,180,136
ds_read2st64_b32 v[104:105], v5 offset0:4 offset1:5
Mvaa 32,184,176
L_ v7,24,s55,0
Mvaa 32,188,216
S_ s55,s71,133120
ds_read2st64_b32 v[106:107], v5 offset0:6 offset1:7
Mvaa 52,220,216
S_ m0,s51,37888
Ta 180,%0,0
Ta 182,%0,2048
Mvaa 52,196,176
ds_read2st64_b32 v[112:113], v5 offset0:8 offset1:9
Mvaa 52,192,136
W_ 5
Ta 184,%0,4096
Ta 186,%0,6144
Mvaa 48,220,212
L_ v7,24,s55,0
Mvaa 48,196,172
S_ s55,s71,143360
Ta 188,%0,8192
Ta 190,%0,10240
Mvaa 48,192,132
S_ m0,s51,38912
ds_read2st64_b32 v[114:115], v5 offset0:10 offset1:11
A_ v96,v16,v96
Mvaa 44,220,208
ds_read2st64_b32 v[120:121], v5 offset0:12 offset1:13
A_ v97,v17,v97
Mvaa 44,196,168
L_ v7,24,s55,0
Mvaa 44,192,128
S_ s55,s71,153600
ds_read2st64_b32 v[122:123], v5 offset0:14 offset1:15
A_ v98,v18,v98
Mvaa 40,220,204
S_ m0,s51,39936
ds_read2st64_b32 v[128:129], v5 offset0:16 offset1:17
A_ v99,v19,v99
Mvaa 40,196,164
W_ 11
ds_read2st64_b32 v[130:131], v5 offset0:18 offset1:19
A_ v104,v20,v104
Mvaa 40,192,124
L_ v7,24,s55,0
Mvaa 36,220,200
S_ s55,s71,163840
ds_read2st64_b32 v[132:133], v5 offset0:36 offset1:37
A_ v105,v21,v105
Mvaa 36,196,160
S_ m0,s51,40960
ds_read2st64_b32 v[134:135], v5 offset0:38 offset1:39
A_ v106,v22,v106
Mvaa 36,192,120
ds_read2st64_b32 v[124:125], v5 offset0:32 offset1:33
A_ v107,v23,v107
s_waitcnt vmcnt(22)
Zaaa 224,70,60
L_ v7,24,s55,0
Zaaa 228,68,60
W_ 7
ds_read2st64_b32 v[126:127], v5 offset0:34 offset1:35
A_ v112,v24,v112
Zaaa 232,70,64
S_ s55,s71,174080
ds_read2st64_b32 v[116:117], v5 offset0:28 offset1:29
A_ v113,v25,v113
Zaaa 236,68,64
ds_read2st64_b32 v[118:119], v5 offset0:30 offset1:31
A_ v114,v26,v114
Zvav 56,180,136
S_ m0,s51,41984
Ta 192,v1,0
Ta 194,v1,2048
Mvav 56,184,156
L_ v7,24,s55,0
Mvav 56,188,176
S_ s79,s6,4
Ta 196,v1,4096
Ta 198,v1,6144
Zvav 60,180,140
s_and_b32 s79, s79, 7
ds_read2st64_b32 v[108:109], v5 offset0:24 offset1:25
A_ v115,v27,v115
Mvav 60,184,160
W_ 13
Ta 220,v1,8192
Ta 222,v1,10240
Mvav 60,188,180
s_lshl_b32 s79, s79, 7
ds_read2st64_b32 v[110:111], v5 offset0:26 offset1:27
A_ v120,v28,v120
Zvav 64,180,144
S_ s71,s72,s79
ds_read2st64_b32 v[100:101], v5 offset0:20 offset1:21
A_ v121,v29,v121
Mvav 64,184,164
S_ s55,s71,491520
ds_read2st64_b32 v[102:103], v5 offset0:22 offset1:23
A_ v122,v30,v122
Mvav 64,188,184
S_ m0,s51,24576
A_ v123,v31,v123
R_ v12,a248
Zvav 68,180,148
L_ v7,24,s55,0
Mvav 68,184,168
W_ 14
A_ v128,v32,v128
A_ v129,v33,v129
Mvav 68,188,188
S_ s55,s71,501760
A_ v130,v34,v130
A_ v131,v35,v131
Zvav 72,180,152
A_ v132,v52,v132
A_ v133,v53,v133
Mvav 72,184,172
S_ m0,s51,25600
A_ v134,v54,v134
A_ v135,v55,v135
Mvav 72,188,192
W_ 3
A_ v124,v48,v124
Zvav 92,220,192
s_nop 0
Ta 180,v2,0
Ta 182,v2,2048
Mvav 92,196,172
L_ v7,24,s55,0
Mvav 92,192,152
S_ s55,s71,512000
Ta 184,v2,4096
Ta 186,v2,6144
Zvav 88,220,188
S_ m0,s51,26624
A_ v125,v49,v125
A_ v126,v50,v126
Mvav 88,196,168
s_nop 0
Ta 188,v2,8192
Ta 190,v2,10240
Mvav 88,192,148
L_ v7,24,s55,0
Zvav 84,220,184
S_ s55,s71,522240
A_ v127,v51,v127
C_ 131,130,131
Mvav 84,196,164
A_ v116,v44,v116
A_ v117,v45,v117
Mvav 84,192,144
S_ m0,s51,27648
A_ v118,v46,v118
A_ v119,v47,v119
Zvav 80,220,180
L_ v7,24,s55,0
Mvav 80,196,160
W_ 8
A_ v108,v40,v108
A_ v109,v41,v109
Mvav 80,192,140
S_ s55,s71,532480
A_ v110,v42,v110
A_ v111,v43,v111
Zvav 76,220,176
W_ 6
A_ v100,v36,v100
A_ v101,v37,v101
Mvav 76,196,156
S_ m0,s51,28672
A_ v102,v38,v102
A_ v103,v39,v103
Mvav 76,192,136
W_ 0
C_ 130,128,129
C_ 132,132,133
Zvav 16,180,136
S_ s85,s6,6
Ta 192,v3,0
Ta 194,v3,2048
Mvav 16,184,156
L_ v7,24,s55,0
Mvav 16,188,176
S_ s55,s71,542720
Ta 196,v3,4096
Ta 198,v3,6144
Zvav 20,180,140
S_ m0,s51,29696
C_ 133,134,135
v_min_u32 v12, 240, v12
Mvav 20,184,160
s_and_b32 s85, s85, 7
Ta 220,v3,8192
Ta 222,v3,10240
Mvav 20,188,180
buffer_store_dwordx4 v[130:133], v6, s[28:31], s73 offen offset:0 sc1
Zvav 24,180,144
s_sub_u32 s73, s73, 4096
v_lshlrev_b32 v12, 3, v12
s_lshl_b32 s85, s85, 7
Mvav 24,184,164
S_ s85,s85,s87
C_ 123,122,123
C_ 122,120,121
Mvav 24,188,184
C_ 124,124,125
C_ 125,126,127
Zvav 28,180,148
buffer_store_dwordx4 v[122:125], v6, s[28:31], s73 offen offset:3072 sc1
Mvav 28,184,168
C_ 115,114,115
C_ 114,112,113
Mvav 28,188,188
C_ 116,116,117
C_ 117,118,119
Zvav 32,180,152
Mvav 32,184,172
buffer_store_dwordx4 v[114:117], v6, s[28:31], s73 offen offset:2048 sc1
Mvav 32,188,192
s_waitcnt vmcnt(23) lgkmcnt(0)
s_barrier
C_ 107,106,107
Zvav 52,220,192
s_nop 0
Ta 180,%0,12288
Ta 182,%0,14336
Mvav 52,196,172
C_ 106,104,105
C_ 108,108,109
Mvav 52,192,152
s_nop 0
Ta 184,%0,16384
Ta 186,%0,18432
Zvav 48,220,188
L_ v7,24,s55,0
Mvav 48,196,168
S_ s55,s71,614400
Ta 188,%0,20480
Ta 190,%0,22528
Mvav 48,192,148
S_ m0,s51,0
C_ 109,110,111
Zvav 44,220,184
Mvav 44,196,164
buffer_store_dwordx4 v[106:109], v6, s[28:31], s73 offen offset:1024 sc1
Mvav 44,192,144
C_ 99,98,99
C_ 98,96,97
Zvav 40,220,180
C_ 100,100,101
C_ 101,102,103
Mvav 40,196,160
Mvav 40,192,140
buffer_store_dwordx4 v[98:101], v6, s[28:31], s73 offen offset:0 sc1
Zvav 36,220,176
Mvav 36,196,156
Mvav 36,192,136
W_ 0
Mvav 56,180,196
s_nop 0
Ta 192,v1,12288
Ta 194,v1,14336
Mvav 56,184,216
L_ v7,24,s55,0
Mvav 56,188,236
S_ s55,s71,624640
Ta 196,v1,16384
Ta 198,v1,18432
Mvav 60,180,200
S_ m0,s51,1024
Mvav 60,184,220
s_nop 0
Ta 220,v1,20480
Ta 222,v1,22528
Mvav 60,188,240
L_ v7,24,s55,0
Mvav 64,180,204
S_ s55,s71,634880
Mvav 64,184,224
S_ m0,s51,2048
Mvav 64,188,244
Mvav 68,180,208
L_ v7,24,s55,0
Mvav 68,184,228
S_ s55,s71,645120
Mvav 68,188,248
S_ m0,s51,3072
Mvav 72,180,212
Mvav 72,184,232
L_ v7,24,s55,0
Mvav 72,188,252
W_ 0
S_ s55,s71,655360
Mvav 92,220,252
S_ m0,s51,4096
Ta 180,v2,12288
Ta 182,v2,14336
Mvav 92,196,232
Mvav 92,192,212
s_nop 0
Ta 184,v2,16384
Ta 186,v2,18432
Mvav 88,220,248
L_ v7,24,s55,0
Mvav 88,196,228
S_ s55,s71,665600
Ta 188,v2,20480
Ta 190,v2,22528
Mvav 88,192,208
S_ m0,s51,5120
Mvav 84,220,244
Mvav 84,196,224
L_ v7,24,s55,0
Mvav 84,192,204
Mvav 80,220,240
Mvav 80,196,220
Mvav 80,192,200
buffer_load_dword a74, v12, s[44:47], s66 offen
Mvav 76,220,236
Mvav 76,196,216
Mvav 76,192,196
W_ 0
Mvav 16,180,196
s_nop 0
Ta 192,v3,12288
Ta 194,v3,14336
Mvav 16,184,216
buffer_load_dword a74, v4, s[24:27], s85 offen
Mvav 16,188,236
s_nop 0
Ta 196,v3,16384
Ta 198,v3,18432
Mvav 20,180,200
Mvav 20,184,220
s_nop 0
Ta 220,v3,20480
Ta 222,v3,22528
Mvav 20,188,240
Mvav 24,180,204
Mvav 24,184,224
Mvav 24,188,244
Mvav 28,180,208
Mvav 28,184,228
Mvav 28,188,248
Mvav 32,180,212
Mvav 32,184,232
Mvav 32,188,252
s_waitcnt vmcnt(26) lgkmcnt(0)
s_barrier
Mvav 52,220,252
S_ s55,s71,0
Ta 180,%0,49152
Ta 182,%0,51200
Mvav 52,196,232
S_ m0,s51,12288
Mvav 52,192,212
s_nop 0
Ta 184,%0,53248
Ta 186,%0,55296
Mvav 48,220,248
L_ v7,24,s55,0
Mvav 48,196,228
S_ s55,s71,10240
Ta 188,%0,57344
Ta 190,%0,59392
Mvav 48,192,208
S_ m0,s51,13312
Mvav 44,220,244
Mvav 44,196,224
L_ v7,24,s55,0
Mvav 44,192,204
S_ s55,s71,20480
Mvav 40,220,240
S_ m0,s51,14336
Mvav 40,196,220
Mvav 40,192,200
L_ v7,24,s55,0
Mvav 36,220,236
S_ s55,s71,30720
Mvav 36,196,216
S_ m0,s51,15360
Mvav 36,192,196
W_ 0
Mvaa 56,180,0
s_nop 0
Ta 192,v1,49152
Ta 194,v1,51200
Mvaa 56,184,40
L_ v7,24,s55,0
Mvaa 56,188,80
S_ s55,s71,40960
Ta 196,v1,53248
Ta 198,v1,55296
Mvaa 60,180,4
S_ m0,s51,16384
Mvaa 60,184,44
s_nop 0
Ta 220,v1,57344
Ta 222,v1,59392
Mvaa 60,188,84
L_ v7,24,s55,0
Mvaa 64,180,8
S_ s55,s71,51200
Mvaa 64,184,48
S_ m0,s51,17408
Mvaa 64,188,88
Mvaa 68,180,12
L_ v7,24,s55,0
Mvaa 68,184,52
Mvaa 68,188,240
Mvaa 72,180,16
Mvaa 72,184,56
Mvaa 72,188,244
W_ 0
Mvaa 92,220,244
s_nop 0
Ta 180,v2,49152
Ta 182,v2,51200
Mvaa 92,196,56
Mvaa 92,192,16
s_nop 0
Ta 184,v2,53248
Ta 186,v2,55296
Mvaa 88,220,240
Mvaa 88,196,52
s_nop 0
Ta 188,v2,57344
Ta 190,v2,59392
Mvaa 88,192,12
Mvaa 84,220,88
Mvaa 84,196,48
Mvaa 84,192,8
Mvaa 80,220,84
Mvaa 80,196,44
Mvaa 80,192,4
Mvaa 76,220,80
Mvaa 76,196,40
Mvaa 76,192,0
W_ 0
Mvaa 16,180,0
s_nop 0
Ta 192,v3,49152
Ta 194,v3,51200
Mvaa 16,184,40
Mvaa 16,188,80
s_nop 0
Ta 196,v3,53248
Ta 198,v3,55296
Mvaa 20,180,4
Mvaa 20,184,44
s_nop 0
Ta 220,v3,57344
Ta 222,v3,59392
Mvaa 20,188,84
Mvaa 24,180,8
Mvaa 24,184,48
Mvaa 24,188,88
Mvaa 28,180,12
Mvaa 28,184,52
Mvaa 28,188,240
Mvaa 32,180,16
Mvaa 32,184,56
Mvaa 32,188,244
s_waitcnt vmcnt(25) lgkmcnt(0)
s_barrier
Mvaa 52,220,244
s_nop 0
Ta 180,%0,36864
Ta 182,%0,38912
Mvaa 52,196,56
Mvaa 52,192,16
s_nop 0
Ta 184,%0,40960
Ta 186,%0,43008
Mvaa 48,220,240
Mvaa 48,196,52
s_nop 0
Ta 188,%0,45056
Ta 190,%0,47104
Mvaa 48,192,12
Mvaa 44,220,88
Mvaa 44,196,48
Mvaa 44,192,8
Mvaa 40,220,84
Mvaa 40,196,44
Mvaa 40,192,4
Mvaa 36,220,80
Mvaa 36,196,40
Mvaa 36,192,0
W_ 0
Mvaa 56,180,120
S_ s81,s6,3
Ta 192,v1,36864
Ta 194,v1,38912
Mvaa 56,184,160
s_and_b32 s81, s81, 7
Mvaa 56,188,200
s_mul_i32 s81, s81, 20480
Ta 196,v1,40960
Ta 198,v1,43008
Mvaa 60,180,124
s_mov_b32 m0, s52
S_ s73,s81,s78
Mvaa 60,184,164
S_ s73,s73,4096
Ta 220,v1,45056
Ta 222,v1,47104
Mvaa 60,188,204
s_nop 0
Y_ 56,0
Y_ 57,256
Mvaa 64,180,128
s_nop 0
Y_ 58,512
Y_ 59,768
Mvaa 64,184,168
Mvaa 64,188,208
s_nop 0
Y_ 60,1024
Y_ 61,1280
Mvaa 68,180,132
s_nop 0
Y_ 62,1536
Y_ 63,1792
Mvaa 68,184,172
Mvaa 68,188,212
s_nop 0
Y_ 64,2048
Y_ 65,2304
Mvaa 72,180,136
s_nop 0
Y_ 66,2560
Y_ 67,2816
Mvaa 72,184,176
Mvaa 72,188,216
W_ 12
Y_ 68,3072
Y_ 69,3328
Mvaa 92,220,216
s_nop 0
Ta 180,v2,36864
Ta 182,v2,38912
Mvaa 92,196,176
s_nop 0
Y_ 70,3584
Y_ 71,3840
Mvaa 92,192,136
s_nop 0
Ta 184,v2,40960
Ta 186,v2,43008
Mvaa 88,220,212
s_nop 0
Y_ 72,4096
Y_ 73,4352
Mvaa 88,196,172
S_ s83,s6,2
Ta 188,v2,45056
Ta 190,v2,47104
Mvaa 88,192,132
s_and_b32 s83, s83, 7
Y_ 74,4608
Y_ 75,4864
Mvaa 84,220,208
s_lshl_b32 s83, s83, 2
Y_ 92,9216
Y_ 93,9472
Mvaa 84,196,168
s_nop 0
Y_ 94,9728
Y_ 95,9984
Mvaa 84,192,128
s_nop 0
Y_ 88,8192
Y_ 89,8448
Mvaa 80,220,204
s_nop 0
Y_ 90,8704
Y_ 91,8960
Mvaa 80,196,164
v_cmp_eq_u32_e64 s[36:37], s83, v8
Mvaa 80,192,124
s_nop 0
Y_ 84,7168
Y_ 85,7424
Mvaa 76,220,200
s_nop 0
Y_ 86,7680
Y_ 87,7936
Mvaa 76,196,160
v_mov_b32 v10, 0x7fff0000
Mvaa 76,192,120
W_ 14
Y_ 80,6144
Y_ 81,6400
Mvaa 16,180,120
s_nop 0
Ta 192,v3,36864
Ta 194,v3,38912
Mvaa 16,184,160
s_nop 0
Y_ 82,6656
Y_ 83,6912
Mvaa 16,188,200
s_nop 0
Ta 196,v3,40960
Ta 198,v3,43008
Mvaa 20,180,124
s_nop 0
Y_ 76,5120
Y_ 77,5376
Mvaa 20,184,164
s_nop 0
Ta 220,v3,45056
Ta 222,v3,47104
Mvaa 20,188,204
s_nop 0
Y_ 78,5632
Y_ 79,5888
Mvaa 24,180,128
v_cndmask_b32_e64 v10, v10, v9, s[36:37]
Mvaa 24,184,168
Mvaa 24,188,208
s_waitcnt vmcnt(14) lgkmcnt(0)
s_barrier
Mvaa 28,180,132
buffer_store_dword a255, v10, s[92:95], 0 offen offset:384 sc1
Mvaa 28,184,172
s_sub_u32 s75, s6, 1
ds_read2st64_b32 v[96:97], v5 offset0:0 offset1:1
S_ s55,s71,122880
Mvaa 28,188,212
s_and_b32 s75, s75, 7
ds_read2st64_b32 v[98:99], v5 offset0:2 offset1:3
S_ m0,s51,49152
Mvaa 32,180,136
ds_read2st64_b32 v[104:105], v5 offset0:4 offset1:5
Mvaa 32,184,176
buffer_load_dword v11, v8, s[92:95], s96 offen offset:384 sc1
Mvaa 32,188,216
ds_read2st64_b32 v[106:107], v5 offset0:6 offset1:7
Mvaa 52,220,216
s_nop 0
Ta 180,%0,24576
Ta 182,%0,26624
Mvaa 52,196,176
ds_read2st64_b32 v[112:113], v5 offset0:8 offset1:9
Mvaa 52,192,136
W_ 5
Ta 184,%0,28672
Ta 186,%0,30720
Mvaa 48,220,212
L_ v7,24,s55,0
Mvaa 48,196,172
S_ s55,s71,133120
Ta 188,%0,32768
Ta 190,%0,34816
Mvaa 48,192,132
S_ m0,s51,50176
ds_read2st64_b32 v[114:115], v5 offset0:10 offset1:11
A_ v96,v16,v96
Mvaa 44,220,208
ds_read2st64_b32 v[120:121], v5 offset0:12 offset1:13
A_ v97,v17,v97
Mvaa 44,196,168
L_ v7,24,s55,0
Mvaa 44,192,128
S_ s55,s71,143360
ds_read2st64_b32 v[122:123], v5 offset0:14 offset1:15
A_ v98,v18,v98
Mvaa 40,220,204
S_ m0,s51,51200
ds_read2st64_b32 v[128:129], v5 offset0:16 offset1:17
A_ v99,v19,v99
Mvaa 40,196,164
W_ 11
ds_read2st64_b32 v[130:131], v5 offset0:18 offset1:19
A_ v104,v20,v104
Mvaa 40,192,124
L_ v7,24,s55,0
Mvaa 36,220,200
S_ s55,s71,153600
ds_read2st64_b32 v[132:133], v5 offset0:36 offset1:37
A_ v105,v21,v105
Mvaa 36,196,160
S_ m0,s51,52224
ds_read2st64_b32 v[134:135], v5 offset0:38 offset1:39
A_ v106,v22,v106
Mvaa 36,192,120
W_ 7
ds_read2st64_b32 v[124:125], v5 offset0:32 offset1:33
A_ v107,v23,v107
Zvav 56,180,136
W_ 7
Ta 192,v1,24576
Ta 194,v1,26624
Mvav 56,184,156
L_ v7,24,s55,0
Mvav 56,188,176
S_ s55,s71,163840
Ta 196,v1,28672
Ta 198,v1,30720
Zvav 60,180,140
S_ m0,s51,53248
ds_read2st64_b32 v[126:127], v5 offset0:34 offset1:35
A_ v112,v24,v112
Mvav 60,184,160
s_nop 0
Ta 220,v1,32768
Ta 222,v1,34816
Mvav 60,188,180
L_ v7,24,s55,0
Zvav 64,180,144
S_ s55,s71,174080
ds_read2st64_b32 v[116:117], v5 offset0:28 offset1:29
A_ v113,v25,v113
Mvav 64,184,164
S_ m0,s51,54272
ds_read2st64_b32 v[118:119], v5 offset0:30 offset1:31
A_ v114,v26,v114
Mvav 64,188,184
ds_read2st64_b32 v[108:109], v5 offset0:24 offset1:25
A_ v115,v27,v115
Zvav 68,180,148
L_ v7,24,s55,0
Mvav 68,184,168
W_ 14
ds_read2st64_b32 v[110:111], v5 offset0:26 offset1:27
A_ v120,v28,v120
Mvav 68,188,188
S_ s79,s6,5
ds_read2st64_b32 v[100:101], v5 offset0:20 offset1:21
A_ v121,v29,v121
Zvav 72,180,152
s_and_b32 s79, s79, 7
ds_read2st64_b32 v[102:103], v5 offset0:22 offset1:23
A_ v122,v30,v122
Mvav 72,184,172
s_lshl_b32 s79, s79, 7
A_ v123,v31,v123
Mvav 72,188,192
W_ 6
A_ v128,v32,v128
Zvav 92,220,192
S_ s71,s72,s79
Ta 180,v2,24576
Ta 182,v2,26624
Mvav 92,196,172
S_ s55,s71,491520
A_ v129,v33,v129
A_ v130,v34,v130
Mvav 92,192,152
S_ m0,s51,36864
Ta 184,v2,28672
Ta 186,v2,30720
Zvav 88,220,188
L_ v7,24,s55,0
Mvav 88,196,168
S_ s55,s71,501760
Ta 188,v2,32768
Ta 190,v2,34816
Mvav 88,192,148
S_ m0,s51,37888
A_ v131,v35,v131
S_ s85,s6,7
Zvav 84,220,184
A_ v132,v52,v132
A_ v133,v53,v133
Mvav 84,196,164
L_ v7,24,s55,0
Mvav 84,192,144
S_ s55,s71,512000
A_ v134,v54,v134
A_ v135,v55,v135
Zvav 80,220,180
A_ v124,v48,v124
A_ v125,v49,v125
Mvav 80,196,160
S_ m0,s51,38912
A_ v126,v50,v126
A_ v127,v51,v127
Mvav 80,192,140
L_ v7,24,s55,0
Zvav 76,220,176
W_ 10
A_ v116,v44,v116
A_ v117,v45,v117
Mvav 76,196,156
S_ s55,s71,522240
A_ v118,v46,v118
A_ v119,v47,v119
Mvav 76,192,136
W_ 0
A_ v108,v40,v108
Zvav 16,180,136
S_ m0,s51,39936
Ta 192,v3,24576
Ta 194,v3,26624
Mvav 16,184,156
L_ v7,24,s55,0
Mvav 16,188,176
S_ s55,s71,532480
Ta 196,v3,28672
Ta 198,v3,30720
Zvav 20,180,140
S_ m0,s51,40960
A_ v109,v41,v109
A_ v110,v42,v110
Mvav 20,184,160
)A"
R"A(s_and_b32 s85, s85, 7
Ta 220,v3,32768
Ta 222,v3,34816
Mvav 20,188,180
L_ v7,24,s55,0
Zvav 24,180,144
S_ s55,s71,542720
A_ v111,v43,v111
C_ 131,130,131
Mvav 24,184,164
A_ v100,v36,v100
A_ v101,v37,v101
Mvav 24,188,184
S_ m0,s51,41984
A_ v102,v38,v102
A_ v103,v39,v103
Zvav 28,180,148
L_ v7,24,s55,0
Mvav 28,184,168
s_lshl_b32 s85, s85, 7
C_ 130,128,129
C_ 132,132,133
Mvav 28,188,188
S_ s85,s85,s87
C_ 133,134,135
Zvav 32,180,152
Mvav 32,184,172
buffer_store_dwordx4 v[130:133], v6, s[28:31], s73 offen offset:0 sc1
Mvav 32,188,192
s_waitcnt vmcnt(23) lgkmcnt(0)
s_barrier
s_sub_u32 s73, s73, 4096
Zvav 52,220,192
S_ s55,s71,614400
Ta 180,%0,0
Ta 182,%0,2048
Mvav 52,196,172
S_ m0,s51,24576
C_ 123,122,123
C_ 122,120,121
Mvav 52,192,152
s_nop 0
Ta 184,%0,4096
Ta 186,%0,6144
Zvav 48,220,188
L_ v7,24,s55,0
Mvav 48,196,168
S_ s55,s71,624640
Ta 188,%0,8192
Ta 190,%0,10240
Mvav 48,192,148
S_ m0,s51,25600
C_ 124,124,125
C_ 125,126,127
Zvav 44,220,184
Mvav 44,196,164
buffer_store_dwordx4 v[122:125], v6, s[28:31], s73 offen offset:3072 sc1
Mvav 44,192,144
C_ 115,114,115
C_ 114,112,113
Zvav 40,220,180
C_ 116,116,117
C_ 117,118,119
Mvav 40,196,160
Mvav 40,192,140
buffer_store_dwordx4 v[114:117], v6, s[28:31], s73 offen offset:2048 sc1
Zvav 36,220,176
C_ 107,106,107
C_ 106,104,105
Mvav 36,196,156
C_ 108,108,109
C_ 109,110,111
Mvav 36,192,136
W_ 0
Mvav 56,180,196
s_nop 0
Ta 192,v1,0
Ta 194,v1,2048
Mvav 56,184,216
buffer_store_dwordx4 v[106:109], v6, s[28:31], s73 offen offset:1024 sc1
Mvav 56,188,236
s_nop 0
Ta 196,v1,4096
Ta 198,v1,6144
Mvav 60,180,200
C_ 99,98,99
C_ 98,96,97
Mvav 60,184,220
s_nop 0
Ta 220,v1,8192
Ta 222,v1,10240
Mvav 60,188,240
L_ v7,24,s55,0
Mvav 64,180,204
S_ s55,s71,634880
C_ 100,100,101
C_ 101,102,103
Mvav 64,184,224
S_ m0,s51,26624
Mvav 64,188,244
Mvav 68,180,208
buffer_store_dwordx4 v[98:101], v6, s[28:31], s73 offen offset:0 sc1
Mvav 68,184,228
Mvav 68,188,248
Mvav 72,180,212
Mvav 72,184,232
L_ v7,24,s55,0
Mvav 72,188,252
W_ 0
S_ s55,s71,645120
Mvav 92,220,252
S_ m0,s51,27648
Ta 180,v2,0
Ta 182,v2,2048
Mvav 92,196,232
Mvav 92,192,212
s_nop 0
Ta 184,v2,4096
Ta 186,v2,6144
Mvav 88,220,248
L_ v7,24,s55,0
Mvav 88,196,228
s_waitcnt vmcnt(21)
Ta 188,v2,8192
Ta 190,v2,10240
Mvav 88,192,208
S_ s55,s71,655360
v_cmp_eq_u32 vcc, s62, v11
Mvav 84,220,244
s_mul_i32 s76, s75, 204800
S_ m0,s51,28672
Mvav 84,196,224
L_ v7,24,s55,0
Mvav 84,192,204
s_bitcmp1_b32 vcc_lo, s75
s_cbranch_scc0 .Lspin0
.Lspun0:
S_ s55,s71,665600
Mvav 80,220,240
S_ s56,s76,s84
S_ m0,s51,29696
Mvav 80,196,220
Mvav 80,192,200
buffer_load_dwordx4 a[20:23], v6, s[28:31], s56 offen offset:0 sc1
Mvav 76,220,236
Mvav 76,196,216
Mvav 76,192,196
W_ 0
Mvav 16,180,196
s_nop 0
Ta 192,v3,0
Ta 194,v3,2048
Mvav 16,184,216
buffer_load_dwordx4 a[24:27], v6, s[28:31], s56 offen offset:1024 sc1
Mvav 16,188,236
s_nop 0
Ta 196,v3,4096
Ta 198,v3,6144
Mvav 20,180,200
Mvav 20,184,220
s_nop 0
Ta 220,v3,8192
Ta 222,v3,10240
Mvav 20,188,240
buffer_load_dwordx4 a[28:31], v6, s[28:31], s56 offen offset:2048 sc1
Mvav 24,180,204
Mvav 24,184,224
Mvav 24,188,244
Mvav 28,180,208
buffer_load_dwordx4 a[32:35], v6, s[28:31], s56 offen offset:3072 sc1
Mvav 28,184,228
S_ s56,s56,4096
Mvav 28,188,248
Mvav 32,180,212
Mvav 32,184,232
buffer_load_dwordx4 a[36:39], v6, s[28:31], s56 offen offset:0 sc1
Mvav 32,188,252
s_waitcnt vmcnt(29) lgkmcnt(0)
s_barrier
S_ s56,s76,s74
Mvav 52,220,252
s_nop 0
Ta 180,%0,12288
Ta 182,%0,14336
Mvav 52,196,232
Mvav 52,192,212
s_nop 0
Ta 184,%0,16384
Ta 186,%0,18432
Mvav 48,220,248
buffer_load_dwordx4 a[60:63], v6, s[28:31], s56 offen offset:0 sc1
Mvav 48,196,228
S_ s56,s76,s82
Ta 188,%0,20480
Ta 190,%0,22528
Mvav 48,192,208
Mvav 44,220,244
Mvav 44,196,224
buffer_load_dwordx4 a[64:67], v6, s[28:31], s56 offen offset:0 sc1
Mvav 44,192,204
Mvav 40,220,240
Mvav 40,196,220
Mvav 40,192,200
L_ v7,24,s55,0
Mvav 36,220,236
S_ s55,s71,0
Mvav 36,196,216
S_ m0,s51,0
Mvav 36,192,196
W_ 0
Mvaa 56,180,0
s_nop 0
Ta 192,v1,12288
Ta 194,v1,14336
Mvaa 56,184,40
L_ v7,24,s55,0
Mvaa 56,188,80
S_ s55,s71,10240
Ta 196,v1,16384
Ta 198,v1,18432
Mvaa 60,180,4
S_ m0,s51,1024
Mvaa 60,184,44
s_nop 0
Ta 220,v1,20480
Ta 222,v1,22528
Mvaa 60,188,84
L_ v7,24,s55,0
Mvaa 64,180,8
S_ s55,s71,20480
Mvaa 64,184,48
S_ m0,s51,2048
Mvaa 64,188,88
Mvaa 68,180,12
L_ v7,24,s55,0
Mvaa 68,184,52
S_ s55,s71,30720
Mvaa 68,188,240
S_ m0,s51,3072
Mvaa 72,180,16
Mvaa 72,184,56
L_ v7,24,s55,0
Mvaa 72,188,244
W_ 0
S_ s55,s71,40960
Mvaa 92,220,244
S_ m0,s51,4096
Ta 180,v2,12288
Ta 182,v2,14336
Mvaa 92,196,56
Mvaa 92,192,16
s_nop 0
Ta 184,v2,16384
Ta 186,v2,18432
Mvaa 88,220,240
L_ v7,24,s55,0
Mvaa 88,196,52
S_ s55,s71,51200
Ta 188,v2,20480
Ta 190,v2,22528
Mvaa 88,192,12
S_ m0,s51,5120
Mvaa 84,220,88
Mvaa 84,196,48
L_ v7,24,s55,0
Mvaa 84,192,8
Mvaa 80,220,84
Mvaa 80,196,44
Mvaa 80,192,4
buffer_load_dword a74, v4, s[24:27], s85 offen
Mvaa 76,220,80
Mvaa 76,196,40
Mvaa 76,192,0
W_ 0
Mvaa 16,180,0
s_nop 0
Ta 192,v3,12288
Ta 194,v3,14336
Mvaa 16,184,40
Mvaa 16,188,80
s_nop 0
Ta 196,v3,16384
Ta 198,v3,18432
Mvaa 20,180,4
Mvaa 20,184,44
s_nop 0
Ta 220,v3,20480
Ta 222,v3,22528
Mvaa 20,188,84
Mvaa 24,180,8
Mvaa 24,184,48
Mvaa 24,188,88
Mvaa 28,180,12
Mvaa 28,184,52
Mvaa 28,188,240
Mvaa 32,180,16
Mvaa 32,184,56
Mvaa 32,188,244
s_waitcnt vmcnt(31) lgkmcnt(0)
s_barrier
Mvaa 52,220,244
s_nop 0
Ta 180,%0,49152
Ta 182,%0,51200
Mvaa 52,196,56
Mvaa 52,192,16
s_nop 0
Ta 184,%0,53248
Ta 186,%0,55296
Mvaa 48,220,240
Mvaa 48,196,52
s_nop 0
Ta 188,%0,57344
Ta 190,%0,59392
Mvaa 48,192,12
Mvaa 44,220,88
Mvaa 44,196,48
Mvaa 44,192,8
Mvaa 40,220,84
Mvaa 40,196,44
Mvaa 40,192,4
Mvaa 36,220,80
Mvaa 36,196,40
Mvaa 36,192,0
W_ 0
Mvaa 56,180,120
S_ s81,s6,4
Ta 192,v1,49152
Ta 194,v1,51200
Mvaa 56,184,160
s_and_b32 s81, s81, 7
Mvaa 56,188,200
s_mul_i32 s81, s81, 20480
Ta 196,v1,53248
Ta 198,v1,55296
Mvaa 60,180,124
s_mov_b32 m0, s52
S_ s73,s81,s78
Mvaa 60,184,164
S_ s73,s73,4096
Ta 220,v1,57344
Ta 222,v1,59392
Mvaa 60,188,204
s_nop 0
Y_ 56,0
Y_ 57,256
Mvaa 64,180,128
s_nop 0
Y_ 58,512
Y_ 59,768
Mvaa 64,184,168
Mvaa 64,188,208
s_nop 0
Y_ 60,1024
Y_ 61,1280
Mvaa 68,180,132
s_nop 0
Y_ 62,1536
Y_ 63,1792
Mvaa 68,184,172
Mvaa 68,188,212
s_nop 0
Y_ 64,2048
Y_ 65,2304
Mvaa 72,180,136
s_nop 0
Y_ 66,2560
Y_ 67,2816
Mvaa 72,184,176
Mvaa 72,188,216
W_ 12
Y_ 68,3072
Y_ 69,3328
Mvaa 92,220,216
s_nop 0
Ta 180,v2,49152
Ta 182,v2,51200
Mvaa 92,196,176
s_nop 0
Y_ 70,3584
Y_ 71,3840
Mvaa 92,192,136
s_nop 0
Ta 184,v2,53248
Ta 186,v2,55296
Mvaa 88,220,212
s_nop 0
Y_ 72,4096
Y_ 73,4352
Mvaa 88,196,172
S_ s83,s6,3
Ta 188,v2,57344
Ta 190,v2,59392
Mvaa 88,192,132
s_and_b32 s83, s83, 7
Y_ 74,4608
Y_ 75,4864
Mvaa 84,220,208
s_lshl_b32 s83, s83, 2
Y_ 92,9216
Y_ 93,9472
Mvaa 84,196,168
s_nop 0
Y_ 94,9728
Y_ 95,9984
Mvaa 84,192,128
s_nop 0
Y_ 88,8192
Y_ 89,8448
Mvaa 80,220,204
s_nop 0
Y_ 90,8704
Y_ 91,8960
Mvaa 80,196,164
v_cmp_eq_u32_e64 s[36:37], s83, v8
Mvaa 80,192,124
s_nop 0
Y_ 84,7168
Y_ 85,7424
Mvaa 76,220,200
s_nop 0
Y_ 86,7680
Y_ 87,7936
Mvaa 76,196,160
v_mov_b32 v10, 0x7fff0000
Mvaa 76,192,120
W_ 14
Y_ 80,6144
Y_ 81,6400
Mvaa 16,180,120
s_nop 0
Ta 192,v3,49152
Ta 194,v3,51200
Mvaa 16,184,160
s_nop 0
Y_ 82,6656
Y_ 83,6912
Mvaa 16,188,200
s_nop 0
Ta 196,v3,53248
Ta 198,v3,55296
Mvaa 20,180,124
s_nop 0
Y_ 76,5120
Y_ 77,5376
Mvaa 20,184,164
s_nop 0
Ta 220,v3,57344
Ta 222,v3,59392
Mvaa 20,188,204
s_nop 0
Y_ 78,5632
Y_ 79,5888
Mvaa 24,180,128
v_cndmask_b32_e64 v10, v10, v9, s[36:37]
Mvaa 24,184,168
Mvaa 24,188,208
s_waitcnt vmcnt(18) lgkmcnt(0)
s_barrier
Mvaa 28,180,132
buffer_store_dword a255, v10, s[92:95], 0 offen offset:384 sc1
Mvaa 28,184,172
s_sub_u32 s75, s6, 2
ds_read2st64_b32 v[96:97], v5 offset0:0 offset1:1
S_ s55,s71,122880
Mvaa 28,188,212
s_and_b32 s75, s75, 7
ds_read2st64_b32 v[98:99], v5 offset0:2 offset1:3
S_ m0,s51,12288
Mvaa 32,180,136
ds_read2st64_b32 v[104:105], v5 offset0:4 offset1:5
Mvaa 32,184,176
buffer_load_dword v11, v8, s[92:95], s96 offen offset:384 sc1
Mvaa 32,188,216
ds_read2st64_b32 v[106:107], v5 offset0:6 offset1:7
Mvaa 52,220,216
s_nop 0
Ta 180,%0,36864
Ta 182,%0,38912
Mvaa 52,196,176
ds_read2st64_b32 v[112:113], v5 offset0:8 offset1:9
Mvaa 52,192,136
W_ 5
Ta 184,%0,40960
Ta 186,%0,43008
Mvaa 48,220,212
L_ v7,24,s55,0
Mvaa 48,196,172
S_ s55,s71,133120
Ta 188,%0,45056
Ta 190,%0,47104
Mvaa 48,192,132
S_ m0,s51,13312
ds_read2st64_b32 v[114:115], v5 offset0:10 offset1:11
A_ v96,v16,v96
Mvaa 44,220,208
ds_read2st64_b32 v[120:121], v5 offset0:12 offset1:13
A_ v97,v17,v97
Mvaa 44,196,168
L_ v7,24,s55,0
Mvaa 44,192,128
S_ s55,s71,143360
ds_read2st64_b32 v[122:123], v5 offset0:14 offset1:15
A_ v98,v18,v98
Mvaa 40,220,204
S_ m0,s51,14336
ds_read2st64_b32 v[128:129], v5 offset0:16 offset1:17
A_ v99,v19,v99
Mvaa 40,196,164
W_ 11
ds_read2st64_b32 v[130:131], v5 offset0:18 offset1:19
A_ v104,v20,v104
Mvaa 40,192,124
L_ v7,24,s55,0
Mvaa 36,220,200
S_ s55,s71,153600
ds_read2st64_b32 v[132:133], v5 offset0:36 offset1:37
A_ v105,v21,v105
Mvaa 36,196,160
S_ m0,s51,15360
ds_read2st64_b32 v[134:135], v5 offset0:38 offset1:39
A_ v106,v22,v106
Mvaa 36,192,120
ds_read2st64_b32 v[124:125], v5 offset0:32 offset1:33
A_ v107,v23,v107
s_waitcnt vmcnt(13)
Zaaa 100,70,20
L_ v7,24,s55,0
Zaaa 140,68,20
W_ 7
ds_read2st64_b32 v[126:127], v5 offset0:34 offset1:35
A_ v112,v24,v112
Zaaa 104,70,24
S_ s55,s71,163840
ds_read2st64_b32 v[116:117], v5 offset0:28 offset1:29
A_ v113,v25,v113
Zaaa 144,68,24
S_ m0,s51,16384
ds_read2st64_b32 v[118:119], v5 offset0:30 offset1:31
A_ v114,v26,v114
Zaaa 108,70,28
L_ v7,24,s55,0
Zaaa 148,68,28
S_ s55,s71,174080
ds_read2st64_b32 v[108:109], v5 offset0:24 offset1:25
A_ v115,v27,v115
Zaaa 112,70,32
W_ 9
ds_read2st64_b32 v[110:111], v5 offset0:26 offset1:27
A_ v120,v28,v120
Zaaa 152,68,32
S_ m0,s51,17408
ds_read2st64_b32 v[100:101], v5 offset0:20 offset1:21
A_ v121,v29,v121
Zaaa 116,70,36
L_ v7,24,s55,0
Zaaa 156,68,36
S_ s79,s6,6
ds_read2st64_b32 v[102:103], v5 offset0:22 offset1:23
A_ v122,v30,v122
Maaa 224,70,60
s_and_b32 s79, s79, 7
A_ v123,v31,v123
Maaa 228,68,60
W_ 10
A_ v128,v32,v128
A_ v129,v33,v129
Maaa 232,70,64
s_lshl_b32 s79, s79, 7
A_ v130,v34,v130
A_ v131,v35,v131
Maaa 236,68,64
W_ 9
A_ v132,v52,v132
W_ 8
Zvav 56,180,136
S_ s71,s72,s79
Ta 192,v1,36864
Ta 194,v1,38912
Mvav 56,184,156
S_ s55,s71,491520
A_ v133,v53,v133
A_ v134,v54,v134
Mvav 56,188,176
S_ m0,s51,49152
Ta 196,v1,40960
Ta 198,v1,43008
Zvav 60,180,140
L_ v7,24,s55,0
Mvav 60,184,160
S_ s55,s71,501760
Ta 220,v1,45056
Ta 222,v1,47104
Mvav 60,188,180
S_ m0,s51,50176
A_ v135,v55,v135
Zvav 64,180,144
W_ 12
A_ v124,v48,v124
A_ v125,v49,v125
Mvav 64,184,164
L_ v7,24,s55,0
Mvav 64,188,184
S_ s55,s71,512000
A_ v126,v50,v126
A_ v127,v51,v127
Zvav 68,180,148
W_ 10
A_ v116,v44,v116
A_ v117,v45,v117
Mvav 68,184,168
S_ m0,s51,51200
A_ v118,v46,v118
A_ v119,v47,v119
Mvav 68,188,188
L_ v7,24,s55,0
Zvav 72,180,152
W_ 8
A_ v108,v40,v108
A_ v109,v41,v109
Mvav 72,184,172
S_ s55,s71,522240
A_ v110,v42,v110
A_ v111,v43,v111
Mvav 72,188,192
W_ 0
A_ v100,v36,v100
Zvav 92,220,192
S_ m0,s51,52224
Ta 180,v2,36864
Ta 182,v2,38912
Mvav 92,196,172
L_ v7,24,s55,0
Mvav 92,192,152
S_ s55,s71,532480
Ta 184,v2,40960
Ta 186,v2,43008
Zvav 88,220,188
S_ m0,s51,53248
A_ v101,v37,v101
A_ v102,v38,v102
Mvav 88,196,168
s_nop 0
Ta 188,v2,45056
Ta 190,v2,47104
Mvav 88,192,148
L_ v7,24,s55,0
Zvav 84,220,184
S_ s55,s71,542720
A_ v103,v39,v103
C_ 131,130,131
Mvav 84,196,164
S_ m0,s51,54272
C_ 130,128,129
C_ 132,132,133
Mvav 84,192,144
S_ s85,s6,8
C_ 133,134,135
Zvav 80,220,180
buffer_store_dwordx4 v[130:133], v6, s[28:31], s73 offen offset:0 sc1
Mvav 80,196,160
s_sub_u32 s73, s73, 4096
s_and_b32 s85, s85, 7
Mvav 80,192,140
s_lshl_b32 s85, s85, 7
C_ 123,122,123
C_ 122,120,121
Zvav 76,220,176
S_ s85,s85,s87
C_ 124,124,125
C_ 125,126,127
Mvav 76,196,156
buffer_store_dwordx4 v[122:125], v6, s[28:31], s73 offen offset:3072 sc1
Mvav 76,192,136
W_ 0
C_ 115,114,115
C_ 114,112,113
Zvav 16,180,136
s_nop 0
Ta 192,v3,36864
Ta 194,v3,38912
Mvav 16,184,156
C_ 116,116,117
C_ 117,118,119
Mvav 16,188,176
s_nop 0
Ta 196,v3,40960
Ta 198,v3,43008
Zvav 20,180,140
buffer_store_dwordx4 v[114:117], v6, s[28:31], s73 offen offset:2048 sc1
Mvav 20,184,160
s_nop 0
Ta 220,v3,45056
Ta 222,v3,47104
Mvav 20,188,180
C_ 107,106,107
C_ 106,104,105
Zvav 24,180,144
C_ 108,108,109
C_ 109,110,111
Mvav 24,184,164
buffer_store_dwordx4 v[106:109], v6, s[28:31], s73 offen offset:1024 sc1
Mvav 24,188,184
C_ 99,98,99
C_ 98,96,97
Zvav 28,180,148
C_ 100,100,101
C_ 101,102,103
Mvav 28,184,168
Mvav 28,188,188
buffer_store_dwordx4 v[98:101], v6, s[28:31], s73 offen offset:0 sc1
Zvav 32,180,152
Mvav 32,184,172
Mvav 32,188,192
s_waitcnt vmcnt(25) lgkmcnt(0)
s_barrier
Zvav 52,220,192
s_nop 0
Ta 180,%0,24576
Ta 182,%0,26624
Mvav 52,196,172
L_ v7,24,s55,0
Mvav 52,192,152
S_ s55,s71,614400
Ta 184,%0,28672
Ta 186,%0,30720
Zvav 48,220,188
S_ m0,s51,36864
Mvav 48,196,168
s_nop 0
Ta 188,%0,32768
Ta 190,%0,34816
Mvav 48,192,148
L_ v7,24,s55,0
Zvav 44,220,184
S_ s55,s71,624640
Mvav 44,196,164
S_ m0,s51,37888
Mvav 44,192,144
Zvav 40,220,180
L_ v7,24,s55,0
Mvav 40,196,160
S_ s55,s71,634880
Mvav 40,192,140
S_ m0,s51,38912
Zvav 36,220,176
Mvav 36,196,156
L_ v7,24,s55,0
Mvav 36,192,136
W_ 0
S_ s55,s71,645120
Mvav 56,180,196
S_ m0,s51,39936
Ta 192,v1,24576
Ta 194,v1,26624
Mvav 56,184,216
Mvav 56,188,236
s_nop 0
Ta 196,v1,28672
Ta 198,v1,30720
Mvav 60,180,200
L_ v7,24,s55,0
Mvav 60,184,220
S_ s55,s71,655360
Ta 220,v1,32768
Ta 222,v1,34816
Mvav 60,188,240
S_ m0,s51,40960
Mvav 64,180,204
Mvav 64,184,224
L_ v7,24,s55,0
Mvav 64,188,244
S_ s55,s71,665600
Mvav 68,180,208
S_ m0,s51,41984
Mvav 68,184,228
Mvav 68,188,248
L_ v7,24,s55,0
Mvav 72,180,212
Mvav 72,184,232
Mvav 72,188,252
W_ 0
Mvav 92,220,252
s_nop 0
Ta 180,v2,24576
Ta 182,v2,26624
Mvav 92,196,232
buffer_load_dword a74, v4, s[24:27], s85 offen
Mvav 92,192,212
s_nop 0
Ta 184,v2,28672
Ta 186,v2,30720
Mvav 88,220,248
s_waitcnt vmcnt(24)
Mvav 88,196,228
s_nop 0
Ta 188,v2,32768
Ta 190,v2,34816
Mvav 88,192,208
v_cmp_eq_u32 vcc, s62, v11
Mvav 84,220,244
s_mul_i32 s76, s75, 204800
Mvav 84,196,224
s_bitcmp1_b32 vcc_lo, s75
s_cbranch_scc0 .Lspin1
.Lspun1:
Mvav 84,192,204
S_ s56,s76,s84
Mvav 80,220,240
buffer_load_dwordx4 a[20:23], v6, s[28:31], s56 offen offset:0 sc1
Mvav 80,196,220
Mvav 80,192,200
Mvav 76,220,236
Mvav 76,196,216
buffer_load_dwordx4 a[24:27], v6, s[28:31], s56 offen offset:1024 sc1
Mvav 76,192,196
W_ 0
Mvav 16,180,196
s_nop 0
Ta 192,v3,24576
Ta 194,v3,26624
Mvav 16,184,216
Mvav 16,188,236
s_nop 0
Ta 196,v3,28672
Ta 198,v3,30720
Mvav 20,180,200
buffer_load_dwordx4 a[28:31], v6, s[28:31], s56 offen offset:2048 sc1
Mvav 20,184,220
s_nop 0
Ta 220,v3,32768
Ta 222,v3,34816
Mvav 20,188,240
Mvav 24,180,204
Mvav 24,184,224
buffer_load_dwordx4 a[32:35], v6, s[28:31], s56 offen offset:3072 sc1
Mvav 24,188,244
S_ s56,s56,4096
Mvav 28,180,208
Mvav 28,184,228
Mvav 28,188,248
buffer_load_dwordx4 a[36:39], v6, s[28:31], s56 offen offset:0 sc1
Mvav 32,180,212
S_ s56,s76,s74
Mvav 32,184,232
Mvav 32,188,252
s_waitcnt vmcnt(32) lgkmcnt(0)
s_barrier
Mvav 52,220,252
S_ s55,s71,0
Ta 180,%0,0
Ta 182,%0,2048
Mvav 52,196,232
buffer_load_dwordx4 a[60:63], v6, s[28:31], s56 offen offset:0 sc1
Mvav 52,192,212
S_ s56,s76,s82
Ta 184,%0,4096
Ta 186,%0,6144
Mvav 48,220,248
S_ m0,s51,24576
Mvav 48,196,228
s_nop 0
Ta 188,%0,8192
Ta 190,%0,10240
Mvav 48,192,208
buffer_load_dwordx4 a[64:67], v6, s[28:31], s56 offen offset:0 sc1
Mvav 44,220,244
Mvav 44,196,224
Mvav 44,192,204
Mvav 40,220,240
L_ v7,24,s55,0
Mvav 40,196,220
S_ s55,s71,10240
Mvav 40,192,200
S_ m0,s51,25600
Mvav 36,220,236
Mvav 36,196,216
L_ v7,24,s55,0
Mvav 36,192,196
W_ 0
S_ s55,s71,20480
Mvaa 56,180,0
S_ m0,s51,26624
Ta 192,v1,0
Ta 194,v1,2048
Mvaa 56,184,40
Mvaa 56,188,80
s_nop 0
Ta 196,v1,4096
Ta 198,v1,6144
Mvaa 60,180,4
L_ v7,24,s55,0
Mvaa 60,184,44
S_ s55,s71,30720
Ta 220,v1,8192
Ta 222,v1,10240
Mvaa 60,188,84
S_ m0,s51,27648
Mvaa 64,180,8
Mvaa 64,184,48
L_ v7,24,s55,0
Mvaa 64,188,88
S_ s55,s71,40960
Mvaa 68,180,12
S_ m0,s51,28672
Mvaa 68,184,52
Mvaa 68,188,240
L_ v7,24,s55,0
Mvaa 72,180,16
S_ s55,s71,51200
Mvaa 72,184,56
S_ m0,s51,29696
Mvaa 72,188,244
W_ 0
Mvaa 92,220,244
s_nop 0
Ta 180,v2,0
Ta 182,v2,2048
Mvaa 92,196,56
L_ v7,24,s55,0
Mvaa 92,192,16
s_nop 0
Ta 184,v2,4096
Ta 186,v2,6144
Mvaa 88,220,240
Mvaa 88,196,52
s_nop 0
Ta 188,v2,8192
Ta 190,v2,10240
Mvaa 88,192,12
Mvaa 84,220,88
Mvaa 84,196,48
Mvaa 84,192,8
Mvaa 80,220,84
Mvaa 80,196,44
Mvaa 80,192,4
Mvaa 76,220,80
Mvaa 76,196,40
Mvaa 76,192,0
W_ 0
Mvaa 16,180,0
s_nop 0
Ta 192,v3,0
Ta 194,v3,2048
Mvaa 16,184,40
Mvaa 16,188,80
s_nop 0
Ta 196,v3,4096
Ta 198,v3,6144
Mvaa 20,180,4
Mvaa 20,184,44
s_nop 0
Ta 220,v3,8192
Ta 222,v3,10240
Mvaa 20,188,84
Mvaa 24,180,8
Mvaa 24,184,48
Mvaa 24,188,88
Mvaa 28,180,12
Mvaa 28,184,52
Mvaa 28,188,240
Mvaa 32,180,16
Mvaa 32,184,56
Mvaa 32,188,244
s_waitcnt vmcnt(31) lgkmcnt(0)
s_barrier
Mvaa 52,220,244
s_nop 0
Ta 180,%0,12288
Ta 182,%0,14336
Mvaa 52,196,56
Mvaa 52,192,16
s_nop 0
Ta 184,%0,16384
Ta 186,%0,18432
Mvaa 48,220,240
Mvaa 48,196,52
s_nop 0
Ta 188,%0,20480
Ta 190,%0,22528
Mvaa 48,192,12
Mvaa 44,220,88
Mvaa 44,196,48
Mvaa 44,192,8
Mvaa 40,220,84
Mvaa 40,196,44
Mvaa 40,192,4
Mvaa 36,220,80
Mvaa 36,196,40
Mvaa 36,192,0
W_ 0
Mvaa 56,180,120
S_ s81,s6,5
Ta 192,v1,12288
Ta 194,v1,14336
Mvaa 56,184,160
s_and_b32 s81, s81, 7
Mvaa 56,188,200
s_mul_i32 s81, s81, 20480
Ta 196,v1,16384
Ta 198,v1,18432
Mvaa 60,180,124
s_mov_b32 m0, s52
S_ s73,s81,s78
Mvaa 60,184,164
S_ s73,s73,4096
Ta 220,v1,20480
Ta 222,v1,22528
Mvaa 60,188,204
s_nop 0
Y_ 56,0
Y_ 57,256
Mvaa 64,180,128
s_nop 0
Y_ 58,512
Y_ 59,768
Mvaa 64,184,168
Mvaa 64,188,208
s_nop 0
Y_ 60,1024
Y_ 61,1280
Mvaa 68,180,132
s_nop 0
Y_ 62,1536
Y_ 63,1792
Mvaa 68,184,172
Mvaa 68,188,212
s_nop 0
Y_ 64,2048
Y_ 65,2304
Mvaa 72,180,136
s_nop 0
Y_ 66,2560
Y_ 67,2816
Mvaa 72,184,176
Mvaa 72,188,216
W_ 12
Y_ 68,3072
Y_ 69,3328
Mvaa 92,220,216
s_nop 0
Ta 180,v2,12288
Ta 182,v2,14336
Mvaa 92,196,176
s_nop 0
Y_ 70,3584
Y_ 71,3840
Mvaa 92,192,136
s_nop 0
Ta 184,v2,16384
Ta 186,v2,18432
Mvaa 88,220,212
s_nop 0
Y_ 72,4096
Y_ 73,4352
Mvaa 88,196,172
S_ s83,s6,4
Ta 188,v2,20480
Ta 190,v2,22528
Mvaa 88,192,132
s_and_b32 s83, s83, 7
Y_ 74,4608
Y_ 75,4864
Mvaa 84,220,208
s_lshl_b32 s83, s83, 2
Y_ 92,9216
Y_ 93,9472
Mvaa 84,196,168
s_nop 0
Y_ 94,9728
Y_ 95,9984
Mvaa 84,192,128
s_nop 0
Y_ 88,8192
Y_ 89,8448
Mvaa 80,220,204
s_nop 0
Y_ 90,8704
Y_ 91,8960
Mvaa 80,196,164
v_cmp_eq_u32_e64 s[36:37], s83, v8
Mvaa 80,192,124
s_nop 0
Y_ 84,7168
Y_ 85,7424
Mvaa 76,220,200
s_nop 0
Y_ 86,7680
Y_ 87,7936
Mvaa 76,196,160
v_mov_b32 v10, 0x7fff0000
Mvaa 76,192,120
W_ 14
Y_ 80,6144
Y_ 81,6400
Mvaa 16,180,120
s_nop 0
Ta 192,v3,12288
Ta 194,v3,14336
Mvaa 16,184,160
s_nop 0
Y_ 82,6656
Y_ 83,6912
Mvaa 16,188,200
s_nop 0
Ta 196,v3,16384
Ta 198,v3,18432
Mvaa 20,180,124
s_nop 0
Y_ 76,5120
Y_ 77,5376
Mvaa 20,184,164
s_nop 0
Ta 220,v3,20480
Ta 222,v3,22528
Mvaa 20,188,204
s_nop 0
Y_ 78,5632
Y_ 79,5888
Mvaa 24,180,128
v_cndmask_b32_e64 v10, v10, v9, s[36:37]
Mvaa 24,184,168
Mvaa 24,188,208
s_waitcnt vmcnt(20) lgkmcnt(0)
s_barrier
Mvaa 28,180,132
buffer_store_dword a255, v10, s[92:95], 0 offen offset:384 sc1
Mvaa 28,184,172
s_sub_u32 s75, s6, 3
ds_read2st64_b32 v[96:97], v5 offset0:0 offset1:1
S_ s55,s71,122880
Mvaa 28,188,212
s_and_b32 s75, s75, 7
ds_read2st64_b32 v[98:99], v5 offset0:2 offset1:3
S_ m0,s51,0
Mvaa 32,180,136
ds_read2st64_b32 v[104:105], v5 offset0:4 offset1:5
Mvaa 32,184,176
buffer_load_dword v11, v8, s[92:95], s96 offen offset:384 sc1
Mvaa 32,188,216
ds_read2st64_b32 v[106:107], v5 offset0:6 offset1:7
Mvaa 52,220,216
s_nop 0
Ta 180,%0,49152
Ta 182,%0,51200
Mvaa 52,196,176
ds_read2st64_b32 v[112:113], v5 offset0:8 offset1:9
Mvaa 52,192,136
W_ 5
Ta 184,%0,53248
Ta 186,%0,55296
Mvaa 48,220,212
L_ v7,24,s55,0
Mvaa 48,196,172
S_ s55,s71,133120
Ta 188,%0,57344
Ta 190,%0,59392
Mvaa 48,192,132
S_ m0,s51,1024
ds_read2st64_b32 v[114:115], v5 offset0:10 offset1:11
A_ v96,v16,v96
Mvaa 44,220,208
ds_read2st64_b32 v[120:121], v5 offset0:12 offset1:13
A_ v97,v17,v97
Mvaa 44,196,168
L_ v7,24,s55,0
Mvaa 44,192,128
S_ s55,s71,143360
ds_read2st64_b32 v[122:123], v5 offset0:14 offset1:15
A_ v98,v18,v98
Mvaa 40,220,204
S_ m0,s51,2048
ds_read2st64_b32 v[128:129], v5 offset0:16 offset1:17
A_ v99,v19,v99
Mvaa 40,196,164
W_ 11
ds_read2st64_b32 v[130:131], v5 offset0:18 offset1:19
A_ v104,v20,v104
Mvaa 40,192,124
L_ v7,24,s55,0
Mvaa 36,220,200
S_ s55,s71,153600
ds_read2st64_b32 v[132:133], v5 offset0:36 offset1:37
A_ v105,v21,v105
Mvaa 36,196,160
S_ m0,s51,3072
ds_read2st64_b32 v[134:135], v5 offset0:38 offset1:39
A_ v106,v22,v106
Mvaa 36,192,120
ds_read2st64_b32 v[124:125], v5 offset0:32 offset1:33
A_ v107,v23,v107
s_waitcnt vmcnt(11)
Maaa 100,70,20
L_ v7,24,s55,0
Maaa 140,68,20
W_ 7
ds_read2st64_b32 v[126:127], v5 offset0:34 offset1:35
A_ v112,v24,v112
Maaa 104,70,24
S_ s55,s71,163840
ds_read2st64_b32 v[116:117], v5 offset0:28 offset1:29
A_ v113,v25,v113
Maaa 144,68,24
S_ m0,s51,4096
ds_read2st64_b32 v[118:119], v5 offset0:30 offset1:31
A_ v114,v26,v114
Maaa 108,70,28
L_ v7,24,s55,0
Maaa 148,68,28
S_ s55,s71,174080
ds_read2st64_b32 v[108:109], v5 offset0:24 offset1:25
A_ v115,v27,v115
Maaa 112,70,32
W_ 9
ds_read2st64_b32 v[110:111], v5 offset0:26 offset1:27
A_ v120,v28,v120
Maaa 152,68,32
S_ m0,s51,5120
ds_read2st64_b32 v[100:101], v5 offset0:20 offset1:21
A_ v121,v29,v121
Maaa 116,70,36
L_ v7,24,s55,0
Maaa 156,68,36
S_ s79,s6,7
ds_read2st64_b32 v[102:103], v5 offset0:22 offset1:23
A_ v122,v30,v122
Maaa 224,70,60
s_and_b32 s79, s79, 7
A_ v123,v31,v123
Maaa 228,68,60
W_ 10
A_ v128,v32,v128
A_ v129,v33,v129
Maaa 232,70,64
s_lshl_b32 s79, s79, 7
A_ v130,v34,v130
A_ v131,v35,v131
Maaa 236,68,64
W_ 9
A_ v132,v52,v132
W_ 8
Zvav 56,180,136
S_ s71,s72,s79
Ta 192,v1,49152
Ta 194,v1,51200
Mvav 56,184,156
S_ s55,s71,491520
A_ v133,v53,v133
A_ v134,v54,v134
Mvav 56,188,176
S_ m0,s51,12288
Ta 196,v1,53248
Ta 198,v1,55296
Zvav 60,180,140
L_ v7,24,s55,0
Mvav 60,184,160
S_ s55,s71,501760
Ta 220,v1,57344
Ta 222,v1,59392
Mvav 60,188,180
S_ m0,s51,13312
A_ v135,v55,v135
Zvav 64,180,144
W_ 12
A_ v124,v48,v124
A_ v125,v49,v125
Mvav 64,184,164
L_ v7,24,s55,0
Mvav 64,188,184
S_ s55,s71,512000
A_ v126,v50,v126
A_ v127,v51,v127
Zvav 68,180,148
W_ 10
A_ v116,v44,v116
A_ v117,v45,v117
Mvav 68,184,168
S_ m0,s51,14336
A_ v118,v46,v118
A_ v119,v47,v119
Mvav 68,188,188
L_ v7,24,s55,0
Zvav 72,180,152
W_ 8
A_ v108,v40,v108
A_ v109,v41,v109
Mvav 72,184,172
S_ s55,s71,522240
A_ v110,v42,v110
A_ v111,v43,v111
Mvav 72,188,192
W_ 0
A_ v100,v36,v100
Zvav 92,220,192
S_ m0,s51,15360
Ta 180,v2,49152
Ta 182,v2,51200
Mvav 92,196,172
L_ v7,24,s55,0
Mvav 92,192,152
S_ s55,s71,532480
Ta 184,v2,53248
Ta 186,v2,55296
Zvav 88,220,188
S_ m0,s51,16384
A_ v101,v37,v101
A_ v102,v38,v102
Mvav 88,196,168
s_nop 0
Ta 188,v2,57344
Ta 190,v2,59392
Mvav 88,192,148
L_ v7,24,s55,0
Zvav 84,220,184
S_ s55,s71,542720
A_ v103,v39,v103
C_ 131,130,131
Mvav 84,196,164
S_ m0,s51,17408
C_ 130,128,129
C_ 132,132,133
Mvav 84,192,144
C_ 133,134,135
Zvav 80,220,180
buffer_store_dwordx4 v[130:133], v6, s[28:31], s73 offen offset:0 sc1
Mvav 80,196,160
s_sub_u32 s73, s73, 4096
Mvav 80,192,140
C_ 123,122,123
C_ 122,120,121
Zvav 76,220,176
C_ 124,124,125
C_ 125,126,127
Mvav 76,196,156
buffer_store_dwordx4 v[122:125], v6, s[28:31], s73 offen offset:3072 sc1
Mvav 76,192,136
W_ 0
C_ 115,114,115
C_ 114,112,113
Zvav 16,180,136
s_nop 0
Ta 192,v3,49152
Ta 194,v3,51200
Mvav 16,184,156
C_ 116,116,117
C_ 117,118,119
Mvav 16,188,176
s_nop 0
Ta 196,v3,53248
Ta 198,v3,55296
Zvav 20,180,140
buffer_store_dwordx4 v[114:117], v6, s[28:31], s73 offen offset:2048 sc1
Mvav 20,184,160
s_nop 0
Ta 220,v3,57344
Ta 222,v3,59392
Mvav 20,188,180
C_ 107,106,107
C_ 106,104,105
Zvav 24,180,144
C_ 108,108,109
C_ 109,110,111
Mvav 24,184,164
buffer_store_dwordx4 v[106:109], v6, s[28:31], s73 offen offset:1024 sc1
Mvav 24,188,184
C_ 99,98,99
C_ 98,96,97
Zvav 28,180,148
C_ 100,100,101
C_ 101,102,103
Mvav 28,184,168
Mvav 28,188,188
buffer_store_dwordx4 v[98:101], v6, s[28:31], s73 offen offset:0 sc1
Zvav 32,180,152
Mvav 32,184,172
Mvav 32,188,192
s_waitcnt vmcnt(32) lgkmcnt(0)
s_barrier
Zvav 52,220,192
s_nop 0
Ta 180,%0,36864
Ta 182,%0,38912
Mvav 52,196,172
L_ v7,24,s55,0
Mvav 52,192,152
S_ s55,s71,614400
Ta 184,%0,40960
Ta 186,%0,43008
Zvav 48,220,188
S_ m0,s51,49152
Mvav 48,196,168
s_nop 0
Ta 188,%0,45056
Ta 190,%0,47104
Mvav 48,192,148
L_ v7,24,s55,0
Zvav 44,220,184
S_ s55,s71,624640
Mvav 44,196,164
S_ m0,s51,50176
Mvav 44,192,144
Zvav 40,220,180
L_ v7,24,s55,0
Mvav 40,196,160
S_ s55,s71,634880
Mvav 40,192,140
S_ m0,s51,51200
Zvav 36,220,176
Mvav 36,196,156
L_ v7,24,s55,0
Mvav 36,192,136
W_ 0
S_ s55,s71,645120
Mvav 56,180,196
S_ m0,s51,52224
Ta 192,v1,36864
Ta 194,v1,38912
Mvav 56,184,216
Mvav 56,188,236
s_nop 0
Ta 196,v1,40960
Ta 198,v1,43008
Mvav 60,180,200
L_ v7,24,s55,0
Mvav 60,184,220
S_ s55,s71,655360
Ta 220,v1,45056
Ta 222,v1,47104
Mvav 60,188,240
S_ m0,s51,53248
Mvav 64,180,204
Mvav 64,184,224
L_ v7,24,s55,0
Mvav 64,188,244
S_ s55,s71,665600
Mvav 68,180,208
S_ m0,s51,54272
Mvav 68,184,228
Mvav 68,188,248
L_ v7,24,s55,0
Mvav 72,180,212
Mvav 72,184,232
Mvav 72,188,252
W_ 0
Mvav 92,220,252
s_nop 0
Ta 180,v2,36864
Ta 182,v2,38912
Mvav 92,196,232
Mvav 92,192,212
s_nop 0
Ta 184,v2,40960
Ta 186,v2,43008
Mvav 88,220,248
s_waitcnt vmcnt(23)
Mvav 88,196,228
s_nop 0
Ta 188,v2,45056
Ta 190,v2,47104
Mvav 88,192,208
v_cmp_eq_u32 vcc, s62, v11
Mvav 84,220,244
s_mul_i32 s76, s75, 204800
Mvav 84,196,224
s_bitcmp1_b32 vcc_lo, s75
s_cbranch_scc0 .Lspin2
.Lspun2:
Mvav 84,192,204
S_ s56,s76,s84
Mvav 80,220,240
buffer_load_dwordx4 a[20:23], v6, s[28:31], s56 offen offset:0 sc1
Mvav 80,196,220
Mvav 80,192,200
Mvav 76,220,236
Mvav 76,196,216
buffer_load_dwordx4 a[24:27], v6, s[28:31], s56 offen offset:1024 sc1
Mvav 76,192,196
W_ 0
Mvav 16,180,196
s_nop 0
Ta 192,v3,36864
Ta 194,v3,38912
Mvav 16,184,216
Mvav 16,188,236
s_nop 0
Ta 196,v3,40960
Ta 198,v3,43008
Mvav 20,180,200
buffer_load_dwordx4 a[28:31], v6, s[28:31], s56 offen offset:2048 sc1
Mvav 20,184,220
s_nop 0
Ta 220,v3,45056
Ta 222,v3,47104
Mvav 20,188,240
Mvav 24,180,204
Mvav 24,184,224
buffer_load_dwordx4 a[32:35], v6, s[28:31], s56 offen offset:3072 sc1
Mvav 24,188,244
S_ s56,s56,4096
Mvav 28,180,208
Mvav 28,184,228
Mvav 28,188,248
buffer_load_dwordx4 a[36:39], v6, s[28:31], s56 offen offset:0 sc1
Mvav 32,180,212
S_ s56,s76,s74
Mvav 32,184,232
Mvav 32,188,252
s_waitcnt vmcnt(30) lgkmcnt(0)
s_barrier
Mvav 52,220,252
S_ s55,s71,0
Ta 180,%0,24576
Ta 182,%0,26624
Mvav 52,196,232
buffer_load_dwordx4 a[60:63], v6, s[28:31], s56 offen offset:0 sc1
Mvav 52,192,212
S_ s56,s76,s82
Ta 184,%0,28672
Ta 186,%0,30720
Mvav 48,220,248
S_ m0,s51,36864
Mvav 48,196,228
s_nop 0
Ta 188,%0,32768
Ta 190,%0,34816
Mvav 48,192,208
buffer_load_dwordx4 a[64:67], v6, s[28:31], s56 offen offset:0 sc1
Mvav 44,220,244
Mvav 44,196,224
Mvav 44,192,204
Mvav 40,220,240
L_ v7,24,s55,0
Mvav 40,196,220
S_ s55,s71,10240
Mvav 40,192,200
S_ m0,s51,37888
Mvav 36,220,236
Mvav 36,196,216
L_ v7,24,s55,0
Mvav 36,192,196
W_ 0
S_ s55,s71,20480
Mvaa 56,180,0
S_ m0,s51,38912
Ta 192,v1,24576
Ta 194,v1,26624
Mvaa 56,184,40
Mvaa 56,188,80
s_nop 0
Ta 196,v1,28672
Ta 198,v1,30720
Mvaa 60,180,4
L_ v7,24,s55,0
Mvaa 60,184,44
S_ s55,s71,30720
Ta 220,v1,32768
Ta 222,v1,34816
Mvaa 60,188,84
S_ m0,s51,39936
Mvaa 64,180,8
Mvaa 64,184,48
L_ v7,24,s55,0
Mvaa 64,188,88
S_ s55,s71,40960
Mvaa 68,180,12
S_ m0,s51,40960
Mvaa 68,184,52
Mvaa 68,188,240
L_ v7,24,s55,0
Mvaa 72,180,16
S_ s55,s71,51200
Mvaa 72,184,56
S_ m0,s51,41984
Mvaa 72,188,244
W_ 0
Mvaa 92,220,244
s_nop 0
Ta 180,v2,24576
Ta 182,v2,26624
Mvaa 92,196,56
L_ v7,24,s55,0
Mvaa 92,192,16
s_nop 0
Ta 184,v2,28672
Ta 186,v2,30720
Mvaa 88,220,240
Mvaa 88,196,52
s_nop 0
Ta 188,v2,32768
Ta 190,v2,34816
Mvaa 88,192,12
Mvaa 84,220,88
Mvaa 84,196,48
Mvaa 84,192,8
Mvaa 80,220,84
Mvaa 80,196,44
Mvaa 80,192,4
Mvaa 76,220,80
Mvaa 76,196,40
Mvaa 76,192,0
W_ 0
Mvaa 16,180,0
s_nop 0
Ta 192,v3,24576
Ta 194,v3,26624
Mvaa 16,184,40
Mvaa 16,188,80
s_nop 0
Ta 196,v3,28672
Ta 198,v3,30720
Mvaa 20,180,4
Mvaa 20,184,44
s_nop 0
Ta 220,v3,32768
Ta 222,v3,34816
Mvaa 20,188,84
Mvaa 24,180,8
Mvaa 24,184,48
Mvaa 24,188,88
Mvaa 28,180,12
Mvaa 28,184,52
Mvaa 28,188,240
Mvaa 32,180,16
Mvaa 32,184,56
Mvaa 32,188,244
s_waitcnt vmcnt(30) lgkmcnt(0)
s_barrier
Mvaa 52,220,244
s_nop 0
Ta 180,%0,0
Ta 182,%0,2048
Mvaa 52,196,56
Mvaa 52,192,16
s_nop 0
Ta 184,%0,4096
Ta 186,%0,6144
Mvaa 48,220,240
Mvaa 48,196,52
s_nop 0
Ta 188,%0,8192
Ta 190,%0,10240
Mvaa 48,192,12
Mvaa 44,220,88
Mvaa 44,196,48
Mvaa 44,192,8
Mvaa 40,220,84
Mvaa 40,196,44
Mvaa 40,192,4
Mvaa 36,220,80
Mvaa 36,196,40
Mvaa 36,192,0
W_ 0
Mvaa 56,180,120
S_ s81,s6,6
Ta 192,v1,0
Ta 194,v1,2048
Mvaa 56,184,160
s_and_b32 s81, s81, 7
Mvaa 56,188,200
s_mul_i32 s81, s81, 20480
Ta 196,v1,4096
Ta 198,v1,6144
Mvaa 60,180,124
s_mov_b32 m0, s52
S_ s73,s81,s78
Mvaa 60,184,164
S_ s73,s73,4096
Ta 220,v1,8192
Ta 222,v1,10240
Mvaa 60,188,204
s_nop 0
Y_ 56,0
Y_ 57,256
Mvaa 64,180,128
s_nop 0
Y_ 58,512
Y_ 59,768
Mvaa 64,184,168
Mvaa 64,188,208
s_nop 0
Y_ 60,1024
Y_ 61,1280
Mvaa 68,180,132
s_nop 0
Y_ 62,1536
Y_ 63,1792
Mvaa 68,184,172
Mvaa 68,188,212
s_nop 0
Y_ 64,2048
Y_ 65,2304
Mvaa 72,180,136
s_nop 0
Y_ 66,2560
Y_ 67,2816
Mvaa 72,184,176
Mvaa 72,188,216
W_ 12
Y_ 68,3072
Y_ 69,3328
Mvaa 92,220,216
s_nop 0
Ta 180,v2,0
Ta 182,v2,2048
Mvaa 92,196,176
s_nop 0
Y_ 70,3584
Y_ 71,3840
Mvaa 92,192,136
s_nop 0
Ta 184,v2,4096
Ta 186,v2,6144
Mvaa 88,220,212
s_nop 0
Y_ 72,4096
Y_ 73,4352
Mvaa 88,196,172
S_ s83,s6,5
Ta 188,v2,8192
Ta 190,v2,10240
Mvaa 88,192,132
s_and_b32 s83, s83, 7
Y_ 74,4608
Y_ 75,4864
Mvaa 84,220,208
s_lshl_b32 s83, s83, 2
Y_ 92,9216
Y_ 93,9472
Mvaa 84,196,168
s_nop 0
Y_ 94,9728
Y_ 95,9984
Mvaa 84,192,128
s_nop 0
Y_ 88,8192
Y_ 89,8448
Mvaa 80,220,204
s_nop 0
Y_ 90,8704
Y_ 91,8960
Mvaa 80,196,164
v_cmp_eq_u32_e64 s[36:37], s83, v8
Mvaa 80,192,124
s_nop 0
Y_ 84,7168
Y_ 85,7424
Mvaa 76,220,200
s_nop 0
Y_ 86,7680
Y_ 87,7936
Mvaa 76,196,160
v_mov_b32 v10, 0x7fff0000
Mvaa 76,192,120
W_ 14
Y_ 80,6144
Y_ 81,6400
Mvaa 16,180,120
s_nop 0
Ta 192,v3,0
Ta 194,v3,2048
Mvaa 16,184,160
s_nop 0
Y_ 82,6656
Y_ 83,6912
Mvaa 16,188,200
s_nop 0
Ta 196,v3,4096
Ta 198,v3,6144
Mvaa 20,180,124
s_nop 0
Y_ 76,5120
Y_ 77,5376
Mvaa 20,184,164
s_nop 0
Ta 220,v3,8192
Ta 222,v3,10240
Mvaa 20,188,204
s_nop 0
Y_ 78,5632
Y_ 79,5888
Mvaa 24,180,128
v_cndmask_b32_e64 v10, v10, v9, s[36:37]
Mvaa 24,184,168
Mvaa 24,188,208
s_waitcnt vmcnt(19) lgkmcnt(0)
s_barrier
Mvaa 28,180,132
buffer_store_dword a255, v10, s[92:95], 0 offen offset:384 sc1
Mvaa 28,184,172
s_sub_u32 s75, s6, 4
ds_read2st64_b32 v[96:97], v5 offset0:0 offset1:1
S_ s55,s71,122880
Mvaa 28,188,212
s_and_b32 s75, s75, 7
ds_read2st64_b32 v[98:99], v5 offset0:2 offset1:3
S_ m0,s51,24576
Mvaa 32,180,136
ds_read2st64_b32 v[104:105], v5 offset0:4 offset1:5
Mvaa 32,184,176
buffer_load_dword v11, v8, s[92:95], s96 offen offset:384 sc1
Mvaa 32,188,216
ds_read2st64_b32 v[106:107], v5 offset0:6 offset1:7
Mvaa 52,220,216
s_nop 0
Ta 180,%0,12288
Ta 182,%0,14336
Mvaa 52,196,176
ds_read2st64_b32 v[112:113], v5 offset0:8 offset1:9
Mvaa 52,192,136
W_ 5
Ta 184,%0,16384
Ta 186,%0,18432
Mvaa 48,220,212
L_ v7,24,s55,0
Mvaa 48,196,172
S_ s55,s71,133120
Ta 188,%0,20480
Ta 190,%0,22528
Mvaa 48,192,132
S_ m0,s51,25600
ds_read2st64_b32 v[114:115], v5 offset0:10 offset1:11
A_ v96,v16,v96
Mvaa 44,220,208
ds_read2st64_b32 v[120:121], v5 offset0:12 offset1:13
A_ v97,v17,v97
Mvaa 44,196,168
L_ v7,24,s55,0
Mvaa 44,192,128
S_ s55,s71,143360
ds_read2st64_b32 v[122:123], v5 offset0:14 offset1:15
A_ v98,v18,v98
Mvaa 40,220,204
S_ m0,s51,26624
ds_read2st64_b32 v[128:129], v5 offset0:16 offset1:17
A_ v99,v19,v99
Mvaa 40,196,164
W_ 11
ds_read2st64_b32 v[130:131], v5 offset0:18 offset1:19
A_ v104,v20,v104
Mvaa 40,192,124
L_ v7,24,s55,0
Mvaa 36,220,200
S_ s55,s71,153600
ds_read2st64_b32 v[132:133], v5 offset0:36 offset1:37
A_ v105,v21,v105
Mvaa 36,196,160
S_ m0,s51,27648
ds_read2st64_b32 v[134:135], v5 offset0:38 offset1:39
A_ v106,v22,v106
Mvaa 36,192,120
ds_read2st64_b32 v[124:125], v5 offset0:32 offset1:33
A_ v107,v23,v107
s_waitcnt vmcnt(11)
Maaa 100,70,20
L_ v7,24,s55,0
Maaa 140,68,20
W_ 7
ds_read2st64_b32 v[126:127], v5 offset0:34 offset1:35
A_ v112,v24,v112
Maaa 104,70,24
S_ s55,s71,163840
ds_read2st64_b32 v[116:117], v5 offset0:28 offset1:29
A_ v113,v25,v113
Maaa 144,68,24
S_ m0,s51,28672
ds_read2st64_b32 v[118:119], v5 offset0:30 offset1:31
A_ v114,v26,v114
Maaa 108,70,28
L_ v7,24,s55,0
Maaa 148,68,28
S_ s55,s71,174080
ds_read2st64_b32 v[108:109], v5 offset0:24 offset1:25
A_ v115,v27,v115
Maaa 112,70,32
W_ 9
ds_read2st64_b32 v[110:111], v5 offset0:26 offset1:27
A_ v120,v28,v120
Maaa 152,68,32
S_ m0,s51,29696
ds_read2st64_b32 v[100:101], v5 offset0:20 offset1:21
A_ v121,v29,v121
Maaa 116,70,36
L_ v7,24,s55,0
Maaa 156,68,36
S_ s79,s6,8
ds_read2st64_b32 v[102:103], v5 offset0:22 offset1:23
A_ v122,v30,v122
Maaa 224,70,60
s_and_b32 s79, s79, 7
A_ v123,v31,v123
Maaa 228,68,60
W_ 10
A_ v128,v32,v128
A_ v129,v33,v129
Maaa 232,70,64
s_lshl_b32 s79, s79, 7
A_ v130,v34,v130
A_ v131,v35,v131
Maaa 236,68,64
W_ 9
A_ v132,v52,v132
W_ 8
Zvav 56,180,136
S_ s71,s72,s79
Ta 192,v1,12288
Ta 194,v1,14336
Mvav 56,184,156
S_ s55,s71,491520
A_ v133,v53,v133
A_ v134,v54,v134
Mvav 56,188,176
S_ m0,s51,0
Ta 196,v1,16384
Ta 198,v1,18432
Zvav 60,180,140
L_ v7,24,s55,0
Mvav 60,184,160
S_ s55,s71,501760
Ta 220,v1,20480
Ta 222,v1,22528
Mvav 60,188,180
S_ m0,s51,1024
A_ v135,v55,v135
Zvav 64,180,144
W_ 12
A_ v124,v48,v124
A_ v125,v49,v125
Mvav 64,184,164
L_ v7,24,s55,0
Mvav 64,188,184
S_ s55,s71,512000
A_ v126,v50,v126
A_ v127,v51,v127
Zvav 68,180,148
W_ 10
A_ v116,v44,v116
A_ v117,v45,v117
Mvav 68,184,168
S_ m0,s51,2048
A_ v118,v46,v118
A_ v119,v47,v119
Mvav 68,188,188
L_ v7,24,s55,0
Zvav 72,180,152
W_ 8
A_ v108,v40,v108
A_ v109,v41,v109
Mvav 72,184,172
S_ s55,s71,522240
A_ v110,v42,v110
A_ v111,v43,v111
Mvav 72,188,192
W_ 0
A_ v100,v36,v100
Zvav 92,220,192
S_ m0,s51,3072
Ta 180,v2,12288
Ta 182,v2,14336
Mvav 92,196,172
L_ v7,24,s55,0
Mvav 92,192,152
S_ s55,s71,532480
Ta 184,v2,16384
Ta 186,v2,18432
Zvav 88,220,188
S_ m0,s51,4096
A_ v101,v37,v101
A_ v102,v38,v102
Mvav 88,196,168
s_nop 0
Ta 188,v2,20480
Ta 190,v2,22528
Mvav 88,192,148
L_ v7,24,s55,0
Zvav 84,220,184
S_ s55,s71,542720
A_ v103,v39,v103
C_ 131,130,131
Mvav 84,196,164
S_ m0,s51,5120
C_ 130,128,129
C_ 132,132,133
Mvav 84,192,144
C_ 133,134,135
Zvav 80,220,180
buffer_store_dwordx4 v[130:133], v6, s[28:31], s73 offen offset:0 sc1
Mvav 80,196,160
s_sub_u32 s73, s73, 4096
Mvav 80,192,140
C_ 123,122,123
C_ 122,120,121
Zvav 76,220,176
C_ 124,124,125
C_ 125,126,127
Mvav 76,196,156
buffer_store_dwordx4 v[122:125], v6, s[28:31], s73 offen offset:3072 sc1
Mvav 76,192,136
W_ 0
C_ 115,114,115
C_ 114,112,113
)A"
R"A(Zvav 16,180,136
s_nop 0
Ta 192,v3,12288
Ta 194,v3,14336
Mvav 16,184,156
C_ 116,116,117
C_ 117,118,119
Mvav 16,188,176
s_nop 0
Ta 196,v3,16384
Ta 198,v3,18432
Zvav 20,180,140
buffer_store_dwordx4 v[114:117], v6, s[28:31], s73 offen offset:2048 sc1
Mvav 20,184,160
s_nop 0
Ta 220,v3,20480
Ta 222,v3,22528
Mvav 20,188,180
C_ 107,106,107
C_ 106,104,105
Zvav 24,180,144
C_ 108,108,109
C_ 109,110,111
Mvav 24,184,164
buffer_store_dwordx4 v[106:109], v6, s[28:31], s73 offen offset:1024 sc1
Mvav 24,188,184
C_ 99,98,99
C_ 98,96,97
Zvav 28,180,148
C_ 100,100,101
C_ 101,102,103
Mvav 28,184,168
Mvav 28,188,188
buffer_store_dwordx4 v[98:101], v6, s[28:31], s73 offen offset:0 sc1
Zvav 32,180,152
Mvav 32,184,172
Mvav 32,188,192
s_waitcnt vmcnt(31) lgkmcnt(0)
s_barrier
Zvav 52,220,192
s_nop 0
Ta 180,%0,49152
Ta 182,%0,51200
Mvav 52,196,172
L_ v7,24,s55,0
Mvav 52,192,152
S_ s55,s71,614400
Ta 184,%0,53248
Ta 186,%0,55296
Zvav 48,220,188
S_ m0,s51,12288
Mvav 48,196,168
s_nop 0
Ta 188,%0,57344
Ta 190,%0,59392
Mvav 48,192,148
L_ v7,24,s55,0
Zvav 44,220,184
S_ s55,s71,624640
Mvav 44,196,164
S_ m0,s51,13312
Mvav 44,192,144
Zvav 40,220,180
L_ v7,24,s55,0
Mvav 40,196,160
S_ s55,s71,634880
Mvav 40,192,140
S_ m0,s51,14336
Zvav 36,220,176
Mvav 36,196,156
L_ v7,24,s55,0
Mvav 36,192,136
W_ 0
S_ s55,s71,645120
Mvav 56,180,196
S_ m0,s51,15360
Ta 192,v1,49152
Ta 194,v1,51200
Mvav 56,184,216
Mvav 56,188,236
s_nop 0
Ta 196,v1,53248
Ta 198,v1,55296
Mvav 60,180,200
L_ v7,24,s55,0
Mvav 60,184,220
S_ s55,s71,655360
Ta 220,v1,57344
Ta 222,v1,59392
Mvav 60,188,240
S_ m0,s51,16384
Mvav 64,180,204
Mvav 64,184,224
L_ v7,24,s55,0
Mvav 64,188,244
S_ s55,s71,665600
Mvav 68,180,208
S_ m0,s51,17408
Mvav 68,184,228
Mvav 68,188,248
L_ v7,24,s55,0
Mvav 72,180,212
Mvav 72,184,232
Mvav 72,188,252
W_ 0
Mvav 92,220,252
s_nop 0
Ta 180,v2,49152
Ta 182,v2,51200
Mvav 92,196,232
Mvav 92,192,212
s_nop 0
Ta 184,v2,53248
Ta 186,v2,55296
Mvav 88,220,248
s_waitcnt vmcnt(23)
Mvav 88,196,228
s_nop 0
Ta 188,v2,57344
Ta 190,v2,59392
Mvav 88,192,208
v_cmp_eq_u32 vcc, s62, v11
Mvav 84,220,244
s_mul_i32 s76, s75, 204800
Mvav 84,196,224
s_bitcmp1_b32 vcc_lo, s75
s_cbranch_scc0 .Lspin3
.Lspun3:
Mvav 84,192,204
S_ s56,s76,s84
Mvav 80,220,240
buffer_load_dwordx4 a[20:23], v6, s[28:31], s56 offen offset:0 sc1
Mvav 80,196,220
Mvav 80,192,200
Mvav 76,220,236
Mvav 76,196,216
buffer_load_dwordx4 a[24:27], v6, s[28:31], s56 offen offset:1024 sc1
Mvav 76,192,196
W_ 0
Mvav 16,180,196
s_nop 0
Ta 192,v3,49152
Ta 194,v3,51200
Mvav 16,184,216
Mvav 16,188,236
s_nop 0
Ta 196,v3,53248
Ta 198,v3,55296
Mvav 20,180,200
buffer_load_dwordx4 a[28:31], v6, s[28:31], s56 offen offset:2048 sc1
Mvav 20,184,220
s_nop 0
Ta 220,v3,57344
Ta 222,v3,59392
Mvav 20,188,240
Mvav 24,180,204
Mvav 24,184,224
buffer_load_dwordx4 a[32:35], v6, s[28:31], s56 offen offset:3072 sc1
Mvav 24,188,244
S_ s56,s56,4096
Mvav 28,180,208
Mvav 28,184,228
Mvav 28,188,248
buffer_load_dwordx4 a[36:39], v6, s[28:31], s56 offen offset:0 sc1
Mvav 32,180,212
S_ s56,s76,s74
Mvav 32,184,232
Mvav 32,188,252
s_waitcnt vmcnt(30) lgkmcnt(0)
s_barrier
Mvav 52,220,252
S_ s55,s71,0
Ta 180,%0,36864
Ta 182,%0,38912
Mvav 52,196,232
buffer_load_dwordx4 a[60:63], v6, s[28:31], s56 offen offset:0 sc1
Mvav 52,192,212
S_ s56,s76,s82
Ta 184,%0,40960
Ta 186,%0,43008
Mvav 48,220,248
S_ m0,s51,49152
Mvav 48,196,228
s_nop 0
Ta 188,%0,45056
Ta 190,%0,47104
Mvav 48,192,208
buffer_load_dwordx4 a[64:67], v6, s[28:31], s56 offen offset:0 sc1
Mvav 44,220,244
s_sub_u32 s75, s6, 5
Mvav 44,196,224
s_and_b32 s75, s75, 7
Mvav 44,192,204
Mvav 40,220,240
buffer_load_dword v11, v8, s[92:95], s96 offen offset:384 sc1
Mvav 40,196,220
Mvav 40,192,200
Mvav 36,220,236
Mvav 36,196,216
L_ v7,24,s55,0
Mvav 36,192,196
W_ 0
S_ s55,s71,10240
Mvaa 56,180,0
S_ m0,s51,50176
Ta 192,v1,36864
Ta 194,v1,38912
Mvaa 56,184,40
Mvaa 56,188,80
s_nop 0
Ta 196,v1,40960
Ta 198,v1,43008
Mvaa 60,180,4
L_ v7,24,s55,0
Mvaa 60,184,44
S_ s55,s71,20480
Ta 220,v1,45056
Ta 222,v1,47104
Mvaa 60,188,84
S_ m0,s51,51200
Mvaa 64,180,8
Mvaa 64,184,48
L_ v7,24,s55,0
Mvaa 64,188,88
S_ s55,s71,30720
Mvaa 68,180,12
S_ m0,s51,52224
Mvaa 68,184,52
Mvaa 68,188,240
L_ v7,24,s55,0
Mvaa 72,180,16
S_ s55,s71,40960
Mvaa 72,184,56
S_ m0,s51,53248
Mvaa 72,188,244
W_ 0
Mvaa 92,220,244
s_nop 0
Ta 180,v2,36864
Ta 182,v2,38912
Mvaa 92,196,56
L_ v7,24,s55,0
Mvaa 92,192,16
S_ s55,s71,51200
Ta 184,v2,40960
Ta 186,v2,43008
Mvaa 88,220,240
S_ m0,s51,54272
Mvaa 88,196,52
s_nop 0
Ta 188,v2,45056
Ta 190,v2,47104
Mvaa 88,192,12
L_ v7,24,s55,0
Mvaa 84,220,88
Mvaa 84,196,48
Mvaa 84,192,8
Mvaa 80,220,84
Mvaa 80,196,44
S_ s83,s6,6
Mvaa 80,192,4
s_and_b32 s83, s83, 7
Mvaa 76,220,80
s_lshl_b32 s83, s83, 2
Mvaa 76,196,40
v_cmp_eq_u32_e64 s[36:37], s83, v8
Mvaa 76,192,0
W_ 0
v_mov_b32 v10, 0x7fff0000
Mvaa 16,180,0
s_nop 0
Ta 192,v3,36864
Ta 194,v3,38912
Mvaa 16,184,40
v_cndmask_b32_e64 v10, v10, v9, s[36:37]
Mvaa 16,188,80
s_nop 0
Ta 196,v3,40960
Ta 198,v3,43008
Mvaa 20,180,4
Mvaa 20,184,44
s_nop 0
Ta 220,v3,45056
Ta 222,v3,47104
Mvaa 20,188,84
Mvaa 24,180,8
Mvaa 24,184,48
Mvaa 24,188,88
Mvaa 28,180,12
Mvaa 28,184,52
Mvaa 28,188,240
Mvaa 32,180,16
Mvaa 32,184,56
Mvaa 32,188,244
s_waitcnt vmcnt(21) lgkmcnt(0)
s_barrier
Mvaa 52,220,244
s_nop 0
Ta 180,%0,24576
Ta 182,%0,26624
Mvaa 52,196,56
buffer_store_dword a255, v10, s[92:95], 0 offen offset:384 sc1
Mvaa 52,192,16
s_nop 0
Ta 184,%0,28672
Ta 186,%0,30720
Mvaa 48,220,240
Mvaa 48,196,52
s_nop 0
Ta 188,%0,32768
Ta 190,%0,34816
Mvaa 48,192,12
Mvaa 44,220,88
Mvaa 44,196,48
Mvaa 44,192,8
Mvaa 40,220,84
Mvaa 40,196,44
Mvaa 40,192,4
Mvaa 36,220,80
Mvaa 36,196,40
Mvaa 36,192,0
W_ 0
Mvaa 56,180,120
S_ s81,s6,7
Ta 192,v1,24576
Ta 194,v1,26624
Mvaa 56,184,160
s_and_b32 s81, s81, 7
Mvaa 56,188,200
s_mul_i32 s81, s81, 20480
Ta 196,v1,28672
Ta 198,v1,30720
Mvaa 60,180,124
s_mov_b32 m0, s52
S_ s73,s81,s78
Mvaa 60,184,164
S_ s73,s73,4096
Ta 220,v1,32768
Ta 222,v1,34816
Mvaa 60,188,204
s_nop 0
Y_ 56,0
Y_ 57,256
Mvaa 64,180,128
s_nop 0
Y_ 58,512
Y_ 59,768
Mvaa 64,184,168
Mvaa 64,188,208
s_nop 0
Y_ 60,1024
Y_ 61,1280
Mvaa 68,180,132
s_nop 0
Y_ 62,1536
Y_ 63,1792
Mvaa 68,184,172
Mvaa 68,188,212
s_nop 0
Y_ 64,2048
Y_ 65,2304
Mvaa 72,180,136
s_nop 0
Y_ 66,2560
Y_ 67,2816
Mvaa 72,184,176
Mvaa 72,188,216
W_ 12
Y_ 68,3072
Y_ 69,3328
Mvaa 92,220,216
s_nop 0
Ta 180,v2,24576
Ta 182,v2,26624
Mvaa 92,196,176
s_nop 0
Y_ 70,3584
Y_ 71,3840
Mvaa 92,192,136
s_nop 0
Ta 184,v2,28672
Ta 186,v2,30720
Mvaa 88,220,212
s_nop 0
Y_ 72,4096
Y_ 73,4352
Mvaa 88,196,172
s_nop 0
Ta 188,v2,32768
Ta 190,v2,34816
Mvaa 88,192,132
s_nop 0
Y_ 74,4608
Y_ 75,4864
Mvaa 84,220,208
s_nop 0
Y_ 92,9216
Y_ 93,9472
Mvaa 84,196,168
s_nop 0
Y_ 94,9728
Y_ 95,9984
Mvaa 84,192,128
s_nop 0
Y_ 88,8192
Y_ 89,8448
Mvaa 80,220,204
s_nop 0
Y_ 90,8704
Y_ 91,8960
Mvaa 80,196,164
Mvaa 80,192,124
s_nop 0
Y_ 84,7168
Y_ 85,7424
Mvaa 76,220,200
s_nop 0
Y_ 86,7680
Y_ 87,7936
Mvaa 76,196,160
Mvaa 76,192,120
W_ 14
Y_ 80,6144
Y_ 81,6400
Mvaa 16,180,120
s_nop 0
Ta 192,v3,24576
Ta 194,v3,26624
Mvaa 16,184,160
s_nop 0
Y_ 82,6656
Y_ 83,6912
Mvaa 16,188,200
s_nop 0
Ta 196,v3,28672
Ta 198,v3,30720
Mvaa 20,180,124
s_nop 0
Y_ 76,5120
Y_ 77,5376
Mvaa 20,184,164
s_nop 0
Ta 220,v3,32768
Ta 222,v3,34816
Mvaa 20,188,204
s_nop 0
Y_ 78,5632
Y_ 79,5888
Mvaa 24,180,128
Mvaa 24,184,168
Mvaa 24,188,208
s_waitcnt vmcnt(21) lgkmcnt(0)
s_barrier
Mvaa 28,180,132
S_ s55,s71,122880
ds_read2st64_b32 v[96:97], v5 offset0:0 offset1:1
Mvaa 28,184,172
S_ m0,s51,36864
ds_read2st64_b32 v[98:99], v5 offset0:2 offset1:3
Mvaa 28,188,212
L_ v7,24,s55,0
Mvaa 32,180,136
S_ s55,s71,133120
ds_read2st64_b32 v[104:105], v5 offset0:4 offset1:5
Mvaa 32,184,176
s_waitcnt vmcnt(8)
ds_read2st64_b32 v[106:107], v5 offset0:6 offset1:7
S_ m0,s51,37888
Mvaa 32,188,216
ds_read2st64_b32 v[112:113], v5 offset0:8 offset1:9
v_cmp_eq_u32 vcc, s62, v11
Mvaa 52,220,216
s_mul_i32 s76, s75, 204800
Ta 180,%0,0
Ta 182,%0,2048
Mvaa 52,196,176
L_ v7,24,s55,0
Mvaa 52,192,136
W_ 5
Ta 184,%0,4096
Ta 186,%0,6144
Mvaa 48,220,212
s_bitcmp1_b32 vcc_lo, s75
s_cbranch_scc0 .Lspin4
.Lspun4:
ds_read2st64_b32 v[114:115], v5 offset0:10 offset1:11
Mvaa 48,196,172
S_ s55,s71,143360
Ta 188,%0,8192
Ta 190,%0,10240
Mvaa 48,192,132
S_ m0,s51,38912
ds_read2st64_b32 v[120:121], v5 offset0:12 offset1:13
A_ v96,v16,v96
Mvaa 44,220,208
L_ v7,24,s55,0
Mvaa 44,196,168
S_ s55,s71,153600
ds_read2st64_b32 v[122:123], v5 offset0:14 offset1:15
A_ v97,v17,v97
Mvaa 44,192,128
S_ m0,s51,39936
ds_read2st64_b32 v[128:129], v5 offset0:16 offset1:17
A_ v98,v18,v98
Mvaa 40,220,204
ds_read2st64_b32 v[130:131], v5 offset0:18 offset1:19
A_ v99,v19,v99
Mvaa 40,196,164
L_ v7,24,s55,0
Mvaa 40,192,124
W_ 12
ds_read2st64_b32 v[132:133], v5 offset0:36 offset1:37
A_ v104,v20,v104
Mvaa 36,220,200
S_ s55,s71,163840
ds_read2st64_b32 v[134:135], v5 offset0:38 offset1:39
A_ v105,v21,v105
Mvaa 36,196,160
S_ m0,s51,40960
ds_read2st64_b32 v[124:125], v5 offset0:32 offset1:33
A_ v106,v22,v106
Mvaa 36,192,120
L_ v7,24,s55,0
s_waitcnt vmcnt(13)
Maaa 100,70,20
S_ s55,s71,174080
ds_read2st64_b32 v[126:127], v5 offset0:34 offset1:35
A_ v107,v23,v107
Maaa 140,68,20
W_ 10
ds_read2st64_b32 v[116:117], v5 offset0:28 offset1:29
A_ v112,v24,v112
Maaa 104,70,24
S_ m0,s51,41984
ds_read2st64_b32 v[118:119], v5 offset0:30 offset1:31
A_ v113,v25,v113
Maaa 144,68,24
L_ v7,24,s55,0
Maaa 108,70,28
ds_read2st64_b32 v[108:109], v5 offset0:24 offset1:25
A_ v114,v26,v114
Maaa 148,68,28
ds_read2st64_b32 v[110:111], v5 offset0:26 offset1:27
A_ v115,v27,v115
Maaa 112,70,32
W_ 10
ds_read2st64_b32 v[100:101], v5 offset0:20 offset1:21
A_ v120,v28,v120
Maaa 152,68,32
ds_read2st64_b32 v[102:103], v5 offset0:22 offset1:23
A_ v121,v29,v121
Maaa 116,70,36
A_ v122,v30,v122
A_ v123,v31,v123
Maaa 156,68,36
W_ 10
A_ v128,v32,v128
A_ v129,v33,v129
Maaa 224,70,60
A_ v130,v34,v130
A_ v131,v35,v131
Maaa 228,68,60
W_ 8
A_ v132,v52,v132
A_ v133,v53,v133
Maaa 232,70,64
A_ v134,v54,v134
A_ v135,v55,v135
Maaa 236,68,64
W_ 7
A_ v124,v48,v124
W_ 6
Zvav 56,180,136
s_nop 0
Ta 192,v1,0
Ta 194,v1,2048
Mvav 56,184,156
A_ v125,v49,v125
A_ v126,v50,v126
Mvav 56,188,176
S_ s56,s76,s84
Ta 196,v1,4096
Ta 198,v1,6144
Zvav 60,180,140
buffer_load_dwordx4 a[20:23], v6, s[28:31], s56 offen offset:0 sc1
Mvav 60,184,160
s_nop 0
Ta 220,v1,8192
Ta 222,v1,10240
Mvav 60,188,180
A_ v127,v51,v127
C_ 131,130,131
Zvav 64,180,144
W_ 10
A_ v116,v44,v116
A_ v117,v45,v117
Mvav 64,184,164
buffer_load_dwordx4 a[24:27], v6, s[28:31], s56 offen offset:1024 sc1
Mvav 64,188,184
A_ v118,v46,v118
A_ v119,v47,v119
Zvav 68,180,148
W_ 8
A_ v108,v40,v108
A_ v109,v41,v109
Mvav 68,184,168
A_ v110,v42,v110
A_ v111,v43,v111
Mvav 68,188,188
buffer_load_dwordx4 a[28:31], v6, s[28:31], s56 offen offset:2048 sc1
Zvav 72,180,152
W_ 6
A_ v100,v36,v100
A_ v101,v37,v101
Mvav 72,184,172
A_ v102,v38,v102
A_ v103,v39,v103
Mvav 72,188,192
W_ 0
C_ 130,128,129
C_ 132,132,133
Zvav 92,220,192
s_nop 0
Ta 180,v2,0
Ta 182,v2,2048
Mvav 92,196,172
buffer_load_dwordx4 a[32:35], v6, s[28:31], s56 offen offset:3072 sc1
Mvav 92,192,152
S_ s56,s56,4096
Ta 184,v2,4096
Ta 186,v2,6144
Zvav 88,220,188
C_ 133,134,135
Mvav 88,196,168
s_nop 0
Ta 188,v2,8192
Ta 190,v2,10240
Mvav 88,192,148
buffer_load_dwordx4 a[36:39], v6, s[28:31], s56 offen offset:0 sc1
Zvav 84,220,184
S_ s56,s76,s74
Mvav 84,196,164
Mvav 84,192,144
Zvav 80,220,180
buffer_load_dwordx4 a[60:63], v6, s[28:31], s56 offen offset:0 sc1
Mvav 80,196,160
S_ s56,s76,s82
Mvav 80,192,140
Zvav 76,220,176
Mvav 76,196,156
buffer_load_dwordx4 a[64:67], v6, s[28:31], s56 offen offset:0 sc1
Mvav 76,192,136
W_ 0
s_sub_u32 s75, s6, 6
Maav 100,180,136
s_and_b32 s75, s75, 7
Ta 192,v3,0
Ta 194,v3,2048
Maav 100,184,156
Maav 100,188,176
s_nop 0
Ta 196,v3,4096
Ta 198,v3,6144
Maav 104,180,140
buffer_load_dword v11, v8, s[92:95], s96 offen offset:384 sc1
Maav 104,184,160
s_nop 0
Ta 220,v3,8192
Ta 222,v3,10240
Maav 104,188,180
Maav 108,180,144
Maav 108,184,164
buffer_store_dwordx4 v[130:133], v6, s[28:31], s73 offen offset:0 sc1
Maav 108,188,184
s_sub_u32 s73, s73, 4096
Maav 112,180,148
C_ 123,122,123
C_ 122,120,121
Maav 112,184,168
C_ 124,124,125
C_ 125,126,127
Maav 112,188,188
buffer_store_dwordx4 v[122:125], v6, s[28:31], s73 offen offset:3072 sc1
Maav 116,180,152
C_ 115,114,115
C_ 114,112,113
Maav 116,184,172
C_ 116,116,117
C_ 117,118,119
Maav 116,188,192
s_waitcnt vmcnt(31) lgkmcnt(0)
s_barrier
Maav 156,220,192
s_nop 0
Ta 180,%0,12288
Ta 182,%0,14336
Maav 156,196,172
buffer_store_dwordx4 v[114:117], v6, s[28:31], s73 offen offset:2048 sc1
Maav 156,192,152
s_nop 0
Ta 184,%0,16384
Ta 186,%0,18432
Maav 152,220,188
C_ 107,106,107
C_ 106,104,105
Maav 152,196,168
s_nop 0
Ta 188,%0,20480
Ta 190,%0,22528
Maav 152,192,148
C_ 108,108,109
C_ 109,110,111
Maav 148,220,184
buffer_store_dwordx4 v[106:109], v6, s[28:31], s73 offen offset:1024 sc1
Maav 148,196,164
C_ 99,98,99
C_ 98,96,97
Maav 148,192,144
C_ 100,100,101
C_ 101,102,103
Maav 144,220,180
Maav 144,196,160
buffer_store_dwordx4 v[98:101], v6, s[28:31], s73 offen offset:0 sc1
Maav 144,192,140
Maav 140,220,176
Maav 140,196,156
Maav 140,192,136
W_ 0
Mvav 56,180,196
s_nop 0
Ta 192,v1,12288
Ta 194,v1,14336
Mvav 56,184,216
Mvav 56,188,236
s_nop 0
Ta 196,v1,16384
Ta 198,v1,18432
Mvav 60,180,200
Mvav 60,184,220
s_nop 0
Ta 220,v1,20480
Ta 222,v1,22528
Mvav 60,188,240
v_bfe_u32 v4, v6, 4, 4
Mvav 64,180,204
v_mul_u32_u24 v4, 2560, v4
Mvav 64,184,224
v_bfe_u32 v10, v6, 8, 1
Mvav 64,188,244
v_lshl_add_u32 v4, v10, 5, v4
Mvav 68,180,208
v_lshrrev_b32 v10, 9, v6
Mvav 68,184,228
v_lshl_add_u32 v4, v10, 4, v4
Mvav 68,188,248
Mvav 72,180,212
Mvav 72,184,232
Mvav 72,188,252
W_ 0
Mvav 92,220,252
s_nop 0
Ta 180,v2,12288
Ta 182,v2,14336
Mvav 92,196,232
Mvav 92,192,212
s_nop 0
Ta 184,v2,16384
Ta 186,v2,18432
Mvav 88,220,248
Mvav 88,196,228
s_nop 0
Ta 188,v2,20480
Ta 190,v2,22528
Mvav 88,192,208
Mvav 84,220,244
Mvav 84,196,224
Mvav 84,192,204
Mvav 80,220,240
Mvav 80,196,220
Mvav 80,192,200
Mvav 76,220,236
Mvav 76,196,216
Mvav 76,192,196
W_ 0
Maav 100,180,196
s_nop 0
Ta 192,v3,12288
Ta 194,v3,14336
Maav 100,184,216
Maav 100,188,236
s_nop 0
Ta 196,v3,16384
Ta 198,v3,18432
Maav 104,180,200
Maav 104,184,220
s_nop 0
Ta 220,v3,20480
Ta 222,v3,22528
Maav 104,188,240
Maav 108,180,204
S_ s86,s6,1
Maav 108,184,224
s_and_b32 s86, s86, 7
Maav 108,188,244
s_waitcnt vmcnt(5)
Maav 112,180,208
v_cmp_eq_u32 vcc, s62, v11
Maav 112,184,228
s_mul_i32 s76, s75, 204800
Maav 112,188,248
s_bitcmp1_b32 vcc_lo, s75
s_cbranch_scc0 .Lspin5
.Lspun5:
Maav 116,180,212
s_bitcmp1_b32 vcc_lo, s86
s_cbranch_scc0 .Lspin6
.Lspun6:
Maav 116,184,232
S_ s56,s76,s84
Maav 116,188,252
s_waitcnt vmcnt(20) lgkmcnt(0)
s_barrier
Maav 156,220,252
s_nop 0
Ta 180,%0,49152
Ta 182,%0,51200
Maav 156,196,232
buffer_load_dwordx4 v[16:19], v6, s[28:31], s56 offen offset:0 sc1
Maav 156,192,212
s_nop 0
Ta 184,%0,53248
Ta 186,%0,55296
Maav 152,220,248
Maav 152,196,228
s_nop 0
Ta 188,%0,57344
Ta 190,%0,59392
Maav 152,192,208
buffer_load_dwordx4 v[20:23], v6, s[28:31], s56 offen offset:1024 sc1
Maav 148,220,244
Maav 148,196,224
Maav 148,192,204
Maav 144,220,240
buffer_load_dwordx4 v[24:27], v6, s[28:31], s56 offen offset:2048 sc1
Maav 144,196,220
Maav 144,192,200
Maav 140,220,236
Maav 140,196,216
buffer_load_dwordx4 v[28:31], v6, s[28:31], s56 offen offset:3072 sc1
Maav 140,192,196
W_ 0
S_ s56,s56,4096
Mvaa 56,180,0
s_nop 0
Ta 192,v1,49152
Ta 194,v1,51200
Mvaa 56,184,40
R_ v164,a224
R_ v165,a225
Mvaa 56,188,80
s_nop 0
Ta 196,v1,53248
Ta 198,v1,55296
Mvaa 60,180,4
buffer_load_dwordx4 v[32:35], v6, s[28:31], s56 offen offset:0 sc1
Mvaa 60,184,44
S_ s56,s76,s74
Ta 220,v1,57344
Ta 222,v1,59392
Mvaa 60,188,84
R_ v166,a226
R_ v167,a227
Mvaa 64,180,8
R_ v168,a228
R_ v169,a229
Mvaa 64,184,48
buffer_load_dwordx4 v[36:39], v6, s[28:31], s56 offen offset:0 sc1
Mvaa 64,188,88
S_ s56,s76,s82
R_ v170,a230
R_ v171,a231
Mvaa 68,180,12
R_ v172,a232
R_ v173,a233
Mvaa 68,184,52
R_ v174,a234
R_ v175,a235
Mvaa 68,188,240
buffer_load_dwordx4 v[40:43], v6, s[28:31], s56 offen offset:0 sc1
Mvaa 72,180,16
s_mul_i32 s76, s86, 204800
R_ v176,a236
R_ v177,a237
Mvaa 72,184,56
S_ s80,s76,s84
R_ v178,a238
R_ v179,a239
Mvaa 72,188,244
W_ 0
S_ s56,s76,s74
Mvaa 92,220,244
s_nop 0
Ta 180,v2,49152
Ta 182,v2,51200
Mvaa 92,196,56
buffer_load_dwordx4 v[156:159], v6, s[28:31], s56 offen offset:0 sc1
Mvaa 92,192,16
S_ s56,s76,s82
Ta 184,v2,53248
Ta 186,v2,55296
Mvaa 88,220,240
Mvaa 88,196,52
s_nop 0
Ta 188,v2,57344
Ta 190,v2,59392
Mvaa 88,192,12
buffer_load_dwordx4 v[160:163], v6, s[28:31], s56 offen offset:0 sc1
Mvaa 84,220,88
Mvaa 84,196,48
Mvaa 84,192,8
Mvaa 80,220,84
Mvaa 80,196,44
Mvaa 80,192,4
Mvaa 76,220,80
Mvaa 76,196,40
Mvaa 76,192,0
W_ 0
Maaa 100,180,0
s_nop 0
Ta 192,v3,49152
Ta 194,v3,51200
Maaa 100,184,40
Maaa 100,188,80
s_nop 0
Ta 196,v3,53248
Ta 198,v3,55296
Maaa 104,180,4
Maaa 104,184,44
s_nop 0
Ta 220,v3,57344
Ta 222,v3,59392
Maaa 104,188,84
Maaa 108,180,8
R_ v180,a100
R_ v181,a101
Maaa 108,184,48
R_ v182,a102
R_ v183,a103
Maaa 108,188,88
Maaa 112,180,12
R_ v184,a104
R_ v185,a105
Maaa 112,184,52
R_ v186,a106
R_ v187,a107
Maaa 112,188,240
Maaa 116,180,16
R_ v188,a108
R_ v189,a109
Maaa 116,184,56
R_ v190,a110
R_ v191,a111
Maaa 116,188,244
s_waitcnt vmcnt(22) lgkmcnt(0)
s_barrier
Maaa 156,220,244
s_nop 0
Ta 180,%0,36864
Ta 182,%0,38912
Maaa 156,196,56
R_ v192,a112
R_ v193,a113
Maaa 156,192,16
s_nop 0
Ta 184,%0,40960
Ta 186,%0,43008
Maaa 152,220,240
R_ v194,a114
R_ v195,a115
Maaa 152,196,52
s_nop 0
Ta 188,%0,45056
Ta 190,%0,47104
Maaa 152,192,12
R_ v196,a116
R_ v197,a117
Maaa 148,220,88
R_ v198,a118
R_ v199,a119
Maaa 148,196,48
R_ v216,a156
R_ v217,a157
Maaa 148,192,8
R_ v218,a158
R_ v219,a159
Maaa 144,220,84
R_ v212,a152
R_ v213,a153
Maaa 144,196,44
R_ v214,a154
R_ v215,a155
Maaa 144,192,4
Maaa 140,220,80
R_ v208,a148
R_ v209,a149
Maaa 140,196,40
R_ v210,a150
R_ v211,a151
Maaa 140,192,0
s_waitcnt vmcnt(15)
Mvaa 164,70,60
R_ v204,a144
R_ v205,a145
Mvaa 168,68,60
R_ v206,a146
R_ v207,a147
Mvaa 172,70,64
Mvaa 176,68,64
R_ v200,a140
R_ v201,a141
Mvaa 180,70,20
R_ v202,a142
R_ v203,a143
Mvaa 184,70,24
Mvaa 188,70,28
Mvaa 192,70,32
Mvaa 196,70,36
Mvaa 216,68,36
Mvaa 212,68,32
Mvaa 208,68,28
Mvaa 204,68,24
Mvaa 200,68,20
W_ 0
Mvaa 56,180,120
S_ s56,s76,s84
Ta 192,v1,36864
Ta 194,v1,38912
Mvaa 56,184,160
buffer_load_dwordx4 v[136:139], v6, s[28:31], s56 offen offset:0 sc1
Mvaa 56,188,200
s_nop 0
Ta 196,v1,40960
Ta 198,v1,43008
Mvaa 60,180,124
s_mov_b32 m0, s52
Mvaa 60,184,164
s_nop 0
Ta 220,v1,45056
Ta 222,v1,47104
Mvaa 60,188,204
s_nop 0
Y_ 56,0
Y_ 57,256
Mvaa 64,180,128
s_nop 0
Y_ 58,512
Y_ 59,768
Mvaa 64,184,168
buffer_load_dwordx4 v[140:143], v6, s[28:31], s56 offen offset:1024 sc1
Mvaa 64,188,208
s_nop 0
Y_ 60,1024
Y_ 61,1280
Mvaa 68,180,132
s_nop 0
Y_ 62,1536
Y_ 63,1792
Mvaa 68,184,172
Mvaa 68,188,212
s_nop 0
Y_ 64,2048
Y_ 65,2304
Mvaa 72,180,136
s_nop 0
Y_ 66,2560
Y_ 67,2816
Mvaa 72,184,176
buffer_load_dwordx4 v[144:147], v6, s[28:31], s56 offen offset:2048 sc1
Mvaa 72,188,216
W_ 12
Y_ 68,3072
Y_ 69,3328
Mvaa 92,220,216
s_nop 0
Ta 180,v2,36864
Ta 182,v2,38912
Mvaa 92,196,176
s_nop 0
Y_ 70,3584
Y_ 71,3840
Mvaa 92,192,136
s_nop 0
Ta 184,v2,40960
Ta 186,v2,43008
Mvaa 88,220,212
s_nop 0
Y_ 72,4096
Y_ 73,4352
Mvaa 88,196,172
s_nop 0
Ta 188,v2,45056
Ta 190,v2,47104
Mvaa 88,192,132
s_nop 0
Y_ 74,4608
Y_ 75,4864
Mvaa 84,220,208
s_nop 0
Y_ 92,9216
Y_ 93,9472
Mvaa 84,196,168
s_nop 0
Y_ 94,9728
Y_ 95,9984
Mvaa 84,192,128
s_nop 0
Y_ 88,8192
Y_ 89,8448
Mvaa 80,220,204
s_nop 0
Y_ 90,8704
Y_ 91,8960
Mvaa 80,196,164
buffer_load_dwordx4 v[148:151], v6, s[28:31], s56 offen offset:3072 sc1
Mvaa 80,192,124
S_ s56,s56,4096
Y_ 84,7168
Y_ 85,7424
Mvaa 76,220,200
s_nop 0
Y_ 86,7680
Y_ 87,7936
Mvaa 76,196,160
Mvaa 76,192,120
W_ 14
Y_ 80,6144
Y_ 81,6400
Mvaa 180,180,120
s_nop 0
Ta 192,v3,36864
Ta 194,v3,38912
Mvaa 180,184,160
s_nop 0
Y_ 82,6656
Y_ 83,6912
Mvaa 180,188,200
s_nop 0
Ta 196,v3,40960
Ta 198,v3,43008
Mvaa 184,180,124
s_nop 0
Y_ 76,5120
Y_ 77,5376
Mvaa 184,184,164
s_nop 0
Ta 220,v3,45056
Ta 222,v3,47104
Mvaa 184,188,204
s_nop 0
Y_ 78,5632
Y_ 79,5888
Mvaa 188,180,128
buffer_load_dwordx4 v[152:155], v6, s[28:31], s56 offen offset:0 sc1
Mvaa 188,184,168
Mvaa 188,188,208
s_waitcnt vmcnt(63) lgkmcnt(0)
s_barrier
Mvaa 192,180,132
ds_read2st64_b32 v[96:97], v5 offset0:0 offset1:1
Mvaa 192,184,172
ds_read2st64_b32 v[98:99], v5 offset0:2 offset1:3
Mvaa 192,188,212
ds_read2st64_b32 v[104:105], v5 offset0:4 offset1:5
Mvaa 196,180,136
ds_read2st64_b32 v[106:107], v5 offset0:6 offset1:7
Mvaa 196,184,176
ds_read2st64_b32 v[112:113], v5 offset0:8 offset1:9
Mvaa 196,188,216
ds_read2st64_b32 v[114:115], v5 offset0:10 offset1:11
Mvaa 216,220,216
ds_read2st64_b32 v[120:121], v5 offset0:12 offset1:13
Mvaa 216,196,176
ds_read2st64_b32 v[122:123], v5 offset0:14 offset1:15
W_ 7
A_ v96,v96,v180
Mvaa 216,192,136
ds_read2st64_b32 v[128:129], v5 offset0:16 offset1:17
A_ v97,v97,v181
Mvaa 212,220,212
ds_read2st64_b32 v[130:131], v5 offset0:18 offset1:19
W_ 8
A_ v98,v98,v182
Mvaa 212,196,172
ds_read2st64_b32 v[132:133], v5 offset0:36 offset1:37
A_ v99,v99,v183
Mvaa 212,192,132
ds_read2st64_b32 v[134:135], v5 offset0:38 offset1:39
W_ 9
A_ v104,v104,v184
Mvaa 208,220,208
ds_read2st64_b32 v[124:125], v5 offset0:32 offset1:33
A_ v105,v105,v185
Mvaa 208,196,168
ds_read2st64_b32 v[126:127], v5 offset0:34 offset1:35
W_ 10
A_ v106,v106,v186
Mvaa 208,192,128
ds_read2st64_b32 v[116:117], v5 offset0:28 offset1:29
A_ v107,v107,v187
Mvaa 204,220,204
ds_read2st64_b32 v[118:119], v5 offset0:30 offset1:31
W_ 11
A_ v112,v112,v188
Mvaa 204,196,164
ds_read2st64_b32 v[108:109], v5 offset0:24 offset1:25
A_ v113,v113,v189
Mvaa 204,192,124
ds_read2st64_b32 v[110:111], v5 offset0:26 offset1:27
W_ 12
A_ v114,v114,v190
Mvaa 200,220,200
ds_read2st64_b32 v[100:101], v5 offset0:20 offset1:21
A_ v115,v115,v191
Mvaa 200,196,160
ds_read2st64_b32 v[102:103], v5 offset0:22 offset1:23
W_ 13
A_ v120,v120,v192
Mvaa 200,192,120
A_ v121,v121,v193
W_ 12
A_ v122,v122,v194
A_ v123,v123,v195
W_ 11
A_ v128,v128,v196
A_ v129,v129,v197
W_ 10
A_ v130,v130,v198
A_ v131,v131,v199
s_waitcnt vmcnt(5)
Mvav 164,70,36
W_ 9
A_ v132,v132,v216
A_ v133,v133,v217
Mvav 168,68,36
W_ 8
A_ v134,v134,v218
A_ v135,v135,v219
Mvav 172,70,40
W_ 7
A_ v124,v124,v212
A_ v125,v125,v213
Mvav 176,68,40
W_ 6
A_ v126,v126,v214
A_ v127,v127,v215
Mvav 164,70,156
W_ 5
A_ v116,v116,v208
A_ v117,v117,v209
Mvav 168,68,156
W_ 4
A_ v118,v118,v210
A_ v119,v119,v211
Mvav 172,70,160
W_ 3
A_ v108,v108,v204
A_ v109,v109,v205
Mvav 176,68,160
W_ 2
A_ v110,v110,v206
A_ v111,v111,v207
Mvav 96,70,16
W_ 1
A_ v100,v100,v200
A_ v101,v101,v201
Mvav 104,70,20
W_ 0
A_ v102,v102,v202
A_ v103,v103,v203
Mvav 112,70,24
C_ 220,164,165
C_ 221,166,167
Mvav 120,70,28
C_ 222,168,169
C_ 223,170,171
Mvav 128,70,32
v_permlane16_swap_b32 v220, v222
v_permlane16_swap_b32 v221, v223
Mvav 132,68,32
buffer_store_dwordx4 v[220:223], v4, s[32:35], s9 offen sc1
Mvav 124,68,28
C_ 228,172,173
C_ 229,174,175
Mvav 116,68,24
C_ 230,176,177
C_ 231,178,179
Mvav 108,68,20
v_permlane16_swap_b32 v228, v230
v_permlane16_swap_b32 v229, v231
Mvav 100,68,16
buffer_store_dwordx4 v[228:231], v4, s[32:35], s10 offen sc1
s_waitcnt vmcnt(2)
.Lsel6:
Nvav 180,70,136,96
Nvav 188,70,140,104
Nvav 196,70,144,112
Nvav 204,70,148,120
Nvav 212,70,152,128
Nvav 184,68,136,100
s_nop 3
A_ v10,v180,v188
Nvav 192,68,140,108
A_ v11,v196,v204
Nvav 200,68,144,116
A_ v10,v10,v212
v_cmp_u_f32 vcc, v10, v11
Nvav 208,68,148,124
s_cbranch_vccnz .Lretry
.Lgood:
Nvav 216,68,152,132
C_ 180,180,181
C_ 181,182,183
C_ 182,184,185
C_ 183,186,187
s_nop 0
v_permlane16_swap_b32 v180, v182
v_permlane16_swap_b32 v181, v183
buffer_store_dwordx4 v[180:183], v4, s[32:35], s8 offen sc1
C_ 188,188,189
C_ 189,190,191
C_ 190,192,193
C_ 191,194,195
S_ s8,s8,40960
v_permlane16_swap_b32 v188, v190
v_permlane16_swap_b32 v189, v191
buffer_store_dwordx4 v[188:191], v4, s[32:35], s8 offen sc1
C_ 196,196,197
C_ 197,198,199
C_ 198,200,201
C_ 199,202,203
S_ s8,s8,40960
v_permlane16_swap_b32 v196, v198
v_permlane16_swap_b32 v197, v199
buffer_store_dwordx4 v[196:199], v4, s[32:35], s8 offen sc1
C_ 204,204,205
C_ 205,206,207
C_ 206,208,209
C_ 207,210,211
S_ s8,s8,40960
v_permlane16_swap_b32 v204, v206
v_permlane16_swap_b32 v205, v207
buffer_store_dwordx4 v[204:207], v4, s[32:35], s8 offen sc1
C_ 212,212,213
C_ 213,214,215
C_ 214,216,217
C_ 215,218,219
S_ s8,s8,40960
v_permlane16_swap_b32 v212, v214
v_permlane16_swap_b32 v213, v215
buffer_store_dwordx4 v[212:215], v4, s[32:35], s8 offen sc1
s_endpgm
.Lspin0:
s_cmp_eq_u32 s57, 0
s_cbranch_scc1 .Lspun0
s_sub_u32 s57, s57, 1
s_sleep 4
buffer_load_dword v11, v8, s[92:95], s96 offen offset:384 sc1
s_waitcnt vmcnt(0)
v_cmp_eq_u32 vcc, s62, v11
s_nop 1
s_bitcmp1_b32 vcc_lo, s75
s_cbranch_scc0 .Lspin0
s_branch .Lspun0
.Lspin1:
s_cmp_eq_u32 s57, 0
s_cbranch_scc1 .Lspun1
s_sub_u32 s57, s57, 1
s_sleep 4
buffer_load_dword v11, v8, s[92:95], s96 offen offset:384 sc1
s_waitcnt vmcnt(0)
v_cmp_eq_u32 vcc, s62, v11
s_nop 1
s_bitcmp1_b32 vcc_lo, s75
s_cbranch_scc0 .Lspin1
s_branch .Lspun1
.Lspin2:
s_cmp_eq_u32 s57, 0
s_cbranch_scc1 .Lspun2
s_sub_u32 s57, s57, 1
s_sleep 4
buffer_load_dword v11, v8, s[92:95], s96 offen offset:384 sc1
s_waitcnt vmcnt(0)
v_cmp_eq_u32 vcc, s62, v11
s_nop 1
s_bitcmp1_b32 vcc_lo, s75
s_cbranch_scc0 .Lspin2
s_branch .Lspun2
.Lspin3:
s_cmp_eq_u32 s57, 0
s_cbranch_scc1 .Lspun3
s_sub_u32 s57, s57, 1
s_sleep 4
buffer_load_dword v11, v8, s[92:95], s96 offen offset:384 sc1
s_waitcnt vmcnt(0)
v_cmp_eq_u32 vcc, s62, v11
s_nop 1
s_bitcmp1_b32 vcc_lo, s75
s_cbranch_scc0 .Lspin3
s_branch .Lspun3
.Lspin4:
s_cmp_eq_u32 s57, 0
s_cbranch_scc1 .Lspun4
s_sub_u32 s57, s57, 1
s_sleep 4
buffer_load_dword v11, v8, s[92:95], s96 offen offset:384 sc1
s_waitcnt vmcnt(0)
v_cmp_eq_u32 vcc, s62, v11
s_nop 1
s_bitcmp1_b32 vcc_lo, s75
s_cbranch_scc0 .Lspin4
s_branch .Lspun4
.Lspin5:
s_cmp_eq_u32 s57, 0
s_cbranch_scc1 .Lspun5
s_sub_u32 s57, s57, 1
s_sleep 4
buffer_load_dword v11, v8, s[92:95], s96 offen offset:384 sc1
s_waitcnt vmcnt(0)
v_cmp_eq_u32 vcc, s62, v11
s_nop 1
s_bitcmp1_b32 vcc_lo, s75
s_cbranch_scc0 .Lspin5
s_branch .Lspun5
.Lspin6:
s_cmp_eq_u32 s57, 0
s_cbranch_scc1 .Lspun6
s_sub_u32 s57, s57, 1
s_sleep 4
buffer_load_dword v11, v8, s[92:95], s96 offen offset:384 sc1
s_waitcnt vmcnt(0)
v_cmp_eq_u32 vcc, s62, v11
s_nop 1
s_bitcmp1_b32 vcc_lo, s86
s_cbranch_scc0 .Lspin6
s_branch .Lspun6
.Lretry:
s_cmp_eq_u32 s57, 0
s_cbranch_scc1 .Lgood
s_sub_u32 s57, s57, 1
v_max_u32 v10, v136, v137
v_max3_u32 v10, v10, v138, v139
v_max3_u32 v10, v10, v140, v141
v_max3_u32 v10, v10, v142, v143
v_max3_u32 v10, v10, v144, v145
v_max3_u32 v10, v10, v146, v147
v_max3_u32 v10, v10, v148, v149
v_max3_u32 v10, v10, v150, v151
v_max3_u32 v10, v10, v152, v153
v_max3_u32 v10, v10, v154, v155
v_cmp_eq_u32 vcc, -1, v10
s_nop 4
s_cbranch_vccz .Lgood
s_nop 7
s_nop 7
S_ s53,s80,4096
buffer_load_dwordx4 v[136:139], v6, s[28:31], s80 offen offset:0 sc1
buffer_load_dwordx4 v[140:143], v6, s[28:31], s80 offen offset:1024 sc1
buffer_load_dwordx4 v[144:147], v6, s[28:31], s80 offen offset:2048 sc1
buffer_load_dwordx4 v[148:151], v6, s[28:31], s80 offen offset:3072 sc1
buffer_load_dwordx4 v[152:155], v6, s[28:31], s53 offen offset:0 sc1
s_waitcnt vmcnt(0)
s_branch .Lsel6
.Ldone:
s_waitcnt vmcnt(0)
.purgem S_
.purgem W_
.purgem L_
.purgem X_
.purgem R_
.purgem Ta
.purgem Da
.purgem Zvaa
.purgem Mvaa
.purgem U_
.purgem C_
.purgem Ln
.purgem Dv
.purgem Zvvv
.purgem Mvvv
.purgem B_
.purgem E_
.purgem Nvvv
.purgem Tv
.purgem Zaav
.purgem Zavv
.purgem A_
.purgem Mavv
.purgem Zvav
.purgem Mvav
.purgem Y_
.purgem Zaaa
.purgem Maaa
.purgem Maav
.purgem Nvav
)A"
    : "+v"(tid)
    : "s"(__builtin_amdgcn_kernarg_segment_ptr()), "s"(blockIdx.x)
    : "v1", "v2", "v3", "v4", "v5", "v6", "v7", "v8", "v9", "v10", "v11", "v12", "v13", "v14", "v15", "v16", "v17", "v18", "v19", "v20", "v21", "v22", "v23", "v24", "v25", "v26", "v27", "v28", "v29", "v30", "v31", "v32", "v33", "v34", "v35", "v36", "v37", "v38", "v39", "v40", "v41", "v42", "v43", "v44", "v45", "v46", "v47", "v48", "v49", "v50", "v51", "v52", "v53", "v54", "v55", "v56", "v57", "v58", "v59", "v60", "v61", "v62", "v63", "v64", "v65", "v66", "v67", "v68", "v69", "v70", "v71", "v72", "v73", "v74", "v75", "v76", "v77", "v78", "v79", "v80", "v81", "v82", "v83", "v84", "v85", "v86", "v87", "v88", "v89", "v90", "v91", "v92", "v93", "v94", "v95", "v96", "v97", "v98", "v99", "v100", "v101", "v102", "v103", "v104", "v105", "v106", "v107", "v108", "v109", "v110", "v111", "v112", "v113", "v114", "v115", "v116", "v117", "v118", "v119", "v120", "v121", "v122", "v123", "v124", "v125", "v126", "v127", "v128", "v129", "v130", "v131", "v132", "v133", "v134", "v135", "v136", "v137", "v138", "v139", "v140", "v141", "v142", "v143", "v144", "v145", "v146", "v147", "v148", "v149", "v150", "v151", "v152", "v153", "v154", "v155", "v156", "v157", "v158", "v159", "v160", "v161", "v162", "v163", "v164", "v165", "v166", "v167", "v168", "v169", "v170", "v171", "v172", "v173", "v174", "v175", "v176", "v177", "v178", "v179", "v180", "v181", "v182", "v183", "v184", "v185", "v186", "v187", "v188", "v189", "v190", "v191", "v192", "v193", "v194", "v195", "v196", "v197", "v198", "v199", "v200", "v201", "v202", "v203", "v204", "v205", "v206", "v207", "v208", "v209", "v210", "v211", "v212", "v213", "v214", "v215", "v216", "v217", "v218", "v219", "v220", "v221", "v222", "v223", "v224", "v225", "v226", "v227", "v228", "v229", "v230", "v231", "v232", "v233", "v234", "v235", "v236", "v237", "v238", "v239", "v240", "v241", "v242", "v243", "v244", "v245", "v246", "v247", "v248", "v249", "v250", "v251", "v252", "v253", "v254", "v255", "a0", "a1", "a2", "a3", "a4", "a5", "a6", "a7", "a8", "a9", "a10", "a11", "a12", "a13", "a14", "a15", "a16", "a17", "a18", "a19", "a20", "a21", "a22", "a23", "a24", "a25", "a26", "a27", "a28", "a29", "a30", "a31", "a32", "a33", "a34", "a35", "a36", "a37", "a38", "a39", "a40", "a41", "a42", "a43", "a44", "a45", "a46", "a47", "a48", "a49", "a50", "a51", "a52", "a53", "a54", "a55", "a56", "a57", "a58", "a59", "a60", "a61", "a62", "a63", "a64", "a65", "a66", "a67", "a68", "a69", "a70", "a71", "a72", "a73", "a74", "a75", "a76", "a77", "a78", "a79", "a80", "a81", "a82", "a83", "a84", "a85", "a86", "a87", "a88", "a89", "a90", "a91", "a92", "a93", "a94", "a95", "a96", "a97", "a98", "a99", "a100", "a101", "a102", "a103", "a104", "a105", "a106", "a107", "a108", "a109", "a110", "a111", "a112", "a113", "a114", "a115", "a116", "a117", "a118", "a119", "a120", "a121", "a122", "a123", "a124", "a125", "a126", "a127", "a128", "a129", "a130", "a131", "a132", "a133", "a134", "a135", "a136", "a137", "a138", "a139", "a140", "a141", "a142", "a143", "a144", "a145", "a146", "a147", "a148", "a149", "a150", "a151", "a152", "a153", "a154", "a155", "a156", "a157", "a158", "a159", "a160", "a161", "a162", "a163", "a164", "a165", "a166", "a167", "a168", "a169", "a170", "a171", "a172", "a173", "a174", "a175", "a176", "a177", "a178", "a179", "a180", "a181", "a182", "a183", "a184", "a185", "a186", "a187", "a188", "a189", "a190", "a191", "a192", "a193", "a194", "a195", "a196", "a197", "a198", "a199", "a200", "a201", "a202", "a203", "a204", "a205", "a206", "a207", "a208", "a209", "a210", "a211", "a212", "a213", "a214", "a215", "a216", "a217", "a218", "a219", "a220", "a221", "a222", "a223", "a224", "a225", "a226", "a227", "a228", "a229", "a230", "a231", "a232", "a233", "a234", "a235", "a236", "a237", "a238", "a239", "a240", "a241", "a242", "a243", "a244", "a245", "a246", "a247", "a248", "a249", "a250", "a251", "a252", "a253", "a254", "a255", "s3", "s4", "s5", "s6", "s7", "s8", "s9", "s10", "s11", "s12", "s13", "s14", "s15", "s16", "s17", "s18", "s19", "s20", "s21", "s22", "s23", "s24", "s25", "s26", "s27", "s28", "s29", "s30", "s31", "s32", "s33", "s34", "s35", "s36", "s37", "s38", "s39", "s40", "s41", "s42", "s43", "s44", "s45", "s46", "s47", "s48", "s49", "s50", "s51", "s52", "s53", "s54", "s55", "s56", "s57", "s58", "s59", "s60", "s61", "s62", "s63", "s64", "s65", "s66", "s67", "s68", "s69", "s70", "s71", "s72", "s73", "s74", "s75", "s76", "s77", "s78", "s79", "s80", "s81", "s82", "s83", "s84", "s85", "s86", "s87", "s88", "s89", "s90", "s91", "s92", "s93", "s94", "s95", "s96", "s97", "s98", "s99", "s100", "s101", "vcc", "scc", "memory");
}

extern "C" void kernel_launch(void* const* d_in, const int* in_sizes, int n_in, void* d_out, int out_size, void* d_ws, size_t ws_size, hipStream_t stream) {
  Args P;
  for (int br = 0; br < 2; ++br) {
    P.p[br][0] = d_in[5 * br]; P.p[br][1] = d_in[5 * br + 3]; P.p[br][2] = d_in[5 * br + 1]; P.p[br][3] = d_in[5 * br + 2]; P.p[br][4] = d_in[5 * br + 4];
    P.p[br][5] = d_ws; P.p[br][6] = (char*)d_out + br * 1280; P.p[br][7] = d_ws;
  }
  (void)hipFuncSetAttribute((const void*)fused_kernel, hipFuncAttributeMaxDynamicSharedMemorySize, LDS1);
  fused_kernel<<<256, 256, LDS1, stream>>>(P);
}
```

```cpp
#include <hip/hip_runtime.h>
#include <hip/hip_bf16.h>

constexpr unsigned LDS1 = 160 * 1024;

struct Args { const void* p[2][8]; };

__global__ __launch_bounds__(256, 1) void fused_kernel(Args P) {
  unsigned tid = threadIdx.x;
  asm volatile(
R"A(.macro S_ a,b,c
s_add_u32 \a, \b, \c
.endm
.macro W_ a
s_waitcnt lgkmcnt(\a)
.endm
.macro L_ x,r,s,o
buffer_load_dwordx4 \x, s[\r:\r+3], \s offen offset:\o lds
.endm
.macro X_ a,b
v_accvgpr_write_b32 \a, \b
.endm
.macro R_ a,b
v_accvgpr_read_b32 \a, \b
.endm
.macro Ta d,x,o
ds_read_b64_tr_b16 a[\d:\d+1], \x offset:\o
.endm
.macro Da d,x,o
ds_read_b128 a[\d:\d+3], \x offset:\o
.endm
.macro Zvaa d,a,b
v_mfma_f32_16x16x32_bf16 v[\d:\d+3], a[\a:\a+3], a[\b:\b+3], 0
.endm
.macro Mvaa d,a,b
v_mfma_f32_16x16x32_bf16 v[\d:\d+3], a[\a:\a+3], a[\b:\b+3], v[\d:\d+3]
.endm
.macro U_ a,b,c
v_mul_f32 \a, \b, \c
.endm
.macro C_ d,a,b
v_cvt_pk_bf16_f32 v\d, v\a, v\b
.endm
.macro Ln x,r,s,o
buffer_load_dwordx4 \x, s[\r:\r+3], \s offen offset:\o nt lds
.endm
.macro Dv d,x,o
ds_read_b128 v[\d:\d+3], \x offset:\o
.endm
.macro Zvvv d,a,b
v_mfma_f32_16x16x32_bf16 v[\d:\d+3], v[\a:\a+3], v[\b:\b+3], 0
.endm
.macro Mvvv d,a,b
v_mfma_f32_16x16x32_bf16 v[\d:\d+3], v[\a:\a+3], v[\b:\b+3], v[\d:\d+3]
.endm
.macro B_ a,b,c
v_sub_f32 \a, \b, \c
.endm
.macro E_ r
v_exp_f32 v\r, v\r
.endm
.macro Nvvv d,a,b,c
v_mfma_f32_16x16x32_bf16 v[\d:\d+3], v[\a:\a+3], v[\b:\b+3], v[\c:\c+3]
.endm
.macro Tv d,x,o
ds_read_b64_tr_b16 v[\d:\d+1], \x offset:\o
.endm
.macro Zaav d,a,b
v_mfma_f32_16x16x32_bf16 a[\d:\d+3], a[\a:\a+3], v[\b:\b+3], 0
.endm
.macro Zavv d,a,b
v_mfma_f32_16x16x32_bf16 a[\d:\d+3], v[\a:\a+3], v[\b:\b+3], 0
.endm
.macro A_ a,b,c
v_add_f32 \a, \b, \c
.endm
.macro Mavv d,a,b
v_mfma_f32_16x16x32_bf16 a[\d:\d+3], v[\a:\a+3], v[\b:\b+3], a[\d:\d+3]
.endm
.macro Zvav d,a,b
v_mfma_f32_16x16x32_bf16 v[\d:\d+3], a[\a:\a+3], v[\b:\b+3], 0
.endm
.macro Mvav d,a,b
v_mfma_f32_16x16x32_bf16 v[\d:\d+3], a[\a:\a+3], v[\b:\b+3], v[\d:\d+3]
.endm
.macro Y_ r,o
ds_write_addtid_b32 v\r offset:\o
.endm
.macro Zaaa d,a,b
v_mfma_f32_16x16x32_bf16 a[\d:\d+3], a[\a:\a+3], a[\b:\b+3], 0
.endm
.macro Maaa d,a,b
v_mfma_f32_16x16x32_bf16 a[\d:\d+3], a[\a:\a+3], a[\b:\b+3], a[\d:\d+3]
.endm
.macro Maav d,a,b
v_mfma_f32_16x16x32_bf16 a[\d:\d+3], a[\a:\a+3], v[\b:\b+3], a[\d:\d+3]
.endm
.macro Nvav d,a,b,c
v_mfma_f32_16x16x32_bf16 v[\d:\d+3], a[\a:\a+3], v[\b:\b+3], v[\c:\c+3]
.endm
.p2align 12
s_and_b32 s3, %2, 4
s_lshl_b32 s3, s3, 4
s_load_dwordx16 s[8:23], %1, s3
s_memrealtime s[100:101]
s_memtime s[90:91]
v_readfirstlane_b32 s4, %0
v_and_b32 v16, 63, %0
v_lshlrev_b32 v10, 4, v16
s_mov_b32 s7, %2
s_lshr_b32 s4, s4, 6
.Lcode0:
s_getpc_b64 s[44:45]
s_mov_b32 s46, .Ldone-.Lcode0
s_mov_b32 s47, 0x00020000
s_lshr_b32 s66, s7, 3
s_lshl_b32 s66, s66, 2
S_ s66,s66,s4
s_lshl_b32 s66, s66, 11
v_min_u32 v40, 15, v16
v_lshlrev_b32 v40, 7, v40
buffer_load_dword v247, v40, s[44:47], s66 offen
s_and_b32 s3, s7, 1
s_lshl_b32 s3, s3, 3
s_lshr_b32 s53, s7, 5
S_ s3,s3,s53
s_and_b32 s6, s7, 2
s_lshl_b32 s6, s6, 1
s_bfe_u32 s53, s7, 0x20003
S_ s6,s6,s53
s_and_b32 s5, s7, 4
s_lshl_b32 s5, s5, 2
S_ s5,s5,s3
s_mul_i32 s50, s4, 5120
s_mul_i32 s60, s4, 51200
s_lshr_b32 s77, s4, 1
s_and_b32 s53, s4, 1
s_mul_i32 s59, s53, 393216
s_lshl_b32 s52, s53, 11
s_mul_i32 s61, s53, 6144
s_mul_i32 s51, s77, 61440
S_ s55,s51,40960
S_ s51,s55,s61
s_sub_u32 s52, s51, s52
s_lshl_b32 s86, s53, 2
s_mul_i32 s56, s53, 10240
v_lshrrev_b32 v17, 3, v16
v_and_b32 v18, 7, v16
v_lshrrev_b32 v23, 4, v16
v_and_b32 v24, 1, v23
v_lshlrev_b32 v24, 2, v24
v_xor_b32 v24, v18, v24
v_mul_u32_u24 v6, 12288, v17
v_lshl_add_u32 v6, v24, 4, v6
v_add_u32 v24, s86, v23
v_xor_b32 v24, v18, v24
v_mul_u32_u24 v4, 1280, v17
v_lshl_add_u32 v4, v24, 4, v4
v_xor_b32 v5, 64, v4
s_getreg_b32 s53, hwreg(HW_REG_XCC_ID, 0, 4)
s_bitcmp1_b32 0xaaaa, s53
s_cselect_b32 s58, 2, 0
s_cselect_b32 s89, 1, 0
s_cselect_b32 s53, 768, 0
S_ s59,s59,s53
s_lshl_b32 s54, s6, 2
S_ s54,s54,s77
s_mul_i32 s86, s54, 384
s_mul_i32 s87, s3, 204800
s_lshl_b32 s53, s3, 5
S_ s53,s53,s54
s_mul_i32 s85, s53, 196608
s_mov_b32 s30, 7853184
s_mov_b32 s31, 0x00020000
s_mov_b32 s26, 204800
s_mov_b32 s27, 0x00020000
W_ 0
s_mov_b32 s98, s100
s_mov_b32 s99, 0
s_mov_b32 s88, s90
S_ s28,s10,s86
s_addc_u32 s29, s11, 0
S_ s24,s8,s87
s_addc_u32 s25, s9, 0
s_buffer_load_dword s67, s[24:27], s60
s_mul_i32 s64, s66, 30
s_buffer_load_dword s67, s[28:31], s64
s_mov_b32 s11, 0
S_ s65,s59,0
S_ m0,s52,8192
s_mov_b64 s[92:93], s[18:19]
s_mov_b32 s53, 251658240
L_ v6,28,s65,0
S_ s65,s59,98304
S_ m0,s52,9216
S_ s92,s92,s53
s_addc_u32 s93, s93, 0
L_ v6,28,s65,0
S_ s65,s59,196608
S_ m0,s52,10240
s_mov_b32 s94, 131072
s_mov_b32 s95, 0x00020000
L_ v6,28,s65,0
S_ s65,s59,294912
S_ m0,s52,11264
s_lshl_b32 s96, s5, 3
S_ s96,s96,s6
L_ v6,28,s65,0
S_ s65,s60,0
S_ m0,s50,0
s_lshl_b32 s96, s96, 9
v_cmp_gt_u32 vcc, 8, v16
L_ v4,24,s65,0
S_ s65,s60,10240
S_ m0,s50,1024
s_lshl_b32 s54, s5, 12
v_lshl_add_u32 v24, v16, 9, s54
L_ v5,24,s65,0
S_ s65,s60,20480
S_ m0,s50,2048
v_mov_b32 v12, 0x7fff0000
v_cndmask_b32 v12, v12, v24, vcc
L_ v4,24,s65,0
S_ s65,s60,30720
S_ m0,s50,3072
s_lshl_b32 s54, s6, 2
v_add_u32 v13, s54, v12
L_ v5,24,s65,0
S_ s65,s60,40960
S_ m0,s50,4096
v_lshrrev_b32 v19, 2, v10
v_mul_u32_u24 v25, 384, v17
L_ v4,24,s65,0
s_load_dword s67, s[12:13], s85
s_load_dword s67, s[14:15], s85
S_ s64,s85,393216
s_load_dword s67, s[12:13], s64
s_load_dword s67, s[14:15], s64
s_lshl_b32 s64, s6, 2
S_ s64,s64,s4
s_mul_i32 s64, s64, 245760
s_load_dword s67, s[16:17], s64
s_cmp_eq_u32 s4, 0
s_cbranch_scc0 .Lhs0a
buffer_load_dword v20, v19, s[92:95], s96 offen offset:128 sc1
buffer_load_dword v21, v19, s[92:95], s96 offen offset:384 sc1
buffer_load_dword a252, v19, s[92:95], s96 offen offset:256 sc1
buffer_load_dword a253, v13, s[92:95], 0 offen offset:256 sc1
buffer_load_dword v22, off, s[92:95], s96 offset:0 sc1
.Lhs0a:
S_ s65,s59,0
S_ m0,s52,16256
v_xor_b32 v24, v18, v23
v_lshl_add_u32 v7, v24, 4, v25
v_lshlrev_b32 v24, 1, v23
v_xor_b32 v24, v18, v24
v_lshl_add_u32 v9, v24, 4, v25
v_and_b32 v26, 15, v16
v_lshrrev_b32 v27, 2, v26
v_and_b32 v28, 3, v16
v_lshrrev_b32 v24, 1, v26
v_xor_b32 v24, v23, v24
L_ v6,28,s65,128
S_ s65,s59,98304
S_ m0,s52,17280
v_lshlrev_b32 v24, 4, v24
v_lshl_add_u32 v24, v18, 7, v24
v_lshrrev_b32 v25, 3, v26
v_lshl_add_u32 v2, v25, 10, v24
v_add_u32 v2, s56, v2
v_mul_u32_u24 v1, 3072, v25
v_add3_u32 v1, v1, v24, s55
v_lshlrev_b32 v24, 2, v25
v_xor_b32 v24, v28, v24
v_lshlrev_b32 v24, 4, v24
L_ v6,28,s65,128
S_ s65,s59,196608
S_ m0,s52,18304
v_lshl_add_u32 v24, v27, 7, v24
v_lshl_add_u32 %0, v23, 10, v24
v_add_u32 %0, s55, %0
v_and_b32 v29, 1, v23
v_lshl_add_u32 v24, v29, 1, v25
v_lshlrev_b32 v24, 1, v24
v_lshrrev_b32 v30, 1, v28
v_xor_b32 v24, v30, v24
v_lshlrev_b32 v24, 4, v24
v_lshl_add_u32 v29, v29, 2, v27
L_ v6,28,s65,128
S_ s65,s59,294912
S_ m0,s52,19328
v_lshl_add_u32 v24, v29, 7, v24
v_and_b32 v29, 1, v28
v_lshl_add_u32 v24, v29, 3, v24
v_lshrrev_b32 v29, 1, v23
v_mul_u32_u24 v3, 3072, v29
v_add3_u32 v3, v3, v24, s55
X_ a242,%0
X_ a244,v1
X_ a243,v2
X_ a245,v3
L_ v6,28,s65,128
S_ s65,s59,0
S_ m0,s52,24320
X_ a240,v4
X_ a248,v10
X_ a241,v12
X_ a251,v13
v_xor_b32 v1, 64, %0
v_xor_b32 v3, 64, v2
v_xor_b32 v8, 64, v7
S_ s32,s12,s85
s_addc_u32 s33, s13, 0
s_mov_b32 s34, 589824
L_ v6,28,s65,256
S_ s65,s59,98304
S_ m0,s52,25344
s_mov_b32 s35, 0x00020000
S_ s36,s14,s85
s_addc_u32 s37, s15, 0
s_mov_b32 s38, 589824
s_mov_b32 s39, 0x00020000
s_lshl_b32 s87, s87, 1
S_ s20,s20,s87
s_addc_u32 s21, s21, 0
s_mov_b32 s57, 20000
s_mov_b32 s48, 1037384597
L_ v6,28,s65,256
S_ s65,s59,196608
S_ m0,s52,26368
s_mov_b32 s49, 1037384597
s_mul_i32 s53, s5, 1638400
S_ s53,s53,134217728
S_ s22,s18,s53
s_addc_u32 s23, s19, 0
s_lshl_b32 s53, s6, 3
s_lshr_b32 s54, s7, 5
S_ s53,s53,s54
s_lshl_b32 s53, s53, 2
S_ s53,s53,s4
L_ v6,28,s65,256
S_ s65,s59,294912
S_ m0,s52,27392
s_mul_i32 s87, s53, 30720
s_lshl_b32 s53, s6, 2
S_ s53,s53,s77
s_mul_i32 s72, s53, 245760
s_and_b32 s54, s4, 1
s_mul_i32 s53, s54, 61440
S_ s72,s72,s53
s_lshl_b32 s53, s77, 6
s_xor_b32 s83, s53, 64
s_xor_b32 s53, s4, 2
L_ v6,28,s65,256
S_ s65,s60,128
S_ m0,s50,20480
s_mul_i32 s75, s53, 10240
s_cmp_eq_u32 s4, 0
s_cselect_b32 s73, 0, 2147418112
s_mul_i32 s53, s4, 5120
s_mul_i32 s78, s6, 204800
S_ s78,s78,s53
s_and_b32 s84, s6, 7
s_mul_i32 s84, s84, 20480
S_ s84,s84,s53
s_mul_i32 s53, s6, 5120
L_ v4,24,s65,0
S_ s65,s60,10368
S_ m0,s50,21504
S_ s53,s53,163840
s_lshl_b32 s54, s4, 10
S_ s74,s53,s54
S_ s53,s53,4096
s_cmp_eq_u32 s4, 0
s_cselect_b32 s82, s53, 4026531840
s_and_b32 s53, s4, 1
s_mul_i32 s53, s53, 204800
s_lshl_b32 s54, s6, 7
S_ s53,s53,s54
L_ v5,24,s65,0
S_ s65,s60,20608
S_ m0,s50,22528
s_lshl_b32 s54, s77, 6
S_ s8,s53,s54
s_and_b32 s53, s6, 1
s_mul_i32 s53, s53, 204800
s_lshr_b32 s54, s6, 1
s_lshl_b32 s54, s54, 6
S_ s53,s53,s54
S_ s53,s53,1024
s_mul_i32 s54, s4, 40960
S_ s9,s53,s54
L_ v4,24,s65,0
S_ s65,s60,30848
S_ m0,s50,23552
S_ s53,s53,163840
s_cmp_eq_u32 s4, 0
s_cselect_b32 s10, s53, 4026531840
R_ v12,a248
v_lshrrev_b32 v12, 4, v12
v_min_u32 v10, 11, v12
s_mul_i32 s56, s4, 12
v_add_u32 v10, s56, v10
v_mul_u32_u24 v11, 43691, v10
v_lshrrev_b32 v11, 18, v11
L_ v5,24,s65,0
S_ s65,s60,41088
S_ m0,s50,24576
v_lshlrev_b32 v10, 7, v10
v_mul_u32_u24 v11, 11520, v11
v_add_u32 v10, v10, v11
v_min_u32 v12, 9, v12
s_and_b32 s56, s6, 3
s_lshl_b32 s56, s56, 2
S_ s56,s56,s4
s_mul_i32 s56, s56, 10
v_add_u32 v12, s56, v12
v_mul_u32_u24 v12, 1280, v12
L_ v4,24,s65,0
S_ s65,s59,786432
S_ m0,s52,0
s_lshr_b32 s56, s7, 5
s_mul_i32 s56, s56, 98304
s_and_b32 s55, s59, 4095
S_ s56,s56,s55
s_mul_i32 s55, s77, 384
s_sub_u32 s56, s56, s55
s_mov_b32 s63, 0
L_ v6,28,s65,0
S_ s65,s59,884736
S_ m0,s52,1024
s_nop 0
L_ v6,28,s65,0
S_ s65,s59,983040
S_ m0,s52,2048
s_nop 0
L_ v6,28,s65,0
S_ s65,s59,1081344
S_ m0,s52,3072
s_nop 0
L_ v6,28,s65,0
S_ s55,s56,786432
buffer_load_dword a254, v10, s[28:31], s55 offen
S_ s55,s56,1572864
buffer_load_dword a254, v10, s[28:31], s55 offen
buffer_load_dword a254, v12, s[24:27], s63 offen offset:256
S_ s55,s56,2359296
buffer_load_dword a254, v10, s[28:31], s55 offen
buffer_load_dword a254, v12, s[24:27], s63 offen offset:384
s_waitcnt vmcnt(22) lgkmcnt(0)
s_barrier
s_memtime s[90:91]
s_memrealtime s[68:69]
Ta 20,%0,8192
Ta 22,%0,8704
Ta 24,%0,12288
Ta 26,%0,12800
Da 60,v2,0
Da 100,v3,0
Da 64,v2,2048
Da 104,v3,2048
Da 68,v2,4096
Da 108,v3,4096
Da 72,v2,6144
Da 112,v3,6144
Da 76,v2,8192
Da 116,v3,8192
Ta 28,%0,8200
Ta 30,%0,8712
Ta 32,%0,12296
Ta 34,%0,12808
s_cmp_eq_u32 s4, 0
s_cbranch_scc0 .Lhs0b
v_readfirstlane_b32 s62, v22
.Lhs0l:
s_nop 3
S_ s62,s62,1
v_cmp_eq_u32 vcc, s62, v20
s_nop 1
s_or_b32 s53, vcc_lo, vcc_hi
v_cmp_eq_u32 vcc, s62, v21
s_nop 1
s_or_b32 s54, vcc_lo, vcc_hi
s_or_b32 s53, s53, s54
s_cbranch_scc1 .Lhs0l
v_mov_b32 v22, s62
s_mov_b64 exec, 1
s_nop 1
buffer_store_dword v22, off, s[92:95], s96 sc1
s_mov_b64 exec, -1
s_nop 1
.Lhs0b:
s_cmp_eq_u32 s58, 0
s_cbranch_scc1 .Lq
.Lqy:
W_ 13
Zvaa 16,20,60
s_nop 0
Ta 36,v1,8192
Ta 38,v1,8704
W_ 14
Mvaa 16,24,100
R_ v12,a248
W_ 13
Zvaa 24,20,64
v_lshrrev_b32 v12, 4, v12
W_ 12
Mvaa 24,24,104
s_nop 0
Ta 224,v1,12288
Ta 226,v1,12800
W_ 13
Zvaa 32,20,68
v_min_u32 v10, 11, v12
W_ 12
Mvaa 32,24,108
s_mul_i32 s56, s4, 12
W_ 11
Zvaa 40,20,72
v_add_u32 v10, s56, v10
W_ 10
Mvaa 40,24,112
v_mul_u32_u24 v11, 43691, v10
W_ 9
Zvaa 48,20,76
v_lshrrev_b32 v11, 18, v11
W_ 8
Mvaa 48,24,116
W_ 4
v_lshlrev_b32 v10, 7, v10
Zvaa 52,32,116
s_nop 0
Ta 20,v1,8200
Ta 22,v1,8712
Mvaa 52,28,76
v_mul_u32_u24 v11, 11520, v11
Zvaa 44,32,112
v_add_u32 v10, v10, v11
Mvaa 44,28,72
s_nop 0
Ta 24,v1,12296
Ta 26,v1,12808
Zvaa 36,32,108
v_min_u32 v12, 9, v12
Mvaa 36,28,68
s_and_b32 s56, s6, 3
Zvaa 28,32,104
s_lshl_b32 s56, s56, 2
Mvaa 28,28,64
S_ s56,s56,s4
Zvaa 20,32,100
s_waitcnt vmcnt(18) lgkmcnt(0)
s_barrier
s_mul_i32 s56, s56, 10
Mvaa 20,28,60
S_ s65,s59,786432
v_add_u32 v12, s56, v12
Zvaa 56,36,60
S_ m0,s52,8064
Ta 28,%0,16384
Ta 30,%0,16896
Mvaa 56,224,100
L_ v6,28,s65,128
Zvaa 64,36,64
S_ s65,s59,884736
v_mul_u32_u24 v12, 1280, v12
Mvaa 64,224,104
S_ m0,s52,9088
Ta 32,%0,20480
Ta 34,%0,20992
Zvaa 72,36,68
s_lshr_b32 s56, s7, 5
Mvaa 72,224,108
L_ v6,28,s65,128
Zvaa 80,36,72
S_ s65,s59,983040
s_mul_i32 s56, s56, 98304
Mvaa 80,224,112
S_ m0,s52,10112
s_and_b32 s55, s59, 4095
Zvaa 88,36,76
S_ s56,s56,s55
Mvaa 88,224,116
s_mul_i32 s55, s77, 384
Zvaa 92,24,116
s_sub_u32 s56, s56, s55
Ta 36,%0,16392
Ta 38,%0,16904
Mvaa 92,20,76
L_ v6,28,s65,128
Zvaa 84,24,112
S_ s65,s59,1081344
s_mov_b32 s63, 0
Mvaa 84,20,72
S_ m0,s52,11136
Ta 224,%0,20488
Ta 226,%0,21000
Zvaa 76,24,108
S_ s55,s56,3145728
Mvaa 76,20,68
L_ v6,28,s65,128
Zvaa 68,24,104
S_ s65,s60,256
Mvaa 68,20,64
S_ m0,s50,0
Zvaa 60,24,100
Mvaa 60,20,60
W_ 4
Zvaa 96,28,60
s_nop 0
Ta 20,v1,16384
Ta 22,v1,16896
Mvaa 96,32,100
L_ v4,24,s65,0
Zvaa 104,28,64
S_ s65,s60,10496
Mvaa 104,32,104
S_ m0,s50,1024
Ta 24,v1,20480
Ta 26,v1,20992
Zvaa 112,28,68
Mvaa 112,32,108
L_ v5,24,s65,0
Zvaa 120,28,72
S_ s65,s60,20736
Mvaa 120,32,112
S_ m0,s50,2048
Zvaa 128,28,76
Mvaa 128,32,116
W_ 4
Zvaa 132,224,116
s_nop 0
Ta 28,v1,16392
Ta 30,v1,16904
Mvaa 132,36,76
L_ v4,24,s65,0
Zvaa 124,224,112
S_ s65,s60,30976
Mvaa 124,36,72
S_ m0,s50,3072
Ta 32,v1,20488
Ta 34,v1,21000
Zvaa 116,224,108
Mvaa 116,36,68
L_ v5,24,s65,0
Zvaa 108,224,104
S_ s65,s60,41216
Mvaa 108,36,64
S_ m0,s50,4096
Zvaa 100,224,100
s_mov_b64 exec, 0
s_bitcmp1_b32 s4, 1
s_cbranch_scc1 .Lhop_q1_b
s_bitcmp1_b32 s4, 0
s_cbranch_scc1 .Lpad_q1_1
s_branch .Lpad_q1_0
.Lhop_q1_b:
s_bitcmp1_b32 s4, 0
s_cbranch_scc1 .Lpad_q1_3
s_branch .Lpad_q1_2
.Lhop_q1_r:
s_mov_b64 exec, -1
s_nop 1
.Lqy_early:
s_waitcnt vmcnt(17) lgkmcnt(0)
s_barrier
Mvaa 100,36,60
Da 140,v2,20480
Zvaa 136,20,60
s_nop 0
Ta 36,%0,24576
Ta 38,%0,25088
Mvaa 136,24,100
L_ v4,24,s65,0
Zvaa 144,20,64
S_ s65,s59,786432
Da 180,v3,20480
Mvaa 144,24,104
S_ m0,s52,16128
Ta 224,%0,28672
Ta 226,%0,29184
Zvaa 152,20,68
Mvaa 152,24,108
Da 144,v2,22528
Zvaa 160,20,72
L_ v6,28,s65,256
Mvaa 160,24,112
S_ s65,s59,884736
Zvaa 168,20,76
S_ m0,s52,17152
Da 184,v3,22528
Mvaa 168,24,116
Zvaa 172,32,116
s_nop 0
Ta 20,%0,24584
Ta 22,%0,25096
Mvaa 172,28,76
Da 148,v2,24576
Zvaa 164,32,112
L_ v6,28,s65,256
Mvaa 164,28,72
S_ s65,s59,983040
Ta 24,%0,28680
Ta 26,%0,29192
Zvaa 156,32,108
S_ m0,s52,18176
Da 188,v3,24576
Mvaa 156,28,68
Zvaa 148,32,104
L_ v6,28,s65,256
Mvaa 148,28,64
S_ s65,s59,1081344
Da 152,v2,26624
Zvaa 140,32,100
S_ m0,s52,19200
Mvaa 140,28,60
W_ 9
Zvaa 176,36,60
s_nop 0
Ta 28,v1,24576
Ta 30,v1,25088
Mvaa 176,224,100
Da 192,v3,26624
Zvaa 184,36,64
L_ v6,28,s65,256
Mvaa 184,224,104
s_nop 0
Ta 32,v1,28672
Ta 34,v1,29184
Zvaa 192,36,68
Da 156,v2,28672
Mvaa 192,224,108
Zvaa 200,36,72
Da 196,v3,28672
Mvaa 200,224,112
buffer_load_dword a254, v10, s[28:31], s55 offen
Zvaa 208,36,76
Mvaa 208,224,116
W_ 9
Zvaa 212,24,116
s_nop 0
Ta 36,v1,24584
Ta 38,v1,25096
Mvaa 212,20,76
buffer_load_dword a254, v12, s[24:27], s63 offen offset:512
Zvaa 204,24,112
.Lpad_q1_0:
s_cbranch_execz .Lhop_q1_r
Mvaa 204,20,72
s_nop 0
Ta 224,v1,28680
Ta 226,v1,29192
Zvaa 196,24,108
Mvaa 196,20,68
Zvaa 188,24,104
Mvaa 188,20,64
Zvaa 180,24,100
s_waitcnt vmcnt(20) lgkmcnt(0)
s_barrier
Mvaa 180,20,60
S_ s65,s59,1572864
Zvaa 216,28,60
S_ m0,s52,24576
Ta 20,%0,0
Ta 22,%0,512
Mvaa 216,32,100
L_ v6,28,s65,0
Zvaa 224,28,64
S_ s65,s59,1671168
Mvaa 224,32,104
S_ m0,s52,25600
Ta 24,%0,4096
Ta 26,%0,4608
Zvaa 232,28,68
Mvaa 232,32,108
L_ v6,28,s65,0
Zvaa 240,28,72
S_ s65,s59,1769472
Mvaa 240,32,112
S_ m0,s52,26624
Zvaa 248,28,76
Mvaa 248,32,116
Zvaa 252,224,116
s_nop 0
Ta 28,%0,8
Ta 30,%0,520
Mvaa 252,36,76
L_ v6,28,s65,0
Zvaa 244,224,112
S_ s65,s59,1867776
Mvaa 244,36,72
S_ m0,s52,27648
Ta 32,%0,4104
Ta 34,%0,4616
Zvaa 236,224,108
Mvaa 236,36,68
L_ v6,28,s65,0
Zvaa 228,224,104
S_ s65,s60,384
Mvaa 228,36,64
S_ m0,s50,20480
Zvaa 220,224,100
Mvaa 220,36,60
W_ 4
Mvaa 16,20,140
s_nop 0
Ta 36,v1,0
Ta 38,v1,512
Mvaa 16,24,180
L_ v4,24,s65,0
Mvaa 24,20,144
S_ s65,s60,10624
Mvaa 24,24,184
S_ m0,s50,21504
Ta 224,v1,4096
Ta 226,v1,4608
Mvaa 32,20,148
Mvaa 32,24,188
L_ v5,24,s65,0
Mvaa 40,20,152
S_ s65,s60,20864
Mvaa 40,24,192
S_ m0,s50,22528
Mvaa 48,20,156
Mvaa 48,24,196
W_ 4
Mvaa 52,32,196
s_nop 0
Ta 20,v1,8
Ta 22,v1,520
Mvaa 52,28,156
L_ v4,24,s65,0
Mvaa 44,32,192
S_ s65,s60,31104
Mvaa 44,28,152
S_ m0,s50,23552
Ta 24,v1,4104
Ta 26,v1,4616
Mvaa 36,32,188
Mvaa 36,28,148
L_ v5,24,s65,0
Mvaa 28,32,184
S_ s65,s60,41344
Mvaa 28,28,144
S_ m0,s50,24576
Mvaa 20,32,180
s_waitcnt vmcnt(19) lgkmcnt(0)
s_barrier
Mvaa 20,28,140
L_ v4,24,s65,0
Mvaa 56,36,140
S_ s65,s59,1572864
Ta 28,%0,8192
Ta 30,%0,8704
Mvaa 56,224,180
S_ m0,s52,-128
Mvaa 64,36,144
Mvaa 64,224,184
s_nop 0
Ta 32,%0,12288
Ta 34,%0,12800
Mvaa 72,36,148
L_ v6,28,s65,128
Mvaa 72,224,188
S_ s65,s59,1671168
Mvaa 80,36,152
S_ m0,s52,896
Mvaa 80,224,192
Mvaa 88,36,156
L_ v6,28,s65,128
Mvaa 88,224,196
S_ s65,s59,1769472
Mvaa 92,24,196
S_ m0,s52,1920
Ta 36,%0,8200
Ta 38,%0,8712
Mvaa 92,20,156
S_ s55,s56,3932160
Mvaa 84,24,192
L_ v6,28,s65,128
Mvaa 84,20,152
S_ s65,s59,1867776
Ta 224,%0,12296
Ta 226,%0,12808
Mvaa 76,24,188
S_ m0,s52,2944
Mvaa 76,20,148
Mvaa 68,24,184
L_ v6,28,s65,128
Mvaa 68,20,144
Mvaa 60,24,180
Mvaa 60,20,140
W_ 4
Mvaa 96,28,140
s_nop 0
Ta 20,v1,8192
Ta 22,v1,8704
Mvaa 96,32,180
buffer_load_dword a254, v10, s[28:31], s55 offen
Mvaa 104,28,144
Mvaa 104,32,184
s_nop 0
Ta 24,v1,12288
Ta 26,v1,12800
Mvaa 112,28,148
Mvaa 112,32,188
buffer_load_dword a254, v12, s[24:27], s63 offen offset:640
Mvaa 120,28,152
Mvaa 120,32,192
Mvaa 128,28,156
Mvaa 128,32,196
W_ 4
Mvaa 132,224,196
s_nop 0
Ta 28,v1,8200
Ta 30,v1,8712
Mvaa 132,36,156
Mvaa 124,224,192
Mvaa 124,36,152
s_nop 0
Ta 32,v1,12296
Ta 34,v1,12808
Mvaa 116,224,188
Mvaa 116,36,148
Mvaa 108,224,184
Mvaa 108,36,144
Mvaa 100,224,180
s_waitcnt vmcnt(17) lgkmcnt(0)
s_barrier
Mvaa 100,36,140
S_ s65,s59,1572864
Da 60,v2,0
Mvaa 136,20,140
S_ m0,s52,7936
Ta 36,%0,16384
Ta 38,%0,16896
Mvaa 136,24,180
L_ v6,28,s65,256
Mvaa 144,20,144
S_ s65,s59,1671168
Da 100,v3,0
Mvaa 144,24,184
S_ m0,s52,8960
Ta 224,%0,20480
Ta 226,%0,20992
Mvaa 152,20,148
Mvaa 152,24,188
Da 64,v2,2048
Mvaa 160,20,152
L_ v6,28,s65,256
Mvaa 160,24,192
S_ s65,s59,1769472
Mvaa 168,20,156
S_ m0,s52,9984
Da 104,v3,2048
Mvaa 168,24,196
Mvaa 172,32,196
s_nop 0
Ta 20,%0,16392
Ta 22,%0,16904
Mvaa 172,28,156
Da 68,v2,4096
Mvaa 164,32,192
L_ v6,28,s65,256
Mvaa 164,28,152
S_ s65,s59,1867776
Ta 24,%0,20488
Ta 26,%0,21000
Mvaa 156,32,188
S_ m0,s52,11008
Da 108,v3,4096
Mvaa 156,28,148
Mvaa 148,32,184
L_ v6,28,s65,256
Mvaa 148,28,144
Da 72,v2,6144
Mvaa 140,32,180
Mvaa 140,28,140
W_ 9
Mvaa 176,36,140
s_nop 0
Ta 28,v1,16384
Ta 30,v1,16896
Mvaa 176,224,180
Da 112,v3,6144
Mvaa 184,36,144
Mvaa 184,224,184
s_nop 0
Ta 32,v1,20480
Ta 34,v1,20992
Mvaa 192,36,148
Da 76,v2,8192
Mvaa 192,224,188
Mvaa 200,36,152
Da 116,v3,8192
Mvaa 200,224,192
Mvaa 208,36,156
Mvaa 208,224,196
W_ 9
Mvaa 212,24,196
s_nop 0
Ta 36,v1,16392
Ta 38,v1,16904
Mvaa 212,20,156
Mvaa 204,24,192
Mvaa 204,20,152
s_nop 0
Ta 224,v1,20488
Ta 226,v1,21000
Mvaa 196,24,188
Mvaa 196,20,148
Mvaa 188,24,184
Mvaa 188,20,144
Mvaa 180,24,180
s_waitcnt vmcnt(15) lgkmcnt(0)
s_barrier
Mvaa 180,20,140
S_ s65,s59,2359296
Mvaa 216,28,140
S_ m0,s52,16384
Ta 20,%0,24576
Ta 22,%0,25088
Mvaa 216,32,180
L_ v6,28,s65,0
Mvaa 224,28,144
S_ s65,s59,2457600
Mvaa 224,32,184
S_ m0,s52,17408
Ta 24,%0,28672
Ta 26,%0,29184
Mvaa 232,28,148
Mvaa 232,32,188
L_ v6,28,s65,0
Mvaa 240,28,152
S_ s65,s59,2555904
Mvaa 240,32,192
S_ m0,s52,18432
Mvaa 248,28,156
Mvaa 248,32,196
Mvaa 252,224,196
s_nop 0
Ta 28,%0,24584
Ta 30,%0,25096
Mvaa 252,36,156
L_ v6,28,s65,0
Mvaa 244,224,192
S_ s65,s59,2654208
Mvaa 244,36,152
S_ m0,s52,19456
Ta 32,%0,28680
Ta 34,%0,29192
Mvaa 236,224,188
Mvaa 236,36,148
L_ v6,28,s65,0
Mvaa 228,224,184
S_ s65,s60,512
Mvaa 228,36,144
S_ m0,s50,0
Mvaa 220,224,180
Mvaa 220,36,140
W_ 4
Mvaa 16,20,60
s_nop 0
Ta 36,v1,24576
Ta 38,v1,25088
Mvaa 16,24,100
L_ v4,24,s65,0
Mvaa 24,20,64
S_ s65,s60,10752
Mvaa 24,24,104
S_ m0,s50,1024
Ta 224,v1,28672
Ta 226,v1,29184
Mvaa 32,20,68
Mvaa 32,24,108
L_ v5,24,s65,0
Mvaa 40,20,72
S_ s65,s60,20992
Mvaa 40,24,112
S_ m0,s50,2048
Mvaa 48,20,76
Mvaa 48,24,116
W_ 4
Mvaa 52,32,116
s_nop 0
Ta 20,v1,24584
Ta 22,v1,25096
Mvaa 52,28,76
L_ v4,24,s65,0
Mvaa 44,32,112
S_ s65,s60,31232
Mvaa 44,28,72
S_ m0,s50,3072
Ta 24,v1,28680
Ta 26,v1,29192
Mvaa 36,32,108
Mvaa 36,28,68
L_ v5,24,s65,0
Mvaa 28,32,104
S_ s65,s60,41472
Mvaa 28,28,64
S_ m0,s50,4096
Mvaa 20,32,100
s_waitcnt vmcnt(14) lgkmcnt(0)
s_barrier
Mvaa 20,28,60
L_ v4,24,s65,0
Mvaa 56,36,60
S_ s65,s59,2359296
Ta 28,%0,0
Ta 30,%0,512
Mvaa 56,224,100
S_ m0,s52,24448
Mvaa 64,36,64
Mvaa 64,224,104
s_nop 0
Ta 32,%0,4096
Ta 34,%0,4608
Mvaa 72,36,68
L_ v6,28,s65,128
Mvaa 72,224,108
S_ s65,s59,2457600
Mvaa 80,36,72
S_ m0,s52,25472
Mvaa 80,224,112
Mvaa 88,36,76
L_ v6,28,s65,128
Mvaa 88,224,116
S_ s65,s59,2555904
Mvaa 92,24,116
S_ m0,s52,26496
Ta 36,%0,8
Ta 38,%0,520
Mvaa 92,20,76
S_ s55,s56,4718592
Mvaa 84,24,112
L_ v6,28,s65,128
Mvaa 84,20,72
S_ s65,s59,2654208
Ta 224,%0,4104
Ta 226,%0,4616
Mvaa 76,24,108
S_ m0,s52,27520
Mvaa 76,20,68
Mvaa 68,24,104
L_ v6,28,s65,128
Mvaa 68,20,64
Mvaa 60,24,100
Mvaa 60,20,60
W_ 4
Mvaa 96,28,60
s_nop 0
Ta 20,v1,0
Ta 22,v1,512
Mvaa 96,32,100
buffer_load_dword a254, v10, s[28:31], s55 offen
Mvaa 104,28,64
Mvaa 104,32,104
s_nop 0
Ta 24,v1,4096
Ta 26,v1,4608
Mvaa 112,28,68
Mvaa 112,32,108
buffer_load_dword a254, v12, s[24:27], s63 offen offset:768
Mvaa 120,28,72
Mvaa 120,32,112
Mvaa 128,28,76
Mvaa 128,32,116
W_ 4
Mvaa 132,224,116
s_nop 0
Ta 28,v1,8
Ta 30,v1,520
Mvaa 132,36,76
Mvaa 124,224,112
Mvaa 124,36,72
s_nop 0
Ta 32,v1,4104
Ta 34,v1,4616
Mvaa 116,224,108
Mvaa 116,36,68
Mvaa 108,224,104
Mvaa 108,36,64
Mvaa 100,224,100
s_waitcnt vmcnt(15) lgkmcnt(0)
s_barrier
Mvaa 100,36,60
S_ s65,s59,2359296
Da 140,v2,20480
Mvaa 136,20,60
S_ m0,s52,-256
Ta 36,%0,8192
Ta 38,%0,8704
Mvaa 136,24,100
L_ v6,28,s65,256
Mvaa 144,20,64
S_ s65,s59,2457600
Da 180,v3,20480
Mvaa 144,24,104
S_ m0,s52,768
Ta 224,%0,12288
Ta 226,%0,12800
Mvaa 152,20,68
Mvaa 152,24,108
Da 144,v2,22528
Mvaa 160,20,72
L_ v6,28,s65,256
Mvaa 160,24,112
S_ s65,s59,2555904
Mvaa 168,20,76
S_ m0,s52,1792
Da 184,v3,22528
Mvaa 168,24,116
Mvaa 172,32,116
s_nop 0
Ta 20,%0,8200
Ta 22,%0,8712
Mvaa 172,28,76
Da 148,v2,24576
Mvaa 164,32,112
L_ v6,28,s65,256
Mvaa 164,28,72
S_ s65,s59,2654208
Ta 24,%0,12296
Ta 26,%0,12808
Mvaa 156,32,108
S_ m0,s52,2816
Da 188,v3,24576
Mvaa 156,28,68
Mvaa 148,32,104
L_ v6,28,s65,256
Mvaa 148,28,64
Da 152,v2,26624
Mvaa 140,32,100
Mvaa 140,28,60
W_ 9
Mvaa 176,36,60
s_nop 0
Ta 28,v1,8192
Ta 30,v1,8704
Mvaa 176,224,100
Da 192,v3,26624
Mvaa 184,36,64
Mvaa 184,224,104
s_nop 0
Ta 32,v1,12288
Ta 34,v1,12800
Mvaa 192,36,68
Da 156,v2,28672
Mvaa 192,224,108
Mvaa 200,36,72
Da 196,v3,28672
Mvaa 200,224,112
Mvaa 208,36,76
Mvaa 208,224,116
W_ 9
Mvaa 212,24,116
s_nop 0
Ta 36,v1,8200
Ta 38,v1,8712
Mvaa 212,20,76
Mvaa 204,24,112
Mvaa 204,20,72
s_nop 0
Ta 224,v1,12296
Ta 226,v1,12808
Mvaa 196,24,108
Mvaa 196,20,68
Mvaa 188,24,104
Mvaa 188,20,64
Mvaa 180,24,100
s_waitcnt vmcnt(15) lgkmcnt(0)
s_barrier
Mvaa 180,20,60
S_ s65,s59,3145728
Mvaa 216,28,60
S_ m0,s52,8192
Ta 20,%0,16384
Ta 22,%0,16896
Mvaa 216,32,100
L_ v6,28,s65,0
Mvaa 224,28,64
S_ s65,s59,3244032
Mvaa 224,32,104
S_ m0,s52,9216
Ta 24,%0,20480
Ta 26,%0,20992
Mvaa 232,28,68
.Lpad_q1_1:
s_cbranch_execz .Lhop_q1_r
Mvaa 232,32,108
L_ v6,28,s65,0
Mvaa 240,28,72
S_ s65,s59,3342336
Mvaa 240,32,112
S_ m0,s52,10240
Mvaa 248,28,76
Mvaa 248,32,116
Mvaa 252,224,116
s_nop 0
Ta 28,%0,16392
Ta 30,%0,16904
Mvaa 252,36,76
L_ v6,28,s65,0
Mvaa 244,224,112
S_ s65,s59,3440640
Mvaa 244,36,72
S_ m0,s52,11264
Ta 32,%0,20488
Ta 34,%0,21000
Mvaa 236,224,108
Mvaa 236,36,68
L_ v6,28,s65,0
Mvaa 228,224,104
S_ s65,s60,640
Mvaa 228,36,64
S_ m0,s50,20480
Mvaa 220,224,100
Mvaa 220,36,60
W_ 4
Mvaa 16,20,140
s_nop 0
Ta 36,v1,16384
Ta 38,v1,16896
Mvaa 16,24,180
L_ v4,24,s65,0
Mvaa 24,20,144
S_ s65,s60,10880
Mvaa 24,24,184
S_ m0,s50,21504
Ta 224,v1,20480
Ta 226,v1,20992
Mvaa 32,20,148
Mvaa 32,24,188
L_ v5,24,s65,0
Mvaa 40,20,152
S_ s65,s60,21120
Mvaa 40,24,192
S_ m0,s50,22528
Mvaa 48,20,156
Mvaa 48,24,196
W_ 4
Mvaa 52,32,196
s_nop 0
Ta 20,v1,16392
Ta 22,v1,16904
Mvaa 52,28,156
L_ v4,24,s65,0
Mvaa 44,32,192
S_ s65,s60,31360
Mvaa 44,28,152
S_ m0,s50,23552
Ta 24,v1,20488
Ta 26,v1,21000
Mvaa 36,32,188
Mvaa 36,28,148
L_ v5,24,s65,0
Mvaa 28,32,184
S_ s65,s60,41600
Mvaa 28,28,144
S_ m0,s50,24576
Mvaa 20,32,180
s_waitcnt vmcnt(14) lgkmcnt(0)
s_barrier
Mvaa 20,28,140
L_ v4,24,s65,0
Mvaa 56,36,140
S_ s65,s59,3145728
Ta 28,%0,24576
Ta 30,%0,25088
Mvaa 56,224,180
S_ m0,s52,16256
Mvaa 64,36,144
Mvaa 64,224,184
s_nop 0
Ta 32,%0,28672
Ta 34,%0,29184
Mvaa 72,36,148
L_ v6,28,s65,128
Mvaa 72,224,188
S_ s65,s59,3244032
Mvaa 80,36,152
S_ m0,s52,17280
Mvaa 80,224,192
Mvaa 88,36,156
L_ v6,28,s65,128
Mvaa 88,224,196
S_ s65,s59,3342336
Mvaa 92,24,196
S_ m0,s52,18304
Ta 36,%0,24584
Ta 38,%0,25096
Mvaa 92,20,156
S_ s55,s56,5505024
Mvaa 84,24,192
L_ v6,28,s65,128
Mvaa 84,20,152
S_ s65,s59,3440640
Ta 224,%0,28680
Ta 226,%0,29192
Mvaa 76,24,188
S_ m0,s52,19328
Mvaa 76,20,148
Mvaa 68,24,184
L_ v6,28,s65,128
Mvaa 68,20,144
Mvaa 60,24,180
Mvaa 60,20,140
W_ 4
Mvaa 96,28,140
s_nop 0
Ta 20,v1,24576
Ta 22,v1,25088
Mvaa 96,32,180
buffer_load_dword a254, v10, s[28:31], s55 offen
Mvaa 104,28,144
Mvaa 104,32,184
s_nop 0
Ta 24,v1,28672
Ta 26,v1,29184
Mvaa 112,28,148
Mvaa 112,32,188
buffer_load_dword a254, v12, s[24:27], s63 offen offset:896
Mvaa 120,28,152
Mvaa 120,32,192
Mvaa 128,28,156
Mvaa 128,32,196
W_ 4
Mvaa 132,224,196
s_nop 0
Ta 28,v1,24584
Ta 30,v1,25096
Mvaa 132,36,156
Mvaa 124,224,192
Mvaa 124,36,152
s_nop 0
Ta 32,v1,28680
Ta 34,v1,29192
Mvaa 116,224,188
Mvaa 116,36,148
Mvaa 108,224,184
Mvaa 108,36,144
Mvaa 100,224,180
s_waitcnt vmcnt(15) lgkmcnt(0)
s_barrier
Mvaa 100,36,140
S_ s65,s59,3145728
Da 60,v2,0
Mvaa 136,20,140
S_ m0,s52,24320
Ta 36,%0,0
Ta 38,%0,512
Mvaa 136,24,180
L_ v6,28,s65,256
Mvaa 144,20,144
S_ s65,s59,3244032
Da 100,v3,0
Mvaa 144,24,184
S_ m0,s52,25344
Ta 224,%0,4096
Ta 226,%0,4608
Mvaa 152,20,148
Mvaa 152,24,188
Da 64,v2,2048
Mvaa 160,20,152
L_ v6,28,s65,256
Mvaa 160,24,192
S_ s65,s59,3342336
Mvaa 168,20,156
S_ m0,s52,26368
Da 104,v3,2048
Mvaa 168,24,196
Mvaa 172,32,196
s_nop 0
Ta 20,%0,8
Ta 22,%0,520
Mvaa 172,28,156
Da 68,v2,4096
Mvaa 164,32,192
L_ v6,28,s65,256
Mvaa 164,28,152
S_ s65,s59,3440640
Ta 24,%0,4104
Ta 26,%0,4616
Mvaa 156,32,188
S_ m0,s52,27392
Da 108,v3,4096
Mvaa 156,28,148
Mvaa 148,32,184
L_ v6,28,s65,256
Mvaa 148,28,144
Da 72,v2,6144
Mvaa 140,32,180
Mvaa 140,28,140
W_ 9
Mvaa 176,36,140
s_nop 0
Ta 28,v1,0
Ta 30,v1,512
Mvaa 176,224,180
Da 112,v3,6144
Mvaa 184,36,144
Mvaa 184,224,184
s_nop 0
Ta 32,v1,4096
Ta 34,v1,4608
Mvaa 192,36,148
Da 76,v2,8192
Mvaa 192,224,188
Mvaa 200,36,152
Da 116,v3,8192
Mvaa 200,224,192
Mvaa 208,36,156
Mvaa 208,224,196
W_ 9
Mvaa 212,24,196
s_nop 0
Ta 36,v1,8
Ta 38,v1,520
Mvaa 212,20,156
Mvaa 204,24,192
Mvaa 204,20,152
s_nop 0
Ta 224,v1,4104
Ta 226,v1,4616
Mvaa 196,24,188
Mvaa 196,20,148
Mvaa 188,24,184
Mvaa 188,20,144
Mvaa 180,24,180
s_waitcnt vmcnt(15) lgkmcnt(0)
s_barrier
Mvaa 180,20,140
S_ s65,s59,3932160
Mvaa 216,28,140
S_ m0,s52,0
Ta 20,%0,8192
Ta 22,%0,8704
Mvaa 216,32,180
L_ v6,28,s65,0
Mvaa 224,28,144
S_ s65,s59,4030464
Mvaa 224,32,184
S_ m0,s52,1024
Ta 24,%0,12288
Ta 26,%0,12800
Mvaa 232,28,148
Mvaa 232,32,188
L_ v6,28,s65,0
Mvaa 240,28,152
S_ s65,s59,4128768
Mvaa 240,32,192
S_ m0,s52,2048
Mvaa 248,28,156
Mvaa 248,32,196
Mvaa 252,224,196
s_nop 0
Ta 28,%0,8200
Ta 30,%0,8712
Mvaa 252,36,156
L_ v6,28,s65,0
Mvaa 244,224,192
S_ s65,s59,4227072
Mvaa 244,36,152
S_ m0,s52,3072
Ta 32,%0,12296
Ta 34,%0,12808
Mvaa 236,224,188
Mvaa 236,36,148
L_ v6,28,s65,0
Mvaa 228,224,184
S_ s65,s60,768
Mvaa 228,36,144
S_ m0,s50,0
Mvaa 220,224,180
Mvaa 220,36,140
W_ 4
Mvaa 16,20,60
s_nop 0
Ta 36,v1,8192
Ta 38,v1,8704
Mvaa 16,24,100
L_ v4,24,s65,0
Mvaa 24,20,64
S_ s65,s60,11008
R_ v13,a248
Mvaa 24,24,104
S_ m0,s50,1024
Ta 224,v1,12288
Ta 226,v1,12800
Mvaa 32,20,68
v_min_u32 v13, 240, v13
Mvaa 32,24,108
L_ v5,24,s65,0
Mvaa 40,20,72
S_ s65,s60,21248
v_lshlrev_b32 v13, 3, v13
Mvaa 40,24,112
S_ m0,s50,2048
Mvaa 48,20,76
Mvaa 48,24,116
W_ 4
Mvaa 52,32,116
s_nop 0
Ta 20,v1,8200
Ta 22,v1,8712
Mvaa 52,28,76
L_ v4,24,s65,0
Mvaa 44,32,112
S_ s65,s60,31488
Mvaa 44,28,72
S_ m0,s50,3072
Ta 24,v1,12296
Ta 26,v1,12808
Mvaa 36,32,108
Mvaa 36,28,68
L_ v5,24,s65,0
Mvaa 28,32,104
S_ s65,s60,41728
Mvaa 28,28,64
S_ m0,s50,4096
Mvaa 20,32,100
s_waitcnt vmcnt(14) lgkmcnt(0)
s_barrier
Mvaa 20,28,60
L_ v4,24,s65,0
Mvaa 56,36,60
S_ s65,s59,3932160
Ta 28,%0,16384
Ta 30,%0,16896
Mvaa 56,224,100
S_ m0,s52,8064
Mvaa 64,36,64
Mvaa 64,224,104
s_nop 0
Ta 32,%0,20480
Ta 34,%0,20992
Mvaa 72,36,68
L_ v6,28,s65,128
Mvaa 72,224,108
S_ s65,s59,4030464
Mvaa 80,36,72
S_ m0,s52,9088
Mvaa 80,224,112
Mvaa 88,36,76
L_ v6,28,s65,128
Mvaa 88,224,116
S_ s65,s59,4128768
Mvaa 92,24,116
S_ m0,s52,10112
Ta 36,%0,16392
Ta 38,%0,16904
Mvaa 92,20,76
S_ s55,s56,6291456
Mvaa 84,24,112
L_ v6,28,s65,128
Mvaa 84,20,72
S_ s65,s59,4227072
Ta 224,%0,20488
Ta 226,%0,21000
Mvaa 76,24,108
S_ m0,s52,11136
Mvaa 76,20,68
Mvaa 68,24,104
L_ v6,28,s65,128
Mvaa 68,20,64
Mvaa 60,24,100
Mvaa 60,20,60
W_ 4
Mvaa 96,28,60
s_nop 0
Ta 20,v1,16384
Ta 22,v1,16896
Mvaa 96,32,100
buffer_load_dword a220, v13, s[44:47], s66 offen
Mvaa 104,28,64
Mvaa 104,32,104
s_nop 0
Ta 24,v1,20480
Ta 26,v1,20992
Mvaa 112,28,68
Mvaa 112,32,108
buffer_load_dword a254, v10, s[28:31], s55 offen
Mvaa 120,28,72
Mvaa 120,32,112
Mvaa 128,28,76
Mvaa 128,32,116
W_ 4
Mvaa 132,224,116
s_nop 0
Ta 28,v1,16392
Ta 30,v1,16904
Mvaa 132,36,76
buffer_load_dword a254, v12, s[24:27], s63 offen offset:1024
Mvaa 124,224,112
Mvaa 124,36,72
s_nop 0
Ta 32,v1,20488
Ta 34,v1,21000
Mvaa 116,224,108
Mvaa 116,36,68
Mvaa 108,224,104
Mvaa 108,36,64
Mvaa 100,224,100
s_waitcnt vmcnt(16) lgkmcnt(0)
s_barrier
Mvaa 100,36,60
S_ s65,s59,3932160
Da 140,v2,20480
Mvaa 136,20,60
S_ m0,s52,16128
Ta 36,%0,24576
Ta 38,%0,25088
Mvaa 136,24,100
L_ v6,28,s65,256
Mvaa 144,20,64
S_ s65,s59,4030464
Da 180,v3,20480
Mvaa 144,24,104
S_ m0,s52,17152
Ta 224,%0,28672
Ta 226,%0,29184
Mvaa 152,20,68
Mvaa 152,24,108
Da 144,v2,22528
Mvaa 160,20,72
L_ v6,28,s65,256
Mvaa 160,24,112
S_ s65,s59,4128768
Mvaa 168,20,76
S_ m0,s52,18176
Da 184,v3,22528
Mvaa 168,24,116
Mvaa 172,32,116
s_nop 0
Ta 20,%0,24584
Ta 22,%0,25096
Mvaa 172,28,76
Da 148,v2,24576
Mvaa 164,32,112
L_ v6,28,s65,256
Mvaa 164,28,72
S_ s65,s59,4227072
Ta 24,%0,28680
Ta 26,%0,29192
Mvaa 156,32,108
S_ m0,s52,19200
Da 188,v3,24576
Mvaa 156,28,68
Mvaa 148,32,104
L_ v6,28,s65,256
Mvaa 148,28,64
Da 152,v2,26624
Mvaa 140,32,100
Mvaa 140,28,60
W_ 9
Mvaa 176,36,60
s_nop 0
Ta 28,v1,24576
Ta 30,v1,25088
Mvaa 176,224,100
Da 192,v3,26624
Mvaa 184,36,64
Mvaa 184,224,104
s_nop 0
Ta 32,v1,28672
Ta 34,v1,29184
Mvaa 192,36,68
Da 156,v2,28672
Mvaa 192,224,108
Mvaa 200,36,72
Da 196,v3,28672
Mvaa 200,224,112
Mvaa 208,36,76
Mvaa 208,224,116
W_ 9
Mvaa 212,24,116
s_nop 0
Ta 36,v1,24584
Ta 38,v1,25096
Mvaa 212,20,76
Mvaa 204,24,112
Mvaa 204,20,72
s_nop 0
Ta 224,v1,28680
Ta 226,v1,29192
Mvaa 196,24,108
Mvaa 196,20,68
Mvaa 188,24,104
Mvaa 188,20,64
Mvaa 180,24,100
s_waitcnt vmcnt(16) lgkmcnt(0)
s_barrier
Mvaa 180,20,60
S_ s65,s59,4718592
Mvaa 216,28,60
S_ m0,s52,24576
Ta 20,%0,0
Ta 22,%0,512
Mvaa 216,32,100
L_ v6,28,s65,0
Mvaa 224,28,64
S_ s65,s59,4816896
Mvaa 224,32,104
S_ m0,s52,25600
Ta 24,%0,4096
Ta 26,%0,4608
Mvaa 232,28,68
Mvaa 232,32,108
L_ v6,28,s65,0
Mvaa 240,28,72
S_ s65,s59,4915200
Mvaa 240,32,112
S_ m0,s52,26624
Mvaa 248,28,76
Mvaa 248,32,116
Mvaa 252,224,116
s_nop 0
Ta 28,%0,8
Ta 30,%0,520
Mvaa 252,36,76
L_ v6,28,s65,0
Mvaa 244,224,112
S_ s65,s59,5013504
Mvaa 244,36,72
S_ m0,s52,27648
Ta 32,%0,4104
Ta 34,%0,4616
Mvaa 236,224,108
.Lpad_q1_2:
s_cbranch_execz .Lhop_q1_r
Mvaa 236,36,68
L_ v6,28,s65,0
Mvaa 228,224,104
S_ s65,s60,896
Mvaa 228,36,64
S_ m0,s50,20480
Mvaa 220,224,100
Mvaa 220,36,60
W_ 4
Mvaa 16,20,140
s_nop 0
Ta 36,v1,0
Ta 38,v1,512
Mvaa 16,24,180
L_ v4,24,s65,0
Mvaa 24,20,144
S_ s65,s60,11136
Mvaa 24,24,184
S_ m0,s50,21504
Ta 224,v1,4096
Ta 226,v1,4608
Mvaa 32,20,148
Mvaa 32,24,188
L_ v5,24,s65,0
Mvaa 40,20,152
S_ s65,s60,21376
Mvaa 40,24,192
S_ m0,s50,22528
Mvaa 48,20,156
Mvaa 48,24,196
W_ 4
Mvaa 52,32,196
s_nop 0
Ta 20,v1,8
Ta 22,v1,520
Mvaa 52,28,156
L_ v4,24,s65,0
Mvaa 44,32,192
S_ s65,s60,31616
Mvaa 44,28,152
S_ m0,s50,23552
Ta 24,v1,4104
Ta 26,v1,4616
Mvaa 36,32,188
Mvaa 36,28,148
L_ v5,24,s65,0
Mvaa 28,32,184
S_ s65,s60,41856
Mvaa 28,28,144
S_ m0,s50,24576
Mvaa 20,32,180
s_waitcnt vmcnt(15) lgkmcnt(0)
s_barrier
Mvaa 20,28,140
L_ v4,24,s65,0
Mvaa 56,36,140
S_ s65,s59,4718592
Ta 28,%0,8192
Ta 30,%0,8704
Mvaa 56,224,180
S_ m0,s52,-128
Mvaa 64,36,144
Mvaa 64,224,184
s_nop 0
Ta 32,%0,12288
Ta 34,%0,12800
Mvaa 72,36,148
L_ v6,28,s65,128
Mvaa 72,224,188
S_ s65,s59,4816896
Mvaa 80,36,152
S_ m0,s52,896
Mvaa 80,224,192
Mvaa 88,36,156
L_ v6,28,s65,128
Mvaa 88,224,196
S_ s65,s59,4915200
Mvaa 92,24,196
S_ m0,s52,1920
Ta 36,%0,8200
Ta 38,%0,8712
Mvaa 92,20,156
S_ s55,s56,7077888
Mvaa 84,24,192
L_ v6,28,s65,128
Mvaa 84,20,152
S_ s65,s59,5013504
Ta 224,%0,12296
Ta 226,%0,12808
Mvaa 76,24,188
S_ m0,s52,2944
Mvaa 76,20,148
Mvaa 68,24,184
L_ v6,28,s65,128
Mvaa 68,20,144
Mvaa 60,24,180
Mvaa 60,20,140
W_ 4
Mvaa 96,28,140
s_nop 0
Ta 20,v1,8192
Ta 22,v1,8704
Mvaa 96,32,180
buffer_load_dword a254, v10, s[28:31], s55 offen
Mvaa 104,28,144
Mvaa 104,32,184
s_nop 0
Ta 24,v1,12288
Ta 26,v1,12800
Mvaa 112,28,148
Mvaa 112,32,188
buffer_load_dword a254, v12, s[24:27], s63 offen offset:1152
Mvaa 120,28,152
Mvaa 120,32,192
Mvaa 128,28,156
Mvaa 128,32,196
W_ 4
Mvaa 132,224,196
s_nop 0
Ta 28,v1,8200
Ta 30,v1,8712
Mvaa 132,36,156
Mvaa 124,224,192
Mvaa 124,36,152
s_nop 0
Ta 32,v1,12296
Ta 34,v1,12808
Mvaa 116,224,188
Mvaa 116,36,148
Mvaa 108,224,184
Mvaa 108,36,144
Mvaa 100,224,180
s_waitcnt vmcnt(15) lgkmcnt(0)
s_barrier
Mvaa 100,36,140
S_ s65,s59,4718592
Da 60,v2,0
Mvaa 136,20,140
S_ m0,s52,7936
Ta 36,%0,16384
Ta 38,%0,16896
Mvaa 136,24,180
L_ v6,28,s65,256
Mvaa 144,20,144
S_ s65,s59,4816896
Da 100,v3,0
Mvaa 144,24,184
S_ m0,s52,8960
Ta 224,%0,20480
Ta 226,%0,20992
Mvaa 152,20,148
Mvaa 152,24,188
Da 64,v2,2048
Mvaa 160,20,152
L_ v6,28,s65,256
Mvaa 160,24,192
S_ s65,s59,4915200
Mvaa 168,20,156
S_ m0,s52,9984
Da 104,v3,2048
Mvaa 168,24,196
Mvaa 172,32,196
s_nop 0
Ta 20,%0,16392
Ta 22,%0,16904
Mvaa 172,28,156
Da 68,v2,4096
Mvaa 164,32,192
L_ v6,28,s65,256
Mvaa 164,28,152
S_ s65,s59,5013504
Ta 24,%0,20488
Ta 26,%0,21000
Mvaa 156,32,188
S_ m0,s52,11008
Da 108,v3,4096
Mvaa 156,28,148
Mvaa 148,32,184
L_ v6,28,s65,256
)A"
R"A(Mvaa 148,28,144
Da 72,v2,6144
Mvaa 140,32,180
Mvaa 140,28,140
W_ 9
Mvaa 176,36,140
s_nop 0
Ta 28,v1,16384
Ta 30,v1,16896
Mvaa 176,224,180
Da 112,v3,6144
Mvaa 184,36,144
Mvaa 184,224,184
s_nop 0
Ta 32,v1,20480
Ta 34,v1,20992
Mvaa 192,36,148
Da 76,v2,8192
Mvaa 192,224,188
Mvaa 200,36,152
Da 116,v3,8192
Mvaa 200,224,192
Mvaa 208,36,156
Mvaa 208,224,196
W_ 9
Mvaa 212,24,196
s_nop 0
Ta 36,v1,16392
Ta 38,v1,16904
Mvaa 212,20,156
Mvaa 204,24,192
Mvaa 204,20,152
s_nop 0
Ta 224,v1,20488
Ta 226,v1,21000
Mvaa 196,24,188
Mvaa 196,20,148
Mvaa 188,24,184
Mvaa 188,20,144
Mvaa 180,24,180
s_waitcnt vmcnt(15) lgkmcnt(0)
s_barrier
Mvaa 180,20,140
S_ s65,s59,5505024
Mvaa 216,28,140
S_ m0,s52,16384
Ta 20,%0,24576
Ta 22,%0,25088
Mvaa 216,32,180
L_ v6,28,s65,0
Mvaa 224,28,144
S_ s65,s59,5603328
Mvaa 224,32,184
S_ m0,s52,17408
Ta 24,%0,28672
Ta 26,%0,29184
Mvaa 232,28,148
Mvaa 232,32,188
L_ v6,28,s65,0
Mvaa 240,28,152
S_ s65,s59,5701632
Mvaa 240,32,192
S_ m0,s52,18432
Mvaa 248,28,156
Mvaa 248,32,196
Mvaa 252,224,196
s_nop 0
Ta 28,%0,24584
Ta 30,%0,25096
Mvaa 252,36,156
L_ v6,28,s65,0
Mvaa 244,224,192
S_ s65,s59,5799936
Mvaa 244,36,152
S_ m0,s52,19456
Ta 32,%0,28680
Ta 34,%0,29192
Mvaa 236,224,188
Mvaa 236,36,148
L_ v6,28,s65,0
Mvaa 228,224,184
S_ s65,s60,1024
Mvaa 228,36,144
S_ m0,s50,0
Mvaa 220,224,180
Mvaa 220,36,140
W_ 4
Mvaa 16,20,60
s_nop 0
Ta 36,v1,24576
Ta 38,v1,25088
Mvaa 16,24,100
L_ v4,24,s65,0
Mvaa 24,20,64
S_ s65,s60,11264
Mvaa 24,24,104
S_ m0,s50,1024
Ta 224,v1,28672
Ta 226,v1,29184
Mvaa 32,20,68
Mvaa 32,24,108
L_ v5,24,s65,0
Mvaa 40,20,72
S_ s65,s60,21504
Mvaa 40,24,112
S_ m0,s50,2048
Mvaa 48,20,76
Mvaa 48,24,116
W_ 4
Mvaa 52,32,116
s_nop 0
Ta 20,v1,24584
Ta 22,v1,25096
Mvaa 52,28,76
L_ v4,24,s65,0
Mvaa 44,32,112
S_ s65,s60,31744
Mvaa 44,28,72
S_ m0,s50,3072
Ta 24,v1,28680
Ta 26,v1,29192
Mvaa 36,32,108
Mvaa 36,28,68
L_ v5,24,s65,0
Mvaa 28,32,104
S_ s65,s60,41984
Mvaa 28,28,64
S_ m0,s50,4096
Mvaa 20,32,100
s_waitcnt vmcnt(14) lgkmcnt(0)
s_barrier
Mvaa 20,28,60
L_ v4,24,s65,0
Mvaa 56,36,60
S_ s65,s59,5505024
Ta 28,%0,0
Ta 30,%0,512
Mvaa 56,224,100
S_ m0,s52,24448
Mvaa 64,36,64
Mvaa 64,224,104
s_nop 0
Ta 32,%0,4096
Ta 34,%0,4608
Mvaa 72,36,68
L_ v6,28,s65,128
Mvaa 72,224,108
S_ s65,s59,5603328
Mvaa 80,36,72
S_ m0,s52,25472
Mvaa 80,224,112
Mvaa 88,36,76
L_ v6,28,s65,128
Mvaa 88,224,116
S_ s65,s59,5701632
Mvaa 92,24,116
S_ m0,s52,26496
Ta 36,%0,8
Ta 38,%0,520
Mvaa 92,20,76
Mvaa 84,24,112
L_ v6,28,s65,128
Mvaa 84,20,72
S_ s65,s59,5799936
Ta 224,%0,4104
Ta 226,%0,4616
Mvaa 76,24,108
S_ m0,s52,27520
Mvaa 76,20,68
Mvaa 68,24,104
L_ v6,28,s65,128
Mvaa 68,20,64
Mvaa 60,24,100
Mvaa 60,20,60
W_ 4
Mvaa 96,28,60
s_nop 0
Ta 20,v1,0
Ta 22,v1,512
Mvaa 96,32,100
Mvaa 104,28,64
Mvaa 104,32,104
s_nop 0
Ta 24,v1,4096
Ta 26,v1,4608
Mvaa 112,28,68
Mvaa 112,32,108
Mvaa 120,28,72
Mvaa 120,32,112
Mvaa 128,28,76
Mvaa 128,32,116
W_ 4
Mvaa 132,224,116
s_nop 0
Ta 28,v1,8
Ta 30,v1,520
Mvaa 132,36,76
Mvaa 124,224,112
Mvaa 124,36,72
s_nop 0
Ta 32,v1,4104
Ta 34,v1,4616
Mvaa 116,224,108
Mvaa 116,36,68
Mvaa 108,224,104
Mvaa 108,36,64
Mvaa 100,224,100
s_waitcnt vmcnt(13) lgkmcnt(0)
s_barrier
Mvaa 100,36,60
S_ s65,s59,5505024
Da 140,v2,20480
Mvaa 136,20,60
S_ m0,s52,-256
Ta 36,%0,8192
Ta 38,%0,8704
Mvaa 136,24,100
L_ v6,28,s65,256
Mvaa 144,20,64
S_ s65,s59,5603328
Da 180,v3,20480
Mvaa 144,24,104
S_ m0,s52,768
Ta 224,%0,12288
Ta 226,%0,12800
Mvaa 152,20,68
Mvaa 152,24,108
Da 144,v2,22528
Mvaa 160,20,72
L_ v6,28,s65,256
Mvaa 160,24,112
S_ s65,s59,5701632
Mvaa 168,20,76
S_ m0,s52,1792
Da 184,v3,22528
Mvaa 168,24,116
Mvaa 172,32,116
s_nop 0
Ta 20,%0,8200
Ta 22,%0,8712
Mvaa 172,28,76
Da 148,v2,24576
Mvaa 164,32,112
L_ v6,28,s65,256
Mvaa 164,28,72
S_ s65,s59,5799936
Ta 24,%0,12296
Ta 26,%0,12808
Mvaa 156,32,108
S_ m0,s52,2816
Da 188,v3,24576
Mvaa 156,28,68
Mvaa 148,32,104
L_ v6,28,s65,256
Mvaa 148,28,64
Da 152,v2,26624
Mvaa 140,32,100
Mvaa 140,28,60
W_ 9
Mvaa 176,36,60
s_nop 0
Ta 28,v1,8192
Ta 30,v1,8704
Mvaa 176,224,100
Da 192,v3,26624
Mvaa 184,36,64
Mvaa 184,224,104
s_nop 0
Ta 32,v1,12288
Ta 34,v1,12800
Mvaa 192,36,68
Da 156,v2,28672
Mvaa 192,224,108
Mvaa 200,36,72
Da 196,v3,28672
Mvaa 200,224,112
Mvaa 208,36,76
Mvaa 208,224,116
W_ 9
Mvaa 212,24,116
s_nop 0
Ta 36,v1,8200
Ta 38,v1,8712
Mvaa 212,20,76
Mvaa 204,24,112
Mvaa 204,20,72
s_nop 0
Ta 224,v1,12296
Ta 226,v1,12808
Mvaa 196,24,108
Mvaa 196,20,68
Mvaa 188,24,104
Mvaa 188,20,64
Mvaa 180,24,100
s_waitcnt vmcnt(13) lgkmcnt(0)
s_barrier
Mvaa 180,20,60
S_ s65,s59,6291456
Mvaa 216,28,60
S_ m0,s52,7936
Ta 20,%0,16384
Ta 22,%0,16896
Mvaa 216,32,100
L_ v6,28,s65,256
Mvaa 224,28,64
S_ s65,s59,6389760
Mvaa 224,32,104
S_ m0,s52,8960
Ta 24,%0,20480
Ta 26,%0,20992
Mvaa 232,28,68
Mvaa 232,32,108
L_ v6,28,s65,256
Mvaa 240,28,72
S_ s65,s59,6488064
Mvaa 240,32,112
S_ m0,s52,9984
Mvaa 248,28,76
Mvaa 248,32,116
Mvaa 252,224,116
s_nop 0
Ta 28,%0,16392
Ta 30,%0,16904
Mvaa 252,36,76
L_ v6,28,s65,256
Mvaa 244,224,112
S_ s65,s59,6586368
Mvaa 244,36,72
S_ m0,s52,11008
Ta 32,%0,20488
Ta 34,%0,21000
Mvaa 236,224,108
Mvaa 236,36,68
L_ v6,28,s65,256
Mvaa 228,224,104
S_ s65,s60,1152
Mvaa 228,36,64
S_ m0,s50,20480
Mvaa 220,224,100
Mvaa 220,36,60
W_ 4
Mvaa 16,20,140
s_nop 0
Ta 36,v1,16384
Ta 38,v1,16896
Mvaa 16,24,180
L_ v4,24,s65,0
Mvaa 24,20,144
S_ s65,s60,11392
Mvaa 24,24,184
S_ m0,s50,21504
Ta 224,v1,20480
Ta 226,v1,20992
Mvaa 32,20,148
.Lpad_q1_3:
s_cbranch_execz .Lhop_q1_r
Mvaa 32,24,188
L_ v5,24,s65,0
Mvaa 40,20,152
S_ s65,s60,21632
Mvaa 40,24,192
S_ m0,s50,22528
Mvaa 48,20,156
Mvaa 48,24,196
W_ 4
Mvaa 52,32,196
s_nop 0
Ta 20,v1,16392
Ta 22,v1,16904
Mvaa 52,28,156
L_ v4,24,s65,0
Mvaa 44,32,192
S_ s65,s60,31872
Mvaa 44,28,152
S_ m0,s50,23552
Ta 24,v1,20488
Ta 26,v1,21000
Mvaa 36,32,188
Mvaa 36,28,148
L_ v5,24,s65,0
Mvaa 28,32,184
S_ s65,s60,42112
Mvaa 28,28,144
S_ m0,s50,24576
Mvaa 20,32,180
s_waitcnt vmcnt(12) lgkmcnt(0)
s_barrier
Mvaa 20,28,140
L_ v4,24,s65,0
Mvaa 56,36,140
S_ s65,s59,7077888
Ta 28,%0,24576
Ta 30,%0,25088
Mvaa 56,224,180
S_ m0,s52,16128
Mvaa 64,36,144
Mvaa 64,224,184
s_nop 0
Ta 32,%0,28672
Ta 34,%0,29184
Mvaa 72,36,148
L_ v6,28,s65,256
Mvaa 72,224,188
S_ s65,s59,7176192
Mvaa 80,36,152
S_ m0,s52,17152
Mvaa 80,224,192
Mvaa 88,36,156
L_ v6,28,s65,256
Mvaa 88,224,196
S_ s65,s59,7274496
Mvaa 92,24,196
S_ m0,s52,18176
Ta 36,%0,24584
Ta 38,%0,25096
Mvaa 92,20,156
Mvaa 84,24,192
L_ v6,28,s65,256
Mvaa 84,20,152
S_ s65,s59,7372800
Ta 224,%0,28680
Ta 226,%0,29192
Mvaa 76,24,188
S_ m0,s52,19200
Mvaa 76,20,148
Mvaa 68,24,184
L_ v6,28,s65,256
Mvaa 68,20,144
Mvaa 60,24,180
Mvaa 60,20,140
W_ 4
Mvaa 96,28,140
s_nop 0
Ta 20,v1,24576
Ta 22,v1,25088
Mvaa 96,32,180
Mvaa 104,28,144
Mvaa 104,32,184
s_nop 0
Ta 24,v1,28672
Ta 26,v1,29184
Mvaa 112,28,148
Mvaa 112,32,188
Mvaa 120,28,152
Mvaa 120,32,192
Mvaa 128,28,156
Mvaa 128,32,196
W_ 4
Mvaa 132,224,196
s_nop 0
Ta 28,v1,24584
Ta 30,v1,25096
Mvaa 132,36,156
Mvaa 124,224,192
Mvaa 124,36,152
s_nop 0
Ta 32,v1,28680
Ta 34,v1,29192
Mvaa 116,224,188
Mvaa 116,36,148
Mvaa 108,224,184
Mvaa 108,36,144
Mvaa 100,224,180
s_waitcnt vmcnt(13) lgkmcnt(0)
s_barrier
Mvaa 100,36,140
S_ s65,s59,6291456
Da 60,v2,0
Mvaa 136,20,140
S_ m0,s52,24448
Ta 36,%0,0
Ta 38,%0,512
Mvaa 136,24,180
L_ v6,28,s65,128
Mvaa 144,20,144
S_ s65,s59,6389760
Da 100,v3,0
Mvaa 144,24,184
S_ m0,s52,25472
Ta 224,%0,4096
Ta 226,%0,4608
Mvaa 152,20,148
Mvaa 152,24,188
Da 64,v2,2048
Mvaa 160,20,152
L_ v6,28,s65,128
Mvaa 160,24,192
S_ s65,s59,6488064
Mvaa 168,20,156
S_ m0,s52,26496
Da 104,v3,2048
Mvaa 168,24,196
Mvaa 172,32,196
s_nop 0
Ta 20,%0,8
Ta 22,%0,520
Mvaa 172,28,156
Da 68,v2,4096
Mvaa 164,32,192
L_ v6,28,s65,128
Mvaa 164,28,152
S_ s65,s59,6586368
Ta 24,%0,4104
Ta 26,%0,4616
Mvaa 156,32,188
S_ m0,s52,27520
Da 108,v3,4096
Mvaa 156,28,148
Mvaa 148,32,184
L_ v6,28,s65,128
Mvaa 148,28,144
Da 72,v2,6144
Mvaa 140,32,180
Mvaa 140,28,140
W_ 9
Mvaa 176,36,140
s_nop 0
Ta 28,v1,0
Ta 30,v1,512
Mvaa 176,224,180
Da 112,v3,6144
Mvaa 184,36,144
Mvaa 184,224,184
s_nop 0
Ta 32,v1,4096
Ta 34,v1,4608
Mvaa 192,36,148
Da 76,v2,8192
Mvaa 192,224,188
Mvaa 200,36,152
Da 116,v3,8192
Mvaa 200,224,192
Mvaa 208,36,156
Mvaa 208,224,196
W_ 9
Mvaa 212,24,196
s_nop 0
Ta 36,v1,8
Ta 38,v1,520
Mvaa 212,20,156
Mvaa 204,24,192
Mvaa 204,20,152
s_nop 0
Ta 224,v1,4104
Ta 226,v1,4616
Mvaa 196,24,188
Mvaa 196,20,148
Mvaa 188,24,184
Mvaa 188,20,144
Mvaa 180,24,180
s_waitcnt vmcnt(8) lgkmcnt(0)
s_barrier
Mvaa 180,20,140
S_ s65,s59,7077888
Mvaa 216,28,140
S_ m0,s52,-128
Ta 20,v1,8200
Ta 22,v1,8712
Mvaa 216,32,180
L_ v6,28,s65,128
Mvaa 224,28,144
S_ s65,s59,7176192
Mvaa 224,32,184
S_ m0,s52,896
Ta 24,v1,12296
Ta 26,v1,12808
Mvaa 232,28,148
Mvaa 232,32,188
L_ v6,28,s65,128
Mvaa 240,28,152
S_ s65,s59,7274496
Mvaa 240,32,192
S_ m0,s52,1920
Mvaa 248,28,156
Mvaa 248,32,196
Mvaa 252,224,196
s_nop 0
Ta 28,v1,8192
Ta 30,v1,8704
Mvaa 252,36,156
L_ v6,28,s65,128
Mvaa 244,224,192
S_ s65,s59,7372800
Mvaa 244,36,152
S_ m0,s52,2944
Ta 32,v1,12288
Ta 34,v1,12800
Mvaa 236,224,188
Mvaa 236,36,148
L_ v6,28,s65,128
Mvaa 228,224,184
S_ s65,s60,0
Mvaa 228,36,144
S_ m0,s50,0
Mvaa 220,224,180
Mvaa 220,36,140
W_ 4
Mvaa 220,20,60
s_nop 0
Ta 36,%0,8200
Ta 38,%0,8712
Mvaa 220,24,100
Da 140,v2,20480
Mvaa 228,20,64
L_ v4,24,s65,0
Mvaa 228,24,104
S_ s65,s60,10240
Ta 224,%0,12296
Ta 226,%0,12808
Mvaa 236,20,68
S_ m0,s50,1024
Da 180,v3,20480
Mvaa 236,24,108
Mvaa 244,20,72
Da 144,v2,22528
Mvaa 244,24,112
L_ v5,24,s65,0
Mvaa 252,20,76
S_ s65,s60,20480
Mvaa 252,24,116
W_ 7
Da 184,v3,22528
S_ m0,s50,2048
Mvaa 248,32,116
s_nop 0
Ta 20,%0,8192
Ta 22,%0,8704
Mvaa 248,28,76
L_ v4,24,s65,0
Mvaa 240,32,112
S_ s65,s60,30720
Da 148,v2,24576
Mvaa 240,28,72
S_ m0,s50,3072
Ta 24,%0,12288
Ta 26,%0,12800
Mvaa 232,32,108
Mvaa 232,28,68
Da 188,v3,24576
Mvaa 224,32,104
L_ v5,24,s65,0
Mvaa 224,28,64
S_ s65,s60,40960
Mvaa 216,32,100
s_mov_b64 exec, 0
s_bitcmp1_b32 s4, 1
s_cbranch_scc1 .Lhop_q2_b
s_bitcmp1_b32 s4, 0
s_cbranch_scc1 .Lpad_q2_1
s_branch .Lpad_q2_0
.Lhop_q2_b:
s_bitcmp1_b32 s4, 0
s_cbranch_scc1 .Lpad_q2_3
s_branch .Lpad_q2_2
.Lhop_q2_r:
s_mov_b64 exec, -1
s_nop 1
.Lqy_late:
s_waitcnt vmcnt(12) lgkmcnt(0)
s_barrier
Da 152,v2,26624
Mvaa 216,28,60
S_ m0,s50,4096
Mvaa 180,36,60
s_nop 0
Ta 28,v1,16392
Ta 30,v1,16904
Mvaa 180,224,100
Da 192,v3,26624
Mvaa 188,36,64
L_ v4,24,s65,0
Mvaa 188,224,104
S_ s65,s59,6291456
Ta 32,v1,20488
Ta 34,v1,21000
Mvaa 196,36,68
S_ m0,s52,8192
Da 156,v2,28672
Mvaa 196,224,108
Mvaa 204,36,72
L_ v6,28,s65,0
Mvaa 204,224,112
S_ s65,s59,6389760
Da 196,v3,28672
Mvaa 212,36,76
S_ m0,s52,9216
Mvaa 212,224,116
Mvaa 208,24,116
s_nop 0
Ta 36,v1,16384
Ta 38,v1,16896
Mvaa 208,20,76
L_ v6,28,s65,0
Mvaa 200,24,112
S_ s65,s59,6488064
Mvaa 200,20,72
S_ m0,s52,10240
Ta 224,v1,20480
Ta 226,v1,20992
Mvaa 192,24,108
Mvaa 192,20,68
L_ v6,28,s65,0
Mvaa 184,24,104
S_ s65,s59,6586368
Mvaa 184,20,64
S_ m0,s52,11264
Mvaa 176,24,100
Mvaa 176,20,60
W_ 6
Mvaa 220,28,140
s_nop 0
Ta 20,%0,16392
Ta 22,%0,16904
Mvaa 220,32,180
L_ v6,28,s65,0
Mvaa 228,28,144
Mvaa 228,32,184
s_nop 0
Ta 24,%0,20488
Ta 26,%0,21000
Mvaa 236,28,148
Mvaa 236,32,188
Mvaa 244,28,152
Mvaa 244,32,192
W_ 9
Mvaa 252,28,156
W_ 8
Mvaa 252,32,196
W_ 4
Mvaa 248,224,196
s_nop 0
Ta 28,%0,16384
Ta 30,%0,16896
Mvaa 248,36,156
Mvaa 240,224,192
Mvaa 240,36,152
s_nop 0
Ta 32,%0,20480
Ta 34,%0,20992
Mvaa 232,224,188
Mvaa 232,36,148
Mvaa 224,224,184
Mvaa 224,36,144
Mvaa 216,224,180
s_waitcnt vmcnt(13) lgkmcnt(0)
s_barrier
Mvaa 216,36,140
S_ s65,s59,7077888
Mvaa 180,20,140
S_ m0,s52,16384
Ta 36,v1,24584
Ta 38,v1,25096
Mvaa 180,24,180
L_ v6,28,s65,0
Mvaa 188,20,144
S_ s65,s59,7176192
Mvaa 188,24,184
S_ m0,s52,17408
Ta 224,v1,28680
Ta 226,v1,29192
Mvaa 196,20,148
U_ v248,s48,v248
U_ v249,s48,v249
Mvaa 196,24,188
L_ v6,28,s65,0
Mvaa 204,20,152
S_ s65,s59,7274496
U_ v250,s48,v250
U_ v251,s48,v251
Mvaa 204,24,192
S_ m0,s52,18432
U_ v252,s48,v252
U_ v253,s48,v253
Mvaa 212,20,156
U_ v254,s48,v254
U_ v255,s48,v255
Mvaa 212,24,196
C_ 255,254,255
C_ 254,252,253
Mvaa 208,32,196
s_nop 0
Ta 20,v1,24576
Ta 22,v1,25088
Mvaa 208,28,156
L_ v6,28,s65,0
Mvaa 200,32,192
S_ s65,s59,7372800
C_ 253,250,251
C_ 252,248,249
Mvaa 200,28,152
S_ m0,s52,19456
Ta 24,v1,28672
Ta 26,v1,29184
Mvaa 192,32,188
X_ a216,v252
X_ a217,v253
Mvaa 192,28,148
L_ v6,28,s65,0
Mvaa 184,32,184
X_ a218,v254
X_ a219,v255
Mvaa 184,28,144
U_ v240,s48,v240
U_ v241,s48,v241
Mvaa 176,32,180
U_ v242,s48,v242
U_ v243,s48,v243
Mvaa 176,28,140
W_ 4
U_ v244,s48,v244
U_ v245,s48,v245
Mvaa 140,36,60
s_nop 0
Ta 28,%0,24584
Ta 30,%0,25096
Mvaa 140,224,100
U_ v246,s48,v246
U_ v247,s48,v247
Mvaa 148,36,64
C_ 251,246,247
C_ 250,244,245
Mvaa 148,224,104
s_nop 0
Ta 32,%0,28680
Ta 34,%0,29192
Mvaa 156,36,68
C_ 249,242,243
C_ 248,240,241
Mvaa 156,224,108
X_ a212,v248
X_ a213,v249
Mvaa 164,36,72
X_ a214,v250
X_ a215,v251
Mvaa 164,224,112
U_ v232,s48,v232
U_ v233,s48,v233
Mvaa 172,36,76
U_ v234,s48,v234
U_ v235,s48,v235
Mvaa 172,224,116
W_ 4
U_ v236,s48,v236
U_ v237,s48,v237
Mvaa 168,24,116
s_nop 0
Ta 36,%0,24576
Ta 38,%0,25088
Mvaa 168,20,76
U_ v238,s48,v238
U_ v239,s48,v239
Mvaa 160,24,112
C_ 247,238,239
C_ 246,236,237
Mvaa 160,20,72
s_nop 0
Ta 224,%0,28672
Ta 226,%0,29184
Mvaa 152,24,108
C_ 245,234,235
C_ 244,232,233
Mvaa 152,20,68
X_ a208,v244
X_ a209,v245
Mvaa 144,24,104
X_ a210,v246
X_ a211,v247
Mvaa 144,20,64
U_ v224,s48,v224
U_ v225,s48,v225
Mvaa 136,24,100
s_waitcnt vmcnt(13) lgkmcnt(0)
s_barrier
U_ v226,s48,v226
Mvaa 136,20,60
S_ s65,s59,-768
U_ v227,s48,v227
Mvaa 100,28,60
S_ m0,s52,24320
Ta 20,v1,8
Ta 22,v1,520
Mvaa 100,32,100
L_ v6,28,s65,256
Mvaa 108,28,64
S_ s65,s59,97536
U_ v228,s48,v228
U_ v229,s48,v229
Mvaa 108,32,104
S_ m0,s52,25344
Ta 24,v1,4104
Ta 26,v1,4616
Mvaa 116,28,68
U_ v230,s48,v230
U_ v231,s48,v231
Mvaa 116,32,108
L_ v6,28,s65,256
Mvaa 124,28,72
S_ s65,s59,195840
C_ 243,230,231
C_ 242,228,229
Mvaa 124,32,112
S_ m0,s52,26368
C_ 241,226,227
C_ 240,224,225
Mvaa 132,28,76
X_ a204,v240
X_ a205,v241
Mvaa 132,32,116
X_ a206,v242
X_ a207,v243
Mvaa 128,224,116
s_nop 0
Ta 28,v1,0
Ta 30,v1,512
Mvaa 128,36,76
L_ v6,28,s65,256
Mvaa 120,224,112
S_ s65,s59,294144
U_ v216,s48,v216
U_ v217,s48,v217
Mvaa 120,36,72
S_ m0,s52,27392
Ta 32,v1,4096
Ta 34,v1,4608
Mvaa 112,224,108
.Lpad_q2_0:
s_cbranch_execz .Lpad_q2_4
U_ v218,s48,v218
U_ v219,s48,v219
Mvaa 112,36,68
L_ v6,28,s65,256
Mvaa 104,224,104
S_ s65,s60,128
U_ v220,s48,v220
U_ v221,s48,v221
Mvaa 104,36,64
S_ m0,s50,20480
U_ v222,s48,v222
U_ v223,s48,v223
Mvaa 96,224,100
C_ 239,222,223
C_ 238,220,221
Mvaa 96,36,60
W_ 4
C_ 237,218,219
C_ 236,216,217
Mvaa 140,20,140
s_nop 0
Ta 36,%0,8
Ta 38,%0,520
Mvaa 140,24,180
L_ v4,24,s65,0
Mvaa 148,20,144
S_ s65,s60,10368
X_ a200,v236
X_ a201,v237
Mvaa 148,24,184
S_ m0,s50,21504
Ta 224,%0,4104
Ta 226,%0,4616
Mvaa 156,20,148
X_ a202,v238
X_ a203,v239
Mvaa 156,24,188
L_ v5,24,s65,0
Mvaa 164,20,152
S_ s65,s60,20608
U_ v208,s48,v208
U_ v209,s48,v209
Mvaa 164,24,192
S_ m0,s50,22528
U_ v210,s48,v210
U_ v211,s48,v211
Mvaa 172,20,156
U_ v212,s48,v212
U_ v213,s48,v213
Mvaa 172,24,196
W_ 4
U_ v214,s48,v214
U_ v215,s48,v215
Mvaa 168,32,196
s_nop 0
Ta 20,%0,0
Ta 22,%0,512
Mvaa 168,28,156
L_ v4,24,s65,0
Mvaa 160,32,192
S_ s65,s60,30848
C_ 235,214,215
C_ 234,212,213
Mvaa 160,28,152
S_ m0,s50,23552
Ta 24,%0,4096
Ta 26,%0,4608
Mvaa 152,32,188
C_ 233,210,211
C_ 232,208,209
Mvaa 152,28,148
L_ v5,24,s65,0
Mvaa 144,32,184
S_ s65,s60,41088
X_ a176,v232
X_ a177,v233
Mvaa 144,28,144
S_ m0,s50,24576
X_ a178,v234
X_ a179,v235
Mvaa 136,32,180
s_waitcnt vmcnt(12) lgkmcnt(0)
s_barrier
U_ v200,s48,v200
Mvaa 136,28,140
L_ v4,24,s65,0
Mvaa 100,36,140
S_ s65,s59,785664
Ta 28,v1,8200
Ta 30,v1,8712
Mvaa 100,224,180
S_ m0,s52,0
U_ v201,s48,v201
Mvaa 108,36,144
U_ v202,s48,v202
U_ v203,s48,v203
Mvaa 108,224,184
s_nop 0
Ta 32,v1,12296
Ta 34,v1,12808
Mvaa 116,36,148
L_ v6,28,s65,0
Mvaa 116,224,188
S_ s65,s59,883968
U_ v204,s48,v204
U_ v205,s48,v205
Mvaa 124,36,152
S_ m0,s52,1024
U_ v206,s48,v206
U_ v207,s48,v207
Mvaa 124,224,192
C_ 231,206,207
C_ 230,204,205
Mvaa 132,36,156
L_ v6,28,s65,0
Mvaa 132,224,196
C_ 229,202,203
S_ s65,s59,982272
Mvaa 128,24,196
S_ m0,s52,2048
Ta 36,v1,8192
Ta 38,v1,8704
Mvaa 128,20,156
C_ 228,200,201
Mvaa 120,24,192
L_ v6,28,s65,0
Mvaa 120,20,152
S_ s65,s59,1080576
Ta 224,v1,12288
Ta 226,v1,12800
Mvaa 112,24,188
S_ m0,s52,3072
X_ a172,v228
X_ a173,v229
Mvaa 112,20,148
X_ a174,v230
X_ a175,v231
Mvaa 104,24,184
L_ v6,28,s65,0
Mvaa 104,20,144
U_ v192,s48,v192
U_ v193,s48,v193
Mvaa 96,24,180
U_ v194,s48,v194
U_ v195,s48,v195
Mvaa 96,20,140
W_ 4
U_ v196,s48,v196
U_ v197,s48,v197
Mvaa 60,28,60
s_nop 0
Ta 20,%0,8200
Ta 22,%0,8712
Mvaa 60,32,100
U_ v198,s48,v198
U_ v199,s48,v199
Mvaa 68,28,64
C_ 227,198,199
C_ 226,196,197
Mvaa 68,32,104
s_nop 0
Ta 24,%0,12296
Ta 26,%0,12808
Mvaa 76,28,68
C_ 225,194,195
C_ 224,192,193
Mvaa 76,32,108
X_ a168,v224
X_ a169,v225
Mvaa 84,28,72
X_ a170,v226
X_ a171,v227
Mvaa 84,32,112
U_ v184,s48,v184
U_ v185,s48,v185
Mvaa 92,28,76
U_ v186,s48,v186
U_ v187,s48,v187
Mvaa 92,32,116
W_ 4
U_ v188,s48,v188
U_ v189,s48,v189
Mvaa 88,224,116
s_nop 0
Ta 28,%0,8192
Ta 30,%0,8704
Mvaa 88,36,76
U_ v190,s48,v190
U_ v191,s48,v191
Mvaa 80,224,112
C_ 223,190,191
C_ 222,188,189
Mvaa 80,36,72
s_nop 0
Ta 32,%0,12288
Ta 34,%0,12800
Mvaa 72,224,108
C_ 221,186,187
C_ 220,184,185
Mvaa 72,36,68
X_ a164,v220
X_ a165,v221
Mvaa 64,224,104
X_ a166,v222
X_ a167,v223
Mvaa 64,36,64
U_ v176,s48,v176
U_ v177,s48,v177
Mvaa 56,224,100
s_waitcnt vmcnt(13) lgkmcnt(0)
s_barrier
U_ v178,s48,v178
Mvaa 56,36,60
S_ s65,s59,-768
U_ v179,s48,v179
Mvaa 20,20,60
S_ m0,s52,8192
Ta 36,v1,16392
Ta 38,v1,16904
Mvaa 20,24,100
L_ v6,28,s65,0
Mvaa 28,20,64
S_ s65,s59,97536
U_ v180,s48,v180
U_ v181,s48,v181
Mvaa 28,24,104
S_ m0,s52,9216
Ta 224,v1,20488
Ta 226,v1,21000
Mvaa 36,20,68
U_ v182,s48,v182
U_ v183,s48,v183
Mvaa 36,24,108
L_ v6,28,s65,0
Mvaa 44,20,72
S_ s65,s59,195840
C_ 219,182,183
C_ 218,180,181
Mvaa 44,24,112
S_ m0,s52,10240
C_ 217,178,179
C_ 216,176,177
Mvaa 52,20,76
X_ a160,v216
X_ a161,v217
Mvaa 52,24,116
X_ a162,v218
X_ a163,v219
Mvaa 48,32,116
s_nop 0
Ta 20,v1,16384
Ta 22,v1,16896
Mvaa 48,28,76
L_ v6,28,s65,0
Mvaa 40,32,112
S_ s65,s59,294144
U_ v168,s48,v168
U_ v169,s48,v169
Mvaa 40,28,72
S_ m0,s52,11264
Ta 24,v1,20480
Ta 26,v1,20992
Mvaa 32,32,108
U_ v170,s48,v170
U_ v171,s48,v171
Mvaa 32,28,68
L_ v6,28,s65,0
Mvaa 24,32,104
U_ v172,s48,v172
U_ v173,s48,v173
Mvaa 24,28,64
U_ v174,s48,v174
U_ v175,s48,v175
Mvaa 16,32,100
C_ 215,174,175
C_ 214,172,173
Mvaa 16,28,60
W_ 4
C_ 213,170,171
C_ 212,168,169
Mvaa 60,36,140
s_nop 0
Ta 28,%0,16392
Ta 30,%0,16904
Mvaa 60,224,180
X_ a136,v212
X_ a137,v213
Mvaa 68,36,144
X_ a138,v214
X_ a139,v215
Mvaa 68,224,184
s_nop 0
Ta 32,%0,20488
Ta 34,%0,21000
Mvaa 76,36,148
U_ v160,s48,v160
U_ v161,s48,v161
Mvaa 76,224,188
U_ v162,s48,v162
U_ v163,s48,v163
Mvaa 84,36,152
U_ v164,s48,v164
U_ v165,s48,v165
Mvaa 84,224,192
U_ v166,s48,v166
U_ v167,s48,v167
Mvaa 92,36,156
C_ 211,166,167
C_ 210,164,165
Mvaa 92,224,196
W_ 4
C_ 209,162,163
C_ 208,160,161
Mvaa 88,24,196
s_nop 0
Ta 36,%0,16384
Ta 38,%0,16896
Mvaa 88,20,156
X_ a132,v208
X_ a133,v209
Mvaa 80,24,192
X_ a134,v210
X_ a135,v211
Mvaa 80,20,152
s_nop 0
Ta 224,%0,20480
Ta 226,%0,20992
Mvaa 72,24,188
U_ v152,s48,v152
U_ v153,s48,v153
Mvaa 72,20,148
U_ v154,s48,v154
U_ v155,s48,v155
Mvaa 64,24,184
U_ v156,s48,v156
U_ v157,s48,v157
Mvaa 64,20,144
U_ v158,s48,v158
U_ v159,s48,v159
Mvaa 56,24,180
s_waitcnt vmcnt(63) lgkmcnt(0)
s_barrier
C_ 207,158,159
Mvaa 56,20,140
S_ s65,s59,-768
C_ 206,156,157
Mvaa 20,28,140
S_ m0,s52,16256
C_ 205,154,155
C_ 204,152,153
Mvaa 20,32,180
L_ v6,28,s65,128
Mvaa 28,28,144
S_ s65,s59,97536
X_ a128,v204
X_ a129,v205
Mvaa 28,32,184
S_ m0,s52,17280
X_ a130,v206
X_ a131,v207
Mvaa 36,28,148
U_ v144,s48,v144
U_ v145,s48,v145
Mvaa 36,32,188
L_ v6,28,s65,128
Mvaa 44,28,152
S_ s65,s59,195840
U_ v146,s48,v146
U_ v147,s48,v147
Mvaa 44,32,192
S_ m0,s52,18304
U_ v148,s48,v148
U_ v149,s48,v149
Mvaa 52,28,156
U_ v150,s48,v150
U_ v151,s48,v151
Mvaa 52,32,196
C_ 203,150,151
C_ 202,148,149
Mvaa 48,224,196
L_ v6,28,s65,128
Mvaa 48,36,156
S_ s65,s59,294144
C_ 201,146,147
C_ 200,144,145
Mvaa 40,224,192
S_ m0,s52,19328
X_ a124,v200
X_ a125,v201
Mvaa 40,36,152
X_ a126,v202
X_ a127,v203
Mvaa 32,224,188
L_ v6,28,s65,128
Mvaa 32,36,148
U_ v136,s48,v136
U_ v137,s48,v137
Mvaa 24,224,184
U_ v138,s48,v138
U_ v139,s48,v139
Mvaa 24,36,144
U_ v140,s48,v140
U_ v141,s48,v141
Mvaa 16,224,180
U_ v142,s48,v142
U_ v143,s48,v143
Mvaa 16,36,140
C_ 199,142,143
C_ 198,140,141
C_ 197,138,139
C_ 196,136,137
X_ a120,v196
X_ a121,v197
X_ a122,v198
X_ a123,v199
U_ v128,s48,v128
U_ v129,s48,v129
U_ v130,s48,v130
U_ v131,s48,v131
U_ v132,s48,v132
U_ v133,s48,v133
U_ v134,s48,v134
U_ v135,s48,v135
C_ 195,134,135
C_ 194,132,133
C_ 193,130,131
C_ 192,128,129
X_ a96,v192
X_ a97,v193
X_ a98,v194
X_ a99,v195
U_ v120,s48,v120
U_ v121,s48,v121
U_ v122,s48,v122
U_ v123,s48,v123
U_ v124,s48,v124
U_ v125,s48,v125
U_ v126,s48,v126
U_ v127,s48,v127
C_ 191,126,127
C_ 190,124,125
C_ 189,122,123
C_ 188,120,121
X_ a92,v188
X_ a93,v189
X_ a94,v190
X_ a95,v191
U_ v112,s48,v112
U_ v113,s48,v113
U_ v114,s48,v114
U_ v115,s48,v115
U_ v116,s48,v116
U_ v117,s48,v117
U_ v118,s48,v118
U_ v119,s48,v119
C_ 187,118,119
C_ 186,116,117
C_ 185,114,115
C_ 184,112,113
X_ a88,v184
X_ a89,v185
X_ a90,v186
X_ a91,v187
U_ v104,s48,v104
U_ v105,s48,v105
U_ v106,s48,v106
U_ v107,s48,v107
U_ v108,s48,v108
U_ v109,s48,v109
U_ v110,s48,v110
U_ v111,s48,v111
C_ 183,110,111
C_ 182,108,109
C_ 181,106,107
C_ 180,104,105
X_ a84,v180
X_ a85,v181
X_ a86,v182
X_ a87,v183
U_ v96,s48,v96
U_ v97,s48,v97
U_ v98,s48,v98
U_ v99,s48,v99
U_ v100,s48,v100
U_ v101,s48,v101
U_ v102,s48,v102
U_ v103,s48,v103
C_ 179,102,103
C_ 178,100,101
C_ 177,98,99
C_ 176,96,97
X_ a80,v176
X_ a81,v177
X_ a82,v178
X_ a83,v179
v_pk_mul_f32 v[88:89], v[88:89], s[48:49]
v_pk_mul_f32 v[90:91], v[90:91], s[48:49]
v_pk_mul_f32 v[92:93], v[92:93], s[48:49]
v_pk_mul_f32 v[94:95], v[94:95], s[48:49]
C_ 175,94,95
C_ 174,92,93
C_ 173,90,91
C_ 172,88,89
X_ a56,v172
X_ a57,v173
X_ a58,v174
X_ a59,v175
v_pk_mul_f32 v[80:81], v[80:81], s[48:49]
v_pk_mul_f32 v[82:83], v[82:83], s[48:49]
v_pk_mul_f32 v[84:85], v[84:85], s[48:49]
v_pk_mul_f32 v[86:87], v[86:87], s[48:49]
C_ 171,86,87
C_ 170,84,85
C_ 169,82,83
C_ 168,80,81
X_ a52,v168
X_ a53,v169
X_ a54,v170
X_ a55,v171
v_pk_mul_f32 v[72:73], v[72:73], s[48:49]
v_pk_mul_f32 v[74:75], v[74:75], s[48:49]
v_pk_mul_f32 v[76:77], v[76:77], s[48:49]
v_pk_mul_f32 v[78:79], v[78:79], s[48:49]
C_ 167,78,79
C_ 166,76,77
C_ 165,74,75
C_ 164,72,73
X_ a48,v164
X_ a49,v165
X_ a50,v166
X_ a51,v167
v_pk_mul_f32 v[64:65], v[64:65], s[48:49]
v_pk_mul_f32 v[66:67], v[66:67], s[48:49]
v_pk_mul_f32 v[68:69], v[68:69], s[48:49]
v_pk_mul_f32 v[70:71], v[70:71], s[48:49]
C_ 163,70,71
C_ 162,68,69
C_ 161,66,67
C_ 160,64,65
X_ a44,v160
X_ a45,v161
X_ a46,v162
X_ a47,v163
v_pk_mul_f32 v[56:57], v[56:57], s[48:49]
v_pk_mul_f32 v[58:59], v[58:59], s[48:49]
v_pk_mul_f32 v[60:61], v[60:61], s[48:49]
v_pk_mul_f32 v[62:63], v[62:63], s[48:49]
C_ 159,62,63
C_ 158,60,61
C_ 157,58,59
C_ 156,56,57
X_ a40,v156
X_ a41,v157
X_ a42,v158
X_ a43,v159
v_pk_mul_f32 v[48:49], v[48:49], s[48:49]
v_pk_mul_f32 v[50:51], v[50:51], s[48:49]
v_pk_mul_f32 v[52:53], v[52:53], s[48:49]
v_pk_mul_f32 v[54:55], v[54:55], s[48:49]
C_ 155,54,55
C_ 154,52,53
C_ 153,50,51
C_ 152,48,49
X_ a16,v152
X_ a17,v153
X_ a18,v154
X_ a19,v155
v_pk_mul_f32 v[40:41], v[40:41], s[48:49]
v_pk_mul_f32 v[42:43], v[42:43], s[48:49]
v_pk_mul_f32 v[44:45], v[44:45], s[48:49]
v_pk_mul_f32 v[46:47], v[46:47], s[48:49]
C_ 151,46,47
C_ 150,44,45
C_ 149,42,43
C_ 148,40,41
X_ a12,v148
X_ a13,v149
X_ a14,v150
X_ a15,v151
s_sub_u32 s59, s59, 768
s_waitcnt vmcnt(0) lgkmcnt(0)
s_barrier
Ta 20,%0,8192
Ta 22,%0,8704
Ta 24,%0,12288
Ta 26,%0,12800
Da 60,v2,0
Da 100,v3,0
Da 64,v2,2048
Da 104,v3,2048
.Lpad_q2_1:
s_cbranch_execz .Lpad_q2_5
Da 68,v2,4096
Da 108,v3,4096
Da 72,v2,6144
Da 112,v3,6144
Da 76,v2,8192
Da 116,v3,8192
Ta 28,%0,8200
Ta 30,%0,8712
Ta 32,%0,12296
Ta 34,%0,12808
v_pk_mul_f32 v[32:33], v[32:33], s[48:49]
v_pk_mul_f32 v[34:35], v[34:35], s[48:49]
v_pk_mul_f32 v[36:37], v[36:37], s[48:49]
v_pk_mul_f32 v[38:39], v[38:39], s[48:49]
C_ 147,38,39
C_ 146,36,37
C_ 145,34,35
C_ 144,32,33
X_ a8,v144
X_ a9,v145
X_ a10,v146
X_ a11,v147
v_pk_mul_f32 v[24:25], v[24:25], s[48:49]
v_pk_mul_f32 v[26:27], v[26:27], s[48:49]
v_pk_mul_f32 v[28:29], v[28:29], s[48:49]
v_pk_mul_f32 v[30:31], v[30:31], s[48:49]
C_ 143,30,31
C_ 142,28,29
C_ 141,26,27
C_ 140,24,25
X_ a4,v140
X_ a5,v141
X_ a6,v142
X_ a7,v143
v_pk_mul_f32 v[16:17], v[16:17], s[48:49]
v_pk_mul_f32 v[18:19], v[18:19], s[48:49]
v_pk_mul_f32 v[20:21], v[20:21], s[48:49]
v_pk_mul_f32 v[22:23], v[22:23], s[48:49]
C_ 139,22,23
C_ 138,20,21
C_ 137,18,19
C_ 136,16,17
X_ a0,v136
X_ a1,v137
X_ a2,v138
X_ a3,v139
s_sub_u32 s90, s90, s88
s_cmp_gt_u32 s90, 4000
s_cselect_b32 s11, 1, 0
s_sub_u32 s12, s68, s98
S_ s12,s12,1252
s_mov_b32 s13, 109
s_nop 1
.p2align 3
.Lq:
W_ 13
Zvaa 16,20,60
s_nop 0
Ta 36,v1,8192
Ta 38,v1,8704
W_ 14
Mvaa 16,24,100
R_ v12,a248
W_ 13
Zvaa 24,20,64
v_lshrrev_b32 v12, 4, v12
W_ 12
Mvaa 24,24,104
s_nop 0
Ta 224,v1,12288
Ta 226,v1,12800
W_ 13
Zvaa 32,20,68
v_min_u32 v10, 11, v12
W_ 12
Mvaa 32,24,108
s_mul_i32 s56, s4, 12
W_ 11
Zvaa 40,20,72
v_add_u32 v10, s56, v10
W_ 10
Mvaa 40,24,112
v_mul_u32_u24 v11, 43691, v10
W_ 9
Zvaa 48,20,76
v_lshrrev_b32 v11, 18, v11
W_ 8
Mvaa 48,24,116
W_ 4
v_lshlrev_b32 v10, 7, v10
Zvaa 52,32,116
s_nop 0
Ta 20,v1,8200
Ta 22,v1,8712
Mvaa 52,28,76
v_mul_u32_u24 v11, 11520, v11
Zvaa 44,32,112
v_add_u32 v10, v10, v11
Mvaa 44,28,72
s_nop 0
Ta 24,v1,12296
Ta 26,v1,12808
Zvaa 36,32,108
v_min_u32 v12, 9, v12
Mvaa 36,28,68
s_and_b32 s56, s6, 3
Zvaa 28,32,104
s_lshl_b32 s56, s56, 2
Mvaa 28,28,64
S_ s56,s56,s4
Zvaa 20,32,100
s_waitcnt vmcnt(18) lgkmcnt(0)
s_barrier
s_mul_i32 s56, s56, 10
Mvaa 20,28,60
S_ s65,s59,786432
v_add_u32 v12, s56, v12
Zvaa 56,36,60
S_ m0,s52,8064
Ta 28,%0,16384
Ta 30,%0,16896
Mvaa 56,224,100
L_ v6,28,s65,128
Zvaa 64,36,64
S_ s65,s59,884736
v_mul_u32_u24 v12, 1280, v12
Mvaa 64,224,104
S_ m0,s52,9088
Ta 32,%0,20480
Ta 34,%0,20992
Zvaa 72,36,68
s_lshr_b32 s56, s7, 5
Mvaa 72,224,108
L_ v6,28,s65,128
Zvaa 80,36,72
S_ s65,s59,983040
s_mul_i32 s56, s56, 98304
Mvaa 80,224,112
S_ m0,s52,10112
s_and_b32 s55, s59, 4095
Zvaa 88,36,76
S_ s56,s56,s55
Mvaa 88,224,116
s_mul_i32 s55, s77, 384
Zvaa 92,24,116
s_sub_u32 s56, s56, s55
Ta 36,%0,16392
Ta 38,%0,16904
Mvaa 92,20,76
L_ v6,28,s65,128
Zvaa 84,24,112
S_ s65,s59,1081344
s_mov_b32 s63, 0
Mvaa 84,20,72
S_ m0,s52,11136
Ta 224,%0,20488
Ta 226,%0,21000
Zvaa 76,24,108
s_bitcmp1_b32 1, s58
s_cselect_b32 s63, 0, 4026531840
Mvaa 76,20,68
L_ v6,28,s65,128
Zvaa 68,24,104
S_ s65,s60,256
S_ s56,s56,s63
Mvaa 68,20,64
S_ m0,s50,0
S_ s55,s56,3145728
Zvaa 60,24,100
Mvaa 60,20,60
W_ 4
Zvaa 96,28,60
s_nop 0
Ta 20,v1,16384
Ta 22,v1,16896
Mvaa 96,32,100
L_ v4,24,s65,0
Zvaa 104,28,64
S_ s65,s60,10496
Mvaa 104,32,104
S_ m0,s50,1024
Ta 24,v1,20480
Ta 26,v1,20992
Zvaa 112,28,68
Mvaa 112,32,108
L_ v5,24,s65,0
Zvaa 120,28,72
S_ s65,s60,20736
Mvaa 120,32,112
S_ m0,s50,2048
Zvaa 128,28,76
Mvaa 128,32,116
W_ 4
Zvaa 132,224,116
s_nop 0
Ta 28,v1,16392
Ta 30,v1,16904
Mvaa 132,36,76
L_ v4,24,s65,0
Zvaa 124,224,112
S_ s65,s60,30976
Mvaa 124,36,72
S_ m0,s50,3072
Ta 32,v1,20488
Ta 34,v1,21000
Zvaa 116,224,108
Mvaa 116,36,68
L_ v5,24,s65,0
Zvaa 108,224,104
S_ s65,s60,41216
Mvaa 108,36,64
S_ m0,s50,4096
Zvaa 100,224,100
s_cmp_eq_u32 s58, 0
s_cbranch_scc0 .Lq_early
s_mov_b64 exec, 0
s_bitcmp1_b32 s4, 1
s_cbranch_scc1 .Lhop_qx_b
s_bitcmp1_b32 s4, 0
s_cbranch_scc1 .Lpad_qx_1
s_branch .Lpad_qx_0
.Lhop_qx_b:
s_bitcmp1_b32 s4, 0
s_cbranch_scc1 .Lpad_qx_3
s_branch .Lpad_qx_2
.Lhop_qx_r:
s_mov_b64 exec, -1
s_nop 1
.Lq_early:
s_waitcnt vmcnt(17) lgkmcnt(0)
s_barrier
s_cmp_eq_u32 s11, 1
s_cbranch_scc1 .Lpace1
.Lpace1_back:
Mvaa 100,36,60
Da 140,v2,20480
Zvaa 136,20,60
s_nop 0
Ta 36,%0,24576
Ta 38,%0,25088
Mvaa 136,24,100
L_ v4,24,s65,0
Zvaa 144,20,64
S_ s65,s59,786432
Da 180,v3,20480
Mvaa 144,24,104
S_ m0,s52,16128
Ta 224,%0,28672
Ta 226,%0,29184
Zvaa 152,20,68
Mvaa 152,24,108
Da 144,v2,22528
Zvaa 160,20,72
L_ v6,28,s65,256
Mvaa 160,24,112
S_ s65,s59,884736
Zvaa 168,20,76
S_ m0,s52,17152
Da 184,v3,22528
Mvaa 168,24,116
Zvaa 172,32,116
s_nop 0
Ta 20,%0,24584
Ta 22,%0,25096
Mvaa 172,28,76
Da 148,v2,24576
Zvaa 164,32,112
L_ v6,28,s65,256
Mvaa 164,28,72
S_ s65,s59,983040
Ta 24,%0,28680
Ta 26,%0,29192
Zvaa 156,32,108
S_ m0,s52,18176
Da 188,v3,24576
Mvaa 156,28,68
Zvaa 148,32,104
L_ v6,28,s65,256
Mvaa 148,28,64
S_ s65,s59,1081344
Da 152,v2,26624
Zvaa 140,32,100
S_ m0,s52,19200
Mvaa 140,28,60
W_ 9
Zvaa 176,36,60
s_nop 0
Ta 28,v1,24576
Ta 30,v1,25088
Mvaa 176,224,100
Da 192,v3,26624
Zvaa 184,36,64
L_ v6,28,s65,256
Mvaa 184,224,104
s_nop 0
Ta 32,v1,28672
Ta 34,v1,29184
Zvaa 192,36,68
Da 156,v2,28672
Mvaa 192,224,108
Zvaa 200,36,72
Da 196,v3,28672
Mvaa 200,224,112
buffer_load_dword a254, v10, s[28:31], s55 offen
Zvaa 208,36,76
Mvaa 208,224,116
W_ 9
Zvaa 212,24,116
s_nop 0
Ta 36,v1,24584
Ta 38,v1,25096
Mvaa 212,20,76
buffer_load_dword a254, v12, s[24:27], s63 offen offset:512
Zvaa 204,24,112
Mvaa 204,20,72
s_nop 0
Ta 224,v1,28680
Ta 226,v1,29192
Zvaa 196,24,108
Mvaa 196,20,68
Zvaa 188,24,104
Mvaa 188,20,64
Zvaa 180,24,100
s_waitcnt vmcnt(20) lgkmcnt(0)
s_barrier
Mvaa 180,20,60
S_ s65,s59,1572864
Zvaa 216,28,60
S_ m0,s52,24576
Ta 20,%0,0
Ta 22,%0,512
Mvaa 216,32,100
L_ v6,28,s65,0
Zvaa 224,28,64
S_ s65,s59,1671168
Mvaa 224,32,104
S_ m0,s52,25600
Ta 24,%0,4096
Ta 26,%0,4608
Zvaa 232,28,68
Mvaa 232,32,108
L_ v6,28,s65,0
Zvaa 240,28,72
S_ s65,s59,1769472
Mvaa 240,32,112
S_ m0,s52,26624
Zvaa 248,28,76
Mvaa 248,32,116
Zvaa 252,224,116
s_nop 0
Ta 28,%0,8
Ta 30,%0,520
Mvaa 252,36,76
L_ v6,28,s65,0
Zvaa 244,224,112
S_ s65,s59,1867776
Mvaa 244,36,72
S_ m0,s52,27648
Ta 32,%0,4104
Ta 34,%0,4616
Zvaa 236,224,108
Mvaa 236,36,68
L_ v6,28,s65,0
Zvaa 228,224,104
S_ s65,s60,384
Mvaa 228,36,64
S_ m0,s50,20480
Zvaa 220,224,100
Mvaa 220,36,60
W_ 4
Mvaa 16,20,140
s_nop 0
Ta 36,v1,0
Ta 38,v1,512
Mvaa 16,24,180
L_ v4,24,s65,0
Mvaa 24,20,144
S_ s65,s60,10624
Mvaa 24,24,184
S_ m0,s50,21504
Ta 224,v1,4096
Ta 226,v1,4608
Mvaa 32,20,148
Mvaa 32,24,188
L_ v5,24,s65,0
Mvaa 40,20,152
S_ s65,s60,20864
Mvaa 40,24,192
S_ m0,s50,22528
Mvaa 48,20,156
Mvaa 48,24,196
W_ 4
Mvaa 52,32,196
s_nop 0
Ta 20,v1,8
Ta 22,v1,520
Mvaa 52,28,156
L_ v4,24,s65,0
Mvaa 44,32,192
S_ s65,s60,31104
Mvaa 44,28,152
S_ m0,s50,23552
Ta 24,v1,4104
Ta 26,v1,4616
Mvaa 36,32,188
Mvaa 36,28,148
L_ v5,24,s65,0
Mvaa 28,32,184
S_ s65,s60,41344
Mvaa 28,28,144
S_ m0,s50,24576
Mvaa 20,32,180
s_waitcnt vmcnt(19) lgkmcnt(0)
s_barrier
s_cmp_eq_u32 s11, 1
s_cbranch_scc1 .Lpace3
.Lpace3_back:
Mvaa 20,28,140
L_ v4,24,s65,0
Mvaa 56,36,140
S_ s65,s59,1572864
Ta 28,%0,8192
Ta 30,%0,8704
Mvaa 56,224,180
S_ m0,s52,-128
Mvaa 64,36,144
Mvaa 64,224,184
s_nop 0
Ta 32,%0,12288
Ta 34,%0,12800
Mvaa 72,36,148
L_ v6,28,s65,128
Mvaa 72,224,188
S_ s65,s59,1671168
Mvaa 80,36,152
S_ m0,s52,896
Mvaa 80,224,192
Mvaa 88,36,156
L_ v6,28,s65,128
Mvaa 88,224,196
S_ s65,s59,1769472
Mvaa 92,24,196
S_ m0,s52,1920
Ta 36,%0,8200
Ta 38,%0,8712
Mvaa 92,20,156
S_ s55,s56,3932160
Mvaa 84,24,192
L_ v6,28,s65,128
Mvaa 84,20,152
S_ s65,s59,1867776
Ta 224,%0,12296
Ta 226,%0,12808
Mvaa 76,24,188
S_ m0,s52,2944
Mvaa 76,20,148
Mvaa 68,24,184
L_ v6,28,s65,128
Mvaa 68,20,144
Mvaa 60,24,180
Mvaa 60,20,140
W_ 4
Mvaa 96,28,140
s_nop 0
Ta 20,v1,8192
Ta 22,v1,8704
Mvaa 96,32,180
buffer_load_dword a254, v10, s[28:31], s55 offen
Mvaa 104,28,144
Mvaa 104,32,184
s_nop 0
Ta 24,v1,12288
Ta 26,v1,12800
Mvaa 112,28,148
Mvaa 112,32,188
buffer_load_dword a254, v12, s[24:27], s63 offen offset:640
Mvaa 120,28,152
Mvaa 120,32,192
Mvaa 128,28,156
Mvaa 128,32,196
W_ 4
)A"
R"A(Mvaa 132,224,196
s_nop 0
Ta 28,v1,8200
Ta 30,v1,8712
Mvaa 132,36,156
Mvaa 124,224,192
Mvaa 124,36,152
s_nop 0
Ta 32,v1,12296
Ta 34,v1,12808
Mvaa 116,224,188
Mvaa 116,36,148
Mvaa 108,224,184
Mvaa 108,36,144
Mvaa 100,224,180
s_waitcnt vmcnt(17) lgkmcnt(0)
s_barrier
Mvaa 100,36,140
S_ s65,s59,1572864
Da 60,v2,0
Mvaa 136,20,140
S_ m0,s52,7936
Ta 36,%0,16384
Ta 38,%0,16896
Mvaa 136,24,180
L_ v6,28,s65,256
Mvaa 144,20,144
S_ s65,s59,1671168
Da 100,v3,0
Mvaa 144,24,184
S_ m0,s52,8960
Ta 224,%0,20480
Ta 226,%0,20992
Mvaa 152,20,148
Mvaa 152,24,188
Da 64,v2,2048
Mvaa 160,20,152
L_ v6,28,s65,256
Mvaa 160,24,192
S_ s65,s59,1769472
Mvaa 168,20,156
S_ m0,s52,9984
Da 104,v3,2048
Mvaa 168,24,196
Mvaa 172,32,196
s_nop 0
Ta 20,%0,16392
Ta 22,%0,16904
Mvaa 172,28,156
Da 68,v2,4096
Mvaa 164,32,192
L_ v6,28,s65,256
Mvaa 164,28,152
S_ s65,s59,1867776
Ta 24,%0,20488
Ta 26,%0,21000
Mvaa 156,32,188
S_ m0,s52,11008
Da 108,v3,4096
Mvaa 156,28,148
Mvaa 148,32,184
L_ v6,28,s65,256
Mvaa 148,28,144
Da 72,v2,6144
Mvaa 140,32,180
Mvaa 140,28,140
W_ 9
Mvaa 176,36,140
s_nop 0
Ta 28,v1,16384
Ta 30,v1,16896
Mvaa 176,224,180
Da 112,v3,6144
Mvaa 184,36,144
Mvaa 184,224,184
s_nop 0
Ta 32,v1,20480
Ta 34,v1,20992
Mvaa 192,36,148
.Lpad_q2_2:
s_cbranch_execz .Lpad_q2_6
Da 76,v2,8192
Mvaa 192,224,188
.Lpad_qx_0:
s_cbranch_execz .Lpad_qx_4
Mvaa 200,36,152
Da 116,v3,8192
Mvaa 200,224,192
Mvaa 208,36,156
Mvaa 208,224,196
W_ 9
Mvaa 212,24,196
s_nop 0
Ta 36,v1,16392
Ta 38,v1,16904
Mvaa 212,20,156
Mvaa 204,24,192
Mvaa 204,20,152
s_nop 0
Ta 224,v1,20488
Ta 226,v1,21000
Mvaa 196,24,188
Mvaa 196,20,148
Mvaa 188,24,184
Mvaa 188,20,144
Mvaa 180,24,180
s_waitcnt vmcnt(15) lgkmcnt(0)
s_barrier
s_cmp_eq_u32 s11, 1
s_cbranch_scc1 .Lpace5
.Lpace5_back:
Mvaa 180,20,140
S_ s65,s59,2359296
Mvaa 216,28,140
S_ m0,s52,16384
Ta 20,%0,24576
Ta 22,%0,25088
Mvaa 216,32,180
L_ v6,28,s65,0
Mvaa 224,28,144
S_ s65,s59,2457600
Mvaa 224,32,184
S_ m0,s52,17408
Ta 24,%0,28672
Ta 26,%0,29184
Mvaa 232,28,148
Mvaa 232,32,188
L_ v6,28,s65,0
Mvaa 240,28,152
S_ s65,s59,2555904
Mvaa 240,32,192
S_ m0,s52,18432
Mvaa 248,28,156
Mvaa 248,32,196
Mvaa 252,224,196
s_nop 0
Ta 28,%0,24584
Ta 30,%0,25096
Mvaa 252,36,156
L_ v6,28,s65,0
Mvaa 244,224,192
S_ s65,s59,2654208
Mvaa 244,36,152
S_ m0,s52,19456
Ta 32,%0,28680
Ta 34,%0,29192
Mvaa 236,224,188
Mvaa 236,36,148
L_ v6,28,s65,0
Mvaa 228,224,184
S_ s65,s60,512
Mvaa 228,36,144
S_ m0,s50,0
Mvaa 220,224,180
Mvaa 220,36,140
W_ 4
Mvaa 16,20,60
s_nop 0
Ta 36,v1,24576
Ta 38,v1,25088
Mvaa 16,24,100
L_ v4,24,s65,0
Mvaa 24,20,64
S_ s65,s60,10752
Mvaa 24,24,104
S_ m0,s50,1024
Ta 224,v1,28672
Ta 226,v1,29184
Mvaa 32,20,68
Mvaa 32,24,108
L_ v5,24,s65,0
Mvaa 40,20,72
S_ s65,s60,20992
Mvaa 40,24,112
S_ m0,s50,2048
Mvaa 48,20,76
Mvaa 48,24,116
W_ 4
Mvaa 52,32,116
s_nop 0
Ta 20,v1,24584
Ta 22,v1,25096
Mvaa 52,28,76
L_ v4,24,s65,0
Mvaa 44,32,112
S_ s65,s60,31232
Mvaa 44,28,72
S_ m0,s50,3072
Ta 24,v1,28680
Ta 26,v1,29192
Mvaa 36,32,108
Mvaa 36,28,68
L_ v5,24,s65,0
Mvaa 28,32,104
S_ s65,s60,41472
Mvaa 28,28,64
S_ m0,s50,4096
Mvaa 20,32,100
s_waitcnt vmcnt(14) lgkmcnt(0)
s_barrier
Mvaa 20,28,60
L_ v4,24,s65,0
Mvaa 56,36,60
S_ s65,s59,2359296
Ta 28,%0,0
Ta 30,%0,512
Mvaa 56,224,100
S_ m0,s52,24448
Mvaa 64,36,64
Mvaa 64,224,104
s_nop 0
Ta 32,%0,4096
Ta 34,%0,4608
Mvaa 72,36,68
L_ v6,28,s65,128
Mvaa 72,224,108
S_ s65,s59,2457600
Mvaa 80,36,72
S_ m0,s52,25472
Mvaa 80,224,112
Mvaa 88,36,76
L_ v6,28,s65,128
Mvaa 88,224,116
S_ s65,s59,2555904
Mvaa 92,24,116
S_ m0,s52,26496
Ta 36,%0,8
Ta 38,%0,520
Mvaa 92,20,76
S_ s55,s56,4718592
Mvaa 84,24,112
L_ v6,28,s65,128
Mvaa 84,20,72
S_ s65,s59,2654208
Ta 224,%0,4104
Ta 226,%0,4616
Mvaa 76,24,108
S_ m0,s52,27520
Mvaa 76,20,68
Mvaa 68,24,104
L_ v6,28,s65,128
Mvaa 68,20,64
Mvaa 60,24,100
Mvaa 60,20,60
W_ 4
Mvaa 96,28,60
s_nop 0
Ta 20,v1,0
Ta 22,v1,512
Mvaa 96,32,100
buffer_load_dword a254, v10, s[28:31], s55 offen
Mvaa 104,28,64
Mvaa 104,32,104
s_nop 0
Ta 24,v1,4096
Ta 26,v1,4608
Mvaa 112,28,68
Mvaa 112,32,108
buffer_load_dword a254, v12, s[24:27], s63 offen offset:768
Mvaa 120,28,72
Mvaa 120,32,112
Mvaa 128,28,76
Mvaa 128,32,116
W_ 4
Mvaa 132,224,116
s_nop 0
Ta 28,v1,8
Ta 30,v1,520
Mvaa 132,36,76
Mvaa 124,224,112
Mvaa 124,36,72
s_nop 0
Ta 32,v1,4104
Ta 34,v1,4616
Mvaa 116,224,108
Mvaa 116,36,68
Mvaa 108,224,104
Mvaa 108,36,64
Mvaa 100,224,100
s_waitcnt vmcnt(15) lgkmcnt(0)
s_barrier
s_cmp_eq_u32 s11, 1
s_cbranch_scc1 .Lpace7
.Lpace7_back:
Mvaa 100,36,60
S_ s65,s59,2359296
Da 140,v2,20480
Mvaa 136,20,60
S_ m0,s52,-256
Ta 36,%0,8192
Ta 38,%0,8704
Mvaa 136,24,100
L_ v6,28,s65,256
Mvaa 144,20,64
S_ s65,s59,2457600
Da 180,v3,20480
Mvaa 144,24,104
S_ m0,s52,768
Ta 224,%0,12288
Ta 226,%0,12800
Mvaa 152,20,68
Mvaa 152,24,108
Da 144,v2,22528
Mvaa 160,20,72
L_ v6,28,s65,256
Mvaa 160,24,112
S_ s65,s59,2555904
Mvaa 168,20,76
S_ m0,s52,1792
Da 184,v3,22528
Mvaa 168,24,116
Mvaa 172,32,116
s_nop 0
Ta 20,%0,8200
Ta 22,%0,8712
Mvaa 172,28,76
Da 148,v2,24576
Mvaa 164,32,112
L_ v6,28,s65,256
Mvaa 164,28,72
S_ s65,s59,2654208
Ta 24,%0,12296
Ta 26,%0,12808
Mvaa 156,32,108
S_ m0,s52,2816
Da 188,v3,24576
Mvaa 156,28,68
Mvaa 148,32,104
L_ v6,28,s65,256
Mvaa 148,28,64
Da 152,v2,26624
Mvaa 140,32,100
Mvaa 140,28,60
W_ 9
Mvaa 176,36,60
s_nop 0
Ta 28,v1,8192
Ta 30,v1,8704
Mvaa 176,224,100
Da 192,v3,26624
Mvaa 184,36,64
Mvaa 184,224,104
s_nop 0
Ta 32,v1,12288
Ta 34,v1,12800
Mvaa 192,36,68
Da 156,v2,28672
Mvaa 192,224,108
Mvaa 200,36,72
Da 196,v3,28672
Mvaa 200,224,112
Mvaa 208,36,76
Mvaa 208,224,116
W_ 9
Mvaa 212,24,116
s_nop 0
Ta 36,v1,8200
Ta 38,v1,8712
Mvaa 212,20,76
Mvaa 204,24,112
Mvaa 204,20,72
s_nop 0
Ta 224,v1,12296
Ta 226,v1,12808
Mvaa 196,24,108
Mvaa 196,20,68
Mvaa 188,24,104
Mvaa 188,20,64
Mvaa 180,24,100
s_waitcnt vmcnt(15) lgkmcnt(0)
s_barrier
Mvaa 180,20,60
S_ s65,s59,3145728
Mvaa 216,28,60
S_ m0,s52,8192
Ta 20,%0,16384
Ta 22,%0,16896
Mvaa 216,32,100
L_ v6,28,s65,0
Mvaa 224,28,64
S_ s65,s59,3244032
Mvaa 224,32,104
S_ m0,s52,9216
Ta 24,%0,20480
Ta 26,%0,20992
Mvaa 232,28,68
Mvaa 232,32,108
L_ v6,28,s65,0
Mvaa 240,28,72
S_ s65,s59,3342336
Mvaa 240,32,112
S_ m0,s52,10240
Mvaa 248,28,76
Mvaa 248,32,116
Mvaa 252,224,116
s_nop 0
Ta 28,%0,16392
Ta 30,%0,16904
Mvaa 252,36,76
L_ v6,28,s65,0
Mvaa 244,224,112
S_ s65,s59,3440640
Mvaa 244,36,72
S_ m0,s52,11264
Ta 32,%0,20488
Ta 34,%0,21000
Mvaa 236,224,108
Mvaa 236,36,68
L_ v6,28,s65,0
Mvaa 228,224,104
S_ s65,s60,640
Mvaa 228,36,64
S_ m0,s50,20480
Mvaa 220,224,100
Mvaa 220,36,60
W_ 4
Mvaa 16,20,140
s_nop 0
Ta 36,v1,16384
Ta 38,v1,16896
Mvaa 16,24,180
L_ v4,24,s65,0
Mvaa 24,20,144
S_ s65,s60,10880
Mvaa 24,24,184
S_ m0,s50,21504
Ta 224,v1,20480
Ta 226,v1,20992
Mvaa 32,20,148
Mvaa 32,24,188
L_ v5,24,s65,0
Mvaa 40,20,152
S_ s65,s60,21120
Mvaa 40,24,192
S_ m0,s50,22528
Mvaa 48,20,156
Mvaa 48,24,196
W_ 4
Mvaa 52,32,196
s_nop 0
Ta 20,v1,16392
Ta 22,v1,16904
Mvaa 52,28,156
L_ v4,24,s65,0
Mvaa 44,32,192
S_ s65,s60,31360
Mvaa 44,28,152
S_ m0,s50,23552
Ta 24,v1,20488
Ta 26,v1,21000
Mvaa 36,32,188
Mvaa 36,28,148
L_ v5,24,s65,0
Mvaa 28,32,184
S_ s65,s60,41600
Mvaa 28,28,144
S_ m0,s50,24576
Mvaa 20,32,180
s_waitcnt vmcnt(14) lgkmcnt(0)
s_barrier
s_cmp_eq_u32 s11, 1
s_cbranch_scc1 .Lpace9
.Lpace9_back:
Mvaa 20,28,140
L_ v4,24,s65,0
Mvaa 56,36,140
S_ s65,s59,3145728
Ta 28,%0,24576
Ta 30,%0,25088
Mvaa 56,224,180
S_ m0,s52,16256
Mvaa 64,36,144
Mvaa 64,224,184
s_nop 0
Ta 32,%0,28672
Ta 34,%0,29184
Mvaa 72,36,148
L_ v6,28,s65,128
Mvaa 72,224,188
S_ s65,s59,3244032
Mvaa 80,36,152
S_ m0,s52,17280
Mvaa 80,224,192
Mvaa 88,36,156
L_ v6,28,s65,128
Mvaa 88,224,196
S_ s65,s59,3342336
Mvaa 92,24,196
S_ m0,s52,18304
Ta 36,%0,24584
Ta 38,%0,25096
Mvaa 92,20,156
S_ s55,s56,5505024
Mvaa 84,24,192
L_ v6,28,s65,128
Mvaa 84,20,152
S_ s65,s59,3440640
Ta 224,%0,28680
Ta 226,%0,29192
Mvaa 76,24,188
S_ m0,s52,19328
Mvaa 76,20,148
Mvaa 68,24,184
L_ v6,28,s65,128
Mvaa 68,20,144
Mvaa 60,24,180
Mvaa 60,20,140
W_ 4
Mvaa 96,28,140
s_nop 0
Ta 20,v1,24576
Ta 22,v1,25088
Mvaa 96,32,180
buffer_load_dword a254, v10, s[28:31], s55 offen
Mvaa 104,28,144
Mvaa 104,32,184
s_nop 0
Ta 24,v1,28672
Ta 26,v1,29184
Mvaa 112,28,148
Mvaa 112,32,188
buffer_load_dword a254, v12, s[24:27], s63 offen offset:896
Mvaa 120,28,152
Mvaa 120,32,192
Mvaa 128,28,156
Mvaa 128,32,196
W_ 4
Mvaa 132,224,196
s_nop 0
Ta 28,v1,24584
Ta 30,v1,25096
Mvaa 132,36,156
Mvaa 124,224,192
Mvaa 124,36,152
s_nop 0
Ta 32,v1,28680
Ta 34,v1,29192
Mvaa 116,224,188
Mvaa 116,36,148
Mvaa 108,224,184
Mvaa 108,36,144
Mvaa 100,224,180
s_waitcnt vmcnt(15) lgkmcnt(0)
s_barrier
Mvaa 100,36,140
S_ s65,s59,3145728
Da 60,v2,0
Mvaa 136,20,140
S_ m0,s52,24320
Ta 36,%0,0
Ta 38,%0,512
Mvaa 136,24,180
L_ v6,28,s65,256
Mvaa 144,20,144
S_ s65,s59,3244032
Da 100,v3,0
Mvaa 144,24,184
S_ m0,s52,25344
Ta 224,%0,4096
Ta 226,%0,4608
Mvaa 152,20,148
Mvaa 152,24,188
Da 64,v2,2048
Mvaa 160,20,152
L_ v6,28,s65,256
Mvaa 160,24,192
S_ s65,s59,3342336
Mvaa 168,20,156
S_ m0,s52,26368
Da 104,v3,2048
Mvaa 168,24,196
Mvaa 172,32,196
s_nop 0
Ta 20,%0,8
Ta 22,%0,520
Mvaa 172,28,156
Da 68,v2,4096
Mvaa 164,32,192
L_ v6,28,s65,256
Mvaa 164,28,152
S_ s65,s59,3440640
Ta 24,%0,4104
Ta 26,%0,4616
Mvaa 156,32,188
S_ m0,s52,27392
Da 108,v3,4096
Mvaa 156,28,148
Mvaa 148,32,184
L_ v6,28,s65,256
Mvaa 148,28,144
Da 72,v2,6144
Mvaa 140,32,180
Mvaa 140,28,140
W_ 9
Mvaa 176,36,140
s_nop 0
Ta 28,v1,0
Ta 30,v1,512
Mvaa 176,224,180
Da 112,v3,6144
Mvaa 184,36,144
Mvaa 184,224,184
s_nop 0
Ta 32,v1,4096
Ta 34,v1,4608
Mvaa 192,36,148
Da 76,v2,8192
Mvaa 192,224,188
Mvaa 200,36,152
Da 116,v3,8192
Mvaa 200,224,192
Mvaa 208,36,156
Mvaa 208,224,196
W_ 9
Mvaa 212,24,196
s_nop 0
Ta 36,v1,8
Ta 38,v1,520
Mvaa 212,20,156
Mvaa 204,24,192
Mvaa 204,20,152
s_nop 0
Ta 224,v1,4104
Ta 226,v1,4616
Mvaa 196,24,188
.Lpad_q2_3:
s_cbranch_execz .Lhop_q2_r
Mvaa 196,20,148
.Lpad_qx_1:
s_cbranch_execz .Lhop_qx_r
Mvaa 188,24,184
Mvaa 188,20,144
Mvaa 180,24,180
s_waitcnt vmcnt(15) lgkmcnt(0)
s_barrier
s_cmp_eq_u32 s11, 1
s_cbranch_scc1 .Lpace11
.Lpace11_back:
Mvaa 180,20,140
S_ s65,s59,3932160
Mvaa 216,28,140
S_ m0,s52,0
Ta 20,%0,8192
Ta 22,%0,8704
Mvaa 216,32,180
L_ v6,28,s65,0
Mvaa 224,28,144
S_ s65,s59,4030464
Mvaa 224,32,184
S_ m0,s52,1024
Ta 24,%0,12288
Ta 26,%0,12800
Mvaa 232,28,148
Mvaa 232,32,188
L_ v6,28,s65,0
Mvaa 240,28,152
S_ s65,s59,4128768
Mvaa 240,32,192
S_ m0,s52,2048
Mvaa 248,28,156
Mvaa 248,32,196
Mvaa 252,224,196
s_nop 0
Ta 28,%0,8200
Ta 30,%0,8712
Mvaa 252,36,156
L_ v6,28,s65,0
Mvaa 244,224,192
S_ s65,s59,4227072
Mvaa 244,36,152
S_ m0,s52,3072
Ta 32,%0,12296
Ta 34,%0,12808
Mvaa 236,224,188
Mvaa 236,36,148
L_ v6,28,s65,0
Mvaa 228,224,184
S_ s65,s60,768
Mvaa 228,36,144
S_ m0,s50,0
Mvaa 220,224,180
Mvaa 220,36,140
W_ 4
Mvaa 16,20,60
s_nop 0
Ta 36,v1,8192
Ta 38,v1,8704
Mvaa 16,24,100
L_ v4,24,s65,0
Mvaa 24,20,64
S_ s65,s60,11008
R_ v13,a248
Mvaa 24,24,104
S_ m0,s50,1024
Ta 224,v1,12288
Ta 226,v1,12800
Mvaa 32,20,68
v_min_u32 v13, 240, v13
Mvaa 32,24,108
L_ v5,24,s65,0
Mvaa 40,20,72
S_ s65,s60,21248
v_lshlrev_b32 v13, 3, v13
Mvaa 40,24,112
S_ m0,s50,2048
Mvaa 48,20,76
Mvaa 48,24,116
W_ 4
Mvaa 52,32,116
s_nop 0
Ta 20,v1,8200
Ta 22,v1,8712
Mvaa 52,28,76
L_ v4,24,s65,0
Mvaa 44,32,112
S_ s65,s60,31488
Mvaa 44,28,72
S_ m0,s50,3072
Ta 24,v1,12296
Ta 26,v1,12808
Mvaa 36,32,108
Mvaa 36,28,68
L_ v5,24,s65,0
Mvaa 28,32,104
S_ s65,s60,41728
Mvaa 28,28,64
S_ m0,s50,4096
Mvaa 20,32,100
s_waitcnt vmcnt(14) lgkmcnt(0)
s_barrier
Mvaa 20,28,60
L_ v4,24,s65,0
Mvaa 56,36,60
S_ s65,s59,3932160
Ta 28,%0,16384
Ta 30,%0,16896
Mvaa 56,224,100
S_ m0,s52,8064
Mvaa 64,36,64
Mvaa 64,224,104
s_nop 0
Ta 32,%0,20480
Ta 34,%0,20992
Mvaa 72,36,68
L_ v6,28,s65,128
Mvaa 72,224,108
S_ s65,s59,4030464
Mvaa 80,36,72
S_ m0,s52,9088
Mvaa 80,224,112
Mvaa 88,36,76
L_ v6,28,s65,128
Mvaa 88,224,116
S_ s65,s59,4128768
Mvaa 92,24,116
S_ m0,s52,10112
Ta 36,%0,16392
Ta 38,%0,16904
Mvaa 92,20,76
S_ s55,s56,6291456
Mvaa 84,24,112
L_ v6,28,s65,128
Mvaa 84,20,72
S_ s65,s59,4227072
Ta 224,%0,20488
Ta 226,%0,21000
Mvaa 76,24,108
S_ m0,s52,11136
Mvaa 76,20,68
Mvaa 68,24,104
L_ v6,28,s65,128
Mvaa 68,20,64
Mvaa 60,24,100
Mvaa 60,20,60
W_ 4
Mvaa 96,28,60
s_nop 0
Ta 20,v1,16384
Ta 22,v1,16896
Mvaa 96,32,100
buffer_load_dword a220, v13, s[44:47], s66 offen
Mvaa 104,28,64
Mvaa 104,32,104
s_nop 0
Ta 24,v1,20480
Ta 26,v1,20992
Mvaa 112,28,68
Mvaa 112,32,108
buffer_load_dword a254, v10, s[28:31], s55 offen
Mvaa 120,28,72
Mvaa 120,32,112
Mvaa 128,28,76
Mvaa 128,32,116
W_ 4
Mvaa 132,224,116
s_nop 0
Ta 28,v1,16392
Ta 30,v1,16904
Mvaa 132,36,76
buffer_load_dword a254, v12, s[24:27], s63 offen offset:1024
Mvaa 124,224,112
Mvaa 124,36,72
s_nop 0
Ta 32,v1,20488
Ta 34,v1,21000
Mvaa 116,224,108
Mvaa 116,36,68
Mvaa 108,224,104
Mvaa 108,36,64
Mvaa 100,224,100
s_waitcnt vmcnt(16) lgkmcnt(0)
s_barrier
s_cmp_eq_u32 s11, 1
s_cbranch_scc1 .Lpace13
.Lpace13_back:
Mvaa 100,36,60
S_ s65,s59,3932160
Da 140,v2,20480
Mvaa 136,20,60
S_ m0,s52,16128
Ta 36,%0,24576
Ta 38,%0,25088
Mvaa 136,24,100
L_ v6,28,s65,256
Mvaa 144,20,64
S_ s65,s59,4030464
Da 180,v3,20480
Mvaa 144,24,104
S_ m0,s52,17152
Ta 224,%0,28672
Ta 226,%0,29184
Mvaa 152,20,68
Mvaa 152,24,108
Da 144,v2,22528
Mvaa 160,20,72
L_ v6,28,s65,256
Mvaa 160,24,112
S_ s65,s59,4128768
Mvaa 168,20,76
S_ m0,s52,18176
Da 184,v3,22528
Mvaa 168,24,116
Mvaa 172,32,116
s_nop 0
Ta 20,%0,24584
Ta 22,%0,25096
Mvaa 172,28,76
Da 148,v2,24576
Mvaa 164,32,112
L_ v6,28,s65,256
Mvaa 164,28,72
S_ s65,s59,4227072
Ta 24,%0,28680
Ta 26,%0,29192
Mvaa 156,32,108
S_ m0,s52,19200
Da 188,v3,24576
Mvaa 156,28,68
Mvaa 148,32,104
L_ v6,28,s65,256
Mvaa 148,28,64
Da 152,v2,26624
Mvaa 140,32,100
Mvaa 140,28,60
W_ 9
Mvaa 176,36,60
s_nop 0
Ta 28,v1,24576
Ta 30,v1,25088
Mvaa 176,224,100
Da 192,v3,26624
Mvaa 184,36,64
Mvaa 184,224,104
s_nop 0
Ta 32,v1,28672
Ta 34,v1,29184
Mvaa 192,36,68
Da 156,v2,28672
Mvaa 192,224,108
Mvaa 200,36,72
Da 196,v3,28672
Mvaa 200,224,112
Mvaa 208,36,76
Mvaa 208,224,116
W_ 9
Mvaa 212,24,116
s_nop 0
Ta 36,v1,24584
Ta 38,v1,25096
Mvaa 212,20,76
Mvaa 204,24,112
Mvaa 204,20,72
s_nop 0
Ta 224,v1,28680
Ta 226,v1,29192
Mvaa 196,24,108
Mvaa 196,20,68
Mvaa 188,24,104
Mvaa 188,20,64
Mvaa 180,24,100
s_waitcnt vmcnt(16) lgkmcnt(0)
s_barrier
Mvaa 180,20,60
S_ s65,s59,4718592
Mvaa 216,28,60
S_ m0,s52,24576
Ta 20,%0,0
Ta 22,%0,512
Mvaa 216,32,100
L_ v6,28,s65,0
Mvaa 224,28,64
S_ s65,s59,4816896
Mvaa 224,32,104
S_ m0,s52,25600
Ta 24,%0,4096
Ta 26,%0,4608
Mvaa 232,28,68
Mvaa 232,32,108
L_ v6,28,s65,0
Mvaa 240,28,72
S_ s65,s59,4915200
Mvaa 240,32,112
S_ m0,s52,26624
Mvaa 248,28,76
Mvaa 248,32,116
Mvaa 252,224,116
s_nop 0
Ta 28,%0,8
Ta 30,%0,520
Mvaa 252,36,76
L_ v6,28,s65,0
Mvaa 244,224,112
S_ s65,s59,5013504
Mvaa 244,36,72
S_ m0,s52,27648
Ta 32,%0,4104
Ta 34,%0,4616
Mvaa 236,224,108
Mvaa 236,36,68
L_ v6,28,s65,0
Mvaa 228,224,104
S_ s65,s60,896
Mvaa 228,36,64
S_ m0,s50,20480
Mvaa 220,224,100
Mvaa 220,36,60
W_ 4
Mvaa 16,20,140
s_nop 0
Ta 36,v1,0
Ta 38,v1,512
Mvaa 16,24,180
L_ v4,24,s65,0
Mvaa 24,20,144
S_ s65,s60,11136
Mvaa 24,24,184
S_ m0,s50,21504
Ta 224,v1,4096
Ta 226,v1,4608
Mvaa 32,20,148
Mvaa 32,24,188
L_ v5,24,s65,0
Mvaa 40,20,152
S_ s65,s60,21376
Mvaa 40,24,192
S_ m0,s50,22528
Mvaa 48,20,156
Mvaa 48,24,196
W_ 4
Mvaa 52,32,196
s_nop 0
Ta 20,v1,8
Ta 22,v1,520
Mvaa 52,28,156
L_ v4,24,s65,0
Mvaa 44,32,192
S_ s65,s60,31616
Mvaa 44,28,152
S_ m0,s50,23552
Ta 24,v1,4104
Ta 26,v1,4616
Mvaa 36,32,188
Mvaa 36,28,148
L_ v5,24,s65,0
Mvaa 28,32,184
S_ s65,s60,41856
Mvaa 28,28,144
S_ m0,s50,24576
Mvaa 20,32,180
s_waitcnt vmcnt(15) lgkmcnt(0)
s_barrier
s_cmp_eq_u32 s11, 1
s_cbranch_scc1 .Lpace15
.Lpace15_back:
Mvaa 20,28,140
L_ v4,24,s65,0
Mvaa 56,36,140
S_ s65,s59,4718592
Ta 28,%0,8192
Ta 30,%0,8704
Mvaa 56,224,180
S_ m0,s52,-128
Mvaa 64,36,144
Mvaa 64,224,184
s_nop 0
Ta 32,%0,12288
Ta 34,%0,12800
Mvaa 72,36,148
L_ v6,28,s65,128
Mvaa 72,224,188
S_ s65,s59,4816896
Mvaa 80,36,152
S_ m0,s52,896
Mvaa 80,224,192
Mvaa 88,36,156
L_ v6,28,s65,128
Mvaa 88,224,196
S_ s65,s59,4915200
Mvaa 92,24,196
S_ m0,s52,1920
Ta 36,%0,8200
Ta 38,%0,8712
Mvaa 92,20,156
S_ s55,s56,7077888
Mvaa 84,24,192
L_ v6,28,s65,128
Mvaa 84,20,152
S_ s65,s59,5013504
Ta 224,%0,12296
Ta 226,%0,12808
Mvaa 76,24,188
S_ m0,s52,2944
Mvaa 76,20,148
Mvaa 68,24,184
L_ v6,28,s65,128
Mvaa 68,20,144
Mvaa 60,24,180
Mvaa 60,20,140
W_ 4
Mvaa 96,28,140
s_nop 0
Ta 20,v1,8192
Ta 22,v1,8704
Mvaa 96,32,180
buffer_load_dword a254, v10, s[28:31], s55 offen
Mvaa 104,28,144
Mvaa 104,32,184
s_nop 0
Ta 24,v1,12288
Ta 26,v1,12800
Mvaa 112,28,148
Mvaa 112,32,188
buffer_load_dword a254, v12, s[24:27], s63 offen offset:1152
Mvaa 120,28,152
Mvaa 120,32,192
Mvaa 128,28,156
Mvaa 128,32,196
W_ 4
Mvaa 132,224,196
s_nop 0
Ta 28,v1,8200
Ta 30,v1,8712
Mvaa 132,36,156
Mvaa 124,224,192
Mvaa 124,36,152
s_nop 0
Ta 32,v1,12296
Ta 34,v1,12808
Mvaa 116,224,188
Mvaa 116,36,148
Mvaa 108,224,184
Mvaa 108,36,144
Mvaa 100,224,180
s_waitcnt vmcnt(15) lgkmcnt(0)
s_barrier
Mvaa 100,36,140
S_ s65,s59,4718592
Da 60,v2,0
Mvaa 136,20,140
S_ m0,s52,7936
Ta 36,%0,16384
Ta 38,%0,16896
Mvaa 136,24,180
L_ v6,28,s65,256
Mvaa 144,20,144
S_ s65,s59,4816896
Da 100,v3,0
Mvaa 144,24,184
S_ m0,s52,8960
Ta 224,%0,20480
Ta 226,%0,20992
Mvaa 152,20,148
Mvaa 152,24,188
Da 64,v2,2048
Mvaa 160,20,152
L_ v6,28,s65,256
Mvaa 160,24,192
S_ s65,s59,4915200
Mvaa 168,20,156
S_ m0,s52,9984
Da 104,v3,2048
Mvaa 168,24,196
Mvaa 172,32,196
s_nop 0
Ta 20,%0,16392
Ta 22,%0,16904
Mvaa 172,28,156
Da 68,v2,4096
Mvaa 164,32,192
L_ v6,28,s65,256
Mvaa 164,28,152
S_ s65,s59,5013504
Ta 24,%0,20488
Ta 26,%0,21000
Mvaa 156,32,188
S_ m0,s52,11008
Da 108,v3,4096
Mvaa 156,28,148
Mvaa 148,32,184
L_ v6,28,s65,256
Mvaa 148,28,144
Da 72,v2,6144
Mvaa 140,32,180
Mvaa 140,28,140
W_ 9
Mvaa 176,36,140
s_nop 0
Ta 28,v1,16384
Ta 30,v1,16896
Mvaa 176,224,180
Da 112,v3,6144
Mvaa 184,36,144
Mvaa 184,224,184
s_nop 0
Ta 32,v1,20480
Ta 34,v1,20992
Mvaa 192,36,148
Da 76,v2,8192
Mvaa 192,224,188
Mvaa 200,36,152
Da 116,v3,8192
Mvaa 200,224,192
Mvaa 208,36,156
Mvaa 208,224,196
W_ 9
Mvaa 212,24,196
s_nop 0
Ta 36,v1,16392
Ta 38,v1,16904
Mvaa 212,20,156
Mvaa 204,24,192
Mvaa 204,20,152
s_nop 0
Ta 224,v1,20488
Ta 226,v1,21000
Mvaa 196,24,188
Mvaa 196,20,148
Mvaa 188,24,184
Mvaa 188,20,144
Mvaa 180,24,180
s_waitcnt vmcnt(15) lgkmcnt(0)
s_barrier
s_cmp_eq_u32 s11, 1
s_cbranch_scc1 .Lpace17
.Lpace17_back:
Mvaa 180,20,140
S_ s65,s59,5505024
Mvaa 216,28,140
S_ m0,s52,16384
Ta 20,%0,24576
Ta 22,%0,25088
Mvaa 216,32,180
L_ v6,28,s65,0
Mvaa 224,28,144
S_ s65,s59,5603328
Mvaa 224,32,184
S_ m0,s52,17408
Ta 24,%0,28672
Ta 26,%0,29184
Mvaa 232,28,148
.Lpad_q2_4:
s_cbranch_execz .Lhop_q2_r
Mvaa 232,32,188
L_ v6,28,s65,0
.Lpad_qx_2:
s_cbranch_execz .Lhop_qx_r
Mvaa 240,28,152
S_ s65,s59,5701632
Mvaa 240,32,192
S_ m0,s52,18432
Mvaa 248,28,156
Mvaa 248,32,196
Mvaa 252,224,196
s_nop 0
Ta 28,%0,24584
Ta 30,%0,25096
Mvaa 252,36,156
L_ v6,28,s65,0
Mvaa 244,224,192
S_ s65,s59,5799936
Mvaa 244,36,152
S_ m0,s52,19456
Ta 32,%0,28680
Ta 34,%0,29192
Mvaa 236,224,188
Mvaa 236,36,148
L_ v6,28,s65,0
Mvaa 228,224,184
S_ s65,s60,1024
Mvaa 228,36,144
S_ m0,s50,0
Mvaa 220,224,180
Mvaa 220,36,140
W_ 4
Mvaa 16,20,60
s_nop 0
Ta 36,v1,24576
Ta 38,v1,25088
Mvaa 16,24,100
L_ v4,24,s65,0
Mvaa 24,20,64
S_ s65,s60,11264
Mvaa 24,24,104
S_ m0,s50,1024
Ta 224,v1,28672
Ta 226,v1,29184
Mvaa 32,20,68
Mvaa 32,24,108
L_ v5,24,s65,0
Mvaa 40,20,72
S_ s65,s60,21504
Mvaa 40,24,112
S_ m0,s50,2048
Mvaa 48,20,76
Mvaa 48,24,116
W_ 4
Mvaa 52,32,116
s_nop 0
Ta 20,v1,24584
Ta 22,v1,25096
Mvaa 52,28,76
L_ v4,24,s65,0
Mvaa 44,32,112
S_ s65,s60,31744
Mvaa 44,28,72
S_ m0,s50,3072
Ta 24,v1,28680
Ta 26,v1,29192
Mvaa 36,32,108
Mvaa 36,28,68
L_ v5,24,s65,0
Mvaa 28,32,104
S_ s65,s60,41984
Mvaa 28,28,64
S_ m0,s50,4096
Mvaa 20,32,100
s_waitcnt vmcnt(14) lgkmcnt(0)
s_barrier
Mvaa 20,28,60
L_ v4,24,s65,0
Mvaa 56,36,60
S_ s65,s59,5505024
Ta 28,%0,0
Ta 30,%0,512
Mvaa 56,224,100
S_ m0,s52,24448
Mvaa 64,36,64
Mvaa 64,224,104
s_nop 0
Ta 32,%0,4096
Ta 34,%0,4608
Mvaa 72,36,68
L_ v6,28,s65,128
Mvaa 72,224,108
S_ s65,s59,5603328
Mvaa 80,36,72
S_ m0,s52,25472
Mvaa 80,224,112
Mvaa 88,36,76
L_ v6,28,s65,128
Mvaa 88,224,116
S_ s65,s59,5701632
Mvaa 92,24,116
S_ m0,s52,26496
Ta 36,%0,8
Ta 38,%0,520
Mvaa 92,20,76
Mvaa 84,24,112
L_ v6,28,s65,128
Mvaa 84,20,72
S_ s65,s59,5799936
Ta 224,%0,4104
Ta 226,%0,4616
Mvaa 76,24,108
S_ m0,s52,27520
Mvaa 76,20,68
Mvaa 68,24,104
L_ v6,28,s65,128
Mvaa 68,20,64
S_ s65,s61,0
Mvaa 60,24,100
S_ m0,s51,36864
Mvaa 60,20,60
W_ 4
Mvaa 96,28,60
s_nop 0
Ta 20,v1,0
Ta 22,v1,512
Mvaa 96,32,100
Ln v7,32,s65,0
Mvaa 104,28,64
Mvaa 104,32,104
s_nop 0
Ta 24,v1,4096
Ta 26,v1,4608
Mvaa 112,28,68
Mvaa 112,32,108
Ln v8,32,s65,3072
Mvaa 120,28,72
S_ m0,s51,37760
Mvaa 120,32,112
Mvaa 128,28,76
Mvaa 128,32,116
W_ 4
Mvaa 132,224,116
s_nop 0
Ta 28,v1,8
Ta 30,v1,520
Mvaa 132,36,76
Ln v7,32,s65,128
Mvaa 124,224,112
Mvaa 124,36,72
s_nop 0
Ta 32,v1,4104
Ta 34,v1,4616
Mvaa 116,224,108
Mvaa 116,36,68
Ln v8,32,s65,3200
Mvaa 108,224,104
S_ m0,s51,38656
Mvaa 108,36,64
Mvaa 100,224,100
s_waitcnt vmcnt(17) lgkmcnt(0)
s_barrier
s_cmp_eq_u32 s11, 1
s_cbranch_scc1 .Lpace19
.Lpace19_back:
Mvaa 100,36,60
Da 140,v2,20480
Mvaa 136,20,60
s_nop 0
Ta 36,%0,8192
Ta 38,%0,8704
Mvaa 136,24,100
Ln v7,32,s65,256
Mvaa 144,20,64
Da 180,v3,20480
Mvaa 144,24,104
s_nop 0
Ta 224,%0,12288
Ta 226,%0,12800
Mvaa 152,20,68
Mvaa 152,24,108
Da 144,v2,22528
Mvaa 160,20,72
Ln v8,32,s65,3328
Mvaa 160,24,112
S_ s65,s59,5505024
Mvaa 168,20,76
S_ m0,s52,-256
Da 184,v3,22528
Mvaa 168,24,116
Mvaa 172,32,116
s_nop 0
Ta 20,%0,8200
Ta 22,%0,8712
Mvaa 172,28,76
Da 148,v2,24576
Mvaa 164,32,112
L_ v6,28,s65,256
Mvaa 164,28,72
S_ s65,s59,5603328
Ta 24,%0,12296
Ta 26,%0,12808
Mvaa 156,32,108
S_ m0,s52,768
Da 188,v3,24576
Mvaa 156,28,68
Mvaa 148,32,104
L_ v6,28,s65,256
Mvaa 148,28,64
S_ s65,s59,5701632
Da 152,v2,26624
Mvaa 140,32,100
S_ m0,s52,1792
Mvaa 140,28,60
W_ 9
Mvaa 176,36,60
s_nop 0
Ta 28,v1,8192
Ta 30,v1,8704
Mvaa 176,224,100
Da 192,v3,26624
Mvaa 184,36,64
L_ v6,28,s65,256
Mvaa 184,224,104
S_ s65,s59,5799936
Ta 32,v1,12288
Ta 34,v1,12800
Mvaa 192,36,68
S_ m0,s52,2816
Da 156,v2,28672
Mvaa 192,224,108
Mvaa 200,36,72
Da 196,v3,28672
Mvaa 200,224,112
L_ v6,28,s65,256
Mvaa 208,36,76
Mvaa 208,224,116
W_ 9
Mvaa 212,24,116
s_nop 0
Ta 36,v1,8200
Ta 38,v1,8712
Mvaa 212,20,76
Mvaa 204,24,112
Mvaa 204,20,72
s_nop 0
Ta 224,v1,12296
Ta 226,v1,12808
Mvaa 196,24,108
Mvaa 196,20,68
Mvaa 188,24,104
Mvaa 188,20,64
Mvaa 180,24,100
s_waitcnt vmcnt(19) lgkmcnt(0)
s_barrier
Mvaa 180,20,60
S_ s65,s59,6291456
Mvaa 216,28,60
S_ m0,s52,7936
Ta 20,%0,16384
Ta 22,%0,16896
Mvaa 216,32,100
L_ v6,28,s65,256
Mvaa 224,28,64
S_ s65,s59,6389760
Mvaa 224,32,104
S_ m0,s52,8960
Ta 24,%0,20480
Ta 26,%0,20992
Mvaa 232,28,68
Mvaa 232,32,108
L_ v6,28,s65,256
Mvaa 240,28,72
S_ s65,s59,6488064
Mvaa 240,32,112
S_ m0,s52,9984
Mvaa 248,28,76
Mvaa 248,32,116
Mvaa 252,224,116
s_nop 0
Ta 28,%0,16392
Ta 30,%0,16904
Mvaa 252,36,76
L_ v6,28,s65,256
Mvaa 244,224,112
S_ s65,s59,6586368
Mvaa 244,36,72
S_ m0,s52,11008
Ta 32,%0,20488
Ta 34,%0,21000
Mvaa 236,224,108
Mvaa 236,36,68
L_ v6,28,s65,256
Mvaa 228,224,104
S_ s65,s60,1152
Mvaa 228,36,64
S_ m0,s50,20480
Mvaa 220,224,100
Mvaa 220,36,60
W_ 4
Mvaa 16,20,140
s_nop 0
Ta 36,v1,16384
Ta 38,v1,16896
Mvaa 16,24,180
L_ v4,24,s65,0
Mvaa 24,20,144
S_ s65,s60,11392
Mvaa 24,24,184
S_ m0,s50,21504
Ta 224,v1,20480
Ta 226,v1,20992
Mvaa 32,20,148
Mvaa 32,24,188
L_ v5,24,s65,0
Mvaa 40,20,152
S_ s65,s60,21632
Mvaa 40,24,192
S_ m0,s50,22528
Mvaa 48,20,156
Mvaa 48,24,196
W_ 4
Mvaa 52,32,196
s_nop 0
Ta 20,v1,16392
Ta 22,v1,16904
Mvaa 52,28,156
L_ v4,24,s65,0
Mvaa 44,32,192
S_ s65,s60,31872
Mvaa 44,28,152
S_ m0,s50,23552
Ta 24,v1,20488
Ta 26,v1,21000
Mvaa 36,32,188
Mvaa 36,28,148
L_ v5,24,s65,0
Mvaa 28,32,184
S_ s65,s60,42112
Mvaa 28,28,144
S_ m0,s50,24576
Mvaa 20,32,180
s_waitcnt vmcnt(18) lgkmcnt(0)
s_barrier
s_cmp_eq_u32 s11, 1
s_cbranch_scc1 .Lpace21
.Lpace21_back:
Mvaa 20,28,140
L_ v4,24,s65,0
Mvaa 56,36,140
S_ s65,s59,7077888
Ta 28,%0,24576
Ta 30,%0,25088
Mvaa 56,224,180
S_ m0,s52,16128
Mvaa 64,36,144
Mvaa 64,224,184
s_nop 0
Ta 32,%0,28672
Ta 34,%0,29184
Mvaa 72,36,148
L_ v6,28,s65,256
Mvaa 72,224,188
S_ s65,s59,7176192
Mvaa 80,36,152
S_ m0,s52,17152
Mvaa 80,224,192
Mvaa 88,36,156
L_ v6,28,s65,256
Mvaa 88,224,196
S_ s65,s59,7274496
Mvaa 92,24,196
S_ m0,s52,18176
Ta 36,%0,24584
Ta 38,%0,25096
Mvaa 92,20,156
Mvaa 84,24,192
L_ v6,28,s65,256
Mvaa 84,20,152
S_ s65,s59,7372800
Ta 224,%0,28680
Ta 226,%0,29192
Mvaa 76,24,188
S_ m0,s52,19200
Mvaa 76,20,148
Mvaa 68,24,184
L_ v6,28,s65,256
Mvaa 68,20,144
S_ s65,s61,12288
Mvaa 60,24,180
S_ m0,s51,49152
Mvaa 60,20,140
W_ 4
Mvaa 96,28,140
s_nop 0
Ta 20,v1,24576
Ta 22,v1,25088
Mvaa 96,32,180
Ln v7,32,s65,0
Mvaa 104,28,144
Mvaa 104,32,184
s_nop 0
Ta 24,v1,28672
Ta 26,v1,29184
Mvaa 112,28,148
Mvaa 112,32,188
Ln v8,32,s65,3072
Mvaa 120,28,152
S_ m0,s51,50048
Mvaa 120,32,192
Mvaa 128,28,156
Mvaa 128,32,196
W_ 4
Mvaa 132,224,196
s_nop 0
Ta 28,v1,24584
Ta 30,v1,25096
Mvaa 132,36,156
Ln v7,32,s65,128
Mvaa 124,224,192
Mvaa 124,36,152
s_nop 0
Ta 32,v1,28680
Ta 34,v1,29192
Mvaa 116,224,188
Mvaa 116,36,148
Ln v8,32,s65,3200
Mvaa 108,224,184
S_ m0,s51,50944
Mvaa 108,36,144
Mvaa 100,224,180
s_waitcnt vmcnt(17) lgkmcnt(0)
s_barrier
Mvaa 100,36,140
Da 60,v2,0
Mvaa 136,20,140
s_nop 0
Ta 36,%0,0
Ta 38,%0,512
Mvaa 136,24,180
Ln v7,32,s65,256
Mvaa 144,20,144
Da 100,v3,0
Mvaa 144,24,184
s_nop 0
Ta 224,%0,4096
Ta 226,%0,4608
Mvaa 152,20,148
Mvaa 152,24,188
Da 64,v2,2048
Mvaa 160,20,152
Ln v8,32,s65,3328
Mvaa 160,24,192
S_ s65,s59,6291456
Mvaa 168,20,156
S_ m0,s52,24448
Da 104,v3,2048
Mvaa 168,24,196
Mvaa 172,32,196
s_nop 0
Ta 20,%0,8
Ta 22,%0,520
Mvaa 172,28,156
Da 68,v2,4096
Mvaa 164,32,192
L_ v6,28,s65,128
Mvaa 164,28,152
S_ s65,s59,6389760
Ta 24,%0,4104
Ta 26,%0,4616
Mvaa 156,32,188
S_ m0,s52,25472
Da 108,v3,4096
Mvaa 156,28,148
Mvaa 148,32,184
L_ v6,28,s65,128
Mvaa 148,28,144
S_ s65,s59,6488064
Da 72,v2,6144
Mvaa 140,32,180
S_ m0,s52,26496
Mvaa 140,28,140
W_ 9
Mvaa 176,36,140
s_nop 0
Ta 28,v1,0
Ta 30,v1,512
Mvaa 176,224,180
Da 112,v3,6144
Mvaa 184,36,144
L_ v6,28,s65,128
Mvaa 184,224,184
S_ s65,s59,6586368
Ta 32,v1,4096
Ta 34,v1,4608
Mvaa 192,36,148
S_ m0,s52,27520
Da 76,v2,8192
Mvaa 192,224,188
Mvaa 200,36,152
Da 116,v3,8192
Mvaa 200,224,192
L_ v6,28,s65,128
Mvaa 208,36,156
Mvaa 208,224,196
W_ 9
Mvaa 212,24,196
s_nop 0
Ta 36,v1,8
Ta 38,v1,520
Mvaa 212,20,156
Mvaa 204,24,192
Mvaa 204,20,152
s_nop 0
Ta 224,v1,4104
Ta 226,v1,4616
Mvaa 196,24,188
Mvaa 196,20,148
Mvaa 188,24,184
Mvaa 188,20,144
Mvaa 180,24,180
s_waitcnt vmcnt(14) lgkmcnt(0)
s_barrier
s_cmp_eq_u32 s11, 1
s_cbranch_scc1 .Lpace23
.Lpace23_back:
Mvaa 180,20,140
S_ s65,s59,7077888
Mvaa 216,28,140
S_ m0,s52,-128
Ta 20,v1,8200
Ta 22,v1,8712
Mvaa 216,32,180
L_ v6,28,s65,128
Mvaa 224,28,144
S_ s65,s59,7176192
Mvaa 224,32,184
S_ m0,s52,896
Ta 24,v1,12296
Ta 26,v1,12808
Mvaa 232,28,148
Mvaa 232,32,188
L_ v6,28,s65,128
Mvaa 240,28,152
S_ s65,s59,7274496
Mvaa 240,32,192
S_ m0,s52,1920
Mvaa 248,28,156
.Lpad_q2_5:
s_cbranch_execz .Lhop_q2_r
.Lpad_qx_3:
s_cbranch_execz .Lhop_qx_r
Mvaa 248,32,196
Mvaa 252,224,196
s_nop 0
Ta 28,v1,8192
Ta 30,v1,8704
Mvaa 252,36,156
L_ v6,28,s65,128
Mvaa 244,224,192
S_ s65,s59,7372800
Mvaa 244,36,152
S_ m0,s52,2944
Ta 32,v1,12288
Ta 34,v1,12800
Mvaa 236,224,188
Mvaa 236,36,148
L_ v6,28,s65,128
Mvaa 228,224,184
Mvaa 228,36,144
Mvaa 220,224,180
Mvaa 220,36,140
W_ 4
Mvaa 220,20,60
s_nop 0
Ta 36,%0,8200
Ta 38,%0,8712
Mvaa 220,24,100
Da 140,v2,20480
Mvaa 228,20,64
Mvaa 228,24,104
s_nop 0
Ta 224,%0,12296
Ta 226,%0,12808
Mvaa 236,20,68
Da 180,v3,20480
Mvaa 236,24,108
Mvaa 244,20,72
Da 144,v2,22528
Mvaa 244,24,112
Mvaa 252,20,76
Mvaa 252,24,116
W_ 7
Da 184,v3,22528
Mvaa 248,32,116
s_nop 0
Ta 20,%0,8192
Ta 22,%0,8704
Mvaa 248,28,76
Mvaa 240,32,112
Da 148,v2,24576
Mvaa 240,28,72
s_nop 0
Ta 24,%0,12288
Ta 26,%0,12800
Mvaa 232,32,108
Mvaa 232,28,68
Da 188,v3,24576
Mvaa 224,32,104
Mvaa 224,28,64
Mvaa 216,32,100
s_waitcnt vmcnt(14) lgkmcnt(0)
s_barrier
Da 152,v2,26624
Mvaa 216,28,60
S_ s65,s59,6291456
Mvaa 180,36,60
S_ m0,s52,8192
Ta 28,v1,16392
Ta 30,v1,16904
Mvaa 180,224,100
Da 192,v3,26624
Mvaa 188,36,64
L_ v6,28,s65,0
Mvaa 188,224,104
S_ s65,s59,6389760
Ta 32,v1,20488
Ta 34,v1,21000
Mvaa 196,36,68
S_ m0,s52,9216
Da 156,v2,28672
Mvaa 196,224,108
Mvaa 204,36,72
L_ v6,28,s65,0
Mvaa 204,224,112
S_ s65,s59,6488064
Da 196,v3,28672
Mvaa 212,36,76
S_ m0,s52,10240
Mvaa 212,224,116
Mvaa 208,24,116
s_nop 0
Ta 36,v1,16384
Ta 38,v1,16896
Mvaa 208,20,76
L_ v6,28,s65,0
Mvaa 200,24,112
S_ s65,s59,6586368
Mvaa 200,20,72
S_ m0,s52,11264
Ta 224,v1,20480
Ta 226,v1,20992
Mvaa 192,24,108
Mvaa 192,20,68
L_ v6,28,s65,0
Mvaa 184,24,104
Mvaa 184,20,64
Mvaa 176,24,100
Mvaa 176,20,60
W_ 6
Mvaa 220,28,140
s_nop 0
Ta 20,%0,16392
Ta 22,%0,16904
Mvaa 220,32,180
Mvaa 228,28,144
Mvaa 228,32,184
s_nop 0
Ta 24,%0,20488
Ta 26,%0,21000
Mvaa 236,28,148
Mvaa 236,32,188
Mvaa 244,28,152
Mvaa 244,32,192
W_ 9
Mvaa 252,28,156
W_ 8
Mvaa 252,32,196
W_ 4
Mvaa 248,224,196
s_nop 0
Ta 28,%0,16384
Ta 30,%0,16896
Mvaa 248,36,156
Mvaa 240,224,192
Mvaa 240,36,152
s_nop 0
Ta 32,%0,20480
Ta 34,%0,20992
Mvaa 232,224,188
Mvaa 232,36,148
Mvaa 224,224,184
Mvaa 224,36,144
Mvaa 216,224,180
s_cmp_eq_u32 s58, 0
s_cbranch_scc0 .Lhq_0
s_mov_b64 exec, 0
s_bitcmp1_b32 s4, 1
s_cbranch_scc1 .Lhop_a0_b
s_bitcmp1_b32 s4, 0
s_cbranch_scc1 .Lpad_a0_1
s_branch .Lpad_a0_0
.Lhop_a0_b:
s_bitcmp1_b32 s4, 0
s_cbranch_scc1 .Lpad_a0_3
s_branch .Lpad_a0_2
.Lhop_a0_r:
s_mov_b64 exec, -1
s_nop 1
.Lhq_0:
s_cmp_eq_u32 s58, 1
s_cbranch_scc0 .Lhq_1
s_mov_b64 exec, 0
s_bitcmp1_b32 s4, 1
s_cbranch_scc1 .Lhop_a1_b
s_bitcmp1_b32 s4, 0
s_cbranch_scc1 .Lpad_a1_1
s_branch .Lpad_a1_0
.Lhop_a1_b:
s_bitcmp1_b32 s4, 0
s_cbranch_scc1 .Lpad_a1_3
s_branch .Lpad_a1_2
.Lhop_a1_r:
s_mov_b64 exec, -1
s_nop 1
.Lhq_1:
s_cmp_eq_u32 s58, 2
s_cbranch_scc0 .Lhq_2
s_mov_b64 exec, 0
s_bitcmp1_b32 s4, 1
s_cbranch_scc1 .Lhop_a2_b
s_bitcmp1_b32 s4, 0
s_cbranch_scc1 .Lpad_a2_1
s_branch .Lpad_a2_0
.Lhop_a2_b:
s_bitcmp1_b32 s4, 0
s_cbranch_scc1 .Lpad_a2_3
s_branch .Lpad_a2_2
.Lhop_a2_r:
s_mov_b64 exec, -1
s_nop 1
.Lhq_2:
s_waitcnt vmcnt(8) lgkmcnt(0)
s_barrier
s_cmp_eq_u32 s11, 1
s_cbranch_scc1 .Lpace25
.Lpace25_back:
Mvaa 216,36,140
S_ s65,s59,7077888
Mvaa 180,20,140
S_ m0,s52,16384
Ta 36,v1,24584
Ta 38,v1,25096
Mvaa 180,24,180
L_ v6,28,s65,0
Mvaa 188,20,144
S_ s65,s59,7176192
Mvaa 188,24,184
S_ m0,s52,17408
Ta 224,v1,28680
Ta 226,v1,29192
Mvaa 196,20,148
U_ v248,s48,v248
U_ v249,s48,v249
Mvaa 196,24,188
L_ v6,28,s65,0
Mvaa 204,20,152
S_ s65,s59,7274496
U_ v250,s48,v250
U_ v251,s48,v251
Mvaa 204,24,192
S_ m0,s52,18432
U_ v252,s48,v252
U_ v253,s48,v253
Mvaa 212,20,156
U_ v254,s48,v254
U_ v255,s48,v255
Mvaa 212,24,196
C_ 255,254,255
C_ 254,252,253
Mvaa 208,32,196
s_nop 0
Ta 20,v1,24576
Ta 22,v1,25088
Mvaa 208,28,156
L_ v6,28,s65,0
Mvaa 200,32,192
S_ s65,s59,7372800
C_ 253,250,251
C_ 252,248,249
Mvaa 200,28,152
S_ m0,s52,19456
Ta 24,v1,28672
Ta 26,v1,29184
Mvaa 192,32,188
U_ v240,s48,v240
U_ v241,s48,v241
Mvaa 192,28,148
L_ v6,28,s65,0
Mvaa 184,32,184
U_ v242,s48,v242
U_ v243,s48,v243
Mvaa 184,28,144
U_ v244,s48,v244
U_ v245,s48,v245
Mvaa 176,32,180
U_ v246,s48,v246
U_ v247,s48,v247
Mvaa 176,28,140
W_ 4
C_ 251,246,247
C_ 250,244,245
Mvaa 140,36,60
s_nop 0
Ta 28,%0,24584
Ta 30,%0,25096
Mvaa 140,224,100
C_ 249,242,243
)A"
R"A(C_ 248,240,241
Mvaa 148,36,64
U_ v232,s48,v232
U_ v233,s48,v233
Mvaa 148,224,104
s_nop 0
Ta 32,%0,28680
Ta 34,%0,29192
Mvaa 156,36,68
U_ v234,s48,v234
U_ v235,s48,v235
Mvaa 156,224,108
U_ v236,s48,v236
U_ v237,s48,v237
Mvaa 164,36,72
U_ v238,s48,v238
U_ v239,s48,v239
Mvaa 164,224,112
C_ 247,238,239
C_ 246,236,237
Mvaa 172,36,76
C_ 245,234,235
C_ 244,232,233
Mvaa 172,224,116
W_ 4
U_ v224,s48,v224
U_ v225,s48,v225
Mvaa 168,24,116
s_nop 0
Ta 36,%0,24576
Ta 38,%0,25088
Mvaa 168,20,76
U_ v226,s48,v226
U_ v227,s48,v227
Mvaa 160,24,112
U_ v228,s48,v228
U_ v229,s48,v229
Mvaa 160,20,72
s_nop 0
Ta 224,%0,28672
Ta 226,%0,29184
Mvaa 152,24,108
U_ v230,s48,v230
U_ v231,s48,v231
Mvaa 152,20,68
C_ 243,230,231
C_ 242,228,229
Mvaa 144,24,104
C_ 241,226,227
C_ 240,224,225
Mvaa 144,20,64
U_ v216,s48,v216
U_ v217,s48,v217
Mvaa 136,24,100
s_waitcnt vmcnt(8) lgkmcnt(0)
s_barrier
U_ v218,s48,v218
Mvaa 136,20,60
S_ s65,s61,0
U_ v219,s48,v219
Mvaa 100,28,60
S_ m0,s51,24576
Ta 20,v1,8
Ta 22,v1,520
Mvaa 100,32,100
Ln v9,36,s65,0
Mvaa 108,28,64
U_ v220,s48,v220
U_ v221,s48,v221
Mvaa 108,32,104
s_nop 0
Ta 24,v1,4104
Ta 26,v1,4616
Mvaa 116,28,68
U_ v222,s48,v222
U_ v223,s48,v223
Mvaa 116,32,108
Ln v9,36,s65,3072
Mvaa 124,28,72
S_ m0,s51,25472
C_ 239,222,223
C_ 238,220,221
Mvaa 124,32,112
C_ 237,218,219
C_ 236,216,217
Mvaa 132,28,76
U_ v208,s48,v208
U_ v209,s48,v209
Mvaa 132,32,116
U_ v210,s48,v210
U_ v211,s48,v211
Mvaa 128,224,116
s_nop 0
Ta 28,v1,0
Ta 30,v1,512
Mvaa 128,36,76
Ln v9,36,s65,128
Mvaa 120,224,112
U_ v212,s48,v212
U_ v213,s48,v213
Mvaa 120,36,72
s_nop 0
Ta 32,v1,4096
Ta 34,v1,4608
Mvaa 112,224,108
U_ v214,s48,v214
U_ v215,s48,v215
Mvaa 112,36,68
Ln v9,36,s65,3200
Mvaa 104,224,104
S_ m0,s51,26368
C_ 235,214,215
C_ 234,212,213
Mvaa 104,36,64
C_ 233,210,211
C_ 232,208,209
Mvaa 96,224,100
U_ v200,s48,v200
U_ v201,s48,v201
Mvaa 96,36,60
W_ 4
U_ v202,s48,v202
U_ v203,s48,v203
Mvaa 140,20,140
s_nop 0
Ta 36,%0,8
Ta 38,%0,520
Mvaa 140,24,180
Ln v9,36,s65,256
Mvaa 148,20,144
U_ v204,s48,v204
U_ v205,s48,v205
Mvaa 148,24,184
s_nop 0
Ta 224,%0,4104
Ta 226,%0,4616
Mvaa 156,20,148
U_ v206,s48,v206
U_ v207,s48,v207
Mvaa 156,24,188
Ln v9,36,s65,3328
Mvaa 164,20,152
C_ 231,206,207
C_ 230,204,205
Mvaa 164,24,192
C_ 229,202,203
C_ 228,200,201
Mvaa 172,20,156
U_ v192,s48,v192
U_ v193,s48,v193
Mvaa 172,24,196
W_ 4
U_ v194,s48,v194
U_ v195,s48,v195
Mvaa 168,32,196
s_nop 0
Ta 20,%0,0
Ta 22,%0,512
Mvaa 168,28,156
U_ v196,s48,v196
U_ v197,s48,v197
Mvaa 160,32,192
U_ v198,s48,v198
U_ v199,s48,v199
Mvaa 160,28,152
s_nop 0
Ta 24,%0,4096
Ta 26,%0,4608
Mvaa 152,32,188
C_ 227,198,199
C_ 226,196,197
Mvaa 152,28,148
C_ 225,194,195
C_ 224,192,193
Mvaa 144,32,184
U_ v184,s48,v184
U_ v185,s48,v185
Mvaa 144,28,144
U_ v186,s48,v186
U_ v187,s48,v187
Mvaa 136,32,180
s_waitcnt vmcnt(10) lgkmcnt(0)
s_barrier
s_cmp_eq_u32 s11, 1
s_cbranch_scc1 .Lpace27
.Lpace27_back:
U_ v188,s48,v188
Mvaa 136,28,140
U_ v189,s48,v189
Mvaa 100,36,140
s_nop 0
Ta 28,v1,8200
Ta 30,v1,8712
Mvaa 100,224,180
U_ v190,s48,v190
U_ v191,s48,v191
Mvaa 108,36,144
C_ 223,190,191
C_ 222,188,189
Mvaa 108,224,184
s_nop 0
Ta 32,v1,12296
Ta 34,v1,12808
Mvaa 116,36,148
C_ 221,186,187
C_ 220,184,185
Mvaa 116,224,188
U_ v176,s48,v176
U_ v177,s48,v177
Mvaa 124,36,152
U_ v178,s48,v178
U_ v179,s48,v179
Mvaa 124,224,192
U_ v180,s48,v180
U_ v181,s48,v181
Mvaa 132,36,156
U_ v182,s48,v182
U_ v183,s48,v183
Mvaa 132,224,196
C_ 219,182,183
C_ 218,180,181
Mvaa 128,24,196
s_nop 0
Ta 36,v1,8192
Ta 38,v1,8704
Mvaa 128,20,156
C_ 217,178,179
C_ 216,176,177
Mvaa 120,24,192
U_ v168,s48,v168
U_ v169,s48,v169
Mvaa 120,20,152
s_nop 0
Ta 224,v1,12288
Ta 226,v1,12800
Mvaa 112,24,188
U_ v170,s48,v170
U_ v171,s48,v171
Mvaa 112,20,148
U_ v172,s48,v172
U_ v173,s48,v173
Mvaa 104,24,184
U_ v174,s48,v174
U_ v175,s48,v175
Mvaa 104,20,144
C_ 215,174,175
C_ 214,172,173
Mvaa 96,24,180
C_ 213,170,171
C_ 212,168,169
Mvaa 96,20,140
W_ 4
U_ v160,s48,v160
U_ v161,s48,v161
Mvaa 60,28,60
s_nop 0
Ta 20,%0,8200
Ta 22,%0,8712
Mvaa 60,32,100
U_ v162,s48,v162
U_ v163,s48,v163
Mvaa 68,28,64
U_ v164,s48,v164
U_ v165,s48,v165
Mvaa 68,32,104
s_nop 0
Ta 24,%0,12296
Ta 26,%0,12808
Mvaa 76,28,68
U_ v166,s48,v166
U_ v167,s48,v167
Mvaa 76,32,108
C_ 211,166,167
C_ 210,164,165
Mvaa 84,28,72
C_ 209,162,163
C_ 208,160,161
Mvaa 84,32,112
U_ v152,s48,v152
U_ v153,s48,v153
Mvaa 92,28,76
U_ v154,s48,v154
U_ v155,s48,v155
Mvaa 92,32,116
W_ 4
U_ v156,s48,v156
U_ v157,s48,v157
Mvaa 88,224,116
s_nop 0
Ta 28,%0,8192
Ta 30,%0,8704
Mvaa 88,36,76
U_ v158,s48,v158
U_ v159,s48,v159
Mvaa 80,224,112
C_ 207,158,159
C_ 206,156,157
Mvaa 80,36,72
s_nop 0
Ta 32,%0,12288
Ta 34,%0,12800
Mvaa 72,224,108
C_ 205,154,155
C_ 204,152,153
Mvaa 72,36,68
U_ v144,s48,v144
U_ v145,s48,v145
Mvaa 64,224,104
U_ v146,s48,v146
U_ v147,s48,v147
Mvaa 64,36,64
U_ v148,s48,v148
U_ v149,s48,v149
Mvaa 56,224,100
s_waitcnt vmcnt(6) lgkmcnt(0)
s_barrier
U_ v150,s48,v150
Mvaa 56,36,60
S_ s65,s61,24576
U_ v151,s48,v151
Mvaa 20,20,60
S_ m0,s51,0
Ta 36,v1,16392
Ta 38,v1,16904
Mvaa 20,24,100
Ln v7,32,s65,0
Mvaa 28,20,64
C_ 203,150,151
C_ 202,148,149
Mvaa 28,24,104
s_nop 0
Ta 224,v1,20488
Ta 226,v1,21000
Mvaa 36,20,68
C_ 201,146,147
C_ 200,144,145
Mvaa 36,24,108
Ln v8,32,s65,3072
Mvaa 44,20,72
S_ m0,s51,896
U_ v136,s48,v136
U_ v137,s48,v137
Mvaa 44,24,112
U_ v138,s48,v138
U_ v139,s48,v139
Mvaa 52,20,76
U_ v140,s48,v140
U_ v141,s48,v141
Mvaa 52,24,116
U_ v142,s48,v142
U_ v143,s48,v143
Mvaa 48,32,116
s_nop 0
Ta 20,v1,16384
Ta 22,v1,16896
Mvaa 48,28,76
Ln v7,32,s65,128
Mvaa 40,32,112
C_ 199,142,143
C_ 198,140,141
Mvaa 40,28,72
.Lpad_q2_6:
s_cbranch_execz .Lhop_q2_r
Ta 24,v1,20480
Ta 26,v1,20992
Mvaa 32,32,108
.Lpad_qx_4:
s_cbranch_execz .Lhop_qx_r
C_ 197,138,139
C_ 196,136,137
Mvaa 32,28,68
Ln v8,32,s65,3200
Mvaa 24,32,104
S_ m0,s51,1792
U_ v128,s48,v128
U_ v129,s48,v129
Mvaa 24,28,64
U_ v130,s48,v130
U_ v131,s48,v131
Mvaa 16,32,100
U_ v132,s48,v132
U_ v133,s48,v133
Mvaa 16,28,60
W_ 4
U_ v134,s48,v134
U_ v135,s48,v135
Mvaa 60,36,140
s_nop 0
Ta 28,%0,16392
Ta 30,%0,16904
Mvaa 60,224,180
Ln v7,32,s65,256
Mvaa 68,36,144
C_ 195,134,135
C_ 194,132,133
Mvaa 68,224,184
s_nop 0
Ta 32,%0,20488
Ta 34,%0,21000
Mvaa 76,36,148
C_ 193,130,131
C_ 192,128,129
Mvaa 76,224,188
Ln v8,32,s65,3328
Mvaa 84,36,152
U_ v120,s48,v120
U_ v121,s48,v121
Mvaa 84,224,192
U_ v122,s48,v122
U_ v123,s48,v123
Mvaa 92,36,156
U_ v124,s48,v124
U_ v125,s48,v125
Mvaa 92,224,196
W_ 4
U_ v126,s48,v126
U_ v127,s48,v127
Mvaa 88,24,196
s_nop 0
Ta 36,%0,16384
Ta 38,%0,16896
Mvaa 88,20,156
C_ 191,126,127
C_ 190,124,125
Mvaa 80,24,192
C_ 189,122,123
C_ 188,120,121
Mvaa 80,20,152
s_nop 0
Ta 224,%0,20480
Ta 226,%0,20992
Mvaa 72,24,188
U_ v112,s48,v112
U_ v113,s48,v113
Mvaa 72,20,148
U_ v114,s48,v114
U_ v115,s48,v115
Mvaa 64,24,184
U_ v116,s48,v116
U_ v117,s48,v117
Mvaa 64,20,144
U_ v118,s48,v118
U_ v119,s48,v119
Mvaa 56,24,180
s_waitcnt vmcnt(51) lgkmcnt(0)
s_barrier
s_cmp_eq_u32 s11, 1
s_cbranch_scc1 .Lpace29
.Lpace29_back:
C_ 187,118,119
Mvaa 56,20,140
S_ s65,s61,12288
C_ 186,116,117
Mvaa 20,28,140
S_ m0,s51,12288
C_ 185,114,115
C_ 184,112,113
Mvaa 20,32,180
Ln v9,36,s65,0
Mvaa 28,28,144
U_ v104,s48,v104
U_ v105,s48,v105
Mvaa 28,32,184
U_ v106,s48,v106
U_ v107,s48,v107
Mvaa 36,28,148
U_ v108,s48,v108
U_ v109,s48,v109
Mvaa 36,32,188
Ln v9,36,s65,3072
Mvaa 44,28,152
S_ m0,s51,13184
U_ v110,s48,v110
U_ v111,s48,v111
Mvaa 44,32,192
C_ 183,110,111
C_ 182,108,109
Mvaa 52,28,156
C_ 181,106,107
C_ 180,104,105
Mvaa 52,32,196
U_ v96,s48,v96
U_ v97,s48,v97
Mvaa 48,224,196
Ln v9,36,s65,128
Mvaa 48,36,156
U_ v98,s48,v98
U_ v99,s48,v99
Mvaa 40,224,192
U_ v100,s48,v100
U_ v101,s48,v101
Mvaa 40,36,152
U_ v102,s48,v102
U_ v103,s48,v103
Mvaa 32,224,188
Ln v9,36,s65,3200
Mvaa 32,36,148
S_ m0,s51,14080
C_ 179,102,103
C_ 178,100,101
Mvaa 24,224,184
C_ 177,98,99
C_ 176,96,97
Mvaa 24,36,144
v_pk_mul_f32 v[88:89], v[88:89], s[48:49]
v_pk_mul_f32 v[90:91], v[90:91], s[48:49]
Mvaa 16,224,180
Ln v9,36,s65,256
Mvaa 16,36,140
v_pk_mul_f32 v[92:93], v[92:93], s[48:49]
v_pk_mul_f32 v[94:95], v[94:95], s[48:49]
Ln v9,36,s65,3328
C_ 175,94,95
C_ 174,92,93
C_ 173,90,91
C_ 172,88,89
v_pk_mul_f32 v[80:81], v[80:81], s[48:49]
v_pk_mul_f32 v[82:83], v[82:83], s[48:49]
v_pk_mul_f32 v[84:85], v[84:85], s[48:49]
v_pk_mul_f32 v[86:87], v[86:87], s[48:49]
C_ 171,86,87
C_ 170,84,85
C_ 169,82,83
C_ 168,80,81
v_pk_mul_f32 v[72:73], v[72:73], s[48:49]
v_pk_mul_f32 v[74:75], v[74:75], s[48:49]
v_pk_mul_f32 v[76:77], v[76:77], s[48:49]
v_pk_mul_f32 v[78:79], v[78:79], s[48:49]
C_ 167,78,79
C_ 166,76,77
C_ 165,74,75
C_ 164,72,73
v_pk_mul_f32 v[40:41], v[40:41], s[48:49]
v_pk_mul_f32 v[42:43], v[42:43], s[48:49]
v_pk_mul_f32 v[44:45], v[44:45], s[48:49]
v_pk_mul_f32 v[46:47], v[46:47], s[48:49]
C_ 151,46,47
C_ 150,44,45
C_ 149,42,43
C_ 148,40,41
v_pk_mul_f32 v[32:33], v[32:33], s[48:49]
v_pk_mul_f32 v[34:35], v[34:35], s[48:49]
v_pk_mul_f32 v[36:37], v[36:37], s[48:49]
v_pk_mul_f32 v[38:39], v[38:39], s[48:49]
C_ 147,38,39
C_ 146,36,37
C_ 145,34,35
C_ 144,32,33
v_pk_mul_f32 v[24:25], v[24:25], s[48:49]
v_pk_mul_f32 v[26:27], v[26:27], s[48:49]
v_pk_mul_f32 v[28:29], v[28:29], s[48:49]
v_pk_mul_f32 v[30:31], v[30:31], s[48:49]
C_ 143,30,31
C_ 142,28,29
C_ 141,26,27
C_ 140,24,25
R_ %0,a244
v_xor_b32 v1, 64, %0
Dv 30,%0,43008
Dv 34,v1,43008
Dv 38,%0,44032
v_pk_mul_f32 v[64:65], v[64:65], s[48:49]
v_pk_mul_f32 v[66:67], v[66:67], s[48:49]
v_pk_mul_f32 v[68:69], v[68:69], s[48:49]
v_pk_mul_f32 v[70:71], v[70:71], s[48:49]
C_ 163,70,71
C_ 162,68,69
C_ 161,66,67
C_ 160,64,65
v_pk_mul_f32 v[56:57], v[56:57], s[48:49]
v_pk_mul_f32 v[58:59], v[58:59], s[48:49]
v_pk_mul_f32 v[60:61], v[60:61], s[48:49]
v_pk_mul_f32 v[62:63], v[62:63], s[48:49]
C_ 159,62,63
C_ 158,60,61
C_ 157,58,59
C_ 156,56,57
v_pk_mul_f32 v[48:49], v[48:49], s[48:49]
v_pk_mul_f32 v[50:51], v[50:51], s[48:49]
v_pk_mul_f32 v[52:53], v[52:53], s[48:49]
v_pk_mul_f32 v[54:55], v[54:55], s[48:49]
C_ 155,54,55
C_ 154,52,53
C_ 153,50,51
C_ 152,48,49
v_pk_mul_f32 v[16:17], v[16:17], s[48:49]
v_pk_mul_f32 v[18:19], v[18:19], s[48:49]
v_pk_mul_f32 v[20:21], v[20:21], s[48:49]
v_pk_mul_f32 v[22:23], v[22:23], s[48:49]
C_ 139,22,23
C_ 138,20,21
C_ 137,18,19
C_ 136,16,17
s_cmp_eq_u32 s58, 1
s_cbranch_scc1 .Latt1
s_cmp_eq_u32 s58, 2
s_cbranch_scc1 .Latt2
.Latt0:
s_cmp_eq_u32 s4, 0
s_cbranch_scc0 .Lhs_tok0
R_ v11,a241
buffer_load_dword a255, v11, s[92:95], 0 offen offset:0 sc1
.Lhs_tok0:
S_ s59,s59,768
W_ 2
Zvvv 96,30,136
Dv 42,v1,44032
R_ v2,a245
Zvvv 100,30,140
v_xor_b32 v3, 32, v2
v_xor_b32 v4, 64, v2
Zvvv 104,30,144
v_xor_b32 v5, 96, v2
Zvvv 108,30,148
s_cmp_eq_u32 s4, 0
s_cselect_b32 s67, 13, 0
Zvvv 112,30,152
W_ 2
s_or_b32 s67, s67, 8192
Mvvv 96,34,156
Dv 30,%0,45056
Mvvv 100,34,160
Mvvv 104,34,164
Mvvv 108,34,168
Mvvv 112,34,172
W_ 2
Mvvv 96,38,176
Dv 34,v1,45056
Mvvv 100,38,180
Mvvv 104,38,184
Mvvv 108,38,188
Mvvv 112,38,192
W_ 2
Mvvv 96,42,196
Dv 38,%0,36864
Mvvv 100,42,200
Mvvv 104,42,204
Mvvv 108,42,208
Mvvv 112,42,212
W_ 2
Mvvv 96,30,216
Dv 42,v1,36864
Mvvv 100,30,220
Mvvv 104,30,224
Mvvv 108,30,228
Mvvv 112,30,232
W_ 2
Mvvv 96,34,236
Dv 30,%0,37888
Mvvv 100,34,240
Mvvv 104,34,244
Mvvv 108,34,248
Mvvv 112,34,252
W_ 2
s_nop 1
v_max3_f32 v56, v96, v97, v98
v_max_f32 v56, v56, v99
Zvvv 76,38,136
s_memtime s[12:13]
Dv 34,v1,37888
v_max3_f32 v60, v100, v101, v102
Zvvv 80,38,140
s_memrealtime s[100:101]
v_max_f32 v60, v60, v103
Zvvv 84,38,144
v_max3_f32 v64, v104, v105, v106
v_max_f32 v64, v64, v107
Zvvv 88,38,148
v_max3_f32 v68, v108, v109, v110
v_max_f32 v68, v68, v111
Zvvv 92,38,152
W_ 2
v_max3_f32 v72, v112, v113, v114
v_max_f32 v72, v72, v115
Mvvv 76,42,156
Dv 38,%0,38912
Mvvv 80,42,160
Mvvv 84,42,164
Mvvv 88,42,168
Mvvv 92,42,172
W_ 2
Mvvv 76,30,176
Dv 42,v1,38912
Mvvv 80,30,180
Mvvv 84,30,184
Mvvv 88,30,188
Mvvv 92,30,192
s_waitcnt vmcnt(34) lgkmcnt(0)
s_barrier
Mvvv 76,34,196
S_ s65,s61,184320
Dv 30,%0,55296
Mvvv 76,38,216
S_ m0,s51,36864
s_sub_u32 s12, s12, s90
Mvvv 76,42,236
L_ v9,36,s65,0
Mvvv 80,34,200
s_sub_u32 s100, s100, s68
Mvvv 80,38,220
L_ v9,36,s65,3072
Mvvv 80,42,240
S_ m0,s51,37760
s_sub_u32 s101, s68, s98
Mvvv 84,34,204
L_ v9,36,s65,128
Mvvv 84,38,224
s_mul_i32 s12, s12, s101
v_max3_f32 v56, v56, v76, v77
v_max3_f32 v56, v56, v78, v79
Mvvv 84,42,244
L_ v9,36,s65,3200
Mvvv 88,34,208
S_ m0,s51,38656
v_max3_f32 v60, v60, v80, v81
s_sub_u32 s101, s90, s88
Mvvv 88,38,228
L_ v9,36,s65,256
Mvvv 88,42,248
s_mul_i32 s100, s100, s101
v_max3_f32 v60, v60, v82, v83
Mvvv 92,34,212
L_ v9,36,s65,3328
Mvvv 92,38,232
s_lshr_b32 s100, s100, 6
v_max3_f32 v64, v64, v84, v85
v_max3_f32 v64, v64, v86, v87
Mvvv 92,42,252
s_mul_i32 s100, s100, 47
s_cmp_gt_u32 s101, 4000
s_cselect_b32 s13, 2, 0
s_cmp_gt_u32 s12, s100
s_cselect_b32 s13, s13, 0
Dv 34,v1,55296
.p2align 3
v_mov_b32 v11, 0x3f803f80
v_permlane32_swap_b32 v56, v60
v_max3_f32 v68, v68, v88, v89
v_max3_f32 v68, v68, v90, v91
v_max_f32 v56, v56, v60
v_max3_f32 v72, v72, v92, v93
v_max3_f32 v72, v72, v94, v95
v_mov_b32 v73, v72
v_permlane32_swap_b32 v64, v68
v_max_f32 v64, v64, v68
v_permlane32_swap_b32 v72, v73
v_max_f32 v72, v72, v73
v_mov_b32 v73, v72
v_permlane16_swap_b32 v56, v64
v_max_f32 v56, v56, v64
v_permlane16_swap_b32 v72, v73
B_ v56,0xc0e00000,v56
v_max_f32 v72, v72, v73
v_mov_b32 v64, v56
B_ v72,0xc0e00000,v72
v_mov_b32 v73, v72
v_permlane16_swap_b32 v56, v64
v_mov_b32 v60, v56
v_mov_b32 v68, v64
v_mov_b64 v[74:75], v[72:73]
v_permlane32_swap_b32 v56, v60
v_permlane32_swap_b32 v64, v68
v_mov_b32 v57, v56
v_mov_b32 v61, v60
v_mov_b32 v65, v64
v_mov_b32 v69, v68
v_pk_add_f32 v[96:97], v[96:97], v[56:57]
v_pk_add_f32 v[98:99], v[98:99], v[56:57]
v_pk_add_f32 v[76:77], v[76:77], v[56:57]
v_pk_add_f32 v[78:79], v[78:79], v[56:57]
v_mov_b64 v[58:59], v[56:57]
v_pk_add_f32 v[100:101], v[100:101], v[60:61]
v_pk_add_f32 v[102:103], v[102:103], v[60:61]
v_pk_add_f32 v[80:81], v[80:81], v[60:61]
v_pk_add_f32 v[82:83], v[82:83], v[60:61]
v_mov_b64 v[62:63], v[60:61]
v_pk_add_f32 v[104:105], v[104:105], v[64:65]
v_pk_add_f32 v[106:107], v[106:107], v[64:65]
v_pk_add_f32 v[84:85], v[84:85], v[64:65]
v_pk_add_f32 v[86:87], v[86:87], v[64:65]
v_mov_b64 v[66:67], v[64:65]
v_pk_add_f32 v[108:109], v[108:109], v[68:69]
v_pk_add_f32 v[110:111], v[110:111], v[68:69]
v_pk_add_f32 v[88:89], v[88:89], v[68:69]
v_pk_add_f32 v[90:91], v[90:91], v[68:69]
v_mov_b64 v[70:71], v[68:69]
v_pk_add_f32 v[112:113], v[112:113], v[72:73]
v_pk_add_f32 v[114:115], v[114:115], v[72:73]
v_pk_add_f32 v[92:93], v[92:93], v[72:73]
v_pk_add_f32 v[94:95], v[94:95], v[72:73]
E_ 96
E_ 97
E_ 98
E_ 99
E_ 76
E_ 77
E_ 78
E_ 79
C_ 99,98,99
C_ 98,96,97
C_ 96,76,77
C_ 97,78,79
E_ 100
E_ 101
E_ 102
E_ 103
E_ 80
E_ 81
E_ 82
E_ 83
C_ 103,102,103
C_ 102,100,101
C_ 100,80,81
C_ 101,82,83
.Lstep0_1:
W_ 1
Nvvv 116,30,136,56
s_nop 0
Dv 38,%0,56320
Dv 42,v1,56320
W_ 2
Mvvv 116,34,156
E_ 104
Nvvv 120,30,140,60
E_ 105
Mvvv 120,34,160
E_ 106
Nvvv 124,30,144,64
E_ 107
Mvvv 124,34,164
E_ 84
Nvvv 128,30,148,68
E_ 85
Mvvv 128,34,168
E_ 86
Nvvv 132,30,152,72
E_ 87
Mvvv 132,34,172
W_ 0
C_ 107,106,107
C_ 106,104,105
Mvvv 132,38,192
s_nop 0
Dv 30,%0,57344
Dv 34,v1,57344
Mvvv 132,42,212
C_ 104,84,85
C_ 105,86,87
Mvvv 128,38,188
E_ 108
Mvvv 128,42,208
E_ 109
Mvvv 124,38,184
E_ 110
Mvvv 124,42,204
E_ 111
Mvvv 120,38,180
E_ 88
Mvvv 120,42,200
E_ 89
Mvvv 116,38,176
E_ 90
Mvvv 116,42,196
W_ 0
E_ 91
Mvvv 116,30,216
s_nop 0
Dv 38,%0,49152
Dv 42,v1,49152
Mvvv 116,34,236
C_ 111,110,111
C_ 110,108,109
Mvvv 120,30,220
C_ 108,88,89
C_ 109,90,91
Mvvv 120,34,240
E_ 112
Mvvv 124,30,224
E_ 113
Mvvv 124,34,244
E_ 114
Mvvv 128,30,228
E_ 115
Mvvv 128,34,248
E_ 92
Mvvv 132,30,232
E_ 93
Mvvv 132,34,252
W_ 0
E_ 94
Nvvv 76,38,136,56
s_nop 0
Dv 30,%0,50176
Dv 34,v1,50176
Mvvv 76,42,156
E_ 95
Nvvv 80,38,140,60
C_ 115,114,115
C_ 114,112,113
Mvvv 80,42,160
C_ 112,92,93
C_ 113,94,95
Nvvv 84,38,144,64
E_ 116
Mvvv 84,42,164
E_ 117
Nvvv 88,38,148,68
E_ 118
Mvvv 88,42,168
E_ 119
Nvvv 92,38,152,72
C_ 119,118,119
C_ 118,116,117
Mvvv 92,42,172
W_ 0
E_ 120
Mvvv 92,30,192
s_nop 0
Dv 38,%0,51200
Dv 42,v1,51200
Mvvv 92,34,212
X_ a180,v11
X_ a181,v11
Mvvv 88,30,188
X_ a182,v11
X_ a183,v11
Mvvv 88,34,208
E_ 121
Mvvv 84,30,184
E_ 122
Mvvv 84,34,204
E_ 123
Mvvv 80,30,180
C_ 123,122,123
C_ 122,120,121
Mvvv 80,34,200
E_ 124
Mvvv 76,30,176
E_ 125
Mvvv 76,34,196
S_ s65,s61,36864
E_ 126
W_ 1
Mvvv 76,38,216
S_ m0,s51,49152
E_ 127
W_ 0
Mvvv 76,42,236
s_waitcnt vmcnt(18) lgkmcnt(0)
s_barrier
C_ 127,126,127
Mvvv 80,38,220
Ln v7,32,s65,0
Mvvv 80,42,240
C_ 126,124,125
Mvvv 84,38,224
Ln v8,32,s65,3072
S_ m0,s51,50048
Mvvv 84,42,244
s_nop 0
Tv 30,v2,24576
Tv 32,v2,30720
Mvvv 88,38,228
Ln v7,32,s65,128
Mvvv 88,42,248
s_nop 0
Tv 34,v3,24576
Tv 36,v3,30720
Mvvv 92,38,232
Ln v8,32,s65,3200
S_ m0,s51,50944
Mvvv 92,42,252
.Lpad_a0_0:
s_cbranch_execz .Lhop_a0_r
E_ 128
Zaav 220,180,96
Ln v7,32,s65,256
Zaav 224,180,100
E_ 129
Zaav 228,180,104
Ln v8,32,s65,3328
Zaav 232,180,108
E_ 130
Zaav 236,180,112
W_ 0
E_ 131
Zavv 0,30,96
s_nop 0
Tv 38,v4,24576
Tv 40,v4,30720
Zavv 20,34,96
C_ 131,130,131
C_ 130,128,129
Zavv 24,34,100
s_nop 0
Tv 42,v5,24576
Tv 44,v5,30720
Zavv 4,30,100
E_ 132
Zavv 8,30,104
E_ 133
Zavv 28,34,104
R_ v46,a220
R_ v47,a224
Zavv 32,34,108
R_ v48,a228
R_ v49,a232
Zavv 12,30,108
R_ v50,a236
Zavv 16,30,112
E_ 134
Zavv 36,34,112
W_ 0
E_ 135
Zavv 56,38,112
s_nop 0
Tv 30,v2,25600
Tv 32,v2,31744
Zavv 76,42,112
C_ 135,134,135
C_ 134,132,133
Zavv 72,42,108
s_nop 0
Tv 34,v3,25600
Tv 36,v3,31744
Zavv 52,38,108
E_ 76
Zavv 48,38,104
E_ 77
Zavv 68,42,104
E_ 78
Zavv 64,42,100
E_ 79
Zavv 44,38,100
C_ 116,76,77
C_ 117,78,79
Zavv 40,38,96
E_ 80
Zavv 60,42,96
W_ 0
E_ 81
Zavv 80,30,96
s_nop 0
Tv 38,v4,25600
Tv 40,v4,31744
Zavv 100,34,96
E_ 82
Zavv 104,34,100
s_nop 0
Tv 42,v5,25600
Tv 44,v5,31744
Zavv 84,30,100
E_ 83
Zavv 88,30,104
C_ 120,80,81
C_ 121,82,83
Zavv 108,34,104
E_ 84
Zavv 112,34,108
E_ 85
Zavv 92,30,108
E_ 86
Zavv 96,30,112
E_ 87
Zavv 116,34,112
W_ 0
C_ 124,84,85
C_ 125,86,87
Zavv 136,38,112
s_nop 0
Tv 30,v2,26624
Tv 32,v2,32768
Zavv 156,42,112
E_ 88
Zavv 152,42,108
s_nop 0
Tv 34,v3,26624
Tv 36,v3,32768
Zavv 132,38,108
E_ 89
Zavv 128,38,104
E_ 90
Zavv 148,42,104
E_ 91
Zavv 144,42,100
C_ 128,88,89
C_ 129,90,91
Zavv 124,38,100
E_ 92
Zavv 120,38,96
E_ 93
Zavv 140,42,96
W_ 0
E_ 94
Zavv 160,30,96
s_nop 0
Tv 38,v4,26624
Tv 40,v4,32768
Zavv 180,34,96
E_ 95
Zavv 184,34,100
s_nop 0
Tv 42,v5,26624
Tv 44,v5,32768
Zavv 164,30,100
C_ 132,92,93
C_ 133,94,95
Zavv 168,30,104
v_mov_b32 v92, 0x3f803f80
v_mov_b32 v93, 0x3f803f80
Zavv 188,34,104
S_ s65,s61,24576
v_mov_b32 v94, 0x3f803f80
v_mov_b32 v95, 0x3f803f80
Zavv 192,34,108
S_ m0,s51,24576
Zavv 172,30,108
s_waitcnt vmcnt(18) lgkmcnt(0)
s_barrier
Zavv 176,30,112
L_ v9,36,s65,0
Zavv 196,34,112
Zavv 216,38,112
Dv 30,%0,6144
Zavv 236,42,112
Dv 34,v1,6144
Zavv 232,42,108
L_ v9,36,s65,3072
S_ m0,s51,25472
Zavv 212,38,108
Zavv 208,38,104
L_ v9,36,s65,128
Zavv 228,42,104
Zavv 224,42,100
L_ v9,36,s65,3200
S_ m0,s51,26368
Zavv 204,38,100
Zavv 200,38,96
L_ v9,36,s65,256
Zavv 220,42,96
.Lstep0_2:
Zvvv 76,92,116
Dv 38,%0,7168
Zvvv 80,92,120
Dv 42,v1,7168
Zvvv 84,92,124
Zvvv 88,92,128
Zvvv 92,92,132
W_ 2
Nvvv 96,30,136,56
Mvvv 96,34,156
Nvvv 100,30,140,60
s_nop 1
v_max3_f32 v10, v76, v80, v84
s_nop 0
v_max3_f32 v10, v10, v88, v92
Mvvv 100,34,160
v_cmp_lt_f32 vcc, 0x42000000, v10
s_cbranch_vccnz .Ltramp0_1
Nvvv 104,30,144,64
A_ v46,v46,v76
A_ v47,v47,v80
Mvvv 104,34,164
A_ v48,v48,v84
A_ v49,v49,v88
Nvvv 108,30,148,68
A_ v50,v50,v92
Mvvv 108,34,168
L_ v9,36,s65,3328
Nvvv 112,30,152,72
R_ v11,a248
Mvvv 112,34,172
W_ 0
v_min_u32 v11, 240, v11
Mvvv 112,38,192
Dv 30,%0,8192
v_lshlrev_b32 v11, 3, v11
Mvvv 112,42,212
Dv 34,v1,8192
Mvvv 108,38,188
buffer_load_dword a246, v11, s[44:47], s66 offen
Mvvv 108,42,208
Mvvv 104,38,184
Mvvv 104,42,204
Mvvv 100,38,180
Mvvv 100,42,200
Mvvv 96,38,176
Mvvv 96,42,196
W_ 0
Mvvv 96,30,216
Dv 38,%0,0
Mvvv 96,34,236
Dv 42,v1,0
Mvvv 100,30,220
Mvvv 100,34,240
Mvvv 104,30,224
Mvvv 104,34,244
Mvvv 108,30,228
Mvvv 108,34,248
s_nop 0
E_ 96
Mvvv 112,30,232
E_ 97
Mvvv 112,34,252
W_ 0
E_ 98
Nvvv 76,38,136,56
Dv 30,%0,1024
Mvvv 76,42,156
Dv 34,v1,1024
Nvvv 80,38,140,60
E_ 99
Mvvv 80,42,160
C_ 99,98,99
C_ 98,96,97
Nvvv 84,38,144,64
E_ 100
Mvvv 84,42,164
E_ 101
Nvvv 88,38,148,68
E_ 102
Mvvv 88,42,168
E_ 103
Nvvv 92,38,152,72
C_ 103,102,103
C_ 102,100,101
Mvvv 92,42,172
W_ 0
E_ 104
Mvvv 92,30,192
Dv 38,%0,2048
Mvvv 92,34,212
Dv 42,v1,2048
Mvvv 88,30,188
E_ 105
Mvvv 88,34,208
E_ 106
Mvvv 84,30,184
S_ s65,s61,49152
E_ 107
Mvvv 84,34,204
S_ m0,s51,0
C_ 107,106,107
C_ 106,104,105
Mvvv 80,30,180
s_waitcnt vmcnt(13) lgkmcnt(0)
s_barrier
s_cmp_eq_u32 s11, 2
s_cbranch_scc1 .Lxs2m
.Lxs2m_back:
Mvvv 80,34,200
Ln v7,32,s65,0
Mvvv 76,30,176
E_ 108
Mvvv 76,34,196
Ln v8,32,s65,3072
S_ m0,s51,896
Mvvv 76,38,216
Tv 30,v2,12288
Tv 32,v2,18432
Mvvv 76,42,236
Ln v7,32,s65,128
Mvvv 80,38,220
s_nop 0
Tv 34,v3,12288
Tv 36,v3,18432
Mvvv 80,42,240
Ln v8,32,s65,3200
S_ m0,s51,1792
Mvvv 84,38,224
E_ 109
Mvvv 84,42,244
Ln v7,32,s65,256
Mvvv 88,38,228
E_ 110
Mvvv 88,42,248
Ln v8,32,s65,3328
Mvvv 92,38,232
E_ 111
Mvvv 92,42,252
W_ 0
C_ 111,110,111
C_ 110,108,109
Mavv 0,30,116
s_nop 0
Tv 38,v4,12288
Tv 40,v4,18432
Mavv 20,34,116
E_ 112
Mavv 24,34,120
s_nop 0
Tv 42,v5,12288
Tv 44,v5,18432
Mavv 4,30,120
E_ 113
Mavv 8,30,124
E_ 114
Mavv 28,34,124
E_ 115
Mavv 32,34,128
C_ 115,114,115
C_ 114,112,113
Mavv 12,30,128
E_ 76
Mavv 16,30,132
E_ 77
Mavv 36,34,132
W_ 0
E_ 78
Mavv 56,38,132
s_nop 0
Tv 30,v2,13312
Tv 32,v2,19456
Mavv 76,42,132
E_ 79
Mavv 72,42,128
s_nop 0
Tv 34,v3,13312
Tv 36,v3,19456
Mavv 52,38,128
C_ 96,76,77
C_ 97,78,79
Mavv 48,38,124
E_ 80
Mavv 68,42,124
E_ 81
Mavv 64,42,120
E_ 82
Mavv 44,38,120
E_ 83
Mavv 40,38,116
C_ 100,80,81
C_ 101,82,83
Mavv 60,42,116
W_ 0
E_ 84
Mavv 80,30,116
s_nop 0
Tv 38,v4,13312
Tv 40,v4,19456
Mavv 100,34,116
E_ 85
Mavv 104,34,120
s_nop 0
Tv 42,v5,13312
Tv 44,v5,19456
Mavv 84,30,120
E_ 86
Mavv 88,30,124
E_ 87
Mavv 108,34,124
C_ 104,84,85
C_ 105,86,87
Mavv 112,34,128
E_ 88
Mavv 92,30,128
E_ 89
Mavv 96,30,132
E_ 90
Mavv 116,34,132
W_ 0
E_ 91
Mavv 136,38,132
s_nop 0
Tv 30,v2,14336
Tv 32,v2,20480
Mavv 156,42,132
C_ 108,88,89
C_ 109,90,91
Mavv 152,42,128
s_nop 0
Tv 34,v3,14336
Tv 36,v3,20480
Mavv 132,38,128
E_ 92
Mavv 128,38,124
E_ 93
Mavv 148,42,124
E_ 94
Mavv 144,42,120
E_ 95
Mavv 124,38,120
C_ 112,92,93
C_ 113,94,95
Mavv 120,38,116
v_mov_b32 v92, 0x3f803f80
v_mov_b32 v93, 0x3f803f80
Mavv 140,42,116
W_ 0
v_mov_b32 v94, 0x3f803f80
v_mov_b32 v95, 0x3f803f80
Mavv 160,30,116
s_nop 0
Tv 38,v4,14336
Tv 40,v4,20480
Mavv 180,34,116
Mavv 184,34,120
s_nop 0
Tv 42,v5,14336
Tv 44,v5,20480
Mavv 164,30,120
Mavv 168,30,124
Mavv 188,34,124
S_ s65,s61,36864
Mavv 192,34,128
S_ m0,s51,12288
Mavv 172,30,128
s_waitcnt vmcnt(13) lgkmcnt(0)
s_barrier
s_cmp_eq_u32 s11, 2
s_cbranch_scc1 .Lxs2e
.Lxs2e_back:
Mavv 176,30,132
L_ v9,36,s65,0
Mavv 196,34,132
Mavv 216,38,132
Dv 30,%0,55296
Mavv 236,42,132
Dv 34,v1,55296
Mavv 232,42,128
L_ v9,36,s65,3072
S_ m0,s51,13184
Mavv 212,38,128
Mavv 208,38,124
L_ v9,36,s65,128
Mavv 228,42,124
Mavv 224,42,120
L_ v9,36,s65,3200
S_ m0,s51,14080
Mavv 204,38,120
Mavv 200,38,116
L_ v9,36,s65,256
Mavv 220,42,116
s_bitcmp1_b32 s67, 0
s_cbranch_scc1 .Lhsd0_0
.Lhsd0_0_back:
.Lstep0_3:
Zvvv 76,92,96
Dv 38,%0,56320
Zvvv 80,92,100
Dv 42,v1,56320
Zvvv 84,92,104
Zvvv 88,92,108
Zvvv 92,92,112
W_ 2
Nvvv 116,30,136,56
Mvvv 116,34,156
Nvvv 120,30,140,60
s_nop 1
v_max3_f32 v10, v76, v80, v84
s_nop 0
v_max3_f32 v10, v10, v88, v92
Mvvv 120,34,160
v_cmp_lt_f32 vcc, 0x42000000, v10
s_cbranch_vccnz .Ltramp0_2
Nvvv 124,30,144,64
A_ v46,v46,v76
A_ v47,v47,v80
Mvvv 124,34,164
A_ v48,v48,v84
A_ v49,v49,v88
Nvvv 128,30,148,68
A_ v50,v50,v92
Mvvv 128,34,168
L_ v9,36,s65,3328
Nvvv 132,30,152,72
Mvvv 132,34,172
W_ 0
Mvvv 132,38,192
Dv 30,%0,57344
Mvvv 132,42,212
Dv 34,v1,57344
Mvvv 128,38,188
Mvvv 128,42,208
Mvvv 124,38,184
Mvvv 124,42,204
Mvvv 120,38,180
Mvvv 120,42,200
Mvvv 116,38,176
Mvvv 116,42,196
W_ 0
Mvvv 116,30,216
Dv 38,%0,49152
Mvvv 116,34,236
Dv 42,v1,49152
Mvvv 120,30,220
Mvvv 120,34,240
Mvvv 124,30,224
Mvvv 124,34,244
Mvvv 128,30,228
Mvvv 128,34,248
s_nop 0
E_ 116
Mvvv 132,30,232
E_ 117
Mvvv 132,34,252
W_ 0
E_ 118
Nvvv 76,38,136,56
Dv 30,%0,50176
Mvvv 76,42,156
Dv 34,v1,50176
Nvvv 80,38,140,60
E_ 119
Mvvv 80,42,160
C_ 119,118,119
C_ 118,116,117
Nvvv 84,38,144,64
E_ 120
Mvvv 84,42,164
E_ 121
Nvvv 88,38,148,68
E_ 122
Mvvv 88,42,168
E_ 123
Nvvv 92,38,152,72
C_ 123,122,123
C_ 122,120,121
Mvvv 92,42,172
W_ 0
E_ 124
Mvvv 92,30,192
Dv 38,%0,51200
Mvvv 92,34,212
Dv 42,v1,51200
Mvvv 88,30,188
E_ 125
Mvvv 88,34,208
E_ 126
Mvvv 84,30,184
S_ s65,s61,61440
E_ 127
Mvvv 84,34,204
S_ m0,s51,49152
C_ 127,126,127
C_ 126,124,125
Mvvv 80,30,180
s_waitcnt vmcnt(13) lgkmcnt(0)
s_barrier
s_cmp_eq_u32 s11, 2
s_cbranch_scc1 .Lxs3m
.Lxs3m_back:
Mvvv 80,34,200
Ln v7,32,s65,0
Mvvv 76,30,176
E_ 128
Mvvv 76,34,196
Ln v8,32,s65,3072
S_ m0,s51,50048
Mvvv 76,38,216
Tv 30,v2,24576
Tv 32,v2,30720
Mvvv 76,42,236
Ln v7,32,s65,128
Mvvv 80,38,220
s_nop 0
Tv 34,v3,24576
Tv 36,v3,30720
Mvvv 80,42,240
Ln v8,32,s65,3200
S_ m0,s51,50944
Mvvv 84,38,224
E_ 129
Mvvv 84,42,244
Ln v7,32,s65,256
Mvvv 88,38,228
E_ 130
Mvvv 88,42,248
Ln v8,32,s65,3328
Mvvv 92,38,232
E_ 131
Mvvv 92,42,252
W_ 0
C_ 131,130,131
C_ 130,128,129
Mavv 0,30,96
.Lpad_a0_1:
s_cbranch_execz .Lhop_a0_r
Tv 38,v4,24576
Tv 40,v4,30720
Mavv 20,34,96
E_ 132
Mavv 24,34,100
s_nop 0
Tv 42,v5,24576
Tv 44,v5,30720
Mavv 4,30,100
E_ 133
Mavv 8,30,104
E_ 134
Mavv 28,34,104
E_ 135
Mavv 32,34,108
C_ 135,134,135
C_ 134,132,133
Mavv 12,30,108
E_ 76
Mavv 16,30,112
E_ 77
Mavv 36,34,112
W_ 0
E_ 78
Mavv 56,38,112
s_nop 0
Tv 30,v2,25600
Tv 32,v2,31744
Mavv 76,42,112
E_ 79
Mavv 72,42,108
s_nop 0
Tv 34,v3,25600
Tv 36,v3,31744
Mavv 52,38,108
C_ 116,76,77
C_ 117,78,79
Mavv 48,38,104
E_ 80
Mavv 68,42,104
E_ 81
Mavv 64,42,100
E_ 82
Mavv 44,38,100
E_ 83
Mavv 40,38,96
C_ 120,80,81
C_ 121,82,83
Mavv 60,42,96
W_ 0
E_ 84
Mavv 80,30,96
s_nop 0
Tv 38,v4,25600
Tv 40,v4,31744
Mavv 100,34,96
E_ 85
Mavv 104,34,100
s_nop 0
Tv 42,v5,25600
Tv 44,v5,31744
Mavv 84,30,100
E_ 86
Mavv 88,30,104
E_ 87
Mavv 108,34,104
C_ 124,84,85
C_ 125,86,87
Mavv 112,34,108
E_ 88
Mavv 92,30,108
E_ 89
Mavv 96,30,112
E_ 90
Mavv 116,34,112
W_ 0
E_ 91
Mavv 136,38,112
s_nop 0
Tv 30,v2,26624
Tv 32,v2,32768
Mavv 156,42,112
C_ 128,88,89
C_ 129,90,91
Mavv 152,42,108
s_nop 0
Tv 34,v3,26624
Tv 36,v3,32768
Mavv 132,38,108
E_ 92
Mavv 128,38,104
E_ 93
Mavv 148,42,104
E_ 94
Mavv 144,42,100
E_ 95
Mavv 124,38,100
C_ 132,92,93
C_ 133,94,95
Mavv 120,38,96
v_mov_b32 v92, 0x3f803f80
v_mov_b32 v93, 0x3f803f80
Mavv 140,42,96
W_ 0
v_mov_b32 v94, 0x3f803f80
v_mov_b32 v95, 0x3f803f80
Mavv 160,30,96
s_nop 0
Tv 38,v4,26624
Tv 40,v4,32768
Mavv 180,34,96
Mavv 184,34,100
s_nop 0
Tv 42,v5,26624
Tv 44,v5,32768
Mavv 164,30,100
Mavv 168,30,104
Mavv 188,34,104
S_ s65,s61,49152
Mavv 192,34,108
S_ m0,s51,24576
Mavv 172,30,108
s_waitcnt vmcnt(12) lgkmcnt(0)
s_barrier
s_cmp_eq_u32 s11, 2
s_cbranch_scc1 .Lxs3e
.Lxs3e_back:
Mavv 176,30,112
L_ v9,36,s65,0
Mavv 196,34,112
S_ s61,s61,24576
Mavv 216,38,112
s_lshr_b32 s67, s67, 1
Dv 30,%0,6144
Mavv 236,42,112
s_bitcmp1_b32 s67, 9
s_cselect_b32 s11, s13, s11
Dv 34,v1,6144
Mavv 232,42,108
L_ v9,36,s65,3072
S_ m0,s51,25472
Mavv 212,38,108
Mavv 208,38,104
L_ v9,36,s65,128
Mavv 228,42,104
Mavv 224,42,100
L_ v9,36,s65,3200
S_ m0,s51,26368
Mavv 204,38,100
Mavv 200,38,96
L_ v9,36,s65,256
Mavv 220,42,96
s_cmp_ge_u32 s67, 256
s_cbranch_scc1 .Lstep0_2
s_sub_u32 s61, s61, 147456
.Lstep0_14:
Zvvv 76,92,116
Dv 38,%0,7168
Zvvv 80,92,120
Dv 42,v1,7168
Zvvv 84,92,124
Zvvv 88,92,128
Zvvv 92,92,132
W_ 2
Nvvv 96,30,136,56
Mvvv 96,34,156
Nvvv 100,30,140,60
s_nop 1
v_max3_f32 v10, v76, v80, v84
s_nop 0
v_max3_f32 v10, v10, v88, v92
Mvvv 100,34,160
v_cmp_lt_f32 vcc, 0x42000000, v10
s_cbranch_vccnz .Ltramp0_13
Nvvv 104,30,144,64
A_ v46,v46,v76
A_ v47,v47,v80
Mvvv 104,34,164
A_ v48,v48,v84
A_ v49,v49,v88
Nvvv 108,30,148,68
A_ v50,v50,v92
Mvvv 108,34,168
L_ v9,36,s65,3328
Nvvv 112,30,152,72
R_ v20,a240
Mvvv 112,34,172
W_ 0
v_xor_b32 v21, 64, v20
Mvvv 112,38,192
S_ s65,s60,0
Dv 30,%0,8192
Mvvv 112,42,212
S_ m0,s50,0
Dv 34,v1,8192
Mvvv 108,38,188
L_ v20,24,s65,0
Mvvv 108,42,208
S_ s65,s60,10240
Mvvv 104,38,184
S_ m0,s50,1024
Mvvv 104,42,204
L_ v21,24,s65,0
Mvvv 100,38,180
S_ s65,s60,20480
Mvvv 100,42,200
S_ m0,s50,2048
Mvvv 96,38,176
L_ v20,24,s65,0
Mvvv 96,42,196
S_ s65,s60,30720
W_ 0
Mvvv 96,30,216
S_ m0,s50,3072
Dv 38,%0,0
Mvvv 96,34,236
Dv 42,v1,0
Mvvv 100,30,220
L_ v21,24,s65,0
Mvvv 100,34,240
S_ s65,s60,40960
Mvvv 104,30,224
S_ m0,s50,4096
Mvvv 104,34,244
L_ v20,24,s65,0
Mvvv 108,30,228
Mvvv 108,34,248
E_ 96
Mvvv 112,30,232
E_ 97
Mvvv 112,34,252
W_ 0
E_ 98
Nvvv 76,38,136,56
Dv 30,%0,1024
Mvvv 76,42,156
Dv 34,v1,1024
Nvvv 80,38,140,60
E_ 99
Mvvv 80,42,160
C_ 99,98,99
C_ 98,96,97
Nvvv 84,38,144,64
E_ 100
Mvvv 84,42,164
E_ 101
Nvvv 88,38,148,68
E_ 102
Mvvv 88,42,168
E_ 103
Nvvv 92,38,152,72
C_ 103,102,103
C_ 102,100,101
Mvvv 92,42,172
W_ 0
E_ 104
Mvvv 92,30,192
Dv 38,%0,2048
Mvvv 92,34,212
Dv 42,v1,2048
Mvvv 88,30,188
E_ 105
Mvvv 88,34,208
E_ 106
Mvvv 84,30,184
E_ 107
Mvvv 84,34,204
C_ 107,106,107
C_ 106,104,105
Mvvv 80,30,180
s_waitcnt vmcnt(17) lgkmcnt(0)
s_barrier
s_cmp_eq_u32 s11, 2
s_cbranch_scc1 .Lxs14m
.Lxs14m_back:
Mvvv 80,34,200
E_ 108
Mvvv 76,30,176
E_ 109
Mvvv 76,34,196
E_ 110
Mvvv 76,38,216
s_nop 0
Tv 30,v2,12288
Tv 32,v2,18432
Mvvv 76,42,236
E_ 111
Mvvv 80,38,220
s_nop 0
Tv 34,v3,12288
Tv 36,v3,18432
Mvvv 80,42,240
C_ 111,110,111
C_ 110,108,109
Mvvv 84,38,224
E_ 112
Mvvv 84,42,244
E_ 113
Mvvv 88,38,228
E_ 114
Mvvv 88,42,248
E_ 115
Mvvv 92,38,232
C_ 115,114,115
C_ 114,112,113
Mvvv 92,42,252
W_ 0
E_ 76
Mavv 0,30,116
s_nop 0
Tv 38,v4,12288
Tv 40,v4,18432
Mavv 20,34,116
E_ 77
Mavv 24,34,120
s_nop 0
Tv 42,v5,12288
Tv 44,v5,18432
Mavv 4,30,120
E_ 78
Mavv 8,30,124
E_ 79
Mavv 28,34,124
C_ 96,76,77
C_ 97,78,79
Mavv 32,34,128
E_ 80
Mavv 12,30,128
E_ 81
Mavv 16,30,132
E_ 82
Mavv 36,34,132
W_ 0
E_ 83
Mavv 56,38,132
s_nop 0
Tv 30,v2,13312
Tv 32,v2,19456
Mavv 76,42,132
C_ 100,80,81
C_ 101,82,83
Mavv 72,42,128
s_nop 0
Tv 34,v3,13312
Tv 36,v3,19456
Mavv 52,38,128
E_ 84
Mavv 48,38,124
E_ 85
Mavv 68,42,124
E_ 86
Mavv 64,42,120
E_ 87
Mavv 44,38,120
C_ 104,84,85
C_ 105,86,87
Mavv 40,38,116
E_ 88
Mavv 60,42,116
W_ 0
E_ 89
Mavv 80,30,116
s_nop 0
Tv 38,v4,13312
Tv 40,v4,19456
Mavv 100,34,116
E_ 90
Mavv 104,34,120
s_nop 0
Tv 42,v5,13312
Tv 44,v5,19456
Mavv 84,30,120
E_ 91
Mavv 88,30,124
C_ 108,88,89
C_ 109,90,91
Mavv 108,34,124
E_ 92
Mavv 112,34,128
E_ 93
Mavv 92,30,128
E_ 94
Mavv 96,30,132
E_ 95
Mavv 116,34,132
W_ 0
C_ 112,92,93
C_ 113,94,95
Mavv 136,38,132
s_nop 0
Tv 30,v2,14336
Tv 32,v2,20480
Mavv 156,42,132
v_mov_b32 v92, 0x3f803f80
v_mov_b32 v93, 0x3f803f80
Mavv 152,42,128
s_nop 0
Tv 34,v3,14336
Tv 36,v3,20480
Mavv 132,38,128
v_mov_b32 v94, 0x3f803f80
v_mov_b32 v95, 0x3f803f80
Mavv 128,38,124
Mavv 148,42,124
Mavv 144,42,120
Mavv 124,38,120
Mavv 120,38,116
Mavv 140,42,116
W_ 0
Mavv 160,30,116
s_nop 0
Tv 38,v4,14336
Tv 40,v4,20480
Mavv 180,34,116
Mavv 184,34,120
s_nop 0
Tv 42,v5,14336
Tv 44,v5,20480
Mavv 164,30,120
Mavv 168,30,124
Mavv 188,34,124
Mavv 192,34,128
)A"
R"A(Mavv 172,30,128
s_waitcnt vmcnt(11) lgkmcnt(0)
s_barrier
s_cmp_eq_u32 s11, 2
s_cbranch_scc1 .Lxs14e
.Lxs14e_back:
Mavv 176,30,132
S_ s65,s59,0
Mavv 196,34,132
S_ m0,s52,8192
Mavv 216,38,132
Dv 30,%0,55296
Mavv 236,42,132
Dv 34,v1,55296
Mavv 232,42,128
L_ v6,28,s65,0
Mavv 212,38,128
S_ s65,s59,98304
Mavv 208,38,124
S_ m0,s52,9216
Mavv 228,42,124
L_ v6,28,s65,0
Mavv 224,42,120
S_ s65,s59,196608
Mavv 204,38,120
S_ m0,s52,10240
Mavv 200,38,116
L_ v6,28,s65,0
Mavv 220,42,116
S_ s65,s59,294912
.Lstep0_15:
Zvvv 76,92,96
Dv 38,%0,56320
Zvvv 80,92,100
Dv 42,v1,56320
Zvvv 84,92,104
Zvvv 88,92,108
Zvvv 92,92,112
W_ 2
Nvvv 116,30,136,56
Mvvv 116,34,156
Nvvv 120,30,140,60
s_nop 1
v_max3_f32 v10, v76, v80, v84
s_nop 0
v_max3_f32 v10, v10, v88, v92
Mvvv 120,34,160
v_cmp_lt_f32 vcc, 0x42000000, v10
s_cbranch_vccnz .Ltramp0_14
Nvvv 124,30,144,64
S_ m0,s52,11264
A_ v46,v46,v76
A_ v47,v47,v80
Mvvv 124,34,164
A_ v48,v48,v84
A_ v49,v49,v88
Nvvv 128,30,148,68
A_ v50,v50,v92
Mvvv 128,34,168
L_ v6,28,s65,0
Nvvv 132,30,152,72
S_ s65,s59,0
Mvvv 132,34,172
S_ m0,s52,16256
W_ 0
Mvvv 132,38,192
Dv 30,%0,57344
Mvvv 132,42,212
Dv 34,v1,57344
Mvvv 128,38,188
L_ v6,28,s65,128
Mvvv 128,42,208
S_ s65,s59,98304
Mvvv 124,38,184
S_ m0,s52,17280
Mvvv 124,42,204
L_ v6,28,s65,128
Mvvv 120,38,180
S_ s65,s59,196608
Mvvv 120,42,200
S_ m0,s52,18304
Mvvv 116,38,176
L_ v6,28,s65,128
Mvvv 116,42,196
S_ s65,s59,294912
W_ 0
Mvvv 116,30,216
S_ m0,s52,19328
Dv 38,%0,49152
Mvvv 116,34,236
Dv 42,v1,49152
Mvvv 120,30,220
L_ v6,28,s65,128
Mvvv 120,34,240
S_ s65,s59,786432
Mvvv 124,30,224
S_ m0,s52,0
Mvvv 124,34,244
L_ v6,28,s65,0
Mvvv 128,30,228
S_ s65,s59,884736
Mvvv 128,34,248
S_ m0,s52,1024
E_ 116
Mvvv 132,30,232
L_ v6,28,s65,0
Mvvv 132,34,252
S_ s65,s59,983040
E_ 117
W_ 1
Nvvv 76,38,136,56
S_ m0,s52,2048
Dv 30,%0,50176
W_ 1
Mvvv 76,42,156
Dv 34,v1,50176
Nvvv 80,38,140,60
L_ v6,28,s65,0
Mvvv 80,42,160
S_ s65,s59,1081344
E_ 118
Nvvv 84,38,144,64
S_ m0,s52,3072
E_ 119
Mvvv 84,42,164
L_ v6,28,s65,0
Nvvv 88,38,148,68
S_ s65,s60,128
C_ 119,118,119
C_ 118,116,117
Mvvv 88,42,168
S_ m0,s50,20480
E_ 120
Nvvv 92,38,152,72
L_ v20,24,s65,0
Mvvv 92,42,172
S_ s65,s60,10368
E_ 121
W_ 1
Mvvv 92,30,192
S_ m0,s50,21504
Dv 38,%0,51200
W_ 1
Mvvv 92,34,212
Dv 42,v1,51200
Mvvv 88,30,188
L_ v21,24,s65,0
Mvvv 88,34,208
S_ s65,s60,20608
E_ 122
Mvvv 84,30,184
S_ m0,s50,22528
E_ 123
Mvvv 84,34,204
L_ v20,24,s65,0
Mvvv 80,30,180
s_waitcnt vmcnt(20) lgkmcnt(0)
s_barrier
s_cmp_eq_u32 s11, 2
s_cbranch_scc1 .Lxs15m
.Lxs15m_back:
S_ s65,s60,30848
Mvvv 80,34,200
S_ m0,s50,23552
C_ 123,122,123
C_ 122,120,121
Mvvv 76,30,176
L_ v21,24,s65,0
Mvvv 76,34,196
S_ s65,s60,41088
E_ 124
Mvvv 76,38,216
S_ m0,s50,24576
Tv 30,v2,24576
Tv 32,v2,30720
Mvvv 76,42,236
L_ v20,24,s65,0
Mvvv 80,38,220
.Lpad_a0_2:
s_cbranch_execz .Lhop_a0_r
Tv 34,v3,24576
Tv 36,v3,30720
Mvvv 80,42,240
E_ 125
Mvvv 84,38,224
E_ 126
Mvvv 84,42,244
E_ 127
Mvvv 88,38,228
C_ 127,126,127
C_ 126,124,125
Mvvv 88,42,248
E_ 128
Mvvv 92,38,232
E_ 129
Mvvv 92,42,252
W_ 0
E_ 130
Mavv 0,30,96
s_nop 0
Tv 38,v4,24576
Tv 40,v4,30720
Mavv 20,34,96
E_ 131
Mavv 24,34,100
s_nop 0
Tv 42,v5,24576
Tv 44,v5,30720
Mavv 4,30,100
C_ 131,130,131
C_ 130,128,129
Mavv 8,30,104
E_ 132
Mavv 28,34,104
E_ 133
Mavv 32,34,108
E_ 134
Mavv 12,30,108
E_ 135
Mavv 16,30,112
C_ 135,134,135
C_ 134,132,133
Mavv 36,34,112
W_ 0
E_ 76
Mavv 56,38,112
s_nop 0
Tv 30,v2,25600
Tv 32,v2,31744
Mavv 76,42,112
E_ 77
Mavv 72,42,108
s_nop 0
Tv 34,v3,25600
Tv 36,v3,31744
Mavv 52,38,108
E_ 78
Mavv 48,38,104
E_ 79
Mavv 68,42,104
C_ 116,76,77
C_ 117,78,79
Mavv 64,42,100
E_ 80
Mavv 44,38,100
E_ 81
Mavv 40,38,96
E_ 82
Mavv 60,42,96
W_ 0
E_ 83
Mavv 80,30,96
s_nop 0
Tv 38,v4,25600
Tv 40,v4,31744
Mavv 100,34,96
C_ 120,80,81
C_ 121,82,83
Mavv 104,34,100
s_nop 0
Tv 42,v5,25600
Tv 44,v5,31744
Mavv 84,30,100
E_ 84
Mavv 88,30,104
E_ 85
Mavv 108,34,104
E_ 86
Mavv 112,34,108
E_ 87
Mavv 92,30,108
C_ 124,84,85
C_ 125,86,87
Mavv 96,30,112
E_ 88
Mavv 116,34,112
W_ 0
E_ 89
Mavv 136,38,112
s_nop 0
Tv 30,v2,26624
Tv 32,v2,32768
Mavv 156,42,112
E_ 90
Mavv 152,42,108
s_nop 0
Tv 34,v3,26624
Tv 36,v3,32768
Mavv 132,38,108
E_ 91
Mavv 128,38,104
C_ 128,88,89
C_ 129,90,91
Mavv 148,42,104
E_ 92
Mavv 144,42,100
E_ 93
Mavv 124,38,100
E_ 94
Mavv 120,38,96
E_ 95
Mavv 140,42,96
W_ 0
C_ 132,92,93
C_ 133,94,95
Mavv 160,30,96
s_nop 0
Tv 38,v4,26624
Tv 40,v4,32768
Mavv 180,34,96
v_mov_b32 v92, 0x3f803f80
v_mov_b32 v93, 0x3f803f80
Mavv 184,34,100
s_nop 0
Tv 42,v5,26624
Tv 44,v5,32768
Mavv 164,30,100
v_mov_b32 v94, 0x3f803f80
v_mov_b32 v95, 0x3f803f80
Mavv 168,30,104
Mavv 188,34,104
Mavv 192,34,108
Mavv 172,30,108
s_waitcnt vmcnt(63) lgkmcnt(0)
s_barrier
s_cmp_eq_u32 s11, 2
s_cbranch_scc1 .Lxs15e
.Lxs15e_back:
Mavv 176,30,112
S_ s65,s59,0
Mavv 196,34,112
S_ m0,s52,24320
Mavv 216,38,112
s_nop 0
Tv 30,v2,36864
Tv 32,v2,43008
Zvvv 76,92,116
L_ v6,28,s65,256
Zvvv 80,92,120
S_ s65,s59,98304
Tv 34,v3,36864
Tv 36,v3,43008
Zvvv 84,92,124
S_ m0,s52,25344
Zvvv 88,92,128
L_ v6,28,s65,256
Zvvv 92,92,132
S_ s65,s59,196608
Mavv 236,42,112
S_ m0,s52,26368
Mavv 232,42,108
L_ v6,28,s65,256
Mavv 212,38,108
S_ s65,s59,294912
Mavv 208,38,104
S_ m0,s52,27392
v_max3_f32 v10, v76, v80, v84
Mavv 228,42,104
v_max3_f32 v10, v10, v88, v92
v_cmp_lt_f32 vcc, 0x42000000, v10
Mavv 224,42,100
L_ v6,28,s65,256
Mavv 204,38,100
Mavv 200,38,96
Mavv 220,42,96
s_cbranch_vccnz .Ltramp0_15
.Ljoin0:
W_ 2
Mavv 0,30,116
s_mov_b32 s58, 1
Tv 38,v4,36864
Tv 40,v4,43008
W_ 2
Mavv 20,34,116
S_ s61,s61,393216
R_ v12,a248
s_mov_b32 s85, 4026531840
Mavv 24,34,120
s_nop 0
Tv 42,v5,36864
Tv 44,v5,43008
Mavv 4,30,120
A_ v46,v46,v76
A_ v47,v47,v80
Mavv 8,30,124
A_ v48,v48,v84
A_ v49,v49,v88
Mavv 28,34,124
A_ v50,v50,v92
Mavv 32,34,128
v_rcp_f32 v46, v46
Mavv 12,30,128
v_rcp_f32 v47, v47
Mavv 16,30,132
v_rcp_f32 v48, v48
Mavv 36,34,132
W_ 0
v_rcp_f32 v49, v49
Mavv 56,38,132
s_nop 0
Tv 30,v2,37888
Tv 32,v2,44032
Mavv 76,42,132
v_rcp_f32 v50, v50
Mavv 72,42,128
s_nop 0
Tv 34,v3,37888
Tv 36,v3,44032
Mavv 52,38,128
R_ v96,a0
R_ v97,a1
Mavv 48,38,124
R_ v98,a2
R_ v99,a3
Mavv 68,42,124
R_ v100,a20
R_ v101,a21
Mavv 64,42,120
R_ v102,a22
R_ v103,a23
Mavv 44,38,120
U_ v96,v96,v46
U_ v97,v97,v46
Mavv 40,38,116
U_ v98,v98,v46
U_ v99,v99,v46
Mavv 60,42,116
W_ 0
U_ v100,v100,v46
U_ v101,v101,v46
Mavv 80,30,116
s_nop 0
Tv 38,v4,37888
Tv 40,v4,44032
Mavv 100,34,116
U_ v102,v102,v46
U_ v103,v103,v46
Mavv 104,34,120
s_nop 0
Tv 42,v5,37888
Tv 44,v5,44032
Mavv 84,30,120
C_ 76,96,97
C_ 77,98,99
Mavv 88,30,124
C_ 78,100,101
C_ 79,102,103
Mavv 108,34,124
X_ a0,v76
X_ a1,v77
Mavv 112,34,128
X_ a2,v78
X_ a3,v79
Mavv 92,30,128
R_ v104,a4
R_ v105,a5
Mavv 96,30,132
R_ v106,a6
R_ v107,a7
Mavv 116,34,132
W_ 0
R_ v108,a24
R_ v109,a25
Mavv 136,38,132
s_nop 0
Tv 30,v2,38912
Tv 32,v2,45056
Mavv 156,42,132
R_ v110,a26
R_ v111,a27
Mavv 152,42,128
s_nop 0
Tv 34,v3,38912
Tv 36,v3,45056
Mavv 132,38,128
U_ v104,v104,v47
U_ v105,v105,v47
Mavv 128,38,124
U_ v106,v106,v47
U_ v107,v107,v47
Mavv 148,42,124
U_ v108,v108,v47
U_ v109,v109,v47
Mavv 144,42,120
U_ v110,v110,v47
U_ v111,v111,v47
Mavv 124,38,120
C_ 80,104,105
C_ 81,106,107
Mavv 120,38,116
C_ 82,108,109
C_ 83,110,111
Mavv 140,42,116
W_ 0
X_ a4,v80
X_ a5,v81
Mavv 160,30,116
s_nop 0
Tv 38,v4,38912
Tv 40,v4,45056
Mavv 180,34,116
X_ a6,v82
X_ a7,v83
Mavv 184,34,120
s_nop 0
Tv 42,v5,38912
Tv 44,v5,45056
Mavv 164,30,120
R_ v96,a8
R_ v97,a9
Mavv 168,30,124
R_ v98,a10
R_ v99,a11
Mavv 188,34,124
R_ v100,a28
R_ v101,a29
Mavv 192,34,128
R_ v102,a30
R_ v103,a31
Mavv 172,30,128
U_ v96,v96,v48
U_ v97,v97,v48
Mavv 176,30,132
U_ v98,v98,v48
U_ v99,v99,v48
Mavv 196,34,132
W_ 0
U_ v100,v100,v48
U_ v101,v101,v48
Mavv 216,38,132
U_ v102,v102,v48
U_ v103,v103,v48
Mavv 236,42,132
C_ 84,96,97
C_ 85,98,99
Mavv 232,42,128
C_ 86,100,101
C_ 87,102,103
Mavv 212,38,128
X_ a8,v84
X_ a9,v85
Mavv 208,38,124
X_ a10,v86
X_ a11,v87
Mavv 228,42,124
Mavv 224,42,120
Mavv 204,38,120
Mavv 200,38,116
Mavv 220,42,116
.p2align 3
R_ v104,a12
R_ v105,a13
R_ v106,a14
R_ v107,a15
R_ v108,a32
R_ v109,a33
R_ v110,a34
R_ v111,a35
v_pk_mul_f32 v[104:105], v[104:105], v[48:49] op_sel:[0,1]
v_pk_mul_f32 v[106:107], v[106:107], v[48:49] op_sel:[0,1]
v_pk_mul_f32 v[108:109], v[108:109], v[48:49] op_sel:[0,1]
v_pk_mul_f32 v[110:111], v[110:111], v[48:49] op_sel:[0,1]
C_ 88,104,105
C_ 89,106,107
C_ 90,108,109
C_ 91,110,111
X_ a12,v88
X_ a13,v89
X_ a14,v90
X_ a15,v91
.p2align 3
R_ v96,a16
R_ v97,a17
R_ v98,a18
R_ v99,a19
R_ v100,a36
R_ v101,a37
R_ v102,a38
R_ v103,a39
v_pk_mul_f32 v[96:97], v[96:97], v[50:51] op_sel_hi:[1,0]
v_pk_mul_f32 v[98:99], v[98:99], v[50:51] op_sel_hi:[1,0]
v_pk_mul_f32 v[100:101], v[100:101], v[50:51] op_sel_hi:[1,0]
v_pk_mul_f32 v[102:103], v[102:103], v[50:51] op_sel_hi:[1,0]
C_ 92,96,97
C_ 93,98,99
C_ 94,100,101
C_ 95,102,103
X_ a16,v92
X_ a17,v93
X_ a18,v94
X_ a19,v95
.p2align 3
R_ v104,a40
R_ v105,a41
R_ v106,a42
R_ v107,a43
R_ v108,a60
R_ v109,a61
R_ v110,a62
R_ v111,a63
v_pk_mul_f32 v[104:105], v[104:105], v[46:47] op_sel_hi:[1,0]
v_pk_mul_f32 v[106:107], v[106:107], v[46:47] op_sel_hi:[1,0]
v_pk_mul_f32 v[108:109], v[108:109], v[46:47] op_sel_hi:[1,0]
v_pk_mul_f32 v[110:111], v[110:111], v[46:47] op_sel_hi:[1,0]
C_ 76,104,105
C_ 77,106,107
C_ 78,108,109
C_ 79,110,111
X_ a40,v76
X_ a41,v77
X_ a42,v78
X_ a43,v79
.p2align 3
R_ v96,a44
R_ v97,a45
R_ v98,a46
R_ v99,a47
R_ v100,a64
R_ v101,a65
R_ v102,a66
R_ v103,a67
v_pk_mul_f32 v[96:97], v[96:97], v[46:47] op_sel:[0,1]
v_pk_mul_f32 v[98:99], v[98:99], v[46:47] op_sel:[0,1]
v_pk_mul_f32 v[100:101], v[100:101], v[46:47] op_sel:[0,1]
v_pk_mul_f32 v[102:103], v[102:103], v[46:47] op_sel:[0,1]
C_ 80,96,97
C_ 81,98,99
C_ 82,100,101
C_ 83,102,103
X_ a44,v80
X_ a45,v81
X_ a46,v82
X_ a47,v83
.p2align 3
R_ v104,a48
R_ v105,a49
R_ v106,a50
R_ v107,a51
R_ v108,a68
R_ v109,a69
R_ v110,a70
R_ v111,a71
v_pk_mul_f32 v[104:105], v[104:105], v[48:49] op_sel_hi:[1,0]
v_pk_mul_f32 v[106:107], v[106:107], v[48:49] op_sel_hi:[1,0]
v_pk_mul_f32 v[108:109], v[108:109], v[48:49] op_sel_hi:[1,0]
v_pk_mul_f32 v[110:111], v[110:111], v[48:49] op_sel_hi:[1,0]
C_ 84,104,105
C_ 85,106,107
C_ 86,108,109
C_ 87,110,111
X_ a48,v84
X_ a49,v85
X_ a50,v86
X_ a51,v87
.p2align 3
R_ v96,a52
R_ v97,a53
R_ v98,a54
R_ v99,a55
R_ v100,a72
R_ v101,a73
R_ v102,a74
R_ v103,a75
v_pk_mul_f32 v[96:97], v[96:97], v[48:49] op_sel:[0,1]
v_pk_mul_f32 v[98:99], v[98:99], v[48:49] op_sel:[0,1]
v_pk_mul_f32 v[100:101], v[100:101], v[48:49] op_sel:[0,1]
v_pk_mul_f32 v[102:103], v[102:103], v[48:49] op_sel:[0,1]
C_ 88,96,97
C_ 89,98,99
C_ 90,100,101
C_ 91,102,103
X_ a52,v88
X_ a53,v89
X_ a54,v90
X_ a55,v91
.p2align 3
R_ v104,a56
R_ v105,a57
R_ v106,a58
R_ v107,a59
R_ v108,a76
R_ v109,a77
R_ v110,a78
R_ v111,a79
v_pk_mul_f32 v[104:105], v[104:105], v[50:51] op_sel_hi:[1,0]
v_pk_mul_f32 v[106:107], v[106:107], v[50:51] op_sel_hi:[1,0]
v_pk_mul_f32 v[108:109], v[108:109], v[50:51] op_sel_hi:[1,0]
v_pk_mul_f32 v[110:111], v[110:111], v[50:51] op_sel_hi:[1,0]
C_ 92,104,105
C_ 93,106,107
C_ 94,108,109
C_ 95,110,111
X_ a56,v92
X_ a57,v93
X_ a58,v94
X_ a59,v95
.p2align 3
R_ v96,a80
R_ v97,a81
R_ v98,a82
R_ v99,a83
R_ v100,a100
R_ v101,a101
R_ v102,a102
R_ v103,a103
v_pk_mul_f32 v[96:97], v[96:97], v[46:47] op_sel_hi:[1,0]
v_pk_mul_f32 v[98:99], v[98:99], v[46:47] op_sel_hi:[1,0]
v_pk_mul_f32 v[100:101], v[100:101], v[46:47] op_sel_hi:[1,0]
v_pk_mul_f32 v[102:103], v[102:103], v[46:47] op_sel_hi:[1,0]
C_ 76,96,97
C_ 77,98,99
C_ 78,100,101
C_ 79,102,103
X_ a80,v76
X_ a81,v77
X_ a82,v78
X_ a83,v79
.p2align 3
R_ v104,a84
R_ v105,a85
R_ v106,a86
R_ v107,a87
R_ v108,a104
R_ v109,a105
R_ v110,a106
R_ v111,a107
v_pk_mul_f32 v[104:105], v[104:105], v[46:47] op_sel:[0,1]
v_pk_mul_f32 v[106:107], v[106:107], v[46:47] op_sel:[0,1]
v_pk_mul_f32 v[108:109], v[108:109], v[46:47] op_sel:[0,1]
v_pk_mul_f32 v[110:111], v[110:111], v[46:47] op_sel:[0,1]
C_ 80,104,105
C_ 81,106,107
C_ 82,108,109
C_ 83,110,111
X_ a84,v80
X_ a85,v81
X_ a86,v82
X_ a87,v83
.p2align 3
R_ v96,a88
R_ v97,a89
R_ v98,a90
R_ v99,a91
R_ v100,a108
R_ v101,a109
R_ v102,a110
R_ v103,a111
v_pk_mul_f32 v[96:97], v[96:97], v[48:49] op_sel_hi:[1,0]
v_pk_mul_f32 v[98:99], v[98:99], v[48:49] op_sel_hi:[1,0]
v_pk_mul_f32 v[100:101], v[100:101], v[48:49] op_sel_hi:[1,0]
v_pk_mul_f32 v[102:103], v[102:103], v[48:49] op_sel_hi:[1,0]
C_ 84,96,97
C_ 85,98,99
C_ 86,100,101
C_ 87,102,103
.Lpad_a0_3:
s_cbranch_execz .Lhop_a0_r
X_ a88,v84
X_ a89,v85
X_ a90,v86
X_ a91,v87
.p2align 3
R_ v104,a92
R_ v105,a93
R_ v106,a94
R_ v107,a95
R_ v108,a112
R_ v109,a113
R_ v110,a114
R_ v111,a115
v_pk_mul_f32 v[104:105], v[104:105], v[48:49] op_sel:[0,1]
v_pk_mul_f32 v[106:107], v[106:107], v[48:49] op_sel:[0,1]
v_pk_mul_f32 v[108:109], v[108:109], v[48:49] op_sel:[0,1]
v_pk_mul_f32 v[110:111], v[110:111], v[48:49] op_sel:[0,1]
C_ 88,104,105
C_ 89,106,107
C_ 90,108,109
C_ 91,110,111
X_ a92,v88
X_ a93,v89
X_ a94,v90
X_ a95,v91
.p2align 3
R_ v96,a96
R_ v97,a97
R_ v98,a98
R_ v99,a99
R_ v100,a116
R_ v101,a117
R_ v102,a118
R_ v103,a119
v_pk_mul_f32 v[96:97], v[96:97], v[50:51] op_sel_hi:[1,0]
v_pk_mul_f32 v[98:99], v[98:99], v[50:51] op_sel_hi:[1,0]
v_pk_mul_f32 v[100:101], v[100:101], v[50:51] op_sel_hi:[1,0]
v_pk_mul_f32 v[102:103], v[102:103], v[50:51] op_sel_hi:[1,0]
C_ 92,96,97
C_ 93,98,99
C_ 94,100,101
C_ 95,102,103
X_ a96,v92
X_ a97,v93
X_ a98,v94
X_ a99,v95
.p2align 3
R_ v104,a120
R_ v105,a121
R_ v106,a122
R_ v107,a123
R_ v108,a140
R_ v109,a141
R_ v110,a142
R_ v111,a143
v_pk_mul_f32 v[104:105], v[104:105], v[46:47] op_sel_hi:[1,0]
v_pk_mul_f32 v[106:107], v[106:107], v[46:47] op_sel_hi:[1,0]
v_pk_mul_f32 v[108:109], v[108:109], v[46:47] op_sel_hi:[1,0]
v_pk_mul_f32 v[110:111], v[110:111], v[46:47] op_sel_hi:[1,0]
C_ 76,104,105
C_ 77,106,107
C_ 78,108,109
C_ 79,110,111
X_ a120,v76
X_ a121,v77
X_ a122,v78
X_ a123,v79
.p2align 3
R_ v96,a124
R_ v97,a125
R_ v98,a126
R_ v99,a127
R_ v100,a144
R_ v101,a145
R_ v102,a146
R_ v103,a147
v_pk_mul_f32 v[96:97], v[96:97], v[46:47] op_sel:[0,1]
v_pk_mul_f32 v[98:99], v[98:99], v[46:47] op_sel:[0,1]
v_pk_mul_f32 v[100:101], v[100:101], v[46:47] op_sel:[0,1]
v_pk_mul_f32 v[102:103], v[102:103], v[46:47] op_sel:[0,1]
C_ 80,96,97
C_ 81,98,99
C_ 82,100,101
C_ 83,102,103
X_ a124,v80
X_ a125,v81
X_ a126,v82
X_ a127,v83
.p2align 3
R_ v104,a128
R_ v105,a129
R_ v106,a130
R_ v107,a131
R_ v108,a148
R_ v109,a149
R_ v110,a150
R_ v111,a151
v_pk_mul_f32 v[104:105], v[104:105], v[48:49] op_sel_hi:[1,0]
v_pk_mul_f32 v[106:107], v[106:107], v[48:49] op_sel_hi:[1,0]
v_pk_mul_f32 v[108:109], v[108:109], v[48:49] op_sel_hi:[1,0]
v_pk_mul_f32 v[110:111], v[110:111], v[48:49] op_sel_hi:[1,0]
C_ 84,104,105
C_ 85,106,107
C_ 86,108,109
C_ 87,110,111
X_ a128,v84
X_ a129,v85
X_ a130,v86
X_ a131,v87
.p2align 3
R_ v96,a132
R_ v97,a133
R_ v98,a134
R_ v99,a135
R_ v100,a152
R_ v101,a153
R_ v102,a154
R_ v103,a155
v_pk_mul_f32 v[96:97], v[96:97], v[48:49] op_sel:[0,1]
v_pk_mul_f32 v[98:99], v[98:99], v[48:49] op_sel:[0,1]
v_pk_mul_f32 v[100:101], v[100:101], v[48:49] op_sel:[0,1]
v_pk_mul_f32 v[102:103], v[102:103], v[48:49] op_sel:[0,1]
C_ 88,96,97
C_ 89,98,99
C_ 90,100,101
C_ 91,102,103
X_ a132,v88
X_ a133,v89
X_ a134,v90
X_ a135,v91
.p2align 3
R_ v104,a136
R_ v105,a137
R_ v106,a138
R_ v107,a139
R_ v108,a156
R_ v109,a157
R_ v110,a158
R_ v111,a159
v_pk_mul_f32 v[104:105], v[104:105], v[50:51] op_sel_hi:[1,0]
v_pk_mul_f32 v[106:107], v[106:107], v[50:51] op_sel_hi:[1,0]
v_pk_mul_f32 v[108:109], v[108:109], v[50:51] op_sel_hi:[1,0]
v_pk_mul_f32 v[110:111], v[110:111], v[50:51] op_sel_hi:[1,0]
C_ 92,104,105
C_ 93,106,107
C_ 94,108,109
C_ 95,110,111
X_ a136,v92
X_ a137,v93
X_ a138,v94
X_ a139,v95
.p2align 3
R_ v96,a160
R_ v97,a161
R_ v98,a162
R_ v99,a163
R_ v100,a180
R_ v101,a181
R_ v102,a182
R_ v103,a183
v_pk_mul_f32 v[96:97], v[96:97], v[46:47] op_sel_hi:[1,0]
v_pk_mul_f32 v[98:99], v[98:99], v[46:47] op_sel_hi:[1,0]
v_pk_mul_f32 v[100:101], v[100:101], v[46:47] op_sel_hi:[1,0]
v_pk_mul_f32 v[102:103], v[102:103], v[46:47] op_sel_hi:[1,0]
C_ 76,96,97
C_ 77,98,99
C_ 78,100,101
C_ 79,102,103
X_ a160,v76
X_ a161,v77
X_ a162,v78
X_ a163,v79
.p2align 3
R_ v104,a164
R_ v105,a165
R_ v106,a166
R_ v107,a167
R_ v108,a184
R_ v109,a185
R_ v110,a186
R_ v111,a187
v_pk_mul_f32 v[104:105], v[104:105], v[46:47] op_sel:[0,1]
v_pk_mul_f32 v[106:107], v[106:107], v[46:47] op_sel:[0,1]
v_pk_mul_f32 v[108:109], v[108:109], v[46:47] op_sel:[0,1]
v_pk_mul_f32 v[110:111], v[110:111], v[46:47] op_sel:[0,1]
C_ 80,104,105
C_ 81,106,107
C_ 82,108,109
C_ 83,110,111
X_ a164,v80
X_ a165,v81
X_ a166,v82
X_ a167,v83
.p2align 3
R_ v96,a168
R_ v97,a169
R_ v98,a170
R_ v99,a171
R_ v100,a188
R_ v101,a189
R_ v102,a190
R_ v103,a191
v_pk_mul_f32 v[96:97], v[96:97], v[48:49] op_sel_hi:[1,0]
v_pk_mul_f32 v[98:99], v[98:99], v[48:49] op_sel_hi:[1,0]
v_pk_mul_f32 v[100:101], v[100:101], v[48:49] op_sel_hi:[1,0]
v_pk_mul_f32 v[102:103], v[102:103], v[48:49] op_sel_hi:[1,0]
C_ 84,96,97
C_ 85,98,99
C_ 86,100,101
C_ 87,102,103
X_ a168,v84
X_ a169,v85
X_ a170,v86
X_ a171,v87
.p2align 3
R_ v104,a172
R_ v105,a173
R_ v106,a174
R_ v107,a175
R_ v108,a192
R_ v109,a193
R_ v110,a194
R_ v111,a195
v_pk_mul_f32 v[104:105], v[104:105], v[48:49] op_sel:[0,1]
v_pk_mul_f32 v[106:107], v[106:107], v[48:49] op_sel:[0,1]
v_pk_mul_f32 v[108:109], v[108:109], v[48:49] op_sel:[0,1]
v_pk_mul_f32 v[110:111], v[110:111], v[48:49] op_sel:[0,1]
C_ 88,104,105
C_ 89,106,107
C_ 90,108,109
C_ 91,110,111
X_ a172,v88
X_ a173,v89
X_ a174,v90
X_ a175,v91
.p2align 3
R_ v96,a176
R_ v97,a177
R_ v98,a178
R_ v99,a179
R_ v100,a196
R_ v101,a197
R_ v102,a198
R_ v103,a199
v_pk_mul_f32 v[96:97], v[96:97], v[50:51] op_sel_hi:[1,0]
v_pk_mul_f32 v[98:99], v[98:99], v[50:51] op_sel_hi:[1,0]
v_pk_mul_f32 v[100:101], v[100:101], v[50:51] op_sel_hi:[1,0]
v_pk_mul_f32 v[102:103], v[102:103], v[50:51] op_sel_hi:[1,0]
C_ 92,96,97
C_ 93,98,99
C_ 94,100,101
C_ 95,102,103
X_ a176,v92
X_ a177,v93
X_ a178,v94
X_ a179,v95
.p2align 3
R_ v104,a200
R_ v105,a201
R_ v106,a202
R_ v107,a203
R_ v108,a220
R_ v109,a221
R_ v110,a222
R_ v111,a223
v_pk_mul_f32 v[104:105], v[104:105], v[46:47] op_sel_hi:[1,0]
v_pk_mul_f32 v[106:107], v[106:107], v[46:47] op_sel_hi:[1,0]
v_pk_mul_f32 v[108:109], v[108:109], v[46:47] op_sel_hi:[1,0]
v_pk_mul_f32 v[110:111], v[110:111], v[46:47] op_sel_hi:[1,0]
C_ 76,104,105
C_ 77,106,107
C_ 78,108,109
C_ 79,110,111
X_ a200,v76
X_ a201,v77
X_ a202,v78
X_ a203,v79
.p2align 3
R_ v96,a204
R_ v97,a205
R_ v98,a206
R_ v99,a207
R_ v100,a224
R_ v101,a225
R_ v102,a226
R_ v103,a227
v_pk_mul_f32 v[96:97], v[96:97], v[46:47] op_sel:[0,1]
v_pk_mul_f32 v[98:99], v[98:99], v[46:47] op_sel:[0,1]
v_pk_mul_f32 v[100:101], v[100:101], v[46:47] op_sel:[0,1]
v_pk_mul_f32 v[102:103], v[102:103], v[46:47] op_sel:[0,1]
C_ 80,96,97
C_ 81,98,99
C_ 82,100,101
C_ 83,102,103
X_ a204,v80
X_ a205,v81
X_ a206,v82
X_ a207,v83
v_mov_b32 v4, v20
v_mov_b32 v5, v21
R_ %0,a242
R_ v2,a243
v_xor_b32 v1, 64, %0
v_xor_b32 v3, 64, v2
s_waitcnt vmcnt(0) lgkmcnt(0)
s_barrier
Ta 20,%0,8192
Ta 22,%0,8704
Ta 24,%0,12288
Ta 26,%0,12800
Da 60,v2,0
Da 100,v3,0
Da 64,v2,2048
Da 104,v3,2048
Da 68,v2,4096
Da 108,v3,4096
Da 72,v2,6144
Da 112,v3,6144
Da 76,v2,8192
Da 116,v3,8192
Ta 28,%0,8200
Ta 30,%0,8712
Ta 32,%0,12296
Ta 34,%0,12808
.p2align 3
R_ v104,a208
R_ v105,a209
R_ v106,a210
R_ v107,a211
R_ v108,a228
R_ v109,a229
R_ v110,a230
R_ v111,a231
v_pk_mul_f32 v[104:105], v[104:105], v[48:49] op_sel_hi:[1,0]
v_pk_mul_f32 v[106:107], v[106:107], v[48:49] op_sel_hi:[1,0]
v_pk_mul_f32 v[108:109], v[108:109], v[48:49] op_sel_hi:[1,0]
v_pk_mul_f32 v[110:111], v[110:111], v[48:49] op_sel_hi:[1,0]
C_ 84,104,105
C_ 85,106,107
C_ 86,108,109
C_ 87,110,111
X_ a208,v84
X_ a209,v85
X_ a210,v86
X_ a211,v87
.p2align 3
R_ v96,a212
R_ v97,a213
R_ v98,a214
R_ v99,a215
R_ v100,a232
R_ v101,a233
R_ v102,a234
R_ v103,a235
v_pk_mul_f32 v[96:97], v[96:97], v[48:49] op_sel:[0,1]
v_pk_mul_f32 v[98:99], v[98:99], v[48:49] op_sel:[0,1]
v_pk_mul_f32 v[100:101], v[100:101], v[48:49] op_sel:[0,1]
v_pk_mul_f32 v[102:103], v[102:103], v[48:49] op_sel:[0,1]
C_ 88,96,97
C_ 89,98,99
C_ 90,100,101
C_ 91,102,103
X_ a212,v88
X_ a213,v89
X_ a214,v90
X_ a215,v91
.p2align 3
R_ v104,a216
R_ v105,a217
R_ v106,a218
R_ v107,a219
R_ v108,a236
R_ v109,a237
R_ v110,a238
R_ v111,a239
v_pk_mul_f32 v[104:105], v[104:105], v[50:51] op_sel_hi:[1,0]
v_pk_mul_f32 v[106:107], v[106:107], v[50:51] op_sel_hi:[1,0]
v_pk_mul_f32 v[108:109], v[108:109], v[50:51] op_sel_hi:[1,0]
v_pk_mul_f32 v[110:111], v[110:111], v[50:51] op_sel_hi:[1,0]
C_ 92,104,105
C_ 93,106,107
C_ 94,108,109
C_ 95,110,111
X_ a216,v92
X_ a217,v93
X_ a218,v94
X_ a219,v95
s_branch .Lq
.Ltramp0_1:
s_mov_b32 s63, 1
s_branch .Lslow1
.Ltramp0_2:
s_mov_b32 s63, 2
s_branch .Lslow0
.Ltramp0_13:
s_mov_b32 s63, 13
s_branch .Lslow1
.Ltramp0_14:
s_mov_b32 s63, 14
s_branch .Lslow0
.Ltramp0_15:
s_mov_b32 s63, 15
s_branch .Lslow1
.Lstep0_16:
Zvvv 76,92,116
Zvvv 80,92,120
Zvvv 84,92,124
Zvvv 88,92,128
Zvvv 92,92,132
s_nop 5
v_max3_f32 v10, v76, v80, v84
s_nop 0
v_max3_f32 v10, v10, v88, v92
v_cmp_lt_f32 vcc, 0x42000000, v10
s_cbranch_vccnz .Ltramp0_15
s_branch .Ljoin0
.Ltramp1_1:
s_mov_b32 s63, 1
s_branch .Lslow1
.Ltramp1_2:
s_mov_b32 s63, 2
s_branch .Lslow0
.Ltramp1_3:
s_mov_b32 s63, 3
s_branch .Lslow1
.Ltramp1_4:
s_mov_b32 s63, 4
s_branch .Lslow0
.Ltramp1_5:
s_mov_b32 s63, 5
s_branch .Lslow1
.Ltramp1_6:
s_mov_b32 s63, 6
s_branch .Lslow0
.Ltramp1_7:
s_mov_b32 s63, 7
s_branch .Lslow1
.Ltramp1_8:
s_mov_b32 s63, 8
s_branch .Lslow0
.Ltramp1_9:
s_mov_b32 s63, 9
s_branch .Lslow1
.Ltramp1_10:
s_mov_b32 s63, 10
s_branch .Lslow0
.Ltramp1_11:
s_mov_b32 s63, 11
s_branch .Lslow1
.Ltramp1_12:
s_mov_b32 s63, 12
s_branch .Lslow0
.Ltramp1_13:
s_mov_b32 s63, 13
s_branch .Lslow1
.Ltramp1_14:
s_mov_b32 s63, 14
s_branch .Lslow0
.Ltramp1_15:
s_mov_b32 s63, 15
s_branch .Lslow1
.Lstep1_16:
Zvvv 76,92,116
Zvvv 80,92,120
Zvvv 84,92,124
Zvvv 88,92,128
Zvvv 92,92,132
s_nop 5
v_max3_f32 v10, v76, v80, v84
s_nop 0
v_max3_f32 v10, v10, v88, v92
v_cmp_lt_f32 vcc, 0x42000000, v10
s_cbranch_vccnz .Ltramp1_15
s_branch .Ljoin1
.Ltramp2_1:
s_mov_b32 s63, 1
s_branch .Lslow1
.Ltramp2_2:
s_mov_b32 s63, 2
s_branch .Lslow0
.Ltramp2_3:
s_mov_b32 s63, 3
s_branch .Lslow1
.Ltramp2_4:
s_mov_b32 s63, 4
s_branch .Lslow0
.Ltramp2_5:
s_mov_b32 s63, 5
s_branch .Lslow1
.Ltramp2_6:
s_mov_b32 s63, 6
s_branch .Lslow0
.Ltramp2_7:
s_mov_b32 s63, 7
s_branch .Lslow1
.Ltramp2_8:
s_mov_b32 s63, 8
s_branch .Lslow0
.Ltramp2_9:
s_mov_b32 s63, 9
s_branch .Lslow1
.Ltramp2_10:
s_mov_b32 s63, 10
s_branch .Lslow0
.Ltramp2_11:
s_mov_b32 s63, 11
s_branch .Lslow1
.Ltramp2_12:
s_mov_b32 s63, 12
s_branch .Lslow0
.Ltramp2_13:
s_mov_b32 s63, 13
s_branch .Lslow1
.Ltramp2_14:
s_mov_b32 s63, 14
s_branch .Lslow0
.Ltramp2_15:
s_mov_b32 s63, 15
s_branch .Lslow1
.Lstep2_16:
Zvvv 76,92,116
Zvvv 80,92,120
Zvvv 84,92,124
Zvvv 88,92,128
Zvvv 92,92,132
s_nop 5
v_max3_f32 v10, v76, v80, v84
s_nop 0
v_max3_f32 v10, v10, v88, v92
v_cmp_lt_f32 vcc, 0x42000000, v10
s_cbranch_vccnz .Ltramp2_15
s_branch .Ljoin2
.Lslow0:
W_ 0
s_nop 7
s_nop 7
s_and_b32 s64, s61, 4294959104
s_mul_i32 s53, s63, 12288
S_ s64,s64,s53
R_ v11,a248
v_and_b32 v10, 0xf0, v11
v_mul_u32_u24 v10, 24, v10
v_lshrrev_b32 v11, 8, v11
v_lshl_add_u32 v10, v11, 4, v10
S_ s53,s64,6144
buffer_load_dwordx4 v[116:119], v10, s[32:35], s53 offen offset:0
S_ s53,s64,6144
buffer_load_dwordx4 v[120:123], v10, s[32:35], s53 offen offset:64
S_ s53,s64,6144
buffer_load_dwordx4 v[124:127], v10, s[32:35], s53 offen offset:128
S_ s53,s64,6144
buffer_load_dwordx4 v[128:131], v10, s[32:35], s53 offen offset:192
s_waitcnt vmcnt(0)
Nvvv 96,116,136,56
Nvvv 100,116,140,60
Nvvv 104,116,144,64
Nvvv 108,116,148,68
Nvvv 112,116,152,72
Mvvv 96,120,156
Mvvv 100,120,160
Mvvv 104,120,164
Mvvv 108,120,168
Mvvv 112,120,172
Mvvv 96,124,176
Mvvv 100,124,180
Mvvv 104,124,184
Mvvv 108,124,188
Mvvv 112,124,192
Mvvv 96,128,196
Mvvv 100,128,200
Mvvv 104,128,204
Mvvv 108,128,208
Mvvv 112,128,212
s_nop 7
S_ s53,s64,6144
buffer_load_dwordx4 v[116:119], v10, s[32:35], s53 offen offset:256
S_ s53,s64,6144
buffer_load_dwordx4 v[120:123], v10, s[32:35], s53 offen offset:320
S_ s53,s64,0
buffer_load_dwordx4 v[124:127], v10, s[32:35], s53 offen offset:0
S_ s53,s64,0
buffer_load_dwordx4 v[128:131], v10, s[32:35], s53 offen offset:64
s_waitcnt vmcnt(0)
Mvvv 96,116,216
Mvvv 100,116,220
Mvvv 104,116,224
Mvvv 108,116,228
Mvvv 112,116,232
Mvvv 96,120,236
Mvvv 100,120,240
Mvvv 104,120,244
Mvvv 108,120,248
Mvvv 112,120,252
Nvvv 76,124,136,56
Nvvv 80,124,140,60
Nvvv 84,124,144,64
Nvvv 88,124,148,68
Nvvv 92,124,152,72
Mvvv 76,128,156
Mvvv 80,128,160
Mvvv 84,128,164
Mvvv 88,128,168
Mvvv 92,128,172
s_nop 7
S_ s53,s64,0
buffer_load_dwordx4 v[116:119], v10, s[32:35], s53 offen offset:128
S_ s53,s64,0
buffer_load_dwordx4 v[120:123], v10, s[32:35], s53 offen offset:192
S_ s53,s64,0
buffer_load_dwordx4 v[124:127], v10, s[32:35], s53 offen offset:256
S_ s53,s64,0
buffer_load_dwordx4 v[128:131], v10, s[32:35], s53 offen offset:320
s_waitcnt vmcnt(0)
Mvvv 76,116,176
Mvvv 80,116,180
Mvvv 84,116,184
Mvvv 88,116,188
Mvvv 92,116,192
Mvvv 76,120,196
Mvvv 80,120,200
Mvvv 84,120,204
Mvvv 88,120,208
Mvvv 92,120,212
Mvvv 76,124,216
Mvvv 80,124,220
Mvvv 84,124,224
Mvvv 88,124,228
Mvvv 92,124,232
Mvvv 76,128,236
Mvvv 80,128,240
Mvvv 84,128,244
Mvvv 88,128,248
Mvvv 92,128,252
s_nop 7
s_nop 2
v_max3_f32 v10, v76, v77, v78
v_max3_f32 v10, v10, v79, v96
v_max3_f32 v10, v10, v97, v98
v_max_f32 v10, v10, v99
v_mov_b32 v11, v10
s_nop 1
v_permlane16_swap_b32 v10, v11
v_max_f32 v10, v10, v11
v_mov_b32 v11, v10
s_nop 1
v_permlane32_swap_b32 v10, v11
v_max_f32 v10, v10, v11
A_ v10,0x40e00000,v10
v_max_f32 v10, 0, v10
v_exp_f32 v11, -v10
s_nop 0
U_ v46,v46,v11
R_ v12,a0
U_ v12,v12,v11
X_ a0,v12
R_ v13,a1
U_ v13,v13,v11
X_ a1,v13
R_ v12,a2
U_ v12,v12,v11
X_ a2,v12
R_ v13,a3
U_ v13,v13,v11
X_ a3,v13
R_ v12,a20
U_ v12,v12,v11
X_ a20,v12
R_ v13,a21
U_ v13,v13,v11
X_ a21,v13
R_ v12,a22
U_ v12,v12,v11
X_ a22,v12
R_ v13,a23
U_ v13,v13,v11
X_ a23,v13
R_ v12,a40
U_ v12,v12,v11
X_ a40,v12
R_ v13,a41
U_ v13,v13,v11
X_ a41,v13
R_ v12,a42
U_ v12,v12,v11
X_ a42,v12
R_ v13,a43
U_ v13,v13,v11
X_ a43,v13
R_ v12,a60
U_ v12,v12,v11
X_ a60,v12
R_ v13,a61
U_ v13,v13,v11
X_ a61,v13
R_ v12,a62
U_ v12,v12,v11
X_ a62,v12
R_ v13,a63
U_ v13,v13,v11
X_ a63,v13
R_ v12,a80
U_ v12,v12,v11
X_ a80,v12
R_ v13,a81
U_ v13,v13,v11
X_ a81,v13
R_ v12,a82
U_ v12,v12,v11
X_ a82,v12
R_ v13,a83
U_ v13,v13,v11
X_ a83,v13
R_ v12,a100
U_ v12,v12,v11
X_ a100,v12
R_ v13,a101
U_ v13,v13,v11
X_ a101,v13
R_ v12,a102
U_ v12,v12,v11
X_ a102,v12
R_ v13,a103
U_ v13,v13,v11
X_ a103,v13
R_ v12,a120
U_ v12,v12,v11
X_ a120,v12
R_ v13,a121
U_ v13,v13,v11
X_ a121,v13
R_ v12,a122
U_ v12,v12,v11
X_ a122,v12
R_ v13,a123
U_ v13,v13,v11
X_ a123,v13
R_ v12,a140
U_ v12,v12,v11
X_ a140,v12
R_ v13,a141
U_ v13,v13,v11
X_ a141,v13
R_ v12,a142
U_ v12,v12,v11
X_ a142,v12
R_ v13,a143
U_ v13,v13,v11
X_ a143,v13
R_ v12,a160
U_ v12,v12,v11
X_ a160,v12
R_ v13,a161
U_ v13,v13,v11
X_ a161,v13
R_ v12,a162
U_ v12,v12,v11
X_ a162,v12
R_ v13,a163
U_ v13,v13,v11
X_ a163,v13
R_ v12,a180
U_ v12,v12,v11
X_ a180,v12
R_ v13,a181
U_ v13,v13,v11
X_ a181,v13
R_ v12,a182
U_ v12,v12,v11
X_ a182,v12
R_ v13,a183
U_ v13,v13,v11
X_ a183,v13
R_ v12,a200
U_ v12,v12,v11
X_ a200,v12
R_ v13,a201
U_ v13,v13,v11
X_ a201,v13
R_ v12,a202
U_ v12,v12,v11
X_ a202,v12
R_ v13,a203
U_ v13,v13,v11
X_ a203,v13
R_ v12,a220
U_ v12,v12,v11
X_ a220,v12
R_ v13,a221
U_ v13,v13,v11
X_ a221,v13
R_ v12,a222
U_ v12,v12,v11
X_ a222,v12
R_ v13,a223
U_ v13,v13,v11
X_ a223,v13
B_ v56,v56,v10
B_ v57,v57,v10
B_ v58,v58,v10
B_ v59,v59,v10
B_ v76,v76,v10
B_ v77,v77,v10
B_ v78,v78,v10
B_ v79,v79,v10
B_ v96,v96,v10
B_ v97,v97,v10
B_ v98,v98,v10
B_ v99,v99,v10
E_ 76
E_ 77
E_ 78
E_ 79
E_ 96
E_ 97
E_ 98
E_ 99
s_nop 0
C_ 99,98,99
C_ 98,96,97
C_ 96,76,77
C_ 97,78,79
v_max3_f32 v10, v80, v81, v82
v_max3_f32 v10, v10, v83, v100
v_max3_f32 v10, v10, v101, v102
v_max_f32 v10, v10, v103
v_mov_b32 v11, v10
s_nop 1
v_permlane16_swap_b32 v10, v11
v_max_f32 v10, v10, v11
v_mov_b32 v11, v10
s_nop 1
v_permlane32_swap_b32 v10, v11
v_max_f32 v10, v10, v11
A_ v10,0x40e00000,v10
v_max_f32 v10, 0, v10
v_exp_f32 v11, -v10
s_nop 0
U_ v47,v47,v11
R_ v12,a4
U_ v12,v12,v11
X_ a4,v12
R_ v13,a5
U_ v13,v13,v11
X_ a5,v13
R_ v12,a6
U_ v12,v12,v11
X_ a6,v12
R_ v13,a7
U_ v13,v13,v11
X_ a7,v13
R_ v12,a24
U_ v12,v12,v11
X_ a24,v12
R_ v13,a25
U_ v13,v13,v11
X_ a25,v13
R_ v12,a26
U_ v12,v12,v11
X_ a26,v12
R_ v13,a27
U_ v13,v13,v11
X_ a27,v13
R_ v12,a44
U_ v12,v12,v11
X_ a44,v12
R_ v13,a45
U_ v13,v13,v11
X_ a45,v13
R_ v12,a46
U_ v12,v12,v11
X_ a46,v12
R_ v13,a47
U_ v13,v13,v11
X_ a47,v13
R_ v12,a64
U_ v12,v12,v11
X_ a64,v12
R_ v13,a65
U_ v13,v13,v11
X_ a65,v13
R_ v12,a66
U_ v12,v12,v11
X_ a66,v12
R_ v13,a67
U_ v13,v13,v11
X_ a67,v13
R_ v12,a84
U_ v12,v12,v11
X_ a84,v12
R_ v13,a85
U_ v13,v13,v11
X_ a85,v13
R_ v12,a86
U_ v12,v12,v11
X_ a86,v12
R_ v13,a87
U_ v13,v13,v11
X_ a87,v13
R_ v12,a104
U_ v12,v12,v11
X_ a104,v12
R_ v13,a105
U_ v13,v13,v11
X_ a105,v13
R_ v12,a106
U_ v12,v12,v11
X_ a106,v12
R_ v13,a107
U_ v13,v13,v11
X_ a107,v13
R_ v12,a124
U_ v12,v12,v11
X_ a124,v12
R_ v13,a125
U_ v13,v13,v11
X_ a125,v13
R_ v12,a126
U_ v12,v12,v11
X_ a126,v12
R_ v13,a127
U_ v13,v13,v11
X_ a127,v13
R_ v12,a144
U_ v12,v12,v11
X_ a144,v12
R_ v13,a145
U_ v13,v13,v11
X_ a145,v13
R_ v12,a146
U_ v12,v12,v11
X_ a146,v12
R_ v13,a147
U_ v13,v13,v11
X_ a147,v13
R_ v12,a164
U_ v12,v12,v11
X_ a164,v12
R_ v13,a165
U_ v13,v13,v11
X_ a165,v13
R_ v12,a166
U_ v12,v12,v11
X_ a166,v12
R_ v13,a167
U_ v13,v13,v11
X_ a167,v13
R_ v12,a184
U_ v12,v12,v11
X_ a184,v12
R_ v13,a185
U_ v13,v13,v11
X_ a185,v13
R_ v12,a186
U_ v12,v12,v11
X_ a186,v12
R_ v13,a187
U_ v13,v13,v11
X_ a187,v13
R_ v12,a204
U_ v12,v12,v11
X_ a204,v12
R_ v13,a205
U_ v13,v13,v11
X_ a205,v13
R_ v12,a206
U_ v12,v12,v11
X_ a206,v12
R_ v13,a207
U_ v13,v13,v11
X_ a207,v13
R_ v12,a224
U_ v12,v12,v11
X_ a224,v12
R_ v13,a225
U_ v13,v13,v11
X_ a225,v13
R_ v12,a226
U_ v12,v12,v11
X_ a226,v12
R_ v13,a227
U_ v13,v13,v11
X_ a227,v13
B_ v60,v60,v10
B_ v61,v61,v10
B_ v62,v62,v10
B_ v63,v63,v10
B_ v80,v80,v10
B_ v81,v81,v10
B_ v82,v82,v10
B_ v83,v83,v10
B_ v100,v100,v10
B_ v101,v101,v10
B_ v102,v102,v10
B_ v103,v103,v10
E_ 80
E_ 81
E_ 82
E_ 83
E_ 100
E_ 101
E_ 102
E_ 103
s_nop 0
C_ 103,102,103
C_ 102,100,101
C_ 100,80,81
C_ 101,82,83
v_max3_f32 v10, v84, v85, v86
v_max3_f32 v10, v10, v87, v104
v_max3_f32 v10, v10, v105, v106
v_max_f32 v10, v10, v107
v_mov_b32 v11, v10
s_nop 1
v_permlane16_swap_b32 v10, v11
v_max_f32 v10, v10, v11
v_mov_b32 v11, v10
s_nop 1
v_permlane32_swap_b32 v10, v11
v_max_f32 v10, v10, v11
A_ v10,0x40e00000,v10
v_max_f32 v10, 0, v10
v_exp_f32 v11, -v10
s_nop 0
U_ v48,v48,v11
R_ v12,a8
U_ v12,v12,v11
X_ a8,v12
R_ v13,a9
U_ v13,v13,v11
X_ a9,v13
R_ v12,a10
U_ v12,v12,v11
X_ a10,v12
R_ v13,a11
U_ v13,v13,v11
X_ a11,v13
R_ v12,a28
U_ v12,v12,v11
X_ a28,v12
R_ v13,a29
U_ v13,v13,v11
X_ a29,v13
R_ v12,a30
U_ v12,v12,v11
X_ a30,v12
R_ v13,a31
U_ v13,v13,v11
X_ a31,v13
R_ v12,a48
U_ v12,v12,v11
X_ a48,v12
R_ v13,a49
U_ v13,v13,v11
X_ a49,v13
R_ v12,a50
U_ v12,v12,v11
X_ a50,v12
R_ v13,a51
U_ v13,v13,v11
X_ a51,v13
R_ v12,a68
U_ v12,v12,v11
X_ a68,v12
R_ v13,a69
U_ v13,v13,v11
X_ a69,v13
R_ v12,a70
U_ v12,v12,v11
X_ a70,v12
R_ v13,a71
U_ v13,v13,v11
X_ a71,v13
R_ v12,a88
U_ v12,v12,v11
X_ a88,v12
R_ v13,a89
U_ v13,v13,v11
X_ a89,v13
R_ v12,a90
U_ v12,v12,v11
X_ a90,v12
R_ v13,a91
U_ v13,v13,v11
X_ a91,v13
R_ v12,a108
U_ v12,v12,v11
X_ a108,v12
R_ v13,a109
U_ v13,v13,v11
X_ a109,v13
R_ v12,a110
U_ v12,v12,v11
X_ a110,v12
R_ v13,a111
U_ v13,v13,v11
X_ a111,v13
R_ v12,a128
U_ v12,v12,v11
X_ a128,v12
R_ v13,a129
U_ v13,v13,v11
X_ a129,v13
R_ v12,a130
U_ v12,v12,v11
X_ a130,v12
R_ v13,a131
U_ v13,v13,v11
X_ a131,v13
R_ v12,a148
U_ v12,v12,v11
X_ a148,v12
R_ v13,a149
U_ v13,v13,v11
X_ a149,v13
R_ v12,a150
U_ v12,v12,v11
X_ a150,v12
R_ v13,a151
U_ v13,v13,v11
X_ a151,v13
R_ v12,a168
U_ v12,v12,v11
X_ a168,v12
R_ v13,a169
U_ v13,v13,v11
X_ a169,v13
R_ v12,a170
U_ v12,v12,v11
X_ a170,v12
R_ v13,a171
U_ v13,v13,v11
X_ a171,v13
R_ v12,a188
U_ v12,v12,v11
X_ a188,v12
R_ v13,a189
U_ v13,v13,v11
X_ a189,v13
R_ v12,a190
U_ v12,v12,v11
X_ a190,v12
R_ v13,a191
U_ v13,v13,v11
X_ a191,v13
R_ v12,a208
U_ v12,v12,v11
X_ a208,v12
R_ v13,a209
U_ v13,v13,v11
X_ a209,v13
R_ v12,a210
U_ v12,v12,v11
X_ a210,v12
R_ v13,a211
U_ v13,v13,v11
X_ a211,v13
R_ v12,a228
U_ v12,v12,v11
X_ a228,v12
R_ v13,a229
U_ v13,v13,v11
X_ a229,v13
R_ v12,a230
U_ v12,v12,v11
X_ a230,v12
R_ v13,a231
U_ v13,v13,v11
X_ a231,v13
B_ v64,v64,v10
B_ v65,v65,v10
B_ v66,v66,v10
B_ v67,v67,v10
B_ v84,v84,v10
B_ v85,v85,v10
B_ v86,v86,v10
B_ v87,v87,v10
B_ v104,v104,v10
B_ v105,v105,v10
B_ v106,v106,v10
B_ v107,v107,v10
E_ 84
E_ 85
E_ 86
E_ 87
E_ 104
E_ 105
E_ 106
E_ 107
s_nop 0
C_ 107,106,107
C_ 106,104,105
C_ 104,84,85
C_ 105,86,87
v_max3_f32 v10, v88, v89, v90
v_max3_f32 v10, v10, v91, v108
v_max3_f32 v10, v10, v109, v110
v_max_f32 v10, v10, v111
v_mov_b32 v11, v10
s_nop 1
v_permlane16_swap_b32 v10, v11
v_max_f32 v10, v10, v11
v_mov_b32 v11, v10
s_nop 1
v_permlane32_swap_b32 v10, v11
v_max_f32 v10, v10, v11
A_ v10,0x40e00000,v10
v_max_f32 v10, 0, v10
v_exp_f32 v11, -v10
s_nop 0
U_ v49,v49,v11
R_ v12,a12
U_ v12,v12,v11
X_ a12,v12
R_ v13,a13
U_ v13,v13,v11
X_ a13,v13
R_ v12,a14
U_ v12,v12,v11
X_ a14,v12
R_ v13,a15
U_ v13,v13,v11
X_ a15,v13
R_ v12,a32
U_ v12,v12,v11
X_ a32,v12
R_ v13,a33
U_ v13,v13,v11
X_ a33,v13
R_ v12,a34
U_ v12,v12,v11
X_ a34,v12
R_ v13,a35
U_ v13,v13,v11
X_ a35,v13
R_ v12,a52
U_ v12,v12,v11
X_ a52,v12
R_ v13,a53
U_ v13,v13,v11
X_ a53,v13
R_ v12,a54
U_ v12,v12,v11
X_ a54,v12
)A"
R"A(R_ v13,a55
U_ v13,v13,v11
X_ a55,v13
R_ v12,a72
U_ v12,v12,v11
X_ a72,v12
R_ v13,a73
U_ v13,v13,v11
X_ a73,v13
R_ v12,a74
U_ v12,v12,v11
X_ a74,v12
R_ v13,a75
U_ v13,v13,v11
X_ a75,v13
R_ v12,a92
U_ v12,v12,v11
X_ a92,v12
R_ v13,a93
U_ v13,v13,v11
X_ a93,v13
R_ v12,a94
U_ v12,v12,v11
X_ a94,v12
R_ v13,a95
U_ v13,v13,v11
X_ a95,v13
R_ v12,a112
U_ v12,v12,v11
X_ a112,v12
R_ v13,a113
U_ v13,v13,v11
X_ a113,v13
R_ v12,a114
U_ v12,v12,v11
X_ a114,v12
R_ v13,a115
U_ v13,v13,v11
X_ a115,v13
R_ v12,a132
U_ v12,v12,v11
X_ a132,v12
R_ v13,a133
U_ v13,v13,v11
X_ a133,v13
R_ v12,a134
U_ v12,v12,v11
X_ a134,v12
R_ v13,a135
U_ v13,v13,v11
X_ a135,v13
R_ v12,a152
U_ v12,v12,v11
X_ a152,v12
R_ v13,a153
U_ v13,v13,v11
X_ a153,v13
R_ v12,a154
U_ v12,v12,v11
X_ a154,v12
R_ v13,a155
U_ v13,v13,v11
X_ a155,v13
R_ v12,a172
U_ v12,v12,v11
X_ a172,v12
R_ v13,a173
U_ v13,v13,v11
X_ a173,v13
R_ v12,a174
U_ v12,v12,v11
X_ a174,v12
R_ v13,a175
U_ v13,v13,v11
X_ a175,v13
R_ v12,a192
U_ v12,v12,v11
X_ a192,v12
R_ v13,a193
U_ v13,v13,v11
X_ a193,v13
R_ v12,a194
U_ v12,v12,v11
X_ a194,v12
R_ v13,a195
U_ v13,v13,v11
X_ a195,v13
R_ v12,a212
U_ v12,v12,v11
X_ a212,v12
R_ v13,a213
U_ v13,v13,v11
X_ a213,v13
R_ v12,a214
U_ v12,v12,v11
X_ a214,v12
R_ v13,a215
U_ v13,v13,v11
X_ a215,v13
R_ v12,a232
U_ v12,v12,v11
X_ a232,v12
R_ v13,a233
U_ v13,v13,v11
X_ a233,v13
R_ v12,a234
U_ v12,v12,v11
X_ a234,v12
R_ v13,a235
U_ v13,v13,v11
X_ a235,v13
B_ v68,v68,v10
B_ v69,v69,v10
B_ v70,v70,v10
B_ v71,v71,v10
B_ v88,v88,v10
B_ v89,v89,v10
B_ v90,v90,v10
B_ v91,v91,v10
B_ v108,v108,v10
B_ v109,v109,v10
B_ v110,v110,v10
B_ v111,v111,v10
E_ 88
E_ 89
E_ 90
E_ 91
E_ 108
E_ 109
E_ 110
E_ 111
s_nop 0
C_ 111,110,111
C_ 110,108,109
C_ 108,88,89
C_ 109,90,91
v_max3_f32 v10, v92, v93, v94
v_max3_f32 v10, v10, v95, v112
v_max3_f32 v10, v10, v113, v114
v_max_f32 v10, v10, v115
v_mov_b32 v11, v10
s_nop 1
v_permlane16_swap_b32 v10, v11
v_max_f32 v10, v10, v11
v_mov_b32 v11, v10
s_nop 1
v_permlane32_swap_b32 v10, v11
v_max_f32 v10, v10, v11
A_ v10,0x40e00000,v10
v_max_f32 v10, 0, v10
v_exp_f32 v11, -v10
s_nop 0
U_ v50,v50,v11
R_ v12,a16
U_ v12,v12,v11
X_ a16,v12
R_ v13,a17
U_ v13,v13,v11
X_ a17,v13
R_ v12,a18
U_ v12,v12,v11
X_ a18,v12
R_ v13,a19
U_ v13,v13,v11
X_ a19,v13
R_ v12,a36
U_ v12,v12,v11
X_ a36,v12
R_ v13,a37
U_ v13,v13,v11
X_ a37,v13
R_ v12,a38
U_ v12,v12,v11
X_ a38,v12
R_ v13,a39
U_ v13,v13,v11
X_ a39,v13
R_ v12,a56
U_ v12,v12,v11
X_ a56,v12
R_ v13,a57
U_ v13,v13,v11
X_ a57,v13
R_ v12,a58
U_ v12,v12,v11
X_ a58,v12
R_ v13,a59
U_ v13,v13,v11
X_ a59,v13
R_ v12,a76
U_ v12,v12,v11
X_ a76,v12
R_ v13,a77
U_ v13,v13,v11
X_ a77,v13
R_ v12,a78
U_ v12,v12,v11
X_ a78,v12
R_ v13,a79
U_ v13,v13,v11
X_ a79,v13
R_ v12,a96
U_ v12,v12,v11
X_ a96,v12
R_ v13,a97
U_ v13,v13,v11
X_ a97,v13
R_ v12,a98
U_ v12,v12,v11
X_ a98,v12
R_ v13,a99
U_ v13,v13,v11
X_ a99,v13
R_ v12,a116
U_ v12,v12,v11
X_ a116,v12
R_ v13,a117
U_ v13,v13,v11
X_ a117,v13
R_ v12,a118
U_ v12,v12,v11
X_ a118,v12
R_ v13,a119
U_ v13,v13,v11
X_ a119,v13
R_ v12,a136
U_ v12,v12,v11
X_ a136,v12
R_ v13,a137
U_ v13,v13,v11
X_ a137,v13
R_ v12,a138
U_ v12,v12,v11
X_ a138,v12
R_ v13,a139
U_ v13,v13,v11
X_ a139,v13
R_ v12,a156
U_ v12,v12,v11
X_ a156,v12
R_ v13,a157
U_ v13,v13,v11
X_ a157,v13
R_ v12,a158
U_ v12,v12,v11
X_ a158,v12
R_ v13,a159
U_ v13,v13,v11
X_ a159,v13
R_ v12,a176
U_ v12,v12,v11
X_ a176,v12
R_ v13,a177
U_ v13,v13,v11
X_ a177,v13
R_ v12,a178
U_ v12,v12,v11
X_ a178,v12
R_ v13,a179
U_ v13,v13,v11
X_ a179,v13
R_ v12,a196
U_ v12,v12,v11
X_ a196,v12
R_ v13,a197
U_ v13,v13,v11
X_ a197,v13
R_ v12,a198
U_ v12,v12,v11
X_ a198,v12
R_ v13,a199
U_ v13,v13,v11
X_ a199,v13
R_ v12,a216
U_ v12,v12,v11
X_ a216,v12
R_ v13,a217
U_ v13,v13,v11
X_ a217,v13
R_ v12,a218
U_ v12,v12,v11
X_ a218,v12
R_ v13,a219
U_ v13,v13,v11
X_ a219,v13
R_ v12,a236
U_ v12,v12,v11
X_ a236,v12
R_ v13,a237
U_ v13,v13,v11
X_ a237,v13
R_ v12,a238
U_ v12,v12,v11
X_ a238,v12
R_ v13,a239
U_ v13,v13,v11
X_ a239,v13
B_ v72,v72,v10
B_ v73,v73,v10
B_ v74,v74,v10
B_ v75,v75,v10
B_ v92,v92,v10
B_ v93,v93,v10
B_ v94,v94,v10
B_ v95,v95,v10
B_ v112,v112,v10
B_ v113,v113,v10
B_ v114,v114,v10
B_ v115,v115,v10
E_ 92
E_ 93
E_ 94
E_ 95
E_ 112
E_ 113
E_ 114
E_ 115
s_nop 0
C_ 115,114,115
C_ 114,112,113
C_ 112,92,93
C_ 113,94,95
v_mov_b32 v92, 0x3f803f80
v_mov_b32 v93, 0x3f803f80
v_mov_b32 v94, 0x3f803f80
v_mov_b32 v95, 0x3f803f80
s_nop 7
s_lshl_b32 s53, s58, 4
S_ s53,s53,s63
s_cmp_eq_u32 s53, 2
s_cbranch_scc1 .Lstep0_3
s_cmp_eq_u32 s53, 14
s_cbranch_scc1 .Lstep0_15
s_cmp_eq_u32 s53, 18
s_cbranch_scc1 .Lstep1_3
s_cmp_eq_u32 s53, 20
s_cbranch_scc1 .Lstep1_5
s_cmp_eq_u32 s53, 22
s_cbranch_scc1 .Lstep1_7
s_cmp_eq_u32 s53, 24
s_cbranch_scc1 .Lstep1_9
s_cmp_eq_u32 s53, 26
s_cbranch_scc1 .Lstep1_11
s_cmp_eq_u32 s53, 28
s_cbranch_scc1 .Lstep1_13
s_cmp_eq_u32 s53, 30
s_cbranch_scc1 .Lstep1_15
s_cmp_eq_u32 s53, 34
s_cbranch_scc1 .Lstep2_3
s_cmp_eq_u32 s53, 36
s_cbranch_scc1 .Lstep2_5
s_cmp_eq_u32 s53, 38
s_cbranch_scc1 .Lstep2_7
s_cmp_eq_u32 s53, 40
s_cbranch_scc1 .Lstep2_9
s_cmp_eq_u32 s53, 42
s_cbranch_scc1 .Lstep2_11
s_cmp_eq_u32 s53, 44
s_cbranch_scc1 .Lstep2_13
s_cmp_eq_u32 s53, 46
s_cbranch_scc1 .Lstep2_15
s_endpgm
.Lslow1:
W_ 0
s_nop 7
s_nop 7
s_and_b32 s64, s61, 4294959104
s_mul_i32 s53, s63, 12288
S_ s64,s64,s53
R_ v11,a248
v_and_b32 v10, 0xf0, v11
v_mul_u32_u24 v10, 24, v10
v_lshrrev_b32 v11, 8, v11
v_lshl_add_u32 v10, v11, 4, v10
S_ s53,s64,6144
buffer_load_dwordx4 v[96:99], v10, s[32:35], s53 offen offset:0
S_ s53,s64,6144
buffer_load_dwordx4 v[100:103], v10, s[32:35], s53 offen offset:64
S_ s53,s64,6144
buffer_load_dwordx4 v[104:107], v10, s[32:35], s53 offen offset:128
S_ s53,s64,6144
buffer_load_dwordx4 v[108:111], v10, s[32:35], s53 offen offset:192
s_waitcnt vmcnt(0)
Nvvv 116,96,136,56
Nvvv 120,96,140,60
Nvvv 124,96,144,64
Nvvv 128,96,148,68
Nvvv 132,96,152,72
Mvvv 116,100,156
Mvvv 120,100,160
Mvvv 124,100,164
Mvvv 128,100,168
Mvvv 132,100,172
Mvvv 116,104,176
Mvvv 120,104,180
Mvvv 124,104,184
Mvvv 128,104,188
Mvvv 132,104,192
Mvvv 116,108,196
Mvvv 120,108,200
Mvvv 124,108,204
Mvvv 128,108,208
Mvvv 132,108,212
s_nop 7
S_ s53,s64,6144
buffer_load_dwordx4 v[96:99], v10, s[32:35], s53 offen offset:256
S_ s53,s64,6144
buffer_load_dwordx4 v[100:103], v10, s[32:35], s53 offen offset:320
S_ s53,s64,0
buffer_load_dwordx4 v[104:107], v10, s[32:35], s53 offen offset:0
S_ s53,s64,0
buffer_load_dwordx4 v[108:111], v10, s[32:35], s53 offen offset:64
s_waitcnt vmcnt(0)
Mvvv 116,96,216
Mvvv 120,96,220
Mvvv 124,96,224
Mvvv 128,96,228
Mvvv 132,96,232
Mvvv 116,100,236
Mvvv 120,100,240
Mvvv 124,100,244
Mvvv 128,100,248
Mvvv 132,100,252
Nvvv 76,104,136,56
Nvvv 80,104,140,60
Nvvv 84,104,144,64
Nvvv 88,104,148,68
Nvvv 92,104,152,72
Mvvv 76,108,156
Mvvv 80,108,160
Mvvv 84,108,164
Mvvv 88,108,168
Mvvv 92,108,172
s_nop 7
S_ s53,s64,0
buffer_load_dwordx4 v[96:99], v10, s[32:35], s53 offen offset:128
S_ s53,s64,0
buffer_load_dwordx4 v[100:103], v10, s[32:35], s53 offen offset:192
S_ s53,s64,0
buffer_load_dwordx4 v[104:107], v10, s[32:35], s53 offen offset:256
S_ s53,s64,0
buffer_load_dwordx4 v[108:111], v10, s[32:35], s53 offen offset:320
s_waitcnt vmcnt(0)
Mvvv 76,96,176
Mvvv 80,96,180
Mvvv 84,96,184
Mvvv 88,96,188
Mvvv 92,96,192
Mvvv 76,100,196
Mvvv 80,100,200
Mvvv 84,100,204
Mvvv 88,100,208
Mvvv 92,100,212
Mvvv 76,104,216
Mvvv 80,104,220
Mvvv 84,104,224
Mvvv 88,104,228
Mvvv 92,104,232
Mvvv 76,108,236
Mvvv 80,108,240
Mvvv 84,108,244
Mvvv 88,108,248
Mvvv 92,108,252
s_nop 7
s_nop 2
v_max3_f32 v10, v76, v77, v78
v_max3_f32 v10, v10, v79, v116
v_max3_f32 v10, v10, v117, v118
v_max_f32 v10, v10, v119
v_mov_b32 v11, v10
s_nop 1
v_permlane16_swap_b32 v10, v11
v_max_f32 v10, v10, v11
v_mov_b32 v11, v10
s_nop 1
v_permlane32_swap_b32 v10, v11
v_max_f32 v10, v10, v11
A_ v10,0x40e00000,v10
v_max_f32 v10, 0, v10
v_exp_f32 v11, -v10
s_nop 0
U_ v46,v46,v11
R_ v12,a0
U_ v12,v12,v11
X_ a0,v12
R_ v13,a1
U_ v13,v13,v11
X_ a1,v13
R_ v12,a2
U_ v12,v12,v11
X_ a2,v12
R_ v13,a3
U_ v13,v13,v11
X_ a3,v13
R_ v12,a20
U_ v12,v12,v11
X_ a20,v12
R_ v13,a21
U_ v13,v13,v11
X_ a21,v13
R_ v12,a22
U_ v12,v12,v11
X_ a22,v12
R_ v13,a23
U_ v13,v13,v11
X_ a23,v13
R_ v12,a40
U_ v12,v12,v11
X_ a40,v12
R_ v13,a41
U_ v13,v13,v11
X_ a41,v13
R_ v12,a42
U_ v12,v12,v11
X_ a42,v12
R_ v13,a43
U_ v13,v13,v11
X_ a43,v13
R_ v12,a60
U_ v12,v12,v11
X_ a60,v12
R_ v13,a61
U_ v13,v13,v11
X_ a61,v13
R_ v12,a62
U_ v12,v12,v11
X_ a62,v12
R_ v13,a63
U_ v13,v13,v11
X_ a63,v13
R_ v12,a80
U_ v12,v12,v11
X_ a80,v12
R_ v13,a81
U_ v13,v13,v11
X_ a81,v13
R_ v12,a82
U_ v12,v12,v11
X_ a82,v12
R_ v13,a83
U_ v13,v13,v11
X_ a83,v13
R_ v12,a100
U_ v12,v12,v11
X_ a100,v12
R_ v13,a101
U_ v13,v13,v11
X_ a101,v13
R_ v12,a102
U_ v12,v12,v11
X_ a102,v12
R_ v13,a103
U_ v13,v13,v11
X_ a103,v13
R_ v12,a120
U_ v12,v12,v11
X_ a120,v12
R_ v13,a121
U_ v13,v13,v11
X_ a121,v13
R_ v12,a122
U_ v12,v12,v11
X_ a122,v12
R_ v13,a123
U_ v13,v13,v11
X_ a123,v13
R_ v12,a140
U_ v12,v12,v11
X_ a140,v12
R_ v13,a141
U_ v13,v13,v11
X_ a141,v13
R_ v12,a142
U_ v12,v12,v11
X_ a142,v12
R_ v13,a143
U_ v13,v13,v11
X_ a143,v13
R_ v12,a160
U_ v12,v12,v11
X_ a160,v12
R_ v13,a161
U_ v13,v13,v11
X_ a161,v13
R_ v12,a162
U_ v12,v12,v11
X_ a162,v12
R_ v13,a163
U_ v13,v13,v11
X_ a163,v13
R_ v12,a180
U_ v12,v12,v11
X_ a180,v12
R_ v13,a181
U_ v13,v13,v11
X_ a181,v13
R_ v12,a182
U_ v12,v12,v11
X_ a182,v12
R_ v13,a183
U_ v13,v13,v11
X_ a183,v13
R_ v12,a200
U_ v12,v12,v11
X_ a200,v12
R_ v13,a201
U_ v13,v13,v11
X_ a201,v13
R_ v12,a202
U_ v12,v12,v11
X_ a202,v12
R_ v13,a203
U_ v13,v13,v11
X_ a203,v13
R_ v12,a220
U_ v12,v12,v11
X_ a220,v12
R_ v13,a221
U_ v13,v13,v11
X_ a221,v13
R_ v12,a222
U_ v12,v12,v11
X_ a222,v12
R_ v13,a223
U_ v13,v13,v11
X_ a223,v13
B_ v56,v56,v10
B_ v57,v57,v10
B_ v58,v58,v10
B_ v59,v59,v10
B_ v76,v76,v10
B_ v77,v77,v10
B_ v78,v78,v10
B_ v79,v79,v10
B_ v116,v116,v10
B_ v117,v117,v10
B_ v118,v118,v10
B_ v119,v119,v10
E_ 76
E_ 77
E_ 78
E_ 79
E_ 116
E_ 117
E_ 118
E_ 119
s_nop 0
C_ 119,118,119
C_ 118,116,117
C_ 116,76,77
C_ 117,78,79
v_max3_f32 v10, v80, v81, v82
v_max3_f32 v10, v10, v83, v120
v_max3_f32 v10, v10, v121, v122
v_max_f32 v10, v10, v123
v_mov_b32 v11, v10
s_nop 1
v_permlane16_swap_b32 v10, v11
v_max_f32 v10, v10, v11
v_mov_b32 v11, v10
s_nop 1
v_permlane32_swap_b32 v10, v11
v_max_f32 v10, v10, v11
A_ v10,0x40e00000,v10
v_max_f32 v10, 0, v10
v_exp_f32 v11, -v10
s_nop 0
U_ v47,v47,v11
R_ v12,a4
U_ v12,v12,v11
X_ a4,v12
R_ v13,a5
U_ v13,v13,v11
X_ a5,v13
R_ v12,a6
U_ v12,v12,v11
X_ a6,v12
R_ v13,a7
U_ v13,v13,v11
X_ a7,v13
R_ v12,a24
U_ v12,v12,v11
X_ a24,v12
R_ v13,a25
U_ v13,v13,v11
X_ a25,v13
R_ v12,a26
U_ v12,v12,v11
X_ a26,v12
R_ v13,a27
U_ v13,v13,v11
X_ a27,v13
R_ v12,a44
U_ v12,v12,v11
X_ a44,v12
R_ v13,a45
U_ v13,v13,v11
X_ a45,v13
R_ v12,a46
U_ v12,v12,v11
X_ a46,v12
R_ v13,a47
U_ v13,v13,v11
X_ a47,v13
R_ v12,a64
U_ v12,v12,v11
X_ a64,v12
R_ v13,a65
U_ v13,v13,v11
X_ a65,v13
R_ v12,a66
U_ v12,v12,v11
X_ a66,v12
R_ v13,a67
U_ v13,v13,v11
X_ a67,v13
R_ v12,a84
U_ v12,v12,v11
X_ a84,v12
R_ v13,a85
U_ v13,v13,v11
X_ a85,v13
R_ v12,a86
U_ v12,v12,v11
X_ a86,v12
R_ v13,a87
U_ v13,v13,v11
X_ a87,v13
R_ v12,a104
U_ v12,v12,v11
X_ a104,v12
R_ v13,a105
U_ v13,v13,v11
X_ a105,v13
R_ v12,a106
U_ v12,v12,v11
X_ a106,v12
R_ v13,a107
U_ v13,v13,v11
X_ a107,v13
R_ v12,a124
U_ v12,v12,v11
X_ a124,v12
R_ v13,a125
U_ v13,v13,v11
X_ a125,v13
R_ v12,a126
U_ v12,v12,v11
X_ a126,v12
R_ v13,a127
U_ v13,v13,v11
X_ a127,v13
R_ v12,a144
U_ v12,v12,v11
X_ a144,v12
R_ v13,a145
U_ v13,v13,v11
X_ a145,v13
R_ v12,a146
U_ v12,v12,v11
X_ a146,v12
R_ v13,a147
U_ v13,v13,v11
X_ a147,v13
R_ v12,a164
U_ v12,v12,v11
X_ a164,v12
R_ v13,a165
U_ v13,v13,v11
X_ a165,v13
R_ v12,a166
U_ v12,v12,v11
X_ a166,v12
R_ v13,a167
U_ v13,v13,v11
X_ a167,v13
R_ v12,a184
U_ v12,v12,v11
X_ a184,v12
R_ v13,a185
U_ v13,v13,v11
X_ a185,v13
R_ v12,a186
U_ v12,v12,v11
X_ a186,v12
R_ v13,a187
U_ v13,v13,v11
X_ a187,v13
R_ v12,a204
U_ v12,v12,v11
X_ a204,v12
R_ v13,a205
U_ v13,v13,v11
X_ a205,v13
R_ v12,a206
U_ v12,v12,v11
X_ a206,v12
R_ v13,a207
U_ v13,v13,v11
X_ a207,v13
R_ v12,a224
U_ v12,v12,v11
X_ a224,v12
R_ v13,a225
U_ v13,v13,v11
X_ a225,v13
R_ v12,a226
U_ v12,v12,v11
X_ a226,v12
R_ v13,a227
U_ v13,v13,v11
X_ a227,v13
B_ v60,v60,v10
B_ v61,v61,v10
B_ v62,v62,v10
B_ v63,v63,v10
B_ v80,v80,v10
B_ v81,v81,v10
B_ v82,v82,v10
B_ v83,v83,v10
B_ v120,v120,v10
B_ v121,v121,v10
B_ v122,v122,v10
B_ v123,v123,v10
E_ 80
E_ 81
E_ 82
E_ 83
E_ 120
E_ 121
E_ 122
E_ 123
s_nop 0
C_ 123,122,123
C_ 122,120,121
C_ 120,80,81
C_ 121,82,83
v_max3_f32 v10, v84, v85, v86
v_max3_f32 v10, v10, v87, v124
v_max3_f32 v10, v10, v125, v126
v_max_f32 v10, v10, v127
v_mov_b32 v11, v10
s_nop 1
v_permlane16_swap_b32 v10, v11
v_max_f32 v10, v10, v11
v_mov_b32 v11, v10
s_nop 1
v_permlane32_swap_b32 v10, v11
v_max_f32 v10, v10, v11
A_ v10,0x40e00000,v10
v_max_f32 v10, 0, v10
v_exp_f32 v11, -v10
s_nop 0
U_ v48,v48,v11
R_ v12,a8
U_ v12,v12,v11
X_ a8,v12
R_ v13,a9
U_ v13,v13,v11
X_ a9,v13
R_ v12,a10
U_ v12,v12,v11
X_ a10,v12
R_ v13,a11
U_ v13,v13,v11
X_ a11,v13
R_ v12,a28
U_ v12,v12,v11
X_ a28,v12
R_ v13,a29
U_ v13,v13,v11
X_ a29,v13
R_ v12,a30
U_ v12,v12,v11
X_ a30,v12
R_ v13,a31
U_ v13,v13,v11
X_ a31,v13
R_ v12,a48
U_ v12,v12,v11
X_ a48,v12
R_ v13,a49
U_ v13,v13,v11
X_ a49,v13
R_ v12,a50
U_ v12,v12,v11
X_ a50,v12
R_ v13,a51
U_ v13,v13,v11
X_ a51,v13
R_ v12,a68
U_ v12,v12,v11
X_ a68,v12
R_ v13,a69
U_ v13,v13,v11
X_ a69,v13
R_ v12,a70
U_ v12,v12,v11
X_ a70,v12
R_ v13,a71
U_ v13,v13,v11
X_ a71,v13
R_ v12,a88
U_ v12,v12,v11
X_ a88,v12
R_ v13,a89
U_ v13,v13,v11
X_ a89,v13
R_ v12,a90
U_ v12,v12,v11
X_ a90,v12
R_ v13,a91
U_ v13,v13,v11
X_ a91,v13
R_ v12,a108
U_ v12,v12,v11
X_ a108,v12
R_ v13,a109
U_ v13,v13,v11
X_ a109,v13
R_ v12,a110
U_ v12,v12,v11
X_ a110,v12
R_ v13,a111
U_ v13,v13,v11
X_ a111,v13
R_ v12,a128
U_ v12,v12,v11
X_ a128,v12
R_ v13,a129
U_ v13,v13,v11
X_ a129,v13
R_ v12,a130
U_ v12,v12,v11
X_ a130,v12
R_ v13,a131
U_ v13,v13,v11
X_ a131,v13
R_ v12,a148
U_ v12,v12,v11
X_ a148,v12
R_ v13,a149
U_ v13,v13,v11
X_ a149,v13
R_ v12,a150
U_ v12,v12,v11
X_ a150,v12
R_ v13,a151
U_ v13,v13,v11
X_ a151,v13
R_ v12,a168
U_ v12,v12,v11
X_ a168,v12
R_ v13,a169
U_ v13,v13,v11
X_ a169,v13
R_ v12,a170
U_ v12,v12,v11
X_ a170,v12
R_ v13,a171
U_ v13,v13,v11
X_ a171,v13
R_ v12,a188
U_ v12,v12,v11
X_ a188,v12
R_ v13,a189
U_ v13,v13,v11
X_ a189,v13
R_ v12,a190
U_ v12,v12,v11
X_ a190,v12
R_ v13,a191
U_ v13,v13,v11
X_ a191,v13
R_ v12,a208
U_ v12,v12,v11
X_ a208,v12
R_ v13,a209
U_ v13,v13,v11
X_ a209,v13
R_ v12,a210
U_ v12,v12,v11
X_ a210,v12
R_ v13,a211
U_ v13,v13,v11
X_ a211,v13
R_ v12,a228
U_ v12,v12,v11
X_ a228,v12
R_ v13,a229
U_ v13,v13,v11
X_ a229,v13
R_ v12,a230
U_ v12,v12,v11
X_ a230,v12
R_ v13,a231
U_ v13,v13,v11
X_ a231,v13
B_ v64,v64,v10
B_ v65,v65,v10
B_ v66,v66,v10
B_ v67,v67,v10
B_ v84,v84,v10
B_ v85,v85,v10
B_ v86,v86,v10
B_ v87,v87,v10
B_ v124,v124,v10
B_ v125,v125,v10
B_ v126,v126,v10
B_ v127,v127,v10
E_ 84
E_ 85
E_ 86
E_ 87
E_ 124
E_ 125
E_ 126
E_ 127
s_nop 0
C_ 127,126,127
C_ 126,124,125
C_ 124,84,85
C_ 125,86,87
v_max3_f32 v10, v88, v89, v90
v_max3_f32 v10, v10, v91, v128
v_max3_f32 v10, v10, v129, v130
v_max_f32 v10, v10, v131
v_mov_b32 v11, v10
s_nop 1
v_permlane16_swap_b32 v10, v11
v_max_f32 v10, v10, v11
v_mov_b32 v11, v10
s_nop 1
v_permlane32_swap_b32 v10, v11
v_max_f32 v10, v10, v11
A_ v10,0x40e00000,v10
v_max_f32 v10, 0, v10
v_exp_f32 v11, -v10
s_nop 0
U_ v49,v49,v11
R_ v12,a12
U_ v12,v12,v11
X_ a12,v12
R_ v13,a13
U_ v13,v13,v11
X_ a13,v13
R_ v12,a14
U_ v12,v12,v11
X_ a14,v12
R_ v13,a15
U_ v13,v13,v11
X_ a15,v13
R_ v12,a32
U_ v12,v12,v11
X_ a32,v12
R_ v13,a33
U_ v13,v13,v11
X_ a33,v13
R_ v12,a34
U_ v12,v12,v11
X_ a34,v12
R_ v13,a35
U_ v13,v13,v11
X_ a35,v13
R_ v12,a52
U_ v12,v12,v11
X_ a52,v12
R_ v13,a53
U_ v13,v13,v11
X_ a53,v13
R_ v12,a54
U_ v12,v12,v11
X_ a54,v12
R_ v13,a55
U_ v13,v13,v11
X_ a55,v13
R_ v12,a72
U_ v12,v12,v11
X_ a72,v12
R_ v13,a73
U_ v13,v13,v11
X_ a73,v13
R_ v12,a74
U_ v12,v12,v11
X_ a74,v12
R_ v13,a75
U_ v13,v13,v11
X_ a75,v13
R_ v12,a92
U_ v12,v12,v11
X_ a92,v12
R_ v13,a93
U_ v13,v13,v11
X_ a93,v13
R_ v12,a94
U_ v12,v12,v11
X_ a94,v12
R_ v13,a95
U_ v13,v13,v11
X_ a95,v13
R_ v12,a112
U_ v12,v12,v11
X_ a112,v12
R_ v13,a113
U_ v13,v13,v11
X_ a113,v13
R_ v12,a114
U_ v12,v12,v11
X_ a114,v12
R_ v13,a115
U_ v13,v13,v11
X_ a115,v13
R_ v12,a132
U_ v12,v12,v11
X_ a132,v12
R_ v13,a133
U_ v13,v13,v11
X_ a133,v13
R_ v12,a134
U_ v12,v12,v11
X_ a134,v12
R_ v13,a135
U_ v13,v13,v11
X_ a135,v13
R_ v12,a152
U_ v12,v12,v11
X_ a152,v12
R_ v13,a153
U_ v13,v13,v11
X_ a153,v13
R_ v12,a154
U_ v12,v12,v11
X_ a154,v12
R_ v13,a155
U_ v13,v13,v11
X_ a155,v13
R_ v12,a172
U_ v12,v12,v11
X_ a172,v12
R_ v13,a173
U_ v13,v13,v11
X_ a173,v13
R_ v12,a174
U_ v12,v12,v11
X_ a174,v12
R_ v13,a175
U_ v13,v13,v11
X_ a175,v13
R_ v12,a192
U_ v12,v12,v11
X_ a192,v12
R_ v13,a193
U_ v13,v13,v11
X_ a193,v13
R_ v12,a194
U_ v12,v12,v11
X_ a194,v12
R_ v13,a195
U_ v13,v13,v11
X_ a195,v13
R_ v12,a212
U_ v12,v12,v11
X_ a212,v12
R_ v13,a213
U_ v13,v13,v11
X_ a213,v13
R_ v12,a214
U_ v12,v12,v11
X_ a214,v12
R_ v13,a215
U_ v13,v13,v11
X_ a215,v13
R_ v12,a232
U_ v12,v12,v11
X_ a232,v12
R_ v13,a233
U_ v13,v13,v11
X_ a233,v13
R_ v12,a234
U_ v12,v12,v11
X_ a234,v12
R_ v13,a235
U_ v13,v13,v11
X_ a235,v13
B_ v68,v68,v10
B_ v69,v69,v10
B_ v70,v70,v10
B_ v71,v71,v10
B_ v88,v88,v10
B_ v89,v89,v10
B_ v90,v90,v10
B_ v91,v91,v10
B_ v128,v128,v10
B_ v129,v129,v10
B_ v130,v130,v10
B_ v131,v131,v10
E_ 88
E_ 89
E_ 90
E_ 91
E_ 128
E_ 129
E_ 130
E_ 131
s_nop 0
C_ 131,130,131
C_ 130,128,129
C_ 128,88,89
C_ 129,90,91
v_max3_f32 v10, v92, v93, v94
v_max3_f32 v10, v10, v95, v132
v_max3_f32 v10, v10, v133, v134
v_max_f32 v10, v10, v135
v_mov_b32 v11, v10
s_nop 1
v_permlane16_swap_b32 v10, v11
v_max_f32 v10, v10, v11
v_mov_b32 v11, v10
s_nop 1
v_permlane32_swap_b32 v10, v11
v_max_f32 v10, v10, v11
A_ v10,0x40e00000,v10
v_max_f32 v10, 0, v10
v_exp_f32 v11, -v10
s_nop 0
U_ v50,v50,v11
R_ v12,a16
U_ v12,v12,v11
X_ a16,v12
R_ v13,a17
U_ v13,v13,v11
X_ a17,v13
R_ v12,a18
U_ v12,v12,v11
X_ a18,v12
R_ v13,a19
U_ v13,v13,v11
X_ a19,v13
R_ v12,a36
U_ v12,v12,v11
X_ a36,v12
R_ v13,a37
U_ v13,v13,v11
X_ a37,v13
R_ v12,a38
U_ v12,v12,v11
X_ a38,v12
R_ v13,a39
U_ v13,v13,v11
X_ a39,v13
R_ v12,a56
U_ v12,v12,v11
X_ a56,v12
R_ v13,a57
U_ v13,v13,v11
X_ a57,v13
R_ v12,a58
U_ v12,v12,v11
X_ a58,v12
R_ v13,a59
U_ v13,v13,v11
X_ a59,v13
R_ v12,a76
U_ v12,v12,v11
X_ a76,v12
R_ v13,a77
U_ v13,v13,v11
X_ a77,v13
R_ v12,a78
U_ v12,v12,v11
X_ a78,v12
R_ v13,a79
U_ v13,v13,v11
X_ a79,v13
R_ v12,a96
U_ v12,v12,v11
X_ a96,v12
R_ v13,a97
U_ v13,v13,v11
X_ a97,v13
R_ v12,a98
U_ v12,v12,v11
X_ a98,v12
R_ v13,a99
U_ v13,v13,v11
X_ a99,v13
R_ v12,a116
U_ v12,v12,v11
X_ a116,v12
R_ v13,a117
U_ v13,v13,v11
X_ a117,v13
R_ v12,a118
U_ v12,v12,v11
X_ a118,v12
R_ v13,a119
U_ v13,v13,v11
X_ a119,v13
R_ v12,a136
U_ v12,v12,v11
X_ a136,v12
R_ v13,a137
U_ v13,v13,v11
X_ a137,v13
R_ v12,a138
U_ v12,v12,v11
X_ a138,v12
R_ v13,a139
U_ v13,v13,v11
X_ a139,v13
R_ v12,a156
U_ v12,v12,v11
X_ a156,v12
R_ v13,a157
U_ v13,v13,v11
X_ a157,v13
R_ v12,a158
U_ v12,v12,v11
X_ a158,v12
R_ v13,a159
U_ v13,v13,v11
X_ a159,v13
R_ v12,a176
U_ v12,v12,v11
X_ a176,v12
R_ v13,a177
U_ v13,v13,v11
X_ a177,v13
R_ v12,a178
U_ v12,v12,v11
X_ a178,v12
R_ v13,a179
U_ v13,v13,v11
X_ a179,v13
R_ v12,a196
U_ v12,v12,v11
X_ a196,v12
R_ v13,a197
U_ v13,v13,v11
X_ a197,v13
R_ v12,a198
U_ v12,v12,v11
X_ a198,v12
R_ v13,a199
U_ v13,v13,v11
X_ a199,v13
R_ v12,a216
U_ v12,v12,v11
X_ a216,v12
R_ v13,a217
U_ v13,v13,v11
X_ a217,v13
R_ v12,a218
U_ v12,v12,v11
X_ a218,v12
R_ v13,a219
U_ v13,v13,v11
X_ a219,v13
R_ v12,a236
U_ v12,v12,v11
X_ a236,v12
R_ v13,a237
U_ v13,v13,v11
X_ a237,v13
R_ v12,a238
U_ v12,v12,v11
X_ a238,v12
R_ v13,a239
U_ v13,v13,v11
X_ a239,v13
B_ v72,v72,v10
B_ v73,v73,v10
B_ v74,v74,v10
B_ v75,v75,v10
B_ v92,v92,v10
B_ v93,v93,v10
B_ v94,v94,v10
B_ v95,v95,v10
B_ v132,v132,v10
B_ v133,v133,v10
B_ v134,v134,v10
B_ v135,v135,v10
E_ 92
E_ 93
E_ 94
E_ 95
E_ 132
E_ 133
E_ 134
E_ 135
s_nop 0
C_ 135,134,135
C_ 134,132,133
C_ 132,92,93
C_ 133,94,95
v_mov_b32 v92, 0x3f803f80
v_mov_b32 v93, 0x3f803f80
v_mov_b32 v94, 0x3f803f80
v_mov_b32 v95, 0x3f803f80
s_nop 7
s_lshl_b32 s53, s58, 4
S_ s53,s53,s63
s_cmp_eq_u32 s53, 1
s_cbranch_scc1 .Lstep0_2
s_cmp_eq_u32 s53, 13
s_cbranch_scc1 .Lstep0_14
s_cmp_eq_u32 s53, 15
s_cbranch_scc1 .Lstep0_16
s_cmp_eq_u32 s53, 17
s_cbranch_scc1 .Lstep1_2
s_cmp_eq_u32 s53, 19
s_cbranch_scc1 .Lstep1_4
s_cmp_eq_u32 s53, 21
s_cbranch_scc1 .Lstep1_6
s_cmp_eq_u32 s53, 23
s_cbranch_scc1 .Lstep1_8
s_cmp_eq_u32 s53, 25
s_cbranch_scc1 .Lstep1_10
s_cmp_eq_u32 s53, 27
s_cbranch_scc1 .Lstep1_12
s_cmp_eq_u32 s53, 29
s_cbranch_scc1 .Lstep1_14
s_cmp_eq_u32 s53, 31
s_cbranch_scc1 .Lstep1_16
s_cmp_eq_u32 s53, 33
s_cbranch_scc1 .Lstep2_2
s_cmp_eq_u32 s53, 35
s_cbranch_scc1 .Lstep2_4
s_cmp_eq_u32 s53, 37
s_cbranch_scc1 .Lstep2_6
s_cmp_eq_u32 s53, 39
s_cbranch_scc1 .Lstep2_8
s_cmp_eq_u32 s53, 41
s_cbranch_scc1 .Lstep2_10
s_cmp_eq_u32 s53, 43
s_cbranch_scc1 .Lstep2_12
s_cmp_eq_u32 s53, 45
s_cbranch_scc1 .Lstep2_14
s_cmp_eq_u32 s53, 47
s_cbranch_scc1 .Lstep2_16
s_endpgm
.Lxs2m:
s_sleep 5
s_branch .Lxs2m_back
.Lxs2e:
s_sleep 5
s_branch .Lxs2e_back
.Lxs3m:
s_sleep 5
s_branch .Lxs3m_back
.Lxs3e:
s_sleep 5
s_branch .Lxs3e_back
.Lxs14m:
s_sleep 7
s_branch .Lxs14m_back
.Lxs14e:
s_sleep 7
s_branch .Lxs14e_back
.Lxs15m:
s_sleep 7
s_branch .Lxs15m_back
.Lxs15e:
s_sleep 7
s_branch .Lxs15e_back
.Lpace1:
S_ s12,s12,s13
.Lpace1_l:
s_memrealtime s[100:101]
W_ 0
s_sub_u32 s100, s100, s98
s_cmp_ge_u32 s100, s12
s_cbranch_scc1 .Lpace1_back
s_sleep 2
s_branch .Lpace1_l
.Lpace3:
S_ s12,s12,s13
.Lpace3_l:
s_memrealtime s[100:101]
W_ 0
s_sub_u32 s100, s100, s98
s_cmp_ge_u32 s100, s12
s_cbranch_scc1 .Lpace3_back
s_sleep 2
s_branch .Lpace3_l
.Lpace5:
S_ s12,s12,s13
.Lpace5_l:
s_memrealtime s[100:101]
W_ 0
s_sub_u32 s100, s100, s98
s_cmp_ge_u32 s100, s12
s_cbranch_scc1 .Lpace5_back
s_sleep 2
s_branch .Lpace5_l
.Lpace7:
S_ s12,s12,s13
.Lpace7_l:
s_memrealtime s[100:101]
W_ 0
s_sub_u32 s100, s100, s98
s_cmp_ge_u32 s100, s12
s_cbranch_scc1 .Lpace7_back
s_sleep 2
s_branch .Lpace7_l
.Lpace9:
S_ s12,s12,s13
.Lpace9_l:
s_memrealtime s[100:101]
W_ 0
s_sub_u32 s100, s100, s98
s_cmp_ge_u32 s100, s12
s_cbranch_scc1 .Lpace9_back
s_sleep 2
s_branch .Lpace9_l
.Lpace11:
S_ s12,s12,s13
.Lpace11_l:
s_memrealtime s[100:101]
W_ 0
s_sub_u32 s100, s100, s98
s_cmp_ge_u32 s100, s12
s_cbranch_scc1 .Lpace11_back
s_sleep 2
s_branch .Lpace11_l
.Lpace13:
S_ s12,s12,s13
.Lpace13_l:
s_memrealtime s[100:101]
W_ 0
s_sub_u32 s100, s100, s98
s_cmp_ge_u32 s100, s12
s_cbranch_scc1 .Lpace13_back
s_sleep 2
s_branch .Lpace13_l
.Lpace15:
S_ s12,s12,s13
.Lpace15_l:
s_memrealtime s[100:101]
W_ 0
s_sub_u32 s100, s100, s98
s_cmp_ge_u32 s100, s12
s_cbranch_scc1 .Lpace15_back
s_sleep 2
s_branch .Lpace15_l
.Lpace17:
S_ s12,s12,s13
.Lpace17_l:
s_memrealtime s[100:101]
W_ 0
s_sub_u32 s100, s100, s98
s_cmp_ge_u32 s100, s12
s_cbranch_scc1 .Lpace17_back
s_sleep 2
s_branch .Lpace17_l
.Lpace19:
S_ s12,s12,s13
.Lpace19_l:
s_memrealtime s[100:101]
W_ 0
s_sub_u32 s100, s100, s98
s_cmp_ge_u32 s100, s12
s_cbranch_scc1 .Lpace19_back
s_sleep 2
s_branch .Lpace19_l
.Lpace21:
S_ s12,s12,s13
.Lpace21_l:
s_memrealtime s[100:101]
W_ 0
s_sub_u32 s100, s100, s98
s_cmp_ge_u32 s100, s12
s_cbranch_scc1 .Lpace21_back
s_sleep 2
s_branch .Lpace21_l
.Lpace23:
S_ s12,s12,s13
.Lpace23_l:
s_memrealtime s[100:101]
W_ 0
s_sub_u32 s100, s100, s98
s_cmp_ge_u32 s100, s12
s_cbranch_scc1 .Lpace23_back
s_sleep 2
s_branch .Lpace23_l
.Lpace25:
S_ s12,s12,s13
.Lpace25_l:
s_memrealtime s[100:101]
W_ 0
s_sub_u32 s100, s100, s98
s_cmp_ge_u32 s100, s12
s_cbranch_scc1 .Lpace25_back
s_sleep 2
s_branch .Lpace25_l
.Lpace27:
S_ s12,s12,s13
.Lpace27_l:
s_memrealtime s[100:101]
W_ 0
s_sub_u32 s100, s100, s98
s_cmp_ge_u32 s100, s12
s_cbranch_scc1 .Lpace27_back
s_sleep 2
s_branch .Lpace27_l
.Lpace29:
S_ s12,s12,s13
.Lpace29_l:
s_memrealtime s[100:101]
W_ 0
s_sub_u32 s100, s100, s98
s_cmp_ge_u32 s100, s12
s_cbranch_scc1 .Lpace29_back
s_sleep 2
s_branch .Lpace29_l
.Lhsd0_0:
s_bitcmp1_b32 s67, 13
s_cbranch_scc1 .Lhs_a0
s_bitcmp1_b32 s67, 11
s_cbranch_scc1 .Lhs_c0
s_branch .Lhs_d0
.Lhs_a0:
s_nop 4
s_waitcnt vmcnt(12)
R_ v10,a251
buffer_store_dword a255, v10, s[92:95], 0 offen offset:128 sc1
s_branch .Lhsd0_0_back
.Lhs_a1:
s_nop 4
R_ v11,a248
v_lshrrev_b32 v11, 2, v11
buffer_load_dword a254, v11, s[92:95], s96 offen offset:128 sc1
s_branch .Lhs_a1_back
.Lhs_a2:
s_nop 4
s_waitcnt vmcnt(12)
R_ v10,a251
buffer_store_dword a255, v10, s[92:95], 0 offen offset:128 sc1
s_branch .Lhs_a2_back
.Lhs_b1:
s_nop 4
s_waitcnt vmcnt(12)
R_ v11,a254
v_cmp_eq_u32 vcc, s62, v11
R_ v10,a251
v_mov_b32 v11, 0x7fff0000
v_cndmask_b32 v10, v11, v10, vcc
R_ v11,a253
v_add_u32 v11, 1, v11
buffer_store_dword v11, v10, s[92:95], 0 offen offset:256 sc1
s_and_b32 s53, vcc_lo, 255
s_cmp_eq_u32 s53, 0xFF
s_cselect_b32 s97, 1, 0
s_branch .Lhs_b1_back
.Lhs_c0:
s_nop 4
R_ v11,a248
v_lshrrev_b32 v11, 2, v11
buffer_load_dword a254, v11, s[92:95], s96 offen offset:128 sc1
s_branch .Lhsd0_0_back
.Lhs_c1:
s_nop 4
R_ v11,a248
v_lshrrev_b32 v11, 2, v11
buffer_load_dword a254, v11, s[92:95], s96 offen offset:256 sc1
s_branch .Lhs_c1_back
.Lhs_c2:
s_nop 4
R_ v11,a248
v_lshrrev_b32 v11, 2, v11
buffer_load_dword a254, v11, s[92:95], s96 offen offset:128 sc1
s_branch .Lhs_c2_back
.Lhs_d0:
s_nop 4
s_waitcnt vmcnt(12)
R_ v11,a254
v_cmp_eq_u32 vcc, s62, v11
R_ v10,a251
v_mov_b32 v11, 0x7fff0000
v_cndmask_b32 v10, v11, v10, vcc
R_ v11,a253
v_add_u32 v11, 1, v11
buffer_store_dword v11, v10, s[92:95], 0 offen offset:256 sc1
s_and_b32 s53, vcc_lo, 255
s_cmp_eq_u32 s53, 0xFF
s_cselect_b32 s97, 1, 0
s_branch .Lhsd0_0_back
.Lhs_d2:
s_nop 4
s_waitcnt vmcnt(12)
R_ v11,a254
v_cmp_eq_u32 vcc, s62, v11
R_ v10,a251
v_mov_b32 v11, 0x7fff0000
v_cndmask_b32 v10, v11, v10, vcc
R_ v11,a253
v_add_u32 v11, 1, v11
buffer_store_dword v11, v10, s[92:95], 0 offen offset:256 sc1
s_and_b32 s53, vcc_lo, 255
s_cmp_eq_u32 s53, 0xFF
s_cselect_b32 s97, 1, 0
s_branch .Lhs_d2_back
.Lhs_d1:
s_nop 4
s_waitcnt vmcnt(12)
R_ v11,a254
R_ v10,a252
v_cmp_ne_u32 vcc, v11, v10
s_nop 1
s_and_b32 s53, vcc_lo, 255
s_cmp_eq_u32 s53, 0xFF
s_cselect_b32 s53, s97, 0
s_cmp_eq_u32 s53, 1
s_cbranch_scc1 .Lhs_ok
.Lhs_spin:
s_cmp_eq_u32 s57, 0
s_cbranch_scc1 .Lhs_ok
s_sub_u32 s57, s57, 1
s_sleep 8
R_ v10,a251
s_lshl_b32 s53, s6, 2
v_subrev_u32 v10, s53, v10
buffer_load_dword v11, v10, s[92:95], 0 offen offset:0 sc1
R_ v10,a248
v_lshrrev_b32 v10, 2, v10
buffer_load_dword v12, v10, s[92:95], s96 offen offset:128 sc1
buffer_load_dword v13, v10, s[92:95], s96 offen offset:256 sc1
s_waitcnt vmcnt(0)
X_ a255,v11
R_ v10,a251
buffer_store_dword v11, v10, s[92:95], 0 offen offset:128 sc1
s_nop 1
v_cmp_eq_u32 vcc, s62, v12
R_ v10,a251
v_mov_b32 v12, 0x7fff0000
v_cndmask_b32 v10, v12, v10, vcc
R_ v12,a253
v_add_u32 v12, 1, v12
buffer_store_dword v12, v10, s[92:95], 0 offen offset:256 sc1
s_and_b32 s53, vcc_lo, 255
s_cmp_eq_u32 s53, 0xFF
s_cselect_b32 s97, 1, 0
s_nop 1
R_ v10,a252
v_cmp_ne_u32 vcc, v13, v10
s_nop 1
s_and_b32 s53, vcc_lo, 255
s_cmp_eq_u32 s53, 0xFF
s_cselect_b32 s53, s97, 0
s_cmp_eq_u32 s53, 1
s_cbranch_scc0 .Lhs_spin
.Lhs_ok:
s_branch .Lhs_d1_back
.Latt2:
R_ v13,a248
S_ s53,s51,36864
v_add_u32 v12, s53, v13
.Lpad_a2_0:
s_cbranch_execz .Lpad_a2_4
s_mul_i32 s53, s4, 10240
S_ s53,s53,0
v_add_u32 v13, s53, v13
s_cmp_eq_u32 s4, 0
s_cbranch_scc0 .Lhs_tok2
R_ v11,a241
buffer_load_dword a255, v11, s[92:95], 0 offen offset:0 sc1
.Lhs_tok2:
W_ 2
Zvvv 96,30,136
Dv 42,v1,44032
R_ v2,a245
Zvvv 100,30,140
ds_write_b128 v13, a[0:3] offset:0
Zvvv 104,30,144
v_xor_b32 v3, 32, v2
v_xor_b32 v4, 64, v2
Zvvv 108,30,148
ds_write_b128 v13, a[4:7] offset:1024
Zvvv 112,30,152
W_ 4
v_xor_b32 v5, 96, v2
s_mov_b64 s[24:25], s[16:17]
Mvvv 96,34,156
s_mov_b32 s26, 7864320
Dv 30,%0,45056
s_mov_b64 s[28:29], s[22:23]
Mvvv 100,34,160
ds_write_b128 v13, a[8:11] offset:2048
Mvvv 104,34,164
s_mov_b32 s30, 1638400
R_ v20,a80
s_mul_i32 s52, s4, 10240
Mvvv 108,34,168
ds_write_b128 v13, a[12:15] offset:3072
Mvvv 112,34,172
W_ 6
R_ v21,a81
R_ v22,a82
Mvvv 96,38,176
Dv 34,v1,45056
R_ v23,a83
Mvvv 100,38,180
ds_write_b128 v13, a[16:19] offset:4096
Mvvv 104,38,184
R_ v24,a84
R_ v25,a85
Mvvv 108,38,188
ds_write_b128 v13, a[40:43] offset:5120
Mvvv 112,38,192
W_ 8
R_ v26,a86
R_ v27,a87
Mvvv 96,42,196
Dv 38,%0,36864
R_ v52,a88
Mvvv 100,42,200
ds_write_b128 v13, a[44:47] offset:6144
Mvvv 104,42,204
R_ v53,a89
R_ v54,a90
Mvvv 108,42,208
ds_write_b128 v13, a[48:51] offset:7168
Mvvv 112,42,212
W_ 8
R_ v55,a91
v_accvgpr_mov_b32 a240, a92
Mvvv 96,30,216
Dv 42,v1,36864
v_accvgpr_mov_b32 a241, a93
Mvvv 100,30,220
ds_write_b128 v13, a[52:55] offset:8192
Mvvv 104,30,224
v_accvgpr_mov_b32 a242, a94
v_accvgpr_mov_b32 a243, a95
Mvvv 108,30,228
ds_write_b128 v13, a[56:59] offset:9216
Mvvv 112,30,232
W_ 8
v_accvgpr_mov_b32 a244, a96
v_accvgpr_mov_b32 a245, a97
Mvvv 96,34,236
Dv 30,%0,37888
v_accvgpr_mov_b32 a246, a98
Mvvv 100,34,240
v_accvgpr_mov_b32 a247, a99
R_ v16,a120
Mvvv 104,34,244
R_ v17,a121
R_ v18,a122
Mvvv 108,34,248
R_ v19,a123
R_ v28,a124
Mvvv 112,34,252
W_ 6
R_ v29,a125
R_ v51,a126
Zvvv 76,38,136
Dv 34,v1,37888
R_ v6,a127
Zvvv 80,38,140
R_ v14,a128
R_ v15,a129
Zvvv 84,38,144
v_accvgpr_mov_b32 a249, a130
v_accvgpr_mov_b32 a250, a131
Zvvv 88,38,148
v_max3_f32 v56, v96, v97, v98
v_max_f32 v56, v56, v99
Zvvv 92,38,152
W_ 4
v_max3_f32 v60, v100, v101, v102
v_max_f32 v60, v60, v103
Mvvv 76,42,156
Dv 38,%0,38912
v_max3_f32 v64, v104, v105, v106
Mvvv 80,42,160
v_max_f32 v64, v64, v107
Mvvv 84,42,164
v_max3_f32 v68, v108, v109, v110
v_max_f32 v68, v68, v111
Mvvv 88,42,168
v_max3_f32 v72, v112, v113, v114
v_max_f32 v72, v72, v115
Mvvv 92,42,172
W_ 2
Mvvv 76,30,176
Dv 42,v1,38912
Mvvv 80,30,180
Mvvv 84,30,184
Mvvv 88,30,188
Mvvv 92,30,192
s_waitcnt vmcnt(34) lgkmcnt(0)
s_barrier
Mvvv 76,34,196
Dv 30,%0,55296
Mvvv 76,38,216
Mvvv 76,42,236
Mvvv 80,34,200
Mvvv 80,38,220
Mvvv 80,42,240
Mvvv 84,34,204
s_nop 3
v_max3_f32 v56, v56, v76, v77
v_max3_f32 v56, v56, v78, v79
Mvvv 84,38,224
Mvvv 84,42,244
Mvvv 88,34,208
v_max3_f32 v60, v60, v80, v81
v_max3_f32 v60, v60, v82, v83
Mvvv 88,38,228
Mvvv 88,42,248
Mvvv 92,34,212
s_nop 1
v_max3_f32 v64, v64, v84, v85
v_max3_f32 v64, v64, v86, v87
Mvvv 92,38,232
Mvvv 92,42,252
Dv 34,v1,55296
.p2align 3
v_mov_b32 v11, 0x3f803f80
v_permlane32_swap_b32 v56, v60
v_max3_f32 v68, v68, v88, v89
v_max3_f32 v68, v68, v90, v91
v_max_f32 v56, v56, v60
s_nop 1
v_max3_f32 v72, v72, v92, v93
v_max3_f32 v72, v72, v94, v95
v_mov_b32 v73, v72
v_permlane32_swap_b32 v64, v68
v_max_f32 v64, v64, v68
v_permlane32_swap_b32 v72, v73
v_max_f32 v72, v72, v73
v_mov_b32 v73, v72
v_permlane16_swap_b32 v56, v64
v_max_f32 v56, v56, v64
v_permlane16_swap_b32 v72, v73
B_ v56,0xc0e00000,v56
v_max_f32 v72, v72, v73
v_mov_b32 v64, v56
B_ v72,0xc0e00000,v72
v_mov_b32 v73, v72
v_permlane16_swap_b32 v56, v64
v_mov_b32 v60, v56
v_mov_b32 v68, v64
v_mov_b64 v[74:75], v[72:73]
v_permlane32_swap_b32 v56, v60
v_permlane32_swap_b32 v64, v68
v_mov_b32 v57, v56
v_mov_b32 v61, v60
v_mov_b32 v65, v64
v_mov_b32 v69, v68
v_pk_add_f32 v[96:97], v[96:97], v[56:57]
v_pk_add_f32 v[98:99], v[98:99], v[56:57]
v_pk_add_f32 v[76:77], v[76:77], v[56:57]
v_pk_add_f32 v[78:79], v[78:79], v[56:57]
v_mov_b64 v[58:59], v[56:57]
v_pk_add_f32 v[100:101], v[100:101], v[60:61]
v_pk_add_f32 v[102:103], v[102:103], v[60:61]
v_pk_add_f32 v[80:81], v[80:81], v[60:61]
v_pk_add_f32 v[82:83], v[82:83], v[60:61]
v_mov_b64 v[62:63], v[60:61]
v_pk_add_f32 v[104:105], v[104:105], v[64:65]
v_pk_add_f32 v[106:107], v[106:107], v[64:65]
v_pk_add_f32 v[84:85], v[84:85], v[64:65]
v_pk_add_f32 v[86:87], v[86:87], v[64:65]
v_mov_b64 v[66:67], v[64:65]
v_pk_add_f32 v[108:109], v[108:109], v[68:69]
v_pk_add_f32 v[110:111], v[110:111], v[68:69]
v_pk_add_f32 v[88:89], v[88:89], v[68:69]
v_pk_add_f32 v[90:91], v[90:91], v[68:69]
v_mov_b64 v[70:71], v[68:69]
v_pk_add_f32 v[112:113], v[112:113], v[72:73]
v_pk_add_f32 v[114:115], v[114:115], v[72:73]
v_pk_add_f32 v[92:93], v[92:93], v[72:73]
v_pk_add_f32 v[94:95], v[94:95], v[72:73]
E_ 96
E_ 97
E_ 98
E_ 99
E_ 76
E_ 77
E_ 78
E_ 79
C_ 99,98,99
C_ 98,96,97
C_ 96,76,77
C_ 97,78,79
E_ 100
E_ 101
E_ 102
E_ 103
E_ 80
E_ 81
E_ 82
E_ 83
C_ 103,102,103
C_ 102,100,101
C_ 100,80,81
C_ 101,82,83
.Lstep2_1:
)A"
R"A(W_ 1
Nvvv 116,30,136,56
s_nop 0
Dv 38,%0,56320
Dv 42,v1,56320
W_ 2
Mvvv 116,34,156
E_ 104
Nvvv 120,30,140,60
E_ 105
Mvvv 120,34,160
E_ 106
Nvvv 124,30,144,64
E_ 107
Mvvv 124,34,164
E_ 84
Nvvv 128,30,148,68
E_ 85
Mvvv 128,34,168
E_ 86
Nvvv 132,30,152,72
E_ 87
Mvvv 132,34,172
W_ 0
C_ 107,106,107
C_ 106,104,105
Mvvv 132,38,192
s_nop 0
Dv 30,%0,57344
Dv 34,v1,57344
Mvvv 132,42,212
C_ 104,84,85
C_ 105,86,87
Mvvv 128,38,188
E_ 108
Mvvv 128,42,208
E_ 109
Mvvv 124,38,184
E_ 110
Mvvv 124,42,204
E_ 111
Mvvv 120,38,180
E_ 88
Mvvv 120,42,200
E_ 89
Mvvv 116,38,176
E_ 90
Mvvv 116,42,196
W_ 0
E_ 91
Mvvv 116,30,216
s_nop 0
Dv 38,%0,49152
Dv 42,v1,49152
Mvvv 116,34,236
C_ 111,110,111
C_ 110,108,109
Mvvv 120,30,220
C_ 108,88,89
C_ 109,90,91
Mvvv 120,34,240
E_ 112
Mvvv 124,30,224
E_ 113
Mvvv 124,34,244
E_ 114
Mvvv 128,30,228
E_ 115
Mvvv 128,34,248
E_ 92
Mvvv 132,30,232
E_ 93
Mvvv 132,34,252
W_ 0
E_ 94
Nvvv 76,38,136,56
s_nop 0
Dv 30,%0,50176
Dv 34,v1,50176
Mvvv 76,42,156
E_ 95
Nvvv 80,38,140,60
C_ 115,114,115
C_ 114,112,113
Mvvv 80,42,160
.Lpad_a2_1:
s_cbranch_execz .Lpad_a2_5
C_ 112,92,93
C_ 113,94,95
Nvvv 84,38,144,64
E_ 116
Mvvv 84,42,164
E_ 117
Nvvv 88,38,148,68
E_ 118
Mvvv 88,42,168
E_ 119
Nvvv 92,38,152,72
C_ 119,118,119
C_ 118,116,117
Mvvv 92,42,172
W_ 0
E_ 120
Mvvv 92,30,192
s_nop 0
Dv 38,%0,51200
Dv 42,v1,51200
Mvvv 92,34,212
X_ a180,v11
X_ a181,v11
Mvvv 88,30,188
X_ a182,v11
X_ a183,v11
Mvvv 88,34,208
ds_write_b128 v12, a[132:135] offset:0
Mvvv 84,30,184
E_ 121
Mvvv 84,34,204
E_ 122
Mvvv 80,30,180
ds_write_b128 v12, a[136:139] offset:1024
Mvvv 80,34,200
E_ 123
Mvvv 76,30,176
C_ 123,122,123
C_ 122,120,121
Mvvv 76,34,196
ds_write_b128 v12, a[160:163] offset:2048
W_ 4
Mvvv 76,38,216
W_ 3
E_ 124
Mvvv 76,42,236
s_waitcnt vmcnt(12) lgkmcnt(0)
s_barrier
Mvvv 80,38,220
ds_write_b128 v12, a[164:167] offset:3072
Mvvv 80,42,240
E_ 125
Mvvv 84,38,224
E_ 126
Mvvv 84,42,244
s_nop 0
Tv 30,v2,24576
Tv 32,v2,30720
Mvvv 88,38,228
ds_write_b128 v12, a[168:171] offset:4096
Mvvv 88,42,248
s_nop 0
Tv 34,v3,24576
Tv 36,v3,30720
Mvvv 92,38,232
E_ 127
Mvvv 92,42,252
ds_write_b128 v12, a[172:175] offset:5120
Zaav 220,180,96
C_ 127,126,127
C_ 126,124,125
Zaav 224,180,100
E_ 128
Zaav 228,180,104
ds_write_b128 v12, a[176:179] offset:12288
Zaav 232,180,108
E_ 129
Zaav 236,180,112
W_ 2
E_ 130
Zavv 0,30,96
s_nop 0
Tv 38,v4,24576
Tv 40,v4,30720
Zavv 20,34,96
ds_write_b128 v12, a[200:203] offset:13312
Zavv 24,34,100
s_nop 0
Tv 42,v5,24576
Tv 44,v5,30720
Zavv 4,30,100
E_ 131
Zavv 8,30,104
ds_write_b128 v12, a[204:207] offset:14336
Zavv 28,34,104
R_ v46,a220
R_ v47,a224
Zavv 32,34,108
R_ v48,a228
R_ v49,a232
Zavv 12,30,108
R_ v50,a236
C_ 131,130,131
Zavv 16,30,112
ds_write_b128 v12, a[208:211] offset:15360
Zavv 36,34,112
W_ 2
C_ 130,128,129
Zavv 56,38,112
s_nop 0
Tv 30,v2,25600
Tv 32,v2,31744
Zavv 76,42,112
ds_write_b128 v12, a[212:215] offset:16384
Zavv 72,42,108
s_nop 0
Tv 34,v3,25600
Tv 36,v3,31744
Zavv 52,38,108
E_ 132
Zavv 48,38,104
ds_write_b128 v12, a[216:219] offset:17408
Zavv 68,42,104
E_ 133
Zavv 64,42,100
E_ 134
Zavv 44,38,100
E_ 135
Zavv 40,38,96
C_ 135,134,135
C_ 134,132,133
Zavv 60,42,96
W_ 1
E_ 76
Zavv 80,30,96
s_nop 0
Tv 38,v4,25600
Tv 40,v4,31744
Zavv 100,34,96
E_ 77
Zavv 104,34,100
s_nop 0
Tv 42,v5,25600
Tv 44,v5,31744
Zavv 84,30,100
E_ 78
Zavv 88,30,104
E_ 79
Zavv 108,34,104
C_ 116,76,77
C_ 117,78,79
Zavv 112,34,108
E_ 80
Zavv 92,30,108
E_ 81
Zavv 96,30,112
E_ 82
Zavv 116,34,112
W_ 0
E_ 83
Zavv 136,38,112
s_nop 0
Tv 30,v2,26624
Tv 32,v2,32768
Zavv 156,42,112
C_ 120,80,81
C_ 121,82,83
Zavv 152,42,108
s_nop 0
Tv 34,v3,26624
Tv 36,v3,32768
Zavv 132,38,108
E_ 84
Zavv 128,38,104
E_ 85
Zavv 148,42,104
E_ 86
Zavv 144,42,100
E_ 87
Zavv 124,38,100
C_ 124,84,85
C_ 125,86,87
Zavv 120,38,96
E_ 88
Zavv 140,42,96
W_ 0
E_ 89
Zavv 160,30,96
s_nop 0
Tv 38,v4,26624
Tv 40,v4,32768
Zavv 180,34,96
E_ 90
Zavv 184,34,100
s_nop 0
Tv 42,v5,26624
Tv 44,v5,32768
Zavv 164,30,100
E_ 91
Zavv 168,30,104
C_ 128,88,89
C_ 129,90,91
Zavv 188,34,104
S_ s65,s61,36864
E_ 92
Zavv 192,34,108
S_ m0,s51,24576
E_ 93
Zavv 172,30,108
s_waitcnt vmcnt(6) lgkmcnt(0)
s_barrier
Zavv 176,30,112
Ln v7,32,s65,0
Zavv 196,34,112
E_ 94
Zavv 216,38,112
Dv 30,%0,6144
Zavv 236,42,112
Dv 34,v1,6144
Zavv 232,42,108
Ln v8,32,s65,3072
S_ m0,s51,25472
Zavv 212,38,108
E_ 95
Zavv 208,38,104
Ln v7,32,s65,128
Zavv 228,42,104
C_ 132,92,93
C_ 133,94,95
Zavv 224,42,100
Ln v8,32,s65,3200
S_ m0,s51,26368
Zavv 204,38,100
v_mov_b32 v92, 0x3f803f80
v_mov_b32 v93, 0x3f803f80
Zavv 200,38,96
Ln v7,32,s65,256
Zavv 220,42,96
v_mov_b32 v94, 0x3f803f80
v_mov_b32 v95, 0x3f803f80
Ln v8,32,s65,3328
.Lstep2_2:
s_nop 0
Zvvv 76,92,116
Dv 38,%0,7168
Zvvv 80,92,120
Dv 42,v1,7168
Zvvv 84,92,124
Zvvv 88,92,128
Zvvv 92,92,132
W_ 2
Nvvv 96,30,136,56
Mvvv 96,34,156
Nvvv 100,30,140,60
s_nop 1
v_max3_f32 v10, v76, v80, v84
s_nop 0
v_max3_f32 v10, v10, v88, v92
Mvvv 100,34,160
v_cmp_lt_f32 vcc, 0x42000000, v10
s_cbranch_vccnz .Ltramp2_1
Nvvv 104,30,144,64
A_ v46,v46,v76
A_ v47,v47,v80
Mvvv 104,34,164
A_ v48,v48,v84
A_ v49,v49,v88
Nvvv 108,30,148,68
A_ v50,v50,v92
Mvvv 108,34,168
Nvvv 112,30,152,72
Mvvv 112,34,172
W_ 0
Mvvv 112,38,192
Dv 30,%0,8192
Mvvv 112,42,212
Dv 34,v1,8192
Mvvv 108,38,188
Mvvv 108,42,208
Mvvv 104,38,184
Mvvv 104,42,204
Mvvv 100,38,180
Mvvv 100,42,200
Mvvv 96,38,176
Mvvv 96,42,196
W_ 0
Mvvv 96,30,216
Dv 38,%0,0
Mvvv 96,34,236
Dv 42,v1,0
Mvvv 100,30,220
Mvvv 100,34,240
Mvvv 104,30,224
Mvvv 104,34,244
Mvvv 108,30,228
Mvvv 108,34,248
s_nop 0
E_ 96
Mvvv 112,30,232
E_ 97
Mvvv 112,34,252
W_ 0
E_ 98
Nvvv 76,38,136,56
Dv 30,%0,1024
Mvvv 76,42,156
Dv 34,v1,1024
Nvvv 80,38,140,60
E_ 99
Mvvv 80,42,160
C_ 99,98,99
C_ 98,96,97
Nvvv 84,38,144,64
E_ 100
Mvvv 84,42,164
E_ 101
Nvvv 88,38,148,68
E_ 102
Mvvv 88,42,168
E_ 103
Nvvv 92,38,152,72
C_ 103,102,103
C_ 102,100,101
Mvvv 92,42,172
W_ 0
E_ 104
Mvvv 92,30,192
Dv 38,%0,2048
Mvvv 92,34,212
Dv 42,v1,2048
Mvvv 88,30,188
E_ 105
Mvvv 88,34,208
E_ 106
Mvvv 84,30,184
S_ s65,s61,24576
E_ 107
Mvvv 84,34,204
S_ m0,s51,0
C_ 107,106,107
C_ 106,104,105
Mvvv 80,30,180
s_waitcnt vmcnt(6) lgkmcnt(0)
s_barrier
Mvvv 80,34,200
L_ v9,36,s65,0
Mvvv 76,30,176
E_ 108
Mvvv 76,34,196
L_ v9,36,s65,3072
S_ m0,s51,896
Mvvv 76,38,216
Tv 30,v2,12288
Tv 32,v2,18432
Mvvv 76,42,236
L_ v9,36,s65,128
Mvvv 80,38,220
s_nop 0
Tv 34,v3,12288
Tv 36,v3,18432
Mvvv 80,42,240
L_ v9,36,s65,3200
S_ m0,s51,1792
Mvvv 84,38,224
E_ 109
Mvvv 84,42,244
L_ v9,36,s65,256
Mvvv 88,38,228
E_ 110
Mvvv 88,42,248
L_ v9,36,s65,3328
Mvvv 92,38,232
E_ 111
Mvvv 92,42,252
W_ 0
C_ 111,110,111
C_ 110,108,109
Mavv 0,30,116
s_nop 0
Tv 38,v4,12288
Tv 40,v4,18432
Mavv 20,34,116
E_ 112
Mavv 24,34,120
s_nop 0
Tv 42,v5,12288
Tv 44,v5,18432
Mavv 4,30,120
E_ 113
Mavv 8,30,124
E_ 114
Mavv 28,34,124
E_ 115
Mavv 32,34,128
C_ 115,114,115
C_ 114,112,113
Mavv 12,30,128
E_ 76
Mavv 16,30,132
E_ 77
Mavv 36,34,132
W_ 0
E_ 78
Mavv 56,38,132
s_nop 0
Tv 30,v2,13312
Tv 32,v2,19456
Mavv 76,42,132
E_ 79
Mavv 72,42,128
s_nop 0
Tv 34,v3,13312
Tv 36,v3,19456
Mavv 52,38,128
C_ 96,76,77
C_ 97,78,79
Mavv 48,38,124
E_ 80
Mavv 68,42,124
E_ 81
Mavv 64,42,120
E_ 82
Mavv 44,38,120
E_ 83
Mavv 40,38,116
C_ 100,80,81
C_ 101,82,83
Mavv 60,42,116
W_ 0
E_ 84
Mavv 80,30,116
s_nop 0
Tv 38,v4,13312
Tv 40,v4,19456
Mavv 100,34,116
E_ 85
Mavv 104,34,120
s_nop 0
Tv 42,v5,13312
Tv 44,v5,19456
Mavv 84,30,120
E_ 86
Mavv 88,30,124
E_ 87
Mavv 108,34,124
C_ 104,84,85
C_ 105,86,87
Mavv 112,34,128
E_ 88
Mavv 92,30,128
E_ 89
Mavv 96,30,132
E_ 90
Mavv 116,34,132
W_ 0
E_ 91
Mavv 136,38,132
s_nop 0
Tv 30,v2,14336
Tv 32,v2,20480
Mavv 156,42,132
C_ 108,88,89
C_ 109,90,91
Mavv 152,42,128
s_nop 0
Tv 34,v3,14336
Tv 36,v3,20480
Mavv 132,38,128
E_ 92
Mavv 128,38,124
E_ 93
Mavv 148,42,124
E_ 94
Mavv 144,42,120
E_ 95
Mavv 124,38,120
C_ 112,92,93
C_ 113,94,95
Mavv 120,38,116
v_mov_b32 v92, 0x3f803f80
v_mov_b32 v93, 0x3f803f80
Mavv 140,42,116
W_ 0
v_mov_b32 v94, 0x3f803f80
v_mov_b32 v95, 0x3f803f80
Mavv 160,30,116
s_nop 0
Tv 38,v4,14336
Tv 40,v4,20480
Mavv 180,34,116
Mavv 184,34,120
s_nop 0
Tv 42,v5,14336
Tv 44,v5,20480
Mavv 164,30,120
Mavv 168,30,124
Mavv 188,34,124
S_ s65,s61,49152
Mavv 192,34,128
S_ m0,s51,12288
Mavv 172,30,128
s_waitcnt vmcnt(6) lgkmcnt(0)
s_barrier
Mavv 176,30,132
Ln v7,32,s65,0
Mavv 196,34,132
Mavv 216,38,132
Dv 30,%0,30720
Mavv 236,42,132
Dv 34,v1,30720
Mavv 232,42,128
Ln v8,32,s65,3072
S_ m0,s51,13184
Mavv 212,38,128
Mavv 208,38,124
Ln v7,32,s65,128
Mavv 228,42,124
Mavv 224,42,120
Ln v8,32,s65,3200
S_ m0,s51,14080
Mavv 204,38,120
Mavv 200,38,116
Ln v7,32,s65,256
Mavv 220,42,116
s_cmp_eq_u32 s4, 0
s_cbranch_scc1 .Lhs_a2
.Lhs_a2_back:
.Lstep2_3:
Zvvv 76,92,96
Dv 38,%0,31744
Zvvv 80,92,100
Dv 42,v1,31744
Zvvv 84,92,104
Zvvv 88,92,108
Zvvv 92,92,112
W_ 2
Nvvv 116,30,136,56
Mvvv 116,34,156
Nvvv 120,30,140,60
s_nop 1
v_max3_f32 v10, v76, v80, v84
s_nop 0
v_max3_f32 v10, v10, v88, v92
Mvvv 120,34,160
v_cmp_lt_f32 vcc, 0x42000000, v10
s_cbranch_vccnz .Ltramp2_2
Nvvv 124,30,144,64
A_ v46,v46,v76
A_ v47,v47,v80
Mvvv 124,34,164
A_ v48,v48,v84
A_ v49,v49,v88
Nvvv 128,30,148,68
A_ v50,v50,v92
Mvvv 128,34,168
Ln v8,32,s65,3328
Nvvv 132,30,152,72
Mvvv 132,34,172
W_ 0
Mvvv 132,38,192
Dv 30,%0,32768
Mvvv 132,42,212
Dv 34,v1,32768
Mvvv 128,38,188
Mvvv 128,42,208
Mvvv 124,38,184
Mvvv 124,42,204
Mvvv 120,38,180
Mvvv 120,42,200
Mvvv 116,38,176
Mvvv 116,42,196
W_ 0
Mvvv 116,30,216
Dv 38,%0,24576
Mvvv 116,34,236
Dv 42,v1,24576
Mvvv 120,30,220
Mvvv 120,34,240
Mvvv 124,30,224
Mvvv 124,34,244
Mvvv 128,30,228
Mvvv 128,34,248
s_nop 0
E_ 116
Mvvv 132,30,232
E_ 117
Mvvv 132,34,252
W_ 0
E_ 118
Nvvv 76,38,136,56
Dv 30,%0,25600
Mvvv 76,42,156
.Lpad_a2_2:
s_cbranch_execz .Lpad_a2_6
Dv 34,v1,25600
Nvvv 80,38,140,60
E_ 119
Mvvv 80,42,160
C_ 119,118,119
C_ 118,116,117
Nvvv 84,38,144,64
E_ 120
Mvvv 84,42,164
E_ 121
Nvvv 88,38,148,68
E_ 122
Mvvv 88,42,168
E_ 123
Nvvv 92,38,152,72
C_ 123,122,123
C_ 122,120,121
Mvvv 92,42,172
W_ 0
E_ 124
Mvvv 92,30,192
Dv 38,%0,26624
Mvvv 92,34,212
Dv 42,v1,26624
Mvvv 88,30,188
E_ 125
Mvvv 88,34,208
E_ 126
Mvvv 84,30,184
S_ s65,s61,36864
E_ 127
Mvvv 84,34,204
S_ m0,s51,24576
C_ 127,126,127
C_ 126,124,125
Mvvv 80,30,180
s_waitcnt vmcnt(6) lgkmcnt(0)
s_barrier
Mvvv 80,34,200
L_ v9,36,s65,0
Mvvv 76,30,176
E_ 128
Mvvv 76,34,196
L_ v9,36,s65,3072
S_ m0,s51,25472
Mvvv 76,38,216
Tv 30,v2,0
Tv 32,v2,6144
Mvvv 76,42,236
L_ v9,36,s65,128
Mvvv 80,38,220
s_nop 0
Tv 34,v3,0
Tv 36,v3,6144
Mvvv 80,42,240
L_ v9,36,s65,3200
S_ m0,s51,26368
Mvvv 84,38,224
E_ 129
Mvvv 84,42,244
L_ v9,36,s65,256
Mvvv 88,38,228
E_ 130
Mvvv 88,42,248
L_ v9,36,s65,3328
Mvvv 92,38,232
E_ 131
Mvvv 92,42,252
W_ 0
C_ 131,130,131
C_ 130,128,129
Mavv 0,30,96
s_nop 0
Tv 38,v4,0
Tv 40,v4,6144
Mavv 20,34,96
E_ 132
Mavv 24,34,100
s_nop 0
Tv 42,v5,0
Tv 44,v5,6144
Mavv 4,30,100
E_ 133
Mavv 8,30,104
E_ 134
Mavv 28,34,104
E_ 135
Mavv 32,34,108
C_ 135,134,135
C_ 134,132,133
Mavv 12,30,108
E_ 76
Mavv 16,30,112
E_ 77
Mavv 36,34,112
W_ 0
E_ 78
Mavv 56,38,112
s_nop 0
Tv 30,v2,1024
Tv 32,v2,7168
Mavv 76,42,112
E_ 79
Mavv 72,42,108
s_nop 0
Tv 34,v3,1024
Tv 36,v3,7168
Mavv 52,38,108
C_ 116,76,77
C_ 117,78,79
Mavv 48,38,104
E_ 80
Mavv 68,42,104
E_ 81
Mavv 64,42,100
E_ 82
Mavv 44,38,100
E_ 83
Mavv 40,38,96
C_ 120,80,81
C_ 121,82,83
Mavv 60,42,96
W_ 0
E_ 84
Mavv 80,30,96
s_nop 0
Tv 38,v4,1024
Tv 40,v4,7168
Mavv 100,34,96
E_ 85
Mavv 104,34,100
s_nop 0
Tv 42,v5,1024
Tv 44,v5,7168
Mavv 84,30,100
E_ 86
Mavv 88,30,104
E_ 87
Mavv 108,34,104
C_ 124,84,85
C_ 125,86,87
Mavv 112,34,108
E_ 88
Mavv 92,30,108
E_ 89
Mavv 96,30,112
E_ 90
Mavv 116,34,112
W_ 0
E_ 91
Mavv 136,38,112
s_nop 0
Tv 30,v2,2048
Tv 32,v2,8192
Mavv 156,42,112
C_ 128,88,89
C_ 129,90,91
Mavv 152,42,108
s_nop 0
Tv 34,v3,2048
Tv 36,v3,8192
Mavv 132,38,108
E_ 92
Mavv 128,38,104
E_ 93
Mavv 148,42,104
E_ 94
Mavv 144,42,100
E_ 95
Mavv 124,38,100
C_ 132,92,93
C_ 133,94,95
Mavv 120,38,96
v_mov_b32 v92, 0x3f803f80
v_mov_b32 v93, 0x3f803f80
Mavv 140,42,96
W_ 0
v_mov_b32 v94, 0x3f803f80
v_mov_b32 v95, 0x3f803f80
Mavv 160,30,96
s_nop 0
Tv 38,v4,2048
Tv 40,v4,8192
Mavv 180,34,96
Mavv 184,34,100
s_nop 0
Tv 42,v5,2048
Tv 44,v5,8192
Mavv 164,30,100
Mavv 168,30,104
Mavv 188,34,104
S_ s65,s61,61440
Mavv 192,34,108
S_ m0,s51,0
Mavv 172,30,108
s_waitcnt vmcnt(6) lgkmcnt(0)
s_barrier
Mavv 176,30,112
Ln v7,32,s65,0
Mavv 196,34,112
Mavv 216,38,112
Dv 30,%0,18432
Mavv 236,42,112
Dv 34,v1,18432
Mavv 232,42,108
Ln v8,32,s65,3072
S_ m0,s51,896
Mavv 212,38,108
Mavv 208,38,104
Ln v7,32,s65,128
Mavv 228,42,104
Mavv 224,42,100
Ln v8,32,s65,3200
S_ m0,s51,1792
Mavv 204,38,100
Mavv 200,38,96
Ln v7,32,s65,256
Mavv 220,42,96
.Lstep2_4:
Zvvv 76,92,116
Dv 38,%0,19456
Zvvv 80,92,120
Dv 42,v1,19456
Zvvv 84,92,124
Zvvv 88,92,128
Zvvv 92,92,132
W_ 2
Nvvv 96,30,136,56
Mvvv 96,34,156
Nvvv 100,30,140,60
s_nop 1
v_max3_f32 v10, v76, v80, v84
s_nop 0
v_max3_f32 v10, v10, v88, v92
Mvvv 100,34,160
v_cmp_lt_f32 vcc, 0x42000000, v10
s_cbranch_vccnz .Ltramp2_3
Nvvv 104,30,144,64
A_ v46,v46,v76
A_ v47,v47,v80
Mvvv 104,34,164
A_ v48,v48,v84
A_ v49,v49,v88
Nvvv 108,30,148,68
A_ v50,v50,v92
Mvvv 108,34,168
Ln v8,32,s65,3328
Nvvv 112,30,152,72
Mvvv 112,34,172
W_ 0
Mvvv 112,38,192
Dv 30,%0,20480
Mvvv 112,42,212
Dv 34,v1,20480
Mvvv 108,38,188
Mvvv 108,42,208
Mvvv 104,38,184
Mvvv 104,42,204
Mvvv 100,38,180
Mvvv 100,42,200
Mvvv 96,38,176
Mvvv 96,42,196
W_ 0
Mvvv 96,30,216
Dv 38,%0,12288
Mvvv 96,34,236
Dv 42,v1,12288
Mvvv 100,30,220
Mvvv 100,34,240
Mvvv 104,30,224
Mvvv 104,34,244
Mvvv 108,30,228
Mvvv 108,34,248
s_nop 0
E_ 96
Mvvv 112,30,232
E_ 97
Mvvv 112,34,252
W_ 0
E_ 98
Nvvv 76,38,136,56
Dv 30,%0,13312
Mvvv 76,42,156
Dv 34,v1,13312
Nvvv 80,38,140,60
E_ 99
Mvvv 80,42,160
C_ 99,98,99
C_ 98,96,97
Nvvv 84,38,144,64
E_ 100
Mvvv 84,42,164
E_ 101
Nvvv 88,38,148,68
E_ 102
Mvvv 88,42,168
E_ 103
Nvvv 92,38,152,72
C_ 103,102,103
C_ 102,100,101
Mvvv 92,42,172
W_ 0
E_ 104
Mvvv 92,30,192
Dv 38,%0,14336
Mvvv 92,34,212
Dv 42,v1,14336
Mvvv 88,30,188
E_ 105
Mvvv 88,34,208
E_ 106
Mvvv 84,30,184
S_ s65,s61,49152
E_ 107
Mvvv 84,34,204
S_ m0,s51,12288
C_ 107,106,107
C_ 106,104,105
Mvvv 80,30,180
s_waitcnt vmcnt(6) lgkmcnt(0)
s_barrier
Mvvv 80,34,200
L_ v9,36,s65,0
Mvvv 76,30,176
E_ 108
Mvvv 76,34,196
L_ v9,36,s65,3072
S_ m0,s51,13184
Mvvv 76,38,216
Tv 30,v2,24576
Tv 32,v2,30720
Mvvv 76,42,236
L_ v9,36,s65,128
Mvvv 80,38,220
s_nop 0
Tv 34,v3,24576
Tv 36,v3,30720
Mvvv 80,42,240
L_ v9,36,s65,3200
S_ m0,s51,14080
Mvvv 84,38,224
E_ 109
Mvvv 84,42,244
L_ v9,36,s65,256
Mvvv 88,38,228
E_ 110
Mvvv 88,42,248
L_ v9,36,s65,3328
Mvvv 92,38,232
E_ 111
Mvvv 92,42,252
W_ 0
C_ 111,110,111
C_ 110,108,109
Mavv 0,30,116
s_nop 0
Tv 38,v4,24576
Tv 40,v4,30720
Mavv 20,34,116
E_ 112
Mavv 24,34,120
s_nop 0
Tv 42,v5,24576
Tv 44,v5,30720
Mavv 4,30,120
E_ 113
Mavv 8,30,124
E_ 114
Mavv 28,34,124
E_ 115
Mavv 32,34,128
C_ 115,114,115
C_ 114,112,113
Mavv 12,30,128
E_ 76
Mavv 16,30,132
E_ 77
Mavv 36,34,132
W_ 0
E_ 78
Mavv 56,38,132
s_nop 0
Tv 30,v2,25600
Tv 32,v2,31744
Mavv 76,42,132
E_ 79
Mavv 72,42,128
s_nop 0
Tv 34,v3,25600
Tv 36,v3,31744
Mavv 52,38,128
C_ 96,76,77
C_ 97,78,79
Mavv 48,38,124
E_ 80
Mavv 68,42,124
E_ 81
Mavv 64,42,120
E_ 82
Mavv 44,38,120
E_ 83
Mavv 40,38,116
C_ 100,80,81
C_ 101,82,83
Mavv 60,42,116
W_ 0
E_ 84
Mavv 80,30,116
s_nop 0
Tv 38,v4,25600
Tv 40,v4,31744
Mavv 100,34,116
E_ 85
Mavv 104,34,120
s_nop 0
Tv 42,v5,25600
Tv 44,v5,31744
Mavv 84,30,120
E_ 86
Mavv 88,30,124
E_ 87
Mavv 108,34,124
C_ 104,84,85
C_ 105,86,87
Mavv 112,34,128
E_ 88
Mavv 92,30,128
E_ 89
Mavv 96,30,132
E_ 90
Mavv 116,34,132
W_ 0
E_ 91
Mavv 136,38,132
s_nop 0
Tv 30,v2,26624
Tv 32,v2,32768
Mavv 156,42,132
C_ 108,88,89
C_ 109,90,91
Mavv 152,42,128
s_nop 0
Tv 34,v3,26624
Tv 36,v3,32768
Mavv 132,38,128
E_ 92
Mavv 128,38,124
E_ 93
Mavv 148,42,124
E_ 94
Mavv 144,42,120
E_ 95
Mavv 124,38,120
C_ 112,92,93
C_ 113,94,95
Mavv 120,38,116
v_mov_b32 v92, 0x3f803f80
v_mov_b32 v93, 0x3f803f80
Mavv 140,42,116
W_ 0
v_mov_b32 v94, 0x3f803f80
v_mov_b32 v95, 0x3f803f80
Mavv 160,30,116
s_nop 0
Tv 38,v4,26624
Tv 40,v4,32768
Mavv 180,34,116
Mavv 184,34,120
s_nop 0
Tv 42,v5,26624
Tv 44,v5,32768
Mavv 164,30,120
Mavv 168,30,124
Mavv 188,34,124
S_ s65,s61,73728
Mavv 192,34,128
S_ m0,s51,24576
Mavv 172,30,128
s_waitcnt vmcnt(6) lgkmcnt(0)
s_barrier
Mavv 176,30,132
Ln v7,32,s65,0
Mavv 196,34,132
Mavv 216,38,132
Dv 30,%0,6144
Mavv 236,42,132
Dv 34,v1,6144
Mavv 232,42,128
Ln v8,32,s65,3072
S_ m0,s51,25472
Mavv 212,38,128
Mavv 208,38,124
Ln v7,32,s65,128
Mavv 228,42,124
Mavv 224,42,120
Ln v8,32,s65,3200
S_ m0,s51,26368
Mavv 204,38,120
Mavv 200,38,116
Ln v7,32,s65,256
Mavv 220,42,116
.Lstep2_5:
Zvvv 76,92,96
Dv 38,%0,7168
Zvvv 80,92,100
Dv 42,v1,7168
Zvvv 84,92,104
Zvvv 88,92,108
Zvvv 92,92,112
W_ 2
Nvvv 116,30,136,56
Mvvv 116,34,156
Nvvv 120,30,140,60
s_nop 1
v_max3_f32 v10, v76, v80, v84
s_nop 0
v_max3_f32 v10, v10, v88, v92
Mvvv 120,34,160
v_cmp_lt_f32 vcc, 0x42000000, v10
s_cbranch_vccnz .Ltramp2_4
Nvvv 124,30,144,64
A_ v46,v46,v76
A_ v47,v47,v80
Mvvv 124,34,164
A_ v48,v48,v84
A_ v49,v49,v88
Nvvv 128,30,148,68
A_ v50,v50,v92
Mvvv 128,34,168
Ln v8,32,s65,3328
Nvvv 132,30,152,72
Mvvv 132,34,172
W_ 0
Mvvv 132,38,192
Dv 30,%0,8192
Mvvv 132,42,212
Dv 34,v1,8192
Mvvv 128,38,188
Mvvv 128,42,208
Mvvv 124,38,184
Mvvv 124,42,204
Mvvv 120,38,180
Mvvv 120,42,200
Mvvv 116,38,176
Mvvv 116,42,196
W_ 0
Mvvv 116,30,216
Dv 38,%0,0
Mvvv 116,34,236
Dv 42,v1,0
Mvvv 120,30,220
Mvvv 120,34,240
Mvvv 124,30,224
Mvvv 124,34,244
Mvvv 128,30,228
Mvvv 128,34,248
s_nop 0
E_ 116
Mvvv 132,30,232
E_ 117
Mvvv 132,34,252
W_ 0
E_ 118
Nvvv 76,38,136,56
Dv 30,%0,1024
Mvvv 76,42,156
Dv 34,v1,1024
Nvvv 80,38,140,60
E_ 119
Mvvv 80,42,160
C_ 119,118,119
C_ 118,116,117
Nvvv 84,38,144,64
E_ 120
Mvvv 84,42,164
E_ 121
Nvvv 88,38,148,68
E_ 122
Mvvv 88,42,168
E_ 123
Nvvv 92,38,152,72
C_ 123,122,123
C_ 122,120,121
Mvvv 92,42,172
W_ 0
E_ 124
Mvvv 92,30,192
Dv 38,%0,2048
Mvvv 92,34,212
Dv 42,v1,2048
Mvvv 88,30,188
E_ 125
Mvvv 88,34,208
.Lpad_a2_3:
s_cbranch_execz .Lpad_a2_7
E_ 126
Mvvv 84,30,184
S_ s65,s61,61440
E_ 127
Mvvv 84,34,204
S_ m0,s51,0
C_ 127,126,127
C_ 126,124,125
Mvvv 80,30,180
s_waitcnt vmcnt(6) lgkmcnt(0)
s_barrier
Mvvv 80,34,200
L_ v9,36,s65,0
Mvvv 76,30,176
E_ 128
Mvvv 76,34,196
L_ v9,36,s65,3072
S_ m0,s51,896
Mvvv 76,38,216
Tv 30,v2,12288
Tv 32,v2,18432
Mvvv 76,42,236
L_ v9,36,s65,128
Mvvv 80,38,220
s_nop 0
Tv 34,v3,12288
Tv 36,v3,18432
Mvvv 80,42,240
L_ v9,36,s65,3200
S_ m0,s51,1792
Mvvv 84,38,224
E_ 129
Mvvv 84,42,244
L_ v9,36,s65,256
Mvvv 88,38,228
E_ 130
Mvvv 88,42,248
L_ v9,36,s65,3328
Mvvv 92,38,232
E_ 131
Mvvv 92,42,252
W_ 0
C_ 131,130,131
C_ 130,128,129
Mavv 0,30,96
s_nop 0
Tv 38,v4,12288
Tv 40,v4,18432
Mavv 20,34,96
E_ 132
Mavv 24,34,100
s_nop 0
Tv 42,v5,12288
Tv 44,v5,18432
Mavv 4,30,100
E_ 133
Mavv 8,30,104
E_ 134
Mavv 28,34,104
E_ 135
Mavv 32,34,108
C_ 135,134,135
C_ 134,132,133
Mavv 12,30,108
E_ 76
Mavv 16,30,112
E_ 77
Mavv 36,34,112
W_ 0
E_ 78
Mavv 56,38,112
s_nop 0
Tv 30,v2,13312
Tv 32,v2,19456
Mavv 76,42,112
E_ 79
Mavv 72,42,108
s_nop 0
Tv 34,v3,13312
Tv 36,v3,19456
Mavv 52,38,108
C_ 116,76,77
C_ 117,78,79
Mavv 48,38,104
E_ 80
Mavv 68,42,104
E_ 81
Mavv 64,42,100
E_ 82
Mavv 44,38,100
E_ 83
Mavv 40,38,96
C_ 120,80,81
C_ 121,82,83
Mavv 60,42,96
W_ 0
E_ 84
Mavv 80,30,96
s_nop 0
Tv 38,v4,13312
Tv 40,v4,19456
Mavv 100,34,96
E_ 85
Mavv 104,34,100
s_nop 0
Tv 42,v5,13312
Tv 44,v5,19456
Mavv 84,30,100
E_ 86
Mavv 88,30,104
E_ 87
Mavv 108,34,104
C_ 124,84,85
C_ 125,86,87
Mavv 112,34,108
E_ 88
Mavv 92,30,108
E_ 89
Mavv 96,30,112
E_ 90
Mavv 116,34,112
W_ 0
E_ 91
Mavv 136,38,112
s_nop 0
Tv 30,v2,14336
Tv 32,v2,20480
Mavv 156,42,112
C_ 128,88,89
C_ 129,90,91
Mavv 152,42,108
s_nop 0
Tv 34,v3,14336
Tv 36,v3,20480
Mavv 132,38,108
E_ 92
Mavv 128,38,104
E_ 93
Mavv 148,42,104
E_ 94
Mavv 144,42,100
E_ 95
Mavv 124,38,100
C_ 132,92,93
C_ 133,94,95
Mavv 120,38,96
v_mov_b32 v92, 0x3f803f80
v_mov_b32 v93, 0x3f803f80
Mavv 140,42,96
W_ 0
v_mov_b32 v94, 0x3f803f80
v_mov_b32 v95, 0x3f803f80
Mavv 160,30,96
s_nop 0
Tv 38,v4,14336
Tv 40,v4,20480
Mavv 180,34,96
Mavv 184,34,100
s_nop 0
Tv 42,v5,14336
Tv 44,v5,20480
Mavv 164,30,100
Mavv 168,30,104
Mavv 188,34,104
S_ s65,s61,86016
Mavv 192,34,108
S_ m0,s51,12288
Mavv 172,30,108
s_waitcnt vmcnt(6) lgkmcnt(0)
s_barrier
Mavv 176,30,112
Ln v7,32,s65,0
Mavv 196,34,112
Mavv 216,38,112
Dv 30,%0,30720
Mavv 236,42,112
Dv 34,v1,30720
Mavv 232,42,108
Ln v8,32,s65,3072
S_ m0,s51,13184
Mavv 212,38,108
Mavv 208,38,104
Ln v7,32,s65,128
Mavv 228,42,104
Mavv 224,42,100
Ln v8,32,s65,3200
S_ m0,s51,14080
Mavv 204,38,100
Mavv 200,38,96
Ln v7,32,s65,256
Mavv 220,42,96
.Lstep2_6:
Zvvv 76,92,116
Dv 38,%0,31744
Zvvv 80,92,120
Dv 42,v1,31744
Zvvv 84,92,124
Zvvv 88,92,128
Zvvv 92,92,132
W_ 2
Nvvv 96,30,136,56
Mvvv 96,34,156
Nvvv 100,30,140,60
s_nop 1
v_max3_f32 v10, v76, v80, v84
s_nop 0
v_max3_f32 v10, v10, v88, v92
Mvvv 100,34,160
v_cmp_lt_f32 vcc, 0x42000000, v10
s_cbranch_vccnz .Ltramp2_5
Nvvv 104,30,144,64
A_ v46,v46,v76
A_ v47,v47,v80
Mvvv 104,34,164
A_ v48,v48,v84
A_ v49,v49,v88
Nvvv 108,30,148,68
A_ v50,v50,v92
Mvvv 108,34,168
Ln v8,32,s65,3328
Nvvv 112,30,152,72
R_ v11,a248
Mvvv 112,34,172
W_ 0
v_min_u32 v11, 240, v11
Mvvv 112,38,192
Dv 30,%0,32768
v_lshlrev_b32 v11, 3, v11
Mvvv 112,42,212
Dv 34,v1,32768
Mvvv 108,38,188
buffer_load_dword a254, v11, s[44:47], s66 offen
Mvvv 108,42,208
Mvvv 104,38,184
Mvvv 104,42,204
Mvvv 100,38,180
Mvvv 100,42,200
Mvvv 96,38,176
Mvvv 96,42,196
W_ 0
Mvvv 96,30,216
Dv 38,%0,24576
Mvvv 96,34,236
Dv 42,v1,24576
Mvvv 100,30,220
Mvvv 100,34,240
Mvvv 104,30,224
Mvvv 104,34,244
Mvvv 108,30,228
Mvvv 108,34,248
s_nop 0
E_ 96
Mvvv 112,30,232
E_ 97
Mvvv 112,34,252
W_ 0
E_ 98
Nvvv 76,38,136,56
Dv 30,%0,25600
Mvvv 76,42,156
Dv 34,v1,25600
Nvvv 80,38,140,60
E_ 99
Mvvv 80,42,160
C_ 99,98,99
C_ 98,96,97
Nvvv 84,38,144,64
E_ 100
Mvvv 84,42,164
E_ 101
Nvvv 88,38,148,68
E_ 102
Mvvv 88,42,168
E_ 103
Nvvv 92,38,152,72
C_ 103,102,103
C_ 102,100,101
Mvvv 92,42,172
W_ 0
E_ 104
Mvvv 92,30,192
Dv 38,%0,26624
Mvvv 92,34,212
Dv 42,v1,26624
Mvvv 88,30,188
E_ 105
Mvvv 88,34,208
E_ 106
Mvvv 84,30,184
S_ s65,s61,73728
E_ 107
Mvvv 84,34,204
S_ m0,s51,24576
C_ 107,106,107
C_ 106,104,105
Mvvv 80,30,180
s_waitcnt vmcnt(7) lgkmcnt(0)
s_barrier
Mvvv 80,34,200
L_ v9,36,s65,0
Mvvv 76,30,176
E_ 108
Mvvv 76,34,196
L_ v9,36,s65,3072
S_ m0,s51,25472
Mvvv 76,38,216
Tv 30,v2,0
Tv 32,v2,6144
Mvvv 76,42,236
L_ v9,36,s65,128
Mvvv 80,38,220
s_nop 0
Tv 34,v3,0
Tv 36,v3,6144
Mvvv 80,42,240
L_ v9,36,s65,3200
S_ m0,s51,26368
Mvvv 84,38,224
E_ 109
Mvvv 84,42,244
L_ v9,36,s65,256
Mvvv 88,38,228
E_ 110
Mvvv 88,42,248
L_ v9,36,s65,3328
Mvvv 92,38,232
E_ 111
Mvvv 92,42,252
W_ 0
C_ 111,110,111
C_ 110,108,109
Mavv 0,30,116
s_nop 0
Tv 38,v4,0
Tv 40,v4,6144
Mavv 20,34,116
E_ 112
Mavv 24,34,120
s_nop 0
Tv 42,v5,0
Tv 44,v5,6144
Mavv 4,30,120
E_ 113
Mavv 8,30,124
E_ 114
Mavv 28,34,124
E_ 115
Mavv 32,34,128
C_ 115,114,115
C_ 114,112,113
Mavv 12,30,128
E_ 76
Mavv 16,30,132
E_ 77
Mavv 36,34,132
W_ 0
E_ 78
Mavv 56,38,132
s_nop 0
Tv 30,v2,1024
Tv 32,v2,7168
Mavv 76,42,132
E_ 79
Mavv 72,42,128
s_nop 0
Tv 34,v3,1024
Tv 36,v3,7168
Mavv 52,38,128
C_ 96,76,77
C_ 97,78,79
Mavv 48,38,124
E_ 80
Mavv 68,42,124
E_ 81
Mavv 64,42,120
E_ 82
Mavv 44,38,120
E_ 83
Mavv 40,38,116
C_ 100,80,81
C_ 101,82,83
Mavv 60,42,116
W_ 0
E_ 84
Mavv 80,30,116
s_nop 0
Tv 38,v4,1024
Tv 40,v4,7168
Mavv 100,34,116
E_ 85
Mavv 104,34,120
s_nop 0
Tv 42,v5,1024
Tv 44,v5,7168
Mavv 84,30,120
E_ 86
Mavv 88,30,124
E_ 87
Mavv 108,34,124
C_ 104,84,85
C_ 105,86,87
Mavv 112,34,128
E_ 88
Mavv 92,30,128
E_ 89
Mavv 96,30,132
E_ 90
Mavv 116,34,132
W_ 0
E_ 91
Mavv 136,38,132
s_nop 0
Tv 30,v2,2048
Tv 32,v2,8192
Mavv 156,42,132
C_ 108,88,89
C_ 109,90,91
Mavv 152,42,128
s_nop 0
Tv 34,v3,2048
Tv 36,v3,8192
Mavv 132,38,128
E_ 92
Mavv 128,38,124
E_ 93
Mavv 148,42,124
E_ 94
Mavv 144,42,120
E_ 95
Mavv 124,38,120
C_ 112,92,93
C_ 113,94,95
Mavv 120,38,116
v_mov_b32 v92, 0x3f803f80
v_mov_b32 v93, 0x3f803f80
Mavv 140,42,116
W_ 0
v_mov_b32 v94, 0x3f803f80
v_mov_b32 v95, 0x3f803f80
Mavv 160,30,116
s_nop 0
Tv 38,v4,2048
Tv 40,v4,8192
Mavv 180,34,116
Mavv 184,34,120
s_nop 0
Tv 42,v5,2048
Tv 44,v5,8192
Mavv 164,30,120
Mavv 168,30,124
Mavv 188,34,124
S_ s65,s61,98304
Mavv 192,34,128
S_ m0,s51,0
Mavv 172,30,128
s_waitcnt vmcnt(7) lgkmcnt(0)
s_barrier
Mavv 176,30,132
Ln v7,32,s65,0
Mavv 196,34,132
Mavv 216,38,132
Dv 30,%0,18432
Mavv 236,42,132
Dv 34,v1,18432
Mavv 232,42,128
Ln v8,32,s65,3072
S_ m0,s51,896
Mavv 212,38,128
Mavv 208,38,124
Ln v7,32,s65,128
Mavv 228,42,124
Mavv 224,42,120
Ln v8,32,s65,3200
S_ m0,s51,1792
Mavv 204,38,120
Mavv 200,38,116
Ln v7,32,s65,256
Mavv 220,42,116
s_cmp_eq_u32 s4, 0
s_cbranch_scc1 .Lhs_c2
.Lhs_c2_back:
.Lstep2_7:
Zvvv 76,92,96
Dv 38,%0,19456
Zvvv 80,92,100
Dv 42,v1,19456
Zvvv 84,92,104
Zvvv 88,92,108
Zvvv 92,92,112
W_ 2
Nvvv 116,30,136,56
Mvvv 116,34,156
Nvvv 120,30,140,60
s_nop 1
v_max3_f32 v10, v76, v80, v84
s_nop 0
v_max3_f32 v10, v10, v88, v92
Mvvv 120,34,160
v_cmp_lt_f32 vcc, 0x42000000, v10
s_cbranch_vccnz .Ltramp2_6
Nvvv 124,30,144,64
A_ v46,v46,v76
A_ v47,v47,v80
Mvvv 124,34,164
A_ v48,v48,v84
A_ v49,v49,v88
Nvvv 128,30,148,68
A_ v50,v50,v92
Mvvv 128,34,168
Ln v8,32,s65,3328
Nvvv 132,30,152,72
Mvvv 132,34,172
W_ 0
Mvvv 132,38,192
Dv 30,%0,20480
Mvvv 132,42,212
Dv 34,v1,20480
Mvvv 128,38,188
Mvvv 128,42,208
Mvvv 124,38,184
Mvvv 124,42,204
Mvvv 120,38,180
Mvvv 120,42,200
Mvvv 116,38,176
Mvvv 116,42,196
W_ 0
Mvvv 116,30,216
Dv 38,%0,12288
Mvvv 116,34,236
Dv 42,v1,12288
Mvvv 120,30,220
Mvvv 120,34,240
Mvvv 124,30,224
Mvvv 124,34,244
Mvvv 128,30,228
Mvvv 128,34,248
s_nop 0
E_ 116
Mvvv 132,30,232
E_ 117
Mvvv 132,34,252
W_ 0
E_ 118
Nvvv 76,38,136,56
Dv 30,%0,13312
Mvvv 76,42,156
Dv 34,v1,13312
Nvvv 80,38,140,60
E_ 119
Mvvv 80,42,160
C_ 119,118,119
C_ 118,116,117
Nvvv 84,38,144,64
E_ 120
Mvvv 84,42,164
E_ 121
Nvvv 88,38,148,68
E_ 122
Mvvv 88,42,168
E_ 123
Nvvv 92,38,152,72
C_ 123,122,123
C_ 122,120,121
Mvvv 92,42,172
W_ 0
E_ 124
Mvvv 92,30,192
Dv 38,%0,14336
Mvvv 92,34,212
Dv 42,v1,14336
Mvvv 88,30,188
E_ 125
Mvvv 88,34,208
E_ 126
Mvvv 84,30,184
S_ s65,s61,86016
E_ 127
Mvvv 84,34,204
S_ m0,s51,12288
C_ 127,126,127
C_ 126,124,125
Mvvv 80,30,180
s_waitcnt vmcnt(6) lgkmcnt(0)
s_barrier
Mvvv 80,34,200
L_ v9,36,s65,0
Mvvv 76,30,176
E_ 128
Mvvv 76,34,196
L_ v9,36,s65,3072
S_ m0,s51,13184
Mvvv 76,38,216
Tv 30,v2,24576
Tv 32,v2,30720
Mvvv 76,42,236
L_ v9,36,s65,128
Mvvv 80,38,220
.Lpad_a2_4:
s_cbranch_execz .Lhop_a2_r
Tv 34,v3,24576
Tv 36,v3,30720
Mvvv 80,42,240
L_ v9,36,s65,3200
S_ m0,s51,14080
Mvvv 84,38,224
E_ 129
Mvvv 84,42,244
L_ v9,36,s65,256
Mvvv 88,38,228
E_ 130
Mvvv 88,42,248
L_ v9,36,s65,3328
Mvvv 92,38,232
E_ 131
Mvvv 92,42,252
W_ 0
C_ 131,130,131
C_ 130,128,129
Mavv 0,30,96
s_nop 0
Tv 38,v4,24576
Tv 40,v4,30720
Mavv 20,34,96
E_ 132
Mavv 24,34,100
s_nop 0
Tv 42,v5,24576
Tv 44,v5,30720
Mavv 4,30,100
E_ 133
Mavv 8,30,104
E_ 134
Mavv 28,34,104
E_ 135
Mavv 32,34,108
C_ 135,134,135
C_ 134,132,133
Mavv 12,30,108
E_ 76
Mavv 16,30,112
E_ 77
Mavv 36,34,112
W_ 0
E_ 78
Mavv 56,38,112
s_nop 0
Tv 30,v2,25600
Tv 32,v2,31744
Mavv 76,42,112
E_ 79
Mavv 72,42,108
s_nop 0
Tv 34,v3,25600
Tv 36,v3,31744
Mavv 52,38,108
C_ 116,76,77
C_ 117,78,79
Mavv 48,38,104
E_ 80
Mavv 68,42,104
E_ 81
Mavv 64,42,100
E_ 82
Mavv 44,38,100
E_ 83
Mavv 40,38,96
C_ 120,80,81
C_ 121,82,83
Mavv 60,42,96
W_ 0
E_ 84
Mavv 80,30,96
s_nop 0
Tv 38,v4,25600
Tv 40,v4,31744
Mavv 100,34,96
E_ 85
Mavv 104,34,100
s_nop 0
Tv 42,v5,25600
Tv 44,v5,31744
Mavv 84,30,100
E_ 86
Mavv 88,30,104
E_ 87
Mavv 108,34,104
C_ 124,84,85
C_ 125,86,87
Mavv 112,34,108
E_ 88
Mavv 92,30,108
E_ 89
Mavv 96,30,112
E_ 90
Mavv 116,34,112
W_ 0
E_ 91
Mavv 136,38,112
s_nop 0
Tv 30,v2,26624
Tv 32,v2,32768
Mavv 156,42,112
C_ 128,88,89
C_ 129,90,91
Mavv 152,42,108
s_nop 0
Tv 34,v3,26624
Tv 36,v3,32768
Mavv 132,38,108
E_ 92
Mavv 128,38,104
E_ 93
Mavv 148,42,104
E_ 94
Mavv 144,42,100
E_ 95
Mavv 124,38,100
C_ 132,92,93
C_ 133,94,95
Mavv 120,38,96
v_mov_b32 v92, 0x3f803f80
v_mov_b32 v93, 0x3f803f80
Mavv 140,42,96
W_ 0
)A"
R"A(v_mov_b32 v94, 0x3f803f80
v_mov_b32 v95, 0x3f803f80
Mavv 160,30,96
s_nop 0
Tv 38,v4,26624
Tv 40,v4,32768
Mavv 180,34,96
Mavv 184,34,100
s_nop 0
Tv 42,v5,26624
Tv 44,v5,32768
Mavv 164,30,100
Mavv 168,30,104
Mavv 188,34,104
S_ s65,s61,110592
Mavv 192,34,108
S_ m0,s51,24576
Mavv 172,30,108
s_waitcnt vmcnt(6) lgkmcnt(0)
s_barrier
Mavv 176,30,112
Ln v7,32,s65,0
Mavv 196,34,112
Mavv 216,38,112
Dv 30,%0,6144
Mavv 236,42,112
Dv 34,v1,6144
Mavv 232,42,108
Ln v8,32,s65,3072
S_ m0,s51,25472
Mavv 212,38,108
Mavv 208,38,104
Ln v7,32,s65,128
Mavv 228,42,104
Mavv 224,42,100
Ln v8,32,s65,3200
S_ m0,s51,26368
Mavv 204,38,100
Mavv 200,38,96
Ln v7,32,s65,256
Mavv 220,42,96
.Lstep2_8:
Zvvv 76,92,116
Dv 38,%0,7168
Zvvv 80,92,120
Dv 42,v1,7168
Zvvv 84,92,124
Zvvv 88,92,128
Zvvv 92,92,132
W_ 2
Nvvv 96,30,136,56
Mvvv 96,34,156
Nvvv 100,30,140,60
s_nop 1
v_max3_f32 v10, v76, v80, v84
s_nop 0
v_max3_f32 v10, v10, v88, v92
Mvvv 100,34,160
v_cmp_lt_f32 vcc, 0x42000000, v10
s_cbranch_vccnz .Ltramp2_7
Nvvv 104,30,144,64
A_ v46,v46,v76
A_ v47,v47,v80
Mvvv 104,34,164
A_ v48,v48,v84
A_ v49,v49,v88
Nvvv 108,30,148,68
A_ v50,v50,v92
Mvvv 108,34,168
Ln v8,32,s65,3328
Nvvv 112,30,152,72
Mvvv 112,34,172
W_ 0
Mvvv 112,38,192
Dv 30,%0,8192
Mvvv 112,42,212
Dv 34,v1,8192
Mvvv 108,38,188
Mvvv 108,42,208
Mvvv 104,38,184
Mvvv 104,42,204
Mvvv 100,38,180
Mvvv 100,42,200
Mvvv 96,38,176
Mvvv 96,42,196
W_ 0
Mvvv 96,30,216
Dv 38,%0,0
Mvvv 96,34,236
Dv 42,v1,0
Mvvv 100,30,220
Mvvv 100,34,240
Mvvv 104,30,224
Mvvv 104,34,244
Mvvv 108,30,228
Mvvv 108,34,248
s_nop 0
E_ 96
Mvvv 112,30,232
E_ 97
Mvvv 112,34,252
W_ 0
E_ 98
Nvvv 76,38,136,56
Dv 30,%0,1024
Mvvv 76,42,156
Dv 34,v1,1024
Nvvv 80,38,140,60
E_ 99
Mvvv 80,42,160
C_ 99,98,99
C_ 98,96,97
Nvvv 84,38,144,64
E_ 100
Mvvv 84,42,164
E_ 101
Nvvv 88,38,148,68
E_ 102
Mvvv 88,42,168
E_ 103
Nvvv 92,38,152,72
C_ 103,102,103
C_ 102,100,101
Mvvv 92,42,172
W_ 0
E_ 104
Mvvv 92,30,192
Dv 38,%0,2048
Mvvv 92,34,212
Dv 42,v1,2048
Mvvv 88,30,188
E_ 105
Mvvv 88,34,208
E_ 106
Mvvv 84,30,184
S_ s65,s61,98304
E_ 107
Mvvv 84,34,204
S_ m0,s51,0
C_ 107,106,107
C_ 106,104,105
Mvvv 80,30,180
s_waitcnt vmcnt(6) lgkmcnt(0)
s_barrier
Mvvv 80,34,200
L_ v9,36,s65,0
Mvvv 76,30,176
E_ 108
Mvvv 76,34,196
L_ v9,36,s65,3072
S_ m0,s51,896
Mvvv 76,38,216
Tv 30,v2,12288
Tv 32,v2,18432
Mvvv 76,42,236
L_ v9,36,s65,128
Mvvv 80,38,220
s_nop 0
Tv 34,v3,12288
Tv 36,v3,18432
Mvvv 80,42,240
L_ v9,36,s65,3200
S_ m0,s51,1792
Mvvv 84,38,224
E_ 109
Mvvv 84,42,244
L_ v9,36,s65,256
Mvvv 88,38,228
E_ 110
Mvvv 88,42,248
L_ v9,36,s65,3328
Mvvv 92,38,232
E_ 111
Mvvv 92,42,252
W_ 0
C_ 111,110,111
C_ 110,108,109
Mavv 0,30,116
s_nop 0
Tv 38,v4,12288
Tv 40,v4,18432
Mavv 20,34,116
E_ 112
Mavv 24,34,120
s_nop 0
Tv 42,v5,12288
Tv 44,v5,18432
Mavv 4,30,120
E_ 113
Mavv 8,30,124
E_ 114
Mavv 28,34,124
E_ 115
Mavv 32,34,128
C_ 115,114,115
C_ 114,112,113
Mavv 12,30,128
E_ 76
Mavv 16,30,132
E_ 77
Mavv 36,34,132
W_ 0
E_ 78
Mavv 56,38,132
s_nop 0
Tv 30,v2,13312
Tv 32,v2,19456
Mavv 76,42,132
E_ 79
Mavv 72,42,128
s_nop 0
Tv 34,v3,13312
Tv 36,v3,19456
Mavv 52,38,128
C_ 96,76,77
C_ 97,78,79
Mavv 48,38,124
E_ 80
Mavv 68,42,124
E_ 81
Mavv 64,42,120
E_ 82
Mavv 44,38,120
E_ 83
Mavv 40,38,116
C_ 100,80,81
C_ 101,82,83
Mavv 60,42,116
W_ 0
E_ 84
Mavv 80,30,116
s_nop 0
Tv 38,v4,13312
Tv 40,v4,19456
Mavv 100,34,116
E_ 85
Mavv 104,34,120
s_nop 0
Tv 42,v5,13312
Tv 44,v5,19456
Mavv 84,30,120
E_ 86
Mavv 88,30,124
E_ 87
Mavv 108,34,124
C_ 104,84,85
C_ 105,86,87
Mavv 112,34,128
E_ 88
Mavv 92,30,128
E_ 89
Mavv 96,30,132
E_ 90
Mavv 116,34,132
W_ 0
E_ 91
Mavv 136,38,132
s_nop 0
Tv 30,v2,14336
Tv 32,v2,20480
Mavv 156,42,132
C_ 108,88,89
C_ 109,90,91
Mavv 152,42,128
s_nop 0
Tv 34,v3,14336
Tv 36,v3,20480
Mavv 132,38,128
E_ 92
Mavv 128,38,124
E_ 93
Mavv 148,42,124
E_ 94
Mavv 144,42,120
E_ 95
Mavv 124,38,120
C_ 112,92,93
C_ 113,94,95
Mavv 120,38,116
v_mov_b32 v92, 0x3f803f80
v_mov_b32 v93, 0x3f803f80
Mavv 140,42,116
W_ 0
v_mov_b32 v94, 0x3f803f80
v_mov_b32 v95, 0x3f803f80
Mavv 160,30,116
s_nop 0
Tv 38,v4,14336
Tv 40,v4,20480
Mavv 180,34,116
Mavv 184,34,120
s_nop 0
Tv 42,v5,14336
Tv 44,v5,20480
Mavv 164,30,120
Mavv 168,30,124
Mavv 188,34,124
S_ s65,s61,122880
Mavv 192,34,128
S_ m0,s51,12288
Mavv 172,30,128
s_waitcnt vmcnt(6) lgkmcnt(0)
s_barrier
Mavv 176,30,132
Ln v7,32,s65,0
Mavv 196,34,132
Mavv 216,38,132
Dv 30,%0,30720
Mavv 236,42,132
Dv 34,v1,30720
Mavv 232,42,128
Ln v8,32,s65,3072
S_ m0,s51,13184
Mavv 212,38,128
Mavv 208,38,124
Ln v7,32,s65,128
Mavv 228,42,124
Mavv 224,42,120
Ln v8,32,s65,3200
S_ m0,s51,14080
Mavv 204,38,120
Mavv 200,38,116
Ln v7,32,s65,256
Mavv 220,42,116
s_cmp_eq_u32 s4, 0
s_cbranch_scc1 .Lhs_d2
.Lhs_d2_back:
.Lstep2_9:
Zvvv 76,92,96
Dv 38,%0,31744
Zvvv 80,92,100
Dv 42,v1,31744
Zvvv 84,92,104
Zvvv 88,92,108
Zvvv 92,92,112
W_ 2
Nvvv 116,30,136,56
Mvvv 116,34,156
Nvvv 120,30,140,60
s_nop 1
v_max3_f32 v10, v76, v80, v84
s_nop 0
v_max3_f32 v10, v10, v88, v92
Mvvv 120,34,160
v_cmp_lt_f32 vcc, 0x42000000, v10
s_cbranch_vccnz .Ltramp2_8
Nvvv 124,30,144,64
A_ v46,v46,v76
A_ v47,v47,v80
Mvvv 124,34,164
A_ v48,v48,v84
A_ v49,v49,v88
Nvvv 128,30,148,68
A_ v50,v50,v92
Mvvv 128,34,168
Ln v8,32,s65,3328
Nvvv 132,30,152,72
Mvvv 132,34,172
W_ 0
Mvvv 132,38,192
Dv 30,%0,32768
Mvvv 132,42,212
Dv 34,v1,32768
Mvvv 128,38,188
Mvvv 128,42,208
Mvvv 124,38,184
Mvvv 124,42,204
Mvvv 120,38,180
Mvvv 120,42,200
Mvvv 116,38,176
Mvvv 116,42,196
W_ 0
Mvvv 116,30,216
Dv 38,%0,24576
Mvvv 116,34,236
Dv 42,v1,24576
Mvvv 120,30,220
Mvvv 120,34,240
Mvvv 124,30,224
Mvvv 124,34,244
Mvvv 128,30,228
Mvvv 128,34,248
s_nop 0
E_ 116
Mvvv 132,30,232
E_ 117
Mvvv 132,34,252
W_ 0
E_ 118
Nvvv 76,38,136,56
Dv 30,%0,25600
Mvvv 76,42,156
Dv 34,v1,25600
Nvvv 80,38,140,60
E_ 119
Mvvv 80,42,160
C_ 119,118,119
C_ 118,116,117
Nvvv 84,38,144,64
E_ 120
Mvvv 84,42,164
E_ 121
Nvvv 88,38,148,68
E_ 122
Mvvv 88,42,168
E_ 123
Nvvv 92,38,152,72
C_ 123,122,123
C_ 122,120,121
Mvvv 92,42,172
W_ 0
E_ 124
Mvvv 92,30,192
Dv 38,%0,26624
Mvvv 92,34,212
Dv 42,v1,26624
Mvvv 88,30,188
E_ 125
Mvvv 88,34,208
E_ 126
Mvvv 84,30,184
S_ s65,s61,110592
E_ 127
Mvvv 84,34,204
S_ m0,s51,24576
C_ 127,126,127
C_ 126,124,125
Mvvv 80,30,180
s_waitcnt vmcnt(6) lgkmcnt(0)
s_barrier
Mvvv 80,34,200
Ln v9,36,s65,0
Mvvv 76,30,176
E_ 128
Mvvv 76,34,196
Ln v9,36,s65,3072
S_ m0,s51,25472
Mvvv 76,38,216
Tv 30,v2,0
Tv 32,v2,6144
Mvvv 76,42,236
Ln v9,36,s65,128
Mvvv 80,38,220
s_nop 0
Tv 34,v3,0
Tv 36,v3,6144
Mvvv 80,42,240
Ln v9,36,s65,3200
S_ m0,s51,26368
Mvvv 84,38,224
E_ 129
Mvvv 84,42,244
Ln v9,36,s65,256
Mvvv 88,38,228
E_ 130
Mvvv 88,42,248
Ln v9,36,s65,3328
Mvvv 92,38,232
E_ 131
Mvvv 92,42,252
W_ 0
C_ 131,130,131
C_ 130,128,129
Mavv 0,30,96
.Lpad_a2_5:
s_cbranch_execz .Lhop_a2_r
Tv 38,v4,0
Tv 40,v4,6144
Mavv 20,34,96
E_ 132
Mavv 24,34,100
s_nop 0
Tv 42,v5,0
Tv 44,v5,6144
Mavv 4,30,100
E_ 133
Mavv 8,30,104
E_ 134
Mavv 28,34,104
E_ 135
Mavv 32,34,108
C_ 135,134,135
C_ 134,132,133
Mavv 12,30,108
E_ 76
Mavv 16,30,112
E_ 77
Mavv 36,34,112
W_ 0
E_ 78
Mavv 56,38,112
s_nop 0
Tv 30,v2,1024
Tv 32,v2,7168
Mavv 76,42,112
E_ 79
Mavv 72,42,108
s_nop 0
Tv 34,v3,1024
Tv 36,v3,7168
Mavv 52,38,108
C_ 116,76,77
C_ 117,78,79
Mavv 48,38,104
E_ 80
Mavv 68,42,104
E_ 81
Mavv 64,42,100
E_ 82
Mavv 44,38,100
E_ 83
Mavv 40,38,96
C_ 120,80,81
C_ 121,82,83
Mavv 60,42,96
W_ 0
E_ 84
Mavv 80,30,96
s_nop 0
Tv 38,v4,1024
Tv 40,v4,7168
Mavv 100,34,96
E_ 85
Mavv 104,34,100
s_nop 0
Tv 42,v5,1024
Tv 44,v5,7168
Mavv 84,30,100
E_ 86
Mavv 88,30,104
E_ 87
Mavv 108,34,104
C_ 124,84,85
C_ 125,86,87
Mavv 112,34,108
E_ 88
Mavv 92,30,108
E_ 89
Mavv 96,30,112
E_ 90
Mavv 116,34,112
W_ 0
E_ 91
Mavv 136,38,112
s_nop 0
Tv 30,v2,2048
Tv 32,v2,8192
Mavv 156,42,112
C_ 128,88,89
C_ 129,90,91
Mavv 152,42,108
s_nop 0
Tv 34,v3,2048
Tv 36,v3,8192
Mavv 132,38,108
E_ 92
Mavv 128,38,104
E_ 93
Mavv 148,42,104
E_ 94
Mavv 144,42,100
E_ 95
Mavv 124,38,100
C_ 132,92,93
C_ 133,94,95
Mavv 120,38,96
v_mov_b32 v92, 0x3f803f80
v_mov_b32 v93, 0x3f803f80
Mavv 140,42,96
W_ 0
v_mov_b32 v94, 0x3f803f80
v_mov_b32 v95, 0x3f803f80
Mavv 160,30,96
s_nop 0
Tv 38,v4,2048
Tv 40,v4,8192
Mavv 180,34,96
Mavv 184,34,100
s_nop 0
Tv 42,v5,2048
Tv 44,v5,8192
Mavv 164,30,100
Mavv 168,30,104
Mavv 188,34,104
S_ s65,s61,135168
Mavv 192,34,108
S_ m0,s51,0
Mavv 172,30,108
s_waitcnt vmcnt(6) lgkmcnt(0)
s_barrier
Mavv 176,30,112
Ln v7,32,s65,0
Mavv 196,34,112
Mavv 216,38,112
Dv 30,%0,18432
Mavv 236,42,112
Dv 34,v1,18432
Mavv 232,42,108
Ln v8,32,s65,3072
S_ m0,s51,896
Mavv 212,38,108
Mavv 208,38,104
Ln v7,32,s65,128
Mavv 228,42,104
Mavv 224,42,100
Ln v8,32,s65,3200
S_ m0,s51,1792
Mavv 204,38,100
Mavv 200,38,96
Ln v7,32,s65,256
Mavv 220,42,96
.Lstep2_10:
Zvvv 76,92,116
Dv 38,%0,19456
Zvvv 80,92,120
Dv 42,v1,19456
Zvvv 84,92,124
Zvvv 88,92,128
Zvvv 92,92,132
W_ 2
Nvvv 96,30,136,56
Mvvv 96,34,156
Nvvv 100,30,140,60
s_nop 1
v_max3_f32 v10, v76, v80, v84
s_nop 0
v_max3_f32 v10, v10, v88, v92
Mvvv 100,34,160
v_cmp_lt_f32 vcc, 0x42000000, v10
s_cbranch_vccnz .Ltramp2_9
Nvvv 104,30,144,64
A_ v46,v46,v76
A_ v47,v47,v80
Mvvv 104,34,164
A_ v48,v48,v84
A_ v49,v49,v88
Nvvv 108,30,148,68
A_ v50,v50,v92
Mvvv 108,34,168
Ln v8,32,s65,3328
Nvvv 112,30,152,72
Mvvv 112,34,172
W_ 0
Mvvv 112,38,192
Dv 30,%0,20480
Mvvv 112,42,212
Dv 34,v1,20480
Mvvv 108,38,188
Mvvv 108,42,208
Mvvv 104,38,184
Mvvv 104,42,204
Mvvv 100,38,180
Mvvv 100,42,200
Mvvv 96,38,176
Mvvv 96,42,196
W_ 0
Mvvv 96,30,216
Dv 38,%0,12288
Mvvv 96,34,236
Dv 42,v1,12288
Mvvv 100,30,220
Mvvv 100,34,240
Mvvv 104,30,224
Mvvv 104,34,244
Mvvv 108,30,228
Mvvv 108,34,248
s_nop 0
E_ 96
Mvvv 112,30,232
E_ 97
Mvvv 112,34,252
W_ 0
E_ 98
Nvvv 76,38,136,56
Dv 30,%0,13312
Mvvv 76,42,156
Dv 34,v1,13312
Nvvv 80,38,140,60
E_ 99
Mvvv 80,42,160
C_ 99,98,99
C_ 98,96,97
Nvvv 84,38,144,64
E_ 100
Mvvv 84,42,164
E_ 101
Nvvv 88,38,148,68
E_ 102
Mvvv 88,42,168
E_ 103
Nvvv 92,38,152,72
C_ 103,102,103
C_ 102,100,101
Mvvv 92,42,172
W_ 0
E_ 104
Mvvv 92,30,192
Dv 38,%0,14336
Mvvv 92,34,212
Dv 42,v1,14336
Mvvv 88,30,188
E_ 105
Mvvv 88,34,208
E_ 106
Mvvv 84,30,184
S_ s65,s61,122880
E_ 107
Mvvv 84,34,204
S_ m0,s51,12288
C_ 107,106,107
C_ 106,104,105
Mvvv 80,30,180
s_waitcnt vmcnt(6) lgkmcnt(0)
s_barrier
Mvvv 80,34,200
Ln v9,36,s65,0
Mvvv 76,30,176
E_ 108
Mvvv 76,34,196
Ln v9,36,s65,3072
S_ m0,s51,13184
Mvvv 76,38,216
Tv 30,v2,24576
Tv 32,v2,30720
Mvvv 76,42,236
Ln v9,36,s65,128
Mvvv 80,38,220
s_nop 0
Tv 34,v3,24576
Tv 36,v3,30720
Mvvv 80,42,240
Ln v9,36,s65,3200
S_ m0,s51,14080
Mvvv 84,38,224
E_ 109
Mvvv 84,42,244
Ln v9,36,s65,256
Mvvv 88,38,228
E_ 110
Mvvv 88,42,248
Ln v9,36,s65,3328
Mvvv 92,38,232
E_ 111
Mvvv 92,42,252
W_ 0
C_ 111,110,111
C_ 110,108,109
Mavv 0,30,116
s_nop 0
Tv 38,v4,24576
Tv 40,v4,30720
Mavv 20,34,116
E_ 112
Mavv 24,34,120
s_nop 0
Tv 42,v5,24576
Tv 44,v5,30720
Mavv 4,30,120
E_ 113
Mavv 8,30,124
E_ 114
Mavv 28,34,124
E_ 115
Mavv 32,34,128
C_ 115,114,115
C_ 114,112,113
Mavv 12,30,128
E_ 76
Mavv 16,30,132
E_ 77
Mavv 36,34,132
W_ 0
E_ 78
Mavv 56,38,132
s_nop 0
Tv 30,v2,25600
Tv 32,v2,31744
Mavv 76,42,132
E_ 79
Mavv 72,42,128
s_nop 0
Tv 34,v3,25600
Tv 36,v3,31744
Mavv 52,38,128
C_ 96,76,77
C_ 97,78,79
Mavv 48,38,124
E_ 80
Mavv 68,42,124
E_ 81
Mavv 64,42,120
E_ 82
Mavv 44,38,120
E_ 83
Mavv 40,38,116
C_ 100,80,81
C_ 101,82,83
Mavv 60,42,116
W_ 0
E_ 84
Mavv 80,30,116
s_nop 0
Tv 38,v4,25600
Tv 40,v4,31744
Mavv 100,34,116
E_ 85
Mavv 104,34,120
s_nop 0
Tv 42,v5,25600
Tv 44,v5,31744
Mavv 84,30,120
E_ 86
Mavv 88,30,124
E_ 87
Mavv 108,34,124
C_ 104,84,85
C_ 105,86,87
Mavv 112,34,128
E_ 88
Mavv 92,30,128
E_ 89
Mavv 96,30,132
E_ 90
Mavv 116,34,132
W_ 0
E_ 91
Mavv 136,38,132
s_nop 0
Tv 30,v2,26624
Tv 32,v2,32768
Mavv 156,42,132
C_ 108,88,89
C_ 109,90,91
Mavv 152,42,128
s_nop 0
Tv 34,v3,26624
Tv 36,v3,32768
Mavv 132,38,128
E_ 92
Mavv 128,38,124
E_ 93
Mavv 148,42,124
E_ 94
Mavv 144,42,120
E_ 95
Mavv 124,38,120
C_ 112,92,93
C_ 113,94,95
Mavv 120,38,116
v_mov_b32 v92, 0x3f803f80
v_mov_b32 v93, 0x3f803f80
Mavv 140,42,116
W_ 0
v_mov_b32 v94, 0x3f803f80
v_mov_b32 v95, 0x3f803f80
Mavv 160,30,116
s_nop 0
Tv 38,v4,26624
Tv 40,v4,32768
Mavv 180,34,116
Mavv 184,34,120
s_nop 0
Tv 42,v5,26624
Tv 44,v5,32768
Mavv 164,30,120
Mavv 168,30,124
Mavv 188,34,124
S_ s65,s61,147456
Mavv 192,34,128
S_ m0,s51,24576
Mavv 172,30,128
s_waitcnt vmcnt(6) lgkmcnt(0)
s_barrier
Mavv 176,30,132
Ln v7,32,s65,0
Mavv 196,34,132
Mavv 216,38,132
Dv 30,%0,6144
Mavv 236,42,132
Dv 34,v1,6144
Mavv 232,42,128
Ln v8,32,s65,3072
S_ m0,s51,25472
Mavv 212,38,128
Mavv 208,38,124
Ln v7,32,s65,128
Mavv 228,42,124
Mavv 224,42,120
Ln v8,32,s65,3200
S_ m0,s51,26368
Mavv 204,38,120
Mavv 200,38,116
Ln v7,32,s65,256
Mavv 220,42,116
.Lstep2_11:
Zvvv 76,92,96
Dv 38,%0,7168
Zvvv 80,92,100
Dv 42,v1,7168
Zvvv 84,92,104
Zvvv 88,92,108
Zvvv 92,92,112
W_ 2
Nvvv 116,30,136,56
Mvvv 116,34,156
Nvvv 120,30,140,60
s_nop 1
v_max3_f32 v10, v76, v80, v84
s_nop 0
v_max3_f32 v10, v10, v88, v92
Mvvv 120,34,160
v_cmp_lt_f32 vcc, 0x42000000, v10
s_cbranch_vccnz .Ltramp2_10
Nvvv 124,30,144,64
A_ v46,v46,v76
A_ v47,v47,v80
Mvvv 124,34,164
A_ v48,v48,v84
A_ v49,v49,v88
Nvvv 128,30,148,68
A_ v50,v50,v92
Mvvv 128,34,168
Ln v8,32,s65,3328
Nvvv 132,30,152,72
Mvvv 132,34,172
W_ 0
Mvvv 132,38,192
Dv 30,%0,8192
Mvvv 132,42,212
Dv 34,v1,8192
Mvvv 128,38,188
Mvvv 128,42,208
Mvvv 124,38,184
Mvvv 124,42,204
Mvvv 120,38,180
Mvvv 120,42,200
Mvvv 116,38,176
Mvvv 116,42,196
W_ 0
Mvvv 116,30,216
Dv 38,%0,0
Mvvv 116,34,236
Dv 42,v1,0
Mvvv 120,30,220
Mvvv 120,34,240
Mvvv 124,30,224
Mvvv 124,34,244
Mvvv 128,30,228
Mvvv 128,34,248
s_nop 0
E_ 116
Mvvv 132,30,232
E_ 117
Mvvv 132,34,252
W_ 0
E_ 118
Nvvv 76,38,136,56
Dv 30,%0,1024
Mvvv 76,42,156
Dv 34,v1,1024
Nvvv 80,38,140,60
E_ 119
Mvvv 80,42,160
C_ 119,118,119
C_ 118,116,117
Nvvv 84,38,144,64
E_ 120
Mvvv 84,42,164
E_ 121
Nvvv 88,38,148,68
E_ 122
Mvvv 88,42,168
E_ 123
Nvvv 92,38,152,72
C_ 123,122,123
C_ 122,120,121
Mvvv 92,42,172
W_ 0
E_ 124
Mvvv 92,30,192
Dv 38,%0,2048
Mvvv 92,34,212
Dv 42,v1,2048
Mvvv 88,30,188
E_ 125
Mvvv 88,34,208
E_ 126
Mvvv 84,30,184
S_ s65,s61,135168
E_ 127
Mvvv 84,34,204
S_ m0,s51,0
C_ 127,126,127
C_ 126,124,125
Mvvv 80,30,180
s_waitcnt vmcnt(6) lgkmcnt(0)
s_barrier
Mvvv 80,34,200
Ln v9,36,s65,0
Mvvv 76,30,176
E_ 128
Mvvv 76,34,196
Ln v9,36,s65,3072
S_ m0,s51,896
Mvvv 76,38,216
Tv 30,v2,12288
Tv 32,v2,18432
Mvvv 76,42,236
Ln v9,36,s65,128
Mvvv 80,38,220
s_nop 0
Tv 34,v3,12288
Tv 36,v3,18432
Mvvv 80,42,240
Ln v9,36,s65,3200
S_ m0,s51,1792
Mvvv 84,38,224
E_ 129
Mvvv 84,42,244
Ln v9,36,s65,256
Mvvv 88,38,228
E_ 130
Mvvv 88,42,248
Ln v9,36,s65,3328
Mvvv 92,38,232
E_ 131
Mvvv 92,42,252
W_ 0
C_ 131,130,131
C_ 130,128,129
Mavv 0,30,96
s_nop 0
Tv 38,v4,12288
Tv 40,v4,18432
Mavv 20,34,96
E_ 132
Mavv 24,34,100
s_nop 0
Tv 42,v5,12288
Tv 44,v5,18432
Mavv 4,30,100
E_ 133
Mavv 8,30,104
E_ 134
Mavv 28,34,104
E_ 135
Mavv 32,34,108
C_ 135,134,135
C_ 134,132,133
Mavv 12,30,108
E_ 76
Mavv 16,30,112
E_ 77
Mavv 36,34,112
W_ 0
E_ 78
Mavv 56,38,112
s_nop 0
Tv 30,v2,13312
Tv 32,v2,19456
Mavv 76,42,112
.Lpad_a2_6:
s_cbranch_execz .Lhop_a2_r
E_ 79
Mavv 72,42,108
s_nop 0
Tv 34,v3,13312
Tv 36,v3,19456
Mavv 52,38,108
C_ 116,76,77
C_ 117,78,79
Mavv 48,38,104
E_ 80
Mavv 68,42,104
E_ 81
Mavv 64,42,100
E_ 82
Mavv 44,38,100
E_ 83
Mavv 40,38,96
C_ 120,80,81
C_ 121,82,83
Mavv 60,42,96
W_ 0
E_ 84
Mavv 80,30,96
s_nop 0
Tv 38,v4,13312
Tv 40,v4,19456
Mavv 100,34,96
E_ 85
Mavv 104,34,100
s_nop 0
Tv 42,v5,13312
Tv 44,v5,19456
Mavv 84,30,100
E_ 86
Mavv 88,30,104
E_ 87
Mavv 108,34,104
C_ 124,84,85
C_ 125,86,87
Mavv 112,34,108
E_ 88
Mavv 92,30,108
E_ 89
Mavv 96,30,112
E_ 90
Mavv 116,34,112
W_ 0
E_ 91
Mavv 136,38,112
s_nop 0
Tv 30,v2,14336
Tv 32,v2,20480
Mavv 156,42,112
C_ 128,88,89
C_ 129,90,91
Mavv 152,42,108
s_nop 0
Tv 34,v3,14336
Tv 36,v3,20480
Mavv 132,38,108
E_ 92
Mavv 128,38,104
E_ 93
Mavv 148,42,104
E_ 94
Mavv 144,42,100
E_ 95
Mavv 124,38,100
C_ 132,92,93
C_ 133,94,95
Mavv 120,38,96
v_mov_b32 v92, 0x3f803f80
v_mov_b32 v93, 0x3f803f80
Mavv 140,42,96
W_ 0
v_mov_b32 v94, 0x3f803f80
v_mov_b32 v95, 0x3f803f80
Mavv 160,30,96
s_nop 0
Tv 38,v4,14336
Tv 40,v4,20480
Mavv 180,34,96
Mavv 184,34,100
s_nop 0
Tv 42,v5,14336
Tv 44,v5,20480
Mavv 164,30,100
Mavv 168,30,104
Mavv 188,34,104
S_ s65,s61,159744
Mavv 192,34,108
S_ m0,s51,12288
Mavv 172,30,108
s_waitcnt vmcnt(6) lgkmcnt(0)
s_barrier
Mavv 176,30,112
Ln v7,32,s65,0
Mavv 196,34,112
Mavv 216,38,112
Dv 30,%0,30720
Mavv 236,42,112
Dv 34,v1,30720
Mavv 232,42,108
Ln v8,32,s65,3072
S_ m0,s51,13184
Mavv 212,38,108
Mavv 208,38,104
Ln v7,32,s65,128
Mavv 228,42,104
Mavv 224,42,100
Ln v8,32,s65,3200
S_ m0,s51,14080
Mavv 204,38,100
Mavv 200,38,96
Ln v7,32,s65,256
Mavv 220,42,96
.Lstep2_12:
Zvvv 76,92,116
Dv 38,%0,31744
Zvvv 80,92,120
Dv 42,v1,31744
Zvvv 84,92,124
Zvvv 88,92,128
Zvvv 92,92,132
W_ 2
Nvvv 96,30,136,56
Mvvv 96,34,156
Nvvv 100,30,140,60
s_nop 1
v_max3_f32 v10, v76, v80, v84
s_nop 0
v_max3_f32 v10, v10, v88, v92
Mvvv 100,34,160
v_cmp_lt_f32 vcc, 0x42000000, v10
s_cbranch_vccnz .Ltramp2_11
Nvvv 104,30,144,64
A_ v46,v46,v76
A_ v47,v47,v80
Mvvv 104,34,164
A_ v48,v48,v84
A_ v49,v49,v88
Nvvv 108,30,148,68
A_ v50,v50,v92
Mvvv 108,34,168
Ln v8,32,s65,3328
Nvvv 112,30,152,72
R_ v11,a248
Mvvv 112,34,172
W_ 0
v_min_u32 v11, 240, v11
Mvvv 112,38,192
Dv 30,%0,32768
v_lshlrev_b32 v11, 3, v11
Mvvv 112,42,212
Dv 34,v1,32768
Mvvv 108,38,188
buffer_load_dword a254, v11, s[44:47], s66 offen
Mvvv 108,42,208
Mvvv 104,38,184
Mvvv 104,42,204
Mvvv 100,38,180
Mvvv 100,42,200
Mvvv 96,38,176
Mvvv 96,42,196
W_ 0
Mvvv 96,30,216
Dv 38,%0,24576
Mvvv 96,34,236
Dv 42,v1,24576
Mvvv 100,30,220
Mvvv 100,34,240
Mvvv 104,30,224
Mvvv 104,34,244
Mvvv 108,30,228
Mvvv 108,34,248
s_nop 0
E_ 96
Mvvv 112,30,232
E_ 97
Mvvv 112,34,252
W_ 0
E_ 98
Nvvv 76,38,136,56
Dv 30,%0,25600
Mvvv 76,42,156
Dv 34,v1,25600
Nvvv 80,38,140,60
E_ 99
Mvvv 80,42,160
C_ 99,98,99
C_ 98,96,97
Nvvv 84,38,144,64
E_ 100
Mvvv 84,42,164
E_ 101
Nvvv 88,38,148,68
E_ 102
Mvvv 88,42,168
E_ 103
Nvvv 92,38,152,72
C_ 103,102,103
C_ 102,100,101
Mvvv 92,42,172
W_ 0
E_ 104
Mvvv 92,30,192
Dv 38,%0,26624
Mvvv 92,34,212
Dv 42,v1,26624
Mvvv 88,30,188
E_ 105
Mvvv 88,34,208
E_ 106
Mvvv 84,30,184
S_ s65,s61,147456
E_ 107
Mvvv 84,34,204
S_ m0,s51,24576
C_ 107,106,107
C_ 106,104,105
Mvvv 80,30,180
s_waitcnt vmcnt(7) lgkmcnt(0)
s_barrier
Mvvv 80,34,200
Ln v9,36,s65,0
Mvvv 76,30,176
E_ 108
Mvvv 76,34,196
Ln v9,36,s65,3072
S_ m0,s51,25472
Mvvv 76,38,216
Tv 30,v2,0
Tv 32,v2,6144
Mvvv 76,42,236
Ln v9,36,s65,128
Mvvv 80,38,220
s_nop 0
Tv 34,v3,0
Tv 36,v3,6144
Mvvv 80,42,240
Ln v9,36,s65,3200
S_ m0,s51,26368
Mvvv 84,38,224
E_ 109
Mvvv 84,42,244
Ln v9,36,s65,256
Mvvv 88,38,228
E_ 110
Mvvv 88,42,248
Ln v9,36,s65,3328
Mvvv 92,38,232
E_ 111
Mvvv 92,42,252
W_ 0
C_ 111,110,111
C_ 110,108,109
Mavv 0,30,116
s_nop 0
Tv 38,v4,0
Tv 40,v4,6144
Mavv 20,34,116
E_ 112
Mavv 24,34,120
s_nop 0
Tv 42,v5,0
Tv 44,v5,6144
Mavv 4,30,120
E_ 113
Mavv 8,30,124
E_ 114
Mavv 28,34,124
E_ 115
Mavv 32,34,128
C_ 115,114,115
C_ 114,112,113
Mavv 12,30,128
E_ 76
Mavv 16,30,132
E_ 77
Mavv 36,34,132
W_ 0
E_ 78
Mavv 56,38,132
s_nop 0
Tv 30,v2,1024
Tv 32,v2,7168
Mavv 76,42,132
E_ 79
Mavv 72,42,128
s_nop 0
Tv 34,v3,1024
Tv 36,v3,7168
Mavv 52,38,128
C_ 96,76,77
C_ 97,78,79
Mavv 48,38,124
E_ 80
Mavv 68,42,124
E_ 81
Mavv 64,42,120
E_ 82
Mavv 44,38,120
E_ 83
Mavv 40,38,116
C_ 100,80,81
C_ 101,82,83
Mavv 60,42,116
W_ 0
E_ 84
Mavv 80,30,116
s_nop 0
Tv 38,v4,1024
Tv 40,v4,7168
Mavv 100,34,116
E_ 85
Mavv 104,34,120
s_nop 0
Tv 42,v5,1024
Tv 44,v5,7168
Mavv 84,30,120
E_ 86
Mavv 88,30,124
E_ 87
Mavv 108,34,124
C_ 104,84,85
C_ 105,86,87
Mavv 112,34,128
E_ 88
Mavv 92,30,128
E_ 89
Mavv 96,30,132
E_ 90
Mavv 116,34,132
W_ 0
E_ 91
Mavv 136,38,132
s_nop 0
Tv 30,v2,2048
Tv 32,v2,8192
Mavv 156,42,132
C_ 108,88,89
C_ 109,90,91
Mavv 152,42,128
s_nop 0
Tv 34,v3,2048
Tv 36,v3,8192
Mavv 132,38,128
E_ 92
Mavv 128,38,124
E_ 93
Mavv 148,42,124
E_ 94
Mavv 144,42,120
E_ 95
Mavv 124,38,120
C_ 112,92,93
C_ 113,94,95
Mavv 120,38,116
v_mov_b32 v92, 0x3f803f80
v_mov_b32 v93, 0x3f803f80
Mavv 140,42,116
W_ 0
v_mov_b32 v94, 0x3f803f80
v_mov_b32 v95, 0x3f803f80
Mavv 160,30,116
s_nop 0
Tv 38,v4,2048
Tv 40,v4,8192
Mavv 180,34,116
Mavv 184,34,120
s_nop 0
Tv 42,v5,2048
Tv 44,v5,8192
Mavv 164,30,120
Mavv 168,30,124
Mavv 188,34,124
S_ s65,s61,172032
Mavv 192,34,128
S_ m0,s51,0
Mavv 172,30,128
s_waitcnt vmcnt(7) lgkmcnt(0)
s_barrier
Mavv 176,30,132
Ln v7,32,s65,0
Mavv 196,34,132
Mavv 216,38,132
Dv 30,%0,18432
Mavv 236,42,132
Dv 34,v1,18432
Mavv 232,42,128
Ln v8,32,s65,3072
S_ m0,s51,896
Mavv 212,38,128
Mavv 208,38,124
Ln v7,32,s65,128
Mavv 228,42,124
Mavv 224,42,120
Ln v8,32,s65,3200
S_ m0,s51,1792
Mavv 204,38,120
Mavv 200,38,116
Ln v7,32,s65,256
Mavv 220,42,116
.Lstep2_13:
Zvvv 76,92,96
Dv 38,%0,19456
Zvvv 80,92,100
Dv 42,v1,19456
Zvvv 84,92,104
Zvvv 88,92,108
Zvvv 92,92,112
W_ 2
Nvvv 116,30,136,56
Mvvv 116,34,156
Nvvv 120,30,140,60
s_nop 1
v_max3_f32 v10, v76, v80, v84
s_nop 0
v_max3_f32 v10, v10, v88, v92
Mvvv 120,34,160
v_cmp_lt_f32 vcc, 0x42000000, v10
s_cbranch_vccnz .Ltramp2_12
Nvvv 124,30,144,64
A_ v46,v46,v76
A_ v47,v47,v80
Mvvv 124,34,164
A_ v48,v48,v84
A_ v49,v49,v88
Nvvv 128,30,148,68
A_ v50,v50,v92
Mvvv 128,34,168
Ln v8,32,s65,3328
Nvvv 132,30,152,72
Mvvv 132,34,172
W_ 0
Mvvv 132,38,192
Dv 30,%0,20480
Mvvv 132,42,212
Dv 34,v1,20480
Mvvv 128,38,188
Mvvv 128,42,208
Mvvv 124,38,184
Mvvv 124,42,204
Mvvv 120,38,180
Mvvv 120,42,200
Mvvv 116,38,176
Mvvv 116,42,196
W_ 0
Mvvv 116,30,216
Dv 38,%0,12288
Mvvv 116,34,236
Dv 42,v1,12288
Mvvv 120,30,220
Mvvv 120,34,240
Mvvv 124,30,224
Mvvv 124,34,244
Mvvv 128,30,228
Mvvv 128,34,248
s_nop 0
E_ 116
Mvvv 132,30,232
E_ 117
Mvvv 132,34,252
W_ 0
E_ 118
Nvvv 76,38,136,56
Dv 30,%0,13312
Mvvv 76,42,156
Dv 34,v1,13312
Nvvv 80,38,140,60
E_ 119
Mvvv 80,42,160
C_ 119,118,119
C_ 118,116,117
Nvvv 84,38,144,64
E_ 120
Mvvv 84,42,164
E_ 121
Nvvv 88,38,148,68
E_ 122
Mvvv 88,42,168
E_ 123
Nvvv 92,38,152,72
C_ 123,122,123
C_ 122,120,121
Mvvv 92,42,172
W_ 0
E_ 124
Mvvv 92,30,192
Dv 38,%0,14336
Mvvv 92,34,212
Dv 42,v1,14336
Mvvv 88,30,188
E_ 125
Mvvv 88,34,208
E_ 126
Mvvv 84,30,184
S_ s65,s61,159744
E_ 127
Mvvv 84,34,204
S_ m0,s51,12288
C_ 127,126,127
C_ 126,124,125
Mvvv 80,30,180
s_waitcnt vmcnt(6) lgkmcnt(0)
s_barrier
Mvvv 80,34,200
Ln v9,36,s65,0
Mvvv 76,30,176
E_ 128
Mvvv 76,34,196
Ln v9,36,s65,3072
S_ m0,s51,13184
Mvvv 76,38,216
Tv 30,v2,24576
Tv 32,v2,30720
Mvvv 76,42,236
Ln v9,36,s65,128
Mvvv 80,38,220
s_nop 0
Tv 34,v3,24576
Tv 36,v3,30720
Mvvv 80,42,240
Ln v9,36,s65,3200
S_ m0,s51,14080
Mvvv 84,38,224
E_ 129
Mvvv 84,42,244
Ln v9,36,s65,256
Mvvv 88,38,228
E_ 130
Mvvv 88,42,248
Ln v9,36,s65,3328
Mvvv 92,38,232
E_ 131
Mvvv 92,42,252
W_ 0
C_ 131,130,131
C_ 130,128,129
Mavv 0,30,96
s_nop 0
Tv 38,v4,24576
Tv 40,v4,30720
Mavv 20,34,96
E_ 132
Mavv 24,34,100
s_nop 0
Tv 42,v5,24576
Tv 44,v5,30720
Mavv 4,30,100
E_ 133
Mavv 8,30,104
E_ 134
Mavv 28,34,104
E_ 135
Mavv 32,34,108
C_ 135,134,135
C_ 134,132,133
Mavv 12,30,108
E_ 76
Mavv 16,30,112
E_ 77
Mavv 36,34,112
W_ 0
E_ 78
Mavv 56,38,112
s_nop 0
Tv 30,v2,25600
Tv 32,v2,31744
Mavv 76,42,112
E_ 79
Mavv 72,42,108
s_nop 0
Tv 34,v3,25600
Tv 36,v3,31744
Mavv 52,38,108
C_ 116,76,77
C_ 117,78,79
Mavv 48,38,104
E_ 80
Mavv 68,42,104
E_ 81
Mavv 64,42,100
E_ 82
Mavv 44,38,100
E_ 83
Mavv 40,38,96
C_ 120,80,81
C_ 121,82,83
Mavv 60,42,96
W_ 0
E_ 84
Mavv 80,30,96
.Lpad_a2_7:
s_cbranch_execz .Lhop_a2_r
Tv 38,v4,25600
Tv 40,v4,31744
Mavv 100,34,96
E_ 85
Mavv 104,34,100
s_nop 0
Tv 42,v5,25600
Tv 44,v5,31744
Mavv 84,30,100
E_ 86
Mavv 88,30,104
E_ 87
Mavv 108,34,104
C_ 124,84,85
C_ 125,86,87
Mavv 112,34,108
E_ 88
Mavv 92,30,108
E_ 89
Mavv 96,30,112
E_ 90
Mavv 116,34,112
W_ 0
E_ 91
Mavv 136,38,112
s_nop 0
Tv 30,v2,26624
Tv 32,v2,32768
Mavv 156,42,112
C_ 128,88,89
C_ 129,90,91
Mavv 152,42,108
s_nop 0
Tv 34,v3,26624
Tv 36,v3,32768
Mavv 132,38,108
E_ 92
Mavv 128,38,104
E_ 93
Mavv 148,42,104
E_ 94
Mavv 144,42,100
E_ 95
Mavv 124,38,100
C_ 132,92,93
C_ 133,94,95
Mavv 120,38,96
v_mov_b32 v92, 0x3f803f80
v_mov_b32 v93, 0x3f803f80
Mavv 140,42,96
W_ 0
v_mov_b32 v94, 0x3f803f80
v_mov_b32 v95, 0x3f803f80
Mavv 160,30,96
s_nop 0
Tv 38,v4,26624
Tv 40,v4,32768
Mavv 180,34,96
Mavv 184,34,100
s_nop 0
Tv 42,v5,26624
Tv 44,v5,32768
Mavv 164,30,100
Mavv 168,30,104
Mavv 188,34,104
S_ s65,s61,184320
Mavv 192,34,108
S_ m0,s51,24576
Mavv 172,30,108
s_waitcnt vmcnt(6) lgkmcnt(0)
s_barrier
Mavv 176,30,112
Ln v7,32,s65,0
Mavv 196,34,112
Mavv 216,38,112
Dv 30,%0,6144
Mavv 236,42,112
Dv 34,v1,6144
Mavv 232,42,108
Ln v8,32,s65,3072
S_ m0,s51,25472
Mavv 212,38,108
Mavv 208,38,104
Ln v7,32,s65,128
Mavv 228,42,104
Mavv 224,42,100
Ln v8,32,s65,3200
S_ m0,s51,26368
Mavv 204,38,100
Mavv 200,38,96
Ln v7,32,s65,256
Mavv 220,42,96
.Lstep2_14:
Zvvv 76,92,116
Dv 38,%0,7168
Zvvv 80,92,120
Dv 42,v1,7168
Zvvv 84,92,124
Zvvv 88,92,128
Zvvv 92,92,132
W_ 2
Nvvv 96,30,136,56
Mvvv 96,34,156
Nvvv 100,30,140,60
s_nop 1
v_max3_f32 v10, v76, v80, v84
s_nop 0
v_max3_f32 v10, v10, v88, v92
Mvvv 100,34,160
v_cmp_lt_f32 vcc, 0x42000000, v10
s_cbranch_vccnz .Ltramp2_13
Nvvv 104,30,144,64
A_ v46,v46,v76
A_ v47,v47,v80
Mvvv 104,34,164
A_ v48,v48,v84
A_ v49,v49,v88
Nvvv 108,30,148,68
A_ v50,v50,v92
Mvvv 108,34,168
Ln v8,32,s65,3328
Nvvv 112,30,152,72
Mvvv 112,34,172
W_ 0
Mvvv 112,38,192
Dv 30,%0,8192
Mvvv 112,42,212
Dv 34,v1,8192
Mvvv 108,38,188
Mvvv 108,42,208
Mvvv 104,38,184
Mvvv 104,42,204
Mvvv 100,38,180
Mvvv 100,42,200
Mvvv 96,38,176
Mvvv 96,42,196
W_ 0
Mvvv 96,30,216
Dv 38,%0,0
Mvvv 96,34,236
Dv 42,v1,0
Mvvv 100,30,220
Mvvv 100,34,240
Mvvv 104,30,224
Mvvv 104,34,244
Mvvv 108,30,228
Mvvv 108,34,248
s_nop 0
E_ 96
Mvvv 112,30,232
E_ 97
Mvvv 112,34,252
W_ 0
E_ 98
Nvvv 76,38,136,56
Dv 30,%0,1024
Mvvv 76,42,156
Dv 34,v1,1024
Nvvv 80,38,140,60
E_ 99
Mvvv 80,42,160
C_ 99,98,99
C_ 98,96,97
Nvvv 84,38,144,64
E_ 100
Mvvv 84,42,164
E_ 101
Nvvv 88,38,148,68
E_ 102
Mvvv 88,42,168
E_ 103
Nvvv 92,38,152,72
C_ 103,102,103
C_ 102,100,101
Mvvv 92,42,172
W_ 0
E_ 104
Mvvv 92,30,192
Dv 38,%0,2048
Mvvv 92,34,212
Dv 42,v1,2048
Mvvv 88,30,188
E_ 105
Mvvv 88,34,208
E_ 106
Mvvv 84,30,184
S_ s65,s61,172032
E_ 107
Mvvv 84,34,204
S_ m0,s51,0
C_ 107,106,107
C_ 106,104,105
Mvvv 80,30,180
s_waitcnt vmcnt(6) lgkmcnt(0)
s_barrier
Mvvv 80,34,200
Ln v9,36,s65,0
Mvvv 76,30,176
E_ 108
Mvvv 76,34,196
Ln v9,36,s65,3072
S_ m0,s51,896
Mvvv 76,38,216
Tv 30,v2,12288
Tv 32,v2,18432
Mvvv 76,42,236
Ln v9,36,s65,128
Mvvv 80,38,220
s_nop 0
Tv 34,v3,12288
Tv 36,v3,18432
Mvvv 80,42,240
Ln v9,36,s65,3200
S_ m0,s51,1792
Mvvv 84,38,224
E_ 109
Mvvv 84,42,244
Ln v9,36,s65,256
Mvvv 88,38,228
E_ 110
Mvvv 88,42,248
Ln v9,36,s65,3328
Mvvv 92,38,232
E_ 111
Mvvv 92,42,252
W_ 0
C_ 111,110,111
C_ 110,108,109
Mavv 0,30,116
s_nop 0
Tv 38,v4,12288
Tv 40,v4,18432
Mavv 20,34,116
E_ 112
Mavv 24,34,120
s_nop 0
Tv 42,v5,12288
Tv 44,v5,18432
Mavv 4,30,120
E_ 113
Mavv 8,30,124
E_ 114
Mavv 28,34,124
E_ 115
Mavv 32,34,128
C_ 115,114,115
C_ 114,112,113
Mavv 12,30,128
E_ 76
Mavv 16,30,132
E_ 77
Mavv 36,34,132
W_ 0
E_ 78
Mavv 56,38,132
s_nop 0
Tv 30,v2,13312
Tv 32,v2,19456
Mavv 76,42,132
E_ 79
Mavv 72,42,128
s_nop 0
Tv 34,v3,13312
Tv 36,v3,19456
Mavv 52,38,128
C_ 96,76,77
C_ 97,78,79
Mavv 48,38,124
E_ 80
Mavv 68,42,124
E_ 81
Mavv 64,42,120
E_ 82
Mavv 44,38,120
E_ 83
Mavv 40,38,116
C_ 100,80,81
C_ 101,82,83
Mavv 60,42,116
W_ 0
E_ 84
Mavv 80,30,116
s_nop 0
Tv 38,v4,13312
Tv 40,v4,19456
Mavv 100,34,116
E_ 85
Mavv 104,34,120
s_nop 0
Tv 42,v5,13312
Tv 44,v5,19456
Mavv 84,30,120
E_ 86
Mavv 88,30,124
E_ 87
Mavv 108,34,124
C_ 104,84,85
C_ 105,86,87
Mavv 112,34,128
E_ 88
Mavv 92,30,128
E_ 89
Mavv 96,30,132
E_ 90
Mavv 116,34,132
W_ 0
E_ 91
Mavv 136,38,132
s_nop 0
Tv 30,v2,14336
Tv 32,v2,20480
Mavv 156,42,132
C_ 108,88,89
C_ 109,90,91
Mavv 152,42,128
s_nop 0
Tv 34,v3,14336
Tv 36,v3,20480
Mavv 132,38,128
E_ 92
Mavv 128,38,124
E_ 93
Mavv 148,42,124
E_ 94
Mavv 144,42,120
E_ 95
Mavv 124,38,120
C_ 112,92,93
C_ 113,94,95
Mavv 120,38,116
v_mov_b32 v92, 0x3f803f80
v_mov_b32 v93, 0x3f803f80
Mavv 140,42,116
W_ 0
v_mov_b32 v94, 0x3f803f80
v_mov_b32 v95, 0x3f803f80
Mavv 160,30,116
s_nop 0
Tv 38,v4,14336
Tv 40,v4,20480
Mavv 180,34,116
Mavv 184,34,120
)A"
R"A(s_nop 0
Tv 42,v5,14336
Tv 44,v5,20480
Mavv 164,30,120
Mavv 168,30,124
Mavv 188,34,124
S_ s65,s61,184320
Mavv 192,34,128
S_ m0,s51,12288
Mavv 172,30,128
s_waitcnt vmcnt(6) lgkmcnt(0)
s_barrier
Mavv 176,30,132
Ln v9,36,s65,0
Mavv 196,34,132
Mavv 216,38,132
Dv 30,%0,30720
Mavv 236,42,132
Dv 34,v1,30720
Mavv 232,42,128
Ln v9,36,s65,3072
S_ m0,s51,13184
Mavv 212,38,128
Mavv 208,38,124
Ln v9,36,s65,128
Mavv 228,42,124
Mavv 224,42,120
Ln v9,36,s65,3200
S_ m0,s51,14080
Mavv 204,38,120
Mavv 200,38,116
Ln v9,36,s65,256
Mavv 220,42,116
.Lstep2_15:
Zvvv 76,92,96
Dv 38,%0,31744
Zvvv 80,92,100
Dv 42,v1,31744
Zvvv 84,92,104
Zvvv 88,92,108
Zvvv 92,92,112
W_ 2
Nvvv 116,30,136,56
Mvvv 116,34,156
Nvvv 120,30,140,60
s_nop 1
v_max3_f32 v10, v76, v80, v84
s_nop 0
v_max3_f32 v10, v10, v88, v92
Mvvv 120,34,160
v_cmp_lt_f32 vcc, 0x42000000, v10
s_cbranch_vccnz .Ltramp2_14
Nvvv 124,30,144,64
A_ v46,v46,v76
A_ v47,v47,v80
Mvvv 124,34,164
A_ v48,v48,v84
A_ v49,v49,v88
Nvvv 128,30,148,68
A_ v50,v50,v92
Mvvv 128,34,168
Ln v9,36,s65,3328
Nvvv 132,30,152,72
Mvvv 132,34,172
W_ 0
Mvvv 132,38,192
Dv 30,%0,32768
Mvvv 132,42,212
Dv 34,v1,32768
Mvvv 128,38,188
Mvvv 128,42,208
Mvvv 124,38,184
Mvvv 124,42,204
Mvvv 120,38,180
Mvvv 120,42,200
Mvvv 116,38,176
Mvvv 116,42,196
W_ 0
Mvvv 116,30,216
Dv 38,%0,24576
Mvvv 116,34,236
Dv 42,v1,24576
Mvvv 120,30,220
Mvvv 120,34,240
Mvvv 124,30,224
Mvvv 124,34,244
Mvvv 128,30,228
Mvvv 128,34,248
s_nop 0
E_ 116
Mvvv 132,30,232
E_ 117
Mvvv 132,34,252
W_ 0
E_ 118
Nvvv 76,38,136,56
Dv 30,%0,25600
Mvvv 76,42,156
Dv 34,v1,25600
Nvvv 80,38,140,60
E_ 119
Mvvv 80,42,160
C_ 119,118,119
C_ 118,116,117
Nvvv 84,38,144,64
E_ 120
Mvvv 84,42,164
E_ 121
Nvvv 88,38,148,68
E_ 122
Mvvv 88,42,168
E_ 123
Nvvv 92,38,152,72
C_ 123,122,123
C_ 122,120,121
Mvvv 92,42,172
W_ 0
E_ 124
Mvvv 92,30,192
Dv 38,%0,26624
Mvvv 92,34,212
Dv 42,v1,26624
Mvvv 88,30,188
E_ 125
Mvvv 88,34,208
E_ 126
Mvvv 84,30,184
S_ s65,s61,393216
E_ 127
Mvvv 84,34,204
S_ m0,s51,24576
C_ 127,126,127
C_ 126,124,125
Mvvv 80,30,180
s_waitcnt vmcnt(6) lgkmcnt(0)
s_barrier
Mvvv 80,34,200
L_ v9,36,s65,0
Mvvv 76,30,176
E_ 128
Mvvv 76,34,196
L_ v9,36,s65,3072
S_ m0,s51,25472
Mvvv 76,38,216
Tv 30,v2,0
Tv 32,v2,6144
Mvvv 76,42,236
L_ v9,36,s65,128
Mvvv 80,38,220
s_nop 0
Tv 34,v3,0
Tv 36,v3,6144
Mvvv 80,42,240
L_ v9,36,s65,3200
S_ m0,s51,26368
Mvvv 84,38,224
E_ 129
Mvvv 84,42,244
L_ v9,36,s65,256
Mvvv 88,38,228
E_ 130
Mvvv 88,42,248
L_ v9,36,s65,3328
Mvvv 92,38,232
E_ 131
Mvvv 92,42,252
W_ 0
C_ 131,130,131
C_ 130,128,129
Mavv 0,30,96
s_nop 0
Tv 38,v4,0
Tv 40,v4,6144
Mavv 20,34,96
E_ 132
Mavv 24,34,100
s_nop 0
Tv 42,v5,0
Tv 44,v5,6144
Mavv 4,30,100
E_ 133
Mavv 8,30,104
E_ 134
Mavv 28,34,104
E_ 135
Mavv 32,34,108
C_ 135,134,135
C_ 134,132,133
Mavv 12,30,108
E_ 76
Mavv 16,30,112
E_ 77
Mavv 36,34,112
W_ 0
E_ 78
Mavv 56,38,112
s_nop 0
Tv 30,v2,1024
Tv 32,v2,7168
Mavv 76,42,112
E_ 79
Mavv 72,42,108
s_nop 0
Tv 34,v3,1024
Tv 36,v3,7168
Mavv 52,38,108
C_ 116,76,77
C_ 117,78,79
Mavv 48,38,104
E_ 80
Mavv 68,42,104
E_ 81
Mavv 64,42,100
E_ 82
Mavv 44,38,100
E_ 83
Mavv 40,38,96
C_ 120,80,81
C_ 121,82,83
Mavv 60,42,96
W_ 0
E_ 84
Mavv 80,30,96
s_nop 0
Tv 38,v4,1024
Tv 40,v4,7168
Mavv 100,34,96
E_ 85
Mavv 104,34,100
s_nop 0
Tv 42,v5,1024
Tv 44,v5,7168
Mavv 84,30,100
E_ 86
Mavv 88,30,104
E_ 87
Mavv 108,34,104
C_ 124,84,85
C_ 125,86,87
Mavv 112,34,108
E_ 88
Mavv 92,30,108
E_ 89
Mavv 96,30,112
E_ 90
Mavv 116,34,112
W_ 0
E_ 91
Mavv 136,38,112
s_nop 0
Tv 30,v2,2048
Tv 32,v2,8192
Mavv 156,42,112
C_ 128,88,89
C_ 129,90,91
Mavv 152,42,108
s_nop 0
Tv 34,v3,2048
Tv 36,v3,8192
Mavv 132,38,108
E_ 92
Mavv 128,38,104
E_ 93
Mavv 148,42,104
E_ 94
Mavv 144,42,100
E_ 95
Mavv 124,38,100
C_ 132,92,93
C_ 133,94,95
Mavv 120,38,96
v_mov_b32 v92, 0x3f803f80
v_mov_b32 v93, 0x3f803f80
Mavv 140,42,96
W_ 0
v_mov_b32 v94, 0x3f803f80
v_mov_b32 v95, 0x3f803f80
Mavv 160,30,96
s_nop 0
Tv 38,v4,2048
Tv 40,v4,8192
Mavv 180,34,96
Mavv 184,34,100
s_nop 0
Tv 42,v5,2048
Tv 44,v5,8192
Mavv 164,30,100
Mavv 168,30,104
Mavv 188,34,104
S_ s65,s61,393216
Mavv 192,34,108
S_ m0,s51,0
Mavv 172,30,108
s_waitcnt vmcnt(6) lgkmcnt(0)
s_barrier
Mavv 176,30,112
Ln v7,32,s65,0
Mavv 196,34,112
Mavv 216,38,112
s_nop 0
Tv 30,v2,12288
Tv 32,v2,18432
Zvvv 76,92,116
Ln v8,32,s65,3072
S_ m0,s51,896
Zvvv 80,92,120
s_nop 0
Tv 34,v3,12288
Tv 36,v3,18432
Zvvv 84,92,124
Ln v7,32,s65,128
Zvvv 88,92,128
Zvvv 92,92,132
Ln v8,32,s65,3200
S_ m0,s51,1792
Mavv 236,42,112
Mavv 232,42,108
Ln v7,32,s65,256
Mavv 212,38,108
Mavv 208,38,104
v_max3_f32 v10, v76, v80, v84
Mavv 228,42,104
v_max3_f32 v10, v10, v88, v92
v_cmp_lt_f32 vcc, 0x42000000, v10
Mavv 224,42,100
Ln v8,32,s65,3328
Mavv 204,38,100
Mavv 200,38,96
Mavv 220,42,96
s_cbranch_vccnz .Ltramp2_15
.Ljoin2:
W_ 2
Mavv 0,30,116
s_mov_b32 s58, 1
Tv 38,v4,12288
Tv 40,v4,18432
W_ 2
Mavv 20,34,116
R_ v11,a248
R_ v13,a248
Mavv 24,34,120
S_ s53,s51,36864
Tv 42,v5,12288
Tv 44,v5,18432
Mavv 4,30,120
v_add_u32 v12, s53, v13
s_mul_i32 s53, s4, 10240
Mavv 8,30,124
v_add_u32 v13, s53, v13
A_ v46,v46,v76
Mavv 28,34,124
A_ v47,v47,v80
Mavv 32,34,128
A_ v48,v48,v84
A_ v49,v49,v88
Mavv 12,30,128
A_ v50,v50,v92
Mavv 16,30,132
v_rcp_f32 v46, v46
Mavv 36,34,132
W_ 0
v_rcp_f32 v47, v47
Mavv 56,38,132
s_nop 0
Tv 30,v2,13312
Tv 32,v2,19456
Mavv 76,42,132
v_rcp_f32 v48, v48
Mavv 72,42,128
s_nop 0
Tv 34,v3,13312
Tv 36,v3,19456
Mavv 52,38,128
v_rcp_f32 v49, v49
Mavv 48,38,124
v_rcp_f32 v50, v50
Mavv 68,42,124
s_mov_b32 s56, 12288
v_mov_b32 v176, v20
v_mov_b32 v177, v21
Mavv 64,42,120
Dv 136,v13,0
v_mov_b32 v178, v22
Mavv 44,38,120
Dv 140,v13,1024
v_mov_b32 v179, v23
Mavv 40,38,116
Dv 144,v13,2048
v_mov_b32 v180, v24
Mavv 60,42,116
W_ 3
Dv 148,v13,3072
v_mov_b32 v181, v25
Mavv 80,30,116
s_nop 0
Tv 38,v4,13312
Tv 40,v4,19456
Mavv 100,34,116
Dv 152,v13,4096
v_mov_b32 v182, v26
Mavv 104,34,120
s_nop 0
Tv 42,v5,13312
Tv 44,v5,19456
Mavv 84,30,120
Dv 156,v13,5120
v_mov_b32 v183, v27
Mavv 88,30,124
Dv 160,v13,6144
v_mov_b32 v184, v52
Mavv 108,34,124
Dv 164,v13,7168
v_mov_b32 v185, v53
Mavv 112,34,128
Dv 168,v13,8192
v_mov_b32 v186, v54
Mavv 92,30,128
Dv 172,v13,9216
v_mov_b32 v187, v55
Mavv 96,30,132
Dv 208,v12,0
R_ v188,a240
Mavv 116,34,132
W_ 6
Dv 212,v12,1024
R_ v189,a241
Mavv 136,38,132
s_nop 0
Tv 30,v2,14336
Tv 32,v2,20480
Mavv 156,42,132
Dv 216,v12,2048
R_ v190,a242
Mavv 152,42,128
s_nop 0
Tv 34,v3,14336
Tv 36,v3,20480
Mavv 132,38,128
Dv 220,v12,3072
R_ v191,a243
Mavv 128,38,124
Dv 224,v12,4096
R_ v192,a244
Mavv 148,42,124
Dv 228,v12,5120
R_ v193,a245
Mavv 144,42,120
Dv 232,v12,12288
R_ v194,a246
Mavv 124,38,120
Dv 236,v12,13312
R_ v195,a247
Mavv 120,38,116
Dv 240,v12,14336
v_mov_b32 v196, v16
Mavv 140,42,116
W_ 6
Dv 244,v12,15360
v_mov_b32 v197, v17
Mavv 160,30,116
s_nop 0
Tv 38,v4,14336
Tv 40,v4,20480
Mavv 180,34,116
Dv 248,v12,16384
v_mov_b32 v198, v18
Mavv 184,34,120
s_nop 0
Tv 42,v5,14336
Tv 44,v5,20480
Mavv 164,30,120
Dv 252,v12,17408
v_mov_b32 v199, v19
Mavv 168,30,124
v_mov_b32 v200, v28
v_mov_b32 v201, v29
Mavv 188,34,124
v_mov_b32 v202, v51
v_mov_b32 v203, v6
Mavv 192,34,128
v_mov_b32 v204, v14
v_mov_b32 v205, v15
Mavv 172,30,128
S_ s65,s61,405504
R_ v206,a249
R_ v207,a250
Mavv 176,30,132
S_ m0,s51,12288
R_ v96,a0
R_ v97,a1
Mavv 196,34,132
s_waitcnt vmcnt(63) lgkmcnt(0)
s_barrier
R_ v98,a2
Mavv 216,38,132
Ln v7,32,s65,0
Mavv 236,42,132
R_ v99,a3
Mavv 232,42,128
Ln v8,32,s65,3072
S_ m0,s51,13184
Mavv 212,38,128
R_ v100,a20
R_ v101,a21
Mavv 208,38,124
Ln v7,32,s65,128
Mavv 228,42,124
R_ v102,a22
R_ v103,a23
Mavv 224,42,120
Ln v8,32,s65,3200
S_ m0,s51,14080
Mavv 204,38,120
U_ v96,v96,v46
U_ v97,v97,v46
Mavv 200,38,116
Ln v7,32,s65,256
Mavv 220,42,116
U_ v98,v98,v46
U_ v99,v99,v46
U_ v100,v100,v46
U_ v101,v101,v46
U_ v102,v102,v46
U_ v103,v103,v46
C_ 76,96,97
C_ 77,98,99
C_ 78,100,101
C_ 79,102,103
ds_write_b128 v13, v[76:79] offset:0
Ln v8,32,s65,3328
S_ s61,s61,393216
.p2align 3
R_ v104,a4
R_ v105,a5
R_ v106,a6
R_ v107,a7
R_ v108,a24
R_ v109,a25
R_ v110,a26
R_ v111,a27
v_pk_mul_f32 v[104:105], v[104:105], v[46:47] op_sel:[0,1]
v_pk_mul_f32 v[106:107], v[106:107], v[46:47] op_sel:[0,1]
v_pk_mul_f32 v[108:109], v[108:109], v[46:47] op_sel:[0,1]
v_pk_mul_f32 v[110:111], v[110:111], v[46:47] op_sel:[0,1]
C_ 80,104,105
C_ 81,106,107
C_ 82,108,109
C_ 83,110,111
ds_write_b128 v13, v[80:83] offset:1024
.p2align 3
R_ v96,a8
R_ v97,a9
R_ v98,a10
R_ v99,a11
R_ v100,a28
R_ v101,a29
R_ v102,a30
R_ v103,a31
v_pk_mul_f32 v[96:97], v[96:97], v[48:49] op_sel_hi:[1,0]
v_pk_mul_f32 v[98:99], v[98:99], v[48:49] op_sel_hi:[1,0]
v_pk_mul_f32 v[100:101], v[100:101], v[48:49] op_sel_hi:[1,0]
v_pk_mul_f32 v[102:103], v[102:103], v[48:49] op_sel_hi:[1,0]
C_ 84,96,97
C_ 85,98,99
C_ 86,100,101
C_ 87,102,103
ds_write_b128 v13, v[84:87] offset:2048
.p2align 3
R_ v104,a12
R_ v105,a13
R_ v106,a14
R_ v107,a15
R_ v108,a32
R_ v109,a33
R_ v110,a34
R_ v111,a35
v_pk_mul_f32 v[104:105], v[104:105], v[48:49] op_sel:[0,1]
v_pk_mul_f32 v[106:107], v[106:107], v[48:49] op_sel:[0,1]
v_pk_mul_f32 v[108:109], v[108:109], v[48:49] op_sel:[0,1]
v_pk_mul_f32 v[110:111], v[110:111], v[48:49] op_sel:[0,1]
C_ 88,104,105
C_ 89,106,107
C_ 90,108,109
C_ 91,110,111
ds_write_b128 v13, v[88:91] offset:3072
.p2align 3
R_ v96,a16
R_ v97,a17
R_ v98,a18
R_ v99,a19
R_ v100,a36
R_ v101,a37
R_ v102,a38
R_ v103,a39
v_pk_mul_f32 v[96:97], v[96:97], v[50:51] op_sel_hi:[1,0]
v_pk_mul_f32 v[98:99], v[98:99], v[50:51] op_sel_hi:[1,0]
v_pk_mul_f32 v[100:101], v[100:101], v[50:51] op_sel_hi:[1,0]
v_pk_mul_f32 v[102:103], v[102:103], v[50:51] op_sel_hi:[1,0]
C_ 92,96,97
C_ 93,98,99
C_ 94,100,101
C_ 95,102,103
ds_write_b128 v13, v[92:95] offset:4096
.p2align 3
R_ v104,a40
R_ v105,a41
R_ v106,a42
R_ v107,a43
R_ v108,a60
R_ v109,a61
R_ v110,a62
R_ v111,a63
v_pk_mul_f32 v[104:105], v[104:105], v[46:47] op_sel_hi:[1,0]
v_pk_mul_f32 v[106:107], v[106:107], v[46:47] op_sel_hi:[1,0]
v_pk_mul_f32 v[108:109], v[108:109], v[46:47] op_sel_hi:[1,0]
v_pk_mul_f32 v[110:111], v[110:111], v[46:47] op_sel_hi:[1,0]
C_ 76,104,105
C_ 77,106,107
C_ 78,108,109
C_ 79,110,111
ds_write_b128 v13, v[76:79] offset:5120
.p2align 3
R_ v96,a44
R_ v97,a45
R_ v98,a46
R_ v99,a47
R_ v100,a64
R_ v101,a65
R_ v102,a66
R_ v103,a67
v_pk_mul_f32 v[96:97], v[96:97], v[46:47] op_sel:[0,1]
v_pk_mul_f32 v[98:99], v[98:99], v[46:47] op_sel:[0,1]
v_pk_mul_f32 v[100:101], v[100:101], v[46:47] op_sel:[0,1]
v_pk_mul_f32 v[102:103], v[102:103], v[46:47] op_sel:[0,1]
C_ 80,96,97
C_ 81,98,99
C_ 82,100,101
C_ 83,102,103
ds_write_b128 v13, v[80:83] offset:6144
.p2align 3
R_ v104,a48
R_ v105,a49
R_ v106,a50
R_ v107,a51
R_ v108,a68
R_ v109,a69
R_ v110,a70
R_ v111,a71
v_pk_mul_f32 v[104:105], v[104:105], v[48:49] op_sel_hi:[1,0]
v_pk_mul_f32 v[106:107], v[106:107], v[48:49] op_sel_hi:[1,0]
v_pk_mul_f32 v[108:109], v[108:109], v[48:49] op_sel_hi:[1,0]
v_pk_mul_f32 v[110:111], v[110:111], v[48:49] op_sel_hi:[1,0]
C_ 84,104,105
C_ 85,106,107
C_ 86,108,109
C_ 87,110,111
ds_write_b128 v13, v[84:87] offset:7168
.p2align 3
R_ v96,a52
R_ v97,a53
R_ v98,a54
R_ v99,a55
R_ v100,a72
R_ v101,a73
R_ v102,a74
R_ v103,a75
v_pk_mul_f32 v[96:97], v[96:97], v[48:49] op_sel:[0,1]
v_pk_mul_f32 v[98:99], v[98:99], v[48:49] op_sel:[0,1]
v_pk_mul_f32 v[100:101], v[100:101], v[48:49] op_sel:[0,1]
v_pk_mul_f32 v[102:103], v[102:103], v[48:49] op_sel:[0,1]
C_ 88,96,97
C_ 89,98,99
C_ 90,100,101
C_ 91,102,103
ds_write_b128 v13, v[88:91] offset:8192
.p2align 3
R_ v104,a56
R_ v105,a57
R_ v106,a58
R_ v107,a59
R_ v108,a76
R_ v109,a77
R_ v110,a78
R_ v111,a79
v_pk_mul_f32 v[104:105], v[104:105], v[50:51] op_sel_hi:[1,0]
v_pk_mul_f32 v[106:107], v[106:107], v[50:51] op_sel_hi:[1,0]
v_pk_mul_f32 v[108:109], v[108:109], v[50:51] op_sel_hi:[1,0]
v_pk_mul_f32 v[110:111], v[110:111], v[50:51] op_sel_hi:[1,0]
C_ 92,104,105
C_ 93,106,107
C_ 94,108,109
C_ 95,110,111
ds_write_b128 v13, v[92:95] offset:9216
.p2align 3
R_ v104,a92
R_ v105,a93
R_ v106,a94
R_ v107,a95
R_ v108,a112
R_ v109,a113
R_ v110,a114
R_ v111,a115
v_pk_mul_f32 v[104:105], v[104:105], v[48:49] op_sel:[0,1]
v_pk_mul_f32 v[106:107], v[106:107], v[48:49] op_sel:[0,1]
v_pk_mul_f32 v[108:109], v[108:109], v[48:49] op_sel:[0,1]
v_pk_mul_f32 v[110:111], v[110:111], v[48:49] op_sel:[0,1]
C_ 88,104,105
C_ 89,106,107
C_ 90,108,109
C_ 91,110,111
X_ a240,v88
X_ a241,v89
X_ a242,v90
X_ a243,v91
.p2align 3
R_ v96,a96
R_ v97,a97
R_ v98,a98
R_ v99,a99
R_ v100,a116
R_ v101,a117
R_ v102,a118
R_ v103,a119
v_pk_mul_f32 v[96:97], v[96:97], v[50:51] op_sel_hi:[1,0]
v_pk_mul_f32 v[98:99], v[98:99], v[50:51] op_sel_hi:[1,0]
v_pk_mul_f32 v[100:101], v[100:101], v[50:51] op_sel_hi:[1,0]
v_pk_mul_f32 v[102:103], v[102:103], v[50:51] op_sel_hi:[1,0]
C_ 92,96,97
C_ 93,98,99
C_ 94,100,101
C_ 95,102,103
X_ a244,v92
X_ a245,v93
X_ a246,v94
X_ a247,v95
.p2align 3
R_ v96,a124
R_ v97,a125
R_ v98,a126
R_ v99,a127
R_ v100,a144
R_ v101,a145
R_ v102,a146
R_ v103,a147
v_pk_mul_f32 v[96:97], v[96:97], v[46:47] op_sel:[0,1]
v_pk_mul_f32 v[98:99], v[98:99], v[46:47] op_sel:[0,1]
v_pk_mul_f32 v[100:101], v[100:101], v[46:47] op_sel:[0,1]
v_pk_mul_f32 v[102:103], v[102:103], v[46:47] op_sel:[0,1]
C_ 80,96,97
C_ 81,98,99
C_ 82,100,101
C_ 83,102,103
v_mov_b32 v28, v80
v_mov_b32 v29, v81
v_mov_b32 v51, v82
v_mov_b32 v6, v83
.p2align 3
R_ v104,a128
R_ v105,a129
R_ v106,a130
R_ v107,a131
R_ v108,a148
R_ v109,a149
R_ v110,a150
R_ v111,a151
v_pk_mul_f32 v[104:105], v[104:105], v[48:49] op_sel_hi:[1,0]
v_pk_mul_f32 v[106:107], v[106:107], v[48:49] op_sel_hi:[1,0]
v_pk_mul_f32 v[108:109], v[108:109], v[48:49] op_sel_hi:[1,0]
v_pk_mul_f32 v[110:111], v[110:111], v[48:49] op_sel_hi:[1,0]
C_ 84,104,105
C_ 85,106,107
C_ 86,108,109
C_ 87,110,111
v_mov_b32 v14, v84
v_mov_b32 v15, v85
X_ a249,v86
X_ a250,v87
.p2align 3
R_ v96,a132
R_ v97,a133
R_ v98,a134
R_ v99,a135
R_ v100,a152
R_ v101,a153
R_ v102,a154
R_ v103,a155
v_pk_mul_f32 v[96:97], v[96:97], v[48:49] op_sel:[0,1]
v_pk_mul_f32 v[98:99], v[98:99], v[48:49] op_sel:[0,1]
v_pk_mul_f32 v[100:101], v[100:101], v[48:49] op_sel:[0,1]
v_pk_mul_f32 v[102:103], v[102:103], v[48:49] op_sel:[0,1]
C_ 88,96,97
C_ 89,98,99
C_ 90,100,101
C_ 91,102,103
ds_write_b128 v12, v[88:91] offset:0
.p2align 3
R_ v104,a136
R_ v105,a137
R_ v106,a138
R_ v107,a139
R_ v108,a156
R_ v109,a157
R_ v110,a158
R_ v111,a159
v_pk_mul_f32 v[104:105], v[104:105], v[50:51] op_sel_hi:[1,0]
v_pk_mul_f32 v[106:107], v[106:107], v[50:51] op_sel_hi:[1,0]
v_pk_mul_f32 v[108:109], v[108:109], v[50:51] op_sel_hi:[1,0]
v_pk_mul_f32 v[110:111], v[110:111], v[50:51] op_sel_hi:[1,0]
C_ 92,104,105
C_ 93,106,107
C_ 94,108,109
C_ 95,110,111
ds_write_b128 v12, v[92:95] offset:1024
.p2align 3
R_ v96,a160
R_ v97,a161
R_ v98,a162
R_ v99,a163
R_ v100,a180
R_ v101,a181
R_ v102,a182
R_ v103,a183
v_pk_mul_f32 v[96:97], v[96:97], v[46:47] op_sel_hi:[1,0]
v_pk_mul_f32 v[98:99], v[98:99], v[46:47] op_sel_hi:[1,0]
v_pk_mul_f32 v[100:101], v[100:101], v[46:47] op_sel_hi:[1,0]
v_pk_mul_f32 v[102:103], v[102:103], v[46:47] op_sel_hi:[1,0]
C_ 76,96,97
C_ 77,98,99
C_ 78,100,101
C_ 79,102,103
ds_write_b128 v12, v[76:79] offset:2048
.p2align 3
R_ v104,a164
R_ v105,a165
R_ v106,a166
R_ v107,a167
R_ v108,a184
R_ v109,a185
R_ v110,a186
R_ v111,a187
v_pk_mul_f32 v[104:105], v[104:105], v[46:47] op_sel:[0,1]
v_pk_mul_f32 v[106:107], v[106:107], v[46:47] op_sel:[0,1]
v_pk_mul_f32 v[108:109], v[108:109], v[46:47] op_sel:[0,1]
v_pk_mul_f32 v[110:111], v[110:111], v[46:47] op_sel:[0,1]
C_ 80,104,105
C_ 81,106,107
C_ 82,108,109
C_ 83,110,111
ds_write_b128 v12, v[80:83] offset:3072
.p2align 3
R_ v96,a168
R_ v97,a169
R_ v98,a170
R_ v99,a171
R_ v100,a188
R_ v101,a189
R_ v102,a190
R_ v103,a191
v_pk_mul_f32 v[96:97], v[96:97], v[48:49] op_sel_hi:[1,0]
v_pk_mul_f32 v[98:99], v[98:99], v[48:49] op_sel_hi:[1,0]
v_pk_mul_f32 v[100:101], v[100:101], v[48:49] op_sel_hi:[1,0]
v_pk_mul_f32 v[102:103], v[102:103], v[48:49] op_sel_hi:[1,0]
C_ 84,96,97
C_ 85,98,99
C_ 86,100,101
C_ 87,102,103
ds_write_b128 v12, v[84:87] offset:4096
.p2align 3
R_ v104,a172
R_ v105,a173
R_ v106,a174
R_ v107,a175
R_ v108,a192
R_ v109,a193
R_ v110,a194
R_ v111,a195
v_pk_mul_f32 v[104:105], v[104:105], v[48:49] op_sel:[0,1]
v_pk_mul_f32 v[106:107], v[106:107], v[48:49] op_sel:[0,1]
v_pk_mul_f32 v[108:109], v[108:109], v[48:49] op_sel:[0,1]
v_pk_mul_f32 v[110:111], v[110:111], v[48:49] op_sel:[0,1]
C_ 88,104,105
C_ 89,106,107
C_ 90,108,109
C_ 91,110,111
ds_write_b128 v12, v[88:91] offset:5120
.p2align 3
R_ v96,a176
R_ v97,a177
R_ v98,a178
R_ v99,a179
R_ v100,a196
R_ v101,a197
R_ v102,a198
R_ v103,a199
v_pk_mul_f32 v[96:97], v[96:97], v[50:51] op_sel_hi:[1,0]
v_pk_mul_f32 v[98:99], v[98:99], v[50:51] op_sel_hi:[1,0]
v_pk_mul_f32 v[100:101], v[100:101], v[50:51] op_sel_hi:[1,0]
v_pk_mul_f32 v[102:103], v[102:103], v[50:51] op_sel_hi:[1,0]
C_ 92,96,97
C_ 93,98,99
C_ 94,100,101
C_ 95,102,103
ds_write_b128 v12, v[92:95] offset:12288
.p2align 3
R_ v104,a200
R_ v105,a201
R_ v106,a202
R_ v107,a203
R_ v108,a220
R_ v109,a221
R_ v110,a222
R_ v111,a223
v_pk_mul_f32 v[104:105], v[104:105], v[46:47] op_sel_hi:[1,0]
v_pk_mul_f32 v[106:107], v[106:107], v[46:47] op_sel_hi:[1,0]
v_pk_mul_f32 v[108:109], v[108:109], v[46:47] op_sel_hi:[1,0]
v_pk_mul_f32 v[110:111], v[110:111], v[46:47] op_sel_hi:[1,0]
C_ 76,104,105
C_ 77,106,107
C_ 78,108,109
C_ 79,110,111
ds_write_b128 v12, v[76:79] offset:13312
.p2align 3
R_ v96,a204
R_ v97,a205
R_ v98,a206
R_ v99,a207
R_ v100,a224
R_ v101,a225
R_ v102,a226
R_ v103,a227
v_pk_mul_f32 v[96:97], v[96:97], v[46:47] op_sel:[0,1]
v_pk_mul_f32 v[98:99], v[98:99], v[46:47] op_sel:[0,1]
v_pk_mul_f32 v[100:101], v[100:101], v[46:47] op_sel:[0,1]
v_pk_mul_f32 v[102:103], v[102:103], v[46:47] op_sel:[0,1]
C_ 80,96,97
C_ 81,98,99
C_ 82,100,101
C_ 83,102,103
ds_write_b128 v12, v[80:83] offset:14336
.p2align 3
R_ v104,a208
R_ v105,a209
R_ v106,a210
R_ v107,a211
R_ v108,a228
R_ v109,a229
R_ v110,a230
R_ v111,a231
v_pk_mul_f32 v[104:105], v[104:105], v[48:49] op_sel_hi:[1,0]
v_pk_mul_f32 v[106:107], v[106:107], v[48:49] op_sel_hi:[1,0]
v_pk_mul_f32 v[108:109], v[108:109], v[48:49] op_sel_hi:[1,0]
v_pk_mul_f32 v[110:111], v[110:111], v[48:49] op_sel_hi:[1,0]
C_ 84,104,105
C_ 85,106,107
C_ 86,108,109
C_ 87,110,111
ds_write_b128 v12, v[84:87] offset:15360
s_waitcnt vmcnt(6) lgkmcnt(0)
s_barrier
Dv 30,%0,6144
Dv 34,v1,6144
Dv 38,%0,7168
.p2align 3
R_ v96,a212
R_ v97,a213
R_ v98,a214
R_ v99,a215
R_ v100,a232
R_ v101,a233
R_ v102,a234
R_ v103,a235
v_pk_mul_f32 v[96:97], v[96:97], v[48:49] op_sel:[0,1]
v_pk_mul_f32 v[98:99], v[98:99], v[48:49] op_sel:[0,1]
v_pk_mul_f32 v[100:101], v[100:101], v[48:49] op_sel:[0,1]
v_pk_mul_f32 v[102:103], v[102:103], v[48:49] op_sel:[0,1]
C_ 88,96,97
C_ 89,98,99
C_ 90,100,101
C_ 91,102,103
ds_write_b128 v12, v[88:91] offset:16384
.p2align 3
R_ v104,a216
R_ v105,a217
R_ v106,a218
R_ v107,a219
R_ v108,a236
R_ v109,a237
R_ v110,a238
R_ v111,a239
v_pk_mul_f32 v[104:105], v[104:105], v[50:51] op_sel_hi:[1,0]
v_pk_mul_f32 v[106:107], v[106:107], v[50:51] op_sel_hi:[1,0]
v_pk_mul_f32 v[108:109], v[108:109], v[50:51] op_sel_hi:[1,0]
v_pk_mul_f32 v[110:111], v[110:111], v[50:51] op_sel_hi:[1,0]
C_ 92,104,105
C_ 93,106,107
C_ 94,108,109
C_ 95,110,111
ds_write_b128 v12, v[92:95] offset:17408
.Latt3:
W_ 4
Zvvv 96,30,136
Dv 42,v1,7168
R_ v116,a80
Zvvv 100,30,140
R_ v117,a81
R_ v118,a82
Zvvv 104,30,144
R_ v119,a83
R_ v120,a100
Zvvv 108,30,148
R_ v121,a101
R_ v122,a102
Zvvv 112,30,152
W_ 4
R_ v123,a103
U_ v116,v116,v46
Mvvv 96,34,156
Dv 30,%0,8192
U_ v117,v117,v46
Mvvv 100,34,160
U_ v118,v118,v46
U_ v119,v119,v46
Mvvv 104,34,164
U_ v120,v120,v46
U_ v121,v121,v46
Mvvv 108,34,168
U_ v122,v122,v46
U_ v123,v123,v46
Mvvv 112,34,172
W_ 4
C_ 20,116,117
C_ 21,118,119
Mvvv 96,38,176
Dv 34,v1,8192
C_ 22,120,121
Mvvv 100,38,180
C_ 23,122,123
R_ v124,a84
Mvvv 104,38,184
R_ v125,a85
R_ v126,a86
Mvvv 108,38,188
R_ v127,a87
R_ v128,a104
Mvvv 112,38,192
W_ 2
R_ v129,a105
R_ v130,a106
Mvvv 96,42,196
Dv 38,%0,0
R_ v131,a107
Mvvv 100,42,200
U_ v124,v124,v47
U_ v125,v125,v47
Mvvv 104,42,204
U_ v126,v126,v47
U_ v127,v127,v47
Mvvv 108,42,208
U_ v128,v128,v47
U_ v129,v129,v47
Mvvv 112,42,212
W_ 2
U_ v130,v130,v47
U_ v131,v131,v47
Mvvv 96,30,216
Dv 42,v1,0
C_ 24,124,125
Mvvv 100,30,220
C_ 25,126,127
C_ 26,128,129
Mvvv 104,30,224
C_ 27,130,131
R_ v116,a88
Mvvv 108,30,228
R_ v117,a89
R_ v118,a90
Mvvv 112,30,232
W_ 2
R_ v119,a91
R_ v120,a108
Mvvv 96,34,236
Dv 30,%0,1024
R_ v121,a109
Mvvv 100,34,240
R_ v122,a110
R_ v123,a111
Mvvv 104,34,244
U_ v116,v116,v48
U_ v117,v117,v48
Mvvv 108,34,248
U_ v118,v118,v48
U_ v119,v119,v48
Mvvv 112,34,252
W_ 2
U_ v120,v120,v48
U_ v121,v121,v48
Zvvv 76,38,136
Dv 34,v1,1024
U_ v122,v122,v48
Zvvv 80,38,140
U_ v123,v123,v48
C_ 52,116,117
Zvvv 84,38,144
C_ 53,118,119
C_ 54,120,121
Zvvv 88,38,148
C_ 55,122,123
R_ v124,a120
Zvvv 92,38,152
W_ 2
R_ v125,a121
R_ v126,a122
Mvvv 76,42,156
Dv 38,%0,2048
R_ v127,a123
Mvvv 80,42,160
R_ v128,a140
R_ v129,a141
Mvvv 84,42,164
R_ v130,a142
R_ v131,a143
Mvvv 88,42,168
U_ v124,v124,v46
U_ v125,v125,v46
Mvvv 92,42,172
W_ 2
U_ v126,v126,v46
U_ v127,v127,v46
Mvvv 76,30,176
Dv 42,v1,2048
U_ v128,v128,v46
Mvvv 80,30,180
U_ v129,v129,v46
U_ v130,v130,v46
Mvvv 84,30,184
U_ v131,v131,v46
C_ 16,124,125
Mvvv 88,30,188
C_ 17,126,127
C_ 18,128,129
Mvvv 92,30,192
s_waitcnt vmcnt(0) lgkmcnt(0)
s_barrier
Mvvv 76,34,196
S_ s65,s61,24576
Dv 30,%0,18432
C_ 19,130,131
Mvvv 76,38,216
S_ m0,s51,0
v_max3_f32 v56, v96, v97, v98
v_max_f32 v56, v56, v99
Mvvv 76,42,236
Ln v7,32,s65,0
Mvvv 80,34,200
v_max3_f32 v60, v100, v101, v102
v_max_f32 v60, v60, v103
Mvvv 80,38,220
Ln v8,32,s65,3072
Mvvv 80,42,240
S_ m0,s51,896
v_max3_f32 v64, v104, v105, v106
v_max_f32 v64, v64, v107
Mvvv 84,34,204
Ln v7,32,s65,128
Mvvv 84,38,224
v_max3_f32 v68, v108, v109, v110
v_max_f32 v68, v68, v111
Mvvv 84,42,244
Ln v8,32,s65,3200
Mvvv 88,34,208
S_ m0,s51,1792
v_max3_f32 v72, v112, v113, v114
v_max_f32 v72, v72, v115
Mvvv 88,38,228
Ln v7,32,s65,256
Mvvv 88,42,248
v_max3_f32 v56, v56, v76, v77
v_max3_f32 v56, v56, v78, v79
Mvvv 92,34,212
Ln v8,32,s65,3328
Mvvv 92,38,232
v_max3_f32 v60, v60, v80, v81
v_max3_f32 v60, v60, v82, v83
Mvvv 92,42,252
v_max3_f32 v64, v64, v84, v85
v_max3_f32 v64, v64, v86, v87
Dv 34,v1,18432
.p2align 3
v_mov_b32 v11, 0x3f803f80
v_permlane32_swap_b32 v56, v60
v_max3_f32 v68, v68, v88, v89
v_max3_f32 v68, v68, v90, v91
v_max_f32 v56, v56, v60
v_max3_f32 v72, v72, v92, v93
v_max3_f32 v72, v72, v94, v95
v_mov_b32 v73, v72
v_permlane32_swap_b32 v64, v68
v_max_f32 v64, v64, v68
v_permlane32_swap_b32 v72, v73
v_max_f32 v72, v72, v73
v_mov_b32 v73, v72
v_permlane16_swap_b32 v56, v64
v_max_f32 v56, v56, v64
v_permlane16_swap_b32 v72, v73
B_ v56,0xc0e00000,v56
v_max_f32 v72, v72, v73
v_mov_b32 v64, v56
B_ v72,0xc0e00000,v72
v_mov_b32 v73, v72
v_permlane16_swap_b32 v56, v64
v_mov_b32 v60, v56
v_mov_b32 v68, v64
v_mov_b64 v[74:75], v[72:73]
v_permlane32_swap_b32 v56, v60
v_permlane32_swap_b32 v64, v68
v_mov_b32 v57, v56
v_mov_b32 v61, v60
v_mov_b32 v65, v64
v_mov_b32 v69, v68
v_pk_add_f32 v[96:97], v[96:97], v[56:57]
v_pk_add_f32 v[98:99], v[98:99], v[56:57]
v_pk_add_f32 v[76:77], v[76:77], v[56:57]
v_pk_add_f32 v[78:79], v[78:79], v[56:57]
v_mov_b64 v[58:59], v[56:57]
v_pk_add_f32 v[100:101], v[100:101], v[60:61]
v_pk_add_f32 v[102:103], v[102:103], v[60:61]
v_pk_add_f32 v[80:81], v[80:81], v[60:61]
v_pk_add_f32 v[82:83], v[82:83], v[60:61]
v_mov_b64 v[62:63], v[60:61]
v_pk_add_f32 v[104:105], v[104:105], v[64:65]
v_pk_add_f32 v[106:107], v[106:107], v[64:65]
v_pk_add_f32 v[84:85], v[84:85], v[64:65]
v_pk_add_f32 v[86:87], v[86:87], v[64:65]
v_mov_b64 v[66:67], v[64:65]
v_pk_add_f32 v[108:109], v[108:109], v[68:69]
v_pk_add_f32 v[110:111], v[110:111], v[68:69]
v_pk_add_f32 v[88:89], v[88:89], v[68:69]
v_pk_add_f32 v[90:91], v[90:91], v[68:69]
v_mov_b64 v[70:71], v[68:69]
v_pk_add_f32 v[112:113], v[112:113], v[72:73]
v_pk_add_f32 v[114:115], v[114:115], v[72:73]
v_pk_add_f32 v[92:93], v[92:93], v[72:73]
v_pk_add_f32 v[94:95], v[94:95], v[72:73]
E_ 96
E_ 97
E_ 98
E_ 99
E_ 76
E_ 77
E_ 78
E_ 79
C_ 99,98,99
C_ 98,96,97
C_ 96,76,77
C_ 97,78,79
E_ 100
E_ 101
E_ 102
E_ 103
E_ 80
E_ 81
E_ 82
E_ 83
C_ 103,102,103
C_ 102,100,101
C_ 100,80,81
C_ 101,82,83
.Lstep3_1:
W_ 1
Nvvv 116,30,136,56
s_nop 0
Dv 38,%0,19456
Dv 42,v1,19456
W_ 2
Mvvv 116,34,156
E_ 104
Nvvv 120,30,140,60
E_ 105
Mvvv 120,34,160
E_ 106
Nvvv 124,30,144,64
E_ 107
Mvvv 124,34,164
E_ 84
Nvvv 128,30,148,68
E_ 85
Mvvv 128,34,168
E_ 86
Nvvv 132,30,152,72
E_ 87
Mvvv 132,34,172
W_ 0
C_ 107,106,107
C_ 106,104,105
Mvvv 132,38,192
s_nop 0
Dv 30,%0,20480
Dv 34,v1,20480
Mvvv 132,42,212
C_ 104,84,85
C_ 105,86,87
Mvvv 128,38,188
E_ 108
Mvvv 128,42,208
E_ 109
Mvvv 124,38,184
E_ 110
Mvvv 124,42,204
E_ 111
Mvvv 120,38,180
E_ 88
Mvvv 120,42,200
E_ 89
Mvvv 116,38,176
E_ 90
Mvvv 116,42,196
W_ 0
E_ 91
Mvvv 116,30,216
s_nop 0
Dv 38,%0,12288
Dv 42,v1,12288
Mvvv 116,34,236
C_ 111,110,111
C_ 110,108,109
Mvvv 120,30,220
C_ 108,88,89
C_ 109,90,91
Mvvv 120,34,240
E_ 112
Mvvv 124,30,224
E_ 113
Mvvv 124,34,244
E_ 114
Mvvv 128,30,228
E_ 115
Mvvv 128,34,248
E_ 92
Mvvv 132,30,232
E_ 93
Mvvv 132,34,252
W_ 0
E_ 94
Nvvv 76,38,136,56
s_nop 0
Dv 30,%0,13312
Dv 34,v1,13312
Mvvv 76,42,156
E_ 95
Nvvv 80,38,140,60
C_ 115,114,115
C_ 114,112,113
Mvvv 80,42,160
C_ 112,92,93
C_ 113,94,95
Nvvv 84,38,144,64
E_ 116
Mvvv 84,42,164
E_ 117
Nvvv 88,38,148,68
E_ 118
Mvvv 88,42,168
E_ 119
Nvvv 92,38,152,72
C_ 119,118,119
C_ 118,116,117
Mvvv 92,42,172
W_ 0
E_ 120
Mvvv 92,30,192
s_nop 0
Dv 38,%0,14336
Dv 42,v1,14336
Mvvv 92,34,212
X_ a180,v11
X_ a181,v11
Mvvv 88,30,188
X_ a182,v11
X_ a183,v11
Mvvv 88,34,208
E_ 121
Mvvv 84,30,184
E_ 122
Mvvv 84,34,204
E_ 123
Mvvv 80,30,180
C_ 123,122,123
C_ 122,120,121
Mvvv 80,34,200
E_ 124
Mvvv 76,30,176
E_ 125
Mvvv 76,34,196
S_ s65,s61,12288
E_ 126
W_ 1
Mvvv 76,38,216
S_ m0,s51,12288
E_ 127
W_ 0
Mvvv 76,42,236
s_waitcnt vmcnt(18) lgkmcnt(0)
s_barrier
C_ 127,126,127
Mvvv 80,38,220
L_ v9,36,s65,0
Mvvv 80,42,240
C_ 126,124,125
Mvvv 84,38,224
L_ v9,36,s65,3072
S_ m0,s51,13184
Mvvv 84,42,244
s_nop 0
Tv 30,v2,24576
Tv 32,v2,30720
Mvvv 88,38,228
L_ v9,36,s65,128
Mvvv 88,42,248
s_nop 0
Tv 34,v3,24576
Tv 36,v3,30720
Mvvv 92,38,232
L_ v9,36,s65,3200
S_ m0,s51,14080
Mvvv 92,42,252
E_ 128
Zaav 220,180,96
L_ v9,36,s65,256
Zaav 224,180,100
E_ 129
Zaav 228,180,104
L_ v9,36,s65,3328
Zaav 232,180,108
E_ 130
Zaav 236,180,112
W_ 0
E_ 131
Zavv 0,30,96
s_nop 0
Tv 38,v4,24576
Tv 40,v4,30720
Zavv 20,34,96
C_ 131,130,131
C_ 130,128,129
Zavv 24,34,100
s_nop 0
Tv 42,v5,24576
Tv 44,v5,30720
Zavv 4,30,100
E_ 132
Zavv 8,30,104
E_ 133
Zavv 28,34,104
R_ v46,a220
R_ v47,a224
Zavv 32,34,108
R_ v48,a228
R_ v49,a232
Zavv 12,30,108
R_ v50,a236
Zavv 16,30,112
E_ 134
Zavv 36,34,112
W_ 0
E_ 135
Zavv 56,38,112
s_nop 0
Tv 30,v2,25600
Tv 32,v2,31744
Zavv 76,42,112
C_ 135,134,135
C_ 134,132,133
Zavv 72,42,108
s_nop 0
Tv 34,v3,25600
Tv 36,v3,31744
Zavv 52,38,108
E_ 76
Zavv 48,38,104
E_ 77
Zavv 68,42,104
E_ 78
Zavv 64,42,100
E_ 79
Zavv 44,38,100
C_ 116,76,77
C_ 117,78,79
Zavv 40,38,96
E_ 80
Zavv 60,42,96
W_ 0
E_ 81
Zavv 80,30,96
s_nop 0
Tv 38,v4,25600
Tv 40,v4,31744
Zavv 100,34,96
E_ 82
Zavv 104,34,100
s_nop 0
Tv 42,v5,25600
Tv 44,v5,31744
Zavv 84,30,100
E_ 83
Zavv 88,30,104
C_ 120,80,81
C_ 121,82,83
Zavv 108,34,104
E_ 84
Zavv 112,34,108
E_ 85
Zavv 92,30,108
E_ 86
Zavv 96,30,112
E_ 87
Zavv 116,34,112
W_ 0
C_ 124,84,85
C_ 125,86,87
Zavv 136,38,112
s_nop 0
Tv 30,v2,26624
Tv 32,v2,32768
Zavv 156,42,112
E_ 88
Zavv 152,42,108
s_nop 0
Tv 34,v3,26624
Tv 36,v3,32768
Zavv 132,38,108
E_ 89
Zavv 128,38,104
E_ 90
Zavv 148,42,104
E_ 91
Zavv 144,42,100
C_ 128,88,89
C_ 129,90,91
Zavv 124,38,100
E_ 92
Zavv 120,38,96
E_ 93
Zavv 140,42,96
W_ 0
E_ 94
Zavv 160,30,96
s_nop 0
Tv 38,v4,26624
Tv 40,v4,32768
Zavv 180,34,96
E_ 95
Zavv 184,34,100
s_nop 0
Tv 42,v5,26624
Tv 44,v5,32768
Zavv 164,30,100
C_ 132,92,93
C_ 133,94,95
Zavv 168,30,104
v_mov_b32 v92, 0x3f803f80
v_mov_b32 v93, 0x3f803f80
Zavv 188,34,104
S_ s65,s61,36864
v_mov_b32 v94, 0x3f803f80
v_mov_b32 v95, 0x3f803f80
Zavv 192,34,108
S_ m0,s51,24576
Zavv 172,30,108
s_waitcnt vmcnt(6) lgkmcnt(0)
s_barrier
Zavv 176,30,112
Ln v7,32,s65,0
Zavv 196,34,112
Zavv 216,38,112
Dv 30,%0,6144
Zavv 236,42,112
Dv 34,v1,6144
Zavv 232,42,108
Ln v8,32,s65,3072
S_ m0,s51,25472
Zavv 212,38,108
Zavv 208,38,104
Ln v7,32,s65,128
Zavv 228,42,104
Zavv 224,42,100
Ln v8,32,s65,3200
S_ m0,s51,26368
Zavv 204,38,100
Zavv 200,38,96
Ln v7,32,s65,256
Zavv 220,42,96
Ln v8,32,s65,3328
W_ 0
s_nop 7
s_branch .Lstep1_2
.Latt1:
R_ v13,a248
S_ s53,s51,36864
.Lpad_a1_0:
s_cbranch_execz .Lhop_a1_r
v_add_u32 v12, s53, v13
s_mul_i32 s53, s4, 10240
S_ s53,s53,0
v_add_u32 v13, s53, v13
W_ 2
Zvvv 96,30,136
Dv 42,v1,44032
R_ v2,a245
Zvvv 100,30,140
ds_write_b128 v13, a[0:3] offset:0
Zvvv 104,30,144
v_xor_b32 v3, 32, v2
v_xor_b32 v4, 64, v2
Zvvv 108,30,148
ds_write_b128 v13, a[4:7] offset:1024
Zvvv 112,30,152
W_ 4
v_xor_b32 v5, 96, v2
s_mov_b64 s[24:25], s[16:17]
Mvvv 96,34,156
s_mov_b32 s26, 7864320
Dv 30,%0,45056
s_mov_b64 s[28:29], s[22:23]
Mvvv 100,34,160
ds_write_b128 v13, a[8:11] offset:2048
Mvvv 104,34,164
s_mov_b32 s30, 1638400
R_ v20,a80
s_mul_i32 s52, s4, 10240
Mvvv 108,34,168
ds_write_b128 v13, a[12:15] offset:3072
Mvvv 112,34,172
W_ 6
R_ v21,a81
R_ v22,a82
Mvvv 96,38,176
Dv 34,v1,45056
R_ v23,a83
Mvvv 100,38,180
ds_write_b128 v13, a[16:19] offset:4096
Mvvv 104,38,184
R_ v24,a84
R_ v25,a85
Mvvv 108,38,188
ds_write_b128 v13, a[40:43] offset:5120
Mvvv 112,38,192
W_ 8
R_ v26,a86
R_ v27,a87
Mvvv 96,42,196
Dv 38,%0,36864
R_ v52,a88
Mvvv 100,42,200
ds_write_b128 v13, a[44:47] offset:6144
Mvvv 104,42,204
R_ v53,a89
R_ v54,a90
Mvvv 108,42,208
ds_write_b128 v13, a[48:51] offset:7168
Mvvv 112,42,212
W_ 8
R_ v55,a91
v_accvgpr_mov_b32 a240, a92
Mvvv 96,30,216
Dv 42,v1,36864
v_accvgpr_mov_b32 a241, a93
Mvvv 100,30,220
ds_write_b128 v13, a[52:55] offset:8192
Mvvv 104,30,224
v_accvgpr_mov_b32 a242, a94
v_accvgpr_mov_b32 a243, a95
Mvvv 108,30,228
ds_write_b128 v13, a[56:59] offset:9216
Mvvv 112,30,232
W_ 8
v_accvgpr_mov_b32 a244, a96
v_accvgpr_mov_b32 a245, a97
Mvvv 96,34,236
Dv 30,%0,37888
v_accvgpr_mov_b32 a246, a98
Mvvv 100,34,240
v_accvgpr_mov_b32 a247, a99
R_ v16,a120
Mvvv 104,34,244
R_ v17,a121
R_ v18,a122
Mvvv 108,34,248
R_ v19,a123
R_ v28,a124
Mvvv 112,34,252
W_ 6
R_ v29,a125
R_ v51,a126
Zvvv 76,38,136
Dv 34,v1,37888
R_ v6,a127
Zvvv 80,38,140
R_ v14,a128
R_ v15,a129
Zvvv 84,38,144
v_accvgpr_mov_b32 a249, a130
v_accvgpr_mov_b32 a250, a131
Zvvv 88,38,148
v_max3_f32 v56, v96, v97, v98
v_max_f32 v56, v56, v99
Zvvv 92,38,152
W_ 4
v_max3_f32 v60, v100, v101, v102
v_max_f32 v60, v60, v103
Mvvv 76,42,156
Dv 38,%0,38912
v_max3_f32 v64, v104, v105, v106
Mvvv 80,42,160
v_max_f32 v64, v64, v107
Mvvv 84,42,164
v_max3_f32 v68, v108, v109, v110
v_max_f32 v68, v68, v111
Mvvv 88,42,168
v_max3_f32 v72, v112, v113, v114
v_max_f32 v72, v72, v115
Mvvv 92,42,172
W_ 2
Mvvv 76,30,176
Dv 42,v1,38912
Mvvv 80,30,180
Mvvv 84,30,184
Mvvv 88,30,188
Mvvv 92,30,192
s_waitcnt vmcnt(34) lgkmcnt(0)
s_barrier
Mvvv 76,34,196
Dv 30,%0,55296
Mvvv 76,38,216
Mvvv 76,42,236
Mvvv 80,34,200
Mvvv 80,38,220
Mvvv 80,42,240
Mvvv 84,34,204
s_nop 3
v_max3_f32 v56, v56, v76, v77
v_max3_f32 v56, v56, v78, v79
Mvvv 84,38,224
Mvvv 84,42,244
Mvvv 88,34,208
v_max3_f32 v60, v60, v80, v81
v_max3_f32 v60, v60, v82, v83
Mvvv 88,38,228
Mvvv 88,42,248
Mvvv 92,34,212
s_nop 1
v_max3_f32 v64, v64, v84, v85
v_max3_f32 v64, v64, v86, v87
Mvvv 92,38,232
Mvvv 92,42,252
Dv 34,v1,55296
.p2align 3
v_mov_b32 v11, 0x3f803f80
v_permlane32_swap_b32 v56, v60
v_max3_f32 v68, v68, v88, v89
v_max3_f32 v68, v68, v90, v91
v_max_f32 v56, v56, v60
s_nop 1
v_max3_f32 v72, v72, v92, v93
v_max3_f32 v72, v72, v94, v95
v_mov_b32 v73, v72
v_permlane32_swap_b32 v64, v68
v_max_f32 v64, v64, v68
v_permlane32_swap_b32 v72, v73
v_max_f32 v72, v72, v73
v_mov_b32 v73, v72
v_permlane16_swap_b32 v56, v64
v_max_f32 v56, v56, v64
v_permlane16_swap_b32 v72, v73
B_ v56,0xc0e00000,v56
v_max_f32 v72, v72, v73
v_mov_b32 v64, v56
B_ v72,0xc0e00000,v72
v_mov_b32 v73, v72
v_permlane16_swap_b32 v56, v64
v_mov_b32 v60, v56
v_mov_b32 v68, v64
v_mov_b64 v[74:75], v[72:73]
v_permlane32_swap_b32 v56, v60
v_permlane32_swap_b32 v64, v68
v_mov_b32 v57, v56
v_mov_b32 v61, v60
v_mov_b32 v65, v64
v_mov_b32 v69, v68
v_pk_add_f32 v[96:97], v[96:97], v[56:57]
v_pk_add_f32 v[98:99], v[98:99], v[56:57]
v_pk_add_f32 v[76:77], v[76:77], v[56:57]
v_pk_add_f32 v[78:79], v[78:79], v[56:57]
v_mov_b64 v[58:59], v[56:57]
v_pk_add_f32 v[100:101], v[100:101], v[60:61]
v_pk_add_f32 v[102:103], v[102:103], v[60:61]
v_pk_add_f32 v[80:81], v[80:81], v[60:61]
v_pk_add_f32 v[82:83], v[82:83], v[60:61]
v_mov_b64 v[62:63], v[60:61]
v_pk_add_f32 v[104:105], v[104:105], v[64:65]
v_pk_add_f32 v[106:107], v[106:107], v[64:65]
v_pk_add_f32 v[84:85], v[84:85], v[64:65]
v_pk_add_f32 v[86:87], v[86:87], v[64:65]
v_mov_b64 v[66:67], v[64:65]
v_pk_add_f32 v[108:109], v[108:109], v[68:69]
v_pk_add_f32 v[110:111], v[110:111], v[68:69]
v_pk_add_f32 v[88:89], v[88:89], v[68:69]
v_pk_add_f32 v[90:91], v[90:91], v[68:69]
v_mov_b64 v[70:71], v[68:69]
v_pk_add_f32 v[112:113], v[112:113], v[72:73]
v_pk_add_f32 v[114:115], v[114:115], v[72:73]
v_pk_add_f32 v[92:93], v[92:93], v[72:73]
v_pk_add_f32 v[94:95], v[94:95], v[72:73]
E_ 96
E_ 97
E_ 98
E_ 99
E_ 76
E_ 77
E_ 78
E_ 79
C_ 99,98,99
C_ 98,96,97
C_ 96,76,77
C_ 97,78,79
E_ 100
E_ 101
E_ 102
E_ 103
E_ 80
E_ 81
E_ 82
E_ 83
C_ 103,102,103
C_ 102,100,101
C_ 100,80,81
C_ 101,82,83
.Lstep1_1:
W_ 1
Nvvv 116,30,136,56
s_nop 0
Dv 38,%0,56320
Dv 42,v1,56320
W_ 2
Mvvv 116,34,156
E_ 104
Nvvv 120,30,140,60
E_ 105
Mvvv 120,34,160
E_ 106
Nvvv 124,30,144,64
E_ 107
Mvvv 124,34,164
E_ 84
Nvvv 128,30,148,68
E_ 85
Mvvv 128,34,168
E_ 86
Nvvv 132,30,152,72
E_ 87
Mvvv 132,34,172
W_ 0
C_ 107,106,107
C_ 106,104,105
Mvvv 132,38,192
s_nop 0
Dv 30,%0,57344
Dv 34,v1,57344
Mvvv 132,42,212
C_ 104,84,85
C_ 105,86,87
Mvvv 128,38,188
E_ 108
Mvvv 128,42,208
E_ 109
Mvvv 124,38,184
E_ 110
Mvvv 124,42,204
E_ 111
Mvvv 120,38,180
E_ 88
Mvvv 120,42,200
E_ 89
Mvvv 116,38,176
E_ 90
Mvvv 116,42,196
W_ 0
E_ 91
Mvvv 116,30,216
s_nop 0
Dv 38,%0,49152
Dv 42,v1,49152
Mvvv 116,34,236
C_ 111,110,111
C_ 110,108,109
Mvvv 120,30,220
C_ 108,88,89
C_ 109,90,91
Mvvv 120,34,240
E_ 112
Mvvv 124,30,224
E_ 113
Mvvv 124,34,244
E_ 114
Mvvv 128,30,228
E_ 115
Mvvv 128,34,248
E_ 92
Mvvv 132,30,232
E_ 93
Mvvv 132,34,252
W_ 0
E_ 94
Nvvv 76,38,136,56
s_nop 0
Dv 30,%0,50176
Dv 34,v1,50176
Mvvv 76,42,156
E_ 95
Nvvv 80,38,140,60
C_ 115,114,115
C_ 114,112,113
Mvvv 80,42,160
C_ 112,92,93
C_ 113,94,95
Nvvv 84,38,144,64
E_ 116
Mvvv 84,42,164
E_ 117
Nvvv 88,38,148,68
E_ 118
Mvvv 88,42,168
E_ 119
Nvvv 92,38,152,72
C_ 119,118,119
C_ 118,116,117
Mvvv 92,42,172
W_ 0
E_ 120
Mvvv 92,30,192
s_nop 0
Dv 38,%0,51200
Dv 42,v1,51200
Mvvv 92,34,212
X_ a180,v11
X_ a181,v11
Mvvv 88,30,188
X_ a182,v11
X_ a183,v11
Mvvv 88,34,208
ds_write_b128 v12, a[132:135] offset:0
Mvvv 84,30,184
E_ 121
Mvvv 84,34,204
E_ 122
Mvvv 80,30,180
ds_write_b128 v12, a[136:139] offset:1024
Mvvv 80,34,200
E_ 123
Mvvv 76,30,176
C_ 123,122,123
C_ 122,120,121
Mvvv 76,34,196
ds_write_b128 v12, a[160:163] offset:2048
W_ 4
Mvvv 76,38,216
W_ 3
E_ 124
Mvvv 76,42,236
s_waitcnt vmcnt(12) lgkmcnt(0)
s_barrier
Mvvv 80,38,220
ds_write_b128 v12, a[164:167] offset:3072
Mvvv 80,42,240
E_ 125
Mvvv 84,38,224
E_ 126
Mvvv 84,42,244
s_nop 0
Tv 30,v2,24576
Tv 32,v2,30720
Mvvv 88,38,228
ds_write_b128 v12, a[168:171] offset:4096
Mvvv 88,42,248
s_nop 0
Tv 34,v3,24576
Tv 36,v3,30720
Mvvv 92,38,232
E_ 127
Mvvv 92,42,252
ds_write_b128 v12, a[172:175] offset:5120
Zaav 220,180,96
C_ 127,126,127
C_ 126,124,125
Zaav 224,180,100
E_ 128
Zaav 228,180,104
ds_write_b128 v12, a[176:179] offset:12288
Zaav 232,180,108
E_ 129
Zaav 236,180,112
)A"
R"A(W_ 2
E_ 130
Zavv 0,30,96
s_nop 0
Tv 38,v4,24576
Tv 40,v4,30720
Zavv 20,34,96
ds_write_b128 v12, a[200:203] offset:13312
Zavv 24,34,100
s_nop 0
Tv 42,v5,24576
Tv 44,v5,30720
Zavv 4,30,100
E_ 131
Zavv 8,30,104
ds_write_b128 v12, a[204:207] offset:14336
Zavv 28,34,104
R_ v46,a220
R_ v47,a224
Zavv 32,34,108
R_ v48,a228
R_ v49,a232
Zavv 12,30,108
R_ v50,a236
C_ 131,130,131
Zavv 16,30,112
ds_write_b128 v12, a[208:211] offset:15360
Zavv 36,34,112
W_ 2
C_ 130,128,129
Zavv 56,38,112
.Lpad_a1_1:
s_cbranch_execz .Lhop_a1_r
Tv 30,v2,25600
Tv 32,v2,31744
Zavv 76,42,112
ds_write_b128 v12, a[212:215] offset:16384
Zavv 72,42,108
s_nop 0
Tv 34,v3,25600
Tv 36,v3,31744
Zavv 52,38,108
E_ 132
Zavv 48,38,104
ds_write_b128 v12, a[216:219] offset:17408
Zavv 68,42,104
E_ 133
Zavv 64,42,100
E_ 134
Zavv 44,38,100
E_ 135
Zavv 40,38,96
C_ 135,134,135
C_ 134,132,133
Zavv 60,42,96
W_ 1
E_ 76
Zavv 80,30,96
s_nop 0
Tv 38,v4,25600
Tv 40,v4,31744
Zavv 100,34,96
E_ 77
Zavv 104,34,100
s_nop 0
Tv 42,v5,25600
Tv 44,v5,31744
Zavv 84,30,100
E_ 78
Zavv 88,30,104
E_ 79
Zavv 108,34,104
C_ 116,76,77
C_ 117,78,79
Zavv 112,34,108
E_ 80
Zavv 92,30,108
E_ 81
Zavv 96,30,112
E_ 82
Zavv 116,34,112
W_ 0
E_ 83
Zavv 136,38,112
s_nop 0
Tv 30,v2,26624
Tv 32,v2,32768
Zavv 156,42,112
C_ 120,80,81
C_ 121,82,83
Zavv 152,42,108
s_nop 0
Tv 34,v3,26624
Tv 36,v3,32768
Zavv 132,38,108
E_ 84
Zavv 128,38,104
E_ 85
Zavv 148,42,104
E_ 86
Zavv 144,42,100
E_ 87
Zavv 124,38,100
C_ 124,84,85
C_ 125,86,87
Zavv 120,38,96
E_ 88
Zavv 140,42,96
W_ 0
E_ 89
Zavv 160,30,96
s_nop 0
Tv 38,v4,26624
Tv 40,v4,32768
Zavv 180,34,96
E_ 90
Zavv 184,34,100
s_nop 0
Tv 42,v5,26624
Tv 44,v5,32768
Zavv 164,30,100
E_ 91
Zavv 168,30,104
C_ 128,88,89
C_ 129,90,91
Zavv 188,34,104
S_ s65,s61,36864
E_ 92
Zavv 192,34,108
S_ m0,s51,24576
E_ 93
Zavv 172,30,108
s_waitcnt vmcnt(6) lgkmcnt(0)
s_barrier
Zavv 176,30,112
Ln v7,32,s65,0
Zavv 196,34,112
E_ 94
Zavv 216,38,112
Dv 30,%0,6144
Zavv 236,42,112
Dv 34,v1,6144
Zavv 232,42,108
Ln v8,32,s65,3072
S_ m0,s51,25472
Zavv 212,38,108
E_ 95
Zavv 208,38,104
Ln v7,32,s65,128
Zavv 228,42,104
C_ 132,92,93
C_ 133,94,95
Zavv 224,42,100
Ln v8,32,s65,3200
S_ m0,s51,26368
Zavv 204,38,100
v_mov_b32 v92, 0x3f803f80
v_mov_b32 v93, 0x3f803f80
Zavv 200,38,96
Ln v7,32,s65,256
Zavv 220,42,96
v_mov_b32 v94, 0x3f803f80
v_mov_b32 v95, 0x3f803f80
Ln v8,32,s65,3328
.Lstep1_2:
s_nop 0
Zvvv 76,92,116
Dv 38,%0,7168
Zvvv 80,92,120
Dv 42,v1,7168
Zvvv 84,92,124
Zvvv 88,92,128
Zvvv 92,92,132
W_ 2
Nvvv 96,30,136,56
Mvvv 96,34,156
Nvvv 100,30,140,60
s_nop 1
v_max3_f32 v10, v76, v80, v84
s_nop 0
v_max3_f32 v10, v10, v88, v92
Mvvv 100,34,160
v_cmp_lt_f32 vcc, 0x42000000, v10
s_cbranch_vccnz .Ltramp1_1
Nvvv 104,30,144,64
A_ v46,v46,v76
A_ v47,v47,v80
Mvvv 104,34,164
A_ v48,v48,v84
A_ v49,v49,v88
Nvvv 108,30,148,68
A_ v50,v50,v92
Mvvv 108,34,168
Nvvv 112,30,152,72
Mvvv 112,34,172
W_ 0
Mvvv 112,38,192
Dv 30,%0,8192
Mvvv 112,42,212
Dv 34,v1,8192
Mvvv 108,38,188
Mvvv 108,42,208
Mvvv 104,38,184
Mvvv 104,42,204
Mvvv 100,38,180
Mvvv 100,42,200
Mvvv 96,38,176
Mvvv 96,42,196
W_ 0
Mvvv 96,30,216
Dv 38,%0,0
Mvvv 96,34,236
Dv 42,v1,0
Mvvv 100,30,220
Mvvv 100,34,240
Mvvv 104,30,224
Mvvv 104,34,244
Mvvv 108,30,228
Mvvv 108,34,248
s_nop 0
E_ 96
Mvvv 112,30,232
E_ 97
Mvvv 112,34,252
W_ 0
E_ 98
Nvvv 76,38,136,56
Dv 30,%0,1024
Mvvv 76,42,156
Dv 34,v1,1024
Nvvv 80,38,140,60
E_ 99
Mvvv 80,42,160
C_ 99,98,99
C_ 98,96,97
Nvvv 84,38,144,64
E_ 100
Mvvv 84,42,164
E_ 101
Nvvv 88,38,148,68
E_ 102
Mvvv 88,42,168
E_ 103
Nvvv 92,38,152,72
C_ 103,102,103
C_ 102,100,101
Mvvv 92,42,172
W_ 0
E_ 104
Mvvv 92,30,192
Dv 38,%0,2048
Mvvv 92,34,212
Dv 42,v1,2048
Mvvv 88,30,188
E_ 105
Mvvv 88,34,208
E_ 106
Mvvv 84,30,184
S_ s65,s61,24576
E_ 107
Mvvv 84,34,204
S_ m0,s51,0
C_ 107,106,107
C_ 106,104,105
Mvvv 80,30,180
s_waitcnt vmcnt(6) lgkmcnt(0)
s_barrier
Mvvv 80,34,200
L_ v9,36,s65,0
Mvvv 76,30,176
E_ 108
Mvvv 76,34,196
L_ v9,36,s65,3072
S_ m0,s51,896
Mvvv 76,38,216
Tv 30,v2,12288
Tv 32,v2,18432
Mvvv 76,42,236
L_ v9,36,s65,128
Mvvv 80,38,220
s_nop 0
Tv 34,v3,12288
Tv 36,v3,18432
Mvvv 80,42,240
L_ v9,36,s65,3200
S_ m0,s51,1792
Mvvv 84,38,224
E_ 109
Mvvv 84,42,244
L_ v9,36,s65,256
Mvvv 88,38,228
E_ 110
Mvvv 88,42,248
L_ v9,36,s65,3328
Mvvv 92,38,232
E_ 111
Mvvv 92,42,252
W_ 0
C_ 111,110,111
C_ 110,108,109
Mavv 0,30,116
s_nop 0
Tv 38,v4,12288
Tv 40,v4,18432
Mavv 20,34,116
E_ 112
Mavv 24,34,120
s_nop 0
Tv 42,v5,12288
Tv 44,v5,18432
Mavv 4,30,120
E_ 113
Mavv 8,30,124
E_ 114
Mavv 28,34,124
E_ 115
Mavv 32,34,128
C_ 115,114,115
C_ 114,112,113
Mavv 12,30,128
E_ 76
Mavv 16,30,132
E_ 77
Mavv 36,34,132
W_ 0
E_ 78
Mavv 56,38,132
s_nop 0
Tv 30,v2,13312
Tv 32,v2,19456
Mavv 76,42,132
E_ 79
Mavv 72,42,128
s_nop 0
Tv 34,v3,13312
Tv 36,v3,19456
Mavv 52,38,128
C_ 96,76,77
C_ 97,78,79
Mavv 48,38,124
E_ 80
Mavv 68,42,124
E_ 81
Mavv 64,42,120
E_ 82
Mavv 44,38,120
E_ 83
Mavv 40,38,116
C_ 100,80,81
C_ 101,82,83
Mavv 60,42,116
W_ 0
E_ 84
Mavv 80,30,116
s_nop 0
Tv 38,v4,13312
Tv 40,v4,19456
Mavv 100,34,116
E_ 85
Mavv 104,34,120
s_nop 0
Tv 42,v5,13312
Tv 44,v5,19456
Mavv 84,30,120
E_ 86
Mavv 88,30,124
E_ 87
Mavv 108,34,124
C_ 104,84,85
C_ 105,86,87
Mavv 112,34,128
E_ 88
Mavv 92,30,128
E_ 89
Mavv 96,30,132
E_ 90
Mavv 116,34,132
W_ 0
E_ 91
Mavv 136,38,132
s_nop 0
Tv 30,v2,14336
Tv 32,v2,20480
Mavv 156,42,132
C_ 108,88,89
C_ 109,90,91
Mavv 152,42,128
s_nop 0
Tv 34,v3,14336
Tv 36,v3,20480
Mavv 132,38,128
E_ 92
Mavv 128,38,124
E_ 93
Mavv 148,42,124
E_ 94
Mavv 144,42,120
E_ 95
Mavv 124,38,120
C_ 112,92,93
C_ 113,94,95
Mavv 120,38,116
v_mov_b32 v92, 0x3f803f80
v_mov_b32 v93, 0x3f803f80
Mavv 140,42,116
W_ 0
v_mov_b32 v94, 0x3f803f80
v_mov_b32 v95, 0x3f803f80
Mavv 160,30,116
s_nop 0
Tv 38,v4,14336
Tv 40,v4,20480
Mavv 180,34,116
Mavv 184,34,120
s_nop 0
Tv 42,v5,14336
Tv 44,v5,20480
Mavv 164,30,120
Mavv 168,30,124
Mavv 188,34,124
S_ s65,s61,49152
Mavv 192,34,128
S_ m0,s51,12288
Mavv 172,30,128
s_waitcnt vmcnt(6) lgkmcnt(0)
s_barrier
Mavv 176,30,132
Ln v7,32,s65,0
Mavv 196,34,132
Mavv 216,38,132
Dv 30,%0,30720
Mavv 236,42,132
Dv 34,v1,30720
Mavv 232,42,128
Ln v8,32,s65,3072
S_ m0,s51,13184
Mavv 212,38,128
Mavv 208,38,124
Ln v7,32,s65,128
Mavv 228,42,124
Mavv 224,42,120
Ln v8,32,s65,3200
S_ m0,s51,14080
Mavv 204,38,120
Mavv 200,38,116
Ln v7,32,s65,256
Mavv 220,42,116
s_cmp_eq_u32 s4, 0
s_cbranch_scc1 .Lhs_a1
.Lhs_a1_back:
.Lstep1_3:
Zvvv 76,92,96
Dv 38,%0,31744
Zvvv 80,92,100
Dv 42,v1,31744
Zvvv 84,92,104
Zvvv 88,92,108
Zvvv 92,92,112
W_ 2
Nvvv 116,30,136,56
Mvvv 116,34,156
Nvvv 120,30,140,60
s_nop 1
v_max3_f32 v10, v76, v80, v84
s_nop 0
v_max3_f32 v10, v10, v88, v92
Mvvv 120,34,160
v_cmp_lt_f32 vcc, 0x42000000, v10
s_cbranch_vccnz .Ltramp1_2
Nvvv 124,30,144,64
A_ v46,v46,v76
A_ v47,v47,v80
Mvvv 124,34,164
A_ v48,v48,v84
A_ v49,v49,v88
Nvvv 128,30,148,68
A_ v50,v50,v92
Mvvv 128,34,168
Ln v8,32,s65,3328
Nvvv 132,30,152,72
Mvvv 132,34,172
W_ 0
Mvvv 132,38,192
Dv 30,%0,32768
Mvvv 132,42,212
Dv 34,v1,32768
Mvvv 128,38,188
Mvvv 128,42,208
Mvvv 124,38,184
Mvvv 124,42,204
Mvvv 120,38,180
Mvvv 120,42,200
Mvvv 116,38,176
Mvvv 116,42,196
W_ 0
Mvvv 116,30,216
Dv 38,%0,24576
Mvvv 116,34,236
Dv 42,v1,24576
Mvvv 120,30,220
Mvvv 120,34,240
Mvvv 124,30,224
Mvvv 124,34,244
Mvvv 128,30,228
Mvvv 128,34,248
s_nop 0
E_ 116
Mvvv 132,30,232
E_ 117
Mvvv 132,34,252
W_ 0
E_ 118
Nvvv 76,38,136,56
Dv 30,%0,25600
Mvvv 76,42,156
Dv 34,v1,25600
Nvvv 80,38,140,60
E_ 119
Mvvv 80,42,160
C_ 119,118,119
C_ 118,116,117
Nvvv 84,38,144,64
E_ 120
Mvvv 84,42,164
E_ 121
Nvvv 88,38,148,68
E_ 122
Mvvv 88,42,168
E_ 123
Nvvv 92,38,152,72
C_ 123,122,123
C_ 122,120,121
Mvvv 92,42,172
W_ 0
E_ 124
Mvvv 92,30,192
Dv 38,%0,26624
Mvvv 92,34,212
Dv 42,v1,26624
Mvvv 88,30,188
E_ 125
Mvvv 88,34,208
E_ 126
Mvvv 84,30,184
S_ s65,s61,36864
E_ 127
Mvvv 84,34,204
S_ m0,s51,24576
C_ 127,126,127
C_ 126,124,125
Mvvv 80,30,180
s_waitcnt vmcnt(6) lgkmcnt(0)
s_barrier
Mvvv 80,34,200
L_ v9,36,s65,0
Mvvv 76,30,176
E_ 128
Mvvv 76,34,196
L_ v9,36,s65,3072
S_ m0,s51,25472
Mvvv 76,38,216
Tv 30,v2,0
Tv 32,v2,6144
Mvvv 76,42,236
L_ v9,36,s65,128
Mvvv 80,38,220
s_nop 0
Tv 34,v3,0
Tv 36,v3,6144
Mvvv 80,42,240
L_ v9,36,s65,3200
S_ m0,s51,26368
Mvvv 84,38,224
E_ 129
Mvvv 84,42,244
L_ v9,36,s65,256
Mvvv 88,38,228
E_ 130
Mvvv 88,42,248
L_ v9,36,s65,3328
Mvvv 92,38,232
E_ 131
Mvvv 92,42,252
W_ 0
C_ 131,130,131
C_ 130,128,129
Mavv 0,30,96
s_nop 0
Tv 38,v4,0
Tv 40,v4,6144
Mavv 20,34,96
E_ 132
Mavv 24,34,100
s_nop 0
Tv 42,v5,0
Tv 44,v5,6144
Mavv 4,30,100
E_ 133
Mavv 8,30,104
E_ 134
Mavv 28,34,104
E_ 135
Mavv 32,34,108
C_ 135,134,135
C_ 134,132,133
Mavv 12,30,108
E_ 76
Mavv 16,30,112
E_ 77
Mavv 36,34,112
W_ 0
E_ 78
Mavv 56,38,112
s_nop 0
Tv 30,v2,1024
Tv 32,v2,7168
Mavv 76,42,112
E_ 79
Mavv 72,42,108
s_nop 0
Tv 34,v3,1024
Tv 36,v3,7168
Mavv 52,38,108
.Lpad_a1_2:
s_cbranch_execz .Lhop_a1_r
C_ 116,76,77
C_ 117,78,79
Mavv 48,38,104
E_ 80
Mavv 68,42,104
E_ 81
Mavv 64,42,100
E_ 82
Mavv 44,38,100
E_ 83
Mavv 40,38,96
C_ 120,80,81
C_ 121,82,83
Mavv 60,42,96
W_ 0
E_ 84
Mavv 80,30,96
s_nop 0
Tv 38,v4,1024
Tv 40,v4,7168
Mavv 100,34,96
E_ 85
Mavv 104,34,100
s_nop 0
Tv 42,v5,1024
Tv 44,v5,7168
Mavv 84,30,100
E_ 86
Mavv 88,30,104
E_ 87
Mavv 108,34,104
C_ 124,84,85
C_ 125,86,87
Mavv 112,34,108
E_ 88
Mavv 92,30,108
E_ 89
Mavv 96,30,112
E_ 90
Mavv 116,34,112
W_ 0
E_ 91
Mavv 136,38,112
s_nop 0
Tv 30,v2,2048
Tv 32,v2,8192
Mavv 156,42,112
C_ 128,88,89
C_ 129,90,91
Mavv 152,42,108
s_nop 0
Tv 34,v3,2048
Tv 36,v3,8192
Mavv 132,38,108
E_ 92
Mavv 128,38,104
E_ 93
Mavv 148,42,104
E_ 94
Mavv 144,42,100
E_ 95
Mavv 124,38,100
C_ 132,92,93
C_ 133,94,95
Mavv 120,38,96
v_mov_b32 v92, 0x3f803f80
v_mov_b32 v93, 0x3f803f80
Mavv 140,42,96
W_ 0
v_mov_b32 v94, 0x3f803f80
v_mov_b32 v95, 0x3f803f80
Mavv 160,30,96
s_nop 0
Tv 38,v4,2048
Tv 40,v4,8192
Mavv 180,34,96
Mavv 184,34,100
s_nop 0
Tv 42,v5,2048
Tv 44,v5,8192
Mavv 164,30,100
Mavv 168,30,104
Mavv 188,34,104
S_ s65,s61,61440
Mavv 192,34,108
S_ m0,s51,0
Mavv 172,30,108
s_waitcnt vmcnt(6) lgkmcnt(0)
s_barrier
Mavv 176,30,112
Ln v7,32,s65,0
Mavv 196,34,112
Mavv 216,38,112
Dv 30,%0,18432
Mavv 236,42,112
Dv 34,v1,18432
Mavv 232,42,108
Ln v8,32,s65,3072
S_ m0,s51,896
Mavv 212,38,108
Mavv 208,38,104
Ln v7,32,s65,128
Mavv 228,42,104
Mavv 224,42,100
Ln v8,32,s65,3200
S_ m0,s51,1792
Mavv 204,38,100
Mavv 200,38,96
Ln v7,32,s65,256
Mavv 220,42,96
.Lstep1_4:
Zvvv 76,92,116
Dv 38,%0,19456
Zvvv 80,92,120
Dv 42,v1,19456
Zvvv 84,92,124
Zvvv 88,92,128
Zvvv 92,92,132
W_ 2
Nvvv 96,30,136,56
Mvvv 96,34,156
Nvvv 100,30,140,60
s_nop 1
v_max3_f32 v10, v76, v80, v84
s_nop 0
v_max3_f32 v10, v10, v88, v92
Mvvv 100,34,160
v_cmp_lt_f32 vcc, 0x42000000, v10
s_cbranch_vccnz .Ltramp1_3
Nvvv 104,30,144,64
A_ v46,v46,v76
A_ v47,v47,v80
Mvvv 104,34,164
A_ v48,v48,v84
A_ v49,v49,v88
Nvvv 108,30,148,68
A_ v50,v50,v92
Mvvv 108,34,168
Ln v8,32,s65,3328
Nvvv 112,30,152,72
Mvvv 112,34,172
W_ 0
Mvvv 112,38,192
Dv 30,%0,20480
Mvvv 112,42,212
Dv 34,v1,20480
Mvvv 108,38,188
Mvvv 108,42,208
Mvvv 104,38,184
Mvvv 104,42,204
Mvvv 100,38,180
Mvvv 100,42,200
Mvvv 96,38,176
Mvvv 96,42,196
W_ 0
Mvvv 96,30,216
Dv 38,%0,12288
Mvvv 96,34,236
Dv 42,v1,12288
Mvvv 100,30,220
Mvvv 100,34,240
Mvvv 104,30,224
Mvvv 104,34,244
Mvvv 108,30,228
Mvvv 108,34,248
s_nop 0
E_ 96
Mvvv 112,30,232
E_ 97
Mvvv 112,34,252
W_ 0
E_ 98
Nvvv 76,38,136,56
Dv 30,%0,13312
Mvvv 76,42,156
Dv 34,v1,13312
Nvvv 80,38,140,60
E_ 99
Mvvv 80,42,160
C_ 99,98,99
C_ 98,96,97
Nvvv 84,38,144,64
E_ 100
Mvvv 84,42,164
E_ 101
Nvvv 88,38,148,68
E_ 102
Mvvv 88,42,168
E_ 103
Nvvv 92,38,152,72
C_ 103,102,103
C_ 102,100,101
Mvvv 92,42,172
W_ 0
E_ 104
Mvvv 92,30,192
Dv 38,%0,14336
Mvvv 92,34,212
Dv 42,v1,14336
Mvvv 88,30,188
E_ 105
Mvvv 88,34,208
E_ 106
Mvvv 84,30,184
S_ s65,s61,49152
E_ 107
Mvvv 84,34,204
S_ m0,s51,12288
C_ 107,106,107
C_ 106,104,105
Mvvv 80,30,180
s_waitcnt vmcnt(6) lgkmcnt(0)
s_barrier
Mvvv 80,34,200
L_ v9,36,s65,0
Mvvv 76,30,176
E_ 108
Mvvv 76,34,196
L_ v9,36,s65,3072
S_ m0,s51,13184
Mvvv 76,38,216
Tv 30,v2,24576
Tv 32,v2,30720
Mvvv 76,42,236
L_ v9,36,s65,128
Mvvv 80,38,220
s_nop 0
Tv 34,v3,24576
Tv 36,v3,30720
Mvvv 80,42,240
L_ v9,36,s65,3200
S_ m0,s51,14080
Mvvv 84,38,224
E_ 109
Mvvv 84,42,244
L_ v9,36,s65,256
Mvvv 88,38,228
E_ 110
Mvvv 88,42,248
L_ v9,36,s65,3328
Mvvv 92,38,232
E_ 111
Mvvv 92,42,252
W_ 0
C_ 111,110,111
C_ 110,108,109
Mavv 0,30,116
s_nop 0
Tv 38,v4,24576
Tv 40,v4,30720
Mavv 20,34,116
E_ 112
Mavv 24,34,120
s_nop 0
Tv 42,v5,24576
Tv 44,v5,30720
Mavv 4,30,120
E_ 113
Mavv 8,30,124
E_ 114
Mavv 28,34,124
E_ 115
Mavv 32,34,128
C_ 115,114,115
C_ 114,112,113
Mavv 12,30,128
E_ 76
Mavv 16,30,132
E_ 77
Mavv 36,34,132
W_ 0
E_ 78
Mavv 56,38,132
s_nop 0
Tv 30,v2,25600
Tv 32,v2,31744
Mavv 76,42,132
E_ 79
Mavv 72,42,128
s_nop 0
Tv 34,v3,25600
Tv 36,v3,31744
Mavv 52,38,128
C_ 96,76,77
C_ 97,78,79
Mavv 48,38,124
E_ 80
Mavv 68,42,124
E_ 81
Mavv 64,42,120
E_ 82
Mavv 44,38,120
E_ 83
Mavv 40,38,116
C_ 100,80,81
C_ 101,82,83
Mavv 60,42,116
W_ 0
E_ 84
Mavv 80,30,116
s_nop 0
Tv 38,v4,25600
Tv 40,v4,31744
Mavv 100,34,116
E_ 85
Mavv 104,34,120
s_nop 0
Tv 42,v5,25600
Tv 44,v5,31744
Mavv 84,30,120
E_ 86
Mavv 88,30,124
E_ 87
Mavv 108,34,124
C_ 104,84,85
C_ 105,86,87
Mavv 112,34,128
E_ 88
Mavv 92,30,128
E_ 89
Mavv 96,30,132
E_ 90
Mavv 116,34,132
W_ 0
E_ 91
Mavv 136,38,132
s_nop 0
Tv 30,v2,26624
Tv 32,v2,32768
Mavv 156,42,132
C_ 108,88,89
C_ 109,90,91
Mavv 152,42,128
s_nop 0
Tv 34,v3,26624
Tv 36,v3,32768
Mavv 132,38,128
E_ 92
Mavv 128,38,124
E_ 93
Mavv 148,42,124
E_ 94
Mavv 144,42,120
E_ 95
Mavv 124,38,120
C_ 112,92,93
C_ 113,94,95
Mavv 120,38,116
v_mov_b32 v92, 0x3f803f80
v_mov_b32 v93, 0x3f803f80
Mavv 140,42,116
W_ 0
v_mov_b32 v94, 0x3f803f80
v_mov_b32 v95, 0x3f803f80
Mavv 160,30,116
s_nop 0
Tv 38,v4,26624
Tv 40,v4,32768
Mavv 180,34,116
Mavv 184,34,120
s_nop 0
Tv 42,v5,26624
Tv 44,v5,32768
Mavv 164,30,120
Mavv 168,30,124
Mavv 188,34,124
S_ s65,s61,73728
Mavv 192,34,128
S_ m0,s51,24576
Mavv 172,30,128
s_waitcnt vmcnt(6) lgkmcnt(0)
s_barrier
Mavv 176,30,132
Ln v7,32,s65,0
Mavv 196,34,132
Mavv 216,38,132
Dv 30,%0,6144
Mavv 236,42,132
Dv 34,v1,6144
Mavv 232,42,128
Ln v8,32,s65,3072
S_ m0,s51,25472
Mavv 212,38,128
Mavv 208,38,124
Ln v7,32,s65,128
Mavv 228,42,124
Mavv 224,42,120
Ln v8,32,s65,3200
S_ m0,s51,26368
Mavv 204,38,120
Mavv 200,38,116
Ln v7,32,s65,256
Mavv 220,42,116
s_cmp_eq_u32 s4, 0
s_cbranch_scc1 .Lhs_b1
.Lhs_b1_back:
.Lstep1_5:
Zvvv 76,92,96
Dv 38,%0,7168
Zvvv 80,92,100
Dv 42,v1,7168
Zvvv 84,92,104
Zvvv 88,92,108
Zvvv 92,92,112
W_ 2
Nvvv 116,30,136,56
Mvvv 116,34,156
Nvvv 120,30,140,60
s_nop 1
v_max3_f32 v10, v76, v80, v84
s_nop 0
v_max3_f32 v10, v10, v88, v92
Mvvv 120,34,160
v_cmp_lt_f32 vcc, 0x42000000, v10
s_cbranch_vccnz .Ltramp1_4
Nvvv 124,30,144,64
A_ v46,v46,v76
A_ v47,v47,v80
Mvvv 124,34,164
A_ v48,v48,v84
A_ v49,v49,v88
Nvvv 128,30,148,68
A_ v50,v50,v92
Mvvv 128,34,168
Ln v8,32,s65,3328
Nvvv 132,30,152,72
Mvvv 132,34,172
W_ 0
Mvvv 132,38,192
Dv 30,%0,8192
Mvvv 132,42,212
Dv 34,v1,8192
Mvvv 128,38,188
Mvvv 128,42,208
Mvvv 124,38,184
Mvvv 124,42,204
Mvvv 120,38,180
Mvvv 120,42,200
Mvvv 116,38,176
Mvvv 116,42,196
W_ 0
Mvvv 116,30,216
Dv 38,%0,0
Mvvv 116,34,236
Dv 42,v1,0
Mvvv 120,30,220
Mvvv 120,34,240
Mvvv 124,30,224
Mvvv 124,34,244
Mvvv 128,30,228
Mvvv 128,34,248
s_nop 0
E_ 116
Mvvv 132,30,232
E_ 117
Mvvv 132,34,252
W_ 0
E_ 118
Nvvv 76,38,136,56
Dv 30,%0,1024
Mvvv 76,42,156
Dv 34,v1,1024
Nvvv 80,38,140,60
E_ 119
Mvvv 80,42,160
C_ 119,118,119
C_ 118,116,117
Nvvv 84,38,144,64
E_ 120
Mvvv 84,42,164
E_ 121
Nvvv 88,38,148,68
E_ 122
Mvvv 88,42,168
E_ 123
Nvvv 92,38,152,72
C_ 123,122,123
C_ 122,120,121
Mvvv 92,42,172
W_ 0
E_ 124
Mvvv 92,30,192
Dv 38,%0,2048
Mvvv 92,34,212
Dv 42,v1,2048
Mvvv 88,30,188
E_ 125
Mvvv 88,34,208
E_ 126
Mvvv 84,30,184
S_ s65,s61,61440
E_ 127
Mvvv 84,34,204
S_ m0,s51,0
C_ 127,126,127
C_ 126,124,125
Mvvv 80,30,180
s_waitcnt vmcnt(6) lgkmcnt(0)
s_barrier
Mvvv 80,34,200
L_ v9,36,s65,0
Mvvv 76,30,176
E_ 128
Mvvv 76,34,196
L_ v9,36,s65,3072
S_ m0,s51,896
Mvvv 76,38,216
Tv 30,v2,12288
Tv 32,v2,18432
Mvvv 76,42,236
L_ v9,36,s65,128
Mvvv 80,38,220
s_nop 0
Tv 34,v3,12288
Tv 36,v3,18432
Mvvv 80,42,240
L_ v9,36,s65,3200
S_ m0,s51,1792
Mvvv 84,38,224
E_ 129
Mvvv 84,42,244
L_ v9,36,s65,256
Mvvv 88,38,228
E_ 130
Mvvv 88,42,248
L_ v9,36,s65,3328
Mvvv 92,38,232
E_ 131
Mvvv 92,42,252
W_ 0
C_ 131,130,131
C_ 130,128,129
Mavv 0,30,96
s_nop 0
Tv 38,v4,12288
Tv 40,v4,18432
Mavv 20,34,96
E_ 132
Mavv 24,34,100
s_nop 0
Tv 42,v5,12288
Tv 44,v5,18432
Mavv 4,30,100
E_ 133
Mavv 8,30,104
E_ 134
Mavv 28,34,104
E_ 135
Mavv 32,34,108
C_ 135,134,135
C_ 134,132,133
Mavv 12,30,108
E_ 76
Mavv 16,30,112
E_ 77
Mavv 36,34,112
W_ 0
E_ 78
Mavv 56,38,112
s_nop 0
Tv 30,v2,13312
Tv 32,v2,19456
Mavv 76,42,112
E_ 79
Mavv 72,42,108
s_nop 0
Tv 34,v3,13312
Tv 36,v3,19456
Mavv 52,38,108
C_ 116,76,77
C_ 117,78,79
Mavv 48,38,104
E_ 80
Mavv 68,42,104
E_ 81
Mavv 64,42,100
E_ 82
Mavv 44,38,100
E_ 83
Mavv 40,38,96
C_ 120,80,81
C_ 121,82,83
Mavv 60,42,96
W_ 0
E_ 84
Mavv 80,30,96
s_nop 0
Tv 38,v4,13312
Tv 40,v4,19456
Mavv 100,34,96
E_ 85
Mavv 104,34,100
s_nop 0
Tv 42,v5,13312
Tv 44,v5,19456
Mavv 84,30,100
.Lpad_a1_3:
s_cbranch_execz .Lhop_a1_r
E_ 86
Mavv 88,30,104
E_ 87
Mavv 108,34,104
C_ 124,84,85
C_ 125,86,87
Mavv 112,34,108
E_ 88
Mavv 92,30,108
E_ 89
Mavv 96,30,112
E_ 90
Mavv 116,34,112
W_ 0
E_ 91
Mavv 136,38,112
s_nop 0
Tv 30,v2,14336
Tv 32,v2,20480
Mavv 156,42,112
C_ 128,88,89
C_ 129,90,91
Mavv 152,42,108
s_nop 0
Tv 34,v3,14336
Tv 36,v3,20480
Mavv 132,38,108
E_ 92
Mavv 128,38,104
E_ 93
Mavv 148,42,104
E_ 94
Mavv 144,42,100
E_ 95
Mavv 124,38,100
C_ 132,92,93
C_ 133,94,95
Mavv 120,38,96
v_mov_b32 v92, 0x3f803f80
v_mov_b32 v93, 0x3f803f80
Mavv 140,42,96
W_ 0
v_mov_b32 v94, 0x3f803f80
v_mov_b32 v95, 0x3f803f80
Mavv 160,30,96
s_nop 0
Tv 38,v4,14336
Tv 40,v4,20480
Mavv 180,34,96
Mavv 184,34,100
s_nop 0
Tv 42,v5,14336
Tv 44,v5,20480
Mavv 164,30,100
Mavv 168,30,104
Mavv 188,34,104
S_ s65,s61,86016
Mavv 192,34,108
S_ m0,s51,12288
Mavv 172,30,108
s_waitcnt vmcnt(6) lgkmcnt(0)
s_barrier
Mavv 176,30,112
Ln v7,32,s65,0
Mavv 196,34,112
Mavv 216,38,112
Dv 30,%0,30720
Mavv 236,42,112
Dv 34,v1,30720
Mavv 232,42,108
Ln v8,32,s65,3072
S_ m0,s51,13184
Mavv 212,38,108
Mavv 208,38,104
Ln v7,32,s65,128
Mavv 228,42,104
Mavv 224,42,100
Ln v8,32,s65,3200
S_ m0,s51,14080
Mavv 204,38,100
Mavv 200,38,96
Ln v7,32,s65,256
Mavv 220,42,96
.Lstep1_6:
Zvvv 76,92,116
Dv 38,%0,31744
Zvvv 80,92,120
Dv 42,v1,31744
Zvvv 84,92,124
Zvvv 88,92,128
Zvvv 92,92,132
W_ 2
Nvvv 96,30,136,56
Mvvv 96,34,156
Nvvv 100,30,140,60
s_nop 1
v_max3_f32 v10, v76, v80, v84
s_nop 0
v_max3_f32 v10, v10, v88, v92
Mvvv 100,34,160
v_cmp_lt_f32 vcc, 0x42000000, v10
s_cbranch_vccnz .Ltramp1_5
Nvvv 104,30,144,64
A_ v46,v46,v76
A_ v47,v47,v80
Mvvv 104,34,164
A_ v48,v48,v84
A_ v49,v49,v88
Nvvv 108,30,148,68
A_ v50,v50,v92
Mvvv 108,34,168
Ln v8,32,s65,3328
Nvvv 112,30,152,72
R_ v11,a248
Mvvv 112,34,172
W_ 0
v_min_u32 v11, 240, v11
Mvvv 112,38,192
Dv 30,%0,32768
v_lshlrev_b32 v11, 3, v11
Mvvv 112,42,212
Dv 34,v1,32768
Mvvv 108,38,188
buffer_load_dword a254, v11, s[44:47], s66 offen
Mvvv 108,42,208
Mvvv 104,38,184
Mvvv 104,42,204
Mvvv 100,38,180
Mvvv 100,42,200
Mvvv 96,38,176
Mvvv 96,42,196
W_ 0
Mvvv 96,30,216
Dv 38,%0,24576
Mvvv 96,34,236
Dv 42,v1,24576
Mvvv 100,30,220
Mvvv 100,34,240
Mvvv 104,30,224
Mvvv 104,34,244
Mvvv 108,30,228
Mvvv 108,34,248
s_nop 0
E_ 96
Mvvv 112,30,232
E_ 97
Mvvv 112,34,252
W_ 0
E_ 98
Nvvv 76,38,136,56
Dv 30,%0,25600
Mvvv 76,42,156
Dv 34,v1,25600
Nvvv 80,38,140,60
E_ 99
Mvvv 80,42,160
C_ 99,98,99
C_ 98,96,97
Nvvv 84,38,144,64
E_ 100
Mvvv 84,42,164
E_ 101
Nvvv 88,38,148,68
E_ 102
Mvvv 88,42,168
E_ 103
Nvvv 92,38,152,72
C_ 103,102,103
C_ 102,100,101
Mvvv 92,42,172
W_ 0
E_ 104
Mvvv 92,30,192
Dv 38,%0,26624
Mvvv 92,34,212
Dv 42,v1,26624
Mvvv 88,30,188
E_ 105
Mvvv 88,34,208
E_ 106
Mvvv 84,30,184
S_ s65,s61,73728
E_ 107
Mvvv 84,34,204
S_ m0,s51,24576
C_ 107,106,107
C_ 106,104,105
Mvvv 80,30,180
s_waitcnt vmcnt(7) lgkmcnt(0)
s_barrier
Mvvv 80,34,200
L_ v9,36,s65,0
Mvvv 76,30,176
E_ 108
Mvvv 76,34,196
L_ v9,36,s65,3072
S_ m0,s51,25472
Mvvv 76,38,216
Tv 30,v2,0
Tv 32,v2,6144
Mvvv 76,42,236
L_ v9,36,s65,128
Mvvv 80,38,220
s_nop 0
Tv 34,v3,0
Tv 36,v3,6144
Mvvv 80,42,240
L_ v9,36,s65,3200
S_ m0,s51,26368
Mvvv 84,38,224
E_ 109
Mvvv 84,42,244
L_ v9,36,s65,256
Mvvv 88,38,228
E_ 110
Mvvv 88,42,248
L_ v9,36,s65,3328
Mvvv 92,38,232
E_ 111
Mvvv 92,42,252
W_ 0
C_ 111,110,111
C_ 110,108,109
Mavv 0,30,116
s_nop 0
Tv 38,v4,0
Tv 40,v4,6144
Mavv 20,34,116
E_ 112
Mavv 24,34,120
s_nop 0
Tv 42,v5,0
Tv 44,v5,6144
Mavv 4,30,120
E_ 113
Mavv 8,30,124
E_ 114
Mavv 28,34,124
E_ 115
Mavv 32,34,128
C_ 115,114,115
C_ 114,112,113
Mavv 12,30,128
E_ 76
Mavv 16,30,132
E_ 77
Mavv 36,34,132
W_ 0
E_ 78
Mavv 56,38,132
s_nop 0
Tv 30,v2,1024
Tv 32,v2,7168
Mavv 76,42,132
E_ 79
Mavv 72,42,128
s_nop 0
Tv 34,v3,1024
Tv 36,v3,7168
Mavv 52,38,128
C_ 96,76,77
C_ 97,78,79
Mavv 48,38,124
E_ 80
Mavv 68,42,124
E_ 81
Mavv 64,42,120
E_ 82
Mavv 44,38,120
E_ 83
Mavv 40,38,116
C_ 100,80,81
C_ 101,82,83
Mavv 60,42,116
W_ 0
E_ 84
Mavv 80,30,116
s_nop 0
Tv 38,v4,1024
Tv 40,v4,7168
Mavv 100,34,116
E_ 85
Mavv 104,34,120
s_nop 0
Tv 42,v5,1024
Tv 44,v5,7168
Mavv 84,30,120
E_ 86
Mavv 88,30,124
E_ 87
Mavv 108,34,124
C_ 104,84,85
C_ 105,86,87
Mavv 112,34,128
E_ 88
Mavv 92,30,128
E_ 89
Mavv 96,30,132
E_ 90
Mavv 116,34,132
W_ 0
E_ 91
Mavv 136,38,132
s_nop 0
Tv 30,v2,2048
Tv 32,v2,8192
Mavv 156,42,132
C_ 108,88,89
C_ 109,90,91
Mavv 152,42,128
s_nop 0
Tv 34,v3,2048
Tv 36,v3,8192
Mavv 132,38,128
E_ 92
Mavv 128,38,124
E_ 93
Mavv 148,42,124
E_ 94
Mavv 144,42,120
E_ 95
Mavv 124,38,120
C_ 112,92,93
C_ 113,94,95
Mavv 120,38,116
v_mov_b32 v92, 0x3f803f80
v_mov_b32 v93, 0x3f803f80
Mavv 140,42,116
W_ 0
v_mov_b32 v94, 0x3f803f80
v_mov_b32 v95, 0x3f803f80
Mavv 160,30,116
s_nop 0
Tv 38,v4,2048
Tv 40,v4,8192
Mavv 180,34,116
Mavv 184,34,120
s_nop 0
Tv 42,v5,2048
Tv 44,v5,8192
Mavv 164,30,120
Mavv 168,30,124
Mavv 188,34,124
S_ s65,s61,98304
Mavv 192,34,128
S_ m0,s51,0
Mavv 172,30,128
s_waitcnt vmcnt(7) lgkmcnt(0)
s_barrier
Mavv 176,30,132
Ln v7,32,s65,0
Mavv 196,34,132
Mavv 216,38,132
Dv 30,%0,18432
Mavv 236,42,132
Dv 34,v1,18432
Mavv 232,42,128
Ln v8,32,s65,3072
S_ m0,s51,896
Mavv 212,38,128
Mavv 208,38,124
Ln v7,32,s65,128
Mavv 228,42,124
Mavv 224,42,120
Ln v8,32,s65,3200
S_ m0,s51,1792
Mavv 204,38,120
Mavv 200,38,116
Ln v7,32,s65,256
Mavv 220,42,116
s_cmp_eq_u32 s4, 0
s_cbranch_scc1 .Lhs_c1
.Lhs_c1_back:
.Lstep1_7:
Zvvv 76,92,96
Dv 38,%0,19456
Zvvv 80,92,100
Dv 42,v1,19456
Zvvv 84,92,104
Zvvv 88,92,108
Zvvv 92,92,112
W_ 2
Nvvv 116,30,136,56
Mvvv 116,34,156
Nvvv 120,30,140,60
s_nop 1
v_max3_f32 v10, v76, v80, v84
s_nop 0
v_max3_f32 v10, v10, v88, v92
Mvvv 120,34,160
v_cmp_lt_f32 vcc, 0x42000000, v10
s_cbranch_vccnz .Ltramp1_6
Nvvv 124,30,144,64
A_ v46,v46,v76
A_ v47,v47,v80
Mvvv 124,34,164
A_ v48,v48,v84
A_ v49,v49,v88
Nvvv 128,30,148,68
A_ v50,v50,v92
Mvvv 128,34,168
Ln v8,32,s65,3328
Nvvv 132,30,152,72
Mvvv 132,34,172
W_ 0
Mvvv 132,38,192
Dv 30,%0,20480
Mvvv 132,42,212
Dv 34,v1,20480
Mvvv 128,38,188
Mvvv 128,42,208
Mvvv 124,38,184
Mvvv 124,42,204
Mvvv 120,38,180
Mvvv 120,42,200
Mvvv 116,38,176
Mvvv 116,42,196
W_ 0
Mvvv 116,30,216
Dv 38,%0,12288
Mvvv 116,34,236
Dv 42,v1,12288
Mvvv 120,30,220
Mvvv 120,34,240
Mvvv 124,30,224
Mvvv 124,34,244
Mvvv 128,30,228
Mvvv 128,34,248
s_nop 0
E_ 116
Mvvv 132,30,232
E_ 117
Mvvv 132,34,252
W_ 0
E_ 118
Nvvv 76,38,136,56
Dv 30,%0,13312
Mvvv 76,42,156
Dv 34,v1,13312
Nvvv 80,38,140,60
E_ 119
Mvvv 80,42,160
C_ 119,118,119
C_ 118,116,117
Nvvv 84,38,144,64
E_ 120
Mvvv 84,42,164
E_ 121
Nvvv 88,38,148,68
E_ 122
Mvvv 88,42,168
E_ 123
Nvvv 92,38,152,72
C_ 123,122,123
C_ 122,120,121
Mvvv 92,42,172
W_ 0
E_ 124
Mvvv 92,30,192
Dv 38,%0,14336
Mvvv 92,34,212
Dv 42,v1,14336
Mvvv 88,30,188
E_ 125
Mvvv 88,34,208
E_ 126
Mvvv 84,30,184
S_ s65,s61,86016
E_ 127
Mvvv 84,34,204
S_ m0,s51,12288
C_ 127,126,127
C_ 126,124,125
Mvvv 80,30,180
s_waitcnt vmcnt(6) lgkmcnt(0)
s_barrier
Mvvv 80,34,200
L_ v9,36,s65,0
Mvvv 76,30,176
E_ 128
Mvvv 76,34,196
L_ v9,36,s65,3072
S_ m0,s51,13184
Mvvv 76,38,216
Tv 30,v2,24576
Tv 32,v2,30720
Mvvv 76,42,236
L_ v9,36,s65,128
Mvvv 80,38,220
s_nop 0
Tv 34,v3,24576
Tv 36,v3,30720
Mvvv 80,42,240
L_ v9,36,s65,3200
S_ m0,s51,14080
Mvvv 84,38,224
E_ 129
Mvvv 84,42,244
L_ v9,36,s65,256
Mvvv 88,38,228
E_ 130
Mvvv 88,42,248
L_ v9,36,s65,3328
Mvvv 92,38,232
E_ 131
Mvvv 92,42,252
W_ 0
C_ 131,130,131
C_ 130,128,129
Mavv 0,30,96
s_nop 0
Tv 38,v4,24576
Tv 40,v4,30720
Mavv 20,34,96
E_ 132
Mavv 24,34,100
s_nop 0
Tv 42,v5,24576
Tv 44,v5,30720
Mavv 4,30,100
E_ 133
Mavv 8,30,104
E_ 134
Mavv 28,34,104
E_ 135
Mavv 32,34,108
C_ 135,134,135
C_ 134,132,133
Mavv 12,30,108
E_ 76
Mavv 16,30,112
E_ 77
Mavv 36,34,112
W_ 0
E_ 78
Mavv 56,38,112
s_nop 0
Tv 30,v2,25600
Tv 32,v2,31744
Mavv 76,42,112
E_ 79
Mavv 72,42,108
s_nop 0
Tv 34,v3,25600
Tv 36,v3,31744
Mavv 52,38,108
C_ 116,76,77
C_ 117,78,79
Mavv 48,38,104
E_ 80
Mavv 68,42,104
E_ 81
Mavv 64,42,100
E_ 82
Mavv 44,38,100
E_ 83
Mavv 40,38,96
C_ 120,80,81
C_ 121,82,83
Mavv 60,42,96
W_ 0
E_ 84
Mavv 80,30,96
s_nop 0
Tv 38,v4,25600
Tv 40,v4,31744
Mavv 100,34,96
E_ 85
Mavv 104,34,100
s_nop 0
Tv 42,v5,25600
Tv 44,v5,31744
Mavv 84,30,100
E_ 86
Mavv 88,30,104
E_ 87
Mavv 108,34,104
C_ 124,84,85
C_ 125,86,87
Mavv 112,34,108
E_ 88
Mavv 92,30,108
E_ 89
Mavv 96,30,112
E_ 90
Mavv 116,34,112
W_ 0
E_ 91
Mavv 136,38,112
s_nop 0
Tv 30,v2,26624
Tv 32,v2,32768
Mavv 156,42,112
C_ 128,88,89
C_ 129,90,91
Mavv 152,42,108
s_nop 0
Tv 34,v3,26624
Tv 36,v3,32768
Mavv 132,38,108
E_ 92
Mavv 128,38,104
E_ 93
Mavv 148,42,104
E_ 94
Mavv 144,42,100
E_ 95
Mavv 124,38,100
C_ 132,92,93
C_ 133,94,95
Mavv 120,38,96
v_mov_b32 v92, 0x3f803f80
v_mov_b32 v93, 0x3f803f80
Mavv 140,42,96
W_ 0
v_mov_b32 v94, 0x3f803f80
v_mov_b32 v95, 0x3f803f80
Mavv 160,30,96
s_nop 0
Tv 38,v4,26624
Tv 40,v4,32768
Mavv 180,34,96
Mavv 184,34,100
s_nop 0
Tv 42,v5,26624
Tv 44,v5,32768
Mavv 164,30,100
Mavv 168,30,104
Mavv 188,34,104
S_ s65,s61,110592
Mavv 192,34,108
S_ m0,s51,24576
Mavv 172,30,108
s_waitcnt vmcnt(6) lgkmcnt(0)
s_barrier
Mavv 176,30,112
Ln v7,32,s65,0
Mavv 196,34,112
Mavv 216,38,112
Dv 30,%0,6144
Mavv 236,42,112
Dv 34,v1,6144
Mavv 232,42,108
Ln v8,32,s65,3072
S_ m0,s51,25472
Mavv 212,38,108
Mavv 208,38,104
Ln v7,32,s65,128
Mavv 228,42,104
Mavv 224,42,100
Ln v8,32,s65,3200
S_ m0,s51,26368
Mavv 204,38,100
Mavv 200,38,96
Ln v7,32,s65,256
Mavv 220,42,96
.Lstep1_8:
Zvvv 76,92,116
Dv 38,%0,7168
Zvvv 80,92,120
Dv 42,v1,7168
Zvvv 84,92,124
Zvvv 88,92,128
Zvvv 92,92,132
W_ 2
Nvvv 96,30,136,56
Mvvv 96,34,156
Nvvv 100,30,140,60
s_nop 1
v_max3_f32 v10, v76, v80, v84
s_nop 0
v_max3_f32 v10, v10, v88, v92
Mvvv 100,34,160
v_cmp_lt_f32 vcc, 0x42000000, v10
s_cbranch_vccnz .Ltramp1_7
Nvvv 104,30,144,64
A_ v46,v46,v76
A_ v47,v47,v80
Mvvv 104,34,164
A_ v48,v48,v84
A_ v49,v49,v88
Nvvv 108,30,148,68
A_ v50,v50,v92
Mvvv 108,34,168
Ln v8,32,s65,3328
Nvvv 112,30,152,72
Mvvv 112,34,172
W_ 0
Mvvv 112,38,192
Dv 30,%0,8192
Mvvv 112,42,212
Dv 34,v1,8192
Mvvv 108,38,188
Mvvv 108,42,208
Mvvv 104,38,184
Mvvv 104,42,204
Mvvv 100,38,180
Mvvv 100,42,200
Mvvv 96,38,176
Mvvv 96,42,196
W_ 0
Mvvv 96,30,216
Dv 38,%0,0
Mvvv 96,34,236
Dv 42,v1,0
Mvvv 100,30,220
Mvvv 100,34,240
Mvvv 104,30,224
Mvvv 104,34,244
Mvvv 108,30,228
Mvvv 108,34,248
s_nop 0
E_ 96
Mvvv 112,30,232
E_ 97
Mvvv 112,34,252
W_ 0
E_ 98
Nvvv 76,38,136,56
Dv 30,%0,1024
Mvvv 76,42,156
Dv 34,v1,1024
Nvvv 80,38,140,60
E_ 99
Mvvv 80,42,160
C_ 99,98,99
C_ 98,96,97
Nvvv 84,38,144,64
E_ 100
Mvvv 84,42,164
E_ 101
Nvvv 88,38,148,68
E_ 102
Mvvv 88,42,168
E_ 103
Nvvv 92,38,152,72
C_ 103,102,103
C_ 102,100,101
Mvvv 92,42,172
W_ 0
E_ 104
Mvvv 92,30,192
Dv 38,%0,2048
Mvvv 92,34,212
Dv 42,v1,2048
Mvvv 88,30,188
E_ 105
Mvvv 88,34,208
E_ 106
Mvvv 84,30,184
S_ s65,s61,98304
E_ 107
Mvvv 84,34,204
S_ m0,s51,0
C_ 107,106,107
C_ 106,104,105
Mvvv 80,30,180
s_waitcnt vmcnt(6) lgkmcnt(0)
s_barrier
Mvvv 80,34,200
L_ v9,36,s65,0
Mvvv 76,30,176
E_ 108
Mvvv 76,34,196
L_ v9,36,s65,3072
S_ m0,s51,896
Mvvv 76,38,216
Tv 30,v2,12288
Tv 32,v2,18432
Mvvv 76,42,236
L_ v9,36,s65,128
Mvvv 80,38,220
s_nop 0
Tv 34,v3,12288
Tv 36,v3,18432
Mvvv 80,42,240
L_ v9,36,s65,3200
)A"
R"A(S_ m0,s51,1792
Mvvv 84,38,224
E_ 109
Mvvv 84,42,244
L_ v9,36,s65,256
Mvvv 88,38,228
E_ 110
Mvvv 88,42,248
L_ v9,36,s65,3328
Mvvv 92,38,232
E_ 111
Mvvv 92,42,252
W_ 0
C_ 111,110,111
C_ 110,108,109
Mavv 0,30,116
s_nop 0
Tv 38,v4,12288
Tv 40,v4,18432
Mavv 20,34,116
E_ 112
Mavv 24,34,120
s_nop 0
Tv 42,v5,12288
Tv 44,v5,18432
Mavv 4,30,120
E_ 113
Mavv 8,30,124
E_ 114
Mavv 28,34,124
E_ 115
Mavv 32,34,128
C_ 115,114,115
C_ 114,112,113
Mavv 12,30,128
E_ 76
Mavv 16,30,132
E_ 77
Mavv 36,34,132
W_ 0
E_ 78
Mavv 56,38,132
s_nop 0
Tv 30,v2,13312
Tv 32,v2,19456
Mavv 76,42,132
E_ 79
Mavv 72,42,128
s_nop 0
Tv 34,v3,13312
Tv 36,v3,19456
Mavv 52,38,128
C_ 96,76,77
C_ 97,78,79
Mavv 48,38,124
E_ 80
Mavv 68,42,124
E_ 81
Mavv 64,42,120
E_ 82
Mavv 44,38,120
E_ 83
Mavv 40,38,116
C_ 100,80,81
C_ 101,82,83
Mavv 60,42,116
W_ 0
E_ 84
Mavv 80,30,116
s_nop 0
Tv 38,v4,13312
Tv 40,v4,19456
Mavv 100,34,116
E_ 85
Mavv 104,34,120
s_nop 0
Tv 42,v5,13312
Tv 44,v5,19456
Mavv 84,30,120
E_ 86
Mavv 88,30,124
E_ 87
Mavv 108,34,124
C_ 104,84,85
C_ 105,86,87
Mavv 112,34,128
E_ 88
Mavv 92,30,128
E_ 89
Mavv 96,30,132
E_ 90
Mavv 116,34,132
W_ 0
E_ 91
Mavv 136,38,132
s_nop 0
Tv 30,v2,14336
Tv 32,v2,20480
Mavv 156,42,132
C_ 108,88,89
C_ 109,90,91
Mavv 152,42,128
s_nop 0
Tv 34,v3,14336
Tv 36,v3,20480
Mavv 132,38,128
E_ 92
Mavv 128,38,124
E_ 93
Mavv 148,42,124
E_ 94
Mavv 144,42,120
E_ 95
Mavv 124,38,120
C_ 112,92,93
C_ 113,94,95
Mavv 120,38,116
v_mov_b32 v92, 0x3f803f80
v_mov_b32 v93, 0x3f803f80
Mavv 140,42,116
W_ 0
v_mov_b32 v94, 0x3f803f80
v_mov_b32 v95, 0x3f803f80
Mavv 160,30,116
s_nop 0
Tv 38,v4,14336
Tv 40,v4,20480
Mavv 180,34,116
Mavv 184,34,120
s_nop 0
Tv 42,v5,14336
Tv 44,v5,20480
Mavv 164,30,120
Mavv 168,30,124
Mavv 188,34,124
S_ s65,s61,122880
Mavv 192,34,128
S_ m0,s51,12288
Mavv 172,30,128
s_waitcnt vmcnt(6) lgkmcnt(0)
s_barrier
Mavv 176,30,132
Ln v7,32,s65,0
Mavv 196,34,132
Mavv 216,38,132
Dv 30,%0,30720
Mavv 236,42,132
Dv 34,v1,30720
Mavv 232,42,128
Ln v8,32,s65,3072
S_ m0,s51,13184
Mavv 212,38,128
Mavv 208,38,124
Ln v7,32,s65,128
Mavv 228,42,124
Mavv 224,42,120
Ln v8,32,s65,3200
S_ m0,s51,14080
Mavv 204,38,120
Mavv 200,38,116
Ln v7,32,s65,256
Mavv 220,42,116
s_cmp_eq_u32 s4, 0
s_cbranch_scc1 .Lhs_d1
.Lhs_d1_back:
.Lstep1_9:
Zvvv 76,92,96
Dv 38,%0,31744
Zvvv 80,92,100
Dv 42,v1,31744
Zvvv 84,92,104
Zvvv 88,92,108
Zvvv 92,92,112
W_ 2
Nvvv 116,30,136,56
Mvvv 116,34,156
Nvvv 120,30,140,60
s_nop 1
v_max3_f32 v10, v76, v80, v84
s_nop 0
v_max3_f32 v10, v10, v88, v92
Mvvv 120,34,160
v_cmp_lt_f32 vcc, 0x42000000, v10
s_cbranch_vccnz .Ltramp1_8
Nvvv 124,30,144,64
A_ v46,v46,v76
A_ v47,v47,v80
Mvvv 124,34,164
A_ v48,v48,v84
A_ v49,v49,v88
Nvvv 128,30,148,68
A_ v50,v50,v92
Mvvv 128,34,168
Ln v8,32,s65,3328
Nvvv 132,30,152,72
Mvvv 132,34,172
W_ 0
Mvvv 132,38,192
Dv 30,%0,32768
Mvvv 132,42,212
Dv 34,v1,32768
Mvvv 128,38,188
Mvvv 128,42,208
Mvvv 124,38,184
Mvvv 124,42,204
Mvvv 120,38,180
Mvvv 120,42,200
Mvvv 116,38,176
Mvvv 116,42,196
W_ 0
Mvvv 116,30,216
Dv 38,%0,24576
Mvvv 116,34,236
Dv 42,v1,24576
Mvvv 120,30,220
Mvvv 120,34,240
Mvvv 124,30,224
Mvvv 124,34,244
Mvvv 128,30,228
Mvvv 128,34,248
s_nop 0
E_ 116
Mvvv 132,30,232
E_ 117
Mvvv 132,34,252
W_ 0
E_ 118
Nvvv 76,38,136,56
Dv 30,%0,25600
Mvvv 76,42,156
Dv 34,v1,25600
Nvvv 80,38,140,60
E_ 119
Mvvv 80,42,160
C_ 119,118,119
C_ 118,116,117
Nvvv 84,38,144,64
E_ 120
Mvvv 84,42,164
E_ 121
Nvvv 88,38,148,68
E_ 122
Mvvv 88,42,168
E_ 123
Nvvv 92,38,152,72
C_ 123,122,123
C_ 122,120,121
Mvvv 92,42,172
W_ 0
E_ 124
Mvvv 92,30,192
Dv 38,%0,26624
Mvvv 92,34,212
Dv 42,v1,26624
Mvvv 88,30,188
E_ 125
Mvvv 88,34,208
E_ 126
Mvvv 84,30,184
S_ s65,s61,110592
E_ 127
Mvvv 84,34,204
S_ m0,s51,24576
C_ 127,126,127
C_ 126,124,125
Mvvv 80,30,180
s_waitcnt vmcnt(6) lgkmcnt(0)
s_barrier
Mvvv 80,34,200
L_ v9,36,s65,0
Mvvv 76,30,176
E_ 128
Mvvv 76,34,196
L_ v9,36,s65,3072
S_ m0,s51,25472
Mvvv 76,38,216
Tv 30,v2,0
Tv 32,v2,6144
Mvvv 76,42,236
L_ v9,36,s65,128
Mvvv 80,38,220
s_nop 0
Tv 34,v3,0
Tv 36,v3,6144
Mvvv 80,42,240
L_ v9,36,s65,3200
S_ m0,s51,26368
Mvvv 84,38,224
E_ 129
Mvvv 84,42,244
L_ v9,36,s65,256
Mvvv 88,38,228
E_ 130
Mvvv 88,42,248
L_ v9,36,s65,3328
Mvvv 92,38,232
E_ 131
Mvvv 92,42,252
W_ 0
C_ 131,130,131
C_ 130,128,129
Mavv 0,30,96
s_nop 0
Tv 38,v4,0
Tv 40,v4,6144
Mavv 20,34,96
E_ 132
Mavv 24,34,100
s_nop 0
Tv 42,v5,0
Tv 44,v5,6144
Mavv 4,30,100
E_ 133
Mavv 8,30,104
E_ 134
Mavv 28,34,104
E_ 135
Mavv 32,34,108
C_ 135,134,135
C_ 134,132,133
Mavv 12,30,108
E_ 76
Mavv 16,30,112
E_ 77
Mavv 36,34,112
W_ 0
E_ 78
Mavv 56,38,112
s_nop 0
Tv 30,v2,1024
Tv 32,v2,7168
Mavv 76,42,112
E_ 79
Mavv 72,42,108
s_nop 0
Tv 34,v3,1024
Tv 36,v3,7168
Mavv 52,38,108
C_ 116,76,77
C_ 117,78,79
Mavv 48,38,104
E_ 80
Mavv 68,42,104
E_ 81
Mavv 64,42,100
E_ 82
Mavv 44,38,100
E_ 83
Mavv 40,38,96
C_ 120,80,81
C_ 121,82,83
Mavv 60,42,96
W_ 0
E_ 84
Mavv 80,30,96
s_nop 0
Tv 38,v4,1024
Tv 40,v4,7168
Mavv 100,34,96
E_ 85
Mavv 104,34,100
s_nop 0
Tv 42,v5,1024
Tv 44,v5,7168
Mavv 84,30,100
E_ 86
Mavv 88,30,104
E_ 87
Mavv 108,34,104
C_ 124,84,85
C_ 125,86,87
Mavv 112,34,108
E_ 88
Mavv 92,30,108
E_ 89
Mavv 96,30,112
E_ 90
Mavv 116,34,112
W_ 0
E_ 91
Mavv 136,38,112
s_nop 0
Tv 30,v2,2048
Tv 32,v2,8192
Mavv 156,42,112
C_ 128,88,89
C_ 129,90,91
Mavv 152,42,108
s_nop 0
Tv 34,v3,2048
Tv 36,v3,8192
Mavv 132,38,108
E_ 92
Mavv 128,38,104
E_ 93
Mavv 148,42,104
E_ 94
Mavv 144,42,100
E_ 95
Mavv 124,38,100
C_ 132,92,93
C_ 133,94,95
Mavv 120,38,96
v_mov_b32 v92, 0x3f803f80
v_mov_b32 v93, 0x3f803f80
Mavv 140,42,96
W_ 0
v_mov_b32 v94, 0x3f803f80
v_mov_b32 v95, 0x3f803f80
Mavv 160,30,96
s_nop 0
Tv 38,v4,2048
Tv 40,v4,8192
Mavv 180,34,96
Mavv 184,34,100
s_nop 0
Tv 42,v5,2048
Tv 44,v5,8192
Mavv 164,30,100
Mavv 168,30,104
Mavv 188,34,104
S_ s65,s61,135168
Mavv 192,34,108
S_ m0,s51,0
Mavv 172,30,108
s_waitcnt vmcnt(6) lgkmcnt(0)
s_barrier
Mavv 176,30,112
Ln v7,32,s65,0
Mavv 196,34,112
Mavv 216,38,112
Dv 30,%0,18432
Mavv 236,42,112
Dv 34,v1,18432
Mavv 232,42,108
Ln v8,32,s65,3072
S_ m0,s51,896
Mavv 212,38,108
Mavv 208,38,104
Ln v7,32,s65,128
Mavv 228,42,104
Mavv 224,42,100
Ln v8,32,s65,3200
S_ m0,s51,1792
Mavv 204,38,100
Mavv 200,38,96
Ln v7,32,s65,256
Mavv 220,42,96
.Lstep1_10:
Zvvv 76,92,116
Dv 38,%0,19456
Zvvv 80,92,120
Dv 42,v1,19456
Zvvv 84,92,124
Zvvv 88,92,128
Zvvv 92,92,132
W_ 2
Nvvv 96,30,136,56
Mvvv 96,34,156
Nvvv 100,30,140,60
s_nop 1
v_max3_f32 v10, v76, v80, v84
s_nop 0
v_max3_f32 v10, v10, v88, v92
Mvvv 100,34,160
v_cmp_lt_f32 vcc, 0x42000000, v10
s_cbranch_vccnz .Ltramp1_9
Nvvv 104,30,144,64
A_ v46,v46,v76
A_ v47,v47,v80
Mvvv 104,34,164
A_ v48,v48,v84
A_ v49,v49,v88
Nvvv 108,30,148,68
A_ v50,v50,v92
Mvvv 108,34,168
Ln v8,32,s65,3328
Nvvv 112,30,152,72
Mvvv 112,34,172
W_ 0
Mvvv 112,38,192
Dv 30,%0,20480
Mvvv 112,42,212
Dv 34,v1,20480
Mvvv 108,38,188
Mvvv 108,42,208
Mvvv 104,38,184
Mvvv 104,42,204
Mvvv 100,38,180
Mvvv 100,42,200
Mvvv 96,38,176
Mvvv 96,42,196
W_ 0
Mvvv 96,30,216
Dv 38,%0,12288
Mvvv 96,34,236
Dv 42,v1,12288
Mvvv 100,30,220
Mvvv 100,34,240
Mvvv 104,30,224
Mvvv 104,34,244
Mvvv 108,30,228
Mvvv 108,34,248
s_nop 0
E_ 96
Mvvv 112,30,232
E_ 97
Mvvv 112,34,252
W_ 0
E_ 98
Nvvv 76,38,136,56
Dv 30,%0,13312
Mvvv 76,42,156
Dv 34,v1,13312
Nvvv 80,38,140,60
E_ 99
Mvvv 80,42,160
C_ 99,98,99
C_ 98,96,97
Nvvv 84,38,144,64
E_ 100
Mvvv 84,42,164
E_ 101
Nvvv 88,38,148,68
E_ 102
Mvvv 88,42,168
E_ 103
Nvvv 92,38,152,72
C_ 103,102,103
C_ 102,100,101
Mvvv 92,42,172
W_ 0
E_ 104
Mvvv 92,30,192
Dv 38,%0,14336
Mvvv 92,34,212
Dv 42,v1,14336
Mvvv 88,30,188
E_ 105
Mvvv 88,34,208
E_ 106
Mvvv 84,30,184
S_ s65,s61,122880
E_ 107
Mvvv 84,34,204
S_ m0,s51,12288
C_ 107,106,107
C_ 106,104,105
Mvvv 80,30,180
s_waitcnt vmcnt(6) lgkmcnt(0)
s_barrier
Mvvv 80,34,200
L_ v9,36,s65,0
Mvvv 76,30,176
E_ 108
Mvvv 76,34,196
L_ v9,36,s65,3072
S_ m0,s51,13184
Mvvv 76,38,216
Tv 30,v2,24576
Tv 32,v2,30720
Mvvv 76,42,236
L_ v9,36,s65,128
Mvvv 80,38,220
s_nop 0
Tv 34,v3,24576
Tv 36,v3,30720
Mvvv 80,42,240
L_ v9,36,s65,3200
S_ m0,s51,14080
Mvvv 84,38,224
E_ 109
Mvvv 84,42,244
L_ v9,36,s65,256
Mvvv 88,38,228
E_ 110
Mvvv 88,42,248
L_ v9,36,s65,3328
Mvvv 92,38,232
E_ 111
Mvvv 92,42,252
W_ 0
C_ 111,110,111
C_ 110,108,109
Mavv 0,30,116
s_nop 0
Tv 38,v4,24576
Tv 40,v4,30720
Mavv 20,34,116
E_ 112
Mavv 24,34,120
s_nop 0
Tv 42,v5,24576
Tv 44,v5,30720
Mavv 4,30,120
E_ 113
Mavv 8,30,124
E_ 114
Mavv 28,34,124
E_ 115
Mavv 32,34,128
C_ 115,114,115
C_ 114,112,113
Mavv 12,30,128
E_ 76
Mavv 16,30,132
E_ 77
Mavv 36,34,132
W_ 0
E_ 78
Mavv 56,38,132
s_nop 0
Tv 30,v2,25600
Tv 32,v2,31744
Mavv 76,42,132
E_ 79
Mavv 72,42,128
s_nop 0
Tv 34,v3,25600
Tv 36,v3,31744
Mavv 52,38,128
C_ 96,76,77
C_ 97,78,79
Mavv 48,38,124
E_ 80
Mavv 68,42,124
E_ 81
Mavv 64,42,120
E_ 82
Mavv 44,38,120
E_ 83
Mavv 40,38,116
C_ 100,80,81
C_ 101,82,83
Mavv 60,42,116
W_ 0
E_ 84
Mavv 80,30,116
s_nop 0
Tv 38,v4,25600
Tv 40,v4,31744
Mavv 100,34,116
E_ 85
Mavv 104,34,120
s_nop 0
Tv 42,v5,25600
Tv 44,v5,31744
Mavv 84,30,120
E_ 86
Mavv 88,30,124
E_ 87
Mavv 108,34,124
C_ 104,84,85
C_ 105,86,87
Mavv 112,34,128
E_ 88
Mavv 92,30,128
E_ 89
Mavv 96,30,132
E_ 90
Mavv 116,34,132
W_ 0
E_ 91
Mavv 136,38,132
s_nop 0
Tv 30,v2,26624
Tv 32,v2,32768
Mavv 156,42,132
C_ 108,88,89
C_ 109,90,91
Mavv 152,42,128
s_nop 0
Tv 34,v3,26624
Tv 36,v3,32768
Mavv 132,38,128
E_ 92
Mavv 128,38,124
E_ 93
Mavv 148,42,124
E_ 94
Mavv 144,42,120
E_ 95
Mavv 124,38,120
C_ 112,92,93
C_ 113,94,95
Mavv 120,38,116
v_mov_b32 v92, 0x3f803f80
v_mov_b32 v93, 0x3f803f80
Mavv 140,42,116
W_ 0
v_mov_b32 v94, 0x3f803f80
v_mov_b32 v95, 0x3f803f80
Mavv 160,30,116
s_nop 0
Tv 38,v4,26624
Tv 40,v4,32768
Mavv 180,34,116
Mavv 184,34,120
s_nop 0
Tv 42,v5,26624
Tv 44,v5,32768
Mavv 164,30,120
Mavv 168,30,124
Mavv 188,34,124
S_ s65,s61,147456
Mavv 192,34,128
S_ m0,s51,24576
Mavv 172,30,128
s_waitcnt vmcnt(6) lgkmcnt(0)
s_barrier
Mavv 176,30,132
Ln v7,32,s65,0
Mavv 196,34,132
Mavv 216,38,132
Dv 30,%0,6144
Mavv 236,42,132
Dv 34,v1,6144
Mavv 232,42,128
Ln v8,32,s65,3072
S_ m0,s51,25472
Mavv 212,38,128
Mavv 208,38,124
Ln v7,32,s65,128
Mavv 228,42,124
Mavv 224,42,120
Ln v8,32,s65,3200
S_ m0,s51,26368
Mavv 204,38,120
Mavv 200,38,116
Ln v7,32,s65,256
Mavv 220,42,116
.Lstep1_11:
Zvvv 76,92,96
Dv 38,%0,7168
Zvvv 80,92,100
Dv 42,v1,7168
Zvvv 84,92,104
Zvvv 88,92,108
Zvvv 92,92,112
W_ 2
Nvvv 116,30,136,56
Mvvv 116,34,156
Nvvv 120,30,140,60
s_nop 1
v_max3_f32 v10, v76, v80, v84
s_nop 0
v_max3_f32 v10, v10, v88, v92
Mvvv 120,34,160
v_cmp_lt_f32 vcc, 0x42000000, v10
s_cbranch_vccnz .Ltramp1_10
Nvvv 124,30,144,64
A_ v46,v46,v76
A_ v47,v47,v80
Mvvv 124,34,164
A_ v48,v48,v84
A_ v49,v49,v88
Nvvv 128,30,148,68
A_ v50,v50,v92
Mvvv 128,34,168
Ln v8,32,s65,3328
Nvvv 132,30,152,72
Mvvv 132,34,172
W_ 0
Mvvv 132,38,192
Dv 30,%0,8192
Mvvv 132,42,212
Dv 34,v1,8192
Mvvv 128,38,188
Mvvv 128,42,208
Mvvv 124,38,184
Mvvv 124,42,204
Mvvv 120,38,180
Mvvv 120,42,200
Mvvv 116,38,176
Mvvv 116,42,196
W_ 0
Mvvv 116,30,216
Dv 38,%0,0
Mvvv 116,34,236
Dv 42,v1,0
Mvvv 120,30,220
Mvvv 120,34,240
Mvvv 124,30,224
Mvvv 124,34,244
Mvvv 128,30,228
Mvvv 128,34,248
s_nop 0
E_ 116
Mvvv 132,30,232
E_ 117
Mvvv 132,34,252
W_ 0
E_ 118
Nvvv 76,38,136,56
Dv 30,%0,1024
Mvvv 76,42,156
Dv 34,v1,1024
Nvvv 80,38,140,60
E_ 119
Mvvv 80,42,160
C_ 119,118,119
C_ 118,116,117
Nvvv 84,38,144,64
E_ 120
Mvvv 84,42,164
E_ 121
Nvvv 88,38,148,68
E_ 122
Mvvv 88,42,168
E_ 123
Nvvv 92,38,152,72
C_ 123,122,123
C_ 122,120,121
Mvvv 92,42,172
W_ 0
E_ 124
Mvvv 92,30,192
Dv 38,%0,2048
Mvvv 92,34,212
Dv 42,v1,2048
Mvvv 88,30,188
E_ 125
Mvvv 88,34,208
E_ 126
Mvvv 84,30,184
S_ s65,s61,135168
E_ 127
Mvvv 84,34,204
S_ m0,s51,0
C_ 127,126,127
C_ 126,124,125
Mvvv 80,30,180
s_waitcnt vmcnt(6) lgkmcnt(0)
s_barrier
Mvvv 80,34,200
L_ v9,36,s65,0
Mvvv 76,30,176
E_ 128
Mvvv 76,34,196
L_ v9,36,s65,3072
S_ m0,s51,896
Mvvv 76,38,216
Tv 30,v2,12288
Tv 32,v2,18432
Mvvv 76,42,236
L_ v9,36,s65,128
Mvvv 80,38,220
s_nop 0
Tv 34,v3,12288
Tv 36,v3,18432
Mvvv 80,42,240
L_ v9,36,s65,3200
S_ m0,s51,1792
Mvvv 84,38,224
E_ 129
Mvvv 84,42,244
L_ v9,36,s65,256
Mvvv 88,38,228
E_ 130
Mvvv 88,42,248
L_ v9,36,s65,3328
Mvvv 92,38,232
E_ 131
Mvvv 92,42,252
W_ 0
C_ 131,130,131
C_ 130,128,129
Mavv 0,30,96
s_nop 0
Tv 38,v4,12288
Tv 40,v4,18432
Mavv 20,34,96
E_ 132
Mavv 24,34,100
s_nop 0
Tv 42,v5,12288
Tv 44,v5,18432
Mavv 4,30,100
E_ 133
Mavv 8,30,104
E_ 134
Mavv 28,34,104
E_ 135
Mavv 32,34,108
C_ 135,134,135
C_ 134,132,133
Mavv 12,30,108
E_ 76
Mavv 16,30,112
E_ 77
Mavv 36,34,112
W_ 0
E_ 78
Mavv 56,38,112
s_nop 0
Tv 30,v2,13312
Tv 32,v2,19456
Mavv 76,42,112
E_ 79
Mavv 72,42,108
s_nop 0
Tv 34,v3,13312
Tv 36,v3,19456
Mavv 52,38,108
C_ 116,76,77
C_ 117,78,79
Mavv 48,38,104
E_ 80
Mavv 68,42,104
E_ 81
Mavv 64,42,100
E_ 82
Mavv 44,38,100
E_ 83
Mavv 40,38,96
C_ 120,80,81
C_ 121,82,83
Mavv 60,42,96
W_ 0
E_ 84
Mavv 80,30,96
s_nop 0
Tv 38,v4,13312
Tv 40,v4,19456
Mavv 100,34,96
E_ 85
Mavv 104,34,100
s_nop 0
Tv 42,v5,13312
Tv 44,v5,19456
Mavv 84,30,100
E_ 86
Mavv 88,30,104
E_ 87
Mavv 108,34,104
C_ 124,84,85
C_ 125,86,87
Mavv 112,34,108
E_ 88
Mavv 92,30,108
E_ 89
Mavv 96,30,112
E_ 90
Mavv 116,34,112
W_ 0
E_ 91
Mavv 136,38,112
s_nop 0
Tv 30,v2,14336
Tv 32,v2,20480
Mavv 156,42,112
C_ 128,88,89
C_ 129,90,91
Mavv 152,42,108
s_nop 0
Tv 34,v3,14336
Tv 36,v3,20480
Mavv 132,38,108
E_ 92
Mavv 128,38,104
E_ 93
Mavv 148,42,104
E_ 94
Mavv 144,42,100
E_ 95
Mavv 124,38,100
C_ 132,92,93
C_ 133,94,95
Mavv 120,38,96
v_mov_b32 v92, 0x3f803f80
v_mov_b32 v93, 0x3f803f80
Mavv 140,42,96
W_ 0
v_mov_b32 v94, 0x3f803f80
v_mov_b32 v95, 0x3f803f80
Mavv 160,30,96
s_nop 0
Tv 38,v4,14336
Tv 40,v4,20480
Mavv 180,34,96
Mavv 184,34,100
s_nop 0
Tv 42,v5,14336
Tv 44,v5,20480
Mavv 164,30,100
Mavv 168,30,104
Mavv 188,34,104
S_ s65,s61,159744
Mavv 192,34,108
S_ m0,s51,12288
Mavv 172,30,108
s_waitcnt vmcnt(6) lgkmcnt(0)
s_barrier
Mavv 176,30,112
Ln v7,32,s65,0
Mavv 196,34,112
Mavv 216,38,112
Dv 30,%0,30720
Mavv 236,42,112
Dv 34,v1,30720
Mavv 232,42,108
Ln v8,32,s65,3072
S_ m0,s51,13184
Mavv 212,38,108
Mavv 208,38,104
Ln v7,32,s65,128
Mavv 228,42,104
Mavv 224,42,100
Ln v8,32,s65,3200
S_ m0,s51,14080
Mavv 204,38,100
Mavv 200,38,96
Ln v7,32,s65,256
Mavv 220,42,96
.Lstep1_12:
Zvvv 76,92,116
Dv 38,%0,31744
Zvvv 80,92,120
Dv 42,v1,31744
Zvvv 84,92,124
Zvvv 88,92,128
Zvvv 92,92,132
W_ 2
Nvvv 96,30,136,56
Mvvv 96,34,156
Nvvv 100,30,140,60
s_nop 1
v_max3_f32 v10, v76, v80, v84
s_nop 0
v_max3_f32 v10, v10, v88, v92
Mvvv 100,34,160
v_cmp_lt_f32 vcc, 0x42000000, v10
s_cbranch_vccnz .Ltramp1_11
Nvvv 104,30,144,64
A_ v46,v46,v76
A_ v47,v47,v80
Mvvv 104,34,164
A_ v48,v48,v84
A_ v49,v49,v88
Nvvv 108,30,148,68
A_ v50,v50,v92
Mvvv 108,34,168
Ln v8,32,s65,3328
Nvvv 112,30,152,72
R_ v11,a248
Mvvv 112,34,172
W_ 0
v_min_u32 v11, 240, v11
Mvvv 112,38,192
Dv 30,%0,32768
v_lshlrev_b32 v11, 3, v11
Mvvv 112,42,212
Dv 34,v1,32768
Mvvv 108,38,188
buffer_load_dword a254, v11, s[44:47], s66 offen
Mvvv 108,42,208
Mvvv 104,38,184
Mvvv 104,42,204
Mvvv 100,38,180
Mvvv 100,42,200
Mvvv 96,38,176
Mvvv 96,42,196
W_ 0
Mvvv 96,30,216
Dv 38,%0,24576
Mvvv 96,34,236
Dv 42,v1,24576
Mvvv 100,30,220
Mvvv 100,34,240
Mvvv 104,30,224
Mvvv 104,34,244
Mvvv 108,30,228
Mvvv 108,34,248
s_nop 0
E_ 96
Mvvv 112,30,232
E_ 97
Mvvv 112,34,252
W_ 0
E_ 98
Nvvv 76,38,136,56
Dv 30,%0,25600
Mvvv 76,42,156
Dv 34,v1,25600
Nvvv 80,38,140,60
E_ 99
Mvvv 80,42,160
C_ 99,98,99
C_ 98,96,97
Nvvv 84,38,144,64
E_ 100
Mvvv 84,42,164
E_ 101
Nvvv 88,38,148,68
E_ 102
Mvvv 88,42,168
E_ 103
Nvvv 92,38,152,72
C_ 103,102,103
C_ 102,100,101
Mvvv 92,42,172
W_ 0
E_ 104
Mvvv 92,30,192
Dv 38,%0,26624
Mvvv 92,34,212
Dv 42,v1,26624
Mvvv 88,30,188
E_ 105
Mvvv 88,34,208
E_ 106
Mvvv 84,30,184
S_ s65,s61,147456
E_ 107
Mvvv 84,34,204
S_ m0,s51,24576
C_ 107,106,107
C_ 106,104,105
Mvvv 80,30,180
s_waitcnt vmcnt(7) lgkmcnt(0)
s_barrier
Mvvv 80,34,200
Ln v9,36,s65,0
Mvvv 76,30,176
E_ 108
Mvvv 76,34,196
Ln v9,36,s65,3072
S_ m0,s51,25472
Mvvv 76,38,216
Tv 30,v2,0
Tv 32,v2,6144
Mvvv 76,42,236
Ln v9,36,s65,128
Mvvv 80,38,220
s_nop 0
Tv 34,v3,0
Tv 36,v3,6144
Mvvv 80,42,240
Ln v9,36,s65,3200
S_ m0,s51,26368
Mvvv 84,38,224
E_ 109
Mvvv 84,42,244
Ln v9,36,s65,256
Mvvv 88,38,228
E_ 110
Mvvv 88,42,248
Ln v9,36,s65,3328
Mvvv 92,38,232
E_ 111
Mvvv 92,42,252
W_ 0
C_ 111,110,111
C_ 110,108,109
Mavv 0,30,116
s_nop 0
Tv 38,v4,0
Tv 40,v4,6144
Mavv 20,34,116
E_ 112
Mavv 24,34,120
s_nop 0
Tv 42,v5,0
Tv 44,v5,6144
Mavv 4,30,120
E_ 113
Mavv 8,30,124
E_ 114
Mavv 28,34,124
E_ 115
Mavv 32,34,128
C_ 115,114,115
C_ 114,112,113
Mavv 12,30,128
E_ 76
Mavv 16,30,132
E_ 77
Mavv 36,34,132
W_ 0
E_ 78
Mavv 56,38,132
s_nop 0
Tv 30,v2,1024
Tv 32,v2,7168
Mavv 76,42,132
E_ 79
Mavv 72,42,128
s_nop 0
Tv 34,v3,1024
Tv 36,v3,7168
Mavv 52,38,128
C_ 96,76,77
C_ 97,78,79
Mavv 48,38,124
E_ 80
Mavv 68,42,124
E_ 81
Mavv 64,42,120
E_ 82
Mavv 44,38,120
E_ 83
Mavv 40,38,116
C_ 100,80,81
C_ 101,82,83
Mavv 60,42,116
W_ 0
E_ 84
Mavv 80,30,116
s_nop 0
Tv 38,v4,1024
Tv 40,v4,7168
Mavv 100,34,116
E_ 85
Mavv 104,34,120
s_nop 0
Tv 42,v5,1024
Tv 44,v5,7168
Mavv 84,30,120
E_ 86
Mavv 88,30,124
E_ 87
Mavv 108,34,124
C_ 104,84,85
C_ 105,86,87
Mavv 112,34,128
E_ 88
Mavv 92,30,128
E_ 89
Mavv 96,30,132
E_ 90
Mavv 116,34,132
W_ 0
E_ 91
Mavv 136,38,132
s_nop 0
Tv 30,v2,2048
Tv 32,v2,8192
Mavv 156,42,132
C_ 108,88,89
C_ 109,90,91
Mavv 152,42,128
s_nop 0
Tv 34,v3,2048
Tv 36,v3,8192
Mavv 132,38,128
E_ 92
Mavv 128,38,124
E_ 93
Mavv 148,42,124
E_ 94
Mavv 144,42,120
E_ 95
Mavv 124,38,120
C_ 112,92,93
C_ 113,94,95
Mavv 120,38,116
v_mov_b32 v92, 0x3f803f80
v_mov_b32 v93, 0x3f803f80
Mavv 140,42,116
W_ 0
v_mov_b32 v94, 0x3f803f80
v_mov_b32 v95, 0x3f803f80
Mavv 160,30,116
s_nop 0
Tv 38,v4,2048
Tv 40,v4,8192
Mavv 180,34,116
Mavv 184,34,120
s_nop 0
Tv 42,v5,2048
Tv 44,v5,8192
Mavv 164,30,120
Mavv 168,30,124
Mavv 188,34,124
S_ s65,s61,172032
Mavv 192,34,128
S_ m0,s51,0
Mavv 172,30,128
s_waitcnt vmcnt(7) lgkmcnt(0)
s_barrier
Mavv 176,30,132
Ln v7,32,s65,0
Mavv 196,34,132
Mavv 216,38,132
Dv 30,%0,18432
Mavv 236,42,132
Dv 34,v1,18432
Mavv 232,42,128
Ln v8,32,s65,3072
S_ m0,s51,896
Mavv 212,38,128
Mavv 208,38,124
Ln v7,32,s65,128
Mavv 228,42,124
Mavv 224,42,120
Ln v8,32,s65,3200
S_ m0,s51,1792
Mavv 204,38,120
Mavv 200,38,116
Ln v7,32,s65,256
Mavv 220,42,116
.Lstep1_13:
Zvvv 76,92,96
Dv 38,%0,19456
Zvvv 80,92,100
Dv 42,v1,19456
Zvvv 84,92,104
Zvvv 88,92,108
Zvvv 92,92,112
W_ 2
Nvvv 116,30,136,56
Mvvv 116,34,156
Nvvv 120,30,140,60
s_nop 1
v_max3_f32 v10, v76, v80, v84
s_nop 0
v_max3_f32 v10, v10, v88, v92
Mvvv 120,34,160
v_cmp_lt_f32 vcc, 0x42000000, v10
s_cbranch_vccnz .Ltramp1_12
Nvvv 124,30,144,64
A_ v46,v46,v76
A_ v47,v47,v80
Mvvv 124,34,164
A_ v48,v48,v84
A_ v49,v49,v88
Nvvv 128,30,148,68
A_ v50,v50,v92
Mvvv 128,34,168
Ln v8,32,s65,3328
Nvvv 132,30,152,72
Mvvv 132,34,172
W_ 0
Mvvv 132,38,192
Dv 30,%0,20480
Mvvv 132,42,212
Dv 34,v1,20480
Mvvv 128,38,188
Mvvv 128,42,208
Mvvv 124,38,184
Mvvv 124,42,204
Mvvv 120,38,180
Mvvv 120,42,200
Mvvv 116,38,176
Mvvv 116,42,196
W_ 0
Mvvv 116,30,216
Dv 38,%0,12288
Mvvv 116,34,236
Dv 42,v1,12288
Mvvv 120,30,220
Mvvv 120,34,240
Mvvv 124,30,224
Mvvv 124,34,244
Mvvv 128,30,228
Mvvv 128,34,248
s_nop 0
E_ 116
Mvvv 132,30,232
E_ 117
Mvvv 132,34,252
W_ 0
E_ 118
Nvvv 76,38,136,56
Dv 30,%0,13312
Mvvv 76,42,156
Dv 34,v1,13312
Nvvv 80,38,140,60
E_ 119
Mvvv 80,42,160
C_ 119,118,119
C_ 118,116,117
Nvvv 84,38,144,64
E_ 120
Mvvv 84,42,164
E_ 121
Nvvv 88,38,148,68
E_ 122
Mvvv 88,42,168
E_ 123
Nvvv 92,38,152,72
C_ 123,122,123
C_ 122,120,121
Mvvv 92,42,172
W_ 0
E_ 124
Mvvv 92,30,192
Dv 38,%0,14336
Mvvv 92,34,212
Dv 42,v1,14336
Mvvv 88,30,188
E_ 125
Mvvv 88,34,208
E_ 126
Mvvv 84,30,184
S_ s65,s61,159744
E_ 127
Mvvv 84,34,204
S_ m0,s51,12288
C_ 127,126,127
C_ 126,124,125
Mvvv 80,30,180
s_waitcnt vmcnt(6) lgkmcnt(0)
s_barrier
Mvvv 80,34,200
Ln v9,36,s65,0
Mvvv 76,30,176
E_ 128
Mvvv 76,34,196
Ln v9,36,s65,3072
S_ m0,s51,13184
Mvvv 76,38,216
Tv 30,v2,24576
Tv 32,v2,30720
Mvvv 76,42,236
Ln v9,36,s65,128
Mvvv 80,38,220
s_nop 0
Tv 34,v3,24576
Tv 36,v3,30720
Mvvv 80,42,240
Ln v9,36,s65,3200
S_ m0,s51,14080
Mvvv 84,38,224
E_ 129
Mvvv 84,42,244
Ln v9,36,s65,256
Mvvv 88,38,228
E_ 130
Mvvv 88,42,248
Ln v9,36,s65,3328
Mvvv 92,38,232
E_ 131
Mvvv 92,42,252
W_ 0
C_ 131,130,131
C_ 130,128,129
Mavv 0,30,96
s_nop 0
Tv 38,v4,24576
Tv 40,v4,30720
Mavv 20,34,96
E_ 132
Mavv 24,34,100
s_nop 0
Tv 42,v5,24576
Tv 44,v5,30720
Mavv 4,30,100
E_ 133
Mavv 8,30,104
E_ 134
Mavv 28,34,104
E_ 135
Mavv 32,34,108
C_ 135,134,135
C_ 134,132,133
Mavv 12,30,108
E_ 76
Mavv 16,30,112
E_ 77
Mavv 36,34,112
W_ 0
E_ 78
Mavv 56,38,112
s_nop 0
Tv 30,v2,25600
Tv 32,v2,31744
Mavv 76,42,112
E_ 79
Mavv 72,42,108
s_nop 0
Tv 34,v3,25600
Tv 36,v3,31744
Mavv 52,38,108
C_ 116,76,77
C_ 117,78,79
Mavv 48,38,104
E_ 80
Mavv 68,42,104
E_ 81
Mavv 64,42,100
E_ 82
Mavv 44,38,100
E_ 83
Mavv 40,38,96
C_ 120,80,81
C_ 121,82,83
Mavv 60,42,96
W_ 0
E_ 84
Mavv 80,30,96
s_nop 0
Tv 38,v4,25600
Tv 40,v4,31744
Mavv 100,34,96
E_ 85
Mavv 104,34,100
s_nop 0
Tv 42,v5,25600
Tv 44,v5,31744
Mavv 84,30,100
E_ 86
Mavv 88,30,104
E_ 87
Mavv 108,34,104
C_ 124,84,85
C_ 125,86,87
Mavv 112,34,108
E_ 88
Mavv 92,30,108
E_ 89
Mavv 96,30,112
E_ 90
Mavv 116,34,112
W_ 0
E_ 91
Mavv 136,38,112
s_nop 0
Tv 30,v2,26624
Tv 32,v2,32768
Mavv 156,42,112
C_ 128,88,89
C_ 129,90,91
Mavv 152,42,108
s_nop 0
Tv 34,v3,26624
Tv 36,v3,32768
Mavv 132,38,108
E_ 92
Mavv 128,38,104
E_ 93
Mavv 148,42,104
E_ 94
Mavv 144,42,100
E_ 95
Mavv 124,38,100
C_ 132,92,93
C_ 133,94,95
Mavv 120,38,96
v_mov_b32 v92, 0x3f803f80
v_mov_b32 v93, 0x3f803f80
Mavv 140,42,96
W_ 0
v_mov_b32 v94, 0x3f803f80
v_mov_b32 v95, 0x3f803f80
Mavv 160,30,96
s_nop 0
Tv 38,v4,26624
Tv 40,v4,32768
Mavv 180,34,96
Mavv 184,34,100
s_nop 0
Tv 42,v5,26624
Tv 44,v5,32768
Mavv 164,30,100
Mavv 168,30,104
Mavv 188,34,104
S_ s65,s61,184320
Mavv 192,34,108
S_ m0,s51,24576
Mavv 172,30,108
s_waitcnt vmcnt(6) lgkmcnt(0)
s_barrier
Mavv 176,30,112
Ln v7,32,s65,0
Mavv 196,34,112
Mavv 216,38,112
Dv 30,%0,6144
Mavv 236,42,112
Dv 34,v1,6144
Mavv 232,42,108
Ln v8,32,s65,3072
S_ m0,s51,25472
Mavv 212,38,108
Mavv 208,38,104
Ln v7,32,s65,128
Mavv 228,42,104
Mavv 224,42,100
Ln v8,32,s65,3200
S_ m0,s51,26368
Mavv 204,38,100
Mavv 200,38,96
Ln v7,32,s65,256
Mavv 220,42,96
.Lstep1_14:
Zvvv 76,92,116
Dv 38,%0,7168
Zvvv 80,92,120
Dv 42,v1,7168
Zvvv 84,92,124
Zvvv 88,92,128
Zvvv 92,92,132
W_ 2
Nvvv 96,30,136,56
Mvvv 96,34,156
Nvvv 100,30,140,60
s_nop 1
v_max3_f32 v10, v76, v80, v84
s_nop 0
v_max3_f32 v10, v10, v88, v92
Mvvv 100,34,160
v_cmp_lt_f32 vcc, 0x42000000, v10
s_cbranch_vccnz .Ltramp1_13
Nvvv 104,30,144,64
A_ v46,v46,v76
A_ v47,v47,v80
Mvvv 104,34,164
A_ v48,v48,v84
A_ v49,v49,v88
Nvvv 108,30,148,68
A_ v50,v50,v92
Mvvv 108,34,168
Ln v8,32,s65,3328
Nvvv 112,30,152,72
Mvvv 112,34,172
W_ 0
Mvvv 112,38,192
Dv 30,%0,8192
Mvvv 112,42,212
Dv 34,v1,8192
Mvvv 108,38,188
Mvvv 108,42,208
Mvvv 104,38,184
Mvvv 104,42,204
Mvvv 100,38,180
Mvvv 100,42,200
Mvvv 96,38,176
Mvvv 96,42,196
W_ 0
Mvvv 96,30,216
Dv 38,%0,0
Mvvv 96,34,236
Dv 42,v1,0
Mvvv 100,30,220
Mvvv 100,34,240
Mvvv 104,30,224
Mvvv 104,34,244
Mvvv 108,30,228
Mvvv 108,34,248
s_nop 0
E_ 96
Mvvv 112,30,232
E_ 97
Mvvv 112,34,252
W_ 0
E_ 98
Nvvv 76,38,136,56
Dv 30,%0,1024
Mvvv 76,42,156
Dv 34,v1,1024
Nvvv 80,38,140,60
E_ 99
Mvvv 80,42,160
C_ 99,98,99
C_ 98,96,97
Nvvv 84,38,144,64
E_ 100
Mvvv 84,42,164
E_ 101
Nvvv 88,38,148,68
E_ 102
Mvvv 88,42,168
E_ 103
Nvvv 92,38,152,72
C_ 103,102,103
C_ 102,100,101
Mvvv 92,42,172
W_ 0
E_ 104
Mvvv 92,30,192
Dv 38,%0,2048
Mvvv 92,34,212
Dv 42,v1,2048
Mvvv 88,30,188
E_ 105
Mvvv 88,34,208
E_ 106
Mvvv 84,30,184
S_ s65,s61,172032
E_ 107
Mvvv 84,34,204
S_ m0,s51,0
C_ 107,106,107
C_ 106,104,105
Mvvv 80,30,180
s_mov_b64 exec, 0
s_bitcmp1_b32 s4, 1
s_cbranch_scc1 .Lhop_o_b
s_bitcmp1_b32 s4, 0
s_cbranch_scc1 .Lpad_o_1
s_branch .Lpad_o_0
.Lhop_o_b:
s_bitcmp1_b32 s4, 0
s_cbranch_scc1 .Lpad_o_3
s_branch .Lpad_o_2
.Lhop_o_r:
s_mov_b64 exec, -1
s_nop 1
s_waitcnt vmcnt(6) lgkmcnt(0)
s_barrier
Mvvv 80,34,200
Ln v9,36,s65,0
Mvvv 76,30,176
E_ 108
Mvvv 76,34,196
Ln v9,36,s65,3072
S_ m0,s51,896
Mvvv 76,38,216
Tv 30,v2,12288
Tv 32,v2,18432
Mvvv 76,42,236
Ln v9,36,s65,128
Mvvv 80,38,220
s_nop 0
Tv 34,v3,12288
Tv 36,v3,18432
Mvvv 80,42,240
Ln v9,36,s65,3200
S_ m0,s51,1792
Mvvv 84,38,224
E_ 109
Mvvv 84,42,244
Ln v9,36,s65,256
Mvvv 88,38,228
E_ 110
Mvvv 88,42,248
Ln v9,36,s65,3328
Mvvv 92,38,232
E_ 111
Mvvv 92,42,252
W_ 0
C_ 111,110,111
C_ 110,108,109
Mavv 0,30,116
s_nop 0
Tv 38,v4,12288
Tv 40,v4,18432
Mavv 20,34,116
E_ 112
Mavv 24,34,120
s_nop 0
Tv 42,v5,12288
Tv 44,v5,18432
Mavv 4,30,120
E_ 113
Mavv 8,30,124
E_ 114
Mavv 28,34,124
E_ 115
Mavv 32,34,128
C_ 115,114,115
C_ 114,112,113
Mavv 12,30,128
E_ 76
Mavv 16,30,132
E_ 77
Mavv 36,34,132
W_ 0
E_ 78
Mavv 56,38,132
s_nop 0
Tv 30,v2,13312
Tv 32,v2,19456
Mavv 76,42,132
E_ 79
Mavv 72,42,128
s_nop 0
Tv 34,v3,13312
Tv 36,v3,19456
Mavv 52,38,128
C_ 96,76,77
C_ 97,78,79
Mavv 48,38,124
E_ 80
Mavv 68,42,124
E_ 81
Mavv 64,42,120
E_ 82
Mavv 44,38,120
E_ 83
Mavv 40,38,116
C_ 100,80,81
C_ 101,82,83
Mavv 60,42,116
W_ 0
E_ 84
Mavv 80,30,116
s_nop 0
Tv 38,v4,13312
Tv 40,v4,19456
Mavv 100,34,116
E_ 85
Mavv 104,34,120
s_nop 0
Tv 42,v5,13312
Tv 44,v5,19456
Mavv 84,30,120
E_ 86
Mavv 88,30,124
E_ 87
Mavv 108,34,124
C_ 104,84,85
C_ 105,86,87
Mavv 112,34,128
E_ 88
Mavv 92,30,128
E_ 89
Mavv 96,30,132
E_ 90
Mavv 116,34,132
W_ 0
E_ 91
Mavv 136,38,132
s_nop 0
Tv 30,v2,14336
Tv 32,v2,20480
Mavv 156,42,132
C_ 108,88,89
C_ 109,90,91
Mavv 152,42,128
s_nop 0
Tv 34,v3,14336
Tv 36,v3,20480
Mavv 132,38,128
E_ 92
Mavv 128,38,124
E_ 93
Mavv 148,42,124
E_ 94
Mavv 144,42,120
E_ 95
Mavv 124,38,120
C_ 112,92,93
C_ 113,94,95
Mavv 120,38,116
v_mov_b32 v92, 0x3f803f80
v_mov_b32 v93, 0x3f803f80
Mavv 140,42,116
W_ 0
v_mov_b32 v94, 0x3f803f80
v_mov_b32 v95, 0x3f803f80
Mavv 160,30,116
s_nop 0
Tv 38,v4,14336
Tv 40,v4,20480
Mavv 180,34,116
Mavv 184,34,120
s_nop 0
Tv 42,v5,14336
Tv 44,v5,20480
Mavv 164,30,120
Mavv 168,30,124
Mavv 188,34,124
S_ s65,s61,184320
Mavv 192,34,128
S_ m0,s51,12288
Mavv 172,30,128
s_waitcnt vmcnt(6) lgkmcnt(0)
s_barrier
Mavv 176,30,132
Ln v9,36,s65,0
Mavv 196,34,132
Mavv 216,38,132
Dv 30,%0,30720
Mavv 236,42,132
Dv 34,v1,30720
Mavv 232,42,128
Ln v9,36,s65,3072
S_ m0,s51,13184
Mavv 212,38,128
Mavv 208,38,124
Ln v9,36,s65,128
Mavv 228,42,124
Mavv 224,42,120
Ln v9,36,s65,3200
S_ m0,s51,14080
Mavv 204,38,120
Mavv 200,38,116
Ln v9,36,s65,256
Mavv 220,42,116
.Lstep1_15:
Zvvv 76,92,96
Dv 38,%0,31744
Zvvv 80,92,100
Dv 42,v1,31744
Zvvv 84,92,104
Zvvv 88,92,108
Zvvv 92,92,112
W_ 2
Nvvv 116,30,136,56
Mvvv 116,34,156
Nvvv 120,30,140,60
s_nop 1
v_max3_f32 v10, v76, v80, v84
s_nop 0
v_max3_f32 v10, v10, v88, v92
Mvvv 120,34,160
v_cmp_lt_f32 vcc, 0x42000000, v10
s_cbranch_vccnz .Ltramp1_14
Nvvv 124,30,144,64
A_ v46,v46,v76
A_ v47,v47,v80
Mvvv 124,34,164
A_ v48,v48,v84
A_ v49,v49,v88
Nvvv 128,30,148,68
A_ v50,v50,v92
Mvvv 128,34,168
Ln v9,36,s65,3328
Nvvv 132,30,152,72
Mvvv 132,34,172
W_ 0
Mvvv 132,38,192
Dv 30,%0,32768
Mvvv 132,42,212
Dv 34,v1,32768
Mvvv 128,38,188
Mvvv 128,42,208
Mvvv 124,38,184
Mvvv 124,42,204
Mvvv 120,38,180
Mvvv 120,42,200
Mvvv 116,38,176
Mvvv 116,42,196
W_ 0
Mvvv 116,30,216
Dv 38,%0,24576
Mvvv 116,34,236
Dv 42,v1,24576
Mvvv 120,30,220
Mvvv 120,34,240
Mvvv 124,30,224
Mvvv 124,34,244
Mvvv 128,30,228
Mvvv 128,34,248
s_nop 0
E_ 116
Mvvv 132,30,232
E_ 117
Mvvv 132,34,252
W_ 0
E_ 118
Nvvv 76,38,136,56
Dv 30,%0,25600
Mvvv 76,42,156
Dv 34,v1,25600
Nvvv 80,38,140,60
E_ 119
Mvvv 80,42,160
C_ 119,118,119
C_ 118,116,117
Nvvv 84,38,144,64
E_ 120
Mvvv 84,42,164
E_ 121
Nvvv 88,38,148,68
E_ 122
Mvvv 88,42,168
E_ 123
Nvvv 92,38,152,72
C_ 123,122,123
C_ 122,120,121
Mvvv 92,42,172
W_ 0
E_ 124
Mvvv 92,30,192
Dv 38,%0,26624
Mvvv 92,34,212
Dv 42,v1,26624
Mvvv 88,30,188
E_ 125
Mvvv 88,34,208
E_ 126
Mvvv 84,30,184
E_ 127
Mvvv 84,34,204
C_ 127,126,127
C_ 126,124,125
Mvvv 80,30,180
s_waitcnt vmcnt(6) lgkmcnt(0)
s_barrier
Mvvv 80,34,200
R_ v7,a248
Mvvv 76,30,176
v_lshrrev_b32 v7, 7, v7
Mvvv 76,34,196
v_mul_u32_u24 v7, 896, v7
Mvvv 76,38,216
s_nop 0
Tv 30,v2,0
Tv 32,v2,6144
Mvvv 76,42,236
v_add_u32 v7, v7, v9
Mvvv 80,38,220
S_ s71,s72,1024
Tv 34,v3,0
Tv 36,v3,6144
Mvvv 80,42,240
S_ s55,s71,491520
E_ 128
)A"
R"A(Mvvv 84,38,224
S_ m0,s51,24576
E_ 129
Mvvv 84,42,244
L_ v7,24,s55,0
Mvvv 88,38,228
S_ s55,s71,501760
E_ 130
Mvvv 88,42,248
S_ m0,s51,25600
E_ 131
Mvvv 92,38,232
L_ v7,24,s55,0
Mvvv 92,42,252
S_ s55,s71,512000
C_ 131,130,131
C_ 130,128,129
W_ 2
Mavv 0,30,96
S_ m0,s51,26624
Tv 38,v4,0
Tv 40,v4,6144
W_ 2
Mavv 20,34,96
L_ v7,24,s55,0
Mavv 24,34,100
S_ s55,s71,522240
Tv 42,v5,0
Tv 44,v5,6144
Mavv 4,30,100
S_ m0,s51,27648
E_ 132
Mavv 8,30,104
L_ v7,24,s55,0
Mavv 28,34,104
S_ s55,s71,532480
E_ 133
Mavv 32,34,108
S_ m0,s51,28672
E_ 134
Mavv 12,30,108
L_ v7,24,s55,0
Mavv 16,30,112
S_ s55,s71,542720
E_ 135
Mavv 36,34,112
S_ m0,s51,29696
C_ 135,134,135
C_ 134,132,133
W_ 2
Mavv 56,38,112
Tv 30,v2,1024
Tv 32,v2,7168
W_ 2
Mavv 76,42,112
L_ v7,24,s55,0
Mavv 72,42,108
Tv 34,v3,1024
Tv 36,v3,7168
Mavv 52,38,108
E_ 76
Mavv 48,38,104
E_ 77
Mavv 68,42,104
E_ 78
Mavv 64,42,100
E_ 79
Mavv 44,38,100
C_ 116,76,77
C_ 117,78,79
Mavv 40,38,96
E_ 80
Mavv 60,42,96
W_ 0
E_ 81
Mavv 80,30,96
s_nop 0
Tv 38,v4,1024
Tv 40,v4,7168
Mavv 100,34,96
E_ 82
Mavv 104,34,100
s_nop 0
Tv 42,v5,1024
Tv 44,v5,7168
Mavv 84,30,100
E_ 83
Mavv 88,30,104
C_ 120,80,81
C_ 121,82,83
Mavv 108,34,104
E_ 84
Mavv 112,34,108
E_ 85
Mavv 92,30,108
E_ 86
Mavv 96,30,112
E_ 87
Mavv 116,34,112
W_ 0
C_ 124,84,85
C_ 125,86,87
Mavv 136,38,112
s_nop 0
Tv 30,v2,2048
Tv 32,v2,8192
Mavv 156,42,112
E_ 88
Mavv 152,42,108
s_nop 0
Tv 34,v3,2048
Tv 36,v3,8192
Mavv 132,38,108
E_ 89
Mavv 128,38,104
E_ 90
Mavv 148,42,104
E_ 91
Mavv 144,42,100
C_ 128,88,89
C_ 129,90,91
Mavv 124,38,100
E_ 92
Mavv 120,38,96
E_ 93
Mavv 140,42,96
W_ 0
E_ 94
Mavv 160,30,96
s_nop 0
Tv 38,v4,2048
Tv 40,v4,8192
Mavv 180,34,96
E_ 95
Mavv 184,34,100
s_nop 0
Tv 42,v5,2048
Tv 44,v5,8192
Mavv 164,30,100
C_ 132,92,93
C_ 133,94,95
Mavv 168,30,104
v_mov_b32 v92, 0x3f803f80
v_mov_b32 v93, 0x3f803f80
Mavv 188,34,104
v_mov_b32 v94, 0x3f803f80
v_mov_b32 v95, 0x3f803f80
Mavv 192,34,108
R_ v11,a248
Mavv 172,30,108
s_waitcnt vmcnt(6) lgkmcnt(0)
s_barrier
v_mul_u32_u24 v11, 80, v11
Mavv 176,30,112
S_ s55,s71,614400
v_min_u32 v11, 29440, v11
Mavv 196,34,112
S_ m0,s51,0
S_ s85,s87,1024
Mavv 216,38,112
s_nop 0
Tv 30,v2,12288
Tv 32,v2,18432
Zvvv 76,92,116
L_ v7,24,s55,0
Zvvv 80,92,120
S_ s55,s71,624640
Tv 34,v3,12288
Tv 36,v3,18432
Zvvv 84,92,124
buffer_load_dword a254, v11, s[24:27], s85 offen
Zvvv 88,92,128
S_ m0,s51,1024
S_ s85,s87,1152
Zvvv 92,92,132
L_ v7,24,s55,0
Mavv 236,42,112
S_ s55,s71,634880
Mavv 232,42,108
buffer_load_dword a254, v11, s[24:27], s85 offen
Mavv 212,38,108
S_ m0,s51,2048
S_ s85,s6,1
Mavv 208,38,104
s_and_b32 s85, s85, 7
v_max3_f32 v10, v76, v80, v84
Mavv 228,42,104
s_lshl_b32 s85, s85, 7
v_max3_f32 v10, v10, v88, v92
v_cmp_lt_f32 vcc, 0x42000000, v10
Mavv 224,42,100
L_ v7,24,s55,0
Mavv 204,38,100
S_ s55,s71,645120
S_ s85,s85,s87
Mavv 200,38,96
buffer_load_dword a254, v11, s[24:27], s85 offen
Mavv 220,42,96
S_ m0,s51,3072
s_cbranch_vccnz .Ltramp1_15
.Ljoin1:
W_ 2
Mavv 0,30,116
s_nop 0
Tv 38,v4,12288
Tv 40,v4,18432
W_ 2
Mavv 20,34,116
L_ v7,24,s55,0
Mavv 24,34,120
S_ s55,s71,655360
Tv 42,v5,12288
Tv 44,v5,18432
Mavv 4,30,120
S_ m0,s51,4096
R_ v13,a248
S_ s53,s51,36864
Mavv 8,30,124
L_ v7,24,s55,0
Mavv 28,34,124
S_ s55,s71,665600
v_add_u32 v12, s53, v13
s_mul_i32 s53, s4, 10240
Mavv 32,34,128
S_ m0,s51,5120
v_add_u32 v13, s53, v13
R_ v10,a248
Mavv 12,30,128
L_ v7,24,s55,0
Mavv 16,30,132
A_ v46,v46,v76
A_ v47,v47,v80
Mavv 36,34,132
buffer_load_dword v11, off, s[92:95], s96 offset:0 sc1
W_ 2
Mavv 56,38,132
W_ 0
Tv 30,v2,13312
Tv 32,v2,19456
Mavv 76,42,132
A_ v48,v48,v84
A_ v49,v49,v88
Mavv 72,42,128
s_nop 0
Tv 34,v3,13312
Tv 36,v3,19456
Mavv 52,38,128
A_ v50,v50,v92
Mavv 48,38,124
v_rcp_f32 v46, v46
Mavv 68,42,124
v_rcp_f32 v47, v47
Mavv 64,42,120
v_rcp_f32 v48, v48
Mavv 44,38,120
v_rcp_f32 v49, v49
Mavv 40,38,116
v_rcp_f32 v50, v50
Mavv 60,42,116
W_ 0
R_ v96,a0
R_ v97,a1
Mavv 80,30,116
s_nop 0
Tv 38,v4,13312
Tv 40,v4,19456
Mavv 100,34,116
R_ v98,a2
R_ v99,a3
Mavv 104,34,120
s_nop 0
Tv 42,v5,13312
Tv 44,v5,19456
Mavv 84,30,120
R_ v100,a20
R_ v101,a21
Mavv 88,30,124
R_ v102,a22
R_ v103,a23
Mavv 108,34,124
U_ v96,v96,v46
U_ v97,v97,v46
Mavv 112,34,128
U_ v98,v98,v46
U_ v99,v99,v46
Mavv 92,30,128
U_ v100,v100,v46
U_ v101,v101,v46
Mavv 96,30,132
U_ v102,v102,v46
U_ v103,v103,v46
Mavv 116,34,132
W_ 0
C_ 136,96,97
C_ 137,98,99
Mavv 136,38,132
s_nop 0
Tv 30,v2,14336
Tv 32,v2,20480
Mavv 156,42,132
C_ 138,100,101
C_ 139,102,103
Mavv 152,42,128
s_nop 0
Tv 34,v3,14336
Tv 36,v3,20480
Mavv 132,38,128
Da 0,v13,0
Mavv 128,38,124
R_ v104,a4
R_ v105,a5
Mavv 148,42,124
R_ v106,a6
R_ v107,a7
Mavv 144,42,120
R_ v108,a24
R_ v109,a25
Mavv 124,38,120
R_ v110,a26
R_ v111,a27
Mavv 120,38,116
U_ v104,v104,v47
U_ v105,v105,v47
Mavv 140,42,116
W_ 1
U_ v106,v106,v47
U_ v107,v107,v47
Mavv 160,30,116
s_nop 0
Tv 38,v4,14336
Tv 40,v4,20480
Mavv 180,34,116
U_ v108,v108,v47
U_ v109,v109,v47
Mavv 184,34,120
s_nop 0
Tv 42,v5,14336
Tv 44,v5,20480
Mavv 164,30,120
U_ v110,v110,v47
U_ v111,v111,v47
Mavv 168,30,124
C_ 140,104,105
C_ 141,106,107
Mavv 188,34,124
C_ 142,108,109
C_ 143,110,111
Mavv 192,34,128
Da 4,v13,1024
Mavv 172,30,128
S_ s55,s71,0
R_ v96,a8
R_ v97,a9
Mavv 176,30,132
S_ m0,s51,12288
R_ v98,a10
R_ v99,a11
Mavv 196,34,132
s_waitcnt vmcnt(63) lgkmcnt(0)
s_barrier
R_ v100,a28
Mavv 216,38,132
L_ v7,24,s55,0
S_ s55,s71,10240
S_ m0,s51,13312
Mavv 236,42,132
R_ v101,a29
Mavv 232,42,128
L_ v7,24,s55,0
S_ s55,s71,20480
S_ m0,s51,14336
Mavv 212,38,128
R_ v102,a30
R_ v103,a31
Mavv 208,38,124
L_ v7,24,s55,0
S_ s55,s71,30720
S_ m0,s51,15360
Mavv 228,42,124
U_ v96,v96,v48
U_ v97,v97,v48
Mavv 224,42,120
L_ v7,24,s55,0
S_ s55,s71,40960
S_ m0,s51,16384
Mavv 204,38,120
U_ v98,v98,v48
U_ v99,v99,v48
Mavv 200,38,116
L_ v7,24,s55,0
S_ s55,s71,51200
S_ m0,s51,17408
Mavv 220,42,116
U_ v100,v100,v48
U_ v101,v101,v48
U_ v102,v102,v48
U_ v103,v103,v48
C_ 144,96,97
C_ 145,98,99
C_ 146,100,101
C_ 147,102,103
Da 8,v13,2048
L_ v7,24,s55,0
.p2align 3
R_ v104,a12
R_ v105,a13
R_ v106,a14
R_ v107,a15
R_ v108,a32
R_ v109,a33
R_ v110,a34
R_ v111,a35
v_pk_mul_f32 v[104:105], v[104:105], v[48:49] op_sel:[0,1]
v_pk_mul_f32 v[106:107], v[106:107], v[48:49] op_sel:[0,1]
v_pk_mul_f32 v[108:109], v[108:109], v[48:49] op_sel:[0,1]
v_pk_mul_f32 v[110:111], v[110:111], v[48:49] op_sel:[0,1]
C_ 148,104,105
C_ 149,106,107
C_ 150,108,109
C_ 151,110,111
Da 12,v13,3072
.p2align 3
R_ v96,a16
R_ v97,a17
R_ v98,a18
R_ v99,a19
R_ v100,a36
R_ v101,a37
R_ v102,a38
R_ v103,a39
v_pk_mul_f32 v[96:97], v[96:97], v[50:51] op_sel_hi:[1,0]
v_pk_mul_f32 v[98:99], v[98:99], v[50:51] op_sel_hi:[1,0]
v_pk_mul_f32 v[100:101], v[100:101], v[50:51] op_sel_hi:[1,0]
v_pk_mul_f32 v[102:103], v[102:103], v[50:51] op_sel_hi:[1,0]
C_ 152,96,97
C_ 153,98,99
C_ 154,100,101
C_ 155,102,103
Da 16,v13,4096
.p2align 3
R_ v104,a40
R_ v105,a41
R_ v106,a42
R_ v107,a43
R_ v108,a60
R_ v109,a61
R_ v110,a62
R_ v111,a63
v_pk_mul_f32 v[104:105], v[104:105], v[46:47] op_sel_hi:[1,0]
v_pk_mul_f32 v[106:107], v[106:107], v[46:47] op_sel_hi:[1,0]
v_pk_mul_f32 v[108:109], v[108:109], v[46:47] op_sel_hi:[1,0]
v_pk_mul_f32 v[110:111], v[110:111], v[46:47] op_sel_hi:[1,0]
C_ 156,104,105
C_ 157,106,107
C_ 158,108,109
C_ 159,110,111
Da 40,v13,5120
.p2align 3
R_ v96,a44
R_ v97,a45
R_ v98,a46
R_ v99,a47
R_ v100,a64
R_ v101,a65
R_ v102,a66
R_ v103,a67
v_pk_mul_f32 v[96:97], v[96:97], v[46:47] op_sel:[0,1]
v_pk_mul_f32 v[98:99], v[98:99], v[46:47] op_sel:[0,1]
v_pk_mul_f32 v[100:101], v[100:101], v[46:47] op_sel:[0,1]
v_pk_mul_f32 v[102:103], v[102:103], v[46:47] op_sel:[0,1]
C_ 160,96,97
C_ 161,98,99
C_ 162,100,101
C_ 163,102,103
Da 44,v13,6144
.p2align 3
R_ v104,a48
R_ v105,a49
R_ v106,a50
R_ v107,a51
R_ v108,a68
R_ v109,a69
R_ v110,a70
R_ v111,a71
v_pk_mul_f32 v[104:105], v[104:105], v[48:49] op_sel_hi:[1,0]
v_pk_mul_f32 v[106:107], v[106:107], v[48:49] op_sel_hi:[1,0]
v_pk_mul_f32 v[108:109], v[108:109], v[48:49] op_sel_hi:[1,0]
v_pk_mul_f32 v[110:111], v[110:111], v[48:49] op_sel_hi:[1,0]
C_ 164,104,105
C_ 165,106,107
C_ 166,108,109
C_ 167,110,111
Da 48,v13,7168
.p2align 3
R_ v96,a52
R_ v97,a53
R_ v98,a54
R_ v99,a55
R_ v100,a72
R_ v101,a73
R_ v102,a74
R_ v103,a75
v_pk_mul_f32 v[96:97], v[96:97], v[48:49] op_sel:[0,1]
v_pk_mul_f32 v[98:99], v[98:99], v[48:49] op_sel:[0,1]
v_pk_mul_f32 v[100:101], v[100:101], v[48:49] op_sel:[0,1]
v_pk_mul_f32 v[102:103], v[102:103], v[48:49] op_sel:[0,1]
C_ 168,96,97
C_ 169,98,99
C_ 170,100,101
C_ 171,102,103
Da 52,v13,8192
.p2align 3
R_ v104,a56
R_ v105,a57
R_ v106,a58
R_ v107,a59
R_ v108,a76
R_ v109,a77
R_ v110,a78
R_ v111,a79
v_pk_mul_f32 v[104:105], v[104:105], v[50:51] op_sel_hi:[1,0]
v_pk_mul_f32 v[106:107], v[106:107], v[50:51] op_sel_hi:[1,0]
v_pk_mul_f32 v[108:109], v[108:109], v[50:51] op_sel_hi:[1,0]
v_pk_mul_f32 v[110:111], v[110:111], v[50:51] op_sel_hi:[1,0]
C_ 172,104,105
C_ 173,106,107
C_ 174,108,109
C_ 175,110,111
Da 56,v13,9216
.p2align 3
R_ v96,a80
R_ v97,a81
R_ v98,a82
R_ v99,a83
R_ v100,a100
R_ v101,a101
R_ v102,a102
R_ v103,a103
v_pk_mul_f32 v[96:97], v[96:97], v[46:47] op_sel_hi:[1,0]
v_pk_mul_f32 v[98:99], v[98:99], v[46:47] op_sel_hi:[1,0]
v_pk_mul_f32 v[100:101], v[100:101], v[46:47] op_sel_hi:[1,0]
v_pk_mul_f32 v[102:103], v[102:103], v[46:47] op_sel_hi:[1,0]
C_ 176,96,97
C_ 177,98,99
C_ 178,100,101
C_ 179,102,103
X_ a80,v20
X_ a81,v21
X_ a82,v22
X_ a83,v23
.p2align 3
R_ v104,a84
R_ v105,a85
R_ v106,a86
R_ v107,a87
R_ v108,a104
R_ v109,a105
R_ v110,a106
R_ v111,a107
v_pk_mul_f32 v[104:105], v[104:105], v[46:47] op_sel:[0,1]
v_pk_mul_f32 v[106:107], v[106:107], v[46:47] op_sel:[0,1]
v_pk_mul_f32 v[108:109], v[108:109], v[46:47] op_sel:[0,1]
v_pk_mul_f32 v[110:111], v[110:111], v[46:47] op_sel:[0,1]
C_ 180,104,105
C_ 181,106,107
C_ 182,108,109
C_ 183,110,111
X_ a84,v24
X_ a85,v25
X_ a86,v26
X_ a87,v27
.p2align 3
R_ v96,a88
R_ v97,a89
R_ v98,a90
R_ v99,a91
R_ v100,a108
R_ v101,a109
R_ v102,a110
R_ v103,a111
v_pk_mul_f32 v[96:97], v[96:97], v[48:49] op_sel_hi:[1,0]
v_pk_mul_f32 v[98:99], v[98:99], v[48:49] op_sel_hi:[1,0]
v_pk_mul_f32 v[100:101], v[100:101], v[48:49] op_sel_hi:[1,0]
v_pk_mul_f32 v[102:103], v[102:103], v[48:49] op_sel_hi:[1,0]
C_ 184,96,97
C_ 185,98,99
C_ 186,100,101
C_ 187,102,103
X_ a88,v52
X_ a89,v53
X_ a90,v54
X_ a91,v55
.p2align 3
R_ v104,a92
R_ v105,a93
R_ v106,a94
R_ v107,a95
R_ v108,a112
R_ v109,a113
R_ v110,a114
R_ v111,a115
v_pk_mul_f32 v[104:105], v[104:105], v[48:49] op_sel:[0,1]
v_pk_mul_f32 v[106:107], v[106:107], v[48:49] op_sel:[0,1]
v_pk_mul_f32 v[108:109], v[108:109], v[48:49] op_sel:[0,1]
v_pk_mul_f32 v[110:111], v[110:111], v[48:49] op_sel:[0,1]
C_ 188,104,105
C_ 189,106,107
C_ 190,108,109
C_ 191,110,111
.p2align 3
R_ v96,a96
R_ v97,a97
R_ v98,a98
R_ v99,a99
R_ v100,a116
R_ v101,a117
R_ v102,a118
R_ v103,a119
v_pk_mul_f32 v[96:97], v[96:97], v[50:51] op_sel_hi:[1,0]
v_pk_mul_f32 v[98:99], v[98:99], v[50:51] op_sel_hi:[1,0]
v_pk_mul_f32 v[100:101], v[100:101], v[50:51] op_sel_hi:[1,0]
v_pk_mul_f32 v[102:103], v[102:103], v[50:51] op_sel_hi:[1,0]
C_ 192,96,97
C_ 193,98,99
C_ 194,100,101
C_ 195,102,103
.p2align 3
R_ v104,a120
R_ v105,a121
R_ v106,a122
R_ v107,a123
R_ v108,a140
R_ v109,a141
R_ v110,a142
R_ v111,a143
v_pk_mul_f32 v[104:105], v[104:105], v[46:47] op_sel_hi:[1,0]
v_pk_mul_f32 v[106:107], v[106:107], v[46:47] op_sel_hi:[1,0]
v_pk_mul_f32 v[108:109], v[108:109], v[46:47] op_sel_hi:[1,0]
v_pk_mul_f32 v[110:111], v[110:111], v[46:47] op_sel_hi:[1,0]
C_ 196,104,105
C_ 197,106,107
C_ 198,108,109
C_ 199,110,111
X_ a120,v16
X_ a121,v17
X_ a122,v18
X_ a123,v19
.p2align 3
R_ v96,a124
R_ v97,a125
R_ v98,a126
R_ v99,a127
R_ v100,a144
R_ v101,a145
R_ v102,a146
R_ v103,a147
v_pk_mul_f32 v[96:97], v[96:97], v[46:47] op_sel:[0,1]
v_pk_mul_f32 v[98:99], v[98:99], v[46:47] op_sel:[0,1]
v_pk_mul_f32 v[100:101], v[100:101], v[46:47] op_sel:[0,1]
v_pk_mul_f32 v[102:103], v[102:103], v[46:47] op_sel:[0,1]
C_ 200,96,97
C_ 201,98,99
C_ 202,100,101
C_ 203,102,103
X_ a124,v28
X_ a125,v29
X_ a126,v51
X_ a127,v6
.p2align 3
R_ v104,a128
R_ v105,a129
R_ v106,a130
R_ v107,a131
R_ v108,a148
R_ v109,a149
R_ v110,a150
R_ v111,a151
v_pk_mul_f32 v[104:105], v[104:105], v[48:49] op_sel_hi:[1,0]
v_pk_mul_f32 v[106:107], v[106:107], v[48:49] op_sel_hi:[1,0]
v_pk_mul_f32 v[108:109], v[108:109], v[48:49] op_sel_hi:[1,0]
v_pk_mul_f32 v[110:111], v[110:111], v[48:49] op_sel_hi:[1,0]
C_ 204,104,105
C_ 205,106,107
C_ 206,108,109
C_ 207,110,111
X_ a128,v14
X_ a129,v15
v_accvgpr_mov_b32 a130, a249
v_accvgpr_mov_b32 a131, a250
.p2align 3
R_ v96,a132
R_ v97,a133
R_ v98,a134
R_ v99,a135
R_ v100,a152
R_ v101,a153
R_ v102,a154
R_ v103,a155
v_pk_mul_f32 v[96:97], v[96:97], v[48:49] op_sel:[0,1]
v_pk_mul_f32 v[98:99], v[98:99], v[48:49] op_sel:[0,1]
v_pk_mul_f32 v[100:101], v[100:101], v[48:49] op_sel:[0,1]
v_pk_mul_f32 v[102:103], v[102:103], v[48:49] op_sel:[0,1]
C_ 208,96,97
C_ 209,98,99
C_ 210,100,101
C_ 211,102,103
Da 132,v12,0
.p2align 3
R_ v104,a136
R_ v105,a137
R_ v106,a138
R_ v107,a139
R_ v108,a156
R_ v109,a157
R_ v110,a158
R_ v111,a159
v_pk_mul_f32 v[104:105], v[104:105], v[50:51] op_sel_hi:[1,0]
v_pk_mul_f32 v[106:107], v[106:107], v[50:51] op_sel_hi:[1,0]
v_pk_mul_f32 v[108:109], v[108:109], v[50:51] op_sel_hi:[1,0]
v_pk_mul_f32 v[110:111], v[110:111], v[50:51] op_sel_hi:[1,0]
C_ 212,104,105
C_ 213,106,107
C_ 214,108,109
C_ 215,110,111
Da 136,v12,1024
.p2align 3
R_ v96,a160
R_ v97,a161
R_ v98,a162
R_ v99,a163
R_ v100,a180
R_ v101,a181
R_ v102,a182
R_ v103,a183
v_pk_mul_f32 v[96:97], v[96:97], v[46:47] op_sel_hi:[1,0]
v_pk_mul_f32 v[98:99], v[98:99], v[46:47] op_sel_hi:[1,0]
v_pk_mul_f32 v[100:101], v[100:101], v[46:47] op_sel_hi:[1,0]
v_pk_mul_f32 v[102:103], v[102:103], v[46:47] op_sel_hi:[1,0]
C_ 216,96,97
C_ 217,98,99
C_ 218,100,101
C_ 219,102,103
Da 160,v12,2048
.p2align 3
R_ v104,a164
R_ v105,a165
R_ v106,a166
R_ v107,a167
R_ v108,a184
R_ v109,a185
R_ v110,a186
R_ v111,a187
v_pk_mul_f32 v[104:105], v[104:105], v[46:47] op_sel:[0,1]
v_pk_mul_f32 v[106:107], v[106:107], v[46:47] op_sel:[0,1]
.Lpad_o_0:
s_cbranch_execz .Lpad_o_4
v_pk_mul_f32 v[108:109], v[108:109], v[46:47] op_sel:[0,1]
v_pk_mul_f32 v[110:111], v[110:111], v[46:47] op_sel:[0,1]
C_ 220,104,105
C_ 221,106,107
C_ 222,108,109
C_ 223,110,111
Da 164,v12,3072
.p2align 3
R_ v96,a168
R_ v97,a169
R_ v98,a170
R_ v99,a171
R_ v100,a188
R_ v101,a189
R_ v102,a190
R_ v103,a191
v_pk_mul_f32 v[96:97], v[96:97], v[48:49] op_sel_hi:[1,0]
v_pk_mul_f32 v[98:99], v[98:99], v[48:49] op_sel_hi:[1,0]
v_pk_mul_f32 v[100:101], v[100:101], v[48:49] op_sel_hi:[1,0]
v_pk_mul_f32 v[102:103], v[102:103], v[48:49] op_sel_hi:[1,0]
C_ 224,96,97
C_ 225,98,99
C_ 226,100,101
C_ 227,102,103
Da 168,v12,4096
.p2align 3
R_ v104,a172
R_ v105,a173
R_ v106,a174
R_ v107,a175
R_ v108,a192
R_ v109,a193
R_ v110,a194
R_ v111,a195
v_pk_mul_f32 v[104:105], v[104:105], v[48:49] op_sel:[0,1]
v_pk_mul_f32 v[106:107], v[106:107], v[48:49] op_sel:[0,1]
v_pk_mul_f32 v[108:109], v[108:109], v[48:49] op_sel:[0,1]
v_pk_mul_f32 v[110:111], v[110:111], v[48:49] op_sel:[0,1]
C_ 228,104,105
C_ 229,106,107
C_ 230,108,109
C_ 231,110,111
Da 172,v12,5120
.p2align 3
R_ v96,a176
R_ v97,a177
R_ v98,a178
R_ v99,a179
R_ v100,a196
R_ v101,a197
R_ v102,a198
R_ v103,a199
v_pk_mul_f32 v[96:97], v[96:97], v[50:51] op_sel_hi:[1,0]
v_pk_mul_f32 v[98:99], v[98:99], v[50:51] op_sel_hi:[1,0]
v_pk_mul_f32 v[100:101], v[100:101], v[50:51] op_sel_hi:[1,0]
v_pk_mul_f32 v[102:103], v[102:103], v[50:51] op_sel_hi:[1,0]
C_ 232,96,97
C_ 233,98,99
C_ 234,100,101
C_ 235,102,103
Da 176,v12,12288
.p2align 3
R_ v104,a200
R_ v105,a201
R_ v106,a202
R_ v107,a203
R_ v108,a220
R_ v109,a221
R_ v110,a222
R_ v111,a223
v_pk_mul_f32 v[104:105], v[104:105], v[46:47] op_sel_hi:[1,0]
v_pk_mul_f32 v[106:107], v[106:107], v[46:47] op_sel_hi:[1,0]
v_pk_mul_f32 v[108:109], v[108:109], v[46:47] op_sel_hi:[1,0]
v_pk_mul_f32 v[110:111], v[110:111], v[46:47] op_sel_hi:[1,0]
C_ 236,104,105
C_ 237,106,107
C_ 238,108,109
C_ 239,110,111
Da 200,v12,13312
.p2align 3
R_ v96,a204
R_ v97,a205
R_ v98,a206
R_ v99,a207
R_ v100,a224
R_ v101,a225
R_ v102,a226
R_ v103,a227
v_pk_mul_f32 v[96:97], v[96:97], v[46:47] op_sel:[0,1]
v_pk_mul_f32 v[98:99], v[98:99], v[46:47] op_sel:[0,1]
v_pk_mul_f32 v[100:101], v[100:101], v[46:47] op_sel:[0,1]
v_pk_mul_f32 v[102:103], v[102:103], v[46:47] op_sel:[0,1]
C_ 240,96,97
C_ 241,98,99
C_ 242,100,101
C_ 243,102,103
Da 204,v12,14336
v_mov_b32 v6, v10
v_lshrrev_b32 v10, 9, v6
v_lshlrev_b32 v10, 11, v10
v_sub_u32 %0, v2, v10
v_xor_b32 %0, s83, %0
s_waitcnt vmcnt(16) lgkmcnt(0)
s_barrier
Ta 180,%0,24576
Ta 182,%0,26624
Ta 184,%0,28672
Ta 186,%0,30720
Ta 188,%0,32768
Ta 190,%0,34816
.p2align 3
R_ v104,a208
R_ v105,a209
R_ v106,a210
R_ v107,a211
R_ v108,a228
R_ v109,a229
R_ v110,a230
R_ v111,a231
v_pk_mul_f32 v[104:105], v[104:105], v[48:49] op_sel_hi:[1,0]
v_pk_mul_f32 v[106:107], v[106:107], v[48:49] op_sel_hi:[1,0]
v_pk_mul_f32 v[108:109], v[108:109], v[48:49] op_sel_hi:[1,0]
v_pk_mul_f32 v[110:111], v[110:111], v[48:49] op_sel_hi:[1,0]
C_ 244,104,105
C_ 245,106,107
C_ 246,108,109
C_ 247,110,111
Da 208,v12,15360
.p2align 3
R_ v96,a212
R_ v97,a213
R_ v98,a214
R_ v99,a215
R_ v100,a232
R_ v101,a233
R_ v102,a234
R_ v103,a235
v_pk_mul_f32 v[96:97], v[96:97], v[48:49] op_sel:[0,1]
v_pk_mul_f32 v[98:99], v[98:99], v[48:49] op_sel:[0,1]
v_pk_mul_f32 v[100:101], v[100:101], v[48:49] op_sel:[0,1]
v_pk_mul_f32 v[102:103], v[102:103], v[48:49] op_sel:[0,1]
C_ 248,96,97
C_ 249,98,99
C_ 250,100,101
C_ 251,102,103
Da 212,v12,16384
.p2align 3
R_ v104,a216
R_ v105,a217
R_ v106,a218
R_ v107,a219
R_ v108,a236
R_ v109,a237
R_ v110,a238
R_ v111,a239
v_pk_mul_f32 v[104:105], v[104:105], v[50:51] op_sel_hi:[1,0]
v_pk_mul_f32 v[106:107], v[106:107], v[50:51] op_sel_hi:[1,0]
v_pk_mul_f32 v[108:109], v[108:109], v[50:51] op_sel_hi:[1,0]
v_pk_mul_f32 v[110:111], v[110:111], v[50:51] op_sel_hi:[1,0]
C_ 252,104,105
C_ 253,106,107
C_ 254,108,109
C_ 255,110,111
Da 216,v12,17408
v_xor_b32 v1, 32, %0
W_ 7
Zvav 56,180,136
s_mul_i32 s76, s6, 204800
Ta 192,v1,24576
Ta 194,v1,26624
W_ 7
Mvav 56,184,156
v_xor_b32 v2, 64, %0
v_xor_b32 v3, 96, %0
W_ 5
Mvav 56,188,176
s_nop 0
Ta 196,v1,28672
Ta 198,v1,30720
Zvav 60,180,140
W_ 4
R_ v4,a248
v_mul_u32_u24 v4, 80, v4
Mvav 60,184,160
S_ s55,s71,122880
Ta 220,v1,32768
Ta 222,v1,34816
Mvav 60,188,180
S_ m0,s51,49152
v_min_u32 v4, 29440, v4
s_waitcnt vmcnt(6)
Zvav 64,180,144
v_readfirstlane_b32 s62, v11
v_lshrrev_b32 v8, 2, v6
Mvav 64,184,164
v_add_u32 v5, s75, v8
R_ v9,a251
Mvav 64,188,184
v_or_b32 v9, s73, v9
v_lshrrev_b32 v10, 8, v6
Zvav 68,180,148
v_bfe_u32 v12, v6, 6, 2
v_cmp_eq_u32 vcc, v10, v12
Mvav 68,184,168
v_and_b32 v10, 16, v6
v_mov_b32 v12, 0x3f80
Mvav 68,188,188
v_lshlrev_b32 v12, v10, v12
v_cndmask_b32 v12, 0, v12, vcc
Zvav 72,180,152
v_and_b32 v10, 32, v6
v_cmp_eq_u32 vcc, 0, v10
Mvav 72,184,172
X_ a68,0
X_ a69,0
Mvav 72,188,192
W_ 0
X_ a72,0
X_ a73,0
Zvav 92,220,192
s_nop 0
Ta 180,v2,24576
Ta 182,v2,26624
Mvav 92,196,172
v_cndmask_b32 v10, 0, v12, vcc
v_sub_u32 v12, v12, v10
Mvav 92,192,152
s_nop 0
Ta 184,v2,28672
Ta 186,v2,30720
Zvav 88,220,188
X_ a70,v10
X_ a71,v12
Mvav 88,196,168
s_mov_b64 s[32:33], s[20:21]
Ta 188,v2,32768
Ta 190,v2,34816
Mvav 88,192,148
s_mov_b32 s34, 409600
S_ s85,s6,2
Zvav 84,220,184
L_ v7,24,s55,0
Mvav 84,196,164
S_ s55,s71,133120
s_and_b32 s85, s85, 7
Mvav 84,192,144
S_ m0,s51,50176
s_lshl_b32 s85, s85, 7
Zvav 80,220,180
S_ s85,s85,s87
Mvav 80,196,160
L_ v7,24,s55,0
Mvav 80,192,140
S_ s55,s71,143360
Zvav 76,220,176
S_ m0,s51,51200
Mvav 76,196,156
Mvav 76,192,136
W_ 0
Zvav 16,180,136
s_nop 0
Ta 192,v3,24576
Ta 194,v3,26624
Mvav 16,184,156
L_ v7,24,s55,0
Mvav 16,188,176
S_ s55,s71,153600
Ta 196,v3,28672
Ta 198,v3,30720
Zvav 20,180,140
S_ m0,s51,52224
Mvav 20,184,160
s_nop 0
Ta 220,v3,32768
Ta 222,v3,34816
Mvav 20,188,180
L_ v7,24,s55,0
Zvav 24,180,144
S_ s55,s71,163840
Mvav 24,184,164
S_ m0,s51,53248
Mvav 24,188,184
Zvav 28,180,148
L_ v7,24,s55,0
Mvav 28,184,168
S_ s55,s71,174080
Mvav 28,188,188
S_ m0,s51,54272
Zvav 32,180,152
Mvav 32,184,172
L_ v7,24,s55,0
Mvav 32,188,192
s_waitcnt vmcnt(13) lgkmcnt(0)
s_barrier
S_ s71,s72,1152
Zvav 52,220,192
S_ s55,s71,491520
Ta 180,%0,0
Ta 182,%0,2048
Mvav 52,196,172
S_ m0,s51,36864
Mvav 52,192,152
s_nop 0
Ta 184,%0,4096
Ta 186,%0,6144
Zvav 48,220,188
L_ v7,24,s55,0
Mvav 48,196,168
S_ s55,s71,501760
Ta 188,%0,8192
Ta 190,%0,10240
Mvav 48,192,148
S_ m0,s51,37888
Zvav 44,220,184
Mvav 44,196,164
L_ v7,24,s55,0
Mvav 44,192,144
S_ s55,s71,512000
Zvav 40,220,180
S_ m0,s51,38912
Mvav 40,196,160
Mvav 40,192,140
L_ v7,24,s55,0
Zvav 36,220,176
S_ s55,s71,522240
Mvav 36,196,156
S_ m0,s51,39936
Mvav 36,192,136
W_ 0
Mvav 56,180,196
s_nop 0
Ta 192,v1,0
Ta 194,v1,2048
Mvav 56,184,216
L_ v7,24,s55,0
Mvav 56,188,236
S_ s55,s71,532480
Ta 196,v1,4096
Ta 198,v1,6144
Mvav 60,180,200
S_ m0,s51,40960
Mvav 60,184,220
s_nop 0
Ta 220,v1,8192
Ta 222,v1,10240
Mvav 60,188,240
L_ v7,24,s55,0
Mvav 64,180,204
S_ s55,s71,542720
Mvav 64,184,224
S_ m0,s51,41984
Mvav 64,188,244
Mvav 68,180,208
L_ v7,24,s55,0
Mvav 68,184,228
S_ s55,s71,614400
Mvav 68,188,248
S_ m0,s51,24576
Mvav 72,180,212
Mvav 72,184,232
L_ v7,24,s55,0
Mvav 72,188,252
W_ 0
S_ s55,s71,624640
Mvav 92,220,252
S_ m0,s51,25600
Ta 180,v2,0
Ta 182,v2,2048
Mvav 92,196,232
Mvav 92,192,212
s_nop 0
Ta 184,v2,4096
Ta 186,v2,6144
Mvav 88,220,248
L_ v7,24,s55,0
Mvav 88,196,228
S_ s55,s71,634880
Ta 188,v2,8192
Ta 190,v2,10240
Mvav 88,192,208
S_ m0,s51,26624
Mvav 84,220,244
Mvav 84,196,224
L_ v7,24,s55,0
Mvav 84,192,204
S_ s55,s71,645120
Mvav 80,220,240
S_ m0,s51,27648
Mvav 80,196,220
Mvav 80,192,200
L_ v7,24,s55,0
Mvav 76,220,236
S_ s55,s71,655360
Mvav 76,196,216
S_ m0,s51,28672
Mvav 76,192,196
W_ 0
Mvav 16,180,196
s_nop 0
Ta 192,v3,0
Ta 194,v3,2048
Mvav 16,184,216
L_ v7,24,s55,0
Mvav 16,188,236
S_ s55,s71,665600
Ta 196,v3,4096
Ta 198,v3,6144
Mvav 20,180,200
S_ m0,s51,29696
Mvav 20,184,220
s_nop 0
Ta 220,v3,8192
Ta 222,v3,10240
Mvav 20,188,240
L_ v7,24,s55,0
Mvav 24,180,204
Mvav 24,184,224
Mvav 24,188,244
Mvav 28,180,208
buffer_load_dword a74, v4, s[24:27], s85 offen
Mvav 28,184,228
Mvav 28,188,248
Mvav 32,180,212
Mvav 32,184,232
Mvav 32,188,252
s_waitcnt vmcnt(19) lgkmcnt(0)
s_barrier
Mvav 52,220,252
S_ s55,s71,0
Ta 180,%0,12288
Ta 182,%0,14336
Mvav 52,196,232
S_ m0,s51,0
Mvav 52,192,212
s_nop 0
Ta 184,%0,16384
Ta 186,%0,18432
Mvav 48,220,248
L_ v7,24,s55,0
Mvav 48,196,228
S_ s55,s71,10240
Ta 188,%0,20480
Ta 190,%0,22528
Mvav 48,192,208
S_ m0,s51,1024
Mvav 44,220,244
Mvav 44,196,224
L_ v7,24,s55,0
Mvav 44,192,204
S_ s55,s71,20480
Mvav 40,220,240
S_ m0,s51,2048
Mvav 40,196,220
Mvav 40,192,200
L_ v7,24,s55,0
Mvav 36,220,236
S_ s55,s71,30720
Mvav 36,196,216
S_ m0,s51,3072
Mvav 36,192,196
W_ 0
Mvaa 56,180,0
s_nop 0
Ta 192,v1,12288
Ta 194,v1,14336
Mvaa 56,184,40
L_ v7,24,s55,0
Mvaa 56,188,80
S_ s55,s71,40960
Ta 196,v1,16384
Ta 198,v1,18432
Mvaa 60,180,4
S_ m0,s51,4096
Mvaa 60,184,44
s_nop 0
Ta 220,v1,20480
Ta 222,v1,22528
Mvaa 60,188,84
L_ v7,24,s55,0
Mvaa 64,180,8
S_ s55,s71,51200
Mvaa 64,184,48
S_ m0,s51,5120
Mvaa 64,188,88
Mvaa 68,180,12
L_ v7,24,s55,0
Mvaa 68,184,52
Mvaa 68,188,240
Mvaa 72,180,16
Mvaa 72,184,56
Mvaa 72,188,244
W_ 0
Mvaa 92,220,244
s_nop 0
Ta 180,v2,12288
Ta 182,v2,14336
Mvaa 92,196,56
Mvaa 92,192,16
s_nop 0
Ta 184,v2,16384
Ta 186,v2,18432
Mvaa 88,220,240
Mvaa 88,196,52
s_nop 0
Ta 188,v2,20480
Ta 190,v2,22528
Mvaa 88,192,12
Mvaa 84,220,88
Mvaa 84,196,48
Mvaa 84,192,8
Mvaa 80,220,84
Mvaa 80,196,44
Mvaa 80,192,4
Mvaa 76,220,80
Mvaa 76,196,40
Mvaa 76,192,0
W_ 0
Mvaa 16,180,0
s_nop 0
Ta 192,v3,12288
Ta 194,v3,14336
Mvaa 16,184,40
Mvaa 16,188,80
s_nop 0
Ta 196,v3,16384
Ta 198,v3,18432
Mvaa 20,180,4
Mvaa 20,184,44
s_nop 0
Ta 220,v3,20480
Ta 222,v3,22528
Mvaa 20,188,84
Mvaa 24,180,8
Mvaa 24,184,48
Mvaa 24,188,88
Mvaa 28,180,12
Mvaa 28,184,52
Mvaa 28,188,240
Mvaa 32,180,16
Mvaa 32,184,56
Mvaa 32,188,244
s_waitcnt vmcnt(19) lgkmcnt(0)
s_barrier
Mvaa 52,220,244
s_nop 0
Ta 180,%0,49152
Ta 182,%0,51200
Mvaa 52,196,56
.Lpad_o_1:
s_cbranch_execz .Lpad_o_5
Mvaa 52,192,16
s_nop 0
Ta 184,%0,53248
Ta 186,%0,55296
Mvaa 48,220,240
Mvaa 48,196,52
s_nop 0
Ta 188,%0,57344
Ta 190,%0,59392
Mvaa 48,192,12
Mvaa 44,220,88
Mvaa 44,196,48
Mvaa 44,192,8
Mvaa 40,220,84
Mvaa 40,196,44
Mvaa 40,192,4
Mvaa 36,220,80
Mvaa 36,196,40
Mvaa 36,192,0
W_ 0
Mvaa 56,180,120
S_ s73,s78,163840
Ta 192,v1,49152
Ta 194,v1,51200
Mvaa 56,184,160
S_ s73,s73,4096
Mvaa 56,188,200
s_nop 0
Ta 196,v1,53248
Ta 198,v1,55296
Mvaa 60,180,124
s_mov_b32 m0, s52
Mvaa 60,184,164
s_nop 0
Ta 220,v1,57344
Ta 222,v1,59392
Mvaa 60,188,204
s_nop 0
Y_ 56,0
Y_ 57,256
Mvaa 64,180,128
s_nop 0
Y_ 58,512
Y_ 59,768
Mvaa 64,184,168
Mvaa 64,188,208
s_nop 0
Y_ 60,1024
Y_ 61,1280
Mvaa 68,180,132
s_nop 0
Y_ 62,1536
Y_ 63,1792
Mvaa 68,184,172
Mvaa 68,188,212
s_nop 0
Y_ 64,2048
Y_ 65,2304
Mvaa 72,180,136
s_nop 0
Y_ 66,2560
Y_ 67,2816
Mvaa 72,184,176
Mvaa 72,188,216
W_ 12
Y_ 68,3072
Y_ 69,3328
Mvaa 92,220,216
s_nop 0
Ta 180,v2,49152
Ta 182,v2,51200
Mvaa 92,196,176
s_nop 0
Y_ 70,3584
Y_ 71,3840
Mvaa 92,192,136
s_nop 0
Ta 184,v2,53248
Ta 186,v2,55296
Mvaa 88,220,212
s_nop 0
Y_ 72,4096
Y_ 73,4352
Mvaa 88,196,172
s_nop 0
Ta 188,v2,57344
Ta 190,v2,59392
Mvaa 88,192,132
s_nop 0
Y_ 74,4608
Y_ 75,4864
Mvaa 84,220,208
s_nop 0
Y_ 92,9216
Y_ 93,9472
Mvaa 84,196,168
s_nop 0
Y_ 94,9728
Y_ 95,9984
Mvaa 84,192,128
s_nop 0
Y_ 88,8192
Y_ 89,8448
Mvaa 80,220,204
s_nop 0
Y_ 90,8704
Y_ 91,8960
Mvaa 80,196,164
Mvaa 80,192,124
s_nop 0
Y_ 84,7168
Y_ 85,7424
Mvaa 76,220,200
s_nop 0
Y_ 86,7680
Y_ 87,7936
Mvaa 76,196,160
Mvaa 76,192,120
W_ 14
Y_ 80,6144
Y_ 81,6400
Mvaa 16,180,120
s_nop 0
Ta 192,v3,49152
Ta 194,v3,51200
Mvaa 16,184,160
s_nop 0
Y_ 82,6656
Y_ 83,6912
Mvaa 16,188,200
s_nop 0
Ta 196,v3,53248
Ta 198,v3,55296
Mvaa 20,180,124
s_nop 0
Y_ 76,5120
Y_ 77,5376
Mvaa 20,184,164
s_nop 0
Ta 220,v3,57344
Ta 222,v3,59392
Mvaa 20,188,204
s_nop 0
Y_ 78,5632
Y_ 79,5888
Mvaa 24,180,128
Mvaa 24,184,168
Mvaa 24,188,208
s_waitcnt vmcnt(13) lgkmcnt(0)
s_barrier
Mvaa 28,180,132
S_ s55,s71,122880
ds_read2st64_b32 v[96:97], v5 offset0:0 offset1:1
Mvaa 28,184,172
S_ m0,s51,12288
ds_read2st64_b32 v[98:99], v5 offset0:2 offset1:3
Mvaa 28,188,212
L_ v7,24,s55,0
Mvaa 32,180,136
S_ s55,s71,133120
ds_read2st64_b32 v[104:105], v5 offset0:4 offset1:5
Mvaa 32,184,176
S_ m0,s51,13312
ds_read2st64_b32 v[106:107], v5 offset0:6 offset1:7
Mvaa 32,188,216
ds_read2st64_b32 v[112:113], v5 offset0:8 offset1:9
Mvaa 52,220,216
s_nop 0
Ta 180,%0,36864
Ta 182,%0,38912
Mvaa 52,196,176
L_ v7,24,s55,0
Mvaa 52,192,136
W_ 5
Ta 184,%0,40960
Ta 186,%0,43008
Mvaa 48,220,212
S_ s55,s71,143360
ds_read2st64_b32 v[114:115], v5 offset0:10 offset1:11
A_ v96,v16,v96
Mvaa 48,196,172
S_ m0,s51,14336
Ta 188,%0,45056
Ta 190,%0,47104
Mvaa 48,192,132
L_ v7,24,s55,0
Mvaa 44,220,208
S_ s55,s71,153600
ds_read2st64_b32 v[120:121], v5 offset0:12 offset1:13
A_ v97,v17,v97
Mvaa 44,196,168
S_ m0,s51,15360
ds_read2st64_b32 v[122:123], v5 offset0:14 offset1:15
A_ v98,v18,v98
Mvaa 44,192,128
ds_read2st64_b32 v[128:129], v5 offset0:16 offset1:17
A_ v99,v19,v99
Mvaa 40,220,204
L_ v7,24,s55,0
Mvaa 40,196,164
W_ 11
ds_read2st64_b32 v[130:131], v5 offset0:18 offset1:19
A_ v104,v20,v104
Mvaa 40,192,124
S_ s55,s71,163840
ds_read2st64_b32 v[132:133], v5 offset0:36 offset1:37
A_ v105,v21,v105
Mvaa 36,220,200
S_ m0,s51,16384
ds_read2st64_b32 v[134:135], v5 offset0:38 offset1:39
A_ v106,v22,v106
Mvaa 36,196,160
L_ v7,24,s55,0
Mvaa 36,192,120
W_ 6
ds_read2st64_b32 v[124:125], v5 offset0:32 offset1:33
A_ v107,v23,v107
Zvav 56,180,136
Ta 192,v1,36864
Ta 194,v1,38912
Mvav 56,184,156
S_ s55,s71,174080
ds_read2st64_b32 v[126:127], v5 offset0:34 offset1:35
A_ v112,v24,v112
Mvav 56,188,176
S_ m0,s51,17408
Ta 196,v1,40960
Ta 198,v1,43008
Zvav 60,180,140
L_ v7,24,s55,0
Mvav 60,184,160
S_ s79,s6,1
Ta 220,v1,45056
Ta 222,v1,47104
Mvav 60,188,180
s_and_b32 s79, s79, 7
ds_read2st64_b32 v[116:117], v5 offset0:28 offset1:29
A_ v113,v25,v113
Zvav 64,180,144
s_lshl_b32 s79, s79, 7
ds_read2st64_b32 v[118:119], v5 offset0:30 offset1:31
A_ v114,v26,v114
Mvav 64,184,164
S_ s71,s72,s79
ds_read2st64_b32 v[108:109], v5 offset0:24 offset1:25
A_ v115,v27,v115
Mvav 64,188,184
W_ 14
ds_read2st64_b32 v[110:111], v5 offset0:26 offset1:27
A_ v120,v28,v120
Zvav 68,180,148
S_ s55,s71,491520
ds_read2st64_b32 v[100:101], v5 offset0:20 offset1:21
A_ v121,v29,v121
Mvav 68,184,168
S_ m0,s51,49152
ds_read2st64_b32 v[102:103], v5 offset0:22 offset1:23
A_ v122,v30,v122
Mvav 68,188,188
L_ v7,24,s55,0
Zvav 72,180,152
S_ s55,s71,501760
A_ v123,v31,v123
R_ v12,a248
Mvav 72,184,172
W_ 14
A_ v128,v32,v128
A_ v129,v33,v129
Mvav 72,188,192
W_ 6
A_ v130,v34,v130
A_ v131,v35,v131
Zvav 92,220,192
Ta 180,v2,36864
Ta 182,v2,38912
Mvav 92,196,172
S_ m0,s51,50176
A_ v132,v52,v132
A_ v133,v53,v133
Mvav 92,192,152
s_nop 0
Ta 184,v2,40960
Ta 186,v2,43008
Zvav 88,220,188
L_ v7,24,s55,0
Mvav 88,196,168
S_ s55,s71,512000
Ta 188,v2,45056
Ta 190,v2,47104
Mvav 88,192,148
S_ m0,s51,51200
A_ v134,v54,v134
A_ v135,v55,v135
Zvav 84,220,184
A_ v124,v48,v124
A_ v125,v49,v125
Mvav 84,196,164
L_ v7,24,s55,0
Mvav 84,192,144
S_ s55,s71,522240
A_ v126,v50,v126
A_ v127,v51,v127
Zvav 80,220,180
W_ 10
A_ v116,v44,v116
A_ v117,v45,v117
Mvav 80,196,160
S_ m0,s51,52224
A_ v118,v46,v118
A_ v119,v47,v119
Mvav 80,192,140
L_ v7,24,s55,0
Zvav 76,220,176
W_ 8
A_ v108,v40,v108
A_ v109,v41,v109
Mvav 76,196,156
S_ s55,s71,532480
A_ v110,v42,v110
A_ v111,v43,v111
Mvav 76,192,136
W_ 0
A_ v100,v36,v100
Zvav 16,180,136
S_ m0,s51,53248
Ta 192,v3,36864
Ta 194,v3,38912
Mvav 16,184,156
L_ v7,24,s55,0
Mvav 16,188,176
S_ s55,s71,542720
Ta 196,v3,40960
Ta 198,v3,43008
Zvav 20,180,140
S_ m0,s51,54272
A_ v101,v37,v101
A_ v102,v38,v102
Mvav 20,184,160
S_ s85,s6,3
Ta 220,v3,45056
Ta 222,v3,47104
Mvav 20,188,180
L_ v7,24,s55,0
Zvav 24,180,144
s_and_b32 s85, s85, 7
A_ v103,v39,v103
C_ 131,130,131
Mvav 24,184,164
s_lshl_b32 s85, s85, 7
C_ 130,128,129
C_ 132,132,133
Mvav 24,188,184
S_ s85,s85,s87
C_ 133,134,135
v_min_u32 v12, 240, v12
Zvav 28,180,148
buffer_store_dwordx4 v[130:133], v6, s[28:31], s73 offen offset:0 sc1
Mvav 28,184,168
s_sub_u32 s73, s73, 4096
v_lshlrev_b32 v12, 3, v12
Mvav 28,188,188
C_ 123,122,123
C_ 122,120,121
Zvav 32,180,152
C_ 124,124,125
C_ 125,126,127
Mvav 32,184,172
buffer_store_dwordx4 v[122:125], v6, s[28:31], s73 offen offset:3072 sc1
Mvav 32,188,192
s_waitcnt vmcnt(21) lgkmcnt(0)
s_barrier
C_ 115,114,115
Zvav 52,220,192
S_ s55,s71,614400
Ta 180,%0,24576
Ta 182,%0,26624
Mvav 52,196,172
S_ m0,s51,36864
C_ 114,112,113
C_ 116,116,117
Mvav 52,192,152
s_nop 0
Ta 184,%0,28672
Ta 186,%0,30720
Zvav 48,220,188
L_ v7,24,s55,0
Mvav 48,196,168
S_ s55,s71,624640
Ta 188,%0,32768
Ta 190,%0,34816
Mvav 48,192,148
S_ m0,s51,37888
C_ 117,118,119
Zvav 44,220,184
Mvav 44,196,164
buffer_store_dwordx4 v[114:117], v6, s[28:31], s73 offen offset:2048 sc1
Mvav 44,192,144
C_ 107,106,107
C_ 106,104,105
Zvav 40,220,180
C_ 108,108,109
C_ 109,110,111
Mvav 40,196,160
Mvav 40,192,140
buffer_store_dwordx4 v[106:109], v6, s[28:31], s73 offen offset:1024 sc1
Zvav 36,220,176
C_ 99,98,99
C_ 98,96,97
Mvav 36,196,156
C_ 100,100,101
C_ 101,102,103
Mvav 36,192,136
W_ 0
Mvav 56,180,196
s_nop 0
Ta 192,v1,24576
Ta 194,v1,26624
Mvav 56,184,216
buffer_store_dwordx4 v[98:101], v6, s[28:31], s73 offen offset:0 sc1
Mvav 56,188,236
s_nop 0
Ta 196,v1,28672
Ta 198,v1,30720
Mvav 60,180,200
Mvav 60,184,220
s_nop 0
Ta 220,v1,32768
Ta 222,v1,34816
Mvav 60,188,240
L_ v7,24,s55,0
Mvav 64,180,204
S_ s55,s71,634880
Mvav 64,184,224
S_ m0,s51,38912
Mvav 64,188,244
Mvav 68,180,208
L_ v7,24,s55,0
Mvav 68,184,228
S_ s55,s71,645120
Mvav 68,188,248
S_ m0,s51,39936
Mvav 72,180,212
Mvav 72,184,232
L_ v7,24,s55,0
Mvav 72,188,252
W_ 0
S_ s55,s71,655360
Mvav 92,220,252
S_ m0,s51,40960
Ta 180,v2,24576
Ta 182,v2,26624
Mvav 92,196,232
Mvav 92,192,212
s_nop 0
Ta 184,v2,28672
Ta 186,v2,30720
Mvav 88,220,248
L_ v7,24,s55,0
Mvav 88,196,228
S_ s55,s71,665600
Ta 188,v2,32768
Ta 190,v2,34816
Mvav 88,192,208
S_ m0,s51,41984
Mvav 84,220,244
Mvav 84,196,224
L_ v7,24,s55,0
Mvav 84,192,204
Mvav 80,220,240
Mvav 80,196,220
Mvav 80,192,200
buffer_load_dword a74, v12, s[44:47], s66 offen
Mvav 76,220,236
Mvav 76,196,216
Mvav 76,192,196
W_ 0
Mvav 16,180,196
s_nop 0
Ta 192,v3,24576
Ta 194,v3,26624
Mvav 16,184,216
buffer_load_dword a74, v4, s[24:27], s85 offen
Mvav 16,188,236
s_nop 0
Ta 196,v3,28672
Ta 198,v3,30720
Mvav 20,180,200
Mvav 20,184,220
s_nop 0
Ta 220,v3,32768
Ta 222,v3,34816
Mvav 20,188,240
Mvav 24,180,204
Mvav 24,184,224
Mvav 24,188,244
Mvav 28,180,208
Mvav 28,184,228
Mvav 28,188,248
Mvav 32,180,212
Mvav 32,184,232
Mvav 32,188,252
s_waitcnt vmcnt(25) lgkmcnt(0)
s_barrier
Mvav 52,220,252
S_ s55,s71,0
Ta 180,%0,0
Ta 182,%0,2048
Mvav 52,196,232
S_ m0,s51,24576
Mvav 52,192,212
s_nop 0
Ta 184,%0,4096
Ta 186,%0,6144
Mvav 48,220,248
L_ v7,24,s55,0
Mvav 48,196,228
S_ s55,s71,10240
Ta 188,%0,8192
Ta 190,%0,10240
Mvav 48,192,208
S_ m0,s51,25600
Mvav 44,220,244
Mvav 44,196,224
L_ v7,24,s55,0
Mvav 44,192,204
S_ s55,s71,20480
Mvav 40,220,240
S_ m0,s51,26624
Mvav 40,196,220
Mvav 40,192,200
L_ v7,24,s55,0
Mvav 36,220,236
S_ s55,s71,30720
Mvav 36,196,216
S_ m0,s51,27648
Mvav 36,192,196
W_ 0
Mvaa 56,180,0
s_nop 0
Ta 192,v1,0
Ta 194,v1,2048
Mvaa 56,184,40
L_ v7,24,s55,0
Mvaa 56,188,80
S_ s55,s71,40960
Ta 196,v1,4096
Ta 198,v1,6144
Mvaa 60,180,4
S_ m0,s51,28672
Mvaa 60,184,44
s_nop 0
Ta 220,v1,8192
Ta 222,v1,10240
Mvaa 60,188,84
L_ v7,24,s55,0
Mvaa 64,180,8
S_ s55,s71,51200
Mvaa 64,184,48
S_ m0,s51,29696
Mvaa 64,188,88
Mvaa 68,180,12
L_ v7,24,s55,0
Mvaa 68,184,52
Mvaa 68,188,240
Mvaa 72,180,16
Mvaa 72,184,56
Mvaa 72,188,244
W_ 0
Mvaa 92,220,244
.Lpad_o_2:
s_cbranch_execz .Lpad_o_6
Ta 180,v2,0
Ta 182,v2,2048
Mvaa 92,196,56
Mvaa 92,192,16
s_nop 0
Ta 184,v2,4096
Ta 186,v2,6144
Mvaa 88,220,240
Mvaa 88,196,52
s_nop 0
Ta 188,v2,8192
Ta 190,v2,10240
Mvaa 88,192,12
Mvaa 84,220,88
Mvaa 84,196,48
Mvaa 84,192,8
Mvaa 80,220,84
Mvaa 80,196,44
Mvaa 80,192,4
Mvaa 76,220,80
Mvaa 76,196,40
Mvaa 76,192,0
W_ 0
Mvaa 16,180,0
s_nop 0
Ta 192,v3,0
Ta 194,v3,2048
Mvaa 16,184,40
Mvaa 16,188,80
s_nop 0
Ta 196,v3,4096
Ta 198,v3,6144
Mvaa 20,180,4
Mvaa 20,184,44
s_nop 0
Ta 220,v3,8192
Ta 222,v3,10240
Mvaa 20,188,84
Mvaa 24,180,8
Mvaa 24,184,48
Mvaa 24,188,88
Mvaa 28,180,12
Mvaa 28,184,52
Mvaa 28,188,240
Mvaa 32,180,16
Mvaa 32,184,56
Mvaa 32,188,244
s_waitcnt vmcnt(25) lgkmcnt(0)
s_barrier
Mvaa 52,220,244
s_nop 0
Ta 180,%0,12288
Ta 182,%0,14336
Mvaa 52,196,56
Mvaa 52,192,16
s_nop 0
Ta 184,%0,16384
Ta 186,%0,18432
Mvaa 48,220,240
Mvaa 48,196,52
s_nop 0
Ta 188,%0,20480
Ta 190,%0,22528
Mvaa 48,192,12
Mvaa 44,220,88
Mvaa 44,196,48
Mvaa 44,192,8
Mvaa 40,220,84
Mvaa 40,196,44
Mvaa 40,192,4
Mvaa 36,220,80
Mvaa 36,196,40
Mvaa 36,192,0
W_ 0
Mvaa 56,180,120
S_ s73,s78,184320
Ta 192,v1,12288
Ta 194,v1,14336
Mvaa 56,184,160
S_ s73,s73,4096
Mvaa 56,188,200
s_nop 0
Ta 196,v1,16384
Ta 198,v1,18432
Mvaa 60,180,124
s_mov_b32 m0, s52
Mvaa 60,184,164
s_nop 0
Ta 220,v1,20480
Ta 222,v1,22528
Mvaa 60,188,204
s_nop 0
Y_ 56,0
Y_ 57,256
Mvaa 64,180,128
s_nop 0
Y_ 58,512
Y_ 59,768
Mvaa 64,184,168
Mvaa 64,188,208
s_nop 0
Y_ 60,1024
Y_ 61,1280
Mvaa 68,180,132
s_nop 0
Y_ 62,1536
Y_ 63,1792
Mvaa 68,184,172
Mvaa 68,188,212
s_nop 0
Y_ 64,2048
Y_ 65,2304
Mvaa 72,180,136
s_nop 0
Y_ 66,2560
Y_ 67,2816
Mvaa 72,184,176
Mvaa 72,188,216
W_ 12
Y_ 68,3072
Y_ 69,3328
Mvaa 92,220,216
s_nop 0
)A"
R"A(Ta 180,v2,12288
Ta 182,v2,14336
Mvaa 92,196,176
s_nop 0
Y_ 70,3584
Y_ 71,3840
Mvaa 92,192,136
s_nop 0
Ta 184,v2,16384
Ta 186,v2,18432
Mvaa 88,220,212
s_nop 0
Y_ 72,4096
Y_ 73,4352
Mvaa 88,196,172
s_nop 0
Ta 188,v2,20480
Ta 190,v2,22528
Mvaa 88,192,132
s_nop 0
Y_ 74,4608
Y_ 75,4864
Mvaa 84,220,208
s_nop 0
Y_ 92,9216
Y_ 93,9472
Mvaa 84,196,168
s_nop 0
Y_ 94,9728
Y_ 95,9984
Mvaa 84,192,128
s_nop 0
Y_ 88,8192
Y_ 89,8448
Mvaa 80,220,204
s_nop 0
Y_ 90,8704
Y_ 91,8960
Mvaa 80,196,164
Mvaa 80,192,124
s_nop 0
Y_ 84,7168
Y_ 85,7424
Mvaa 76,220,200
s_nop 0
Y_ 86,7680
Y_ 87,7936
Mvaa 76,196,160
Mvaa 76,192,120
W_ 14
Y_ 80,6144
Y_ 81,6400
Mvaa 16,180,120
s_nop 0
Ta 192,v3,12288
Ta 194,v3,14336
Mvaa 16,184,160
s_nop 0
Y_ 82,6656
Y_ 83,6912
Mvaa 16,188,200
s_nop 0
Ta 196,v3,16384
Ta 198,v3,18432
Mvaa 20,180,124
s_nop 0
Y_ 76,5120
Y_ 77,5376
Mvaa 20,184,164
s_nop 0
Ta 220,v3,20480
Ta 222,v3,22528
Mvaa 20,188,204
s_nop 0
Y_ 78,5632
Y_ 79,5888
Mvaa 24,180,128
Mvaa 24,184,168
Mvaa 24,188,208
s_waitcnt vmcnt(19) lgkmcnt(0)
s_barrier
Mvaa 28,180,132
S_ s55,s71,122880
ds_read2st64_b32 v[96:97], v5 offset0:0 offset1:1
Mvaa 28,184,172
S_ m0,s51,0
ds_read2st64_b32 v[98:99], v5 offset0:2 offset1:3
Mvaa 28,188,212
L_ v7,24,s55,0
Mvaa 32,180,136
S_ s55,s71,133120
ds_read2st64_b32 v[104:105], v5 offset0:4 offset1:5
Mvaa 32,184,176
S_ m0,s51,1024
ds_read2st64_b32 v[106:107], v5 offset0:6 offset1:7
Mvaa 32,188,216
ds_read2st64_b32 v[112:113], v5 offset0:8 offset1:9
Mvaa 52,220,216
s_nop 0
Ta 180,%0,49152
Ta 182,%0,51200
Mvaa 52,196,176
L_ v7,24,s55,0
Mvaa 52,192,136
W_ 5
Ta 184,%0,53248
Ta 186,%0,55296
Mvaa 48,220,212
S_ s55,s71,143360
ds_read2st64_b32 v[114:115], v5 offset0:10 offset1:11
A_ v96,v16,v96
Mvaa 48,196,172
S_ m0,s51,2048
Ta 188,%0,57344
Ta 190,%0,59392
Mvaa 48,192,132
L_ v7,24,s55,0
Mvaa 44,220,208
S_ s55,s71,153600
ds_read2st64_b32 v[120:121], v5 offset0:12 offset1:13
A_ v97,v17,v97
Mvaa 44,196,168
S_ m0,s51,3072
ds_read2st64_b32 v[122:123], v5 offset0:14 offset1:15
A_ v98,v18,v98
Mvaa 44,192,128
ds_read2st64_b32 v[128:129], v5 offset0:16 offset1:17
A_ v99,v19,v99
Mvaa 40,220,204
L_ v7,24,s55,0
Mvaa 40,196,164
W_ 11
ds_read2st64_b32 v[130:131], v5 offset0:18 offset1:19
A_ v104,v20,v104
Mvaa 40,192,124
S_ s55,s71,163840
ds_read2st64_b32 v[132:133], v5 offset0:36 offset1:37
A_ v105,v21,v105
Mvaa 36,220,200
S_ m0,s51,4096
ds_read2st64_b32 v[134:135], v5 offset0:38 offset1:39
A_ v106,v22,v106
Mvaa 36,196,160
L_ v7,24,s55,0
Mvaa 36,192,120
W_ 6
ds_read2st64_b32 v[124:125], v5 offset0:32 offset1:33
A_ v107,v23,v107
Zvav 56,180,136
Ta 192,v1,49152
Ta 194,v1,51200
Mvav 56,184,156
S_ s55,s71,174080
ds_read2st64_b32 v[126:127], v5 offset0:34 offset1:35
A_ v112,v24,v112
Mvav 56,188,176
S_ m0,s51,5120
Ta 196,v1,53248
Ta 198,v1,55296
Zvav 60,180,140
L_ v7,24,s55,0
Mvav 60,184,160
S_ s79,s6,2
Ta 220,v1,57344
Ta 222,v1,59392
Mvav 60,188,180
s_and_b32 s79, s79, 7
ds_read2st64_b32 v[116:117], v5 offset0:28 offset1:29
A_ v113,v25,v113
Zvav 64,180,144
s_lshl_b32 s79, s79, 7
ds_read2st64_b32 v[118:119], v5 offset0:30 offset1:31
A_ v114,v26,v114
Mvav 64,184,164
S_ s71,s72,s79
ds_read2st64_b32 v[108:109], v5 offset0:24 offset1:25
A_ v115,v27,v115
Mvav 64,188,184
W_ 14
ds_read2st64_b32 v[110:111], v5 offset0:26 offset1:27
A_ v120,v28,v120
Zvav 68,180,148
S_ s55,s71,491520
ds_read2st64_b32 v[100:101], v5 offset0:20 offset1:21
A_ v121,v29,v121
Mvav 68,184,168
S_ m0,s51,12288
ds_read2st64_b32 v[102:103], v5 offset0:22 offset1:23
A_ v122,v30,v122
Mvav 68,188,188
L_ v7,24,s55,0
Zvav 72,180,152
S_ s55,s71,501760
A_ v123,v31,v123
Mvav 72,184,172
W_ 14
A_ v128,v32,v128
A_ v129,v33,v129
Mvav 72,188,192
W_ 6
A_ v130,v34,v130
A_ v131,v35,v131
Zvav 92,220,192
Ta 180,v2,49152
Ta 182,v2,51200
Mvav 92,196,172
S_ m0,s51,13312
A_ v132,v52,v132
A_ v133,v53,v133
Mvav 92,192,152
s_nop 0
Ta 184,v2,53248
Ta 186,v2,55296
Zvav 88,220,188
L_ v7,24,s55,0
Mvav 88,196,168
S_ s55,s71,512000
Ta 188,v2,57344
Ta 190,v2,59392
Mvav 88,192,148
S_ m0,s51,14336
A_ v134,v54,v134
A_ v135,v55,v135
Zvav 84,220,184
A_ v124,v48,v124
A_ v125,v49,v125
Mvav 84,196,164
L_ v7,24,s55,0
Mvav 84,192,144
S_ s55,s71,522240
A_ v126,v50,v126
A_ v127,v51,v127
Zvav 80,220,180
W_ 10
A_ v116,v44,v116
A_ v117,v45,v117
Mvav 80,196,160
S_ m0,s51,15360
A_ v118,v46,v118
A_ v119,v47,v119
Mvav 80,192,140
L_ v7,24,s55,0
Zvav 76,220,176
W_ 8
A_ v108,v40,v108
A_ v109,v41,v109
Mvav 76,196,156
S_ s55,s71,532480
A_ v110,v42,v110
A_ v111,v43,v111
Mvav 76,192,136
W_ 0
A_ v100,v36,v100
Zvav 16,180,136
S_ m0,s51,16384
Ta 192,v3,49152
Ta 194,v3,51200
Mvav 16,184,156
L_ v7,24,s55,0
Mvav 16,188,176
S_ s55,s71,542720
Ta 196,v3,53248
Ta 198,v3,55296
Zvav 20,180,140
S_ m0,s51,17408
A_ v101,v37,v101
A_ v102,v38,v102
Mvav 20,184,160
S_ s85,s6,4
Ta 220,v3,57344
Ta 222,v3,59392
Mvav 20,188,180
L_ v7,24,s55,0
Zvav 24,180,144
s_and_b32 s85, s85, 7
A_ v103,v39,v103
C_ 131,130,131
Mvav 24,184,164
s_lshl_b32 s85, s85, 7
C_ 130,128,129
C_ 132,132,133
Mvav 24,188,184
S_ s85,s85,s87
C_ 133,134,135
Zvav 28,180,148
buffer_store_dwordx4 v[130:133], v6, s[28:31], s73 offen offset:0 sc1
Mvav 28,184,168
s_sub_u32 s73, s73, 4096
Mvav 28,188,188
C_ 123,122,123
C_ 122,120,121
Zvav 32,180,152
C_ 124,124,125
C_ 125,126,127
Mvav 32,184,172
buffer_store_dwordx4 v[122:125], v6, s[28:31], s73 offen offset:3072 sc1
Mvav 32,188,192
s_waitcnt vmcnt(22) lgkmcnt(0)
s_barrier
C_ 115,114,115
Zvav 52,220,192
S_ s55,s71,614400
Ta 180,%0,36864
Ta 182,%0,38912
Mvav 52,196,172
S_ m0,s51,49152
C_ 114,112,113
C_ 116,116,117
Mvav 52,192,152
s_nop 0
Ta 184,%0,40960
Ta 186,%0,43008
Zvav 48,220,188
L_ v7,24,s55,0
Mvav 48,196,168
S_ s55,s71,624640
Ta 188,%0,45056
Ta 190,%0,47104
Mvav 48,192,148
S_ m0,s51,50176
C_ 117,118,119
Zvav 44,220,184
Mvav 44,196,164
buffer_store_dwordx4 v[114:117], v6, s[28:31], s73 offen offset:2048 sc1
Mvav 44,192,144
C_ 107,106,107
C_ 106,104,105
Zvav 40,220,180
C_ 108,108,109
C_ 109,110,111
Mvav 40,196,160
Mvav 40,192,140
buffer_store_dwordx4 v[106:109], v6, s[28:31], s73 offen offset:1024 sc1
Zvav 36,220,176
C_ 99,98,99
C_ 98,96,97
Mvav 36,196,156
C_ 100,100,101
C_ 101,102,103
Mvav 36,192,136
W_ 0
Mvav 56,180,196
s_nop 0
Ta 192,v1,36864
Ta 194,v1,38912
Mvav 56,184,216
buffer_store_dwordx4 v[98:101], v6, s[28:31], s73 offen offset:0 sc1
Mvav 56,188,236
s_nop 0
Ta 196,v1,40960
Ta 198,v1,43008
Mvav 60,180,200
X_ a20,-1
X_ a21,-1
Mvav 60,184,220
s_nop 0
Ta 220,v1,45056
Ta 222,v1,47104
Mvav 60,188,240
L_ v7,24,s55,0
Mvav 64,180,204
S_ s55,s71,634880
X_ a22,-1
X_ a23,-1
Mvav 64,184,224
S_ s81,s6,7
S_ m0,s51,51200
Mvav 64,188,244
s_and_b32 s81, s81, 7
Mvav 68,180,208
L_ v7,24,s55,0
Mvav 68,184,228
s_mul_i32 s81, s81, 20480
S_ s55,s71,645120
Mvav 68,188,248
S_ s81,s81,s78
S_ m0,s51,52224
Mvav 72,180,212
Mvav 72,184,232
buffer_store_dwordx4 a[20:23], v6, s[28:31], s81 offen offset:0 sc1
Mvav 72,188,252
W_ 0
Mvav 92,220,252
s_nop 0
Ta 180,v2,36864
Ta 182,v2,38912
Mvav 92,196,232
Mvav 92,192,212
s_nop 0
Ta 184,v2,40960
Ta 186,v2,43008
Mvav 88,220,248
buffer_store_dwordx4 a[20:23], v6, s[28:31], s81 offen offset:1024 sc1
Mvav 88,196,228
s_nop 0
Ta 188,v2,45056
Ta 190,v2,47104
Mvav 88,192,208
Mvav 84,220,244
Mvav 84,196,224
buffer_store_dwordx4 a[20:23], v6, s[28:31], s81 offen offset:2048 sc1
Mvav 84,192,204
Mvav 80,220,240
Mvav 80,196,220
Mvav 80,192,200
buffer_store_dwordx4 a[20:23], v6, s[28:31], s81 offen offset:3072 sc1
Mvav 76,220,236
S_ s81,s81,4096
Mvav 76,196,216
Mvav 76,192,196
W_ 0
Mvav 16,180,196
s_nop 0
Ta 192,v3,36864
Ta 194,v3,38912
Mvav 16,184,216
buffer_store_dwordx4 a[20:23], v6, s[28:31], s81 offen offset:0 sc1
Mvav 16,188,236
s_nop 0
Ta 196,v3,40960
Ta 198,v3,43008
Mvav 20,180,200
Mvav 20,184,220
s_nop 0
Ta 220,v3,45056
Ta 222,v3,47104
Mvav 20,188,240
L_ v7,24,s55,0
Mvav 24,180,204
S_ s55,s71,655360
Mvav 24,184,224
S_ m0,s51,53248
Mvav 24,188,244
Mvav 28,180,208
L_ v7,24,s55,0
Mvav 28,184,228
S_ s55,s71,665600
Mvav 28,188,248
S_ m0,s51,54272
Mvav 32,180,212
Mvav 32,184,232
L_ v7,24,s55,0
Mvav 32,188,252
s_waitcnt vmcnt(28) lgkmcnt(0)
s_barrier
Mvav 52,220,252
S_ s55,s71,0
Ta 180,%0,24576
Ta 182,%0,26624
Mvav 52,196,232
S_ m0,s51,36864
Mvav 52,192,212
.Lpad_o_3:
s_cbranch_execz .Lpad_o_7
Ta 184,%0,28672
Ta 186,%0,30720
Mvav 48,220,248
L_ v7,24,s55,0
Mvav 48,196,228
S_ s55,s71,10240
Ta 188,%0,32768
Ta 190,%0,34816
Mvav 48,192,208
S_ m0,s51,37888
Mvav 44,220,244
Mvav 44,196,224
L_ v7,24,s55,0
Mvav 44,192,204
S_ s55,s71,20480
Mvav 40,220,240
S_ m0,s51,38912
Mvav 40,196,220
Mvav 40,192,200
L_ v7,24,s55,0
Mvav 36,220,236
S_ s55,s71,30720
Mvav 36,196,216
S_ m0,s51,39936
Mvav 36,192,196
W_ 0
Mvaa 56,180,0
s_nop 0
Ta 192,v1,24576
Ta 194,v1,26624
Mvaa 56,184,40
L_ v7,24,s55,0
Mvaa 56,188,80
S_ s55,s71,40960
Ta 196,v1,28672
Ta 198,v1,30720
Mvaa 60,180,4
S_ m0,s51,40960
Mvaa 60,184,44
s_nop 0
Ta 220,v1,32768
Ta 222,v1,34816
Mvaa 60,188,84
L_ v7,24,s55,0
Mvaa 64,180,8
S_ s55,s71,51200
Mvaa 64,184,48
S_ m0,s51,41984
Mvaa 64,188,88
Mvaa 68,180,12
L_ v7,24,s55,0
Mvaa 68,184,52
Mvaa 68,188,240
Mvaa 72,180,16
Mvaa 72,184,56
buffer_load_dword a74, v4, s[24:27], s85 offen
Mvaa 72,188,244
W_ 0
Mvaa 92,220,244
s_nop 0
Ta 180,v2,24576
Ta 182,v2,26624
Mvaa 92,196,56
Mvaa 92,192,16
s_nop 0
Ta 184,v2,28672
Ta 186,v2,30720
Mvaa 88,220,240
Mvaa 88,196,52
s_nop 0
Ta 188,v2,32768
Ta 190,v2,34816
Mvaa 88,192,12
Mvaa 84,220,88
Mvaa 84,196,48
Mvaa 84,192,8
Mvaa 80,220,84
Mvaa 80,196,44
Mvaa 80,192,4
Mvaa 76,220,80
Mvaa 76,196,40
Mvaa 76,192,0
W_ 0
Mvaa 16,180,0
s_nop 0
Ta 192,v3,24576
Ta 194,v3,26624
Mvaa 16,184,40
Mvaa 16,188,80
s_nop 0
Ta 196,v3,28672
Ta 198,v3,30720
Mvaa 20,180,4
Mvaa 20,184,44
s_nop 0
Ta 220,v3,32768
Ta 222,v3,34816
Mvaa 20,188,84
Mvaa 24,180,8
Mvaa 24,184,48
Mvaa 24,188,88
Mvaa 28,180,12
Mvaa 28,184,52
Mvaa 28,188,240
Mvaa 32,180,16
Mvaa 32,184,56
Mvaa 32,188,244
s_waitcnt vmcnt(29) lgkmcnt(0)
s_barrier
Mvaa 52,220,244
s_nop 0
Ta 180,%0,0
Ta 182,%0,2048
Mvaa 52,196,56
Mvaa 52,192,16
s_nop 0
Ta 184,%0,4096
Ta 186,%0,6144
Mvaa 48,220,240
Mvaa 48,196,52
s_nop 0
Ta 188,%0,8192
Ta 190,%0,10240
Mvaa 48,192,12
Mvaa 44,220,88
Mvaa 44,196,48
Mvaa 44,192,8
Mvaa 40,220,84
Mvaa 40,196,44
Mvaa 40,192,4
Mvaa 36,220,80
Mvaa 36,196,40
Mvaa 36,192,0
W_ 0
Mvaa 56,180,120
S_ s81,s6,1
Ta 192,v1,0
Ta 194,v1,2048
Mvaa 56,184,160
s_and_b32 s81, s81, 7
Mvaa 56,188,200
s_mul_i32 s81, s81, 20480
Ta 196,v1,4096
Ta 198,v1,6144
Mvaa 60,180,124
s_mov_b32 m0, s52
S_ s73,s81,s78
Mvaa 60,184,164
S_ s73,s73,4096
Ta 220,v1,8192
Ta 222,v1,10240
Mvaa 60,188,204
s_nop 0
Y_ 56,0
Y_ 57,256
Mvaa 64,180,128
s_nop 0
Y_ 58,512
Y_ 59,768
Mvaa 64,184,168
Mvaa 64,188,208
s_nop 0
Y_ 60,1024
Y_ 61,1280
Mvaa 68,180,132
s_nop 0
Y_ 62,1536
Y_ 63,1792
Mvaa 68,184,172
Mvaa 68,188,212
s_nop 0
Y_ 64,2048
Y_ 65,2304
Mvaa 72,180,136
s_nop 0
Y_ 66,2560
Y_ 67,2816
Mvaa 72,184,176
Mvaa 72,188,216
W_ 12
Y_ 68,3072
Y_ 69,3328
Mvaa 92,220,216
s_nop 0
Ta 180,v2,0
Ta 182,v2,2048
Mvaa 92,196,176
s_nop 0
Y_ 70,3584
Y_ 71,3840
Mvaa 92,192,136
s_nop 0
Ta 184,v2,4096
Ta 186,v2,6144
Mvaa 88,220,212
s_nop 0
Y_ 72,4096
Y_ 73,4352
Mvaa 88,196,172
s_nop 0
Ta 188,v2,8192
Ta 190,v2,10240
Mvaa 88,192,132
s_nop 0
Y_ 74,4608
Y_ 75,4864
Mvaa 84,220,208
s_nop 0
Y_ 92,9216
Y_ 93,9472
Mvaa 84,196,168
s_nop 0
Y_ 94,9728
Y_ 95,9984
Mvaa 84,192,128
s_nop 0
Y_ 88,8192
Y_ 89,8448
Mvaa 80,220,204
s_nop 0
Y_ 90,8704
Y_ 91,8960
Mvaa 80,196,164
Mvaa 80,192,124
s_nop 0
Y_ 84,7168
Y_ 85,7424
Mvaa 76,220,200
s_nop 0
Y_ 86,7680
Y_ 87,7936
Mvaa 76,196,160
Mvaa 76,192,120
W_ 14
Y_ 80,6144
Y_ 81,6400
Mvaa 16,180,120
s_nop 0
Ta 192,v3,0
Ta 194,v3,2048
Mvaa 16,184,160
s_nop 0
Y_ 82,6656
Y_ 83,6912
Mvaa 16,188,200
s_nop 0
Ta 196,v3,4096
Ta 198,v3,6144
Mvaa 20,180,124
s_nop 0
Y_ 76,5120
Y_ 77,5376
Mvaa 20,184,164
s_nop 0
Ta 220,v3,8192
Ta 222,v3,10240
Mvaa 20,188,204
s_nop 0
Y_ 78,5632
Y_ 79,5888
Mvaa 24,180,128
Mvaa 24,184,168
Mvaa 24,188,208
s_waitcnt vmcnt(23) lgkmcnt(0)
s_barrier
Mvaa 28,180,132
S_ s55,s71,122880
ds_read2st64_b32 v[96:97], v5 offset0:0 offset1:1
Mvaa 28,184,172
S_ m0,s51,24576
ds_read2st64_b32 v[98:99], v5 offset0:2 offset1:3
Mvaa 28,188,212
L_ v7,24,s55,0
Mvaa 32,180,136
S_ s55,s71,133120
ds_read2st64_b32 v[104:105], v5 offset0:4 offset1:5
Mvaa 32,184,176
S_ m0,s51,25600
ds_read2st64_b32 v[106:107], v5 offset0:6 offset1:7
Mvaa 32,188,216
ds_read2st64_b32 v[112:113], v5 offset0:8 offset1:9
Mvaa 52,220,216
s_nop 0
Ta 180,%0,12288
Ta 182,%0,14336
Mvaa 52,196,176
L_ v7,24,s55,0
Mvaa 52,192,136
W_ 5
Ta 184,%0,16384
Ta 186,%0,18432
Mvaa 48,220,212
S_ s55,s71,143360
ds_read2st64_b32 v[114:115], v5 offset0:10 offset1:11
A_ v96,v16,v96
Mvaa 48,196,172
S_ m0,s51,26624
Ta 188,%0,20480
Ta 190,%0,22528
Mvaa 48,192,132
L_ v7,24,s55,0
Mvaa 44,220,208
S_ s55,s71,153600
ds_read2st64_b32 v[120:121], v5 offset0:12 offset1:13
A_ v97,v17,v97
Mvaa 44,196,168
S_ m0,s51,27648
ds_read2st64_b32 v[122:123], v5 offset0:14 offset1:15
A_ v98,v18,v98
Mvaa 44,192,128
ds_read2st64_b32 v[128:129], v5 offset0:16 offset1:17
A_ v99,v19,v99
Mvaa 40,220,204
L_ v7,24,s55,0
Mvaa 40,196,164
W_ 11
ds_read2st64_b32 v[130:131], v5 offset0:18 offset1:19
A_ v104,v20,v104
Mvaa 40,192,124
S_ s55,s71,163840
ds_read2st64_b32 v[132:133], v5 offset0:36 offset1:37
A_ v105,v21,v105
Mvaa 36,220,200
S_ m0,s51,28672
ds_read2st64_b32 v[134:135], v5 offset0:38 offset1:39
A_ v106,v22,v106
Mvaa 36,196,160
L_ v7,24,s55,0
Mvaa 36,192,120
W_ 6
ds_read2st64_b32 v[124:125], v5 offset0:32 offset1:33
A_ v107,v23,v107
Zvav 56,180,136
Ta 192,v1,12288
Ta 194,v1,14336
Mvav 56,184,156
S_ s55,s71,174080
ds_read2st64_b32 v[126:127], v5 offset0:34 offset1:35
A_ v112,v24,v112
Mvav 56,188,176
S_ m0,s51,29696
Ta 196,v1,16384
Ta 198,v1,18432
Zvav 60,180,140
L_ v7,24,s55,0
Mvav 60,184,160
S_ s79,s6,3
Ta 220,v1,20480
Ta 222,v1,22528
Mvav 60,188,180
s_and_b32 s79, s79, 7
ds_read2st64_b32 v[116:117], v5 offset0:28 offset1:29
A_ v113,v25,v113
Zvav 64,180,144
s_lshl_b32 s79, s79, 7
ds_read2st64_b32 v[118:119], v5 offset0:30 offset1:31
A_ v114,v26,v114
Mvav 64,184,164
S_ s71,s72,s79
ds_read2st64_b32 v[108:109], v5 offset0:24 offset1:25
A_ v115,v27,v115
Mvav 64,188,184
W_ 14
ds_read2st64_b32 v[110:111], v5 offset0:26 offset1:27
A_ v120,v28,v120
Zvav 68,180,148
S_ s55,s71,491520
ds_read2st64_b32 v[100:101], v5 offset0:20 offset1:21
A_ v121,v29,v121
Mvav 68,184,168
S_ m0,s51,0
ds_read2st64_b32 v[102:103], v5 offset0:22 offset1:23
A_ v122,v30,v122
Mvav 68,188,188
L_ v7,24,s55,0
Zvav 72,180,152
S_ s55,s71,501760
A_ v123,v31,v123
Mvav 72,184,172
W_ 14
A_ v128,v32,v128
A_ v129,v33,v129
Mvav 72,188,192
W_ 6
A_ v130,v34,v130
A_ v131,v35,v131
Zvav 92,220,192
Ta 180,v2,12288
Ta 182,v2,14336
Mvav 92,196,172
S_ m0,s51,1024
A_ v132,v52,v132
A_ v133,v53,v133
Mvav 92,192,152
s_nop 0
Ta 184,v2,16384
Ta 186,v2,18432
Zvav 88,220,188
L_ v7,24,s55,0
Mvav 88,196,168
S_ s55,s71,512000
Ta 188,v2,20480
Ta 190,v2,22528
Mvav 88,192,148
S_ m0,s51,2048
A_ v134,v54,v134
A_ v135,v55,v135
Zvav 84,220,184
A_ v124,v48,v124
A_ v125,v49,v125
Mvav 84,196,164
L_ v7,24,s55,0
Mvav 84,192,144
S_ s55,s71,522240
A_ v126,v50,v126
A_ v127,v51,v127
Zvav 80,220,180
W_ 10
A_ v116,v44,v116
A_ v117,v45,v117
Mvav 80,196,160
S_ m0,s51,3072
A_ v118,v46,v118
A_ v119,v47,v119
Mvav 80,192,140
L_ v7,24,s55,0
Zvav 76,220,176
W_ 8
A_ v108,v40,v108
A_ v109,v41,v109
Mvav 76,196,156
S_ s55,s71,532480
A_ v110,v42,v110
A_ v111,v43,v111
Mvav 76,192,136
W_ 0
A_ v100,v36,v100
Zvav 16,180,136
S_ m0,s51,4096
Ta 192,v3,12288
Ta 194,v3,14336
Mvav 16,184,156
L_ v7,24,s55,0
Mvav 16,188,176
S_ s55,s71,542720
Ta 196,v3,16384
Ta 198,v3,18432
Zvav 20,180,140
S_ m0,s51,5120
A_ v101,v37,v101
A_ v102,v38,v102
Mvav 20,184,160
S_ s85,s6,5
Ta 220,v3,20480
Ta 222,v3,22528
Mvav 20,188,180
L_ v7,24,s55,0
Zvav 24,180,144
s_and_b32 s85, s85, 7
A_ v103,v39,v103
C_ 131,130,131
Mvav 24,184,164
s_lshl_b32 s85, s85, 7
C_ 130,128,129
C_ 132,132,133
Mvav 24,188,184
S_ s85,s85,s87
C_ 133,134,135
Zvav 28,180,148
buffer_store_dwordx4 v[130:133], v6, s[28:31], s73 offen offset:0 sc1
Mvav 28,184,168
s_sub_u32 s73, s73, 4096
Mvav 28,188,188
C_ 123,122,123
C_ 122,120,121
Zvav 32,180,152
C_ 124,124,125
C_ 125,126,127
Mvav 32,184,172
buffer_store_dwordx4 v[122:125], v6, s[28:31], s73 offen offset:3072 sc1
Mvav 32,188,192
s_waitcnt vmcnt(21) lgkmcnt(0)
s_barrier
C_ 115,114,115
Zvav 52,220,192
S_ s56,s76,s74
Ta 180,%0,49152
Ta 182,%0,51200
Mvav 52,196,172
S_ s55,s71,614400
C_ 114,112,113
C_ 116,116,117
Mvav 52,192,152
S_ m0,s51,12288
Ta 184,%0,53248
Ta 186,%0,55296
Zvav 48,220,188
buffer_load_dwordx4 a[60:63], v6, s[28:31], s56 offen offset:0 sc1
Mvav 48,196,168
S_ s56,s76,s82
Ta 188,%0,57344
Ta 190,%0,59392
Mvav 48,192,148
C_ 117,118,119
Zvav 44,220,184
Mvav 44,196,164
buffer_load_dwordx4 a[64:67], v6, s[28:31], s56 offen offset:0 sc1
Mvav 44,192,144
Zvav 40,220,180
Mvav 40,196,160
Mvav 40,192,140
buffer_store_dwordx4 v[114:117], v6, s[28:31], s73 offen offset:2048 sc1
Zvav 36,220,176
C_ 107,106,107
C_ 106,104,105
Mvav 36,196,156
C_ 108,108,109
C_ 109,110,111
Mvav 36,192,136
W_ 0
Mvav 56,180,196
s_nop 0
Ta 192,v1,49152
Ta 194,v1,51200
Mvav 56,184,216
buffer_store_dwordx4 v[106:109], v6, s[28:31], s73 offen offset:1024 sc1
Mvav 56,188,236
s_nop 0
Ta 196,v1,53248
Ta 198,v1,55296
Mvav 60,180,200
C_ 99,98,99
C_ 98,96,97
Mvav 60,184,220
s_nop 0
Ta 220,v1,57344
Ta 222,v1,59392
Mvav 60,188,240
L_ v7,24,s55,0
Mvav 64,180,204
S_ s55,s71,624640
C_ 100,100,101
C_ 101,102,103
Mvav 64,184,224
S_ m0,s51,13312
Mvav 64,188,244
Mvav 68,180,208
buffer_store_dwordx4 v[98:101], v6, s[28:31], s73 offen offset:0 sc1
Mvav 68,184,228
Mvav 68,188,248
Mvav 72,180,212
Mvav 72,184,232
L_ v7,24,s55,0
Mvav 72,188,252
W_ 0
S_ s55,s71,634880
Mvav 92,220,252
S_ m0,s51,14336
Ta 180,v2,49152
Ta 182,v2,51200
Mvav 92,196,232
Mvav 92,192,212
s_nop 0
Ta 184,v2,53248
Ta 186,v2,55296
Mvav 88,220,248
L_ v7,24,s55,0
Mvav 88,196,228
S_ s55,s71,645120
Ta 188,v2,57344
Ta 190,v2,59392
Mvav 88,192,208
S_ m0,s51,15360
Mvav 84,220,244
.Lpad_o_4:
s_cbranch_execz .Lpad_o_8
Mvav 84,196,224
L_ v7,24,s55,0
Mvav 84,192,204
S_ s55,s71,655360
Mvav 80,220,240
S_ m0,s51,16384
Mvav 80,196,220
S_ s83,s6,7
Mvav 80,192,200
L_ v7,24,s55,0
Mvav 76,220,236
s_and_b32 s83, s83, 7
S_ s55,s71,665600
Mvav 76,196,216
s_lshl_b32 s83, s83, 2
S_ m0,s51,17408
Mvav 76,192,196
W_ 0
v_cmp_eq_u32_e64 s[36:37], s83, v8
Mvav 16,180,196
s_nop 0
Ta 192,v3,49152
Ta 194,v3,51200
Mvav 16,184,216
L_ v7,24,s55,0
Mvav 16,188,236
s_nop 0
Ta 196,v3,53248
Ta 198,v3,55296
Mvav 20,180,200
v_mov_b32 v10, 0x7fff0000
Mvav 20,184,220
s_nop 0
Ta 220,v3,57344
Ta 222,v3,59392
Mvav 20,188,240
buffer_load_dword a74, v4, s[24:27], s85 offen
Mvav 24,180,204
v_cndmask_b32_e64 v10, v10, v9, s[36:37]
Mvav 24,184,224
Mvav 24,188,244
Mvav 28,180,208
Mvav 28,184,228
Mvav 28,188,248
Mvav 32,180,212
Mvav 32,184,232
Mvav 32,188,252
s_waitcnt vmcnt(27) lgkmcnt(0)
s_barrier
Mvav 52,220,252
S_ s55,s71,0
Ta 180,%0,36864
Ta 182,%0,38912
Mvav 52,196,232
buffer_store_dword a255, v10, s[92:95], 0 offen offset:384 sc1
Mvav 52,192,212
S_ m0,s51,49152
Ta 184,%0,40960
Ta 186,%0,43008
Mvav 48,220,248
Mvav 48,196,228
s_nop 0
Ta 188,%0,45056
Ta 190,%0,47104
Mvav 48,192,208
L_ v7,24,s55,0
Mvav 44,220,244
S_ s55,s71,10240
Mvav 44,196,224
S_ m0,s51,50176
Mvav 44,192,204
Mvav 40,220,240
L_ v7,24,s55,0
Mvav 40,196,220
S_ s55,s71,20480
Mvav 40,192,200
S_ m0,s51,51200
Mvav 36,220,236
Mvav 36,196,216
L_ v7,24,s55,0
Mvav 36,192,196
W_ 0
S_ s55,s71,30720
Mvaa 56,180,0
S_ m0,s51,52224
Ta 192,v1,36864
Ta 194,v1,38912
Mvaa 56,184,40
Mvaa 56,188,80
s_nop 0
Ta 196,v1,40960
Ta 198,v1,43008
Mvaa 60,180,4
L_ v7,24,s55,0
Mvaa 60,184,44
S_ s55,s71,40960
Ta 220,v1,45056
Ta 222,v1,47104
Mvaa 60,188,84
S_ m0,s51,53248
Mvaa 64,180,8
Mvaa 64,184,48
L_ v7,24,s55,0
Mvaa 64,188,88
S_ s55,s71,51200
Mvaa 68,180,12
S_ m0,s51,54272
Mvaa 68,184,52
Mvaa 68,188,240
L_ v7,24,s55,0
Mvaa 72,180,16
Mvaa 72,184,56
Mvaa 72,188,244
W_ 0
Mvaa 92,220,244
s_nop 0
Ta 180,v2,36864
Ta 182,v2,38912
Mvaa 92,196,56
Mvaa 92,192,16
s_nop 0
Ta 184,v2,40960
Ta 186,v2,43008
Mvaa 88,220,240
Mvaa 88,196,52
s_nop 0
Ta 188,v2,45056
Ta 190,v2,47104
Mvaa 88,192,12
Mvaa 84,220,88
Mvaa 84,196,48
Mvaa 84,192,8
Mvaa 80,220,84
Mvaa 80,196,44
Mvaa 80,192,4
Mvaa 76,220,80
Mvaa 76,196,40
Mvaa 76,192,0
W_ 0
Mvaa 16,180,0
s_nop 0
Ta 192,v3,36864
Ta 194,v3,38912
Mvaa 16,184,40
Mvaa 16,188,80
s_nop 0
Ta 196,v3,40960
Ta 198,v3,43008
Mvaa 20,180,4
Mvaa 20,184,44
s_nop 0
Ta 220,v3,45056
Ta 222,v3,47104
Mvaa 20,188,84
Mvaa 24,180,8
Mvaa 24,184,48
Mvaa 24,188,88
Mvaa 28,180,12
Mvaa 28,184,52
Mvaa 28,188,240
Mvaa 32,180,16
Mvaa 32,184,56
Mvaa 32,188,244
s_waitcnt vmcnt(27) lgkmcnt(0)
s_barrier
Mvaa 52,220,244
s_nop 0
Ta 180,%0,24576
Ta 182,%0,26624
Mvaa 52,196,56
Mvaa 52,192,16
s_nop 0
Ta 184,%0,28672
Ta 186,%0,30720
Mvaa 48,220,240
Mvaa 48,196,52
s_nop 0
Ta 188,%0,32768
Ta 190,%0,34816
Mvaa 48,192,12
Mvaa 44,220,88
Mvaa 44,196,48
Mvaa 44,192,8
Mvaa 40,220,84
Mvaa 40,196,44
Mvaa 40,192,4
Mvaa 36,220,80
Mvaa 36,196,40
Mvaa 36,192,0
W_ 0
Mvaa 56,180,120
S_ s81,s6,2
Ta 192,v1,24576
Ta 194,v1,26624
Mvaa 56,184,160
s_and_b32 s81, s81, 7
Mvaa 56,188,200
s_mul_i32 s81, s81, 20480
Ta 196,v1,28672
Ta 198,v1,30720
Mvaa 60,180,124
s_mov_b32 m0, s52
S_ s73,s81,s78
Mvaa 60,184,164
S_ s73,s73,4096
Ta 220,v1,32768
Ta 222,v1,34816
Mvaa 60,188,204
s_nop 0
Y_ 56,0
Y_ 57,256
Mvaa 64,180,128
s_nop 0
Y_ 58,512
Y_ 59,768
Mvaa 64,184,168
Mvaa 64,188,208
s_nop 0
Y_ 60,1024
Y_ 61,1280
Mvaa 68,180,132
s_nop 0
Y_ 62,1536
Y_ 63,1792
Mvaa 68,184,172
Mvaa 68,188,212
s_nop 0
Y_ 64,2048
Y_ 65,2304
Mvaa 72,180,136
s_nop 0
Y_ 66,2560
Y_ 67,2816
Mvaa 72,184,176
Mvaa 72,188,216
W_ 12
Y_ 68,3072
Y_ 69,3328
Mvaa 92,220,216
s_nop 0
Ta 180,v2,24576
Ta 182,v2,26624
Mvaa 92,196,176
s_nop 0
Y_ 70,3584
Y_ 71,3840
Mvaa 92,192,136
s_nop 0
Ta 184,v2,28672
Ta 186,v2,30720
Mvaa 88,220,212
s_nop 0
Y_ 72,4096
Y_ 73,4352
Mvaa 88,196,172
S_ s83,s6,1
Ta 188,v2,32768
Ta 190,v2,34816
Mvaa 88,192,132
s_and_b32 s83, s83, 7
Y_ 74,4608
Y_ 75,4864
Mvaa 84,220,208
s_lshl_b32 s83, s83, 2
Y_ 92,9216
Y_ 93,9472
Mvaa 84,196,168
s_nop 0
Y_ 94,9728
Y_ 95,9984
Mvaa 84,192,128
s_nop 0
Y_ 88,8192
Y_ 89,8448
Mvaa 80,220,204
s_nop 0
Y_ 90,8704
Y_ 91,8960
Mvaa 80,196,164
v_cmp_eq_u32_e64 s[36:37], s83, v8
Mvaa 80,192,124
s_nop 0
Y_ 84,7168
Y_ 85,7424
Mvaa 76,220,200
s_nop 0
Y_ 86,7680
Y_ 87,7936
Mvaa 76,196,160
v_mov_b32 v10, 0x7fff0000
Mvaa 76,192,120
W_ 14
Y_ 80,6144
Y_ 81,6400
Mvaa 16,180,120
s_nop 0
Ta 192,v3,24576
Ta 194,v3,26624
Mvaa 16,184,160
s_nop 0
Y_ 82,6656
Y_ 83,6912
Mvaa 16,188,200
s_nop 0
Ta 196,v3,28672
Ta 198,v3,30720
Mvaa 20,180,124
s_nop 0
Y_ 76,5120
Y_ 77,5376
Mvaa 20,184,164
s_nop 0
Ta 220,v3,32768
Ta 222,v3,34816
Mvaa 20,188,204
s_nop 0
Y_ 78,5632
Y_ 79,5888
Mvaa 24,180,128
v_cndmask_b32_e64 v10, v10, v9, s[36:37]
Mvaa 24,184,168
Mvaa 24,188,208
s_waitcnt vmcnt(13) lgkmcnt(0)
s_barrier
Mvaa 28,180,132
buffer_store_dword a255, v10, s[92:95], 0 offen offset:384 sc1
Mvaa 28,184,172
S_ s55,s71,122880
ds_read2st64_b32 v[96:97], v5 offset0:0 offset1:1
Mvaa 28,188,212
S_ m0,s51,36864
ds_read2st64_b32 v[98:99], v5 offset0:2 offset1:3
Mvaa 32,180,136
ds_read2st64_b32 v[104:105], v5 offset0:4 offset1:5
Mvaa 32,184,176
L_ v7,24,s55,0
Mvaa 32,188,216
S_ s55,s71,133120
ds_read2st64_b32 v[106:107], v5 offset0:6 offset1:7
Mvaa 52,220,216
S_ m0,s51,37888
Ta 180,%0,0
Ta 182,%0,2048
Mvaa 52,196,176
ds_read2st64_b32 v[112:113], v5 offset0:8 offset1:9
Mvaa 52,192,136
W_ 5
Ta 184,%0,4096
Ta 186,%0,6144
Mvaa 48,220,212
L_ v7,24,s55,0
Mvaa 48,196,172
S_ s55,s71,143360
Ta 188,%0,8192
Ta 190,%0,10240
Mvaa 48,192,132
S_ m0,s51,38912
ds_read2st64_b32 v[114:115], v5 offset0:10 offset1:11
A_ v96,v16,v96
Mvaa 44,220,208
ds_read2st64_b32 v[120:121], v5 offset0:12 offset1:13
A_ v97,v17,v97
Mvaa 44,196,168
L_ v7,24,s55,0
Mvaa 44,192,128
S_ s55,s71,153600
ds_read2st64_b32 v[122:123], v5 offset0:14 offset1:15
A_ v98,v18,v98
Mvaa 40,220,204
S_ m0,s51,39936
ds_read2st64_b32 v[128:129], v5 offset0:16 offset1:17
A_ v99,v19,v99
Mvaa 40,196,164
W_ 11
ds_read2st64_b32 v[130:131], v5 offset0:18 offset1:19
A_ v104,v20,v104
Mvaa 40,192,124
L_ v7,24,s55,0
Mvaa 36,220,200
S_ s55,s71,163840
ds_read2st64_b32 v[132:133], v5 offset0:36 offset1:37
A_ v105,v21,v105
Mvaa 36,196,160
S_ m0,s51,40960
ds_read2st64_b32 v[134:135], v5 offset0:38 offset1:39
A_ v106,v22,v106
Mvaa 36,192,120
ds_read2st64_b32 v[124:125], v5 offset0:32 offset1:33
A_ v107,v23,v107
s_waitcnt vmcnt(22)
Zaaa 224,70,60
L_ v7,24,s55,0
Zaaa 228,68,60
W_ 7
ds_read2st64_b32 v[126:127], v5 offset0:34 offset1:35
A_ v112,v24,v112
Zaaa 232,70,64
S_ s55,s71,174080
ds_read2st64_b32 v[116:117], v5 offset0:28 offset1:29
A_ v113,v25,v113
Zaaa 236,68,64
ds_read2st64_b32 v[118:119], v5 offset0:30 offset1:31
A_ v114,v26,v114
Zvav 56,180,136
S_ m0,s51,41984
Ta 192,v1,0
Ta 194,v1,2048
Mvav 56,184,156
L_ v7,24,s55,0
Mvav 56,188,176
S_ s79,s6,4
Ta 196,v1,4096
Ta 198,v1,6144
Zvav 60,180,140
s_and_b32 s79, s79, 7
ds_read2st64_b32 v[108:109], v5 offset0:24 offset1:25
A_ v115,v27,v115
Mvav 60,184,160
W_ 13
Ta 220,v1,8192
Ta 222,v1,10240
Mvav 60,188,180
s_lshl_b32 s79, s79, 7
ds_read2st64_b32 v[110:111], v5 offset0:26 offset1:27
A_ v120,v28,v120
Zvav 64,180,144
S_ s71,s72,s79
ds_read2st64_b32 v[100:101], v5 offset0:20 offset1:21
A_ v121,v29,v121
Mvav 64,184,164
S_ s55,s71,491520
ds_read2st64_b32 v[102:103], v5 offset0:22 offset1:23
A_ v122,v30,v122
Mvav 64,188,184
S_ m0,s51,24576
A_ v123,v31,v123
R_ v12,a248
Zvav 68,180,148
L_ v7,24,s55,0
Mvav 68,184,168
W_ 14
A_ v128,v32,v128
A_ v129,v33,v129
Mvav 68,188,188
S_ s55,s71,501760
A_ v130,v34,v130
A_ v131,v35,v131
Zvav 72,180,152
A_ v132,v52,v132
A_ v133,v53,v133
Mvav 72,184,172
S_ m0,s51,25600
A_ v134,v54,v134
A_ v135,v55,v135
Mvav 72,188,192
W_ 3
A_ v124,v48,v124
Zvav 92,220,192
s_nop 0
Ta 180,v2,0
Ta 182,v2,2048
Mvav 92,196,172
L_ v7,24,s55,0
Mvav 92,192,152
S_ s55,s71,512000
Ta 184,v2,4096
Ta 186,v2,6144
Zvav 88,220,188
S_ m0,s51,26624
A_ v125,v49,v125
A_ v126,v50,v126
Mvav 88,196,168
s_nop 0
Ta 188,v2,8192
Ta 190,v2,10240
Mvav 88,192,148
L_ v7,24,s55,0
Zvav 84,220,184
S_ s55,s71,522240
A_ v127,v51,v127
C_ 131,130,131
Mvav 84,196,164
A_ v116,v44,v116
A_ v117,v45,v117
Mvav 84,192,144
S_ m0,s51,27648
A_ v118,v46,v118
A_ v119,v47,v119
Zvav 80,220,180
L_ v7,24,s55,0
Mvav 80,196,160
W_ 8
A_ v108,v40,v108
A_ v109,v41,v109
Mvav 80,192,140
S_ s55,s71,532480
A_ v110,v42,v110
A_ v111,v43,v111
Zvav 76,220,176
W_ 6
A_ v100,v36,v100
A_ v101,v37,v101
Mvav 76,196,156
S_ m0,s51,28672
A_ v102,v38,v102
A_ v103,v39,v103
Mvav 76,192,136
W_ 0
C_ 130,128,129
C_ 132,132,133
Zvav 16,180,136
S_ s85,s6,6
Ta 192,v3,0
Ta 194,v3,2048
Mvav 16,184,156
L_ v7,24,s55,0
Mvav 16,188,176
S_ s55,s71,542720
Ta 196,v3,4096
Ta 198,v3,6144
Zvav 20,180,140
S_ m0,s51,29696
C_ 133,134,135
v_min_u32 v12, 240, v12
Mvav 20,184,160
s_and_b32 s85, s85, 7
Ta 220,v3,8192
Ta 222,v3,10240
Mvav 20,188,180
buffer_store_dwordx4 v[130:133], v6, s[28:31], s73 offen offset:0 sc1
Zvav 24,180,144
s_sub_u32 s73, s73, 4096
v_lshlrev_b32 v12, 3, v12
s_lshl_b32 s85, s85, 7
Mvav 24,184,164
S_ s85,s85,s87
C_ 123,122,123
C_ 122,120,121
Mvav 24,188,184
C_ 124,124,125
C_ 125,126,127
Zvav 28,180,148
buffer_store_dwordx4 v[122:125], v6, s[28:31], s73 offen offset:3072 sc1
Mvav 28,184,168
C_ 115,114,115
C_ 114,112,113
Mvav 28,188,188
C_ 116,116,117
C_ 117,118,119
Zvav 32,180,152
Mvav 32,184,172
buffer_store_dwordx4 v[114:117], v6, s[28:31], s73 offen offset:2048 sc1
Mvav 32,188,192
s_waitcnt vmcnt(23) lgkmcnt(0)
s_barrier
C_ 107,106,107
Zvav 52,220,192
s_nop 0
Ta 180,%0,12288
Ta 182,%0,14336
Mvav 52,196,172
C_ 106,104,105
C_ 108,108,109
Mvav 52,192,152
s_nop 0
Ta 184,%0,16384
Ta 186,%0,18432
Zvav 48,220,188
L_ v7,24,s55,0
Mvav 48,196,168
S_ s55,s71,614400
Ta 188,%0,20480
Ta 190,%0,22528
Mvav 48,192,148
S_ m0,s51,0
.Lpad_o_5:
s_cbranch_execz .Lpad_o_9
C_ 109,110,111
Zvav 44,220,184
Mvav 44,196,164
buffer_store_dwordx4 v[106:109], v6, s[28:31], s73 offen offset:1024 sc1
Mvav 44,192,144
C_ 99,98,99
C_ 98,96,97
Zvav 40,220,180
C_ 100,100,101
C_ 101,102,103
Mvav 40,196,160
Mvav 40,192,140
buffer_store_dwordx4 v[98:101], v6, s[28:31], s73 offen offset:0 sc1
Zvav 36,220,176
Mvav 36,196,156
Mvav 36,192,136
W_ 0
Mvav 56,180,196
s_nop 0
Ta 192,v1,12288
Ta 194,v1,14336
Mvav 56,184,216
L_ v7,24,s55,0
Mvav 56,188,236
S_ s55,s71,624640
Ta 196,v1,16384
Ta 198,v1,18432
Mvav 60,180,200
S_ m0,s51,1024
Mvav 60,184,220
s_nop 0
Ta 220,v1,20480
Ta 222,v1,22528
Mvav 60,188,240
L_ v7,24,s55,0
Mvav 64,180,204
S_ s55,s71,634880
Mvav 64,184,224
S_ m0,s51,2048
Mvav 64,188,244
Mvav 68,180,208
L_ v7,24,s55,0
Mvav 68,184,228
S_ s55,s71,645120
Mvav 68,188,248
S_ m0,s51,3072
Mvav 72,180,212
Mvav 72,184,232
L_ v7,24,s55,0
Mvav 72,188,252
W_ 0
S_ s55,s71,655360
Mvav 92,220,252
S_ m0,s51,4096
Ta 180,v2,12288
Ta 182,v2,14336
Mvav 92,196,232
Mvav 92,192,212
s_nop 0
Ta 184,v2,16384
Ta 186,v2,18432
Mvav 88,220,248
L_ v7,24,s55,0
Mvav 88,196,228
S_ s55,s71,665600
Ta 188,v2,20480
Ta 190,v2,22528
Mvav 88,192,208
S_ m0,s51,5120
Mvav 84,220,244
Mvav 84,196,224
L_ v7,24,s55,0
Mvav 84,192,204
Mvav 80,220,240
Mvav 80,196,220
Mvav 80,192,200
buffer_load_dword a74, v12, s[44:47], s66 offen
Mvav 76,220,236
Mvav 76,196,216
Mvav 76,192,196
W_ 0
Mvav 16,180,196
s_nop 0
Ta 192,v3,12288
Ta 194,v3,14336
Mvav 16,184,216
buffer_load_dword a74, v4, s[24:27], s85 offen
Mvav 16,188,236
s_nop 0
Ta 196,v3,16384
Ta 198,v3,18432
Mvav 20,180,200
Mvav 20,184,220
s_nop 0
Ta 220,v3,20480
Ta 222,v3,22528
Mvav 20,188,240
Mvav 24,180,204
Mvav 24,184,224
Mvav 24,188,244
Mvav 28,180,208
Mvav 28,184,228
Mvav 28,188,248
Mvav 32,180,212
Mvav 32,184,232
Mvav 32,188,252
s_waitcnt vmcnt(26) lgkmcnt(0)
s_barrier
Mvav 52,220,252
S_ s55,s71,0
Ta 180,%0,49152
Ta 182,%0,51200
Mvav 52,196,232
S_ m0,s51,12288
Mvav 52,192,212
s_nop 0
Ta 184,%0,53248
Ta 186,%0,55296
Mvav 48,220,248
L_ v7,24,s55,0
Mvav 48,196,228
S_ s55,s71,10240
Ta 188,%0,57344
Ta 190,%0,59392
Mvav 48,192,208
S_ m0,s51,13312
Mvav 44,220,244
Mvav 44,196,224
L_ v7,24,s55,0
Mvav 44,192,204
S_ s55,s71,20480
Mvav 40,220,240
S_ m0,s51,14336
Mvav 40,196,220
Mvav 40,192,200
L_ v7,24,s55,0
Mvav 36,220,236
S_ s55,s71,30720
Mvav 36,196,216
S_ m0,s51,15360
Mvav 36,192,196
W_ 0
Mvaa 56,180,0
s_nop 0
Ta 192,v1,49152
Ta 194,v1,51200
Mvaa 56,184,40
L_ v7,24,s55,0
Mvaa 56,188,80
S_ s55,s71,40960
Ta 196,v1,53248
Ta 198,v1,55296
Mvaa 60,180,4
S_ m0,s51,16384
Mvaa 60,184,44
s_nop 0
Ta 220,v1,57344
Ta 222,v1,59392
Mvaa 60,188,84
L_ v7,24,s55,0
Mvaa 64,180,8
S_ s55,s71,51200
Mvaa 64,184,48
S_ m0,s51,17408
Mvaa 64,188,88
Mvaa 68,180,12
L_ v7,24,s55,0
Mvaa 68,184,52
Mvaa 68,188,240
Mvaa 72,180,16
Mvaa 72,184,56
Mvaa 72,188,244
W_ 0
Mvaa 92,220,244
s_nop 0
Ta 180,v2,49152
Ta 182,v2,51200
Mvaa 92,196,56
Mvaa 92,192,16
s_nop 0
Ta 184,v2,53248
Ta 186,v2,55296
Mvaa 88,220,240
Mvaa 88,196,52
s_nop 0
Ta 188,v2,57344
Ta 190,v2,59392
Mvaa 88,192,12
Mvaa 84,220,88
Mvaa 84,196,48
Mvaa 84,192,8
Mvaa 80,220,84
Mvaa 80,196,44
Mvaa 80,192,4
Mvaa 76,220,80
Mvaa 76,196,40
Mvaa 76,192,0
W_ 0
Mvaa 16,180,0
s_nop 0
Ta 192,v3,49152
Ta 194,v3,51200
Mvaa 16,184,40
Mvaa 16,188,80
s_nop 0
Ta 196,v3,53248
Ta 198,v3,55296
Mvaa 20,180,4
Mvaa 20,184,44
s_nop 0
Ta 220,v3,57344
Ta 222,v3,59392
Mvaa 20,188,84
Mvaa 24,180,8
Mvaa 24,184,48
Mvaa 24,188,88
Mvaa 28,180,12
Mvaa 28,184,52
Mvaa 28,188,240
Mvaa 32,180,16
Mvaa 32,184,56
Mvaa 32,188,244
s_waitcnt vmcnt(25) lgkmcnt(0)
s_barrier
Mvaa 52,220,244
s_nop 0
Ta 180,%0,36864
Ta 182,%0,38912
Mvaa 52,196,56
Mvaa 52,192,16
s_nop 0
Ta 184,%0,40960
Ta 186,%0,43008
Mvaa 48,220,240
Mvaa 48,196,52
s_nop 0
Ta 188,%0,45056
Ta 190,%0,47104
Mvaa 48,192,12
Mvaa 44,220,88
Mvaa 44,196,48
Mvaa 44,192,8
Mvaa 40,220,84
Mvaa 40,196,44
Mvaa 40,192,4
Mvaa 36,220,80
Mvaa 36,196,40
Mvaa 36,192,0
W_ 0
Mvaa 56,180,120
S_ s81,s6,3
Ta 192,v1,36864
Ta 194,v1,38912
Mvaa 56,184,160
s_and_b32 s81, s81, 7
Mvaa 56,188,200
s_mul_i32 s81, s81, 20480
Ta 196,v1,40960
Ta 198,v1,43008
Mvaa 60,180,124
s_mov_b32 m0, s52
S_ s73,s81,s78
Mvaa 60,184,164
S_ s73,s73,4096
Ta 220,v1,45056
Ta 222,v1,47104
Mvaa 60,188,204
s_nop 0
Y_ 56,0
Y_ 57,256
Mvaa 64,180,128
s_nop 0
Y_ 58,512
Y_ 59,768
Mvaa 64,184,168
Mvaa 64,188,208
s_nop 0
Y_ 60,1024
Y_ 61,1280
Mvaa 68,180,132
s_nop 0
Y_ 62,1536
Y_ 63,1792
Mvaa 68,184,172
Mvaa 68,188,212
s_nop 0
Y_ 64,2048
Y_ 65,2304
Mvaa 72,180,136
s_nop 0
Y_ 66,2560
Y_ 67,2816
Mvaa 72,184,176
Mvaa 72,188,216
W_ 12
Y_ 68,3072
Y_ 69,3328
Mvaa 92,220,216
s_nop 0
Ta 180,v2,36864
Ta 182,v2,38912
Mvaa 92,196,176
s_nop 0
Y_ 70,3584
Y_ 71,3840
Mvaa 92,192,136
s_nop 0
Ta 184,v2,40960
Ta 186,v2,43008
Mvaa 88,220,212
s_nop 0
Y_ 72,4096
Y_ 73,4352
Mvaa 88,196,172
S_ s83,s6,2
Ta 188,v2,45056
Ta 190,v2,47104
Mvaa 88,192,132
s_and_b32 s83, s83, 7
Y_ 74,4608
Y_ 75,4864
Mvaa 84,220,208
s_lshl_b32 s83, s83, 2
Y_ 92,9216
Y_ 93,9472
Mvaa 84,196,168
s_nop 0
Y_ 94,9728
Y_ 95,9984
Mvaa 84,192,128
s_nop 0
Y_ 88,8192
Y_ 89,8448
Mvaa 80,220,204
s_nop 0
Y_ 90,8704
Y_ 91,8960
Mvaa 80,196,164
v_cmp_eq_u32_e64 s[36:37], s83, v8
Mvaa 80,192,124
s_nop 0
Y_ 84,7168
Y_ 85,7424
Mvaa 76,220,200
s_nop 0
Y_ 86,7680
Y_ 87,7936
Mvaa 76,196,160
v_mov_b32 v10, 0x7fff0000
Mvaa 76,192,120
W_ 14
Y_ 80,6144
Y_ 81,6400
Mvaa 16,180,120
s_nop 0
Ta 192,v3,36864
Ta 194,v3,38912
Mvaa 16,184,160
s_nop 0
Y_ 82,6656
Y_ 83,6912
Mvaa 16,188,200
s_nop 0
Ta 196,v3,40960
Ta 198,v3,43008
Mvaa 20,180,124
s_nop 0
Y_ 76,5120
Y_ 77,5376
Mvaa 20,184,164
s_nop 0
Ta 220,v3,45056
Ta 222,v3,47104
Mvaa 20,188,204
s_nop 0
Y_ 78,5632
Y_ 79,5888
Mvaa 24,180,128
v_cndmask_b32_e64 v10, v10, v9, s[36:37]
Mvaa 24,184,168
Mvaa 24,188,208
s_waitcnt vmcnt(14) lgkmcnt(0)
s_barrier
Mvaa 28,180,132
buffer_store_dword a255, v10, s[92:95], 0 offen offset:384 sc1
Mvaa 28,184,172
s_sub_u32 s75, s6, 1
ds_read2st64_b32 v[96:97], v5 offset0:0 offset1:1
S_ s55,s71,122880
Mvaa 28,188,212
s_and_b32 s75, s75, 7
ds_read2st64_b32 v[98:99], v5 offset0:2 offset1:3
S_ m0,s51,49152
)A"
R"A(Mvaa 32,180,136
ds_read2st64_b32 v[104:105], v5 offset0:4 offset1:5
Mvaa 32,184,176
buffer_load_dword v11, v8, s[92:95], s96 offen offset:384 sc1
Mvaa 32,188,216
ds_read2st64_b32 v[106:107], v5 offset0:6 offset1:7
Mvaa 52,220,216
s_nop 0
Ta 180,%0,24576
Ta 182,%0,26624
Mvaa 52,196,176
ds_read2st64_b32 v[112:113], v5 offset0:8 offset1:9
Mvaa 52,192,136
W_ 5
Ta 184,%0,28672
Ta 186,%0,30720
Mvaa 48,220,212
L_ v7,24,s55,0
Mvaa 48,196,172
S_ s55,s71,133120
Ta 188,%0,32768
Ta 190,%0,34816
Mvaa 48,192,132
S_ m0,s51,50176
ds_read2st64_b32 v[114:115], v5 offset0:10 offset1:11
A_ v96,v16,v96
Mvaa 44,220,208
ds_read2st64_b32 v[120:121], v5 offset0:12 offset1:13
A_ v97,v17,v97
Mvaa 44,196,168
L_ v7,24,s55,0
Mvaa 44,192,128
S_ s55,s71,143360
ds_read2st64_b32 v[122:123], v5 offset0:14 offset1:15
A_ v98,v18,v98
Mvaa 40,220,204
S_ m0,s51,51200
ds_read2st64_b32 v[128:129], v5 offset0:16 offset1:17
A_ v99,v19,v99
Mvaa 40,196,164
W_ 11
ds_read2st64_b32 v[130:131], v5 offset0:18 offset1:19
A_ v104,v20,v104
Mvaa 40,192,124
L_ v7,24,s55,0
Mvaa 36,220,200
S_ s55,s71,153600
ds_read2st64_b32 v[132:133], v5 offset0:36 offset1:37
A_ v105,v21,v105
Mvaa 36,196,160
S_ m0,s51,52224
ds_read2st64_b32 v[134:135], v5 offset0:38 offset1:39
A_ v106,v22,v106
Mvaa 36,192,120
W_ 7
ds_read2st64_b32 v[124:125], v5 offset0:32 offset1:33
A_ v107,v23,v107
Zvav 56,180,136
W_ 7
Ta 192,v1,24576
Ta 194,v1,26624
Mvav 56,184,156
L_ v7,24,s55,0
Mvav 56,188,176
S_ s55,s71,163840
Ta 196,v1,28672
Ta 198,v1,30720
Zvav 60,180,140
S_ m0,s51,53248
ds_read2st64_b32 v[126:127], v5 offset0:34 offset1:35
A_ v112,v24,v112
Mvav 60,184,160
s_nop 0
Ta 220,v1,32768
Ta 222,v1,34816
Mvav 60,188,180
L_ v7,24,s55,0
Zvav 64,180,144
S_ s55,s71,174080
ds_read2st64_b32 v[116:117], v5 offset0:28 offset1:29
A_ v113,v25,v113
Mvav 64,184,164
S_ m0,s51,54272
ds_read2st64_b32 v[118:119], v5 offset0:30 offset1:31
A_ v114,v26,v114
Mvav 64,188,184
ds_read2st64_b32 v[108:109], v5 offset0:24 offset1:25
A_ v115,v27,v115
Zvav 68,180,148
L_ v7,24,s55,0
Mvav 68,184,168
W_ 14
ds_read2st64_b32 v[110:111], v5 offset0:26 offset1:27
A_ v120,v28,v120
Mvav 68,188,188
S_ s79,s6,5
ds_read2st64_b32 v[100:101], v5 offset0:20 offset1:21
A_ v121,v29,v121
Zvav 72,180,152
s_and_b32 s79, s79, 7
ds_read2st64_b32 v[102:103], v5 offset0:22 offset1:23
A_ v122,v30,v122
Mvav 72,184,172
s_lshl_b32 s79, s79, 7
A_ v123,v31,v123
Mvav 72,188,192
W_ 6
A_ v128,v32,v128
Zvav 92,220,192
S_ s71,s72,s79
Ta 180,v2,24576
Ta 182,v2,26624
Mvav 92,196,172
S_ s55,s71,491520
A_ v129,v33,v129
A_ v130,v34,v130
Mvav 92,192,152
S_ m0,s51,36864
Ta 184,v2,28672
Ta 186,v2,30720
Zvav 88,220,188
L_ v7,24,s55,0
Mvav 88,196,168
S_ s55,s71,501760
Ta 188,v2,32768
Ta 190,v2,34816
Mvav 88,192,148
S_ m0,s51,37888
A_ v131,v35,v131
S_ s85,s6,7
Zvav 84,220,184
A_ v132,v52,v132
A_ v133,v53,v133
Mvav 84,196,164
L_ v7,24,s55,0
Mvav 84,192,144
S_ s55,s71,512000
A_ v134,v54,v134
A_ v135,v55,v135
Zvav 80,220,180
A_ v124,v48,v124
A_ v125,v49,v125
Mvav 80,196,160
S_ m0,s51,38912
A_ v126,v50,v126
A_ v127,v51,v127
Mvav 80,192,140
L_ v7,24,s55,0
Zvav 76,220,176
W_ 10
A_ v116,v44,v116
A_ v117,v45,v117
Mvav 76,196,156
S_ s55,s71,522240
A_ v118,v46,v118
A_ v119,v47,v119
Mvav 76,192,136
W_ 0
A_ v108,v40,v108
Zvav 16,180,136
S_ m0,s51,39936
Ta 192,v3,24576
Ta 194,v3,26624
Mvav 16,184,156
L_ v7,24,s55,0
Mvav 16,188,176
S_ s55,s71,532480
Ta 196,v3,28672
Ta 198,v3,30720
Zvav 20,180,140
S_ m0,s51,40960
A_ v109,v41,v109
A_ v110,v42,v110
Mvav 20,184,160
s_and_b32 s85, s85, 7
Ta 220,v3,32768
.Lpad_o_6:
s_cbranch_execz .Lpad_o_10
Ta 222,v3,34816
Mvav 20,188,180
L_ v7,24,s55,0
Zvav 24,180,144
S_ s55,s71,542720
A_ v111,v43,v111
C_ 131,130,131
Mvav 24,184,164
A_ v100,v36,v100
A_ v101,v37,v101
Mvav 24,188,184
S_ m0,s51,41984
A_ v102,v38,v102
A_ v103,v39,v103
Zvav 28,180,148
L_ v7,24,s55,0
Mvav 28,184,168
s_lshl_b32 s85, s85, 7
C_ 130,128,129
C_ 132,132,133
Mvav 28,188,188
S_ s85,s85,s87
C_ 133,134,135
Zvav 32,180,152
Mvav 32,184,172
buffer_store_dwordx4 v[130:133], v6, s[28:31], s73 offen offset:0 sc1
Mvav 32,188,192
s_waitcnt vmcnt(23) lgkmcnt(0)
s_barrier
s_sub_u32 s73, s73, 4096
Zvav 52,220,192
S_ s55,s71,614400
Ta 180,%0,0
Ta 182,%0,2048
Mvav 52,196,172
S_ m0,s51,24576
C_ 123,122,123
C_ 122,120,121
Mvav 52,192,152
s_nop 0
Ta 184,%0,4096
Ta 186,%0,6144
Zvav 48,220,188
L_ v7,24,s55,0
Mvav 48,196,168
S_ s55,s71,624640
Ta 188,%0,8192
Ta 190,%0,10240
Mvav 48,192,148
S_ m0,s51,25600
C_ 124,124,125
C_ 125,126,127
Zvav 44,220,184
Mvav 44,196,164
buffer_store_dwordx4 v[122:125], v6, s[28:31], s73 offen offset:3072 sc1
Mvav 44,192,144
C_ 115,114,115
C_ 114,112,113
Zvav 40,220,180
C_ 116,116,117
C_ 117,118,119
Mvav 40,196,160
Mvav 40,192,140
buffer_store_dwordx4 v[114:117], v6, s[28:31], s73 offen offset:2048 sc1
Zvav 36,220,176
C_ 107,106,107
C_ 106,104,105
Mvav 36,196,156
C_ 108,108,109
C_ 109,110,111
Mvav 36,192,136
W_ 0
Mvav 56,180,196
s_nop 0
Ta 192,v1,0
Ta 194,v1,2048
Mvav 56,184,216
buffer_store_dwordx4 v[106:109], v6, s[28:31], s73 offen offset:1024 sc1
Mvav 56,188,236
s_nop 0
Ta 196,v1,4096
Ta 198,v1,6144
Mvav 60,180,200
C_ 99,98,99
C_ 98,96,97
Mvav 60,184,220
s_nop 0
Ta 220,v1,8192
Ta 222,v1,10240
Mvav 60,188,240
L_ v7,24,s55,0
Mvav 64,180,204
S_ s55,s71,634880
C_ 100,100,101
C_ 101,102,103
Mvav 64,184,224
S_ m0,s51,26624
Mvav 64,188,244
Mvav 68,180,208
buffer_store_dwordx4 v[98:101], v6, s[28:31], s73 offen offset:0 sc1
Mvav 68,184,228
Mvav 68,188,248
Mvav 72,180,212
Mvav 72,184,232
L_ v7,24,s55,0
Mvav 72,188,252
W_ 0
S_ s55,s71,645120
Mvav 92,220,252
S_ m0,s51,27648
Ta 180,v2,0
Ta 182,v2,2048
Mvav 92,196,232
Mvav 92,192,212
s_nop 0
Ta 184,v2,4096
Ta 186,v2,6144
Mvav 88,220,248
L_ v7,24,s55,0
Mvav 88,196,228
s_waitcnt vmcnt(21)
Ta 188,v2,8192
Ta 190,v2,10240
Mvav 88,192,208
S_ s55,s71,655360
v_cmp_eq_u32 vcc, s62, v11
Mvav 84,220,244
s_mul_i32 s76, s75, 204800
S_ m0,s51,28672
Mvav 84,196,224
L_ v7,24,s55,0
Mvav 84,192,204
s_bitcmp1_b32 vcc_lo, s75
s_cbranch_scc0 .Lspin0
.Lspun0:
S_ s55,s71,665600
Mvav 80,220,240
S_ s56,s76,s84
S_ m0,s51,29696
Mvav 80,196,220
Mvav 80,192,200
buffer_load_dwordx4 a[20:23], v6, s[28:31], s56 offen offset:0 sc1
Mvav 76,220,236
Mvav 76,196,216
Mvav 76,192,196
W_ 0
Mvav 16,180,196
s_nop 0
Ta 192,v3,0
Ta 194,v3,2048
Mvav 16,184,216
buffer_load_dwordx4 a[24:27], v6, s[28:31], s56 offen offset:1024 sc1
Mvav 16,188,236
s_nop 0
Ta 196,v3,4096
Ta 198,v3,6144
Mvav 20,180,200
Mvav 20,184,220
s_nop 0
Ta 220,v3,8192
Ta 222,v3,10240
Mvav 20,188,240
buffer_load_dwordx4 a[28:31], v6, s[28:31], s56 offen offset:2048 sc1
Mvav 24,180,204
Mvav 24,184,224
Mvav 24,188,244
Mvav 28,180,208
buffer_load_dwordx4 a[32:35], v6, s[28:31], s56 offen offset:3072 sc1
Mvav 28,184,228
S_ s56,s56,4096
Mvav 28,188,248
Mvav 32,180,212
Mvav 32,184,232
buffer_load_dwordx4 a[36:39], v6, s[28:31], s56 offen offset:0 sc1
Mvav 32,188,252
s_waitcnt vmcnt(29) lgkmcnt(0)
s_barrier
S_ s56,s76,s74
Mvav 52,220,252
s_nop 0
Ta 180,%0,12288
Ta 182,%0,14336
Mvav 52,196,232
Mvav 52,192,212
s_nop 0
Ta 184,%0,16384
Ta 186,%0,18432
Mvav 48,220,248
buffer_load_dwordx4 a[60:63], v6, s[28:31], s56 offen offset:0 sc1
Mvav 48,196,228
S_ s56,s76,s82
Ta 188,%0,20480
Ta 190,%0,22528
Mvav 48,192,208
Mvav 44,220,244
Mvav 44,196,224
buffer_load_dwordx4 a[64:67], v6, s[28:31], s56 offen offset:0 sc1
Mvav 44,192,204
Mvav 40,220,240
Mvav 40,196,220
Mvav 40,192,200
L_ v7,24,s55,0
Mvav 36,220,236
S_ s55,s71,0
Mvav 36,196,216
S_ m0,s51,0
Mvav 36,192,196
W_ 0
Mvaa 56,180,0
s_nop 0
Ta 192,v1,12288
Ta 194,v1,14336
Mvaa 56,184,40
L_ v7,24,s55,0
Mvaa 56,188,80
S_ s55,s71,10240
Ta 196,v1,16384
Ta 198,v1,18432
Mvaa 60,180,4
S_ m0,s51,1024
Mvaa 60,184,44
s_nop 0
Ta 220,v1,20480
Ta 222,v1,22528
Mvaa 60,188,84
L_ v7,24,s55,0
Mvaa 64,180,8
S_ s55,s71,20480
Mvaa 64,184,48
S_ m0,s51,2048
Mvaa 64,188,88
Mvaa 68,180,12
L_ v7,24,s55,0
Mvaa 68,184,52
S_ s55,s71,30720
Mvaa 68,188,240
S_ m0,s51,3072
Mvaa 72,180,16
Mvaa 72,184,56
L_ v7,24,s55,0
Mvaa 72,188,244
W_ 0
S_ s55,s71,40960
Mvaa 92,220,244
S_ m0,s51,4096
Ta 180,v2,12288
Ta 182,v2,14336
Mvaa 92,196,56
Mvaa 92,192,16
s_nop 0
Ta 184,v2,16384
Ta 186,v2,18432
Mvaa 88,220,240
L_ v7,24,s55,0
Mvaa 88,196,52
S_ s55,s71,51200
Ta 188,v2,20480
Ta 190,v2,22528
Mvaa 88,192,12
S_ m0,s51,5120
Mvaa 84,220,88
Mvaa 84,196,48
L_ v7,24,s55,0
Mvaa 84,192,8
Mvaa 80,220,84
Mvaa 80,196,44
Mvaa 80,192,4
buffer_load_dword a74, v4, s[24:27], s85 offen
Mvaa 76,220,80
Mvaa 76,196,40
Mvaa 76,192,0
W_ 0
Mvaa 16,180,0
s_nop 0
Ta 192,v3,12288
Ta 194,v3,14336
Mvaa 16,184,40
Mvaa 16,188,80
s_nop 0
Ta 196,v3,16384
Ta 198,v3,18432
Mvaa 20,180,4
Mvaa 20,184,44
s_nop 0
Ta 220,v3,20480
Ta 222,v3,22528
Mvaa 20,188,84
Mvaa 24,180,8
Mvaa 24,184,48
Mvaa 24,188,88
Mvaa 28,180,12
Mvaa 28,184,52
Mvaa 28,188,240
Mvaa 32,180,16
Mvaa 32,184,56
Mvaa 32,188,244
s_waitcnt vmcnt(31) lgkmcnt(0)
s_barrier
Mvaa 52,220,244
s_nop 0
Ta 180,%0,49152
Ta 182,%0,51200
Mvaa 52,196,56
Mvaa 52,192,16
s_nop 0
Ta 184,%0,53248
Ta 186,%0,55296
Mvaa 48,220,240
Mvaa 48,196,52
s_nop 0
Ta 188,%0,57344
Ta 190,%0,59392
Mvaa 48,192,12
Mvaa 44,220,88
Mvaa 44,196,48
Mvaa 44,192,8
Mvaa 40,220,84
Mvaa 40,196,44
Mvaa 40,192,4
Mvaa 36,220,80
Mvaa 36,196,40
Mvaa 36,192,0
W_ 0
Mvaa 56,180,120
S_ s81,s6,4
Ta 192,v1,49152
Ta 194,v1,51200
Mvaa 56,184,160
s_and_b32 s81, s81, 7
Mvaa 56,188,200
s_mul_i32 s81, s81, 20480
Ta 196,v1,53248
Ta 198,v1,55296
Mvaa 60,180,124
s_mov_b32 m0, s52
S_ s73,s81,s78
Mvaa 60,184,164
S_ s73,s73,4096
Ta 220,v1,57344
Ta 222,v1,59392
Mvaa 60,188,204
s_nop 0
Y_ 56,0
Y_ 57,256
Mvaa 64,180,128
s_nop 0
Y_ 58,512
Y_ 59,768
Mvaa 64,184,168
Mvaa 64,188,208
s_nop 0
Y_ 60,1024
Y_ 61,1280
Mvaa 68,180,132
s_nop 0
Y_ 62,1536
Y_ 63,1792
Mvaa 68,184,172
Mvaa 68,188,212
s_nop 0
Y_ 64,2048
Y_ 65,2304
Mvaa 72,180,136
s_nop 0
Y_ 66,2560
Y_ 67,2816
Mvaa 72,184,176
Mvaa 72,188,216
W_ 12
Y_ 68,3072
Y_ 69,3328
Mvaa 92,220,216
s_nop 0
Ta 180,v2,49152
Ta 182,v2,51200
Mvaa 92,196,176
s_nop 0
Y_ 70,3584
Y_ 71,3840
Mvaa 92,192,136
s_nop 0
Ta 184,v2,53248
Ta 186,v2,55296
Mvaa 88,220,212
s_nop 0
Y_ 72,4096
Y_ 73,4352
Mvaa 88,196,172
S_ s83,s6,3
Ta 188,v2,57344
Ta 190,v2,59392
Mvaa 88,192,132
s_and_b32 s83, s83, 7
Y_ 74,4608
Y_ 75,4864
Mvaa 84,220,208
s_lshl_b32 s83, s83, 2
Y_ 92,9216
Y_ 93,9472
Mvaa 84,196,168
s_nop 0
Y_ 94,9728
Y_ 95,9984
Mvaa 84,192,128
s_nop 0
Y_ 88,8192
Y_ 89,8448
Mvaa 80,220,204
s_nop 0
Y_ 90,8704
Y_ 91,8960
Mvaa 80,196,164
v_cmp_eq_u32_e64 s[36:37], s83, v8
Mvaa 80,192,124
s_nop 0
Y_ 84,7168
Y_ 85,7424
Mvaa 76,220,200
s_nop 0
Y_ 86,7680
Y_ 87,7936
Mvaa 76,196,160
v_mov_b32 v10, 0x7fff0000
Mvaa 76,192,120
W_ 14
Y_ 80,6144
Y_ 81,6400
Mvaa 16,180,120
s_nop 0
Ta 192,v3,49152
Ta 194,v3,51200
Mvaa 16,184,160
s_nop 0
Y_ 82,6656
Y_ 83,6912
Mvaa 16,188,200
s_nop 0
Ta 196,v3,53248
Ta 198,v3,55296
Mvaa 20,180,124
s_nop 0
Y_ 76,5120
Y_ 77,5376
Mvaa 20,184,164
s_nop 0
Ta 220,v3,57344
Ta 222,v3,59392
Mvaa 20,188,204
s_nop 0
Y_ 78,5632
Y_ 79,5888
Mvaa 24,180,128
v_cndmask_b32_e64 v10, v10, v9, s[36:37]
Mvaa 24,184,168
Mvaa 24,188,208
s_waitcnt vmcnt(18) lgkmcnt(0)
s_barrier
Mvaa 28,180,132
buffer_store_dword a255, v10, s[92:95], 0 offen offset:384 sc1
Mvaa 28,184,172
s_sub_u32 s75, s6, 2
ds_read2st64_b32 v[96:97], v5 offset0:0 offset1:1
S_ s55,s71,122880
Mvaa 28,188,212
s_and_b32 s75, s75, 7
ds_read2st64_b32 v[98:99], v5 offset0:2 offset1:3
S_ m0,s51,12288
Mvaa 32,180,136
ds_read2st64_b32 v[104:105], v5 offset0:4 offset1:5
Mvaa 32,184,176
buffer_load_dword v11, v8, s[92:95], s96 offen offset:384 sc1
Mvaa 32,188,216
ds_read2st64_b32 v[106:107], v5 offset0:6 offset1:7
Mvaa 52,220,216
s_nop 0
Ta 180,%0,36864
Ta 182,%0,38912
Mvaa 52,196,176
ds_read2st64_b32 v[112:113], v5 offset0:8 offset1:9
Mvaa 52,192,136
W_ 5
Ta 184,%0,40960
Ta 186,%0,43008
Mvaa 48,220,212
L_ v7,24,s55,0
Mvaa 48,196,172
S_ s55,s71,133120
Ta 188,%0,45056
Ta 190,%0,47104
Mvaa 48,192,132
S_ m0,s51,13312
ds_read2st64_b32 v[114:115], v5 offset0:10 offset1:11
A_ v96,v16,v96
Mvaa 44,220,208
ds_read2st64_b32 v[120:121], v5 offset0:12 offset1:13
A_ v97,v17,v97
Mvaa 44,196,168
L_ v7,24,s55,0
Mvaa 44,192,128
S_ s55,s71,143360
ds_read2st64_b32 v[122:123], v5 offset0:14 offset1:15
A_ v98,v18,v98
Mvaa 40,220,204
S_ m0,s51,14336
ds_read2st64_b32 v[128:129], v5 offset0:16 offset1:17
A_ v99,v19,v99
Mvaa 40,196,164
W_ 11
ds_read2st64_b32 v[130:131], v5 offset0:18 offset1:19
A_ v104,v20,v104
Mvaa 40,192,124
L_ v7,24,s55,0
Mvaa 36,220,200
S_ s55,s71,153600
ds_read2st64_b32 v[132:133], v5 offset0:36 offset1:37
A_ v105,v21,v105
Mvaa 36,196,160
S_ m0,s51,15360
ds_read2st64_b32 v[134:135], v5 offset0:38 offset1:39
A_ v106,v22,v106
Mvaa 36,192,120
ds_read2st64_b32 v[124:125], v5 offset0:32 offset1:33
A_ v107,v23,v107
s_waitcnt vmcnt(13)
Zaaa 100,70,20
L_ v7,24,s55,0
Zaaa 140,68,20
W_ 7
ds_read2st64_b32 v[126:127], v5 offset0:34 offset1:35
A_ v112,v24,v112
Zaaa 104,70,24
S_ s55,s71,163840
ds_read2st64_b32 v[116:117], v5 offset0:28 offset1:29
A_ v113,v25,v113
Zaaa 144,68,24
S_ m0,s51,16384
ds_read2st64_b32 v[118:119], v5 offset0:30 offset1:31
A_ v114,v26,v114
Zaaa 108,70,28
L_ v7,24,s55,0
Zaaa 148,68,28
S_ s55,s71,174080
ds_read2st64_b32 v[108:109], v5 offset0:24 offset1:25
A_ v115,v27,v115
Zaaa 112,70,32
W_ 9
ds_read2st64_b32 v[110:111], v5 offset0:26 offset1:27
A_ v120,v28,v120
Zaaa 152,68,32
S_ m0,s51,17408
ds_read2st64_b32 v[100:101], v5 offset0:20 offset1:21
A_ v121,v29,v121
Zaaa 116,70,36
L_ v7,24,s55,0
Zaaa 156,68,36
S_ s79,s6,6
ds_read2st64_b32 v[102:103], v5 offset0:22 offset1:23
A_ v122,v30,v122
Maaa 224,70,60
s_and_b32 s79, s79, 7
A_ v123,v31,v123
Maaa 228,68,60
W_ 10
A_ v128,v32,v128
A_ v129,v33,v129
Maaa 232,70,64
s_lshl_b32 s79, s79, 7
A_ v130,v34,v130
.Lpad_o_7:
s_cbranch_execz .Lpad_o_11
A_ v131,v35,v131
Maaa 236,68,64
W_ 9
A_ v132,v52,v132
W_ 8
Zvav 56,180,136
S_ s71,s72,s79
Ta 192,v1,36864
Ta 194,v1,38912
Mvav 56,184,156
S_ s55,s71,491520
A_ v133,v53,v133
A_ v134,v54,v134
Mvav 56,188,176
S_ m0,s51,49152
Ta 196,v1,40960
Ta 198,v1,43008
Zvav 60,180,140
L_ v7,24,s55,0
Mvav 60,184,160
S_ s55,s71,501760
Ta 220,v1,45056
Ta 222,v1,47104
Mvav 60,188,180
S_ m0,s51,50176
A_ v135,v55,v135
Zvav 64,180,144
W_ 12
A_ v124,v48,v124
A_ v125,v49,v125
Mvav 64,184,164
L_ v7,24,s55,0
Mvav 64,188,184
S_ s55,s71,512000
A_ v126,v50,v126
A_ v127,v51,v127
Zvav 68,180,148
W_ 10
A_ v116,v44,v116
A_ v117,v45,v117
Mvav 68,184,168
S_ m0,s51,51200
A_ v118,v46,v118
A_ v119,v47,v119
Mvav 68,188,188
L_ v7,24,s55,0
Zvav 72,180,152
W_ 8
A_ v108,v40,v108
A_ v109,v41,v109
Mvav 72,184,172
S_ s55,s71,522240
A_ v110,v42,v110
A_ v111,v43,v111
Mvav 72,188,192
W_ 0
A_ v100,v36,v100
Zvav 92,220,192
S_ m0,s51,52224
Ta 180,v2,36864
Ta 182,v2,38912
Mvav 92,196,172
L_ v7,24,s55,0
Mvav 92,192,152
S_ s55,s71,532480
Ta 184,v2,40960
Ta 186,v2,43008
Zvav 88,220,188
S_ m0,s51,53248
A_ v101,v37,v101
A_ v102,v38,v102
Mvav 88,196,168
s_nop 0
Ta 188,v2,45056
Ta 190,v2,47104
Mvav 88,192,148
L_ v7,24,s55,0
Zvav 84,220,184
S_ s55,s71,542720
A_ v103,v39,v103
C_ 131,130,131
Mvav 84,196,164
S_ m0,s51,54272
C_ 130,128,129
C_ 132,132,133
Mvav 84,192,144
S_ s85,s6,8
C_ 133,134,135
Zvav 80,220,180
buffer_store_dwordx4 v[130:133], v6, s[28:31], s73 offen offset:0 sc1
Mvav 80,196,160
s_sub_u32 s73, s73, 4096
s_and_b32 s85, s85, 7
Mvav 80,192,140
s_lshl_b32 s85, s85, 7
C_ 123,122,123
C_ 122,120,121
Zvav 76,220,176
S_ s85,s85,s87
C_ 124,124,125
C_ 125,126,127
Mvav 76,196,156
buffer_store_dwordx4 v[122:125], v6, s[28:31], s73 offen offset:3072 sc1
Mvav 76,192,136
W_ 0
C_ 115,114,115
C_ 114,112,113
Zvav 16,180,136
s_nop 0
Ta 192,v3,36864
Ta 194,v3,38912
Mvav 16,184,156
C_ 116,116,117
C_ 117,118,119
Mvav 16,188,176
s_nop 0
Ta 196,v3,40960
Ta 198,v3,43008
Zvav 20,180,140
buffer_store_dwordx4 v[114:117], v6, s[28:31], s73 offen offset:2048 sc1
Mvav 20,184,160
s_nop 0
Ta 220,v3,45056
Ta 222,v3,47104
Mvav 20,188,180
C_ 107,106,107
C_ 106,104,105
Zvav 24,180,144
C_ 108,108,109
C_ 109,110,111
Mvav 24,184,164
buffer_store_dwordx4 v[106:109], v6, s[28:31], s73 offen offset:1024 sc1
Mvav 24,188,184
C_ 99,98,99
C_ 98,96,97
Zvav 28,180,148
C_ 100,100,101
C_ 101,102,103
Mvav 28,184,168
Mvav 28,188,188
buffer_store_dwordx4 v[98:101], v6, s[28:31], s73 offen offset:0 sc1
Zvav 32,180,152
Mvav 32,184,172
Mvav 32,188,192
s_waitcnt vmcnt(25) lgkmcnt(0)
s_barrier
Zvav 52,220,192
s_nop 0
Ta 180,%0,24576
Ta 182,%0,26624
Mvav 52,196,172
L_ v7,24,s55,0
Mvav 52,192,152
S_ s55,s71,614400
Ta 184,%0,28672
Ta 186,%0,30720
Zvav 48,220,188
S_ m0,s51,36864
Mvav 48,196,168
s_nop 0
Ta 188,%0,32768
Ta 190,%0,34816
Mvav 48,192,148
L_ v7,24,s55,0
Zvav 44,220,184
S_ s55,s71,624640
Mvav 44,196,164
S_ m0,s51,37888
Mvav 44,192,144
Zvav 40,220,180
L_ v7,24,s55,0
Mvav 40,196,160
S_ s55,s71,634880
Mvav 40,192,140
S_ m0,s51,38912
Zvav 36,220,176
Mvav 36,196,156
L_ v7,24,s55,0
Mvav 36,192,136
W_ 0
S_ s55,s71,645120
Mvav 56,180,196
S_ m0,s51,39936
Ta 192,v1,24576
Ta 194,v1,26624
Mvav 56,184,216
Mvav 56,188,236
s_nop 0
Ta 196,v1,28672
Ta 198,v1,30720
Mvav 60,180,200
L_ v7,24,s55,0
Mvav 60,184,220
S_ s55,s71,655360
Ta 220,v1,32768
Ta 222,v1,34816
Mvav 60,188,240
S_ m0,s51,40960
Mvav 64,180,204
Mvav 64,184,224
L_ v7,24,s55,0
Mvav 64,188,244
S_ s55,s71,665600
Mvav 68,180,208
S_ m0,s51,41984
Mvav 68,184,228
Mvav 68,188,248
L_ v7,24,s55,0
Mvav 72,180,212
Mvav 72,184,232
Mvav 72,188,252
W_ 0
Mvav 92,220,252
s_nop 0
Ta 180,v2,24576
Ta 182,v2,26624
Mvav 92,196,232
buffer_load_dword a74, v4, s[24:27], s85 offen
Mvav 92,192,212
s_nop 0
Ta 184,v2,28672
Ta 186,v2,30720
Mvav 88,220,248
s_waitcnt vmcnt(24)
Mvav 88,196,228
s_nop 0
Ta 188,v2,32768
Ta 190,v2,34816
Mvav 88,192,208
v_cmp_eq_u32 vcc, s62, v11
Mvav 84,220,244
s_mul_i32 s76, s75, 204800
Mvav 84,196,224
s_bitcmp1_b32 vcc_lo, s75
s_cbranch_scc0 .Lspin1
.Lspun1:
Mvav 84,192,204
S_ s56,s76,s84
Mvav 80,220,240
buffer_load_dwordx4 a[20:23], v6, s[28:31], s56 offen offset:0 sc1
Mvav 80,196,220
Mvav 80,192,200
Mvav 76,220,236
Mvav 76,196,216
buffer_load_dwordx4 a[24:27], v6, s[28:31], s56 offen offset:1024 sc1
Mvav 76,192,196
W_ 0
Mvav 16,180,196
s_nop 0
Ta 192,v3,24576
Ta 194,v3,26624
Mvav 16,184,216
Mvav 16,188,236
s_nop 0
Ta 196,v3,28672
Ta 198,v3,30720
Mvav 20,180,200
buffer_load_dwordx4 a[28:31], v6, s[28:31], s56 offen offset:2048 sc1
Mvav 20,184,220
s_nop 0
Ta 220,v3,32768
Ta 222,v3,34816
Mvav 20,188,240
Mvav 24,180,204
Mvav 24,184,224
buffer_load_dwordx4 a[32:35], v6, s[28:31], s56 offen offset:3072 sc1
Mvav 24,188,244
S_ s56,s56,4096
Mvav 28,180,208
Mvav 28,184,228
Mvav 28,188,248
buffer_load_dwordx4 a[36:39], v6, s[28:31], s56 offen offset:0 sc1
Mvav 32,180,212
S_ s56,s76,s74
Mvav 32,184,232
Mvav 32,188,252
s_waitcnt vmcnt(32) lgkmcnt(0)
s_barrier
Mvav 52,220,252
S_ s55,s71,0
Ta 180,%0,0
Ta 182,%0,2048
Mvav 52,196,232
buffer_load_dwordx4 a[60:63], v6, s[28:31], s56 offen offset:0 sc1
Mvav 52,192,212
S_ s56,s76,s82
Ta 184,%0,4096
Ta 186,%0,6144
Mvav 48,220,248
S_ m0,s51,24576
Mvav 48,196,228
s_nop 0
Ta 188,%0,8192
Ta 190,%0,10240
Mvav 48,192,208
buffer_load_dwordx4 a[64:67], v6, s[28:31], s56 offen offset:0 sc1
Mvav 44,220,244
Mvav 44,196,224
Mvav 44,192,204
Mvav 40,220,240
L_ v7,24,s55,0
Mvav 40,196,220
S_ s55,s71,10240
Mvav 40,192,200
S_ m0,s51,25600
Mvav 36,220,236
Mvav 36,196,216
L_ v7,24,s55,0
Mvav 36,192,196
W_ 0
S_ s55,s71,20480
Mvaa 56,180,0
S_ m0,s51,26624
Ta 192,v1,0
Ta 194,v1,2048
Mvaa 56,184,40
Mvaa 56,188,80
s_nop 0
Ta 196,v1,4096
Ta 198,v1,6144
Mvaa 60,180,4
L_ v7,24,s55,0
Mvaa 60,184,44
S_ s55,s71,30720
Ta 220,v1,8192
Ta 222,v1,10240
Mvaa 60,188,84
S_ m0,s51,27648
Mvaa 64,180,8
Mvaa 64,184,48
L_ v7,24,s55,0
Mvaa 64,188,88
S_ s55,s71,40960
Mvaa 68,180,12
S_ m0,s51,28672
Mvaa 68,184,52
Mvaa 68,188,240
L_ v7,24,s55,0
Mvaa 72,180,16
S_ s55,s71,51200
Mvaa 72,184,56
S_ m0,s51,29696
Mvaa 72,188,244
W_ 0
Mvaa 92,220,244
s_nop 0
Ta 180,v2,0
Ta 182,v2,2048
Mvaa 92,196,56
L_ v7,24,s55,0
Mvaa 92,192,16
s_nop 0
Ta 184,v2,4096
Ta 186,v2,6144
Mvaa 88,220,240
Mvaa 88,196,52
s_nop 0
Ta 188,v2,8192
Ta 190,v2,10240
Mvaa 88,192,12
Mvaa 84,220,88
Mvaa 84,196,48
Mvaa 84,192,8
Mvaa 80,220,84
Mvaa 80,196,44
Mvaa 80,192,4
Mvaa 76,220,80
Mvaa 76,196,40
Mvaa 76,192,0
W_ 0
Mvaa 16,180,0
s_nop 0
Ta 192,v3,0
Ta 194,v3,2048
Mvaa 16,184,40
Mvaa 16,188,80
s_nop 0
Ta 196,v3,4096
Ta 198,v3,6144
Mvaa 20,180,4
Mvaa 20,184,44
s_nop 0
Ta 220,v3,8192
Ta 222,v3,10240
Mvaa 20,188,84
Mvaa 24,180,8
Mvaa 24,184,48
Mvaa 24,188,88
Mvaa 28,180,12
Mvaa 28,184,52
Mvaa 28,188,240
Mvaa 32,180,16
Mvaa 32,184,56
Mvaa 32,188,244
s_waitcnt vmcnt(31) lgkmcnt(0)
s_barrier
Mvaa 52,220,244
s_nop 0
Ta 180,%0,12288
Ta 182,%0,14336
Mvaa 52,196,56
Mvaa 52,192,16
s_nop 0
Ta 184,%0,16384
Ta 186,%0,18432
Mvaa 48,220,240
Mvaa 48,196,52
s_nop 0
Ta 188,%0,20480
Ta 190,%0,22528
Mvaa 48,192,12
Mvaa 44,220,88
Mvaa 44,196,48
Mvaa 44,192,8
Mvaa 40,220,84
Mvaa 40,196,44
Mvaa 40,192,4
Mvaa 36,220,80
Mvaa 36,196,40
Mvaa 36,192,0
W_ 0
Mvaa 56,180,120
S_ s81,s6,5
Ta 192,v1,12288
Ta 194,v1,14336
Mvaa 56,184,160
s_and_b32 s81, s81, 7
Mvaa 56,188,200
s_mul_i32 s81, s81, 20480
Ta 196,v1,16384
Ta 198,v1,18432
Mvaa 60,180,124
s_mov_b32 m0, s52
S_ s73,s81,s78
Mvaa 60,184,164
S_ s73,s73,4096
Ta 220,v1,20480
Ta 222,v1,22528
Mvaa 60,188,204
s_nop 0
Y_ 56,0
Y_ 57,256
Mvaa 64,180,128
s_nop 0
Y_ 58,512
Y_ 59,768
Mvaa 64,184,168
Mvaa 64,188,208
s_nop 0
Y_ 60,1024
Y_ 61,1280
Mvaa 68,180,132
s_nop 0
Y_ 62,1536
Y_ 63,1792
Mvaa 68,184,172
Mvaa 68,188,212
s_nop 0
Y_ 64,2048
Y_ 65,2304
Mvaa 72,180,136
s_nop 0
Y_ 66,2560
Y_ 67,2816
Mvaa 72,184,176
Mvaa 72,188,216
W_ 12
Y_ 68,3072
Y_ 69,3328
Mvaa 92,220,216
s_nop 0
Ta 180,v2,12288
Ta 182,v2,14336
Mvaa 92,196,176
s_nop 0
Y_ 70,3584
Y_ 71,3840
Mvaa 92,192,136
s_nop 0
Ta 184,v2,16384
Ta 186,v2,18432
Mvaa 88,220,212
s_nop 0
Y_ 72,4096
Y_ 73,4352
Mvaa 88,196,172
S_ s83,s6,4
Ta 188,v2,20480
Ta 190,v2,22528
Mvaa 88,192,132
s_and_b32 s83, s83, 7
Y_ 74,4608
Y_ 75,4864
Mvaa 84,220,208
s_lshl_b32 s83, s83, 2
Y_ 92,9216
Y_ 93,9472
Mvaa 84,196,168
s_nop 0
Y_ 94,9728
Y_ 95,9984
Mvaa 84,192,128
s_nop 0
Y_ 88,8192
Y_ 89,8448
Mvaa 80,220,204
s_nop 0
Y_ 90,8704
Y_ 91,8960
Mvaa 80,196,164
v_cmp_eq_u32_e64 s[36:37], s83, v8
Mvaa 80,192,124
s_nop 0
Y_ 84,7168
Y_ 85,7424
Mvaa 76,220,200
s_nop 0
Y_ 86,7680
Y_ 87,7936
Mvaa 76,196,160
v_mov_b32 v10, 0x7fff0000
Mvaa 76,192,120
W_ 14
Y_ 80,6144
Y_ 81,6400
Mvaa 16,180,120
s_nop 0
Ta 192,v3,12288
Ta 194,v3,14336
Mvaa 16,184,160
s_nop 0
Y_ 82,6656
Y_ 83,6912
Mvaa 16,188,200
s_nop 0
Ta 196,v3,16384
Ta 198,v3,18432
Mvaa 20,180,124
s_nop 0
Y_ 76,5120
Y_ 77,5376
Mvaa 20,184,164
s_nop 0
Ta 220,v3,20480
Ta 222,v3,22528
Mvaa 20,188,204
s_nop 0
Y_ 78,5632
Y_ 79,5888
Mvaa 24,180,128
v_cndmask_b32_e64 v10, v10, v9, s[36:37]
Mvaa 24,184,168
Mvaa 24,188,208
s_waitcnt vmcnt(20) lgkmcnt(0)
s_barrier
Mvaa 28,180,132
buffer_store_dword a255, v10, s[92:95], 0 offen offset:384 sc1
Mvaa 28,184,172
s_sub_u32 s75, s6, 3
ds_read2st64_b32 v[96:97], v5 offset0:0 offset1:1
S_ s55,s71,122880
Mvaa 28,188,212
s_and_b32 s75, s75, 7
ds_read2st64_b32 v[98:99], v5 offset0:2 offset1:3
S_ m0,s51,0
Mvaa 32,180,136
.Lpad_o_8:
s_cbranch_execz .Lhop_o_r
ds_read2st64_b32 v[104:105], v5 offset0:4 offset1:5
Mvaa 32,184,176
buffer_load_dword v11, v8, s[92:95], s96 offen offset:384 sc1
Mvaa 32,188,216
ds_read2st64_b32 v[106:107], v5 offset0:6 offset1:7
Mvaa 52,220,216
s_nop 0
Ta 180,%0,49152
Ta 182,%0,51200
Mvaa 52,196,176
ds_read2st64_b32 v[112:113], v5 offset0:8 offset1:9
Mvaa 52,192,136
W_ 5
Ta 184,%0,53248
Ta 186,%0,55296
Mvaa 48,220,212
L_ v7,24,s55,0
Mvaa 48,196,172
S_ s55,s71,133120
Ta 188,%0,57344
Ta 190,%0,59392
Mvaa 48,192,132
S_ m0,s51,1024
ds_read2st64_b32 v[114:115], v5 offset0:10 offset1:11
A_ v96,v16,v96
Mvaa 44,220,208
ds_read2st64_b32 v[120:121], v5 offset0:12 offset1:13
A_ v97,v17,v97
Mvaa 44,196,168
L_ v7,24,s55,0
Mvaa 44,192,128
S_ s55,s71,143360
ds_read2st64_b32 v[122:123], v5 offset0:14 offset1:15
A_ v98,v18,v98
Mvaa 40,220,204
S_ m0,s51,2048
ds_read2st64_b32 v[128:129], v5 offset0:16 offset1:17
A_ v99,v19,v99
Mvaa 40,196,164
W_ 11
ds_read2st64_b32 v[130:131], v5 offset0:18 offset1:19
A_ v104,v20,v104
Mvaa 40,192,124
L_ v7,24,s55,0
Mvaa 36,220,200
S_ s55,s71,153600
ds_read2st64_b32 v[132:133], v5 offset0:36 offset1:37
A_ v105,v21,v105
Mvaa 36,196,160
S_ m0,s51,3072
ds_read2st64_b32 v[134:135], v5 offset0:38 offset1:39
A_ v106,v22,v106
Mvaa 36,192,120
ds_read2st64_b32 v[124:125], v5 offset0:32 offset1:33
A_ v107,v23,v107
s_waitcnt vmcnt(11)
Maaa 100,70,20
L_ v7,24,s55,0
Maaa 140,68,20
W_ 7
ds_read2st64_b32 v[126:127], v5 offset0:34 offset1:35
A_ v112,v24,v112
Maaa 104,70,24
S_ s55,s71,163840
ds_read2st64_b32 v[116:117], v5 offset0:28 offset1:29
A_ v113,v25,v113
Maaa 144,68,24
S_ m0,s51,4096
ds_read2st64_b32 v[118:119], v5 offset0:30 offset1:31
A_ v114,v26,v114
Maaa 108,70,28
L_ v7,24,s55,0
Maaa 148,68,28
S_ s55,s71,174080
ds_read2st64_b32 v[108:109], v5 offset0:24 offset1:25
A_ v115,v27,v115
Maaa 112,70,32
W_ 9
ds_read2st64_b32 v[110:111], v5 offset0:26 offset1:27
A_ v120,v28,v120
Maaa 152,68,32
S_ m0,s51,5120
ds_read2st64_b32 v[100:101], v5 offset0:20 offset1:21
A_ v121,v29,v121
Maaa 116,70,36
L_ v7,24,s55,0
Maaa 156,68,36
S_ s79,s6,7
ds_read2st64_b32 v[102:103], v5 offset0:22 offset1:23
A_ v122,v30,v122
Maaa 224,70,60
s_and_b32 s79, s79, 7
A_ v123,v31,v123
Maaa 228,68,60
W_ 10
A_ v128,v32,v128
A_ v129,v33,v129
Maaa 232,70,64
s_lshl_b32 s79, s79, 7
A_ v130,v34,v130
A_ v131,v35,v131
Maaa 236,68,64
W_ 9
A_ v132,v52,v132
W_ 8
Zvav 56,180,136
S_ s71,s72,s79
Ta 192,v1,49152
Ta 194,v1,51200
Mvav 56,184,156
S_ s55,s71,491520
A_ v133,v53,v133
A_ v134,v54,v134
Mvav 56,188,176
S_ m0,s51,12288
Ta 196,v1,53248
Ta 198,v1,55296
Zvav 60,180,140
L_ v7,24,s55,0
Mvav 60,184,160
S_ s55,s71,501760
Ta 220,v1,57344
Ta 222,v1,59392
Mvav 60,188,180
S_ m0,s51,13312
A_ v135,v55,v135
Zvav 64,180,144
W_ 12
A_ v124,v48,v124
A_ v125,v49,v125
Mvav 64,184,164
L_ v7,24,s55,0
Mvav 64,188,184
S_ s55,s71,512000
A_ v126,v50,v126
A_ v127,v51,v127
Zvav 68,180,148
W_ 10
A_ v116,v44,v116
A_ v117,v45,v117
Mvav 68,184,168
S_ m0,s51,14336
A_ v118,v46,v118
A_ v119,v47,v119
Mvav 68,188,188
L_ v7,24,s55,0
Zvav 72,180,152
W_ 8
A_ v108,v40,v108
A_ v109,v41,v109
Mvav 72,184,172
S_ s55,s71,522240
A_ v110,v42,v110
A_ v111,v43,v111
Mvav 72,188,192
W_ 0
A_ v100,v36,v100
Zvav 92,220,192
S_ m0,s51,15360
Ta 180,v2,49152
Ta 182,v2,51200
Mvav 92,196,172
L_ v7,24,s55,0
Mvav 92,192,152
S_ s55,s71,532480
Ta 184,v2,53248
Ta 186,v2,55296
Zvav 88,220,188
S_ m0,s51,16384
A_ v101,v37,v101
A_ v102,v38,v102
Mvav 88,196,168
s_nop 0
Ta 188,v2,57344
Ta 190,v2,59392
Mvav 88,192,148
L_ v7,24,s55,0
Zvav 84,220,184
S_ s55,s71,542720
A_ v103,v39,v103
C_ 131,130,131
Mvav 84,196,164
S_ m0,s51,17408
C_ 130,128,129
C_ 132,132,133
Mvav 84,192,144
C_ 133,134,135
Zvav 80,220,180
buffer_store_dwordx4 v[130:133], v6, s[28:31], s73 offen offset:0 sc1
Mvav 80,196,160
s_sub_u32 s73, s73, 4096
Mvav 80,192,140
C_ 123,122,123
C_ 122,120,121
Zvav 76,220,176
C_ 124,124,125
C_ 125,126,127
Mvav 76,196,156
buffer_store_dwordx4 v[122:125], v6, s[28:31], s73 offen offset:3072 sc1
Mvav 76,192,136
W_ 0
C_ 115,114,115
C_ 114,112,113
Zvav 16,180,136
s_nop 0
Ta 192,v3,49152
Ta 194,v3,51200
Mvav 16,184,156
C_ 116,116,117
C_ 117,118,119
Mvav 16,188,176
s_nop 0
Ta 196,v3,53248
Ta 198,v3,55296
Zvav 20,180,140
buffer_store_dwordx4 v[114:117], v6, s[28:31], s73 offen offset:2048 sc1
Mvav 20,184,160
s_nop 0
Ta 220,v3,57344
Ta 222,v3,59392
Mvav 20,188,180
C_ 107,106,107
C_ 106,104,105
Zvav 24,180,144
C_ 108,108,109
C_ 109,110,111
Mvav 24,184,164
buffer_store_dwordx4 v[106:109], v6, s[28:31], s73 offen offset:1024 sc1
Mvav 24,188,184
C_ 99,98,99
C_ 98,96,97
Zvav 28,180,148
C_ 100,100,101
C_ 101,102,103
Mvav 28,184,168
Mvav 28,188,188
buffer_store_dwordx4 v[98:101], v6, s[28:31], s73 offen offset:0 sc1
Zvav 32,180,152
Mvav 32,184,172
Mvav 32,188,192
s_waitcnt vmcnt(32) lgkmcnt(0)
s_barrier
Zvav 52,220,192
s_nop 0
Ta 180,%0,36864
Ta 182,%0,38912
Mvav 52,196,172
L_ v7,24,s55,0
Mvav 52,192,152
S_ s55,s71,614400
Ta 184,%0,40960
Ta 186,%0,43008
Zvav 48,220,188
S_ m0,s51,49152
Mvav 48,196,168
s_nop 0
Ta 188,%0,45056
Ta 190,%0,47104
Mvav 48,192,148
L_ v7,24,s55,0
Zvav 44,220,184
S_ s55,s71,624640
Mvav 44,196,164
S_ m0,s51,50176
Mvav 44,192,144
Zvav 40,220,180
L_ v7,24,s55,0
Mvav 40,196,160
S_ s55,s71,634880
Mvav 40,192,140
S_ m0,s51,51200
Zvav 36,220,176
Mvav 36,196,156
L_ v7,24,s55,0
Mvav 36,192,136
W_ 0
S_ s55,s71,645120
Mvav 56,180,196
S_ m0,s51,52224
Ta 192,v1,36864
Ta 194,v1,38912
Mvav 56,184,216
Mvav 56,188,236
s_nop 0
Ta 196,v1,40960
Ta 198,v1,43008
Mvav 60,180,200
L_ v7,24,s55,0
Mvav 60,184,220
S_ s55,s71,655360
Ta 220,v1,45056
Ta 222,v1,47104
Mvav 60,188,240
S_ m0,s51,53248
Mvav 64,180,204
Mvav 64,184,224
L_ v7,24,s55,0
Mvav 64,188,244
S_ s55,s71,665600
Mvav 68,180,208
S_ m0,s51,54272
Mvav 68,184,228
Mvav 68,188,248
L_ v7,24,s55,0
Mvav 72,180,212
Mvav 72,184,232
Mvav 72,188,252
W_ 0
Mvav 92,220,252
s_nop 0
Ta 180,v2,36864
Ta 182,v2,38912
Mvav 92,196,232
Mvav 92,192,212
s_nop 0
Ta 184,v2,40960
Ta 186,v2,43008
Mvav 88,220,248
s_waitcnt vmcnt(23)
Mvav 88,196,228
s_nop 0
Ta 188,v2,45056
Ta 190,v2,47104
Mvav 88,192,208
v_cmp_eq_u32 vcc, s62, v11
Mvav 84,220,244
s_mul_i32 s76, s75, 204800
Mvav 84,196,224
s_bitcmp1_b32 vcc_lo, s75
s_cbranch_scc0 .Lspin2
.Lspun2:
Mvav 84,192,204
S_ s56,s76,s84
Mvav 80,220,240
buffer_load_dwordx4 a[20:23], v6, s[28:31], s56 offen offset:0 sc1
Mvav 80,196,220
Mvav 80,192,200
Mvav 76,220,236
Mvav 76,196,216
buffer_load_dwordx4 a[24:27], v6, s[28:31], s56 offen offset:1024 sc1
Mvav 76,192,196
W_ 0
Mvav 16,180,196
s_nop 0
Ta 192,v3,36864
Ta 194,v3,38912
Mvav 16,184,216
Mvav 16,188,236
s_nop 0
Ta 196,v3,40960
Ta 198,v3,43008
Mvav 20,180,200
buffer_load_dwordx4 a[28:31], v6, s[28:31], s56 offen offset:2048 sc1
Mvav 20,184,220
s_nop 0
Ta 220,v3,45056
Ta 222,v3,47104
Mvav 20,188,240
Mvav 24,180,204
Mvav 24,184,224
buffer_load_dwordx4 a[32:35], v6, s[28:31], s56 offen offset:3072 sc1
Mvav 24,188,244
S_ s56,s56,4096
Mvav 28,180,208
Mvav 28,184,228
Mvav 28,188,248
buffer_load_dwordx4 a[36:39], v6, s[28:31], s56 offen offset:0 sc1
Mvav 32,180,212
S_ s56,s76,s74
Mvav 32,184,232
Mvav 32,188,252
s_waitcnt vmcnt(30) lgkmcnt(0)
s_barrier
Mvav 52,220,252
S_ s55,s71,0
Ta 180,%0,24576
Ta 182,%0,26624
Mvav 52,196,232
buffer_load_dwordx4 a[60:63], v6, s[28:31], s56 offen offset:0 sc1
Mvav 52,192,212
S_ s56,s76,s82
Ta 184,%0,28672
Ta 186,%0,30720
Mvav 48,220,248
S_ m0,s51,36864
Mvav 48,196,228
s_nop 0
Ta 188,%0,32768
Ta 190,%0,34816
Mvav 48,192,208
buffer_load_dwordx4 a[64:67], v6, s[28:31], s56 offen offset:0 sc1
Mvav 44,220,244
Mvav 44,196,224
Mvav 44,192,204
Mvav 40,220,240
L_ v7,24,s55,0
Mvav 40,196,220
S_ s55,s71,10240
Mvav 40,192,200
S_ m0,s51,37888
Mvav 36,220,236
Mvav 36,196,216
L_ v7,24,s55,0
Mvav 36,192,196
W_ 0
S_ s55,s71,20480
Mvaa 56,180,0
S_ m0,s51,38912
Ta 192,v1,24576
Ta 194,v1,26624
Mvaa 56,184,40
Mvaa 56,188,80
s_nop 0
Ta 196,v1,28672
Ta 198,v1,30720
Mvaa 60,180,4
L_ v7,24,s55,0
Mvaa 60,184,44
S_ s55,s71,30720
Ta 220,v1,32768
Ta 222,v1,34816
Mvaa 60,188,84
S_ m0,s51,39936
Mvaa 64,180,8
Mvaa 64,184,48
L_ v7,24,s55,0
Mvaa 64,188,88
S_ s55,s71,40960
Mvaa 68,180,12
S_ m0,s51,40960
Mvaa 68,184,52
Mvaa 68,188,240
L_ v7,24,s55,0
Mvaa 72,180,16
S_ s55,s71,51200
Mvaa 72,184,56
S_ m0,s51,41984
Mvaa 72,188,244
W_ 0
Mvaa 92,220,244
s_nop 0
Ta 180,v2,24576
Ta 182,v2,26624
Mvaa 92,196,56
L_ v7,24,s55,0
Mvaa 92,192,16
s_nop 0
Ta 184,v2,28672
Ta 186,v2,30720
Mvaa 88,220,240
Mvaa 88,196,52
s_nop 0
Ta 188,v2,32768
Ta 190,v2,34816
Mvaa 88,192,12
Mvaa 84,220,88
Mvaa 84,196,48
Mvaa 84,192,8
Mvaa 80,220,84
Mvaa 80,196,44
Mvaa 80,192,4
Mvaa 76,220,80
Mvaa 76,196,40
Mvaa 76,192,0
W_ 0
Mvaa 16,180,0
s_nop 0
Ta 192,v3,24576
Ta 194,v3,26624
Mvaa 16,184,40
Mvaa 16,188,80
s_nop 0
Ta 196,v3,28672
Ta 198,v3,30720
Mvaa 20,180,4
Mvaa 20,184,44
s_nop 0
Ta 220,v3,32768
Ta 222,v3,34816
Mvaa 20,188,84
Mvaa 24,180,8
Mvaa 24,184,48
Mvaa 24,188,88
Mvaa 28,180,12
Mvaa 28,184,52
Mvaa 28,188,240
Mvaa 32,180,16
Mvaa 32,184,56
Mvaa 32,188,244
s_waitcnt vmcnt(30) lgkmcnt(0)
s_barrier
Mvaa 52,220,244
s_nop 0
Ta 180,%0,0
Ta 182,%0,2048
Mvaa 52,196,56
Mvaa 52,192,16
s_nop 0
Ta 184,%0,4096
Ta 186,%0,6144
Mvaa 48,220,240
Mvaa 48,196,52
s_nop 0
Ta 188,%0,8192
Ta 190,%0,10240
Mvaa 48,192,12
Mvaa 44,220,88
Mvaa 44,196,48
Mvaa 44,192,8
Mvaa 40,220,84
Mvaa 40,196,44
Mvaa 40,192,4
Mvaa 36,220,80
Mvaa 36,196,40
Mvaa 36,192,0
W_ 0
Mvaa 56,180,120
S_ s81,s6,6
Ta 192,v1,0
Ta 194,v1,2048
Mvaa 56,184,160
s_and_b32 s81, s81, 7
Mvaa 56,188,200
s_mul_i32 s81, s81, 20480
Ta 196,v1,4096
Ta 198,v1,6144
Mvaa 60,180,124
s_mov_b32 m0, s52
S_ s73,s81,s78
Mvaa 60,184,164
S_ s73,s73,4096
Ta 220,v1,8192
Ta 222,v1,10240
Mvaa 60,188,204
s_nop 0
Y_ 56,0
Y_ 57,256
Mvaa 64,180,128
s_nop 0
Y_ 58,512
Y_ 59,768
Mvaa 64,184,168
Mvaa 64,188,208
s_nop 0
Y_ 60,1024
Y_ 61,1280
Mvaa 68,180,132
s_nop 0
Y_ 62,1536
Y_ 63,1792
Mvaa 68,184,172
Mvaa 68,188,212
s_nop 0
Y_ 64,2048
Y_ 65,2304
Mvaa 72,180,136
s_nop 0
Y_ 66,2560
Y_ 67,2816
Mvaa 72,184,176
Mvaa 72,188,216
W_ 12
Y_ 68,3072
Y_ 69,3328
Mvaa 92,220,216
.Lpad_o_9:
s_cbranch_execz .Lhop_o_r
Ta 180,v2,0
Ta 182,v2,2048
Mvaa 92,196,176
s_nop 0
Y_ 70,3584
Y_ 71,3840
Mvaa 92,192,136
s_nop 0
Ta 184,v2,4096
Ta 186,v2,6144
Mvaa 88,220,212
s_nop 0
Y_ 72,4096
Y_ 73,4352
Mvaa 88,196,172
S_ s83,s6,5
Ta 188,v2,8192
Ta 190,v2,10240
Mvaa 88,192,132
s_and_b32 s83, s83, 7
Y_ 74,4608
Y_ 75,4864
Mvaa 84,220,208
s_lshl_b32 s83, s83, 2
Y_ 92,9216
Y_ 93,9472
Mvaa 84,196,168
s_nop 0
Y_ 94,9728
Y_ 95,9984
Mvaa 84,192,128
s_nop 0
Y_ 88,8192
Y_ 89,8448
Mvaa 80,220,204
s_nop 0
Y_ 90,8704
Y_ 91,8960
Mvaa 80,196,164
v_cmp_eq_u32_e64 s[36:37], s83, v8
Mvaa 80,192,124
s_nop 0
Y_ 84,7168
Y_ 85,7424
Mvaa 76,220,200
s_nop 0
Y_ 86,7680
Y_ 87,7936
Mvaa 76,196,160
v_mov_b32 v10, 0x7fff0000
Mvaa 76,192,120
W_ 14
Y_ 80,6144
Y_ 81,6400
Mvaa 16,180,120
s_nop 0
Ta 192,v3,0
Ta 194,v3,2048
Mvaa 16,184,160
s_nop 0
Y_ 82,6656
Y_ 83,6912
Mvaa 16,188,200
s_nop 0
Ta 196,v3,4096
Ta 198,v3,6144
Mvaa 20,180,124
s_nop 0
Y_ 76,5120
Y_ 77,5376
Mvaa 20,184,164
s_nop 0
Ta 220,v3,8192
Ta 222,v3,10240
Mvaa 20,188,204
s_nop 0
Y_ 78,5632
Y_ 79,5888
Mvaa 24,180,128
v_cndmask_b32_e64 v10, v10, v9, s[36:37]
Mvaa 24,184,168
Mvaa 24,188,208
s_waitcnt vmcnt(19) lgkmcnt(0)
s_barrier
Mvaa 28,180,132
buffer_store_dword a255, v10, s[92:95], 0 offen offset:384 sc1
Mvaa 28,184,172
s_sub_u32 s75, s6, 4
ds_read2st64_b32 v[96:97], v5 offset0:0 offset1:1
S_ s55,s71,122880
Mvaa 28,188,212
s_and_b32 s75, s75, 7
ds_read2st64_b32 v[98:99], v5 offset0:2 offset1:3
S_ m0,s51,24576
Mvaa 32,180,136
ds_read2st64_b32 v[104:105], v5 offset0:4 offset1:5
Mvaa 32,184,176
buffer_load_dword v11, v8, s[92:95], s96 offen offset:384 sc1
Mvaa 32,188,216
ds_read2st64_b32 v[106:107], v5 offset0:6 offset1:7
Mvaa 52,220,216
s_nop 0
Ta 180,%0,12288
Ta 182,%0,14336
Mvaa 52,196,176
ds_read2st64_b32 v[112:113], v5 offset0:8 offset1:9
Mvaa 52,192,136
W_ 5
Ta 184,%0,16384
Ta 186,%0,18432
Mvaa 48,220,212
L_ v7,24,s55,0
Mvaa 48,196,172
S_ s55,s71,133120
Ta 188,%0,20480
Ta 190,%0,22528
Mvaa 48,192,132
)A"
R"A(S_ m0,s51,25600
ds_read2st64_b32 v[114:115], v5 offset0:10 offset1:11
A_ v96,v16,v96
Mvaa 44,220,208
ds_read2st64_b32 v[120:121], v5 offset0:12 offset1:13
A_ v97,v17,v97
Mvaa 44,196,168
L_ v7,24,s55,0
Mvaa 44,192,128
S_ s55,s71,143360
ds_read2st64_b32 v[122:123], v5 offset0:14 offset1:15
A_ v98,v18,v98
Mvaa 40,220,204
S_ m0,s51,26624
ds_read2st64_b32 v[128:129], v5 offset0:16 offset1:17
A_ v99,v19,v99
Mvaa 40,196,164
W_ 11
ds_read2st64_b32 v[130:131], v5 offset0:18 offset1:19
A_ v104,v20,v104
Mvaa 40,192,124
L_ v7,24,s55,0
Mvaa 36,220,200
S_ s55,s71,153600
ds_read2st64_b32 v[132:133], v5 offset0:36 offset1:37
A_ v105,v21,v105
Mvaa 36,196,160
S_ m0,s51,27648
ds_read2st64_b32 v[134:135], v5 offset0:38 offset1:39
A_ v106,v22,v106
Mvaa 36,192,120
ds_read2st64_b32 v[124:125], v5 offset0:32 offset1:33
A_ v107,v23,v107
s_waitcnt vmcnt(11)
Maaa 100,70,20
L_ v7,24,s55,0
Maaa 140,68,20
W_ 7
ds_read2st64_b32 v[126:127], v5 offset0:34 offset1:35
A_ v112,v24,v112
Maaa 104,70,24
S_ s55,s71,163840
ds_read2st64_b32 v[116:117], v5 offset0:28 offset1:29
A_ v113,v25,v113
Maaa 144,68,24
S_ m0,s51,28672
ds_read2st64_b32 v[118:119], v5 offset0:30 offset1:31
A_ v114,v26,v114
Maaa 108,70,28
L_ v7,24,s55,0
Maaa 148,68,28
S_ s55,s71,174080
ds_read2st64_b32 v[108:109], v5 offset0:24 offset1:25
A_ v115,v27,v115
Maaa 112,70,32
W_ 9
ds_read2st64_b32 v[110:111], v5 offset0:26 offset1:27
A_ v120,v28,v120
Maaa 152,68,32
S_ m0,s51,29696
ds_read2st64_b32 v[100:101], v5 offset0:20 offset1:21
A_ v121,v29,v121
Maaa 116,70,36
L_ v7,24,s55,0
Maaa 156,68,36
S_ s79,s6,8
ds_read2st64_b32 v[102:103], v5 offset0:22 offset1:23
A_ v122,v30,v122
Maaa 224,70,60
s_and_b32 s79, s79, 7
A_ v123,v31,v123
Maaa 228,68,60
W_ 10
A_ v128,v32,v128
A_ v129,v33,v129
Maaa 232,70,64
s_lshl_b32 s79, s79, 7
A_ v130,v34,v130
A_ v131,v35,v131
Maaa 236,68,64
W_ 9
A_ v132,v52,v132
W_ 8
Zvav 56,180,136
S_ s71,s72,s79
Ta 192,v1,12288
Ta 194,v1,14336
Mvav 56,184,156
S_ s55,s71,491520
A_ v133,v53,v133
A_ v134,v54,v134
Mvav 56,188,176
S_ m0,s51,0
Ta 196,v1,16384
Ta 198,v1,18432
Zvav 60,180,140
L_ v7,24,s55,0
Mvav 60,184,160
S_ s55,s71,501760
Ta 220,v1,20480
Ta 222,v1,22528
Mvav 60,188,180
S_ m0,s51,1024
A_ v135,v55,v135
Zvav 64,180,144
W_ 12
A_ v124,v48,v124
A_ v125,v49,v125
Mvav 64,184,164
L_ v7,24,s55,0
Mvav 64,188,184
S_ s55,s71,512000
A_ v126,v50,v126
A_ v127,v51,v127
Zvav 68,180,148
W_ 10
A_ v116,v44,v116
A_ v117,v45,v117
Mvav 68,184,168
S_ m0,s51,2048
A_ v118,v46,v118
A_ v119,v47,v119
Mvav 68,188,188
L_ v7,24,s55,0
Zvav 72,180,152
W_ 8
A_ v108,v40,v108
A_ v109,v41,v109
Mvav 72,184,172
S_ s55,s71,522240
A_ v110,v42,v110
A_ v111,v43,v111
Mvav 72,188,192
W_ 0
A_ v100,v36,v100
Zvav 92,220,192
S_ m0,s51,3072
Ta 180,v2,12288
Ta 182,v2,14336
Mvav 92,196,172
L_ v7,24,s55,0
Mvav 92,192,152
S_ s55,s71,532480
Ta 184,v2,16384
Ta 186,v2,18432
Zvav 88,220,188
S_ m0,s51,4096
A_ v101,v37,v101
A_ v102,v38,v102
Mvav 88,196,168
s_nop 0
Ta 188,v2,20480
Ta 190,v2,22528
Mvav 88,192,148
L_ v7,24,s55,0
Zvav 84,220,184
S_ s55,s71,542720
A_ v103,v39,v103
C_ 131,130,131
Mvav 84,196,164
S_ m0,s51,5120
C_ 130,128,129
C_ 132,132,133
Mvav 84,192,144
C_ 133,134,135
Zvav 80,220,180
buffer_store_dwordx4 v[130:133], v6, s[28:31], s73 offen offset:0 sc1
Mvav 80,196,160
s_sub_u32 s73, s73, 4096
Mvav 80,192,140
C_ 123,122,123
C_ 122,120,121
Zvav 76,220,176
C_ 124,124,125
C_ 125,126,127
Mvav 76,196,156
buffer_store_dwordx4 v[122:125], v6, s[28:31], s73 offen offset:3072 sc1
Mvav 76,192,136
W_ 0
C_ 115,114,115
C_ 114,112,113
Zvav 16,180,136
s_nop 0
Ta 192,v3,12288
Ta 194,v3,14336
Mvav 16,184,156
C_ 116,116,117
C_ 117,118,119
Mvav 16,188,176
s_nop 0
Ta 196,v3,16384
Ta 198,v3,18432
Zvav 20,180,140
buffer_store_dwordx4 v[114:117], v6, s[28:31], s73 offen offset:2048 sc1
Mvav 20,184,160
s_nop 0
Ta 220,v3,20480
Ta 222,v3,22528
Mvav 20,188,180
C_ 107,106,107
C_ 106,104,105
Zvav 24,180,144
C_ 108,108,109
C_ 109,110,111
Mvav 24,184,164
buffer_store_dwordx4 v[106:109], v6, s[28:31], s73 offen offset:1024 sc1
Mvav 24,188,184
C_ 99,98,99
C_ 98,96,97
Zvav 28,180,148
C_ 100,100,101
C_ 101,102,103
Mvav 28,184,168
Mvav 28,188,188
buffer_store_dwordx4 v[98:101], v6, s[28:31], s73 offen offset:0 sc1
Zvav 32,180,152
Mvav 32,184,172
Mvav 32,188,192
s_waitcnt vmcnt(31) lgkmcnt(0)
s_barrier
Zvav 52,220,192
s_nop 0
Ta 180,%0,49152
Ta 182,%0,51200
Mvav 52,196,172
L_ v7,24,s55,0
Mvav 52,192,152
S_ s55,s71,614400
Ta 184,%0,53248
Ta 186,%0,55296
Zvav 48,220,188
S_ m0,s51,12288
Mvav 48,196,168
s_nop 0
Ta 188,%0,57344
Ta 190,%0,59392
Mvav 48,192,148
L_ v7,24,s55,0
Zvav 44,220,184
S_ s55,s71,624640
Mvav 44,196,164
S_ m0,s51,13312
Mvav 44,192,144
Zvav 40,220,180
L_ v7,24,s55,0
Mvav 40,196,160
S_ s55,s71,634880
Mvav 40,192,140
S_ m0,s51,14336
Zvav 36,220,176
Mvav 36,196,156
L_ v7,24,s55,0
Mvav 36,192,136
W_ 0
S_ s55,s71,645120
Mvav 56,180,196
S_ m0,s51,15360
Ta 192,v1,49152
Ta 194,v1,51200
Mvav 56,184,216
Mvav 56,188,236
s_nop 0
Ta 196,v1,53248
Ta 198,v1,55296
Mvav 60,180,200
L_ v7,24,s55,0
Mvav 60,184,220
S_ s55,s71,655360
Ta 220,v1,57344
Ta 222,v1,59392
Mvav 60,188,240
S_ m0,s51,16384
Mvav 64,180,204
Mvav 64,184,224
L_ v7,24,s55,0
Mvav 64,188,244
S_ s55,s71,665600
Mvav 68,180,208
S_ m0,s51,17408
Mvav 68,184,228
Mvav 68,188,248
L_ v7,24,s55,0
Mvav 72,180,212
Mvav 72,184,232
Mvav 72,188,252
W_ 0
Mvav 92,220,252
s_nop 0
Ta 180,v2,49152
Ta 182,v2,51200
Mvav 92,196,232
Mvav 92,192,212
s_nop 0
Ta 184,v2,53248
Ta 186,v2,55296
Mvav 88,220,248
s_waitcnt vmcnt(23)
Mvav 88,196,228
s_nop 0
Ta 188,v2,57344
Ta 190,v2,59392
Mvav 88,192,208
v_cmp_eq_u32 vcc, s62, v11
Mvav 84,220,244
s_mul_i32 s76, s75, 204800
Mvav 84,196,224
s_bitcmp1_b32 vcc_lo, s75
s_cbranch_scc0 .Lspin3
.Lspun3:
Mvav 84,192,204
S_ s56,s76,s84
Mvav 80,220,240
buffer_load_dwordx4 a[20:23], v6, s[28:31], s56 offen offset:0 sc1
Mvav 80,196,220
Mvav 80,192,200
Mvav 76,220,236
Mvav 76,196,216
buffer_load_dwordx4 a[24:27], v6, s[28:31], s56 offen offset:1024 sc1
Mvav 76,192,196
W_ 0
Mvav 16,180,196
s_nop 0
Ta 192,v3,49152
Ta 194,v3,51200
Mvav 16,184,216
Mvav 16,188,236
s_nop 0
Ta 196,v3,53248
Ta 198,v3,55296
Mvav 20,180,200
buffer_load_dwordx4 a[28:31], v6, s[28:31], s56 offen offset:2048 sc1
Mvav 20,184,220
s_nop 0
Ta 220,v3,57344
Ta 222,v3,59392
Mvav 20,188,240
Mvav 24,180,204
Mvav 24,184,224
buffer_load_dwordx4 a[32:35], v6, s[28:31], s56 offen offset:3072 sc1
Mvav 24,188,244
S_ s56,s56,4096
Mvav 28,180,208
Mvav 28,184,228
Mvav 28,188,248
buffer_load_dwordx4 a[36:39], v6, s[28:31], s56 offen offset:0 sc1
Mvav 32,180,212
S_ s56,s76,s74
Mvav 32,184,232
Mvav 32,188,252
s_waitcnt vmcnt(30) lgkmcnt(0)
s_barrier
Mvav 52,220,252
S_ s55,s71,0
Ta 180,%0,36864
Ta 182,%0,38912
Mvav 52,196,232
buffer_load_dwordx4 a[60:63], v6, s[28:31], s56 offen offset:0 sc1
Mvav 52,192,212
S_ s56,s76,s82
Ta 184,%0,40960
Ta 186,%0,43008
Mvav 48,220,248
S_ m0,s51,49152
Mvav 48,196,228
s_nop 0
Ta 188,%0,45056
Ta 190,%0,47104
Mvav 48,192,208
buffer_load_dwordx4 a[64:67], v6, s[28:31], s56 offen offset:0 sc1
Mvav 44,220,244
s_sub_u32 s75, s6, 5
Mvav 44,196,224
s_and_b32 s75, s75, 7
Mvav 44,192,204
Mvav 40,220,240
buffer_load_dword v11, v8, s[92:95], s96 offen offset:384 sc1
Mvav 40,196,220
Mvav 40,192,200
Mvav 36,220,236
Mvav 36,196,216
L_ v7,24,s55,0
Mvav 36,192,196
W_ 0
S_ s55,s71,10240
Mvaa 56,180,0
S_ m0,s51,50176
Ta 192,v1,36864
Ta 194,v1,38912
Mvaa 56,184,40
Mvaa 56,188,80
s_nop 0
Ta 196,v1,40960
Ta 198,v1,43008
Mvaa 60,180,4
L_ v7,24,s55,0
Mvaa 60,184,44
S_ s55,s71,20480
Ta 220,v1,45056
Ta 222,v1,47104
Mvaa 60,188,84
S_ m0,s51,51200
Mvaa 64,180,8
Mvaa 64,184,48
L_ v7,24,s55,0
Mvaa 64,188,88
S_ s55,s71,30720
Mvaa 68,180,12
S_ m0,s51,52224
Mvaa 68,184,52
Mvaa 68,188,240
L_ v7,24,s55,0
Mvaa 72,180,16
S_ s55,s71,40960
Mvaa 72,184,56
S_ m0,s51,53248
Mvaa 72,188,244
W_ 0
Mvaa 92,220,244
s_nop 0
Ta 180,v2,36864
Ta 182,v2,38912
Mvaa 92,196,56
L_ v7,24,s55,0
Mvaa 92,192,16
S_ s55,s71,51200
Ta 184,v2,40960
Ta 186,v2,43008
Mvaa 88,220,240
S_ m0,s51,54272
Mvaa 88,196,52
s_nop 0
Ta 188,v2,45056
Ta 190,v2,47104
Mvaa 88,192,12
L_ v7,24,s55,0
Mvaa 84,220,88
Mvaa 84,196,48
Mvaa 84,192,8
Mvaa 80,220,84
Mvaa 80,196,44
S_ s83,s6,6
Mvaa 80,192,4
s_and_b32 s83, s83, 7
Mvaa 76,220,80
s_lshl_b32 s83, s83, 2
Mvaa 76,196,40
v_cmp_eq_u32_e64 s[36:37], s83, v8
Mvaa 76,192,0
W_ 0
v_mov_b32 v10, 0x7fff0000
Mvaa 16,180,0
s_nop 0
Ta 192,v3,36864
Ta 194,v3,38912
Mvaa 16,184,40
v_cndmask_b32_e64 v10, v10, v9, s[36:37]
Mvaa 16,188,80
.Lpad_o_10:
s_cbranch_execz .Lhop_o_r
Ta 196,v3,40960
Ta 198,v3,43008
Mvaa 20,180,4
Mvaa 20,184,44
s_nop 0
Ta 220,v3,45056
Ta 222,v3,47104
Mvaa 20,188,84
Mvaa 24,180,8
Mvaa 24,184,48
Mvaa 24,188,88
Mvaa 28,180,12
Mvaa 28,184,52
Mvaa 28,188,240
Mvaa 32,180,16
Mvaa 32,184,56
Mvaa 32,188,244
s_waitcnt vmcnt(21) lgkmcnt(0)
s_barrier
Mvaa 52,220,244
s_nop 0
Ta 180,%0,24576
Ta 182,%0,26624
Mvaa 52,196,56
buffer_store_dword a255, v10, s[92:95], 0 offen offset:384 sc1
Mvaa 52,192,16
s_nop 0
Ta 184,%0,28672
Ta 186,%0,30720
Mvaa 48,220,240
Mvaa 48,196,52
s_nop 0
Ta 188,%0,32768
Ta 190,%0,34816
Mvaa 48,192,12
Mvaa 44,220,88
Mvaa 44,196,48
Mvaa 44,192,8
Mvaa 40,220,84
Mvaa 40,196,44
Mvaa 40,192,4
Mvaa 36,220,80
Mvaa 36,196,40
Mvaa 36,192,0
W_ 0
Mvaa 56,180,120
S_ s81,s6,7
Ta 192,v1,24576
Ta 194,v1,26624
Mvaa 56,184,160
s_and_b32 s81, s81, 7
Mvaa 56,188,200
s_mul_i32 s81, s81, 20480
Ta 196,v1,28672
Ta 198,v1,30720
Mvaa 60,180,124
s_mov_b32 m0, s52
S_ s73,s81,s78
Mvaa 60,184,164
S_ s73,s73,4096
Ta 220,v1,32768
Ta 222,v1,34816
Mvaa 60,188,204
s_nop 0
Y_ 56,0
Y_ 57,256
Mvaa 64,180,128
s_nop 0
Y_ 58,512
Y_ 59,768
Mvaa 64,184,168
Mvaa 64,188,208
s_nop 0
Y_ 60,1024
Y_ 61,1280
Mvaa 68,180,132
s_nop 0
Y_ 62,1536
Y_ 63,1792
Mvaa 68,184,172
Mvaa 68,188,212
s_nop 0
Y_ 64,2048
Y_ 65,2304
Mvaa 72,180,136
s_nop 0
Y_ 66,2560
Y_ 67,2816
Mvaa 72,184,176
Mvaa 72,188,216
W_ 12
Y_ 68,3072
Y_ 69,3328
Mvaa 92,220,216
s_nop 0
Ta 180,v2,24576
Ta 182,v2,26624
Mvaa 92,196,176
s_nop 0
Y_ 70,3584
Y_ 71,3840
Mvaa 92,192,136
s_nop 0
Ta 184,v2,28672
Ta 186,v2,30720
Mvaa 88,220,212
s_nop 0
Y_ 72,4096
Y_ 73,4352
Mvaa 88,196,172
s_nop 0
Ta 188,v2,32768
Ta 190,v2,34816
Mvaa 88,192,132
s_nop 0
Y_ 74,4608
Y_ 75,4864
Mvaa 84,220,208
s_nop 0
Y_ 92,9216
Y_ 93,9472
Mvaa 84,196,168
s_nop 0
Y_ 94,9728
Y_ 95,9984
Mvaa 84,192,128
s_nop 0
Y_ 88,8192
Y_ 89,8448
Mvaa 80,220,204
s_nop 0
Y_ 90,8704
Y_ 91,8960
Mvaa 80,196,164
Mvaa 80,192,124
s_nop 0
Y_ 84,7168
Y_ 85,7424
Mvaa 76,220,200
s_nop 0
Y_ 86,7680
Y_ 87,7936
Mvaa 76,196,160
Mvaa 76,192,120
W_ 14
Y_ 80,6144
Y_ 81,6400
Mvaa 16,180,120
s_nop 0
Ta 192,v3,24576
Ta 194,v3,26624
Mvaa 16,184,160
s_nop 0
Y_ 82,6656
Y_ 83,6912
Mvaa 16,188,200
s_nop 0
Ta 196,v3,28672
Ta 198,v3,30720
Mvaa 20,180,124
s_nop 0
Y_ 76,5120
Y_ 77,5376
Mvaa 20,184,164
s_nop 0
Ta 220,v3,32768
Ta 222,v3,34816
Mvaa 20,188,204
s_nop 0
Y_ 78,5632
Y_ 79,5888
Mvaa 24,180,128
Mvaa 24,184,168
Mvaa 24,188,208
s_waitcnt vmcnt(21) lgkmcnt(0)
s_barrier
Mvaa 28,180,132
S_ s55,s71,122880
ds_read2st64_b32 v[96:97], v5 offset0:0 offset1:1
Mvaa 28,184,172
S_ m0,s51,36864
ds_read2st64_b32 v[98:99], v5 offset0:2 offset1:3
Mvaa 28,188,212
L_ v7,24,s55,0
Mvaa 32,180,136
S_ s55,s71,133120
ds_read2st64_b32 v[104:105], v5 offset0:4 offset1:5
Mvaa 32,184,176
s_waitcnt vmcnt(8)
ds_read2st64_b32 v[106:107], v5 offset0:6 offset1:7
S_ m0,s51,37888
Mvaa 32,188,216
ds_read2st64_b32 v[112:113], v5 offset0:8 offset1:9
v_cmp_eq_u32 vcc, s62, v11
Mvaa 52,220,216
s_mul_i32 s76, s75, 204800
Ta 180,%0,0
Ta 182,%0,2048
Mvaa 52,196,176
L_ v7,24,s55,0
Mvaa 52,192,136
W_ 5
Ta 184,%0,4096
Ta 186,%0,6144
Mvaa 48,220,212
s_bitcmp1_b32 vcc_lo, s75
s_cbranch_scc0 .Lspin4
.Lspun4:
ds_read2st64_b32 v[114:115], v5 offset0:10 offset1:11
Mvaa 48,196,172
S_ s55,s71,143360
Ta 188,%0,8192
Ta 190,%0,10240
Mvaa 48,192,132
S_ m0,s51,38912
ds_read2st64_b32 v[120:121], v5 offset0:12 offset1:13
A_ v96,v16,v96
Mvaa 44,220,208
L_ v7,24,s55,0
Mvaa 44,196,168
S_ s55,s71,153600
ds_read2st64_b32 v[122:123], v5 offset0:14 offset1:15
A_ v97,v17,v97
Mvaa 44,192,128
S_ m0,s51,39936
ds_read2st64_b32 v[128:129], v5 offset0:16 offset1:17
A_ v98,v18,v98
Mvaa 40,220,204
ds_read2st64_b32 v[130:131], v5 offset0:18 offset1:19
A_ v99,v19,v99
Mvaa 40,196,164
L_ v7,24,s55,0
Mvaa 40,192,124
W_ 12
ds_read2st64_b32 v[132:133], v5 offset0:36 offset1:37
A_ v104,v20,v104
Mvaa 36,220,200
S_ s55,s71,163840
ds_read2st64_b32 v[134:135], v5 offset0:38 offset1:39
A_ v105,v21,v105
Mvaa 36,196,160
S_ m0,s51,40960
ds_read2st64_b32 v[124:125], v5 offset0:32 offset1:33
A_ v106,v22,v106
Mvaa 36,192,120
L_ v7,24,s55,0
s_waitcnt vmcnt(13)
Maaa 100,70,20
S_ s55,s71,174080
ds_read2st64_b32 v[126:127], v5 offset0:34 offset1:35
A_ v107,v23,v107
Maaa 140,68,20
W_ 10
ds_read2st64_b32 v[116:117], v5 offset0:28 offset1:29
A_ v112,v24,v112
Maaa 104,70,24
S_ m0,s51,41984
ds_read2st64_b32 v[118:119], v5 offset0:30 offset1:31
A_ v113,v25,v113
Maaa 144,68,24
L_ v7,24,s55,0
Maaa 108,70,28
ds_read2st64_b32 v[108:109], v5 offset0:24 offset1:25
A_ v114,v26,v114
Maaa 148,68,28
ds_read2st64_b32 v[110:111], v5 offset0:26 offset1:27
A_ v115,v27,v115
Maaa 112,70,32
W_ 10
ds_read2st64_b32 v[100:101], v5 offset0:20 offset1:21
A_ v120,v28,v120
Maaa 152,68,32
ds_read2st64_b32 v[102:103], v5 offset0:22 offset1:23
A_ v121,v29,v121
Maaa 116,70,36
A_ v122,v30,v122
A_ v123,v31,v123
Maaa 156,68,36
W_ 10
A_ v128,v32,v128
A_ v129,v33,v129
Maaa 224,70,60
A_ v130,v34,v130
A_ v131,v35,v131
Maaa 228,68,60
W_ 8
A_ v132,v52,v132
A_ v133,v53,v133
Maaa 232,70,64
A_ v134,v54,v134
A_ v135,v55,v135
Maaa 236,68,64
W_ 7
A_ v124,v48,v124
W_ 6
Zvav 56,180,136
s_nop 0
Ta 192,v1,0
Ta 194,v1,2048
Mvav 56,184,156
A_ v125,v49,v125
A_ v126,v50,v126
Mvav 56,188,176
S_ s56,s76,s84
Ta 196,v1,4096
Ta 198,v1,6144
Zvav 60,180,140
buffer_load_dwordx4 a[20:23], v6, s[28:31], s56 offen offset:0 sc1
Mvav 60,184,160
s_nop 0
Ta 220,v1,8192
Ta 222,v1,10240
Mvav 60,188,180
A_ v127,v51,v127
C_ 131,130,131
Zvav 64,180,144
W_ 10
A_ v116,v44,v116
A_ v117,v45,v117
Mvav 64,184,164
buffer_load_dwordx4 a[24:27], v6, s[28:31], s56 offen offset:1024 sc1
Mvav 64,188,184
A_ v118,v46,v118
A_ v119,v47,v119
Zvav 68,180,148
W_ 8
A_ v108,v40,v108
A_ v109,v41,v109
Mvav 68,184,168
A_ v110,v42,v110
A_ v111,v43,v111
Mvav 68,188,188
buffer_load_dwordx4 a[28:31], v6, s[28:31], s56 offen offset:2048 sc1
Zvav 72,180,152
W_ 6
A_ v100,v36,v100
A_ v101,v37,v101
Mvav 72,184,172
A_ v102,v38,v102
A_ v103,v39,v103
Mvav 72,188,192
W_ 0
C_ 130,128,129
C_ 132,132,133
Zvav 92,220,192
s_nop 0
Ta 180,v2,0
Ta 182,v2,2048
Mvav 92,196,172
buffer_load_dwordx4 a[32:35], v6, s[28:31], s56 offen offset:3072 sc1
Mvav 92,192,152
S_ s56,s56,4096
Ta 184,v2,4096
Ta 186,v2,6144
Zvav 88,220,188
C_ 133,134,135
Mvav 88,196,168
s_nop 0
Ta 188,v2,8192
Ta 190,v2,10240
Mvav 88,192,148
buffer_load_dwordx4 a[36:39], v6, s[28:31], s56 offen offset:0 sc1
Zvav 84,220,184
S_ s56,s76,s74
Mvav 84,196,164
Mvav 84,192,144
Zvav 80,220,180
buffer_load_dwordx4 a[60:63], v6, s[28:31], s56 offen offset:0 sc1
Mvav 80,196,160
S_ s56,s76,s82
Mvav 80,192,140
Zvav 76,220,176
Mvav 76,196,156
buffer_load_dwordx4 a[64:67], v6, s[28:31], s56 offen offset:0 sc1
Mvav 76,192,136
W_ 0
s_sub_u32 s75, s6, 6
Maav 100,180,136
s_and_b32 s75, s75, 7
Ta 192,v3,0
Ta 194,v3,2048
Maav 100,184,156
Maav 100,188,176
s_nop 0
Ta 196,v3,4096
Ta 198,v3,6144
Maav 104,180,140
buffer_load_dword v11, v8, s[92:95], s96 offen offset:384 sc1
Maav 104,184,160
s_nop 0
Ta 220,v3,8192
Ta 222,v3,10240
Maav 104,188,180
Maav 108,180,144
Maav 108,184,164
buffer_store_dwordx4 v[130:133], v6, s[28:31], s73 offen offset:0 sc1
Maav 108,188,184
s_sub_u32 s73, s73, 4096
Maav 112,180,148
C_ 123,122,123
C_ 122,120,121
Maav 112,184,168
C_ 124,124,125
C_ 125,126,127
Maav 112,188,188
buffer_store_dwordx4 v[122:125], v6, s[28:31], s73 offen offset:3072 sc1
Maav 116,180,152
C_ 115,114,115
C_ 114,112,113
Maav 116,184,172
C_ 116,116,117
C_ 117,118,119
Maav 116,188,192
s_waitcnt vmcnt(31) lgkmcnt(0)
s_barrier
Maav 156,220,192
s_nop 0
Ta 180,%0,12288
Ta 182,%0,14336
Maav 156,196,172
buffer_store_dwordx4 v[114:117], v6, s[28:31], s73 offen offset:2048 sc1
Maav 156,192,152
s_nop 0
Ta 184,%0,16384
Ta 186,%0,18432
Maav 152,220,188
C_ 107,106,107
C_ 106,104,105
Maav 152,196,168
s_nop 0
Ta 188,%0,20480
Ta 190,%0,22528
Maav 152,192,148
C_ 108,108,109
C_ 109,110,111
Maav 148,220,184
buffer_store_dwordx4 v[106:109], v6, s[28:31], s73 offen offset:1024 sc1
Maav 148,196,164
C_ 99,98,99
C_ 98,96,97
Maav 148,192,144
C_ 100,100,101
C_ 101,102,103
Maav 144,220,180
Maav 144,196,160
buffer_store_dwordx4 v[98:101], v6, s[28:31], s73 offen offset:0 sc1
Maav 144,192,140
Maav 140,220,176
Maav 140,196,156
Maav 140,192,136
W_ 0
Mvav 56,180,196
s_nop 0
Ta 192,v1,12288
Ta 194,v1,14336
Mvav 56,184,216
Mvav 56,188,236
s_nop 0
Ta 196,v1,16384
Ta 198,v1,18432
Mvav 60,180,200
Mvav 60,184,220
s_nop 0
Ta 220,v1,20480
Ta 222,v1,22528
Mvav 60,188,240
v_bfe_u32 v4, v6, 4, 4
Mvav 64,180,204
v_mul_u32_u24 v4, 2560, v4
Mvav 64,184,224
v_bfe_u32 v10, v6, 8, 1
Mvav 64,188,244
v_lshl_add_u32 v4, v10, 5, v4
Mvav 68,180,208
v_lshrrev_b32 v10, 9, v6
Mvav 68,184,228
v_lshl_add_u32 v4, v10, 4, v4
Mvav 68,188,248
Mvav 72,180,212
Mvav 72,184,232
Mvav 72,188,252
W_ 0
Mvav 92,220,252
s_nop 0
Ta 180,v2,12288
Ta 182,v2,14336
Mvav 92,196,232
Mvav 92,192,212
s_nop 0
Ta 184,v2,16384
Ta 186,v2,18432
Mvav 88,220,248
Mvav 88,196,228
s_nop 0
Ta 188,v2,20480
Ta 190,v2,22528
Mvav 88,192,208
Mvav 84,220,244
Mvav 84,196,224
Mvav 84,192,204
Mvav 80,220,240
Mvav 80,196,220
Mvav 80,192,200
Mvav 76,220,236
Mvav 76,196,216
Mvav 76,192,196
W_ 0
Maav 100,180,196
s_nop 0
Ta 192,v3,12288
Ta 194,v3,14336
Maav 100,184,216
Maav 100,188,236
s_nop 0
Ta 196,v3,16384
Ta 198,v3,18432
Maav 104,180,200
Maav 104,184,220
s_nop 0
Ta 220,v3,20480
Ta 222,v3,22528
Maav 104,188,240
Maav 108,180,204
S_ s86,s6,1
Maav 108,184,224
s_and_b32 s86, s86, 7
Maav 108,188,244
s_waitcnt vmcnt(5)
Maav 112,180,208
v_cmp_eq_u32 vcc, s62, v11
Maav 112,184,228
s_mul_i32 s76, s75, 204800
Maav 112,188,248
s_bitcmp1_b32 vcc_lo, s75
s_cbranch_scc0 .Lspin5
.Lspun5:
Maav 116,180,212
s_bitcmp1_b32 vcc_lo, s86
s_cbranch_scc0 .Lspin6
.Lspun6:
Maav 116,184,232
S_ s56,s76,s84
Maav 116,188,252
s_waitcnt vmcnt(20) lgkmcnt(0)
s_barrier
Maav 156,220,252
s_nop 0
Ta 180,%0,49152
Ta 182,%0,51200
Maav 156,196,232
buffer_load_dwordx4 v[16:19], v6, s[28:31], s56 offen offset:0 sc1
Maav 156,192,212
s_nop 0
Ta 184,%0,53248
Ta 186,%0,55296
Maav 152,220,248
Maav 152,196,228
s_nop 0
Ta 188,%0,57344
Ta 190,%0,59392
Maav 152,192,208
buffer_load_dwordx4 v[20:23], v6, s[28:31], s56 offen offset:1024 sc1
Maav 148,220,244
Maav 148,196,224
Maav 148,192,204
Maav 144,220,240
buffer_load_dwordx4 v[24:27], v6, s[28:31], s56 offen offset:2048 sc1
Maav 144,196,220
Maav 144,192,200
Maav 140,220,236
Maav 140,196,216
buffer_load_dwordx4 v[28:31], v6, s[28:31], s56 offen offset:3072 sc1
Maav 140,192,196
W_ 0
S_ s56,s56,4096
Mvaa 56,180,0
s_nop 0
Ta 192,v1,49152
Ta 194,v1,51200
Mvaa 56,184,40
R_ v164,a224
R_ v165,a225
Mvaa 56,188,80
s_nop 0
Ta 196,v1,53248
Ta 198,v1,55296
Mvaa 60,180,4
buffer_load_dwordx4 v[32:35], v6, s[28:31], s56 offen offset:0 sc1
Mvaa 60,184,44
S_ s56,s76,s74
Ta 220,v1,57344
Ta 222,v1,59392
Mvaa 60,188,84
.Lpad_o_11:
s_cbranch_execz .Lhop_o_r
R_ v166,a226
R_ v167,a227
Mvaa 64,180,8
R_ v168,a228
R_ v169,a229
Mvaa 64,184,48
buffer_load_dwordx4 v[36:39], v6, s[28:31], s56 offen offset:0 sc1
Mvaa 64,188,88
S_ s56,s76,s82
R_ v170,a230
R_ v171,a231
Mvaa 68,180,12
R_ v172,a232
R_ v173,a233
Mvaa 68,184,52
R_ v174,a234
R_ v175,a235
Mvaa 68,188,240
buffer_load_dwordx4 v[40:43], v6, s[28:31], s56 offen offset:0 sc1
Mvaa 72,180,16
s_mul_i32 s76, s86, 204800
R_ v176,a236
R_ v177,a237
Mvaa 72,184,56
S_ s80,s76,s84
R_ v178,a238
R_ v179,a239
Mvaa 72,188,244
W_ 0
S_ s56,s76,s74
Mvaa 92,220,244
s_nop 0
Ta 180,v2,49152
Ta 182,v2,51200
Mvaa 92,196,56
buffer_load_dwordx4 v[156:159], v6, s[28:31], s56 offen offset:0 sc1
Mvaa 92,192,16
S_ s56,s76,s82
Ta 184,v2,53248
Ta 186,v2,55296
Mvaa 88,220,240
Mvaa 88,196,52
s_nop 0
Ta 188,v2,57344
Ta 190,v2,59392
Mvaa 88,192,12
buffer_load_dwordx4 v[160:163], v6, s[28:31], s56 offen offset:0 sc1
Mvaa 84,220,88
Mvaa 84,196,48
Mvaa 84,192,8
Mvaa 80,220,84
Mvaa 80,196,44
Mvaa 80,192,4
Mvaa 76,220,80
Mvaa 76,196,40
Mvaa 76,192,0
W_ 0
Maaa 100,180,0
s_nop 0
Ta 192,v3,49152
Ta 194,v3,51200
Maaa 100,184,40
Maaa 100,188,80
s_nop 0
Ta 196,v3,53248
Ta 198,v3,55296
Maaa 104,180,4
Maaa 104,184,44
s_nop 0
Ta 220,v3,57344
Ta 222,v3,59392
Maaa 104,188,84
Maaa 108,180,8
R_ v180,a100
R_ v181,a101
Maaa 108,184,48
R_ v182,a102
R_ v183,a103
Maaa 108,188,88
Maaa 112,180,12
R_ v184,a104
R_ v185,a105
Maaa 112,184,52
R_ v186,a106
R_ v187,a107
Maaa 112,188,240
Maaa 116,180,16
R_ v188,a108
R_ v189,a109
Maaa 116,184,56
R_ v190,a110
R_ v191,a111
Maaa 116,188,244
s_waitcnt vmcnt(22) lgkmcnt(0)
s_barrier
Maaa 156,220,244
s_nop 0
Ta 180,%0,36864
Ta 182,%0,38912
Maaa 156,196,56
R_ v192,a112
R_ v193,a113
Maaa 156,192,16
s_nop 0
Ta 184,%0,40960
Ta 186,%0,43008
Maaa 152,220,240
R_ v194,a114
R_ v195,a115
Maaa 152,196,52
s_nop 0
Ta 188,%0,45056
Ta 190,%0,47104
Maaa 152,192,12
R_ v196,a116
R_ v197,a117
Maaa 148,220,88
R_ v198,a118
R_ v199,a119
Maaa 148,196,48
R_ v216,a156
R_ v217,a157
Maaa 148,192,8
R_ v218,a158
R_ v219,a159
Maaa 144,220,84
R_ v212,a152
R_ v213,a153
Maaa 144,196,44
R_ v214,a154
R_ v215,a155
Maaa 144,192,4
Maaa 140,220,80
R_ v208,a148
R_ v209,a149
Maaa 140,196,40
R_ v210,a150
R_ v211,a151
Maaa 140,192,0
s_waitcnt vmcnt(15)
Mvaa 164,70,60
R_ v204,a144
R_ v205,a145
Mvaa 168,68,60
R_ v206,a146
R_ v207,a147
Mvaa 172,70,64
Mvaa 176,68,64
R_ v200,a140
R_ v201,a141
Mvaa 180,70,20
R_ v202,a142
R_ v203,a143
Mvaa 184,70,24
Mvaa 188,70,28
Mvaa 192,70,32
Mvaa 196,70,36
Mvaa 216,68,36
Mvaa 212,68,32
Mvaa 208,68,28
Mvaa 204,68,24
Mvaa 200,68,20
W_ 0
Mvaa 56,180,120
S_ s56,s76,s84
Ta 192,v1,36864
Ta 194,v1,38912
Mvaa 56,184,160
buffer_load_dwordx4 v[136:139], v6, s[28:31], s56 offen offset:0 sc1
Mvaa 56,188,200
s_nop 0
Ta 196,v1,40960
Ta 198,v1,43008
Mvaa 60,180,124
s_mov_b32 m0, s52
Mvaa 60,184,164
s_nop 0
Ta 220,v1,45056
Ta 222,v1,47104
Mvaa 60,188,204
s_nop 0
Y_ 56,0
Y_ 57,256
Mvaa 64,180,128
s_nop 0
Y_ 58,512
Y_ 59,768
Mvaa 64,184,168
buffer_load_dwordx4 v[140:143], v6, s[28:31], s56 offen offset:1024 sc1
Mvaa 64,188,208
s_nop 0
Y_ 60,1024
Y_ 61,1280
Mvaa 68,180,132
s_nop 0
Y_ 62,1536
Y_ 63,1792
Mvaa 68,184,172
Mvaa 68,188,212
s_nop 0
Y_ 64,2048
Y_ 65,2304
Mvaa 72,180,136
s_nop 0
Y_ 66,2560
Y_ 67,2816
Mvaa 72,184,176
buffer_load_dwordx4 v[144:147], v6, s[28:31], s56 offen offset:2048 sc1
Mvaa 72,188,216
W_ 12
Y_ 68,3072
Y_ 69,3328
Mvaa 92,220,216
s_nop 0
Ta 180,v2,36864
Ta 182,v2,38912
Mvaa 92,196,176
s_nop 0
Y_ 70,3584
Y_ 71,3840
Mvaa 92,192,136
s_nop 0
Ta 184,v2,40960
Ta 186,v2,43008
Mvaa 88,220,212
s_nop 0
Y_ 72,4096
Y_ 73,4352
Mvaa 88,196,172
s_nop 0
Ta 188,v2,45056
Ta 190,v2,47104
Mvaa 88,192,132
s_nop 0
Y_ 74,4608
Y_ 75,4864
Mvaa 84,220,208
s_nop 0
Y_ 92,9216
Y_ 93,9472
Mvaa 84,196,168
s_nop 0
Y_ 94,9728
Y_ 95,9984
Mvaa 84,192,128
s_nop 0
Y_ 88,8192
Y_ 89,8448
Mvaa 80,220,204
s_nop 0
Y_ 90,8704
Y_ 91,8960
Mvaa 80,196,164
buffer_load_dwordx4 v[148:151], v6, s[28:31], s56 offen offset:3072 sc1
Mvaa 80,192,124
S_ s56,s56,4096
Y_ 84,7168
Y_ 85,7424
Mvaa 76,220,200
s_nop 0
Y_ 86,7680
Y_ 87,7936
Mvaa 76,196,160
Mvaa 76,192,120
W_ 14
Y_ 80,6144
Y_ 81,6400
Mvaa 180,180,120
s_nop 0
Ta 192,v3,36864
Ta 194,v3,38912
Mvaa 180,184,160
s_nop 0
Y_ 82,6656
Y_ 83,6912
Mvaa 180,188,200
s_nop 0
Ta 196,v3,40960
Ta 198,v3,43008
Mvaa 184,180,124
s_nop 0
Y_ 76,5120
Y_ 77,5376
Mvaa 184,184,164
s_nop 0
Ta 220,v3,45056
Ta 222,v3,47104
Mvaa 184,188,204
s_nop 0
Y_ 78,5632
Y_ 79,5888
Mvaa 188,180,128
buffer_load_dwordx4 v[152:155], v6, s[28:31], s56 offen offset:0 sc1
Mvaa 188,184,168
Mvaa 188,188,208
s_waitcnt vmcnt(63) lgkmcnt(0)
s_barrier
Mvaa 192,180,132
ds_read2st64_b32 v[96:97], v5 offset0:0 offset1:1
Mvaa 192,184,172
ds_read2st64_b32 v[98:99], v5 offset0:2 offset1:3
Mvaa 192,188,212
ds_read2st64_b32 v[104:105], v5 offset0:4 offset1:5
Mvaa 196,180,136
ds_read2st64_b32 v[106:107], v5 offset0:6 offset1:7
Mvaa 196,184,176
ds_read2st64_b32 v[112:113], v5 offset0:8 offset1:9
Mvaa 196,188,216
ds_read2st64_b32 v[114:115], v5 offset0:10 offset1:11
Mvaa 216,220,216
ds_read2st64_b32 v[120:121], v5 offset0:12 offset1:13
Mvaa 216,196,176
ds_read2st64_b32 v[122:123], v5 offset0:14 offset1:15
W_ 7
A_ v96,v96,v180
Mvaa 216,192,136
ds_read2st64_b32 v[128:129], v5 offset0:16 offset1:17
A_ v97,v97,v181
Mvaa 212,220,212
ds_read2st64_b32 v[130:131], v5 offset0:18 offset1:19
W_ 8
A_ v98,v98,v182
Mvaa 212,196,172
ds_read2st64_b32 v[132:133], v5 offset0:36 offset1:37
A_ v99,v99,v183
Mvaa 212,192,132
ds_read2st64_b32 v[134:135], v5 offset0:38 offset1:39
W_ 9
A_ v104,v104,v184
Mvaa 208,220,208
ds_read2st64_b32 v[124:125], v5 offset0:32 offset1:33
A_ v105,v105,v185
Mvaa 208,196,168
ds_read2st64_b32 v[126:127], v5 offset0:34 offset1:35
W_ 10
A_ v106,v106,v186
Mvaa 208,192,128
ds_read2st64_b32 v[116:117], v5 offset0:28 offset1:29
A_ v107,v107,v187
Mvaa 204,220,204
ds_read2st64_b32 v[118:119], v5 offset0:30 offset1:31
W_ 11
A_ v112,v112,v188
Mvaa 204,196,164
ds_read2st64_b32 v[108:109], v5 offset0:24 offset1:25
A_ v113,v113,v189
Mvaa 204,192,124
ds_read2st64_b32 v[110:111], v5 offset0:26 offset1:27
W_ 12
A_ v114,v114,v190
Mvaa 200,220,200
ds_read2st64_b32 v[100:101], v5 offset0:20 offset1:21
A_ v115,v115,v191
Mvaa 200,196,160
ds_read2st64_b32 v[102:103], v5 offset0:22 offset1:23
W_ 13
A_ v120,v120,v192
Mvaa 200,192,120
A_ v121,v121,v193
W_ 12
A_ v122,v122,v194
A_ v123,v123,v195
W_ 11
A_ v128,v128,v196
A_ v129,v129,v197
W_ 10
A_ v130,v130,v198
A_ v131,v131,v199
s_waitcnt vmcnt(5)
Mvav 164,70,36
W_ 9
A_ v132,v132,v216
A_ v133,v133,v217
Mvav 168,68,36
W_ 8
A_ v134,v134,v218
A_ v135,v135,v219
Mvav 172,70,40
W_ 7
A_ v124,v124,v212
A_ v125,v125,v213
Mvav 176,68,40
W_ 6
A_ v126,v126,v214
A_ v127,v127,v215
Mvav 164,70,156
W_ 5
A_ v116,v116,v208
A_ v117,v117,v209
Mvav 168,68,156
W_ 4
A_ v118,v118,v210
A_ v119,v119,v211
Mvav 172,70,160
W_ 3
A_ v108,v108,v204
A_ v109,v109,v205
Mvav 176,68,160
W_ 2
A_ v110,v110,v206
A_ v111,v111,v207
Mvav 96,70,16
W_ 1
A_ v100,v100,v200
A_ v101,v101,v201
Mvav 104,70,20
W_ 0
A_ v102,v102,v202
A_ v103,v103,v203
Mvav 112,70,24
C_ 220,164,165
C_ 221,166,167
Mvav 120,70,28
C_ 222,168,169
C_ 223,170,171
Mvav 128,70,32
v_permlane16_swap_b32 v220, v222
v_permlane16_swap_b32 v221, v223
Mvav 132,68,32
buffer_store_dwordx4 v[220:223], v4, s[32:35], s9 offen sc1
Mvav 124,68,28
C_ 228,172,173
C_ 229,174,175
Mvav 116,68,24
C_ 230,176,177
C_ 231,178,179
Mvav 108,68,20
v_permlane16_swap_b32 v228, v230
v_permlane16_swap_b32 v229, v231
Mvav 100,68,16
buffer_store_dwordx4 v[228:231], v4, s[32:35], s10 offen sc1
s_waitcnt vmcnt(2)
.Lsel6:
Nvav 180,70,136,96
Nvav 188,70,140,104
Nvav 196,70,144,112
Nvav 204,70,148,120
Nvav 212,70,152,128
Nvav 184,68,136,100
s_nop 3
A_ v10,v180,v188
Nvav 192,68,140,108
A_ v11,v196,v204
Nvav 200,68,144,116
A_ v10,v10,v212
v_cmp_u_f32 vcc, v10, v11
Nvav 208,68,148,124
s_cbranch_vccnz .Lretry
.Lgood:
Nvav 216,68,152,132
C_ 180,180,181
C_ 181,182,183
C_ 182,184,185
C_ 183,186,187
s_nop 0
v_permlane16_swap_b32 v180, v182
v_permlane16_swap_b32 v181, v183
buffer_store_dwordx4 v[180:183], v4, s[32:35], s8 offen sc1
C_ 188,188,189
C_ 189,190,191
C_ 190,192,193
C_ 191,194,195
S_ s8,s8,40960
v_permlane16_swap_b32 v188, v190
v_permlane16_swap_b32 v189, v191
buffer_store_dwordx4 v[188:191], v4, s[32:35], s8 offen sc1
C_ 196,196,197
C_ 197,198,199
C_ 198,200,201
C_ 199,202,203
S_ s8,s8,40960
v_permlane16_swap_b32 v196, v198
v_permlane16_swap_b32 v197, v199
buffer_store_dwordx4 v[196:199], v4, s[32:35], s8 offen sc1
C_ 204,204,205
C_ 205,206,207
C_ 206,208,209
C_ 207,210,211
S_ s8,s8,40960
v_permlane16_swap_b32 v204, v206
v_permlane16_swap_b32 v205, v207
buffer_store_dwordx4 v[204:207], v4, s[32:35], s8 offen sc1
C_ 212,212,213
C_ 213,214,215
C_ 214,216,217
C_ 215,218,219
S_ s8,s8,40960
v_permlane16_swap_b32 v212, v214
v_permlane16_swap_b32 v213, v215
buffer_store_dwordx4 v[212:215], v4, s[32:35], s8 offen sc1
s_endpgm
.Lspin0:
s_cmp_eq_u32 s57, 0
s_cbranch_scc1 .Lspun0
s_sub_u32 s57, s57, 1
s_sleep 4
buffer_load_dword v11, v8, s[92:95], s96 offen offset:384 sc1
s_waitcnt vmcnt(0)
v_cmp_eq_u32 vcc, s62, v11
s_nop 1
s_bitcmp1_b32 vcc_lo, s75
s_cbranch_scc0 .Lspin0
s_branch .Lspun0
.Lspin1:
s_cmp_eq_u32 s57, 0
s_cbranch_scc1 .Lspun1
s_sub_u32 s57, s57, 1
s_sleep 4
buffer_load_dword v11, v8, s[92:95], s96 offen offset:384 sc1
s_waitcnt vmcnt(0)
v_cmp_eq_u32 vcc, s62, v11
s_nop 1
s_bitcmp1_b32 vcc_lo, s75
s_cbranch_scc0 .Lspin1
s_branch .Lspun1
.Lspin2:
s_cmp_eq_u32 s57, 0
s_cbranch_scc1 .Lspun2
s_sub_u32 s57, s57, 1
s_sleep 4
buffer_load_dword v11, v8, s[92:95], s96 offen offset:384 sc1
s_waitcnt vmcnt(0)
v_cmp_eq_u32 vcc, s62, v11
s_nop 1
s_bitcmp1_b32 vcc_lo, s75
s_cbranch_scc0 .Lspin2
s_branch .Lspun2
.Lspin3:
s_cmp_eq_u32 s57, 0
s_cbranch_scc1 .Lspun3
s_sub_u32 s57, s57, 1
s_sleep 4
buffer_load_dword v11, v8, s[92:95], s96 offen offset:384 sc1
s_waitcnt vmcnt(0)
v_cmp_eq_u32 vcc, s62, v11
s_nop 1
s_bitcmp1_b32 vcc_lo, s75
s_cbranch_scc0 .Lspin3
s_branch .Lspun3
.Lspin4:
s_cmp_eq_u32 s57, 0
s_cbranch_scc1 .Lspun4
s_sub_u32 s57, s57, 1
s_sleep 4
buffer_load_dword v11, v8, s[92:95], s96 offen offset:384 sc1
s_waitcnt vmcnt(0)
v_cmp_eq_u32 vcc, s62, v11
s_nop 1
s_bitcmp1_b32 vcc_lo, s75
s_cbranch_scc0 .Lspin4
s_branch .Lspun4
.Lspin5:
s_cmp_eq_u32 s57, 0
s_cbranch_scc1 .Lspun5
s_sub_u32 s57, s57, 1
s_sleep 4
buffer_load_dword v11, v8, s[92:95], s96 offen offset:384 sc1
s_waitcnt vmcnt(0)
v_cmp_eq_u32 vcc, s62, v11
s_nop 1
s_bitcmp1_b32 vcc_lo, s75
s_cbranch_scc0 .Lspin5
s_branch .Lspun5
.Lspin6:
s_cmp_eq_u32 s57, 0
s_cbranch_scc1 .Lspun6
s_sub_u32 s57, s57, 1
s_sleep 4
buffer_load_dword v11, v8, s[92:95], s96 offen offset:384 sc1
s_waitcnt vmcnt(0)
v_cmp_eq_u32 vcc, s62, v11
s_nop 1
s_bitcmp1_b32 vcc_lo, s86
s_cbranch_scc0 .Lspin6
s_branch .Lspun6
.Lretry:
s_cmp_eq_u32 s57, 0
s_cbranch_scc1 .Lgood
s_sub_u32 s57, s57, 1
v_max_u32 v10, v136, v137
v_max3_u32 v10, v10, v138, v139
v_max3_u32 v10, v10, v140, v141
v_max3_u32 v10, v10, v142, v143
v_max3_u32 v10, v10, v144, v145
v_max3_u32 v10, v10, v146, v147
v_max3_u32 v10, v10, v148, v149
v_max3_u32 v10, v10, v150, v151
v_max3_u32 v10, v10, v152, v153
v_max3_u32 v10, v10, v154, v155
v_cmp_eq_u32 vcc, -1, v10
s_nop 4
s_cbranch_vccz .Lgood
s_nop 7
s_nop 7
S_ s53,s80,4096
buffer_load_dwordx4 v[136:139], v6, s[28:31], s80 offen offset:0 sc1
buffer_load_dwordx4 v[140:143], v6, s[28:31], s80 offen offset:1024 sc1
buffer_load_dwordx4 v[144:147], v6, s[28:31], s80 offen offset:2048 sc1
buffer_load_dwordx4 v[148:151], v6, s[28:31], s80 offen offset:3072 sc1
buffer_load_dwordx4 v[152:155], v6, s[28:31], s53 offen offset:0 sc1
s_waitcnt vmcnt(0)
s_branch .Lsel6
.Ldone:
s_waitcnt vmcnt(0)
.purgem S_
.purgem W_
.purgem L_
.purgem X_
.purgem R_
.purgem Ta
.purgem Da
.purgem Zvaa
.purgem Mvaa
.purgem U_
.purgem C_
.purgem Ln
.purgem Dv
.purgem Zvvv
.purgem Mvvv
.purgem B_
.purgem E_
.purgem Nvvv
.purgem Tv
.purgem Zaav
.purgem Zavv
.purgem A_
.purgem Mavv
.purgem Zvav
.purgem Mvav
.purgem Y_
.purgem Zaaa
.purgem Maaa
.purgem Maav
.purgem Nvav
)A"
    : "+v"(tid)
    : "s"(__builtin_amdgcn_kernarg_segment_ptr()), "s"(blockIdx.x)
    : "v1", "v2", "v3", "v4", "v5", "v6", "v7", "v8", "v9", "v10", "v11", "v12", "v13", "v14", "v15", "v16", "v17", "v18", "v19", "v20", "v21", "v22", "v23", "v24", "v25", "v26", "v27", "v28", "v29", "v30", "v31", "v32", "v33", "v34", "v35", "v36", "v37", "v38", "v39", "v40", "v41", "v42", "v43", "v44", "v45", "v46", "v47", "v48", "v49", "v50", "v51", "v52", "v53", "v54", "v55", "v56", "v57", "v58", "v59", "v60", "v61", "v62", "v63", "v64", "v65", "v66", "v67", "v68", "v69", "v70", "v71", "v72", "v73", "v74", "v75", "v76", "v77", "v78", "v79", "v80", "v81", "v82", "v83", "v84", "v85", "v86", "v87", "v88", "v89", "v90", "v91", "v92", "v93", "v94", "v95", "v96", "v97", "v98", "v99", "v100", "v101", "v102", "v103", "v104", "v105", "v106", "v107", "v108", "v109", "v110", "v111", "v112", "v113", "v114", "v115", "v116", "v117", "v118", "v119", "v120", "v121", "v122", "v123", "v124", "v125", "v126", "v127", "v128", "v129", "v130", "v131", "v132", "v133", "v134", "v135", "v136", "v137", "v138", "v139", "v140", "v141", "v142", "v143", "v144", "v145", "v146", "v147", "v148", "v149", "v150", "v151", "v152", "v153", "v154", "v155", "v156", "v157", "v158", "v159", "v160", "v161", "v162", "v163", "v164", "v165", "v166", "v167", "v168", "v169", "v170", "v171", "v172", "v173", "v174", "v175", "v176", "v177", "v178", "v179", "v180", "v181", "v182", "v183", "v184", "v185", "v186", "v187", "v188", "v189", "v190", "v191", "v192", "v193", "v194", "v195", "v196", "v197", "v198", "v199", "v200", "v201", "v202", "v203", "v204", "v205", "v206", "v207", "v208", "v209", "v210", "v211", "v212", "v213", "v214", "v215", "v216", "v217", "v218", "v219", "v220", "v221", "v222", "v223", "v224", "v225", "v226", "v227", "v228", "v229", "v230", "v231", "v232", "v233", "v234", "v235", "v236", "v237", "v238", "v239", "v240", "v241", "v242", "v243", "v244", "v245", "v246", "v247", "v248", "v249", "v250", "v251", "v252", "v253", "v254", "v255", "a0", "a1", "a2", "a3", "a4", "a5", "a6", "a7", "a8", "a9", "a10", "a11", "a12", "a13", "a14", "a15", "a16", "a17", "a18", "a19", "a20", "a21", "a22", "a23", "a24", "a25", "a26", "a27", "a28", "a29", "a30", "a31", "a32", "a33", "a34", "a35", "a36", "a37", "a38", "a39", "a40", "a41", "a42", "a43", "a44", "a45", "a46", "a47", "a48", "a49", "a50", "a51", "a52", "a53", "a54", "a55", "a56", "a57", "a58", "a59", "a60", "a61", "a62", "a63", "a64", "a65", "a66", "a67", "a68", "a69", "a70", "a71", "a72", "a73", "a74", "a75", "a76", "a77", "a78", "a79", "a80", "a81", "a82", "a83", "a84", "a85", "a86", "a87", "a88", "a89", "a90", "a91", "a92", "a93", "a94", "a95", "a96", "a97", "a98", "a99", "a100", "a101", "a102", "a103", "a104", "a105", "a106", "a107", "a108", "a109", "a110", "a111", "a112", "a113", "a114", "a115", "a116", "a117", "a118", "a119", "a120", "a121", "a122", "a123", "a124", "a125", "a126", "a127", "a128", "a129", "a130", "a131", "a132", "a133", "a134", "a135", "a136", "a137", "a138", "a139", "a140", "a141", "a142", "a143", "a144", "a145", "a146", "a147", "a148", "a149", "a150", "a151", "a152", "a153", "a154", "a155", "a156", "a157", "a158", "a159", "a160", "a161", "a162", "a163", "a164", "a165", "a166", "a167", "a168", "a169", "a170", "a171", "a172", "a173", "a174", "a175", "a176", "a177", "a178", "a179", "a180", "a181", "a182", "a183", "a184", "a185", "a186", "a187", "a188", "a189", "a190", "a191", "a192", "a193", "a194", "a195", "a196", "a197", "a198", "a199", "a200", "a201", "a202", "a203", "a204", "a205", "a206", "a207", "a208", "a209", "a210", "a211", "a212", "a213", "a214", "a215", "a216", "a217", "a218", "a219", "a220", "a221", "a222", "a223", "a224", "a225", "a226", "a227", "a228", "a229", "a230", "a231", "a232", "a233", "a234", "a235", "a236", "a237", "a238", "a239", "a240", "a241", "a242", "a243", "a244", "a245", "a246", "a247", "a248", "a249", "a250", "a251", "a252", "a253", "a254", "a255", "s3", "s4", "s5", "s6", "s7", "s8", "s9", "s10", "s11", "s12", "s13", "s14", "s15", "s16", "s17", "s18", "s19", "s20", "s21", "s22", "s23", "s24", "s25", "s26", "s27", "s28", "s29", "s30", "s31", "s32", "s33", "s34", "s35", "s36", "s37", "s38", "s39", "s40", "s41", "s42", "s43", "s44", "s45", "s46", "s47", "s48", "s49", "s50", "s51", "s52", "s53", "s54", "s55", "s56", "s57", "s58", "s59", "s60", "s61", "s62", "s63", "s64", "s65", "s66", "s67", "s68", "s69", "s70", "s71", "s72", "s73", "s74", "s75", "s76", "s77", "s78", "s79", "s80", "s81", "s82", "s83", "s84", "s85", "s86", "s87", "s88", "s89", "s90", "s91", "s92", "s93", "s94", "s95", "s96", "s97", "s98", "s99", "s100", "s101", "vcc", "scc", "memory");
}

extern "C" void kernel_launch(void* const* d_in, const int* in_sizes, int n_in, void* d_out, int out_size, void* d_ws, size_t ws_size, hipStream_t stream) {
  Args P;
  for (int br = 0; br < 2; ++br) {
    P.p[br][0] = d_in[5 * br]; P.p[br][1] = d_in[5 * br + 3]; P.p[br][2] = d_in[5 * br + 1]; P.p[br][3] = d_in[5 * br + 2]; P.p[br][4] = d_in[5 * br + 4];
    P.p[br][5] = d_ws; P.p[br][6] = (char*)d_out + br * 1280; P.p[br][7] = d_ws;
  }
  (void)hipFuncSetAttribute((const void*)fused_kernel, hipFuncAttributeMaxDynamicSharedMemorySize, LDS1);
  fused_kernel<<<256, 256, LDS1, stream>>>(P);
}
```

```cpp
#include <hip/hip_runtime.h>
#include <hip/hip_bf16.h>

constexpr unsigned LDS1 = 160 * 1024;

struct Args { const void* p[2][8]; };

__global__ __launch_bounds__(256, 1) void fused_kernel(Args P) {
  unsigned tid = threadIdx.x;
  asm volatile(
R"A(.macro S_ a,b,c
s_add_u32 \a, \b, \c
.endm
.macro W_ a
s_waitcnt lgkmcnt(\a)
.endm
.macro L_ x,r,s,o
buffer_load_dwordx4 \x, s[\r:\r+3], \s offen offset:\o lds
.endm
.macro X_ a,b
v_accvgpr_write_b32 \a, \b
.endm
.macro R_ a,b
v_accvgpr_read_b32 \a, \b
.endm
.macro Ta d,x,o
ds_read_b64_tr_b16 a[\d:\d+1], \x offset:\o
.endm
.macro Da d,x,o
ds_read_b128 a[\d:\d+3], \x offset:\o
.endm
.macro Zvaa d,a,b
v_mfma_f32_16x16x32_bf16 v[\d:\d+3], a[\a:\a+3], a[\b:\b+3], 0
.endm
.macro Mvaa d,a,b
v_mfma_f32_16x16x32_bf16 v[\d:\d+3], a[\a:\a+3], a[\b:\b+3], v[\d:\d+3]
.endm
.macro U_ a,b,c
v_mul_f32 \a, \b, \c
.endm
.macro C_ d,a,b
v_cvt_pk_bf16_f32 v\d, v\a, v\b
.endm
.macro Ln x,r,s,o
buffer_load_dwordx4 \x, s[\r:\r+3], \s offen offset:\o nt lds
.endm
.macro Dv d,x,o
ds_read_b128 v[\d:\d+3], \x offset:\o
.endm
.macro Zvvv d,a,b
v_mfma_f32_16x16x32_bf16 v[\d:\d+3], v[\a:\a+3], v[\b:\b+3], 0
.endm
.macro Mvvv d,a,b
v_mfma_f32_16x16x32_bf16 v[\d:\d+3], v[\a:\a+3], v[\b:\b+3], v[\d:\d+3]
.endm
.macro B_ a,b,c
v_sub_f32 \a, \b, \c
.endm
.macro E_ r
v_exp_f32 v\r, v\r
.endm
.macro Nvvv d,a,b,c
v_mfma_f32_16x16x32_bf16 v[\d:\d+3], v[\a:\a+3], v[\b:\b+3], v[\c:\c+3]
.endm
.macro Tv d,x,o
ds_read_b64_tr_b16 v[\d:\d+1], \x offset:\o
.endm
.macro Zaav d,a,b
v_mfma_f32_16x16x32_bf16 a[\d:\d+3], a[\a:\a+3], v[\b:\b+3], 0
.endm
.macro Zavv d,a,b
v_mfma_f32_16x16x32_bf16 a[\d:\d+3], v[\a:\a+3], v[\b:\b+3], 0
.endm
.macro A_ a,b,c
v_add_f32 \a, \b, \c
.endm
.macro Mavv d,a,b
v_mfma_f32_16x16x32_bf16 a[\d:\d+3], v[\a:\a+3], v[\b:\b+3], a[\d:\d+3]
.endm
.macro Zvav d,a,b
v_mfma_f32_16x16x32_bf16 v[\d:\d+3], a[\a:\a+3], v[\b:\b+3], 0
.endm
.macro Mvav d,a,b
v_mfma_f32_16x16x32_bf16 v[\d:\d+3], a[\a:\a+3], v[\b:\b+3], v[\d:\d+3]
.endm
.macro Y_ r,o
ds_write_addtid_b32 v\r offset:\o
.endm
.macro Zaaa d,a,b
v_mfma_f32_16x16x32_bf16 a[\d:\d+3], a[\a:\a+3], a[\b:\b+3], 0
.endm
.macro Maaa d,a,b
v_mfma_f32_16x16x32_bf16 a[\d:\d+3], a[\a:\a+3], a[\b:\b+3], a[\d:\d+3]
.endm
.macro Maav d,a,b
v_mfma_f32_16x16x32_bf16 a[\d:\d+3], a[\a:\a+3], v[\b:\b+3], a[\d:\d+3]
.endm
.macro Nvav d,a,b,c
v_mfma_f32_16x16x32_bf16 v[\d:\d+3], a[\a:\a+3], v[\b:\b+3], v[\c:\c+3]
.endm
.p2align 12
s_and_b32 s3, %2, 4
s_lshl_b32 s3, s3, 4
s_load_dwordx16 s[8:23], %1, s3
s_memrealtime s[100:101]
s_memtime s[90:91]
v_readfirstlane_b32 s4, %0
v_and_b32 v16, 63, %0
v_lshlrev_b32 v10, 4, v16
s_mov_b32 s7, %2
s_lshr_b32 s4, s4, 6
.Lcode0:
s_getpc_b64 s[44:45]
s_mov_b32 s46, .Ldone-.Lcode0
s_mov_b32 s47, 0x00020000
s_lshr_b32 s66, s7, 3
s_lshl_b32 s66, s66, 2
S_ s66,s66,s4
s_lshl_b32 s66, s66, 11
v_min_u32 v40, 15, v16
v_lshlrev_b32 v40, 7, v40
buffer_load_dword v247, v40, s[44:47], s66 offen
s_and_b32 s3, s7, 1
s_lshl_b32 s3, s3, 3
s_lshr_b32 s53, s7, 5
S_ s3,s3,s53
s_and_b32 s6, s7, 2
s_lshl_b32 s6, s6, 1
s_bfe_u32 s53, s7, 0x20003
S_ s6,s6,s53
s_and_b32 s5, s7, 4
s_lshl_b32 s5, s5, 2
S_ s5,s5,s3
s_mul_i32 s50, s4, 5120
s_mul_i32 s60, s4, 51200
s_lshr_b32 s77, s4, 1
s_and_b32 s53, s4, 1
s_mul_i32 s59, s53, 393216
s_lshl_b32 s52, s53, 11
s_mul_i32 s61, s53, 6144
s_mul_i32 s51, s77, 61440
S_ s55,s51,40960
S_ s51,s55,s61
s_sub_u32 s52, s51, s52
s_lshl_b32 s86, s53, 2
s_mul_i32 s56, s53, 10240
v_lshrrev_b32 v17, 3, v16
v_and_b32 v18, 7, v16
v_lshrrev_b32 v23, 4, v16
v_and_b32 v24, 1, v23
v_lshlrev_b32 v24, 2, v24
v_xor_b32 v24, v18, v24
v_mul_u32_u24 v6, 12288, v17
v_lshl_add_u32 v6, v24, 4, v6
v_add_u32 v24, s86, v23
v_xor_b32 v24, v18, v24
v_mul_u32_u24 v4, 1280, v17
v_lshl_add_u32 v4, v24, 4, v4
v_xor_b32 v5, 64, v4
s_getreg_b32 s53, hwreg(HW_REG_XCC_ID, 0, 4)
s_bitcmp1_b32 0xaaaa, s53
s_cselect_b32 s58, 2, 0
s_cselect_b32 s89, 1, 0
s_cselect_b32 s53, 768, 0
S_ s59,s59,s53
s_lshl_b32 s54, s6, 2
S_ s54,s54,s77
s_mul_i32 s86, s54, 384
s_mul_i32 s87, s3, 204800
s_lshl_b32 s53, s3, 5
S_ s53,s53,s54
s_mul_i32 s85, s53, 196608
s_mov_b32 s30, 7853184
s_mov_b32 s31, 0x00020000
s_mov_b32 s26, 204800
s_mov_b32 s27, 0x00020000
W_ 0
s_mov_b32 s98, s100
s_mov_b32 s99, 0
s_mov_b32 s88, s90
S_ s28,s10,s86
s_addc_u32 s29, s11, 0
S_ s24,s8,s87
s_addc_u32 s25, s9, 0
s_buffer_load_dword s67, s[24:27], s60
s_mul_i32 s64, s66, 30
s_buffer_load_dword s67, s[28:31], s64
s_mov_b32 s11, 0
S_ s65,s59,0
S_ m0,s52,8192
s_mov_b64 s[92:93], s[18:19]
s_mov_b32 s53, 251658240
L_ v6,28,s65,0
S_ s65,s59,98304
S_ m0,s52,9216
S_ s92,s92,s53
s_addc_u32 s93, s93, 0
L_ v6,28,s65,0
S_ s65,s59,196608
S_ m0,s52,10240
s_mov_b32 s94, 131072
s_mov_b32 s95, 0x00020000
L_ v6,28,s65,0
S_ s65,s59,294912
S_ m0,s52,11264
s_lshl_b32 s96, s5, 3
S_ s96,s96,s6
L_ v6,28,s65,0
S_ s65,s60,0
S_ m0,s50,0
s_lshl_b32 s96, s96, 9
v_cmp_gt_u32 vcc, 8, v16
L_ v4,24,s65,0
S_ s65,s60,10240
S_ m0,s50,1024
s_lshl_b32 s54, s5, 12
v_lshl_add_u32 v24, v16, 9, s54
L_ v5,24,s65,0
S_ s65,s60,20480
S_ m0,s50,2048
v_mov_b32 v12, 0x7fff0000
v_cndmask_b32 v12, v12, v24, vcc
L_ v4,24,s65,0
S_ s65,s60,30720
S_ m0,s50,3072
s_lshl_b32 s54, s6, 2
v_add_u32 v13, s54, v12
L_ v5,24,s65,0
S_ s65,s60,40960
S_ m0,s50,4096
v_lshrrev_b32 v19, 2, v10
v_mul_u32_u24 v25, 384, v17
L_ v4,24,s65,0
s_load_dword s67, s[12:13], s85
s_load_dword s67, s[14:15], s85
S_ s64,s85,393216
s_load_dword s67, s[12:13], s64
s_load_dword s67, s[14:15], s64
s_lshl_b32 s64, s6, 2
S_ s64,s64,s4
s_mul_i32 s64, s64, 245760
s_load_dword s67, s[16:17], s64
s_cmp_eq_u32 s4, 0
s_cbranch_scc0 .Lhs0a
buffer_load_dword v20, v19, s[92:95], s96 offen offset:128 sc1
buffer_load_dword v21, v19, s[92:95], s96 offen offset:384 sc1
buffer_load_dword a252, v19, s[92:95], s96 offen offset:256 sc1
buffer_load_dword a253, v13, s[92:95], 0 offen offset:256 sc1
buffer_load_dword v22, off, s[92:95], s96 offset:0 sc1
.Lhs0a:
S_ s65,s59,0
S_ m0,s52,16256
v_xor_b32 v24, v18, v23
v_lshl_add_u32 v7, v24, 4, v25
v_lshlrev_b32 v24, 1, v23
v_xor_b32 v24, v18, v24
v_lshl_add_u32 v9, v24, 4, v25
v_and_b32 v26, 15, v16
v_lshrrev_b32 v27, 2, v26
v_and_b32 v28, 3, v16
v_lshrrev_b32 v24, 1, v26
v_xor_b32 v24, v23, v24
L_ v6,28,s65,128
S_ s65,s59,98304
S_ m0,s52,17280
v_lshlrev_b32 v24, 4, v24
v_lshl_add_u32 v24, v18, 7, v24
v_lshrrev_b32 v25, 3, v26
v_lshl_add_u32 v2, v25, 10, v24
v_add_u32 v2, s56, v2
v_mul_u32_u24 v1, 3072, v25
v_add3_u32 v1, v1, v24, s55
v_lshlrev_b32 v24, 2, v25
v_xor_b32 v24, v28, v24
v_lshlrev_b32 v24, 4, v24
L_ v6,28,s65,128
S_ s65,s59,196608
S_ m0,s52,18304
v_lshl_add_u32 v24, v27, 7, v24
v_lshl_add_u32 %0, v23, 10, v24
v_add_u32 %0, s55, %0
v_and_b32 v29, 1, v23
v_lshl_add_u32 v24, v29, 1, v25
v_lshlrev_b32 v24, 1, v24
v_lshrrev_b32 v30, 1, v28
v_xor_b32 v24, v30, v24
v_lshlrev_b32 v24, 4, v24
v_lshl_add_u32 v29, v29, 2, v27
L_ v6,28,s65,128
S_ s65,s59,294912
S_ m0,s52,19328
v_lshl_add_u32 v24, v29, 7, v24
v_and_b32 v29, 1, v28
v_lshl_add_u32 v24, v29, 3, v24
v_lshrrev_b32 v29, 1, v23
v_mul_u32_u24 v3, 3072, v29
v_add3_u32 v3, v3, v24, s55
X_ a242,%0
X_ a244,v1
X_ a243,v2
X_ a245,v3
L_ v6,28,s65,128
S_ s65,s59,0
S_ m0,s52,24320
X_ a240,v4
X_ a248,v10
X_ a241,v12
X_ a251,v13
v_xor_b32 v1, 64, %0
v_xor_b32 v3, 64, v2
v_xor_b32 v8, 64, v7
S_ s32,s12,s85
s_addc_u32 s33, s13, 0
s_mov_b32 s34, 589824
L_ v6,28,s65,256
S_ s65,s59,98304
S_ m0,s52,25344
s_mov_b32 s35, 0x00020000
S_ s36,s14,s85
s_addc_u32 s37, s15, 0
s_mov_b32 s38, 589824
s_mov_b32 s39, 0x00020000
s_lshl_b32 s87, s87, 1
S_ s20,s20,s87
s_addc_u32 s21, s21, 0
s_mov_b32 s57, 20000
s_mov_b32 s48, 1037384597
L_ v6,28,s65,256
S_ s65,s59,196608
S_ m0,s52,26368
s_mov_b32 s49, 1037384597
s_mul_i32 s53, s5, 1638400
S_ s53,s53,134217728
S_ s22,s18,s53
s_addc_u32 s23, s19, 0
s_lshl_b32 s53, s6, 3
s_lshr_b32 s54, s7, 5
S_ s53,s53,s54
s_lshl_b32 s53, s53, 2
S_ s53,s53,s4
L_ v6,28,s65,256
S_ s65,s59,294912
S_ m0,s52,27392
s_mul_i32 s87, s53, 30720
s_lshl_b32 s53, s6, 2
S_ s53,s53,s77
s_mul_i32 s72, s53, 245760
s_and_b32 s54, s4, 1
s_mul_i32 s53, s54, 61440
S_ s72,s72,s53
s_lshl_b32 s53, s77, 6
s_xor_b32 s83, s53, 64
s_xor_b32 s53, s4, 2
L_ v6,28,s65,256
S_ s65,s60,128
S_ m0,s50,20480
s_mul_i32 s75, s53, 10240
s_cmp_eq_u32 s4, 0
s_cselect_b32 s73, 0, 2147418112
s_mul_i32 s53, s4, 5120
s_mul_i32 s78, s6, 204800
S_ s78,s78,s53
s_and_b32 s84, s6, 7
s_mul_i32 s84, s84, 20480
S_ s84,s84,s53
s_mul_i32 s53, s6, 5120
L_ v4,24,s65,0
S_ s65,s60,10368
S_ m0,s50,21504
S_ s53,s53,163840
s_lshl_b32 s54, s4, 10
S_ s74,s53,s54
S_ s53,s53,4096
s_cmp_eq_u32 s4, 0
s_cselect_b32 s82, s53, 4026531840
s_and_b32 s53, s4, 1
s_mul_i32 s53, s53, 204800
s_lshl_b32 s54, s6, 7
S_ s53,s53,s54
L_ v5,24,s65,0
S_ s65,s60,20608
S_ m0,s50,22528
s_lshl_b32 s54, s77, 6
S_ s8,s53,s54
s_and_b32 s53, s6, 1
s_mul_i32 s53, s53, 204800
s_lshr_b32 s54, s6, 1
s_lshl_b32 s54, s54, 6
S_ s53,s53,s54
S_ s53,s53,1024
s_mul_i32 s54, s4, 40960
S_ s9,s53,s54
L_ v4,24,s65,0
S_ s65,s60,30848
S_ m0,s50,23552
S_ s53,s53,163840
s_cmp_eq_u32 s4, 0
s_cselect_b32 s10, s53, 4026531840
R_ v12,a248
v_lshrrev_b32 v12, 4, v12
v_min_u32 v10, 11, v12
s_mul_i32 s56, s4, 12
v_add_u32 v10, s56, v10
v_mul_u32_u24 v11, 43691, v10
v_lshrrev_b32 v11, 18, v11
L_ v5,24,s65,0
S_ s65,s60,41088
S_ m0,s50,24576
v_lshlrev_b32 v10, 7, v10
v_mul_u32_u24 v11, 11520, v11
v_add_u32 v10, v10, v11
v_min_u32 v12, 9, v12
s_and_b32 s56, s6, 3
s_lshl_b32 s56, s56, 2
S_ s56,s56,s4
s_mul_i32 s56, s56, 10
v_add_u32 v12, s56, v12
v_mul_u32_u24 v12, 1280, v12
L_ v4,24,s65,0
S_ s65,s59,786432
S_ m0,s52,0
s_lshr_b32 s56, s7, 5
s_mul_i32 s56, s56, 98304
s_and_b32 s55, s59, 4095
S_ s56,s56,s55
s_mul_i32 s55, s77, 384
s_sub_u32 s56, s56, s55
s_mov_b32 s63, 0
L_ v6,28,s65,0
S_ s65,s59,884736
S_ m0,s52,1024
s_nop 0
L_ v6,28,s65,0
S_ s65,s59,983040
S_ m0,s52,2048
s_nop 0
L_ v6,28,s65,0
S_ s65,s59,1081344
S_ m0,s52,3072
s_nop 0
L_ v6,28,s65,0
S_ s55,s56,786432
buffer_load_dword a254, v10, s[28:31], s55 offen
S_ s55,s56,1572864
buffer_load_dword a254, v10, s[28:31], s55 offen
buffer_load_dword a254, v12, s[24:27], s63 offen offset:256
S_ s55,s56,2359296
buffer_load_dword a254, v10, s[28:31], s55 offen
buffer_load_dword a254, v12, s[24:27], s63 offen offset:384
s_waitcnt vmcnt(22) lgkmcnt(0)
s_barrier
s_memtime s[90:91]
s_memrealtime s[68:69]
Ta 20,%0,8192
Ta 22,%0,8704
Ta 24,%0,12288
Ta 26,%0,12800
Da 60,v2,0
Da 100,v3,0
Da 64,v2,2048
Da 104,v3,2048
Da 68,v2,4096
Da 108,v3,4096
Da 72,v2,6144
Da 112,v3,6144
Da 76,v2,8192
Da 116,v3,8192
Ta 28,%0,8200
Ta 30,%0,8712
Ta 32,%0,12296
Ta 34,%0,12808
s_cmp_eq_u32 s4, 0
s_cbranch_scc0 .Lhs0b
v_readfirstlane_b32 s62, v22
.Lhs0l:
s_nop 3
S_ s62,s62,1
v_cmp_eq_u32 vcc, s62, v20
s_nop 1
s_or_b32 s53, vcc_lo, vcc_hi
v_cmp_eq_u32 vcc, s62, v21
s_nop 1
s_or_b32 s54, vcc_lo, vcc_hi
s_or_b32 s53, s53, s54
s_cbranch_scc1 .Lhs0l
v_mov_b32 v22, s62
s_mov_b64 exec, 1
s_nop 1
buffer_store_dword v22, off, s[92:95], s96 sc1
s_mov_b64 exec, -1
s_nop 1
.Lhs0b:
s_cmp_eq_u32 s58, 0
s_cbranch_scc1 .Lq
.Lqy:
W_ 13
Zvaa 16,20,60
s_nop 0
Ta 36,v1,8192
Ta 38,v1,8704
W_ 14
Mvaa 16,24,100
R_ v12,a248
W_ 13
Zvaa 24,20,64
v_lshrrev_b32 v12, 4, v12
W_ 12
Mvaa 24,24,104
s_nop 0
Ta 224,v1,12288
Ta 226,v1,12800
W_ 13
Zvaa 32,20,68
v_min_u32 v10, 11, v12
W_ 12
Mvaa 32,24,108
s_mul_i32 s56, s4, 12
W_ 11
Zvaa 40,20,72
v_add_u32 v10, s56, v10
W_ 10
Mvaa 40,24,112
v_mul_u32_u24 v11, 43691, v10
W_ 9
Zvaa 48,20,76
v_lshrrev_b32 v11, 18, v11
W_ 8
Mvaa 48,24,116
W_ 4
v_lshlrev_b32 v10, 7, v10
Zvaa 52,32,116
s_nop 0
Ta 20,v1,8200
Ta 22,v1,8712
Mvaa 52,28,76
v_mul_u32_u24 v11, 11520, v11
Zvaa 44,32,112
v_add_u32 v10, v10, v11
Mvaa 44,28,72
s_nop 0
Ta 24,v1,12296
Ta 26,v1,12808
Zvaa 36,32,108
v_min_u32 v12, 9, v12
Mvaa 36,28,68
s_and_b32 s56, s6, 3
Zvaa 28,32,104
s_lshl_b32 s56, s56, 2
Mvaa 28,28,64
S_ s56,s56,s4
Zvaa 20,32,100
s_waitcnt vmcnt(18) lgkmcnt(0)
s_barrier
s_mul_i32 s56, s56, 10
Mvaa 20,28,60
S_ s65,s59,786432
v_add_u32 v12, s56, v12
Zvaa 56,36,60
S_ m0,s52,8064
Ta 28,%0,16384
Ta 30,%0,16896
Mvaa 56,224,100
L_ v6,28,s65,128
Zvaa 64,36,64
S_ s65,s59,884736
v_mul_u32_u24 v12, 1280, v12
Mvaa 64,224,104
S_ m0,s52,9088
Ta 32,%0,20480
Ta 34,%0,20992
Zvaa 72,36,68
s_lshr_b32 s56, s7, 5
Mvaa 72,224,108
L_ v6,28,s65,128
Zvaa 80,36,72
S_ s65,s59,983040
s_mul_i32 s56, s56, 98304
Mvaa 80,224,112
S_ m0,s52,10112
s_and_b32 s55, s59, 4095
Zvaa 88,36,76
S_ s56,s56,s55
Mvaa 88,224,116
s_mul_i32 s55, s77, 384
Zvaa 92,24,116
s_sub_u32 s56, s56, s55
Ta 36,%0,16392
Ta 38,%0,16904
Mvaa 92,20,76
L_ v6,28,s65,128
Zvaa 84,24,112
S_ s65,s59,1081344
s_mov_b32 s63, 0
Mvaa 84,20,72
S_ m0,s52,11136
Ta 224,%0,20488
Ta 226,%0,21000
Zvaa 76,24,108
S_ s55,s56,3145728
Mvaa 76,20,68
L_ v6,28,s65,128
Zvaa 68,24,104
S_ s65,s60,256
Mvaa 68,20,64
S_ m0,s50,0
Zvaa 60,24,100
Mvaa 60,20,60
W_ 4
Zvaa 96,28,60
s_nop 0
Ta 20,v1,16384
Ta 22,v1,16896
Mvaa 96,32,100
L_ v4,24,s65,0
Zvaa 104,28,64
S_ s65,s60,10496
Mvaa 104,32,104
S_ m0,s50,1024
Ta 24,v1,20480
Ta 26,v1,20992
Zvaa 112,28,68
Mvaa 112,32,108
L_ v5,24,s65,0
Zvaa 120,28,72
S_ s65,s60,20736
Mvaa 120,32,112
S_ m0,s50,2048
Zvaa 128,28,76
Mvaa 128,32,116
W_ 4
Zvaa 132,224,116
s_nop 0
Ta 28,v1,16392
Ta 30,v1,16904
Mvaa 132,36,76
L_ v4,24,s65,0
Zvaa 124,224,112
S_ s65,s60,30976
Mvaa 124,36,72
S_ m0,s50,3072
Ta 32,v1,20488
Ta 34,v1,21000
Zvaa 116,224,108
Mvaa 116,36,68
L_ v5,24,s65,0
Zvaa 108,224,104
S_ s65,s60,41216
Mvaa 108,36,64
S_ m0,s50,4096
Zvaa 100,224,100
s_mov_b64 exec, 0
s_bitcmp1_b32 s4, 1
s_cbranch_scc1 .Lhop_q1_b
s_bitcmp1_b32 s4, 0
s_cbranch_scc1 .Lpad_q1_1
s_branch .Lpad_q1_0
.Lhop_q1_b:
s_bitcmp1_b32 s4, 0
s_cbranch_scc1 .Lpad_q1_3
s_branch .Lpad_q1_2
.Lhop_q1_r:
s_mov_b64 exec, -1
s_nop 1
.Lqy_early:
s_waitcnt vmcnt(17) lgkmcnt(0)
s_barrier
Mvaa 100,36,60
Da 140,v2,20480
Zvaa 136,20,60
s_nop 0
Ta 36,%0,24576
Ta 38,%0,25088
Mvaa 136,24,100
L_ v4,24,s65,0
Zvaa 144,20,64
S_ s65,s59,786432
Da 180,v3,20480
Mvaa 144,24,104
S_ m0,s52,16128
Ta 224,%0,28672
Ta 226,%0,29184
Zvaa 152,20,68
Mvaa 152,24,108
Da 144,v2,22528
Zvaa 160,20,72
L_ v6,28,s65,256
Mvaa 160,24,112
S_ s65,s59,884736
Zvaa 168,20,76
S_ m0,s52,17152
Da 184,v3,22528
Mvaa 168,24,116
Zvaa 172,32,116
s_nop 0
Ta 20,%0,24584
Ta 22,%0,25096
Mvaa 172,28,76
Da 148,v2,24576
Zvaa 164,32,112
L_ v6,28,s65,256
Mvaa 164,28,72
S_ s65,s59,983040
Ta 24,%0,28680
Ta 26,%0,29192
Zvaa 156,32,108
S_ m0,s52,18176
Da 188,v3,24576
Mvaa 156,28,68
Zvaa 148,32,104
L_ v6,28,s65,256
Mvaa 148,28,64
S_ s65,s59,1081344
Da 152,v2,26624
Zvaa 140,32,100
S_ m0,s52,19200
Mvaa 140,28,60
W_ 9
Zvaa 176,36,60
s_nop 0
Ta 28,v1,24576
Ta 30,v1,25088
Mvaa 176,224,100
Da 192,v3,26624
Zvaa 184,36,64
L_ v6,28,s65,256
Mvaa 184,224,104
s_nop 0
Ta 32,v1,28672
Ta 34,v1,29184
Zvaa 192,36,68
Da 156,v2,28672
Mvaa 192,224,108
Zvaa 200,36,72
Da 196,v3,28672
Mvaa 200,224,112
buffer_load_dword a254, v10, s[28:31], s55 offen
Zvaa 208,36,76
Mvaa 208,224,116
W_ 9
Zvaa 212,24,116
s_nop 0
Ta 36,v1,24584
Ta 38,v1,25096
Mvaa 212,20,76
buffer_load_dword a254, v12, s[24:27], s63 offen offset:512
Zvaa 204,24,112
.Lpad_q1_0:
s_cbranch_execz .Lhop_q1_r
Mvaa 204,20,72
s_nop 0
Ta 224,v1,28680
Ta 226,v1,29192
Zvaa 196,24,108
Mvaa 196,20,68
Zvaa 188,24,104
Mvaa 188,20,64
Zvaa 180,24,100
s_waitcnt vmcnt(20) lgkmcnt(0)
s_barrier
Mvaa 180,20,60
S_ s65,s59,1572864
Zvaa 216,28,60
S_ m0,s52,24576
Ta 20,%0,0
Ta 22,%0,512
Mvaa 216,32,100
L_ v6,28,s65,0
Zvaa 224,28,64
S_ s65,s59,1671168
Mvaa 224,32,104
S_ m0,s52,25600
Ta 24,%0,4096
Ta 26,%0,4608
Zvaa 232,28,68
Mvaa 232,32,108
L_ v6,28,s65,0
Zvaa 240,28,72
S_ s65,s59,1769472
Mvaa 240,32,112
S_ m0,s52,26624
Zvaa 248,28,76
Mvaa 248,32,116
Zvaa 252,224,116
s_nop 0
Ta 28,%0,8
Ta 30,%0,520
Mvaa 252,36,76
L_ v6,28,s65,0
Zvaa 244,224,112
S_ s65,s59,1867776
Mvaa 244,36,72
S_ m0,s52,27648
Ta 32,%0,4104
Ta 34,%0,4616
Zvaa 236,224,108
Mvaa 236,36,68
L_ v6,28,s65,0
Zvaa 228,224,104
S_ s65,s60,384
Mvaa 228,36,64
S_ m0,s50,20480
Zvaa 220,224,100
Mvaa 220,36,60
W_ 4
Mvaa 16,20,140
s_nop 0
Ta 36,v1,0
Ta 38,v1,512
Mvaa 16,24,180
L_ v4,24,s65,0
Mvaa 24,20,144
S_ s65,s60,10624
Mvaa 24,24,184
S_ m0,s50,21504
Ta 224,v1,4096
Ta 226,v1,4608
Mvaa 32,20,148
Mvaa 32,24,188
L_ v5,24,s65,0
Mvaa 40,20,152
S_ s65,s60,20864
Mvaa 40,24,192
S_ m0,s50,22528
Mvaa 48,20,156
Mvaa 48,24,196
W_ 4
Mvaa 52,32,196
s_nop 0
Ta 20,v1,8
Ta 22,v1,520
Mvaa 52,28,156
L_ v4,24,s65,0
Mvaa 44,32,192
S_ s65,s60,31104
Mvaa 44,28,152
S_ m0,s50,23552
Ta 24,v1,4104
Ta 26,v1,4616
Mvaa 36,32,188
Mvaa 36,28,148
L_ v5,24,s65,0
Mvaa 28,32,184
S_ s65,s60,41344
Mvaa 28,28,144
S_ m0,s50,24576
Mvaa 20,32,180
s_waitcnt vmcnt(19) lgkmcnt(0)
s_barrier
Mvaa 20,28,140
L_ v4,24,s65,0
Mvaa 56,36,140
S_ s65,s59,1572864
Ta 28,%0,8192
Ta 30,%0,8704
Mvaa 56,224,180
S_ m0,s52,-128
Mvaa 64,36,144
Mvaa 64,224,184
s_nop 0
Ta 32,%0,12288
Ta 34,%0,12800
Mvaa 72,36,148
L_ v6,28,s65,128
Mvaa 72,224,188
S_ s65,s59,1671168
Mvaa 80,36,152
S_ m0,s52,896
Mvaa 80,224,192
Mvaa 88,36,156
L_ v6,28,s65,128
Mvaa 88,224,196
S_ s65,s59,1769472
Mvaa 92,24,196
S_ m0,s52,1920
Ta 36,%0,8200
Ta 38,%0,8712
Mvaa 92,20,156
S_ s55,s56,3932160
Mvaa 84,24,192
L_ v6,28,s65,128
Mvaa 84,20,152
S_ s65,s59,1867776
Ta 224,%0,12296
Ta 226,%0,12808
Mvaa 76,24,188
S_ m0,s52,2944
Mvaa 76,20,148
Mvaa 68,24,184
L_ v6,28,s65,128
Mvaa 68,20,144
Mvaa 60,24,180
Mvaa 60,20,140
W_ 4
Mvaa 96,28,140
s_nop 0
Ta 20,v1,8192
Ta 22,v1,8704
Mvaa 96,32,180
buffer_load_dword a254, v10, s[28:31], s55 offen
Mvaa 104,28,144
Mvaa 104,32,184
s_nop 0
Ta 24,v1,12288
Ta 26,v1,12800
Mvaa 112,28,148
Mvaa 112,32,188
buffer_load_dword a254, v12, s[24:27], s63 offen offset:640
Mvaa 120,28,152
Mvaa 120,32,192
Mvaa 128,28,156
Mvaa 128,32,196
W_ 4
Mvaa 132,224,196
s_nop 0
Ta 28,v1,8200
Ta 30,v1,8712
Mvaa 132,36,156
Mvaa 124,224,192
Mvaa 124,36,152
s_nop 0
Ta 32,v1,12296
Ta 34,v1,12808
Mvaa 116,224,188
Mvaa 116,36,148
Mvaa 108,224,184
Mvaa 108,36,144
Mvaa 100,224,180
s_waitcnt vmcnt(17) lgkmcnt(0)
s_barrier
Mvaa 100,36,140
S_ s65,s59,1572864
Da 60,v2,0
Mvaa 136,20,140
S_ m0,s52,7936
Ta 36,%0,16384
Ta 38,%0,16896
Mvaa 136,24,180
L_ v6,28,s65,256
Mvaa 144,20,144
S_ s65,s59,1671168
Da 100,v3,0
Mvaa 144,24,184
S_ m0,s52,8960
Ta 224,%0,20480
Ta 226,%0,20992
Mvaa 152,20,148
Mvaa 152,24,188
Da 64,v2,2048
Mvaa 160,20,152
L_ v6,28,s65,256
Mvaa 160,24,192
S_ s65,s59,1769472
Mvaa 168,20,156
S_ m0,s52,9984
Da 104,v3,2048
Mvaa 168,24,196
Mvaa 172,32,196
s_nop 0
Ta 20,%0,16392
Ta 22,%0,16904
Mvaa 172,28,156
Da 68,v2,4096
Mvaa 164,32,192
L_ v6,28,s65,256
Mvaa 164,28,152
S_ s65,s59,1867776
Ta 24,%0,20488
Ta 26,%0,21000
Mvaa 156,32,188
S_ m0,s52,11008
Da 108,v3,4096
Mvaa 156,28,148
Mvaa 148,32,184
L_ v6,28,s65,256
Mvaa 148,28,144
Da 72,v2,6144
Mvaa 140,32,180
Mvaa 140,28,140
W_ 9
Mvaa 176,36,140
s_nop 0
Ta 28,v1,16384
Ta 30,v1,16896
Mvaa 176,224,180
Da 112,v3,6144
Mvaa 184,36,144
Mvaa 184,224,184
s_nop 0
Ta 32,v1,20480
Ta 34,v1,20992
Mvaa 192,36,148
Da 76,v2,8192
Mvaa 192,224,188
Mvaa 200,36,152
Da 116,v3,8192
Mvaa 200,224,192
Mvaa 208,36,156
Mvaa 208,224,196
W_ 9
Mvaa 212,24,196
s_nop 0
Ta 36,v1,16392
Ta 38,v1,16904
Mvaa 212,20,156
Mvaa 204,24,192
Mvaa 204,20,152
s_nop 0
Ta 224,v1,20488
Ta 226,v1,21000
Mvaa 196,24,188
Mvaa 196,20,148
Mvaa 188,24,184
Mvaa 188,20,144
Mvaa 180,24,180
s_waitcnt vmcnt(15) lgkmcnt(0)
s_barrier
Mvaa 180,20,140
S_ s65,s59,2359296
Mvaa 216,28,140
S_ m0,s52,16384
Ta 20,%0,24576
Ta 22,%0,25088
Mvaa 216,32,180
L_ v6,28,s65,0
Mvaa 224,28,144
S_ s65,s59,2457600
Mvaa 224,32,184
S_ m0,s52,17408
Ta 24,%0,28672
Ta 26,%0,29184
Mvaa 232,28,148
Mvaa 232,32,188
L_ v6,28,s65,0
Mvaa 240,28,152
S_ s65,s59,2555904
Mvaa 240,32,192
S_ m0,s52,18432
Mvaa 248,28,156
Mvaa 248,32,196
Mvaa 252,224,196
s_nop 0
Ta 28,%0,24584
Ta 30,%0,25096
Mvaa 252,36,156
L_ v6,28,s65,0
Mvaa 244,224,192
S_ s65,s59,2654208
Mvaa 244,36,152
S_ m0,s52,19456
Ta 32,%0,28680
Ta 34,%0,29192
Mvaa 236,224,188
Mvaa 236,36,148
L_ v6,28,s65,0
Mvaa 228,224,184
S_ s65,s60,512
Mvaa 228,36,144
S_ m0,s50,0
Mvaa 220,224,180
Mvaa 220,36,140
W_ 4
Mvaa 16,20,60
s_nop 0
Ta 36,v1,24576
Ta 38,v1,25088
Mvaa 16,24,100
L_ v4,24,s65,0
Mvaa 24,20,64
S_ s65,s60,10752
Mvaa 24,24,104
S_ m0,s50,1024
Ta 224,v1,28672
Ta 226,v1,29184
Mvaa 32,20,68
Mvaa 32,24,108
L_ v5,24,s65,0
Mvaa 40,20,72
S_ s65,s60,20992
Mvaa 40,24,112
S_ m0,s50,2048
Mvaa 48,20,76
Mvaa 48,24,116
W_ 4
Mvaa 52,32,116
s_nop 0
Ta 20,v1,24584
Ta 22,v1,25096
Mvaa 52,28,76
L_ v4,24,s65,0
Mvaa 44,32,112
S_ s65,s60,31232
Mvaa 44,28,72
S_ m0,s50,3072
Ta 24,v1,28680
Ta 26,v1,29192
Mvaa 36,32,108
Mvaa 36,28,68
L_ v5,24,s65,0
Mvaa 28,32,104
S_ s65,s60,41472
Mvaa 28,28,64
S_ m0,s50,4096
Mvaa 20,32,100
s_waitcnt vmcnt(14) lgkmcnt(0)
s_barrier
Mvaa 20,28,60
L_ v4,24,s65,0
Mvaa 56,36,60
S_ s65,s59,2359296
Ta 28,%0,0
Ta 30,%0,512
Mvaa 56,224,100
S_ m0,s52,24448
Mvaa 64,36,64
Mvaa 64,224,104
s_nop 0
Ta 32,%0,4096
Ta 34,%0,4608
Mvaa 72,36,68
L_ v6,28,s65,128
Mvaa 72,224,108
S_ s65,s59,2457600
Mvaa 80,36,72
S_ m0,s52,25472
Mvaa 80,224,112
Mvaa 88,36,76
L_ v6,28,s65,128
Mvaa 88,224,116
S_ s65,s59,2555904
Mvaa 92,24,116
S_ m0,s52,26496
Ta 36,%0,8
Ta 38,%0,520
Mvaa 92,20,76
S_ s55,s56,4718592
Mvaa 84,24,112
L_ v6,28,s65,128
Mvaa 84,20,72
S_ s65,s59,2654208
Ta 224,%0,4104
Ta 226,%0,4616
Mvaa 76,24,108
S_ m0,s52,27520
Mvaa 76,20,68
Mvaa 68,24,104
L_ v6,28,s65,128
Mvaa 68,20,64
Mvaa 60,24,100
Mvaa 60,20,60
W_ 4
Mvaa 96,28,60
s_nop 0
Ta 20,v1,0
Ta 22,v1,512
Mvaa 96,32,100
buffer_load_dword a254, v10, s[28:31], s55 offen
Mvaa 104,28,64
Mvaa 104,32,104
s_nop 0
Ta 24,v1,4096
Ta 26,v1,4608
Mvaa 112,28,68
Mvaa 112,32,108
buffer_load_dword a254, v12, s[24:27], s63 offen offset:768
Mvaa 120,28,72
Mvaa 120,32,112
Mvaa 128,28,76
Mvaa 128,32,116
W_ 4
Mvaa 132,224,116
s_nop 0
Ta 28,v1,8
Ta 30,v1,520
Mvaa 132,36,76
Mvaa 124,224,112
Mvaa 124,36,72
s_nop 0
Ta 32,v1,4104
Ta 34,v1,4616
Mvaa 116,224,108
Mvaa 116,36,68
Mvaa 108,224,104
Mvaa 108,36,64
Mvaa 100,224,100
s_waitcnt vmcnt(15) lgkmcnt(0)
s_barrier
Mvaa 100,36,60
S_ s65,s59,2359296
Da 140,v2,20480
Mvaa 136,20,60
S_ m0,s52,-256
Ta 36,%0,8192
Ta 38,%0,8704
Mvaa 136,24,100
L_ v6,28,s65,256
Mvaa 144,20,64
S_ s65,s59,2457600
Da 180,v3,20480
Mvaa 144,24,104
S_ m0,s52,768
Ta 224,%0,12288
Ta 226,%0,12800
Mvaa 152,20,68
Mvaa 152,24,108
Da 144,v2,22528
Mvaa 160,20,72
L_ v6,28,s65,256
Mvaa 160,24,112
S_ s65,s59,2555904
Mvaa 168,20,76
S_ m0,s52,1792
Da 184,v3,22528
Mvaa 168,24,116
Mvaa 172,32,116
s_nop 0
Ta 20,%0,8200
Ta 22,%0,8712
Mvaa 172,28,76
Da 148,v2,24576
Mvaa 164,32,112
L_ v6,28,s65,256
Mvaa 164,28,72
S_ s65,s59,2654208
Ta 24,%0,12296
Ta 26,%0,12808
Mvaa 156,32,108
S_ m0,s52,2816
Da 188,v3,24576
Mvaa 156,28,68
Mvaa 148,32,104
L_ v6,28,s65,256
Mvaa 148,28,64
Da 152,v2,26624
Mvaa 140,32,100
Mvaa 140,28,60
W_ 9
Mvaa 176,36,60
s_nop 0
Ta 28,v1,8192
Ta 30,v1,8704
Mvaa 176,224,100
Da 192,v3,26624
Mvaa 184,36,64
Mvaa 184,224,104
s_nop 0
Ta 32,v1,12288
Ta 34,v1,12800
Mvaa 192,36,68
Da 156,v2,28672
Mvaa 192,224,108
Mvaa 200,36,72
Da 196,v3,28672
Mvaa 200,224,112
Mvaa 208,36,76
Mvaa 208,224,116
W_ 9
Mvaa 212,24,116
s_nop 0
Ta 36,v1,8200
Ta 38,v1,8712
Mvaa 212,20,76
Mvaa 204,24,112
Mvaa 204,20,72
s_nop 0
Ta 224,v1,12296
Ta 226,v1,12808
Mvaa 196,24,108
Mvaa 196,20,68
Mvaa 188,24,104
Mvaa 188,20,64
Mvaa 180,24,100
s_waitcnt vmcnt(15) lgkmcnt(0)
s_barrier
Mvaa 180,20,60
S_ s65,s59,3145728
Mvaa 216,28,60
S_ m0,s52,8192
Ta 20,%0,16384
Ta 22,%0,16896
Mvaa 216,32,100
L_ v6,28,s65,0
Mvaa 224,28,64
S_ s65,s59,3244032
Mvaa 224,32,104
S_ m0,s52,9216
Ta 24,%0,20480
Ta 26,%0,20992
Mvaa 232,28,68
.Lpad_q1_1:
s_cbranch_execz .Lhop_q1_r
Mvaa 232,32,108
L_ v6,28,s65,0
Mvaa 240,28,72
S_ s65,s59,3342336
Mvaa 240,32,112
S_ m0,s52,10240
Mvaa 248,28,76
Mvaa 248,32,116
Mvaa 252,224,116
s_nop 0
Ta 28,%0,16392
Ta 30,%0,16904
Mvaa 252,36,76
L_ v6,28,s65,0
Mvaa 244,224,112
S_ s65,s59,3440640
Mvaa 244,36,72
S_ m0,s52,11264
Ta 32,%0,20488
Ta 34,%0,21000
Mvaa 236,224,108
Mvaa 236,36,68
L_ v6,28,s65,0
Mvaa 228,224,104
S_ s65,s60,640
Mvaa 228,36,64
S_ m0,s50,20480
Mvaa 220,224,100
Mvaa 220,36,60
W_ 4
Mvaa 16,20,140
s_nop 0
Ta 36,v1,16384
Ta 38,v1,16896
Mvaa 16,24,180
L_ v4,24,s65,0
Mvaa 24,20,144
S_ s65,s60,10880
Mvaa 24,24,184
S_ m0,s50,21504
Ta 224,v1,20480
Ta 226,v1,20992
Mvaa 32,20,148
Mvaa 32,24,188
L_ v5,24,s65,0
Mvaa 40,20,152
S_ s65,s60,21120
Mvaa 40,24,192
S_ m0,s50,22528
Mvaa 48,20,156
Mvaa 48,24,196
W_ 4
Mvaa 52,32,196
s_nop 0
Ta 20,v1,16392
Ta 22,v1,16904
Mvaa 52,28,156
L_ v4,24,s65,0
Mvaa 44,32,192
S_ s65,s60,31360
Mvaa 44,28,152
S_ m0,s50,23552
Ta 24,v1,20488
Ta 26,v1,21000
Mvaa 36,32,188
Mvaa 36,28,148
L_ v5,24,s65,0
Mvaa 28,32,184
S_ s65,s60,41600
Mvaa 28,28,144
S_ m0,s50,24576
Mvaa 20,32,180
s_waitcnt vmcnt(14) lgkmcnt(0)
s_barrier
Mvaa 20,28,140
L_ v4,24,s65,0
Mvaa 56,36,140
S_ s65,s59,3145728
Ta 28,%0,24576
Ta 30,%0,25088
Mvaa 56,224,180
S_ m0,s52,16256
Mvaa 64,36,144
Mvaa 64,224,184
s_nop 0
Ta 32,%0,28672
Ta 34,%0,29184
Mvaa 72,36,148
L_ v6,28,s65,128
Mvaa 72,224,188
S_ s65,s59,3244032
Mvaa 80,36,152
S_ m0,s52,17280
Mvaa 80,224,192
Mvaa 88,36,156
L_ v6,28,s65,128
Mvaa 88,224,196
S_ s65,s59,3342336
Mvaa 92,24,196
S_ m0,s52,18304
Ta 36,%0,24584
Ta 38,%0,25096
Mvaa 92,20,156
S_ s55,s56,5505024
Mvaa 84,24,192
L_ v6,28,s65,128
Mvaa 84,20,152
S_ s65,s59,3440640
Ta 224,%0,28680
Ta 226,%0,29192
Mvaa 76,24,188
S_ m0,s52,19328
Mvaa 76,20,148
Mvaa 68,24,184
L_ v6,28,s65,128
Mvaa 68,20,144
Mvaa 60,24,180
Mvaa 60,20,140
W_ 4
Mvaa 96,28,140
s_nop 0
Ta 20,v1,24576
Ta 22,v1,25088
Mvaa 96,32,180
buffer_load_dword a254, v10, s[28:31], s55 offen
Mvaa 104,28,144
Mvaa 104,32,184
s_nop 0
Ta 24,v1,28672
Ta 26,v1,29184
Mvaa 112,28,148
Mvaa 112,32,188
buffer_load_dword a254, v12, s[24:27], s63 offen offset:896
Mvaa 120,28,152
Mvaa 120,32,192
Mvaa 128,28,156
Mvaa 128,32,196
W_ 4
Mvaa 132,224,196
s_nop 0
Ta 28,v1,24584
Ta 30,v1,25096
Mvaa 132,36,156
Mvaa 124,224,192
Mvaa 124,36,152
s_nop 0
Ta 32,v1,28680
Ta 34,v1,29192
Mvaa 116,224,188
Mvaa 116,36,148
Mvaa 108,224,184
Mvaa 108,36,144
Mvaa 100,224,180
s_waitcnt vmcnt(15) lgkmcnt(0)
s_barrier
Mvaa 100,36,140
S_ s65,s59,3145728
Da 60,v2,0
Mvaa 136,20,140
S_ m0,s52,24320
Ta 36,%0,0
Ta 38,%0,512
Mvaa 136,24,180
L_ v6,28,s65,256
Mvaa 144,20,144
S_ s65,s59,3244032
Da 100,v3,0
Mvaa 144,24,184
S_ m0,s52,25344
Ta 224,%0,4096
Ta 226,%0,4608
Mvaa 152,20,148
Mvaa 152,24,188
Da 64,v2,2048
Mvaa 160,20,152
L_ v6,28,s65,256
Mvaa 160,24,192
S_ s65,s59,3342336
Mvaa 168,20,156
S_ m0,s52,26368
Da 104,v3,2048
Mvaa 168,24,196
Mvaa 172,32,196
s_nop 0
Ta 20,%0,8
Ta 22,%0,520
Mvaa 172,28,156
Da 68,v2,4096
Mvaa 164,32,192
L_ v6,28,s65,256
Mvaa 164,28,152
S_ s65,s59,3440640
Ta 24,%0,4104
Ta 26,%0,4616
Mvaa 156,32,188
S_ m0,s52,27392
Da 108,v3,4096
Mvaa 156,28,148
Mvaa 148,32,184
L_ v6,28,s65,256
Mvaa 148,28,144
Da 72,v2,6144
Mvaa 140,32,180
Mvaa 140,28,140
W_ 9
Mvaa 176,36,140
s_nop 0
Ta 28,v1,0
Ta 30,v1,512
Mvaa 176,224,180
Da 112,v3,6144
Mvaa 184,36,144
Mvaa 184,224,184
s_nop 0
Ta 32,v1,4096
Ta 34,v1,4608
Mvaa 192,36,148
Da 76,v2,8192
Mvaa 192,224,188
Mvaa 200,36,152
Da 116,v3,8192
Mvaa 200,224,192
Mvaa 208,36,156
Mvaa 208,224,196
W_ 9
Mvaa 212,24,196
s_nop 0
Ta 36,v1,8
Ta 38,v1,520
Mvaa 212,20,156
Mvaa 204,24,192
Mvaa 204,20,152
s_nop 0
Ta 224,v1,4104
Ta 226,v1,4616
Mvaa 196,24,188
Mvaa 196,20,148
Mvaa 188,24,184
Mvaa 188,20,144
Mvaa 180,24,180
s_waitcnt vmcnt(15) lgkmcnt(0)
s_barrier
Mvaa 180,20,140
S_ s65,s59,3932160
Mvaa 216,28,140
S_ m0,s52,0
Ta 20,%0,8192
Ta 22,%0,8704
Mvaa 216,32,180
L_ v6,28,s65,0
Mvaa 224,28,144
S_ s65,s59,4030464
Mvaa 224,32,184
S_ m0,s52,1024
Ta 24,%0,12288
Ta 26,%0,12800
Mvaa 232,28,148
Mvaa 232,32,188
L_ v6,28,s65,0
Mvaa 240,28,152
S_ s65,s59,4128768
Mvaa 240,32,192
S_ m0,s52,2048
Mvaa 248,28,156
Mvaa 248,32,196
Mvaa 252,224,196
s_nop 0
Ta 28,%0,8200
Ta 30,%0,8712
Mvaa 252,36,156
L_ v6,28,s65,0
Mvaa 244,224,192
S_ s65,s59,4227072
Mvaa 244,36,152
S_ m0,s52,3072
Ta 32,%0,12296
Ta 34,%0,12808
Mvaa 236,224,188
Mvaa 236,36,148
L_ v6,28,s65,0
Mvaa 228,224,184
S_ s65,s60,768
Mvaa 228,36,144
S_ m0,s50,0
Mvaa 220,224,180
Mvaa 220,36,140
W_ 4
Mvaa 16,20,60
s_nop 0
Ta 36,v1,8192
Ta 38,v1,8704
Mvaa 16,24,100
L_ v4,24,s65,0
Mvaa 24,20,64
S_ s65,s60,11008
R_ v13,a248
Mvaa 24,24,104
S_ m0,s50,1024
Ta 224,v1,12288
Ta 226,v1,12800
Mvaa 32,20,68
v_min_u32 v13, 240, v13
Mvaa 32,24,108
L_ v5,24,s65,0
Mvaa 40,20,72
S_ s65,s60,21248
v_lshlrev_b32 v13, 3, v13
Mvaa 40,24,112
S_ m0,s50,2048
Mvaa 48,20,76
Mvaa 48,24,116
W_ 4
Mvaa 52,32,116
s_nop 0
Ta 20,v1,8200
Ta 22,v1,8712
Mvaa 52,28,76
L_ v4,24,s65,0
Mvaa 44,32,112
S_ s65,s60,31488
Mvaa 44,28,72
S_ m0,s50,3072
Ta 24,v1,12296
Ta 26,v1,12808
Mvaa 36,32,108
Mvaa 36,28,68
L_ v5,24,s65,0
Mvaa 28,32,104
S_ s65,s60,41728
Mvaa 28,28,64
S_ m0,s50,4096
Mvaa 20,32,100
s_waitcnt vmcnt(14) lgkmcnt(0)
s_barrier
Mvaa 20,28,60
L_ v4,24,s65,0
Mvaa 56,36,60
S_ s65,s59,3932160
Ta 28,%0,16384
Ta 30,%0,16896
Mvaa 56,224,100
S_ m0,s52,8064
Mvaa 64,36,64
Mvaa 64,224,104
s_nop 0
Ta 32,%0,20480
Ta 34,%0,20992
Mvaa 72,36,68
L_ v6,28,s65,128
Mvaa 72,224,108
S_ s65,s59,4030464
Mvaa 80,36,72
S_ m0,s52,9088
Mvaa 80,224,112
Mvaa 88,36,76
L_ v6,28,s65,128
Mvaa 88,224,116
S_ s65,s59,4128768
Mvaa 92,24,116
S_ m0,s52,10112
Ta 36,%0,16392
Ta 38,%0,16904
Mvaa 92,20,76
S_ s55,s56,6291456
Mvaa 84,24,112
L_ v6,28,s65,128
Mvaa 84,20,72
S_ s65,s59,4227072
Ta 224,%0,20488
Ta 226,%0,21000
Mvaa 76,24,108
S_ m0,s52,11136
Mvaa 76,20,68
Mvaa 68,24,104
L_ v6,28,s65,128
Mvaa 68,20,64
Mvaa 60,24,100
Mvaa 60,20,60
W_ 4
Mvaa 96,28,60
s_nop 0
Ta 20,v1,16384
Ta 22,v1,16896
Mvaa 96,32,100
buffer_load_dword a220, v13, s[44:47], s66 offen
Mvaa 104,28,64
Mvaa 104,32,104
s_nop 0
Ta 24,v1,20480
Ta 26,v1,20992
Mvaa 112,28,68
Mvaa 112,32,108
buffer_load_dword a254, v10, s[28:31], s55 offen
Mvaa 120,28,72
Mvaa 120,32,112
Mvaa 128,28,76
Mvaa 128,32,116
W_ 4
Mvaa 132,224,116
s_nop 0
Ta 28,v1,16392
Ta 30,v1,16904
Mvaa 132,36,76
buffer_load_dword a254, v12, s[24:27], s63 offen offset:1024
Mvaa 124,224,112
Mvaa 124,36,72
s_nop 0
Ta 32,v1,20488
Ta 34,v1,21000
Mvaa 116,224,108
Mvaa 116,36,68
Mvaa 108,224,104
Mvaa 108,36,64
Mvaa 100,224,100
s_waitcnt vmcnt(16) lgkmcnt(0)
s_barrier
Mvaa 100,36,60
S_ s65,s59,3932160
Da 140,v2,20480
Mvaa 136,20,60
S_ m0,s52,16128
Ta 36,%0,24576
Ta 38,%0,25088
Mvaa 136,24,100
L_ v6,28,s65,256
Mvaa 144,20,64
S_ s65,s59,4030464
Da 180,v3,20480
Mvaa 144,24,104
S_ m0,s52,17152
Ta 224,%0,28672
Ta 226,%0,29184
Mvaa 152,20,68
Mvaa 152,24,108
Da 144,v2,22528
Mvaa 160,20,72
L_ v6,28,s65,256
Mvaa 160,24,112
S_ s65,s59,4128768
Mvaa 168,20,76
S_ m0,s52,18176
Da 184,v3,22528
Mvaa 168,24,116
Mvaa 172,32,116
s_nop 0
Ta 20,%0,24584
Ta 22,%0,25096
Mvaa 172,28,76
Da 148,v2,24576
Mvaa 164,32,112
L_ v6,28,s65,256
Mvaa 164,28,72
S_ s65,s59,4227072
Ta 24,%0,28680
Ta 26,%0,29192
Mvaa 156,32,108
S_ m0,s52,19200
Da 188,v3,24576
Mvaa 156,28,68
Mvaa 148,32,104
L_ v6,28,s65,256
Mvaa 148,28,64
Da 152,v2,26624
Mvaa 140,32,100
Mvaa 140,28,60
W_ 9
Mvaa 176,36,60
s_nop 0
Ta 28,v1,24576
Ta 30,v1,25088
Mvaa 176,224,100
Da 192,v3,26624
Mvaa 184,36,64
Mvaa 184,224,104
s_nop 0
Ta 32,v1,28672
Ta 34,v1,29184
Mvaa 192,36,68
Da 156,v2,28672
Mvaa 192,224,108
Mvaa 200,36,72
Da 196,v3,28672
Mvaa 200,224,112
Mvaa 208,36,76
Mvaa 208,224,116
W_ 9
Mvaa 212,24,116
s_nop 0
Ta 36,v1,24584
Ta 38,v1,25096
Mvaa 212,20,76
Mvaa 204,24,112
Mvaa 204,20,72
s_nop 0
Ta 224,v1,28680
Ta 226,v1,29192
Mvaa 196,24,108
Mvaa 196,20,68
Mvaa 188,24,104
Mvaa 188,20,64
Mvaa 180,24,100
s_waitcnt vmcnt(16) lgkmcnt(0)
s_barrier
Mvaa 180,20,60
S_ s65,s59,4718592
Mvaa 216,28,60
S_ m0,s52,24576
Ta 20,%0,0
Ta 22,%0,512
Mvaa 216,32,100
L_ v6,28,s65,0
Mvaa 224,28,64
S_ s65,s59,4816896
Mvaa 224,32,104
S_ m0,s52,25600
Ta 24,%0,4096
Ta 26,%0,4608
Mvaa 232,28,68
Mvaa 232,32,108
L_ v6,28,s65,0
Mvaa 240,28,72
S_ s65,s59,4915200
Mvaa 240,32,112
S_ m0,s52,26624
Mvaa 248,28,76
Mvaa 248,32,116
Mvaa 252,224,116
s_nop 0
Ta 28,%0,8
Ta 30,%0,520
Mvaa 252,36,76
L_ v6,28,s65,0
Mvaa 244,224,112
S_ s65,s59,5013504
Mvaa 244,36,72
S_ m0,s52,27648
Ta 32,%0,4104
Ta 34,%0,4616
Mvaa 236,224,108
.Lpad_q1_2:
s_cbranch_execz .Lhop_q1_r
Mvaa 236,36,68
L_ v6,28,s65,0
Mvaa 228,224,104
S_ s65,s60,896
Mvaa 228,36,64
S_ m0,s50,20480
Mvaa 220,224,100
Mvaa 220,36,60
W_ 4
Mvaa 16,20,140
s_nop 0
Ta 36,v1,0
Ta 38,v1,512
Mvaa 16,24,180
L_ v4,24,s65,0
Mvaa 24,20,144
S_ s65,s60,11136
Mvaa 24,24,184
S_ m0,s50,21504
Ta 224,v1,4096
Ta 226,v1,4608
Mvaa 32,20,148
Mvaa 32,24,188
L_ v5,24,s65,0
Mvaa 40,20,152
S_ s65,s60,21376
Mvaa 40,24,192
S_ m0,s50,22528
Mvaa 48,20,156
Mvaa 48,24,196
W_ 4
Mvaa 52,32,196
s_nop 0
Ta 20,v1,8
Ta 22,v1,520
Mvaa 52,28,156
L_ v4,24,s65,0
Mvaa 44,32,192
S_ s65,s60,31616
Mvaa 44,28,152
S_ m0,s50,23552
Ta 24,v1,4104
Ta 26,v1,4616
Mvaa 36,32,188
Mvaa 36,28,148
L_ v5,24,s65,0
Mvaa 28,32,184
S_ s65,s60,41856
Mvaa 28,28,144
S_ m0,s50,24576
Mvaa 20,32,180
s_waitcnt vmcnt(15) lgkmcnt(0)
s_barrier
Mvaa 20,28,140
L_ v4,24,s65,0
Mvaa 56,36,140
S_ s65,s59,4718592
Ta 28,%0,8192
Ta 30,%0,8704
Mvaa 56,224,180
S_ m0,s52,-128
Mvaa 64,36,144
Mvaa 64,224,184
s_nop 0
Ta 32,%0,12288
Ta 34,%0,12800
Mvaa 72,36,148
L_ v6,28,s65,128
Mvaa 72,224,188
S_ s65,s59,4816896
Mvaa 80,36,152
S_ m0,s52,896
Mvaa 80,224,192
Mvaa 88,36,156
L_ v6,28,s65,128
Mvaa 88,224,196
S_ s65,s59,4915200
Mvaa 92,24,196
S_ m0,s52,1920
Ta 36,%0,8200
Ta 38,%0,8712
Mvaa 92,20,156
S_ s55,s56,7077888
Mvaa 84,24,192
L_ v6,28,s65,128
Mvaa 84,20,152
S_ s65,s59,5013504
Ta 224,%0,12296
Ta 226,%0,12808
Mvaa 76,24,188
S_ m0,s52,2944
Mvaa 76,20,148
Mvaa 68,24,184
L_ v6,28,s65,128
Mvaa 68,20,144
Mvaa 60,24,180
Mvaa 60,20,140
W_ 4
Mvaa 96,28,140
s_nop 0
Ta 20,v1,8192
Ta 22,v1,8704
Mvaa 96,32,180
buffer_load_dword a254, v10, s[28:31], s55 offen
Mvaa 104,28,144
Mvaa 104,32,184
s_nop 0
Ta 24,v1,12288
Ta 26,v1,12800
Mvaa 112,28,148
Mvaa 112,32,188
buffer_load_dword a254, v12, s[24:27], s63 offen offset:1152
Mvaa 120,28,152
Mvaa 120,32,192
Mvaa 128,28,156
Mvaa 128,32,196
W_ 4
Mvaa 132,224,196
s_nop 0
Ta 28,v1,8200
Ta 30,v1,8712
Mvaa 132,36,156
Mvaa 124,224,192
Mvaa 124,36,152
s_nop 0
Ta 32,v1,12296
Ta 34,v1,12808
Mvaa 116,224,188
Mvaa 116,36,148
Mvaa 108,224,184
Mvaa 108,36,144
Mvaa 100,224,180
s_waitcnt vmcnt(15) lgkmcnt(0)
s_barrier
Mvaa 100,36,140
S_ s65,s59,4718592
Da 60,v2,0
Mvaa 136,20,140
S_ m0,s52,7936
Ta 36,%0,16384
Ta 38,%0,16896
Mvaa 136,24,180
L_ v6,28,s65,256
Mvaa 144,20,144
S_ s65,s59,4816896
Da 100,v3,0
Mvaa 144,24,184
S_ m0,s52,8960
Ta 224,%0,20480
Ta 226,%0,20992
Mvaa 152,20,148
Mvaa 152,24,188
Da 64,v2,2048
Mvaa 160,20,152
L_ v6,28,s65,256
Mvaa 160,24,192
S_ s65,s59,4915200
Mvaa 168,20,156
S_ m0,s52,9984
Da 104,v3,2048
Mvaa 168,24,196
Mvaa 172,32,196
s_nop 0
Ta 20,%0,16392
Ta 22,%0,16904
Mvaa 172,28,156
Da 68,v2,4096
Mvaa 164,32,192
L_ v6,28,s65,256
Mvaa 164,28,152
S_ s65,s59,5013504
Ta 24,%0,20488
Ta 26,%0,21000
Mvaa 156,32,188
S_ m0,s52,11008
Da 108,v3,4096
Mvaa 156,28,148
Mvaa 148,32,184
L_ v6,28,s65,256
)A"
R"A(Mvaa 148,28,144
Da 72,v2,6144
Mvaa 140,32,180
Mvaa 140,28,140
W_ 9
Mvaa 176,36,140
s_nop 0
Ta 28,v1,16384
Ta 30,v1,16896
Mvaa 176,224,180
Da 112,v3,6144
Mvaa 184,36,144
Mvaa 184,224,184
s_nop 0
Ta 32,v1,20480
Ta 34,v1,20992
Mvaa 192,36,148
Da 76,v2,8192
Mvaa 192,224,188
Mvaa 200,36,152
Da 116,v3,8192
Mvaa 200,224,192
Mvaa 208,36,156
Mvaa 208,224,196
W_ 9
Mvaa 212,24,196
s_nop 0
Ta 36,v1,16392
Ta 38,v1,16904
Mvaa 212,20,156
Mvaa 204,24,192
Mvaa 204,20,152
s_nop 0
Ta 224,v1,20488
Ta 226,v1,21000
Mvaa 196,24,188
Mvaa 196,20,148
Mvaa 188,24,184
Mvaa 188,20,144
Mvaa 180,24,180
s_waitcnt vmcnt(15) lgkmcnt(0)
s_barrier
Mvaa 180,20,140
S_ s65,s59,5505024
Mvaa 216,28,140
S_ m0,s52,16384
Ta 20,%0,24576
Ta 22,%0,25088
Mvaa 216,32,180
L_ v6,28,s65,0
Mvaa 224,28,144
S_ s65,s59,5603328
Mvaa 224,32,184
S_ m0,s52,17408
Ta 24,%0,28672
Ta 26,%0,29184
Mvaa 232,28,148
Mvaa 232,32,188
L_ v6,28,s65,0
Mvaa 240,28,152
S_ s65,s59,5701632
Mvaa 240,32,192
S_ m0,s52,18432
Mvaa 248,28,156
Mvaa 248,32,196
Mvaa 252,224,196
s_nop 0
Ta 28,%0,24584
Ta 30,%0,25096
Mvaa 252,36,156
L_ v6,28,s65,0
Mvaa 244,224,192
S_ s65,s59,5799936
Mvaa 244,36,152
S_ m0,s52,19456
Ta 32,%0,28680
Ta 34,%0,29192
Mvaa 236,224,188
Mvaa 236,36,148
L_ v6,28,s65,0
Mvaa 228,224,184
S_ s65,s60,1024
Mvaa 228,36,144
S_ m0,s50,0
Mvaa 220,224,180
Mvaa 220,36,140
W_ 4
Mvaa 16,20,60
s_nop 0
Ta 36,v1,24576
Ta 38,v1,25088
Mvaa 16,24,100
L_ v4,24,s65,0
Mvaa 24,20,64
S_ s65,s60,11264
Mvaa 24,24,104
S_ m0,s50,1024
Ta 224,v1,28672
Ta 226,v1,29184
Mvaa 32,20,68
Mvaa 32,24,108
L_ v5,24,s65,0
Mvaa 40,20,72
S_ s65,s60,21504
Mvaa 40,24,112
S_ m0,s50,2048
Mvaa 48,20,76
Mvaa 48,24,116
W_ 4
Mvaa 52,32,116
s_nop 0
Ta 20,v1,24584
Ta 22,v1,25096
Mvaa 52,28,76
L_ v4,24,s65,0
Mvaa 44,32,112
S_ s65,s60,31744
Mvaa 44,28,72
S_ m0,s50,3072
Ta 24,v1,28680
Ta 26,v1,29192
Mvaa 36,32,108
Mvaa 36,28,68
L_ v5,24,s65,0
Mvaa 28,32,104
S_ s65,s60,41984
Mvaa 28,28,64
S_ m0,s50,4096
Mvaa 20,32,100
s_waitcnt vmcnt(14) lgkmcnt(0)
s_barrier
Mvaa 20,28,60
L_ v4,24,s65,0
Mvaa 56,36,60
S_ s65,s59,5505024
Ta 28,%0,0
Ta 30,%0,512
Mvaa 56,224,100
S_ m0,s52,24448
Mvaa 64,36,64
Mvaa 64,224,104
s_nop 0
Ta 32,%0,4096
Ta 34,%0,4608
Mvaa 72,36,68
L_ v6,28,s65,128
Mvaa 72,224,108
S_ s65,s59,5603328
Mvaa 80,36,72
S_ m0,s52,25472
Mvaa 80,224,112
Mvaa 88,36,76
L_ v6,28,s65,128
Mvaa 88,224,116
S_ s65,s59,5701632
Mvaa 92,24,116
S_ m0,s52,26496
Ta 36,%0,8
Ta 38,%0,520
Mvaa 92,20,76
Mvaa 84,24,112
L_ v6,28,s65,128
Mvaa 84,20,72
S_ s65,s59,5799936
Ta 224,%0,4104
Ta 226,%0,4616
Mvaa 76,24,108
S_ m0,s52,27520
Mvaa 76,20,68
Mvaa 68,24,104
L_ v6,28,s65,128
Mvaa 68,20,64
Mvaa 60,24,100
Mvaa 60,20,60
W_ 4
Mvaa 96,28,60
s_nop 0
Ta 20,v1,0
Ta 22,v1,512
Mvaa 96,32,100
Mvaa 104,28,64
Mvaa 104,32,104
s_nop 0
Ta 24,v1,4096
Ta 26,v1,4608
Mvaa 112,28,68
Mvaa 112,32,108
Mvaa 120,28,72
Mvaa 120,32,112
Mvaa 128,28,76
Mvaa 128,32,116
W_ 4
Mvaa 132,224,116
s_nop 0
Ta 28,v1,8
Ta 30,v1,520
Mvaa 132,36,76
Mvaa 124,224,112
Mvaa 124,36,72
s_nop 0
Ta 32,v1,4104
Ta 34,v1,4616
Mvaa 116,224,108
Mvaa 116,36,68
Mvaa 108,224,104
Mvaa 108,36,64
Mvaa 100,224,100
s_waitcnt vmcnt(13) lgkmcnt(0)
s_barrier
Mvaa 100,36,60
S_ s65,s59,5505024
Da 140,v2,20480
Mvaa 136,20,60
S_ m0,s52,-256
Ta 36,%0,8192
Ta 38,%0,8704
Mvaa 136,24,100
L_ v6,28,s65,256
Mvaa 144,20,64
S_ s65,s59,5603328
Da 180,v3,20480
Mvaa 144,24,104
S_ m0,s52,768
Ta 224,%0,12288
Ta 226,%0,12800
Mvaa 152,20,68
Mvaa 152,24,108
Da 144,v2,22528
Mvaa 160,20,72
L_ v6,28,s65,256
Mvaa 160,24,112
S_ s65,s59,5701632
Mvaa 168,20,76
S_ m0,s52,1792
Da 184,v3,22528
Mvaa 168,24,116
Mvaa 172,32,116
s_nop 0
Ta 20,%0,8200
Ta 22,%0,8712
Mvaa 172,28,76
Da 148,v2,24576
Mvaa 164,32,112
L_ v6,28,s65,256
Mvaa 164,28,72
S_ s65,s59,5799936
Ta 24,%0,12296
Ta 26,%0,12808
Mvaa 156,32,108
S_ m0,s52,2816
Da 188,v3,24576
Mvaa 156,28,68
Mvaa 148,32,104
L_ v6,28,s65,256
Mvaa 148,28,64
Da 152,v2,26624
Mvaa 140,32,100
Mvaa 140,28,60
W_ 9
Mvaa 176,36,60
s_nop 0
Ta 28,v1,8192
Ta 30,v1,8704
Mvaa 176,224,100
Da 192,v3,26624
Mvaa 184,36,64
Mvaa 184,224,104
s_nop 0
Ta 32,v1,12288
Ta 34,v1,12800
Mvaa 192,36,68
Da 156,v2,28672
Mvaa 192,224,108
Mvaa 200,36,72
Da 196,v3,28672
Mvaa 200,224,112
Mvaa 208,36,76
Mvaa 208,224,116
W_ 9
Mvaa 212,24,116
s_nop 0
Ta 36,v1,8200
Ta 38,v1,8712
Mvaa 212,20,76
Mvaa 204,24,112
Mvaa 204,20,72
s_nop 0
Ta 224,v1,12296
Ta 226,v1,12808
Mvaa 196,24,108
Mvaa 196,20,68
Mvaa 188,24,104
Mvaa 188,20,64
Mvaa 180,24,100
s_waitcnt vmcnt(13) lgkmcnt(0)
s_barrier
Mvaa 180,20,60
S_ s65,s59,6291456
Mvaa 216,28,60
S_ m0,s52,7936
Ta 20,%0,16384
Ta 22,%0,16896
Mvaa 216,32,100
L_ v6,28,s65,256
Mvaa 224,28,64
S_ s65,s59,6389760
Mvaa 224,32,104
S_ m0,s52,8960
Ta 24,%0,20480
Ta 26,%0,20992
Mvaa 232,28,68
Mvaa 232,32,108
L_ v6,28,s65,256
Mvaa 240,28,72
S_ s65,s59,6488064
Mvaa 240,32,112
S_ m0,s52,9984
Mvaa 248,28,76
Mvaa 248,32,116
Mvaa 252,224,116
s_nop 0
Ta 28,%0,16392
Ta 30,%0,16904
Mvaa 252,36,76
L_ v6,28,s65,256
Mvaa 244,224,112
S_ s65,s59,6586368
Mvaa 244,36,72
S_ m0,s52,11008
Ta 32,%0,20488
Ta 34,%0,21000
Mvaa 236,224,108
Mvaa 236,36,68
L_ v6,28,s65,256
Mvaa 228,224,104
S_ s65,s60,1152
Mvaa 228,36,64
S_ m0,s50,20480
Mvaa 220,224,100
Mvaa 220,36,60
W_ 4
Mvaa 16,20,140
s_nop 0
Ta 36,v1,16384
Ta 38,v1,16896
Mvaa 16,24,180
L_ v4,24,s65,0
Mvaa 24,20,144
S_ s65,s60,11392
Mvaa 24,24,184
S_ m0,s50,21504
Ta 224,v1,20480
Ta 226,v1,20992
Mvaa 32,20,148
.Lpad_q1_3:
s_cbranch_execz .Lhop_q1_r
Mvaa 32,24,188
L_ v5,24,s65,0
Mvaa 40,20,152
S_ s65,s60,21632
Mvaa 40,24,192
S_ m0,s50,22528
Mvaa 48,20,156
Mvaa 48,24,196
W_ 4
Mvaa 52,32,196
s_nop 0
Ta 20,v1,16392
Ta 22,v1,16904
Mvaa 52,28,156
L_ v4,24,s65,0
Mvaa 44,32,192
S_ s65,s60,31872
Mvaa 44,28,152
S_ m0,s50,23552
Ta 24,v1,20488
Ta 26,v1,21000
Mvaa 36,32,188
Mvaa 36,28,148
L_ v5,24,s65,0
Mvaa 28,32,184
S_ s65,s60,42112
Mvaa 28,28,144
S_ m0,s50,24576
Mvaa 20,32,180
s_waitcnt vmcnt(12) lgkmcnt(0)
s_barrier
Mvaa 20,28,140
L_ v4,24,s65,0
Mvaa 56,36,140
S_ s65,s59,7077888
Ta 28,%0,24576
Ta 30,%0,25088
Mvaa 56,224,180
S_ m0,s52,16128
Mvaa 64,36,144
Mvaa 64,224,184
s_nop 0
Ta 32,%0,28672
Ta 34,%0,29184
Mvaa 72,36,148
L_ v6,28,s65,256
Mvaa 72,224,188
S_ s65,s59,7176192
Mvaa 80,36,152
S_ m0,s52,17152
Mvaa 80,224,192
Mvaa 88,36,156
L_ v6,28,s65,256
Mvaa 88,224,196
S_ s65,s59,7274496
Mvaa 92,24,196
S_ m0,s52,18176
Ta 36,%0,24584
Ta 38,%0,25096
Mvaa 92,20,156
Mvaa 84,24,192
L_ v6,28,s65,256
Mvaa 84,20,152
S_ s65,s59,7372800
Ta 224,%0,28680
Ta 226,%0,29192
Mvaa 76,24,188
S_ m0,s52,19200
Mvaa 76,20,148
Mvaa 68,24,184
L_ v6,28,s65,256
Mvaa 68,20,144
Mvaa 60,24,180
Mvaa 60,20,140
W_ 4
Mvaa 96,28,140
s_nop 0
Ta 20,v1,24576
Ta 22,v1,25088
Mvaa 96,32,180
Mvaa 104,28,144
Mvaa 104,32,184
s_nop 0
Ta 24,v1,28672
Ta 26,v1,29184
Mvaa 112,28,148
Mvaa 112,32,188
Mvaa 120,28,152
Mvaa 120,32,192
Mvaa 128,28,156
Mvaa 128,32,196
W_ 4
Mvaa 132,224,196
s_nop 0
Ta 28,v1,24584
Ta 30,v1,25096
Mvaa 132,36,156
Mvaa 124,224,192
Mvaa 124,36,152
s_nop 0
Ta 32,v1,28680
Ta 34,v1,29192
Mvaa 116,224,188
Mvaa 116,36,148
Mvaa 108,224,184
Mvaa 108,36,144
Mvaa 100,224,180
s_waitcnt vmcnt(13) lgkmcnt(0)
s_barrier
Mvaa 100,36,140
S_ s65,s59,6291456
Da 60,v2,0
Mvaa 136,20,140
S_ m0,s52,24448
Ta 36,%0,0
Ta 38,%0,512
Mvaa 136,24,180
L_ v6,28,s65,128
Mvaa 144,20,144
S_ s65,s59,6389760
Da 100,v3,0
Mvaa 144,24,184
S_ m0,s52,25472
Ta 224,%0,4096
Ta 226,%0,4608
Mvaa 152,20,148
Mvaa 152,24,188
Da 64,v2,2048
Mvaa 160,20,152
L_ v6,28,s65,128
Mvaa 160,24,192
S_ s65,s59,6488064
Mvaa 168,20,156
S_ m0,s52,26496
Da 104,v3,2048
Mvaa 168,24,196
Mvaa 172,32,196
s_nop 0
Ta 20,%0,8
Ta 22,%0,520
Mvaa 172,28,156
Da 68,v2,4096
Mvaa 164,32,192
L_ v6,28,s65,128
Mvaa 164,28,152
S_ s65,s59,6586368
Ta 24,%0,4104
Ta 26,%0,4616
Mvaa 156,32,188
S_ m0,s52,27520
Da 108,v3,4096
Mvaa 156,28,148
Mvaa 148,32,184
L_ v6,28,s65,128
Mvaa 148,28,144
Da 72,v2,6144
Mvaa 140,32,180
Mvaa 140,28,140
W_ 9
Mvaa 176,36,140
s_nop 0
Ta 28,v1,0
Ta 30,v1,512
Mvaa 176,224,180
Da 112,v3,6144
Mvaa 184,36,144
Mvaa 184,224,184
s_nop 0
Ta 32,v1,4096
Ta 34,v1,4608
Mvaa 192,36,148
Da 76,v2,8192
Mvaa 192,224,188
Mvaa 200,36,152
Da 116,v3,8192
Mvaa 200,224,192
Mvaa 208,36,156
Mvaa 208,224,196
W_ 9
Mvaa 212,24,196
s_nop 0
Ta 36,v1,8
Ta 38,v1,520
Mvaa 212,20,156
Mvaa 204,24,192
Mvaa 204,20,152
s_nop 0
Ta 224,v1,4104
Ta 226,v1,4616
Mvaa 196,24,188
Mvaa 196,20,148
Mvaa 188,24,184
Mvaa 188,20,144
Mvaa 180,24,180
s_waitcnt vmcnt(8) lgkmcnt(0)
s_barrier
Mvaa 180,20,140
S_ s65,s59,7077888
Mvaa 216,28,140
S_ m0,s52,-128
Ta 20,v1,8200
Ta 22,v1,8712
Mvaa 216,32,180
L_ v6,28,s65,128
Mvaa 224,28,144
S_ s65,s59,7176192
Mvaa 224,32,184
S_ m0,s52,896
Ta 24,v1,12296
Ta 26,v1,12808
Mvaa 232,28,148
Mvaa 232,32,188
L_ v6,28,s65,128
Mvaa 240,28,152
S_ s65,s59,7274496
Mvaa 240,32,192
S_ m0,s52,1920
Mvaa 248,28,156
Mvaa 248,32,196
Mvaa 252,224,196
s_nop 0
Ta 28,v1,8192
Ta 30,v1,8704
Mvaa 252,36,156
L_ v6,28,s65,128
Mvaa 244,224,192
S_ s65,s59,7372800
Mvaa 244,36,152
S_ m0,s52,2944
Ta 32,v1,12288
Ta 34,v1,12800
Mvaa 236,224,188
Mvaa 236,36,148
L_ v6,28,s65,128
Mvaa 228,224,184
S_ s65,s60,0
Mvaa 228,36,144
S_ m0,s50,0
Mvaa 220,224,180
Mvaa 220,36,140
W_ 4
Mvaa 220,20,60
s_nop 0
Ta 36,%0,8200
Ta 38,%0,8712
Mvaa 220,24,100
Da 140,v2,20480
Mvaa 228,20,64
L_ v4,24,s65,0
Mvaa 228,24,104
S_ s65,s60,10240
Ta 224,%0,12296
Ta 226,%0,12808
Mvaa 236,20,68
S_ m0,s50,1024
Da 180,v3,20480
Mvaa 236,24,108
Mvaa 244,20,72
Da 144,v2,22528
Mvaa 244,24,112
L_ v5,24,s65,0
Mvaa 252,20,76
S_ s65,s60,20480
Mvaa 252,24,116
W_ 7
Da 184,v3,22528
S_ m0,s50,2048
Mvaa 248,32,116
s_nop 0
Ta 20,%0,8192
Ta 22,%0,8704
Mvaa 248,28,76
L_ v4,24,s65,0
Mvaa 240,32,112
S_ s65,s60,30720
Da 148,v2,24576
Mvaa 240,28,72
S_ m0,s50,3072
Ta 24,%0,12288
Ta 26,%0,12800
Mvaa 232,32,108
Mvaa 232,28,68
Da 188,v3,24576
Mvaa 224,32,104
L_ v5,24,s65,0
Mvaa 224,28,64
S_ s65,s60,40960
Mvaa 216,32,100
s_mov_b64 exec, 0
s_bitcmp1_b32 s4, 1
s_cbranch_scc1 .Lhop_q2_b
s_bitcmp1_b32 s4, 0
s_cbranch_scc1 .Lpad_q2_1
s_branch .Lpad_q2_0
.Lhop_q2_b:
s_bitcmp1_b32 s4, 0
s_cbranch_scc1 .Lpad_q2_3
s_branch .Lpad_q2_2
.Lhop_q2_r:
s_mov_b64 exec, -1
s_nop 1
.Lqy_late:
s_waitcnt vmcnt(12) lgkmcnt(0)
s_barrier
Da 152,v2,26624
Mvaa 216,28,60
S_ m0,s50,4096
Mvaa 180,36,60
s_nop 0
Ta 28,v1,16392
Ta 30,v1,16904
Mvaa 180,224,100
Da 192,v3,26624
Mvaa 188,36,64
L_ v4,24,s65,0
Mvaa 188,224,104
S_ s65,s59,6291456
Ta 32,v1,20488
Ta 34,v1,21000
Mvaa 196,36,68
S_ m0,s52,8192
Da 156,v2,28672
Mvaa 196,224,108
Mvaa 204,36,72
L_ v6,28,s65,0
Mvaa 204,224,112
S_ s65,s59,6389760
Da 196,v3,28672
Mvaa 212,36,76
S_ m0,s52,9216
Mvaa 212,224,116
Mvaa 208,24,116
s_nop 0
Ta 36,v1,16384
Ta 38,v1,16896
Mvaa 208,20,76
L_ v6,28,s65,0
Mvaa 200,24,112
S_ s65,s59,6488064
Mvaa 200,20,72
S_ m0,s52,10240
Ta 224,v1,20480
Ta 226,v1,20992
Mvaa 192,24,108
Mvaa 192,20,68
L_ v6,28,s65,0
Mvaa 184,24,104
S_ s65,s59,6586368
Mvaa 184,20,64
S_ m0,s52,11264
Mvaa 176,24,100
Mvaa 176,20,60
W_ 6
Mvaa 220,28,140
s_nop 0
Ta 20,%0,16392
Ta 22,%0,16904
Mvaa 220,32,180
L_ v6,28,s65,0
Mvaa 228,28,144
Mvaa 228,32,184
s_nop 0
Ta 24,%0,20488
Ta 26,%0,21000
Mvaa 236,28,148
Mvaa 236,32,188
Mvaa 244,28,152
Mvaa 244,32,192
W_ 9
Mvaa 252,28,156
W_ 8
Mvaa 252,32,196
W_ 4
Mvaa 248,224,196
s_nop 0
Ta 28,%0,16384
Ta 30,%0,16896
Mvaa 248,36,156
Mvaa 240,224,192
Mvaa 240,36,152
s_nop 0
Ta 32,%0,20480
Ta 34,%0,20992
Mvaa 232,224,188
Mvaa 232,36,148
Mvaa 224,224,184
Mvaa 224,36,144
Mvaa 216,224,180
s_waitcnt vmcnt(13) lgkmcnt(0)
s_barrier
Mvaa 216,36,140
S_ s65,s59,7077888
Mvaa 180,20,140
S_ m0,s52,16384
Ta 36,v1,24584
Ta 38,v1,25096
Mvaa 180,24,180
L_ v6,28,s65,0
Mvaa 188,20,144
S_ s65,s59,7176192
Mvaa 188,24,184
S_ m0,s52,17408
Ta 224,v1,28680
Ta 226,v1,29192
Mvaa 196,20,148
U_ v248,s48,v248
U_ v249,s48,v249
Mvaa 196,24,188
L_ v6,28,s65,0
Mvaa 204,20,152
S_ s65,s59,7274496
U_ v250,s48,v250
U_ v251,s48,v251
Mvaa 204,24,192
S_ m0,s52,18432
U_ v252,s48,v252
U_ v253,s48,v253
Mvaa 212,20,156
U_ v254,s48,v254
U_ v255,s48,v255
Mvaa 212,24,196
C_ 255,254,255
C_ 254,252,253
Mvaa 208,32,196
s_nop 0
Ta 20,v1,24576
Ta 22,v1,25088
Mvaa 208,28,156
L_ v6,28,s65,0
Mvaa 200,32,192
S_ s65,s59,7372800
C_ 253,250,251
C_ 252,248,249
Mvaa 200,28,152
S_ m0,s52,19456
Ta 24,v1,28672
Ta 26,v1,29184
Mvaa 192,32,188
X_ a216,v252
X_ a217,v253
Mvaa 192,28,148
L_ v6,28,s65,0
Mvaa 184,32,184
X_ a218,v254
X_ a219,v255
Mvaa 184,28,144
U_ v240,s48,v240
U_ v241,s48,v241
Mvaa 176,32,180
U_ v242,s48,v242
U_ v243,s48,v243
Mvaa 176,28,140
W_ 4
U_ v244,s48,v244
U_ v245,s48,v245
Mvaa 140,36,60
s_nop 0
Ta 28,%0,24584
Ta 30,%0,25096
Mvaa 140,224,100
U_ v246,s48,v246
U_ v247,s48,v247
Mvaa 148,36,64
C_ 251,246,247
C_ 250,244,245
Mvaa 148,224,104
s_nop 0
Ta 32,%0,28680
Ta 34,%0,29192
Mvaa 156,36,68
C_ 249,242,243
C_ 248,240,241
Mvaa 156,224,108
X_ a212,v248
X_ a213,v249
Mvaa 164,36,72
X_ a214,v250
X_ a215,v251
Mvaa 164,224,112
U_ v232,s48,v232
U_ v233,s48,v233
Mvaa 172,36,76
U_ v234,s48,v234
U_ v235,s48,v235
Mvaa 172,224,116
W_ 4
U_ v236,s48,v236
U_ v237,s48,v237
Mvaa 168,24,116
s_nop 0
Ta 36,%0,24576
Ta 38,%0,25088
Mvaa 168,20,76
U_ v238,s48,v238
U_ v239,s48,v239
Mvaa 160,24,112
C_ 247,238,239
C_ 246,236,237
Mvaa 160,20,72
s_nop 0
Ta 224,%0,28672
Ta 226,%0,29184
Mvaa 152,24,108
C_ 245,234,235
C_ 244,232,233
Mvaa 152,20,68
X_ a208,v244
X_ a209,v245
Mvaa 144,24,104
X_ a210,v246
X_ a211,v247
Mvaa 144,20,64
U_ v224,s48,v224
U_ v225,s48,v225
Mvaa 136,24,100
s_waitcnt vmcnt(13) lgkmcnt(0)
s_barrier
U_ v226,s48,v226
Mvaa 136,20,60
S_ s65,s59,-768
U_ v227,s48,v227
Mvaa 100,28,60
S_ m0,s52,24320
Ta 20,v1,8
Ta 22,v1,520
Mvaa 100,32,100
L_ v6,28,s65,256
Mvaa 108,28,64
S_ s65,s59,97536
U_ v228,s48,v228
U_ v229,s48,v229
Mvaa 108,32,104
S_ m0,s52,25344
Ta 24,v1,4104
Ta 26,v1,4616
Mvaa 116,28,68
U_ v230,s48,v230
U_ v231,s48,v231
Mvaa 116,32,108
L_ v6,28,s65,256
Mvaa 124,28,72
S_ s65,s59,195840
C_ 243,230,231
C_ 242,228,229
Mvaa 124,32,112
S_ m0,s52,26368
C_ 241,226,227
C_ 240,224,225
Mvaa 132,28,76
X_ a204,v240
X_ a205,v241
Mvaa 132,32,116
X_ a206,v242
X_ a207,v243
Mvaa 128,224,116
s_nop 0
Ta 28,v1,0
Ta 30,v1,512
Mvaa 128,36,76
L_ v6,28,s65,256
Mvaa 120,224,112
S_ s65,s59,294144
U_ v216,s48,v216
U_ v217,s48,v217
Mvaa 120,36,72
S_ m0,s52,27392
Ta 32,v1,4096
Ta 34,v1,4608
Mvaa 112,224,108
.Lpad_q2_0:
s_cbranch_execz .Lpad_q2_4
U_ v218,s48,v218
U_ v219,s48,v219
Mvaa 112,36,68
L_ v6,28,s65,256
Mvaa 104,224,104
S_ s65,s60,128
U_ v220,s48,v220
U_ v221,s48,v221
Mvaa 104,36,64
S_ m0,s50,20480
U_ v222,s48,v222
U_ v223,s48,v223
Mvaa 96,224,100
C_ 239,222,223
C_ 238,220,221
Mvaa 96,36,60
W_ 4
C_ 237,218,219
C_ 236,216,217
Mvaa 140,20,140
s_nop 0
Ta 36,%0,8
Ta 38,%0,520
Mvaa 140,24,180
L_ v4,24,s65,0
Mvaa 148,20,144
S_ s65,s60,10368
X_ a200,v236
X_ a201,v237
Mvaa 148,24,184
S_ m0,s50,21504
Ta 224,%0,4104
Ta 226,%0,4616
Mvaa 156,20,148
X_ a202,v238
X_ a203,v239
Mvaa 156,24,188
L_ v5,24,s65,0
Mvaa 164,20,152
S_ s65,s60,20608
U_ v208,s48,v208
U_ v209,s48,v209
Mvaa 164,24,192
S_ m0,s50,22528
U_ v210,s48,v210
U_ v211,s48,v211
Mvaa 172,20,156
U_ v212,s48,v212
U_ v213,s48,v213
Mvaa 172,24,196
W_ 4
U_ v214,s48,v214
U_ v215,s48,v215
Mvaa 168,32,196
s_nop 0
Ta 20,%0,0
Ta 22,%0,512
Mvaa 168,28,156
L_ v4,24,s65,0
Mvaa 160,32,192
S_ s65,s60,30848
C_ 235,214,215
C_ 234,212,213
Mvaa 160,28,152
S_ m0,s50,23552
Ta 24,%0,4096
Ta 26,%0,4608
Mvaa 152,32,188
C_ 233,210,211
C_ 232,208,209
Mvaa 152,28,148
L_ v5,24,s65,0
Mvaa 144,32,184
S_ s65,s60,41088
X_ a176,v232
X_ a177,v233
Mvaa 144,28,144
S_ m0,s50,24576
X_ a178,v234
X_ a179,v235
Mvaa 136,32,180
s_waitcnt vmcnt(12) lgkmcnt(0)
s_barrier
U_ v200,s48,v200
Mvaa 136,28,140
L_ v4,24,s65,0
Mvaa 100,36,140
S_ s65,s59,785664
Ta 28,v1,8200
Ta 30,v1,8712
Mvaa 100,224,180
S_ m0,s52,0
U_ v201,s48,v201
Mvaa 108,36,144
U_ v202,s48,v202
U_ v203,s48,v203
Mvaa 108,224,184
s_nop 0
Ta 32,v1,12296
Ta 34,v1,12808
Mvaa 116,36,148
L_ v6,28,s65,0
Mvaa 116,224,188
S_ s65,s59,883968
U_ v204,s48,v204
U_ v205,s48,v205
Mvaa 124,36,152
S_ m0,s52,1024
U_ v206,s48,v206
U_ v207,s48,v207
Mvaa 124,224,192
C_ 231,206,207
C_ 230,204,205
Mvaa 132,36,156
L_ v6,28,s65,0
Mvaa 132,224,196
C_ 229,202,203
S_ s65,s59,982272
Mvaa 128,24,196
S_ m0,s52,2048
Ta 36,v1,8192
Ta 38,v1,8704
Mvaa 128,20,156
C_ 228,200,201
Mvaa 120,24,192
L_ v6,28,s65,0
Mvaa 120,20,152
S_ s65,s59,1080576
Ta 224,v1,12288
Ta 226,v1,12800
Mvaa 112,24,188
S_ m0,s52,3072
X_ a172,v228
X_ a173,v229
Mvaa 112,20,148
X_ a174,v230
X_ a175,v231
Mvaa 104,24,184
L_ v6,28,s65,0
Mvaa 104,20,144
U_ v192,s48,v192
U_ v193,s48,v193
Mvaa 96,24,180
U_ v194,s48,v194
U_ v195,s48,v195
Mvaa 96,20,140
W_ 4
U_ v196,s48,v196
U_ v197,s48,v197
Mvaa 60,28,60
s_nop 0
Ta 20,%0,8200
Ta 22,%0,8712
Mvaa 60,32,100
U_ v198,s48,v198
U_ v199,s48,v199
Mvaa 68,28,64
C_ 227,198,199
C_ 226,196,197
Mvaa 68,32,104
s_nop 0
Ta 24,%0,12296
Ta 26,%0,12808
Mvaa 76,28,68
C_ 225,194,195
C_ 224,192,193
Mvaa 76,32,108
X_ a168,v224
X_ a169,v225
Mvaa 84,28,72
X_ a170,v226
X_ a171,v227
Mvaa 84,32,112
U_ v184,s48,v184
U_ v185,s48,v185
Mvaa 92,28,76
U_ v186,s48,v186
U_ v187,s48,v187
Mvaa 92,32,116
W_ 4
U_ v188,s48,v188
U_ v189,s48,v189
Mvaa 88,224,116
s_nop 0
Ta 28,%0,8192
Ta 30,%0,8704
Mvaa 88,36,76
U_ v190,s48,v190
U_ v191,s48,v191
Mvaa 80,224,112
C_ 223,190,191
C_ 222,188,189
Mvaa 80,36,72
s_nop 0
Ta 32,%0,12288
Ta 34,%0,12800
Mvaa 72,224,108
C_ 221,186,187
C_ 220,184,185
Mvaa 72,36,68
X_ a164,v220
X_ a165,v221
Mvaa 64,224,104
X_ a166,v222
X_ a167,v223
Mvaa 64,36,64
U_ v176,s48,v176
U_ v177,s48,v177
Mvaa 56,224,100
s_waitcnt vmcnt(13) lgkmcnt(0)
s_barrier
U_ v178,s48,v178
Mvaa 56,36,60
S_ s65,s59,-768
U_ v179,s48,v179
Mvaa 20,20,60
S_ m0,s52,8192
Ta 36,v1,16392
Ta 38,v1,16904
Mvaa 20,24,100
L_ v6,28,s65,0
Mvaa 28,20,64
S_ s65,s59,97536
U_ v180,s48,v180
U_ v181,s48,v181
Mvaa 28,24,104
S_ m0,s52,9216
Ta 224,v1,20488
Ta 226,v1,21000
Mvaa 36,20,68
U_ v182,s48,v182
U_ v183,s48,v183
Mvaa 36,24,108
L_ v6,28,s65,0
Mvaa 44,20,72
S_ s65,s59,195840
C_ 219,182,183
C_ 218,180,181
Mvaa 44,24,112
S_ m0,s52,10240
C_ 217,178,179
C_ 216,176,177
Mvaa 52,20,76
X_ a160,v216
X_ a161,v217
Mvaa 52,24,116
X_ a162,v218
X_ a163,v219
Mvaa 48,32,116
s_nop 0
Ta 20,v1,16384
Ta 22,v1,16896
Mvaa 48,28,76
L_ v6,28,s65,0
Mvaa 40,32,112
S_ s65,s59,294144
U_ v168,s48,v168
U_ v169,s48,v169
Mvaa 40,28,72
S_ m0,s52,11264
Ta 24,v1,20480
Ta 26,v1,20992
Mvaa 32,32,108
U_ v170,s48,v170
U_ v171,s48,v171
Mvaa 32,28,68
L_ v6,28,s65,0
Mvaa 24,32,104
U_ v172,s48,v172
U_ v173,s48,v173
Mvaa 24,28,64
U_ v174,s48,v174
U_ v175,s48,v175
Mvaa 16,32,100
C_ 215,174,175
C_ 214,172,173
Mvaa 16,28,60
W_ 4
C_ 213,170,171
C_ 212,168,169
Mvaa 60,36,140
s_nop 0
Ta 28,%0,16392
Ta 30,%0,16904
Mvaa 60,224,180
X_ a136,v212
X_ a137,v213
Mvaa 68,36,144
X_ a138,v214
X_ a139,v215
Mvaa 68,224,184
s_nop 0
Ta 32,%0,20488
Ta 34,%0,21000
Mvaa 76,36,148
U_ v160,s48,v160
U_ v161,s48,v161
Mvaa 76,224,188
U_ v162,s48,v162
U_ v163,s48,v163
Mvaa 84,36,152
U_ v164,s48,v164
U_ v165,s48,v165
Mvaa 84,224,192
U_ v166,s48,v166
U_ v167,s48,v167
Mvaa 92,36,156
C_ 211,166,167
C_ 210,164,165
Mvaa 92,224,196
W_ 4
C_ 209,162,163
C_ 208,160,161
Mvaa 88,24,196
s_nop 0
Ta 36,%0,16384
Ta 38,%0,16896
Mvaa 88,20,156
X_ a132,v208
X_ a133,v209
Mvaa 80,24,192
X_ a134,v210
X_ a135,v211
Mvaa 80,20,152
s_nop 0
Ta 224,%0,20480
Ta 226,%0,20992
Mvaa 72,24,188
U_ v152,s48,v152
U_ v153,s48,v153
Mvaa 72,20,148
U_ v154,s48,v154
U_ v155,s48,v155
Mvaa 64,24,184
U_ v156,s48,v156
U_ v157,s48,v157
Mvaa 64,20,144
U_ v158,s48,v158
U_ v159,s48,v159
Mvaa 56,24,180
s_waitcnt vmcnt(63) lgkmcnt(0)
s_barrier
C_ 207,158,159
Mvaa 56,20,140
S_ s65,s59,-768
C_ 206,156,157
Mvaa 20,28,140
S_ m0,s52,16256
C_ 205,154,155
C_ 204,152,153
Mvaa 20,32,180
L_ v6,28,s65,128
Mvaa 28,28,144
S_ s65,s59,97536
X_ a128,v204
X_ a129,v205
Mvaa 28,32,184
S_ m0,s52,17280
X_ a130,v206
X_ a131,v207
Mvaa 36,28,148
U_ v144,s48,v144
U_ v145,s48,v145
Mvaa 36,32,188
L_ v6,28,s65,128
Mvaa 44,28,152
S_ s65,s59,195840
U_ v146,s48,v146
U_ v147,s48,v147
Mvaa 44,32,192
S_ m0,s52,18304
U_ v148,s48,v148
U_ v149,s48,v149
Mvaa 52,28,156
U_ v150,s48,v150
U_ v151,s48,v151
Mvaa 52,32,196
C_ 203,150,151
C_ 202,148,149
Mvaa 48,224,196
L_ v6,28,s65,128
Mvaa 48,36,156
S_ s65,s59,294144
C_ 201,146,147
C_ 200,144,145
Mvaa 40,224,192
S_ m0,s52,19328
X_ a124,v200
X_ a125,v201
Mvaa 40,36,152
X_ a126,v202
X_ a127,v203
Mvaa 32,224,188
L_ v6,28,s65,128
Mvaa 32,36,148
U_ v136,s48,v136
U_ v137,s48,v137
Mvaa 24,224,184
U_ v138,s48,v138
U_ v139,s48,v139
Mvaa 24,36,144
U_ v140,s48,v140
U_ v141,s48,v141
Mvaa 16,224,180
U_ v142,s48,v142
U_ v143,s48,v143
Mvaa 16,36,140
C_ 199,142,143
C_ 198,140,141
C_ 197,138,139
C_ 196,136,137
X_ a120,v196
X_ a121,v197
X_ a122,v198
X_ a123,v199
U_ v128,s48,v128
U_ v129,s48,v129
U_ v130,s48,v130
U_ v131,s48,v131
U_ v132,s48,v132
U_ v133,s48,v133
U_ v134,s48,v134
U_ v135,s48,v135
C_ 195,134,135
C_ 194,132,133
C_ 193,130,131
C_ 192,128,129
X_ a96,v192
X_ a97,v193
X_ a98,v194
X_ a99,v195
U_ v120,s48,v120
U_ v121,s48,v121
U_ v122,s48,v122
U_ v123,s48,v123
U_ v124,s48,v124
U_ v125,s48,v125
U_ v126,s48,v126
U_ v127,s48,v127
C_ 191,126,127
C_ 190,124,125
C_ 189,122,123
C_ 188,120,121
X_ a92,v188
X_ a93,v189
X_ a94,v190
X_ a95,v191
U_ v112,s48,v112
U_ v113,s48,v113
U_ v114,s48,v114
U_ v115,s48,v115
U_ v116,s48,v116
U_ v117,s48,v117
U_ v118,s48,v118
U_ v119,s48,v119
C_ 187,118,119
C_ 186,116,117
C_ 185,114,115
C_ 184,112,113
X_ a88,v184
X_ a89,v185
X_ a90,v186
X_ a91,v187
U_ v104,s48,v104
U_ v105,s48,v105
U_ v106,s48,v106
U_ v107,s48,v107
U_ v108,s48,v108
U_ v109,s48,v109
U_ v110,s48,v110
U_ v111,s48,v111
C_ 183,110,111
C_ 182,108,109
C_ 181,106,107
C_ 180,104,105
X_ a84,v180
X_ a85,v181
X_ a86,v182
X_ a87,v183
U_ v96,s48,v96
U_ v97,s48,v97
U_ v98,s48,v98
U_ v99,s48,v99
U_ v100,s48,v100
U_ v101,s48,v101
U_ v102,s48,v102
U_ v103,s48,v103
C_ 179,102,103
C_ 178,100,101
C_ 177,98,99
C_ 176,96,97
X_ a80,v176
X_ a81,v177
X_ a82,v178
X_ a83,v179
v_pk_mul_f32 v[88:89], v[88:89], s[48:49]
v_pk_mul_f32 v[90:91], v[90:91], s[48:49]
v_pk_mul_f32 v[92:93], v[92:93], s[48:49]
v_pk_mul_f32 v[94:95], v[94:95], s[48:49]
C_ 175,94,95
C_ 174,92,93
C_ 173,90,91
C_ 172,88,89
X_ a56,v172
X_ a57,v173
X_ a58,v174
X_ a59,v175
v_pk_mul_f32 v[80:81], v[80:81], s[48:49]
v_pk_mul_f32 v[82:83], v[82:83], s[48:49]
v_pk_mul_f32 v[84:85], v[84:85], s[48:49]
v_pk_mul_f32 v[86:87], v[86:87], s[48:49]
C_ 171,86,87
C_ 170,84,85
C_ 169,82,83
C_ 168,80,81
X_ a52,v168
X_ a53,v169
X_ a54,v170
X_ a55,v171
v_pk_mul_f32 v[72:73], v[72:73], s[48:49]
v_pk_mul_f32 v[74:75], v[74:75], s[48:49]
v_pk_mul_f32 v[76:77], v[76:77], s[48:49]
v_pk_mul_f32 v[78:79], v[78:79], s[48:49]
C_ 167,78,79
C_ 166,76,77
C_ 165,74,75
C_ 164,72,73
X_ a48,v164
X_ a49,v165
X_ a50,v166
X_ a51,v167
v_pk_mul_f32 v[64:65], v[64:65], s[48:49]
v_pk_mul_f32 v[66:67], v[66:67], s[48:49]
v_pk_mul_f32 v[68:69], v[68:69], s[48:49]
v_pk_mul_f32 v[70:71], v[70:71], s[48:49]
C_ 163,70,71
C_ 162,68,69
C_ 161,66,67
C_ 160,64,65
X_ a44,v160
X_ a45,v161
X_ a46,v162
X_ a47,v163
v_pk_mul_f32 v[56:57], v[56:57], s[48:49]
v_pk_mul_f32 v[58:59], v[58:59], s[48:49]
v_pk_mul_f32 v[60:61], v[60:61], s[48:49]
v_pk_mul_f32 v[62:63], v[62:63], s[48:49]
C_ 159,62,63
C_ 158,60,61
C_ 157,58,59
C_ 156,56,57
X_ a40,v156
X_ a41,v157
X_ a42,v158
X_ a43,v159
v_pk_mul_f32 v[48:49], v[48:49], s[48:49]
v_pk_mul_f32 v[50:51], v[50:51], s[48:49]
v_pk_mul_f32 v[52:53], v[52:53], s[48:49]
v_pk_mul_f32 v[54:55], v[54:55], s[48:49]
C_ 155,54,55
C_ 154,52,53
C_ 153,50,51
C_ 152,48,49
X_ a16,v152
X_ a17,v153
X_ a18,v154
X_ a19,v155
v_pk_mul_f32 v[40:41], v[40:41], s[48:49]
v_pk_mul_f32 v[42:43], v[42:43], s[48:49]
v_pk_mul_f32 v[44:45], v[44:45], s[48:49]
v_pk_mul_f32 v[46:47], v[46:47], s[48:49]
C_ 151,46,47
C_ 150,44,45
C_ 149,42,43
C_ 148,40,41
X_ a12,v148
X_ a13,v149
X_ a14,v150
X_ a15,v151
s_sub_u32 s59, s59, 768
s_waitcnt vmcnt(0) lgkmcnt(0)
s_barrier
Ta 20,%0,8192
Ta 22,%0,8704
Ta 24,%0,12288
Ta 26,%0,12800
Da 60,v2,0
Da 100,v3,0
Da 64,v2,2048
Da 104,v3,2048
.Lpad_q2_1:
s_cbranch_execz .Lpad_q2_5
Da 68,v2,4096
Da 108,v3,4096
Da 72,v2,6144
Da 112,v3,6144
Da 76,v2,8192
Da 116,v3,8192
Ta 28,%0,8200
Ta 30,%0,8712
Ta 32,%0,12296
Ta 34,%0,12808
v_pk_mul_f32 v[32:33], v[32:33], s[48:49]
v_pk_mul_f32 v[34:35], v[34:35], s[48:49]
v_pk_mul_f32 v[36:37], v[36:37], s[48:49]
v_pk_mul_f32 v[38:39], v[38:39], s[48:49]
C_ 147,38,39
C_ 146,36,37
C_ 145,34,35
C_ 144,32,33
X_ a8,v144
X_ a9,v145
X_ a10,v146
X_ a11,v147
v_pk_mul_f32 v[24:25], v[24:25], s[48:49]
v_pk_mul_f32 v[26:27], v[26:27], s[48:49]
v_pk_mul_f32 v[28:29], v[28:29], s[48:49]
v_pk_mul_f32 v[30:31], v[30:31], s[48:49]
C_ 143,30,31
C_ 142,28,29
C_ 141,26,27
C_ 140,24,25
X_ a4,v140
X_ a5,v141
X_ a6,v142
X_ a7,v143
v_pk_mul_f32 v[16:17], v[16:17], s[48:49]
v_pk_mul_f32 v[18:19], v[18:19], s[48:49]
v_pk_mul_f32 v[20:21], v[20:21], s[48:49]
v_pk_mul_f32 v[22:23], v[22:23], s[48:49]
C_ 139,22,23
C_ 138,20,21
C_ 137,18,19
C_ 136,16,17
X_ a0,v136
X_ a1,v137
X_ a2,v138
X_ a3,v139
s_sub_u32 s90, s90, s88
s_cmp_gt_u32 s90, 4000
s_cselect_b32 s11, 1, 0
s_sub_u32 s12, s68, s98
S_ s12,s12,1252
s_mov_b32 s13, 109
s_nop 1
.p2align 3
.Lq:
W_ 13
Zvaa 16,20,60
s_nop 0
Ta 36,v1,8192
Ta 38,v1,8704
W_ 14
Mvaa 16,24,100
R_ v12,a248
W_ 13
Zvaa 24,20,64
v_lshrrev_b32 v12, 4, v12
W_ 12
Mvaa 24,24,104
s_nop 0
Ta 224,v1,12288
Ta 226,v1,12800
W_ 13
Zvaa 32,20,68
v_min_u32 v10, 11, v12
W_ 12
Mvaa 32,24,108
s_mul_i32 s56, s4, 12
W_ 11
Zvaa 40,20,72
v_add_u32 v10, s56, v10
W_ 10
Mvaa 40,24,112
v_mul_u32_u24 v11, 43691, v10
W_ 9
Zvaa 48,20,76
v_lshrrev_b32 v11, 18, v11
W_ 8
Mvaa 48,24,116
W_ 4
v_lshlrev_b32 v10, 7, v10
Zvaa 52,32,116
s_nop 0
Ta 20,v1,8200
Ta 22,v1,8712
Mvaa 52,28,76
v_mul_u32_u24 v11, 11520, v11
Zvaa 44,32,112
v_add_u32 v10, v10, v11
Mvaa 44,28,72
s_nop 0
Ta 24,v1,12296
Ta 26,v1,12808
Zvaa 36,32,108
v_min_u32 v12, 9, v12
Mvaa 36,28,68
s_and_b32 s56, s6, 3
Zvaa 28,32,104
s_lshl_b32 s56, s56, 2
Mvaa 28,28,64
S_ s56,s56,s4
Zvaa 20,32,100
s_waitcnt vmcnt(18) lgkmcnt(0)
s_barrier
s_mul_i32 s56, s56, 10
Mvaa 20,28,60
S_ s65,s59,786432
v_add_u32 v12, s56, v12
Zvaa 56,36,60
S_ m0,s52,8064
Ta 28,%0,16384
Ta 30,%0,16896
Mvaa 56,224,100
L_ v6,28,s65,128
Zvaa 64,36,64
S_ s65,s59,884736
v_mul_u32_u24 v12, 1280, v12
Mvaa 64,224,104
S_ m0,s52,9088
Ta 32,%0,20480
Ta 34,%0,20992
Zvaa 72,36,68
s_lshr_b32 s56, s7, 5
Mvaa 72,224,108
L_ v6,28,s65,128
Zvaa 80,36,72
S_ s65,s59,983040
s_mul_i32 s56, s56, 98304
Mvaa 80,224,112
S_ m0,s52,10112
s_and_b32 s55, s59, 4095
Zvaa 88,36,76
S_ s56,s56,s55
Mvaa 88,224,116
s_mul_i32 s55, s77, 384
Zvaa 92,24,116
s_sub_u32 s56, s56, s55
Ta 36,%0,16392
Ta 38,%0,16904
Mvaa 92,20,76
L_ v6,28,s65,128
Zvaa 84,24,112
S_ s65,s59,1081344
s_mov_b32 s63, 0
Mvaa 84,20,72
S_ m0,s52,11136
Ta 224,%0,20488
Ta 226,%0,21000
Zvaa 76,24,108
s_bitcmp1_b32 1, s58
s_cselect_b32 s63, 0, 4026531840
Mvaa 76,20,68
L_ v6,28,s65,128
Zvaa 68,24,104
S_ s65,s60,256
S_ s56,s56,s63
Mvaa 68,20,64
S_ m0,s50,0
S_ s55,s56,3145728
Zvaa 60,24,100
Mvaa 60,20,60
W_ 4
Zvaa 96,28,60
s_nop 0
Ta 20,v1,16384
Ta 22,v1,16896
Mvaa 96,32,100
L_ v4,24,s65,0
Zvaa 104,28,64
S_ s65,s60,10496
Mvaa 104,32,104
S_ m0,s50,1024
Ta 24,v1,20480
Ta 26,v1,20992
Zvaa 112,28,68
Mvaa 112,32,108
L_ v5,24,s65,0
Zvaa 120,28,72
S_ s65,s60,20736
Mvaa 120,32,112
S_ m0,s50,2048
Zvaa 128,28,76
Mvaa 128,32,116
W_ 4
Zvaa 132,224,116
s_nop 0
Ta 28,v1,16392
Ta 30,v1,16904
Mvaa 132,36,76
L_ v4,24,s65,0
Zvaa 124,224,112
S_ s65,s60,30976
Mvaa 124,36,72
S_ m0,s50,3072
Ta 32,v1,20488
Ta 34,v1,21000
Zvaa 116,224,108
Mvaa 116,36,68
L_ v5,24,s65,0
Zvaa 108,224,104
S_ s65,s60,41216
Mvaa 108,36,64
S_ m0,s50,4096
Zvaa 100,224,100
s_cmp_eq_u32 s58, 0
s_cbranch_scc0 .Lq_early
s_mov_b64 exec, 0
s_bitcmp1_b32 s4, 1
s_cbranch_scc1 .Lhop_qx_b
s_bitcmp1_b32 s4, 0
s_cbranch_scc1 .Lpad_qx_1
s_branch .Lpad_qx_0
.Lhop_qx_b:
s_bitcmp1_b32 s4, 0
s_cbranch_scc1 .Lpad_qx_3
s_branch .Lpad_qx_2
.Lhop_qx_r:
s_mov_b64 exec, -1
s_nop 1
.Lq_early:
s_waitcnt vmcnt(17) lgkmcnt(0)
s_barrier
s_cmp_eq_u32 s11, 1
s_cbranch_scc1 .Lpace1
.Lpace1_back:
Mvaa 100,36,60
Da 140,v2,20480
Zvaa 136,20,60
s_nop 0
Ta 36,%0,24576
Ta 38,%0,25088
Mvaa 136,24,100
L_ v4,24,s65,0
Zvaa 144,20,64
S_ s65,s59,786432
Da 180,v3,20480
Mvaa 144,24,104
S_ m0,s52,16128
Ta 224,%0,28672
Ta 226,%0,29184
Zvaa 152,20,68
Mvaa 152,24,108
Da 144,v2,22528
Zvaa 160,20,72
L_ v6,28,s65,256
Mvaa 160,24,112
S_ s65,s59,884736
Zvaa 168,20,76
S_ m0,s52,17152
Da 184,v3,22528
Mvaa 168,24,116
Zvaa 172,32,116
s_nop 0
Ta 20,%0,24584
Ta 22,%0,25096
Mvaa 172,28,76
Da 148,v2,24576
Zvaa 164,32,112
L_ v6,28,s65,256
Mvaa 164,28,72
S_ s65,s59,983040
Ta 24,%0,28680
Ta 26,%0,29192
Zvaa 156,32,108
S_ m0,s52,18176
Da 188,v3,24576
Mvaa 156,28,68
Zvaa 148,32,104
L_ v6,28,s65,256
Mvaa 148,28,64
S_ s65,s59,1081344
Da 152,v2,26624
Zvaa 140,32,100
S_ m0,s52,19200
Mvaa 140,28,60
W_ 9
Zvaa 176,36,60
s_nop 0
Ta 28,v1,24576
Ta 30,v1,25088
Mvaa 176,224,100
Da 192,v3,26624
Zvaa 184,36,64
L_ v6,28,s65,256
Mvaa 184,224,104
s_nop 0
Ta 32,v1,28672
Ta 34,v1,29184
Zvaa 192,36,68
Da 156,v2,28672
Mvaa 192,224,108
Zvaa 200,36,72
Da 196,v3,28672
Mvaa 200,224,112
buffer_load_dword a254, v10, s[28:31], s55 offen
Zvaa 208,36,76
Mvaa 208,224,116
W_ 9
Zvaa 212,24,116
s_nop 0
Ta 36,v1,24584
Ta 38,v1,25096
Mvaa 212,20,76
buffer_load_dword a254, v12, s[24:27], s63 offen offset:512
Zvaa 204,24,112
Mvaa 204,20,72
s_nop 0
Ta 224,v1,28680
Ta 226,v1,29192
Zvaa 196,24,108
Mvaa 196,20,68
Zvaa 188,24,104
Mvaa 188,20,64
Zvaa 180,24,100
s_waitcnt vmcnt(20) lgkmcnt(0)
s_barrier
Mvaa 180,20,60
S_ s65,s59,1572864
Zvaa 216,28,60
S_ m0,s52,24576
Ta 20,%0,0
Ta 22,%0,512
Mvaa 216,32,100
L_ v6,28,s65,0
Zvaa 224,28,64
S_ s65,s59,1671168
Mvaa 224,32,104
S_ m0,s52,25600
Ta 24,%0,4096
Ta 26,%0,4608
Zvaa 232,28,68
Mvaa 232,32,108
L_ v6,28,s65,0
Zvaa 240,28,72
S_ s65,s59,1769472
Mvaa 240,32,112
S_ m0,s52,26624
Zvaa 248,28,76
Mvaa 248,32,116
Zvaa 252,224,116
s_nop 0
Ta 28,%0,8
Ta 30,%0,520
Mvaa 252,36,76
L_ v6,28,s65,0
Zvaa 244,224,112
S_ s65,s59,1867776
Mvaa 244,36,72
S_ m0,s52,27648
Ta 32,%0,4104
Ta 34,%0,4616
Zvaa 236,224,108
Mvaa 236,36,68
L_ v6,28,s65,0
Zvaa 228,224,104
S_ s65,s60,384
Mvaa 228,36,64
S_ m0,s50,20480
Zvaa 220,224,100
Mvaa 220,36,60
W_ 4
Mvaa 16,20,140
s_nop 0
Ta 36,v1,0
Ta 38,v1,512
Mvaa 16,24,180
L_ v4,24,s65,0
Mvaa 24,20,144
S_ s65,s60,10624
Mvaa 24,24,184
S_ m0,s50,21504
Ta 224,v1,4096
Ta 226,v1,4608
Mvaa 32,20,148
Mvaa 32,24,188
L_ v5,24,s65,0
Mvaa 40,20,152
S_ s65,s60,20864
Mvaa 40,24,192
S_ m0,s50,22528
Mvaa 48,20,156
Mvaa 48,24,196
W_ 4
Mvaa 52,32,196
s_nop 0
Ta 20,v1,8
Ta 22,v1,520
Mvaa 52,28,156
L_ v4,24,s65,0
Mvaa 44,32,192
S_ s65,s60,31104
Mvaa 44,28,152
S_ m0,s50,23552
Ta 24,v1,4104
Ta 26,v1,4616
Mvaa 36,32,188
Mvaa 36,28,148
L_ v5,24,s65,0
Mvaa 28,32,184
S_ s65,s60,41344
Mvaa 28,28,144
S_ m0,s50,24576
Mvaa 20,32,180
s_waitcnt vmcnt(19) lgkmcnt(0)
s_barrier
s_cmp_eq_u32 s11, 1
s_cbranch_scc1 .Lpace3
.Lpace3_back:
Mvaa 20,28,140
L_ v4,24,s65,0
Mvaa 56,36,140
S_ s65,s59,1572864
Ta 28,%0,8192
Ta 30,%0,8704
Mvaa 56,224,180
S_ m0,s52,-128
Mvaa 64,36,144
Mvaa 64,224,184
s_nop 0
Ta 32,%0,12288
Ta 34,%0,12800
Mvaa 72,36,148
L_ v6,28,s65,128
Mvaa 72,224,188
S_ s65,s59,1671168
Mvaa 80,36,152
S_ m0,s52,896
Mvaa 80,224,192
Mvaa 88,36,156
L_ v6,28,s65,128
Mvaa 88,224,196
S_ s65,s59,1769472
Mvaa 92,24,196
S_ m0,s52,1920
Ta 36,%0,8200
Ta 38,%0,8712
Mvaa 92,20,156
S_ s55,s56,3932160
Mvaa 84,24,192
L_ v6,28,s65,128
Mvaa 84,20,152
S_ s65,s59,1867776
Ta 224,%0,12296
Ta 226,%0,12808
Mvaa 76,24,188
S_ m0,s52,2944
Mvaa 76,20,148
Mvaa 68,24,184
L_ v6,28,s65,128
Mvaa 68,20,144
Mvaa 60,24,180
Mvaa 60,20,140
W_ 4
Mvaa 96,28,140
s_nop 0
Ta 20,v1,8192
Ta 22,v1,8704
Mvaa 96,32,180
buffer_load_dword a254, v10, s[28:31], s55 offen
Mvaa 104,28,144
Mvaa 104,32,184
s_nop 0
Ta 24,v1,12288
Ta 26,v1,12800
Mvaa 112,28,148
Mvaa 112,32,188
buffer_load_dword a254, v12, s[24:27], s63 offen offset:640
Mvaa 120,28,152
Mvaa 120,32,192
Mvaa 128,28,156
Mvaa 128,32,196
W_ 4
)A"
R"A(Mvaa 132,224,196
s_nop 0
Ta 28,v1,8200
Ta 30,v1,8712
Mvaa 132,36,156
Mvaa 124,224,192
Mvaa 124,36,152
s_nop 0
Ta 32,v1,12296
Ta 34,v1,12808
Mvaa 116,224,188
Mvaa 116,36,148
Mvaa 108,224,184
Mvaa 108,36,144
Mvaa 100,224,180
s_waitcnt vmcnt(17) lgkmcnt(0)
s_barrier
Mvaa 100,36,140
S_ s65,s59,1572864
Da 60,v2,0
Mvaa 136,20,140
S_ m0,s52,7936
Ta 36,%0,16384
Ta 38,%0,16896
Mvaa 136,24,180
L_ v6,28,s65,256
Mvaa 144,20,144
S_ s65,s59,1671168
Da 100,v3,0
Mvaa 144,24,184
S_ m0,s52,8960
Ta 224,%0,20480
Ta 226,%0,20992
Mvaa 152,20,148
Mvaa 152,24,188
Da 64,v2,2048
Mvaa 160,20,152
L_ v6,28,s65,256
Mvaa 160,24,192
S_ s65,s59,1769472
Mvaa 168,20,156
S_ m0,s52,9984
Da 104,v3,2048
Mvaa 168,24,196
Mvaa 172,32,196
s_nop 0
Ta 20,%0,16392
Ta 22,%0,16904
Mvaa 172,28,156
Da 68,v2,4096
Mvaa 164,32,192
L_ v6,28,s65,256
Mvaa 164,28,152
S_ s65,s59,1867776
Ta 24,%0,20488
Ta 26,%0,21000
Mvaa 156,32,188
S_ m0,s52,11008
Da 108,v3,4096
Mvaa 156,28,148
Mvaa 148,32,184
L_ v6,28,s65,256
Mvaa 148,28,144
Da 72,v2,6144
Mvaa 140,32,180
Mvaa 140,28,140
W_ 9
Mvaa 176,36,140
s_nop 0
Ta 28,v1,16384
Ta 30,v1,16896
Mvaa 176,224,180
Da 112,v3,6144
Mvaa 184,36,144
Mvaa 184,224,184
s_nop 0
Ta 32,v1,20480
Ta 34,v1,20992
Mvaa 192,36,148
.Lpad_q2_2:
s_cbranch_execz .Lpad_q2_6
Da 76,v2,8192
Mvaa 192,224,188
.Lpad_qx_0:
s_cbranch_execz .Lpad_qx_4
Mvaa 200,36,152
Da 116,v3,8192
Mvaa 200,224,192
Mvaa 208,36,156
Mvaa 208,224,196
W_ 9
Mvaa 212,24,196
s_nop 0
Ta 36,v1,16392
Ta 38,v1,16904
Mvaa 212,20,156
Mvaa 204,24,192
Mvaa 204,20,152
s_nop 0
Ta 224,v1,20488
Ta 226,v1,21000
Mvaa 196,24,188
Mvaa 196,20,148
Mvaa 188,24,184
Mvaa 188,20,144
Mvaa 180,24,180
s_waitcnt vmcnt(15) lgkmcnt(0)
s_barrier
s_cmp_eq_u32 s11, 1
s_cbranch_scc1 .Lpace5
.Lpace5_back:
Mvaa 180,20,140
S_ s65,s59,2359296
Mvaa 216,28,140
S_ m0,s52,16384
Ta 20,%0,24576
Ta 22,%0,25088
Mvaa 216,32,180
L_ v6,28,s65,0
Mvaa 224,28,144
S_ s65,s59,2457600
Mvaa 224,32,184
S_ m0,s52,17408
Ta 24,%0,28672
Ta 26,%0,29184
Mvaa 232,28,148
Mvaa 232,32,188
L_ v6,28,s65,0
Mvaa 240,28,152
S_ s65,s59,2555904
Mvaa 240,32,192
S_ m0,s52,18432
Mvaa 248,28,156
Mvaa 248,32,196
Mvaa 252,224,196
s_nop 0
Ta 28,%0,24584
Ta 30,%0,25096
Mvaa 252,36,156
L_ v6,28,s65,0
Mvaa 244,224,192
S_ s65,s59,2654208
Mvaa 244,36,152
S_ m0,s52,19456
Ta 32,%0,28680
Ta 34,%0,29192
Mvaa 236,224,188
Mvaa 236,36,148
L_ v6,28,s65,0
Mvaa 228,224,184
S_ s65,s60,512
Mvaa 228,36,144
S_ m0,s50,0
Mvaa 220,224,180
Mvaa 220,36,140
W_ 4
Mvaa 16,20,60
s_nop 0
Ta 36,v1,24576
Ta 38,v1,25088
Mvaa 16,24,100
L_ v4,24,s65,0
Mvaa 24,20,64
S_ s65,s60,10752
Mvaa 24,24,104
S_ m0,s50,1024
Ta 224,v1,28672
Ta 226,v1,29184
Mvaa 32,20,68
Mvaa 32,24,108
L_ v5,24,s65,0
Mvaa 40,20,72
S_ s65,s60,20992
Mvaa 40,24,112
S_ m0,s50,2048
Mvaa 48,20,76
Mvaa 48,24,116
W_ 4
Mvaa 52,32,116
s_nop 0
Ta 20,v1,24584
Ta 22,v1,25096
Mvaa 52,28,76
L_ v4,24,s65,0
Mvaa 44,32,112
S_ s65,s60,31232
Mvaa 44,28,72
S_ m0,s50,3072
Ta 24,v1,28680
Ta 26,v1,29192
Mvaa 36,32,108
Mvaa 36,28,68
L_ v5,24,s65,0
Mvaa 28,32,104
S_ s65,s60,41472
Mvaa 28,28,64
S_ m0,s50,4096
Mvaa 20,32,100
s_waitcnt vmcnt(14) lgkmcnt(0)
s_barrier
Mvaa 20,28,60
L_ v4,24,s65,0
Mvaa 56,36,60
S_ s65,s59,2359296
Ta 28,%0,0
Ta 30,%0,512
Mvaa 56,224,100
S_ m0,s52,24448
Mvaa 64,36,64
Mvaa 64,224,104
s_nop 0
Ta 32,%0,4096
Ta 34,%0,4608
Mvaa 72,36,68
L_ v6,28,s65,128
Mvaa 72,224,108
S_ s65,s59,2457600
Mvaa 80,36,72
S_ m0,s52,25472
Mvaa 80,224,112
Mvaa 88,36,76
L_ v6,28,s65,128
Mvaa 88,224,116
S_ s65,s59,2555904
Mvaa 92,24,116
S_ m0,s52,26496
Ta 36,%0,8
Ta 38,%0,520
Mvaa 92,20,76
S_ s55,s56,4718592
Mvaa 84,24,112
L_ v6,28,s65,128
Mvaa 84,20,72
S_ s65,s59,2654208
Ta 224,%0,4104
Ta 226,%0,4616
Mvaa 76,24,108
S_ m0,s52,27520
Mvaa 76,20,68
Mvaa 68,24,104
L_ v6,28,s65,128
Mvaa 68,20,64
Mvaa 60,24,100
Mvaa 60,20,60
W_ 4
Mvaa 96,28,60
s_nop 0
Ta 20,v1,0
Ta 22,v1,512
Mvaa 96,32,100
buffer_load_dword a254, v10, s[28:31], s55 offen
Mvaa 104,28,64
Mvaa 104,32,104
s_nop 0
Ta 24,v1,4096
Ta 26,v1,4608
Mvaa 112,28,68
Mvaa 112,32,108
buffer_load_dword a254, v12, s[24:27], s63 offen offset:768
Mvaa 120,28,72
Mvaa 120,32,112
Mvaa 128,28,76
Mvaa 128,32,116
W_ 4
Mvaa 132,224,116
s_nop 0
Ta 28,v1,8
Ta 30,v1,520
Mvaa 132,36,76
Mvaa 124,224,112
Mvaa 124,36,72
s_nop 0
Ta 32,v1,4104
Ta 34,v1,4616
Mvaa 116,224,108
Mvaa 116,36,68
Mvaa 108,224,104
Mvaa 108,36,64
Mvaa 100,224,100
s_waitcnt vmcnt(15) lgkmcnt(0)
s_barrier
s_cmp_eq_u32 s11, 1
s_cbranch_scc1 .Lpace7
.Lpace7_back:
Mvaa 100,36,60
S_ s65,s59,2359296
Da 140,v2,20480
Mvaa 136,20,60
S_ m0,s52,-256
Ta 36,%0,8192
Ta 38,%0,8704
Mvaa 136,24,100
L_ v6,28,s65,256
Mvaa 144,20,64
S_ s65,s59,2457600
Da 180,v3,20480
Mvaa 144,24,104
S_ m0,s52,768
Ta 224,%0,12288
Ta 226,%0,12800
Mvaa 152,20,68
Mvaa 152,24,108
Da 144,v2,22528
Mvaa 160,20,72
L_ v6,28,s65,256
Mvaa 160,24,112
S_ s65,s59,2555904
Mvaa 168,20,76
S_ m0,s52,1792
Da 184,v3,22528
Mvaa 168,24,116
Mvaa 172,32,116
s_nop 0
Ta 20,%0,8200
Ta 22,%0,8712
Mvaa 172,28,76
Da 148,v2,24576
Mvaa 164,32,112
L_ v6,28,s65,256
Mvaa 164,28,72
S_ s65,s59,2654208
Ta 24,%0,12296
Ta 26,%0,12808
Mvaa 156,32,108
S_ m0,s52,2816
Da 188,v3,24576
Mvaa 156,28,68
Mvaa 148,32,104
L_ v6,28,s65,256
Mvaa 148,28,64
Da 152,v2,26624
Mvaa 140,32,100
Mvaa 140,28,60
W_ 9
Mvaa 176,36,60
s_nop 0
Ta 28,v1,8192
Ta 30,v1,8704
Mvaa 176,224,100
Da 192,v3,26624
Mvaa 184,36,64
Mvaa 184,224,104
s_nop 0
Ta 32,v1,12288
Ta 34,v1,12800
Mvaa 192,36,68
Da 156,v2,28672
Mvaa 192,224,108
Mvaa 200,36,72
Da 196,v3,28672
Mvaa 200,224,112
Mvaa 208,36,76
Mvaa 208,224,116
W_ 9
Mvaa 212,24,116
s_nop 0
Ta 36,v1,8200
Ta 38,v1,8712
Mvaa 212,20,76
Mvaa 204,24,112
Mvaa 204,20,72
s_nop 0
Ta 224,v1,12296
Ta 226,v1,12808
Mvaa 196,24,108
Mvaa 196,20,68
Mvaa 188,24,104
Mvaa 188,20,64
Mvaa 180,24,100
s_waitcnt vmcnt(15) lgkmcnt(0)
s_barrier
Mvaa 180,20,60
S_ s65,s59,3145728
Mvaa 216,28,60
S_ m0,s52,8192
Ta 20,%0,16384
Ta 22,%0,16896
Mvaa 216,32,100
L_ v6,28,s65,0
Mvaa 224,28,64
S_ s65,s59,3244032
Mvaa 224,32,104
S_ m0,s52,9216
Ta 24,%0,20480
Ta 26,%0,20992
Mvaa 232,28,68
Mvaa 232,32,108
L_ v6,28,s65,0
Mvaa 240,28,72
S_ s65,s59,3342336
Mvaa 240,32,112
S_ m0,s52,10240
Mvaa 248,28,76
Mvaa 248,32,116
Mvaa 252,224,116
s_nop 0
Ta 28,%0,16392
Ta 30,%0,16904
Mvaa 252,36,76
L_ v6,28,s65,0
Mvaa 244,224,112
S_ s65,s59,3440640
Mvaa 244,36,72
S_ m0,s52,11264
Ta 32,%0,20488
Ta 34,%0,21000
Mvaa 236,224,108
Mvaa 236,36,68
L_ v6,28,s65,0
Mvaa 228,224,104
S_ s65,s60,640
Mvaa 228,36,64
S_ m0,s50,20480
Mvaa 220,224,100
Mvaa 220,36,60
W_ 4
Mvaa 16,20,140
s_nop 0
Ta 36,v1,16384
Ta 38,v1,16896
Mvaa 16,24,180
L_ v4,24,s65,0
Mvaa 24,20,144
S_ s65,s60,10880
Mvaa 24,24,184
S_ m0,s50,21504
Ta 224,v1,20480
Ta 226,v1,20992
Mvaa 32,20,148
Mvaa 32,24,188
L_ v5,24,s65,0
Mvaa 40,20,152
S_ s65,s60,21120
Mvaa 40,24,192
S_ m0,s50,22528
Mvaa 48,20,156
Mvaa 48,24,196
W_ 4
Mvaa 52,32,196
s_nop 0
Ta 20,v1,16392
Ta 22,v1,16904
Mvaa 52,28,156
L_ v4,24,s65,0
Mvaa 44,32,192
S_ s65,s60,31360
Mvaa 44,28,152
S_ m0,s50,23552
Ta 24,v1,20488
Ta 26,v1,21000
Mvaa 36,32,188
Mvaa 36,28,148
L_ v5,24,s65,0
Mvaa 28,32,184
S_ s65,s60,41600
Mvaa 28,28,144
S_ m0,s50,24576
Mvaa 20,32,180
s_waitcnt vmcnt(14) lgkmcnt(0)
s_barrier
s_cmp_eq_u32 s11, 1
s_cbranch_scc1 .Lpace9
.Lpace9_back:
Mvaa 20,28,140
L_ v4,24,s65,0
Mvaa 56,36,140
S_ s65,s59,3145728
Ta 28,%0,24576
Ta 30,%0,25088
Mvaa 56,224,180
S_ m0,s52,16256
Mvaa 64,36,144
Mvaa 64,224,184
s_nop 0
Ta 32,%0,28672
Ta 34,%0,29184
Mvaa 72,36,148
L_ v6,28,s65,128
Mvaa 72,224,188
S_ s65,s59,3244032
Mvaa 80,36,152
S_ m0,s52,17280
Mvaa 80,224,192
Mvaa 88,36,156
L_ v6,28,s65,128
Mvaa 88,224,196
S_ s65,s59,3342336
Mvaa 92,24,196
S_ m0,s52,18304
Ta 36,%0,24584
Ta 38,%0,25096
Mvaa 92,20,156
S_ s55,s56,5505024
Mvaa 84,24,192
L_ v6,28,s65,128
Mvaa 84,20,152
S_ s65,s59,3440640
Ta 224,%0,28680
Ta 226,%0,29192
Mvaa 76,24,188
S_ m0,s52,19328
Mvaa 76,20,148
Mvaa 68,24,184
L_ v6,28,s65,128
Mvaa 68,20,144
Mvaa 60,24,180
Mvaa 60,20,140
W_ 4
Mvaa 96,28,140
s_nop 0
Ta 20,v1,24576
Ta 22,v1,25088
Mvaa 96,32,180
buffer_load_dword a254, v10, s[28:31], s55 offen
Mvaa 104,28,144
Mvaa 104,32,184
s_nop 0
Ta 24,v1,28672
Ta 26,v1,29184
Mvaa 112,28,148
Mvaa 112,32,188
buffer_load_dword a254, v12, s[24:27], s63 offen offset:896
Mvaa 120,28,152
Mvaa 120,32,192
Mvaa 128,28,156
Mvaa 128,32,196
W_ 4
Mvaa 132,224,196
s_nop 0
Ta 28,v1,24584
Ta 30,v1,25096
Mvaa 132,36,156
Mvaa 124,224,192
Mvaa 124,36,152
s_nop 0
Ta 32,v1,28680
Ta 34,v1,29192
Mvaa 116,224,188
Mvaa 116,36,148
Mvaa 108,224,184
Mvaa 108,36,144
Mvaa 100,224,180
s_waitcnt vmcnt(15) lgkmcnt(0)
s_barrier
Mvaa 100,36,140
S_ s65,s59,3145728
Da 60,v2,0
Mvaa 136,20,140
S_ m0,s52,24320
Ta 36,%0,0
Ta 38,%0,512
Mvaa 136,24,180
L_ v6,28,s65,256
Mvaa 144,20,144
S_ s65,s59,3244032
Da 100,v3,0
Mvaa 144,24,184
S_ m0,s52,25344
Ta 224,%0,4096
Ta 226,%0,4608
Mvaa 152,20,148
Mvaa 152,24,188
Da 64,v2,2048
Mvaa 160,20,152
L_ v6,28,s65,256
Mvaa 160,24,192
S_ s65,s59,3342336
Mvaa 168,20,156
S_ m0,s52,26368
Da 104,v3,2048
Mvaa 168,24,196
Mvaa 172,32,196
s_nop 0
Ta 20,%0,8
Ta 22,%0,520
Mvaa 172,28,156
Da 68,v2,4096
Mvaa 164,32,192
L_ v6,28,s65,256
Mvaa 164,28,152
S_ s65,s59,3440640
Ta 24,%0,4104
Ta 26,%0,4616
Mvaa 156,32,188
S_ m0,s52,27392
Da 108,v3,4096
Mvaa 156,28,148
Mvaa 148,32,184
L_ v6,28,s65,256
Mvaa 148,28,144
Da 72,v2,6144
Mvaa 140,32,180
Mvaa 140,28,140
W_ 9
Mvaa 176,36,140
s_nop 0
Ta 28,v1,0
Ta 30,v1,512
Mvaa 176,224,180
Da 112,v3,6144
Mvaa 184,36,144
Mvaa 184,224,184
s_nop 0
Ta 32,v1,4096
Ta 34,v1,4608
Mvaa 192,36,148
Da 76,v2,8192
Mvaa 192,224,188
Mvaa 200,36,152
Da 116,v3,8192
Mvaa 200,224,192
Mvaa 208,36,156
Mvaa 208,224,196
W_ 9
Mvaa 212,24,196
s_nop 0
Ta 36,v1,8
Ta 38,v1,520
Mvaa 212,20,156
Mvaa 204,24,192
Mvaa 204,20,152
s_nop 0
Ta 224,v1,4104
Ta 226,v1,4616
Mvaa 196,24,188
.Lpad_q2_3:
s_cbranch_execz .Lhop_q2_r
Mvaa 196,20,148
.Lpad_qx_1:
s_cbranch_execz .Lhop_qx_r
Mvaa 188,24,184
Mvaa 188,20,144
Mvaa 180,24,180
s_waitcnt vmcnt(15) lgkmcnt(0)
s_barrier
s_cmp_eq_u32 s11, 1
s_cbranch_scc1 .Lpace11
.Lpace11_back:
Mvaa 180,20,140
S_ s65,s59,3932160
Mvaa 216,28,140
S_ m0,s52,0
Ta 20,%0,8192
Ta 22,%0,8704
Mvaa 216,32,180
L_ v6,28,s65,0
Mvaa 224,28,144
S_ s65,s59,4030464
Mvaa 224,32,184
S_ m0,s52,1024
Ta 24,%0,12288
Ta 26,%0,12800
Mvaa 232,28,148
Mvaa 232,32,188
L_ v6,28,s65,0
Mvaa 240,28,152
S_ s65,s59,4128768
Mvaa 240,32,192
S_ m0,s52,2048
Mvaa 248,28,156
Mvaa 248,32,196
Mvaa 252,224,196
s_nop 0
Ta 28,%0,8200
Ta 30,%0,8712
Mvaa 252,36,156
L_ v6,28,s65,0
Mvaa 244,224,192
S_ s65,s59,4227072
Mvaa 244,36,152
S_ m0,s52,3072
Ta 32,%0,12296
Ta 34,%0,12808
Mvaa 236,224,188
Mvaa 236,36,148
L_ v6,28,s65,0
Mvaa 228,224,184
S_ s65,s60,768
Mvaa 228,36,144
S_ m0,s50,0
Mvaa 220,224,180
Mvaa 220,36,140
W_ 4
Mvaa 16,20,60
s_nop 0
Ta 36,v1,8192
Ta 38,v1,8704
Mvaa 16,24,100
L_ v4,24,s65,0
Mvaa 24,20,64
S_ s65,s60,11008
R_ v13,a248
Mvaa 24,24,104
S_ m0,s50,1024
Ta 224,v1,12288
Ta 226,v1,12800
Mvaa 32,20,68
v_min_u32 v13, 240, v13
Mvaa 32,24,108
L_ v5,24,s65,0
Mvaa 40,20,72
S_ s65,s60,21248
v_lshlrev_b32 v13, 3, v13
Mvaa 40,24,112
S_ m0,s50,2048
Mvaa 48,20,76
Mvaa 48,24,116
W_ 4
Mvaa 52,32,116
s_nop 0
Ta 20,v1,8200
Ta 22,v1,8712
Mvaa 52,28,76
L_ v4,24,s65,0
Mvaa 44,32,112
S_ s65,s60,31488
Mvaa 44,28,72
S_ m0,s50,3072
Ta 24,v1,12296
Ta 26,v1,12808
Mvaa 36,32,108
Mvaa 36,28,68
L_ v5,24,s65,0
Mvaa 28,32,104
S_ s65,s60,41728
Mvaa 28,28,64
S_ m0,s50,4096
Mvaa 20,32,100
s_waitcnt vmcnt(14) lgkmcnt(0)
s_barrier
Mvaa 20,28,60
L_ v4,24,s65,0
Mvaa 56,36,60
S_ s65,s59,3932160
Ta 28,%0,16384
Ta 30,%0,16896
Mvaa 56,224,100
S_ m0,s52,8064
Mvaa 64,36,64
Mvaa 64,224,104
s_nop 0
Ta 32,%0,20480
Ta 34,%0,20992
Mvaa 72,36,68
L_ v6,28,s65,128
Mvaa 72,224,108
S_ s65,s59,4030464
Mvaa 80,36,72
S_ m0,s52,9088
Mvaa 80,224,112
Mvaa 88,36,76
L_ v6,28,s65,128
Mvaa 88,224,116
S_ s65,s59,4128768
Mvaa 92,24,116
S_ m0,s52,10112
Ta 36,%0,16392
Ta 38,%0,16904
Mvaa 92,20,76
S_ s55,s56,6291456
Mvaa 84,24,112
L_ v6,28,s65,128
Mvaa 84,20,72
S_ s65,s59,4227072
Ta 224,%0,20488
Ta 226,%0,21000
Mvaa 76,24,108
S_ m0,s52,11136
Mvaa 76,20,68
Mvaa 68,24,104
L_ v6,28,s65,128
Mvaa 68,20,64
Mvaa 60,24,100
Mvaa 60,20,60
W_ 4
Mvaa 96,28,60
s_nop 0
Ta 20,v1,16384
Ta 22,v1,16896
Mvaa 96,32,100
buffer_load_dword a220, v13, s[44:47], s66 offen
Mvaa 104,28,64
Mvaa 104,32,104
s_nop 0
Ta 24,v1,20480
Ta 26,v1,20992
Mvaa 112,28,68
Mvaa 112,32,108
buffer_load_dword a254, v10, s[28:31], s55 offen
Mvaa 120,28,72
Mvaa 120,32,112
Mvaa 128,28,76
Mvaa 128,32,116
W_ 4
Mvaa 132,224,116
s_nop 0
Ta 28,v1,16392
Ta 30,v1,16904
Mvaa 132,36,76
buffer_load_dword a254, v12, s[24:27], s63 offen offset:1024
Mvaa 124,224,112
Mvaa 124,36,72
s_nop 0
Ta 32,v1,20488
Ta 34,v1,21000
Mvaa 116,224,108
Mvaa 116,36,68
Mvaa 108,224,104
Mvaa 108,36,64
Mvaa 100,224,100
s_waitcnt vmcnt(16) lgkmcnt(0)
s_barrier
s_cmp_eq_u32 s11, 1
s_cbranch_scc1 .Lpace13
.Lpace13_back:
Mvaa 100,36,60
S_ s65,s59,3932160
Da 140,v2,20480
Mvaa 136,20,60
S_ m0,s52,16128
Ta 36,%0,24576
Ta 38,%0,25088
Mvaa 136,24,100
L_ v6,28,s65,256
Mvaa 144,20,64
S_ s65,s59,4030464
Da 180,v3,20480
Mvaa 144,24,104
S_ m0,s52,17152
Ta 224,%0,28672
Ta 226,%0,29184
Mvaa 152,20,68
Mvaa 152,24,108
Da 144,v2,22528
Mvaa 160,20,72
L_ v6,28,s65,256
Mvaa 160,24,112
S_ s65,s59,4128768
Mvaa 168,20,76
S_ m0,s52,18176
Da 184,v3,22528
Mvaa 168,24,116
Mvaa 172,32,116
s_nop 0
Ta 20,%0,24584
Ta 22,%0,25096
Mvaa 172,28,76
Da 148,v2,24576
Mvaa 164,32,112
L_ v6,28,s65,256
Mvaa 164,28,72
S_ s65,s59,4227072
Ta 24,%0,28680
Ta 26,%0,29192
Mvaa 156,32,108
S_ m0,s52,19200
Da 188,v3,24576
Mvaa 156,28,68
Mvaa 148,32,104
L_ v6,28,s65,256
Mvaa 148,28,64
Da 152,v2,26624
Mvaa 140,32,100
Mvaa 140,28,60
W_ 9
Mvaa 176,36,60
s_nop 0
Ta 28,v1,24576
Ta 30,v1,25088
Mvaa 176,224,100
Da 192,v3,26624
Mvaa 184,36,64
Mvaa 184,224,104
s_nop 0
Ta 32,v1,28672
Ta 34,v1,29184
Mvaa 192,36,68
Da 156,v2,28672
Mvaa 192,224,108
Mvaa 200,36,72
Da 196,v3,28672
Mvaa 200,224,112
Mvaa 208,36,76
Mvaa 208,224,116
W_ 9
Mvaa 212,24,116
s_nop 0
Ta 36,v1,24584
Ta 38,v1,25096
Mvaa 212,20,76
Mvaa 204,24,112
Mvaa 204,20,72
s_nop 0
Ta 224,v1,28680
Ta 226,v1,29192
Mvaa 196,24,108
Mvaa 196,20,68
Mvaa 188,24,104
Mvaa 188,20,64
Mvaa 180,24,100
s_waitcnt vmcnt(16) lgkmcnt(0)
s_barrier
Mvaa 180,20,60
S_ s65,s59,4718592
Mvaa 216,28,60
S_ m0,s52,24576
Ta 20,%0,0
Ta 22,%0,512
Mvaa 216,32,100
L_ v6,28,s65,0
Mvaa 224,28,64
S_ s65,s59,4816896
Mvaa 224,32,104
S_ m0,s52,25600
Ta 24,%0,4096
Ta 26,%0,4608
Mvaa 232,28,68
Mvaa 232,32,108
L_ v6,28,s65,0
Mvaa 240,28,72
S_ s65,s59,4915200
Mvaa 240,32,112
S_ m0,s52,26624
Mvaa 248,28,76
Mvaa 248,32,116
Mvaa 252,224,116
s_nop 0
Ta 28,%0,8
Ta 30,%0,520
Mvaa 252,36,76
L_ v6,28,s65,0
Mvaa 244,224,112
S_ s65,s59,5013504
Mvaa 244,36,72
S_ m0,s52,27648
Ta 32,%0,4104
Ta 34,%0,4616
Mvaa 236,224,108
Mvaa 236,36,68
L_ v6,28,s65,0
Mvaa 228,224,104
S_ s65,s60,896
Mvaa 228,36,64
S_ m0,s50,20480
Mvaa 220,224,100
Mvaa 220,36,60
W_ 4
Mvaa 16,20,140
s_nop 0
Ta 36,v1,0
Ta 38,v1,512
Mvaa 16,24,180
L_ v4,24,s65,0
Mvaa 24,20,144
S_ s65,s60,11136
Mvaa 24,24,184
S_ m0,s50,21504
Ta 224,v1,4096
Ta 226,v1,4608
Mvaa 32,20,148
Mvaa 32,24,188
L_ v5,24,s65,0
Mvaa 40,20,152
S_ s65,s60,21376
Mvaa 40,24,192
S_ m0,s50,22528
Mvaa 48,20,156
Mvaa 48,24,196
W_ 4
Mvaa 52,32,196
s_nop 0
Ta 20,v1,8
Ta 22,v1,520
Mvaa 52,28,156
L_ v4,24,s65,0
Mvaa 44,32,192
S_ s65,s60,31616
Mvaa 44,28,152
S_ m0,s50,23552
Ta 24,v1,4104
Ta 26,v1,4616
Mvaa 36,32,188
Mvaa 36,28,148
L_ v5,24,s65,0
Mvaa 28,32,184
S_ s65,s60,41856
Mvaa 28,28,144
S_ m0,s50,24576
Mvaa 20,32,180
s_waitcnt vmcnt(15) lgkmcnt(0)
s_barrier
s_cmp_eq_u32 s11, 1
s_cbranch_scc1 .Lpace15
.Lpace15_back:
Mvaa 20,28,140
L_ v4,24,s65,0
Mvaa 56,36,140
S_ s65,s59,4718592
Ta 28,%0,8192
Ta 30,%0,8704
Mvaa 56,224,180
S_ m0,s52,-128
Mvaa 64,36,144
Mvaa 64,224,184
s_nop 0
Ta 32,%0,12288
Ta 34,%0,12800
Mvaa 72,36,148
L_ v6,28,s65,128
Mvaa 72,224,188
S_ s65,s59,4816896
Mvaa 80,36,152
S_ m0,s52,896
Mvaa 80,224,192
Mvaa 88,36,156
L_ v6,28,s65,128
Mvaa 88,224,196
S_ s65,s59,4915200
Mvaa 92,24,196
S_ m0,s52,1920
Ta 36,%0,8200
Ta 38,%0,8712
Mvaa 92,20,156
S_ s55,s56,7077888
Mvaa 84,24,192
L_ v6,28,s65,128
Mvaa 84,20,152
S_ s65,s59,5013504
Ta 224,%0,12296
Ta 226,%0,12808
Mvaa 76,24,188
S_ m0,s52,2944
Mvaa 76,20,148
Mvaa 68,24,184
L_ v6,28,s65,128
Mvaa 68,20,144
Mvaa 60,24,180
Mvaa 60,20,140
W_ 4
Mvaa 96,28,140
s_nop 0
Ta 20,v1,8192
Ta 22,v1,8704
Mvaa 96,32,180
buffer_load_dword a254, v10, s[28:31], s55 offen
Mvaa 104,28,144
Mvaa 104,32,184
s_nop 0
Ta 24,v1,12288
Ta 26,v1,12800
Mvaa 112,28,148
Mvaa 112,32,188
buffer_load_dword a254, v12, s[24:27], s63 offen offset:1152
Mvaa 120,28,152
Mvaa 120,32,192
Mvaa 128,28,156
Mvaa 128,32,196
W_ 4
Mvaa 132,224,196
s_nop 0
Ta 28,v1,8200
Ta 30,v1,8712
Mvaa 132,36,156
Mvaa 124,224,192
Mvaa 124,36,152
s_nop 0
Ta 32,v1,12296
Ta 34,v1,12808
Mvaa 116,224,188
Mvaa 116,36,148
Mvaa 108,224,184
Mvaa 108,36,144
Mvaa 100,224,180
s_waitcnt vmcnt(15) lgkmcnt(0)
s_barrier
Mvaa 100,36,140
S_ s65,s59,4718592
Da 60,v2,0
Mvaa 136,20,140
S_ m0,s52,7936
Ta 36,%0,16384
Ta 38,%0,16896
Mvaa 136,24,180
L_ v6,28,s65,256
Mvaa 144,20,144
S_ s65,s59,4816896
Da 100,v3,0
Mvaa 144,24,184
S_ m0,s52,8960
Ta 224,%0,20480
Ta 226,%0,20992
Mvaa 152,20,148
Mvaa 152,24,188
Da 64,v2,2048
Mvaa 160,20,152
L_ v6,28,s65,256
Mvaa 160,24,192
S_ s65,s59,4915200
Mvaa 168,20,156
S_ m0,s52,9984
Da 104,v3,2048
Mvaa 168,24,196
Mvaa 172,32,196
s_nop 0
Ta 20,%0,16392
Ta 22,%0,16904
Mvaa 172,28,156
Da 68,v2,4096
Mvaa 164,32,192
L_ v6,28,s65,256
Mvaa 164,28,152
S_ s65,s59,5013504
Ta 24,%0,20488
Ta 26,%0,21000
Mvaa 156,32,188
S_ m0,s52,11008
Da 108,v3,4096
Mvaa 156,28,148
Mvaa 148,32,184
L_ v6,28,s65,256
Mvaa 148,28,144
Da 72,v2,6144
Mvaa 140,32,180
Mvaa 140,28,140
W_ 9
Mvaa 176,36,140
s_nop 0
Ta 28,v1,16384
Ta 30,v1,16896
Mvaa 176,224,180
Da 112,v3,6144
Mvaa 184,36,144
Mvaa 184,224,184
s_nop 0
Ta 32,v1,20480
Ta 34,v1,20992
Mvaa 192,36,148
Da 76,v2,8192
Mvaa 192,224,188
Mvaa 200,36,152
Da 116,v3,8192
Mvaa 200,224,192
Mvaa 208,36,156
Mvaa 208,224,196
W_ 9
Mvaa 212,24,196
s_nop 0
Ta 36,v1,16392
Ta 38,v1,16904
Mvaa 212,20,156
Mvaa 204,24,192
Mvaa 204,20,152
s_nop 0
Ta 224,v1,20488
Ta 226,v1,21000
Mvaa 196,24,188
Mvaa 196,20,148
Mvaa 188,24,184
Mvaa 188,20,144
Mvaa 180,24,180
s_waitcnt vmcnt(15) lgkmcnt(0)
s_barrier
s_cmp_eq_u32 s11, 1
s_cbranch_scc1 .Lpace17
.Lpace17_back:
Mvaa 180,20,140
S_ s65,s59,5505024
Mvaa 216,28,140
S_ m0,s52,16384
Ta 20,%0,24576
Ta 22,%0,25088
Mvaa 216,32,180
L_ v6,28,s65,0
Mvaa 224,28,144
S_ s65,s59,5603328
Mvaa 224,32,184
S_ m0,s52,17408
Ta 24,%0,28672
Ta 26,%0,29184
Mvaa 232,28,148
.Lpad_q2_4:
s_cbranch_execz .Lhop_q2_r
Mvaa 232,32,188
L_ v6,28,s65,0
.Lpad_qx_2:
s_cbranch_execz .Lhop_qx_r
Mvaa 240,28,152
S_ s65,s59,5701632
Mvaa 240,32,192
S_ m0,s52,18432
Mvaa 248,28,156
Mvaa 248,32,196
Mvaa 252,224,196
s_nop 0
Ta 28,%0,24584
Ta 30,%0,25096
Mvaa 252,36,156
L_ v6,28,s65,0
Mvaa 244,224,192
S_ s65,s59,5799936
Mvaa 244,36,152
S_ m0,s52,19456
Ta 32,%0,28680
Ta 34,%0,29192
Mvaa 236,224,188
Mvaa 236,36,148
L_ v6,28,s65,0
Mvaa 228,224,184
S_ s65,s60,1024
Mvaa 228,36,144
S_ m0,s50,0
Mvaa 220,224,180
Mvaa 220,36,140
W_ 4
Mvaa 16,20,60
s_nop 0
Ta 36,v1,24576
Ta 38,v1,25088
Mvaa 16,24,100
L_ v4,24,s65,0
Mvaa 24,20,64
S_ s65,s60,11264
Mvaa 24,24,104
S_ m0,s50,1024
Ta 224,v1,28672
Ta 226,v1,29184
Mvaa 32,20,68
Mvaa 32,24,108
L_ v5,24,s65,0
Mvaa 40,20,72
S_ s65,s60,21504
Mvaa 40,24,112
S_ m0,s50,2048
Mvaa 48,20,76
Mvaa 48,24,116
W_ 4
Mvaa 52,32,116
s_nop 0
Ta 20,v1,24584
Ta 22,v1,25096
Mvaa 52,28,76
L_ v4,24,s65,0
Mvaa 44,32,112
S_ s65,s60,31744
Mvaa 44,28,72
S_ m0,s50,3072
Ta 24,v1,28680
Ta 26,v1,29192
Mvaa 36,32,108
Mvaa 36,28,68
L_ v5,24,s65,0
Mvaa 28,32,104
S_ s65,s60,41984
Mvaa 28,28,64
S_ m0,s50,4096
Mvaa 20,32,100
s_waitcnt vmcnt(14) lgkmcnt(0)
s_barrier
Mvaa 20,28,60
L_ v4,24,s65,0
Mvaa 56,36,60
S_ s65,s59,5505024
Ta 28,%0,0
Ta 30,%0,512
Mvaa 56,224,100
S_ m0,s52,24448
Mvaa 64,36,64
Mvaa 64,224,104
s_nop 0
Ta 32,%0,4096
Ta 34,%0,4608
Mvaa 72,36,68
L_ v6,28,s65,128
Mvaa 72,224,108
S_ s65,s59,5603328
Mvaa 80,36,72
S_ m0,s52,25472
Mvaa 80,224,112
Mvaa 88,36,76
L_ v6,28,s65,128
Mvaa 88,224,116
S_ s65,s59,5701632
Mvaa 92,24,116
S_ m0,s52,26496
Ta 36,%0,8
Ta 38,%0,520
Mvaa 92,20,76
Mvaa 84,24,112
L_ v6,28,s65,128
Mvaa 84,20,72
S_ s65,s59,5799936
Ta 224,%0,4104
Ta 226,%0,4616
Mvaa 76,24,108
S_ m0,s52,27520
Mvaa 76,20,68
Mvaa 68,24,104
L_ v6,28,s65,128
Mvaa 68,20,64
S_ s65,s61,0
Mvaa 60,24,100
S_ m0,s51,36864
Mvaa 60,20,60
W_ 4
Mvaa 96,28,60
s_nop 0
Ta 20,v1,0
Ta 22,v1,512
Mvaa 96,32,100
Ln v7,32,s65,0
Mvaa 104,28,64
Mvaa 104,32,104
s_nop 0
Ta 24,v1,4096
Ta 26,v1,4608
Mvaa 112,28,68
Mvaa 112,32,108
Ln v8,32,s65,3072
Mvaa 120,28,72
S_ m0,s51,37760
Mvaa 120,32,112
Mvaa 128,28,76
Mvaa 128,32,116
W_ 4
Mvaa 132,224,116
s_nop 0
Ta 28,v1,8
Ta 30,v1,520
Mvaa 132,36,76
Ln v7,32,s65,128
Mvaa 124,224,112
Mvaa 124,36,72
s_nop 0
Ta 32,v1,4104
Ta 34,v1,4616
Mvaa 116,224,108
Mvaa 116,36,68
Ln v8,32,s65,3200
Mvaa 108,224,104
S_ m0,s51,38656
Mvaa 108,36,64
Mvaa 100,224,100
s_waitcnt vmcnt(17) lgkmcnt(0)
s_barrier
s_cmp_eq_u32 s11, 1
s_cbranch_scc1 .Lpace19
.Lpace19_back:
Mvaa 100,36,60
Da 140,v2,20480
Mvaa 136,20,60
s_nop 0
Ta 36,%0,8192
Ta 38,%0,8704
Mvaa 136,24,100
Ln v7,32,s65,256
Mvaa 144,20,64
Da 180,v3,20480
Mvaa 144,24,104
s_nop 0
Ta 224,%0,12288
Ta 226,%0,12800
Mvaa 152,20,68
Mvaa 152,24,108
Da 144,v2,22528
Mvaa 160,20,72
Ln v8,32,s65,3328
Mvaa 160,24,112
S_ s65,s59,5505024
Mvaa 168,20,76
S_ m0,s52,-256
Da 184,v3,22528
Mvaa 168,24,116
Mvaa 172,32,116
s_nop 0
Ta 20,%0,8200
Ta 22,%0,8712
Mvaa 172,28,76
Da 148,v2,24576
Mvaa 164,32,112
L_ v6,28,s65,256
Mvaa 164,28,72
S_ s65,s59,5603328
Ta 24,%0,12296
Ta 26,%0,12808
Mvaa 156,32,108
S_ m0,s52,768
Da 188,v3,24576
Mvaa 156,28,68
Mvaa 148,32,104
L_ v6,28,s65,256
Mvaa 148,28,64
S_ s65,s59,5701632
Da 152,v2,26624
Mvaa 140,32,100
S_ m0,s52,1792
Mvaa 140,28,60
W_ 9
Mvaa 176,36,60
s_nop 0
Ta 28,v1,8192
Ta 30,v1,8704
Mvaa 176,224,100
Da 192,v3,26624
Mvaa 184,36,64
L_ v6,28,s65,256
Mvaa 184,224,104
S_ s65,s59,5799936
Ta 32,v1,12288
Ta 34,v1,12800
Mvaa 192,36,68
S_ m0,s52,2816
Da 156,v2,28672
Mvaa 192,224,108
Mvaa 200,36,72
Da 196,v3,28672
Mvaa 200,224,112
L_ v6,28,s65,256
Mvaa 208,36,76
Mvaa 208,224,116
W_ 9
Mvaa 212,24,116
s_nop 0
Ta 36,v1,8200
Ta 38,v1,8712
Mvaa 212,20,76
Mvaa 204,24,112
Mvaa 204,20,72
s_nop 0
Ta 224,v1,12296
Ta 226,v1,12808
Mvaa 196,24,108
Mvaa 196,20,68
Mvaa 188,24,104
Mvaa 188,20,64
Mvaa 180,24,100
s_waitcnt vmcnt(19) lgkmcnt(0)
s_barrier
Mvaa 180,20,60
S_ s65,s59,6291456
Mvaa 216,28,60
S_ m0,s52,7936
Ta 20,%0,16384
Ta 22,%0,16896
Mvaa 216,32,100
L_ v6,28,s65,256
Mvaa 224,28,64
S_ s65,s59,6389760
Mvaa 224,32,104
S_ m0,s52,8960
Ta 24,%0,20480
Ta 26,%0,20992
Mvaa 232,28,68
Mvaa 232,32,108
L_ v6,28,s65,256
Mvaa 240,28,72
S_ s65,s59,6488064
Mvaa 240,32,112
S_ m0,s52,9984
Mvaa 248,28,76
Mvaa 248,32,116
Mvaa 252,224,116
s_nop 0
Ta 28,%0,16392
Ta 30,%0,16904
Mvaa 252,36,76
L_ v6,28,s65,256
Mvaa 244,224,112
S_ s65,s59,6586368
Mvaa 244,36,72
S_ m0,s52,11008
Ta 32,%0,20488
Ta 34,%0,21000
Mvaa 236,224,108
Mvaa 236,36,68
L_ v6,28,s65,256
Mvaa 228,224,104
S_ s65,s60,1152
Mvaa 228,36,64
S_ m0,s50,20480
Mvaa 220,224,100
Mvaa 220,36,60
W_ 4
Mvaa 16,20,140
s_nop 0
Ta 36,v1,16384
Ta 38,v1,16896
Mvaa 16,24,180
L_ v4,24,s65,0
Mvaa 24,20,144
S_ s65,s60,11392
Mvaa 24,24,184
S_ m0,s50,21504
Ta 224,v1,20480
Ta 226,v1,20992
Mvaa 32,20,148
Mvaa 32,24,188
L_ v5,24,s65,0
Mvaa 40,20,152
S_ s65,s60,21632
Mvaa 40,24,192
S_ m0,s50,22528
Mvaa 48,20,156
Mvaa 48,24,196
W_ 4
Mvaa 52,32,196
s_nop 0
Ta 20,v1,16392
Ta 22,v1,16904
Mvaa 52,28,156
L_ v4,24,s65,0
Mvaa 44,32,192
S_ s65,s60,31872
Mvaa 44,28,152
S_ m0,s50,23552
Ta 24,v1,20488
Ta 26,v1,21000
Mvaa 36,32,188
Mvaa 36,28,148
L_ v5,24,s65,0
Mvaa 28,32,184
S_ s65,s60,42112
Mvaa 28,28,144
S_ m0,s50,24576
Mvaa 20,32,180
s_waitcnt vmcnt(18) lgkmcnt(0)
s_barrier
s_cmp_eq_u32 s11, 1
s_cbranch_scc1 .Lpace21
.Lpace21_back:
Mvaa 20,28,140
L_ v4,24,s65,0
Mvaa 56,36,140
S_ s65,s59,7077888
Ta 28,%0,24576
Ta 30,%0,25088
Mvaa 56,224,180
S_ m0,s52,16128
Mvaa 64,36,144
Mvaa 64,224,184
s_nop 0
Ta 32,%0,28672
Ta 34,%0,29184
Mvaa 72,36,148
L_ v6,28,s65,256
Mvaa 72,224,188
S_ s65,s59,7176192
Mvaa 80,36,152
S_ m0,s52,17152
Mvaa 80,224,192
Mvaa 88,36,156
L_ v6,28,s65,256
Mvaa 88,224,196
S_ s65,s59,7274496
Mvaa 92,24,196
S_ m0,s52,18176
Ta 36,%0,24584
Ta 38,%0,25096
Mvaa 92,20,156
Mvaa 84,24,192
L_ v6,28,s65,256
Mvaa 84,20,152
S_ s65,s59,7372800
Ta 224,%0,28680
Ta 226,%0,29192
Mvaa 76,24,188
S_ m0,s52,19200
Mvaa 76,20,148
Mvaa 68,24,184
L_ v6,28,s65,256
Mvaa 68,20,144
Mvaa 60,24,180
Mvaa 60,20,140
W_ 4
Mvaa 96,28,140
s_nop 0
Ta 20,v1,24576
Ta 22,v1,25088
Mvaa 96,32,180
Mvaa 104,28,144
Mvaa 104,32,184
s_nop 0
Ta 24,v1,28672
Ta 26,v1,29184
Mvaa 112,28,148
Mvaa 112,32,188
Mvaa 120,28,152
Mvaa 120,32,192
Mvaa 128,28,156
Mvaa 128,32,196
W_ 4
Mvaa 132,224,196
s_nop 0
Ta 28,v1,24584
Ta 30,v1,25096
Mvaa 132,36,156
Mvaa 124,224,192
Mvaa 124,36,152
s_nop 0
Ta 32,v1,28680
Ta 34,v1,29192
Mvaa 116,224,188
Mvaa 116,36,148
Mvaa 108,224,184
Mvaa 108,36,144
Mvaa 100,224,180
s_waitcnt vmcnt(13) lgkmcnt(0)
s_barrier
Mvaa 100,36,140
S_ s65,s59,6291456
Da 60,v2,0
Mvaa 136,20,140
S_ m0,s52,24448
Ta 36,%0,0
Ta 38,%0,512
Mvaa 136,24,180
L_ v6,28,s65,128
Mvaa 144,20,144
S_ s65,s59,6389760
Da 100,v3,0
Mvaa 144,24,184
S_ m0,s52,25472
Ta 224,%0,4096
Ta 226,%0,4608
Mvaa 152,20,148
Mvaa 152,24,188
Da 64,v2,2048
Mvaa 160,20,152
L_ v6,28,s65,128
Mvaa 160,24,192
S_ s65,s59,6488064
Mvaa 168,20,156
S_ m0,s52,26496
Da 104,v3,2048
Mvaa 168,24,196
Mvaa 172,32,196
s_nop 0
Ta 20,%0,8
Ta 22,%0,520
Mvaa 172,28,156
Da 68,v2,4096
Mvaa 164,32,192
L_ v6,28,s65,128
Mvaa 164,28,152
S_ s65,s59,6586368
Ta 24,%0,4104
Ta 26,%0,4616
Mvaa 156,32,188
S_ m0,s52,27520
Da 108,v3,4096
Mvaa 156,28,148
Mvaa 148,32,184
L_ v6,28,s65,128
Mvaa 148,28,144
S_ s65,s61,12288
Da 72,v2,6144
Mvaa 140,32,180
S_ m0,s51,49152
Mvaa 140,28,140
W_ 9
Mvaa 176,36,140
s_nop 0
Ta 28,v1,0
Ta 30,v1,512
Mvaa 176,224,180
Da 112,v3,6144
Mvaa 184,36,144
Ln v7,32,s65,0
Mvaa 184,224,184
s_nop 0
Ta 32,v1,4096
Ta 34,v1,4608
Mvaa 192,36,148
Da 76,v2,8192
Mvaa 192,224,188
Mvaa 200,36,152
Da 116,v3,8192
Mvaa 200,224,192
Ln v8,32,s65,3072
Mvaa 208,36,156
S_ m0,s51,50048
Mvaa 208,224,196
W_ 9
Mvaa 212,24,196
s_nop 0
Ta 36,v1,8
Ta 38,v1,520
Mvaa 212,20,156
Ln v7,32,s65,128
Mvaa 204,24,192
Mvaa 204,20,152
s_nop 0
Ta 224,v1,4104
Ta 226,v1,4616
Mvaa 196,24,188
Mvaa 196,20,148
Ln v8,32,s65,3200
Mvaa 188,24,184
S_ m0,s51,50944
Mvaa 188,20,144
Mvaa 180,24,180
s_waitcnt vmcnt(12) lgkmcnt(0)
s_barrier
s_cmp_eq_u32 s11, 1
s_cbranch_scc1 .Lpace23
.Lpace23_back:
Mvaa 180,20,140
Ln v7,32,s65,256
Mvaa 216,28,140
s_nop 0
Ta 20,v1,8200
Ta 22,v1,8712
Mvaa 216,32,180
Mvaa 224,28,144
Mvaa 224,32,184
s_nop 0
Ta 24,v1,12296
Ta 26,v1,12808
Mvaa 232,28,148
Ln v8,32,s65,3328
Mvaa 232,32,188
S_ s65,s59,7077888
Mvaa 240,28,152
S_ m0,s52,-128
Mvaa 240,32,192
.Lpad_q2_5:
s_cbranch_execz .Lhop_q2_r
Mvaa 248,28,156
L_ v6,28,s65,128
Mvaa 248,32,196
S_ s65,s59,7176192
Mvaa 252,224,196
S_ m0,s52,896
Ta 28,v1,8192
.Lpad_qx_3:
s_cbranch_execz .Lhop_qx_r
Ta 30,v1,8704
Mvaa 252,36,156
Mvaa 244,224,192
L_ v6,28,s65,128
Mvaa 244,36,152
S_ s65,s59,7274496
Ta 32,v1,12288
Ta 34,v1,12800
Mvaa 236,224,188
S_ m0,s52,1920
Mvaa 236,36,148
Mvaa 228,224,184
L_ v6,28,s65,128
Mvaa 228,36,144
S_ s65,s59,7372800
Mvaa 220,224,180
S_ m0,s52,2944
Mvaa 220,36,140
W_ 4
Mvaa 220,20,60
s_nop 0
Ta 36,%0,8200
Ta 38,%0,8712
Mvaa 220,24,100
Da 140,v2,20480
Mvaa 228,20,64
L_ v6,28,s65,128
Mvaa 228,24,104
s_nop 0
Ta 224,%0,12296
Ta 226,%0,12808
Mvaa 236,20,68
Da 180,v3,20480
Mvaa 236,24,108
Mvaa 244,20,72
Da 144,v2,22528
Mvaa 244,24,112
Mvaa 252,20,76
Mvaa 252,24,116
W_ 7
Da 184,v3,22528
Mvaa 248,32,116
s_nop 0
Ta 20,%0,8192
Ta 22,%0,8704
Mvaa 248,28,76
Mvaa 240,32,112
Da 148,v2,24576
Mvaa 240,28,72
s_nop 0
Ta 24,%0,12288
Ta 26,%0,12800
Mvaa 232,32,108
Mvaa 232,28,68
Da 188,v3,24576
Mvaa 224,32,104
Mvaa 224,28,64
Mvaa 216,32,100
s_waitcnt vmcnt(14) lgkmcnt(0)
s_barrier
Da 152,v2,26624
Mvaa 216,28,60
S_ s65,s59,6291456
Mvaa 180,36,60
S_ m0,s52,8192
Ta 28,v1,16392
Ta 30,v1,16904
Mvaa 180,224,100
Da 192,v3,26624
Mvaa 188,36,64
L_ v6,28,s65,0
Mvaa 188,224,104
S_ s65,s59,6389760
Ta 32,v1,20488
Ta 34,v1,21000
Mvaa 196,36,68
S_ m0,s52,9216
Da 156,v2,28672
Mvaa 196,224,108
Mvaa 204,36,72
L_ v6,28,s65,0
Mvaa 204,224,112
S_ s65,s59,6488064
Da 196,v3,28672
Mvaa 212,36,76
S_ m0,s52,10240
Mvaa 212,224,116
Mvaa 208,24,116
s_nop 0
Ta 36,v1,16384
Ta 38,v1,16896
Mvaa 208,20,76
L_ v6,28,s65,0
Mvaa 200,24,112
S_ s65,s59,6586368
Mvaa 200,20,72
S_ m0,s52,11264
Ta 224,v1,20480
Ta 226,v1,20992
Mvaa 192,24,108
Mvaa 192,20,68
L_ v6,28,s65,0
Mvaa 184,24,104
Mvaa 184,20,64
Mvaa 176,24,100
Mvaa 176,20,60
W_ 6
Mvaa 220,28,140
s_nop 0
Ta 20,%0,16392
Ta 22,%0,16904
Mvaa 220,32,180
Mvaa 228,28,144
Mvaa 228,32,184
s_nop 0
Ta 24,%0,20488
Ta 26,%0,21000
Mvaa 236,28,148
Mvaa 236,32,188
Mvaa 244,28,152
Mvaa 244,32,192
W_ 9
Mvaa 252,28,156
W_ 8
Mvaa 252,32,196
W_ 4
Mvaa 248,224,196
s_nop 0
Ta 28,%0,16384
Ta 30,%0,16896
Mvaa 248,36,156
Mvaa 240,224,192
Mvaa 240,36,152
s_nop 0
Ta 32,%0,20480
Ta 34,%0,20992
Mvaa 232,224,188
Mvaa 232,36,148
Mvaa 224,224,184
Mvaa 224,36,144
Mvaa 216,224,180
s_cmp_eq_u32 s58, 0
s_cbranch_scc0 .Lhq_0
s_mov_b64 exec, 0
s_bitcmp1_b32 s4, 1
s_cbranch_scc1 .Lhop_a0_b
s_bitcmp1_b32 s4, 0
s_cbranch_scc1 .Lpad_a0_1
s_branch .Lpad_a0_0
.Lhop_a0_b:
s_bitcmp1_b32 s4, 0
s_cbranch_scc1 .Lpad_a0_3
s_branch .Lpad_a0_2
.Lhop_a0_r:
s_mov_b64 exec, -1
s_nop 1
.Lhq_0:
s_cmp_eq_u32 s58, 1
s_cbranch_scc0 .Lhq_1
s_mov_b64 exec, 0
s_bitcmp1_b32 s4, 1
s_cbranch_scc1 .Lhop_a1_b
s_bitcmp1_b32 s4, 0
s_cbranch_scc1 .Lpad_a1_1
s_branch .Lpad_a1_0
.Lhop_a1_b:
s_bitcmp1_b32 s4, 0
s_cbranch_scc1 .Lpad_a1_3
s_branch .Lpad_a1_2
.Lhop_a1_r:
s_mov_b64 exec, -1
s_nop 1
.Lhq_1:
s_cmp_eq_u32 s58, 2
s_cbranch_scc0 .Lhq_2
s_mov_b64 exec, 0
s_bitcmp1_b32 s4, 1
s_cbranch_scc1 .Lhop_a2_b
s_bitcmp1_b32 s4, 0
s_cbranch_scc1 .Lpad_a2_1
s_branch .Lpad_a2_0
.Lhop_a2_b:
s_bitcmp1_b32 s4, 0
s_cbranch_scc1 .Lpad_a2_3
s_branch .Lpad_a2_2
.Lhop_a2_r:
s_mov_b64 exec, -1
s_nop 1
.Lhq_2:
s_waitcnt vmcnt(14) lgkmcnt(0)
s_barrier
s_cmp_eq_u32 s11, 1
s_cbranch_scc1 .Lpace25
.Lpace25_back:
Mvaa 216,36,140
S_ s65,s59,7077888
Mvaa 180,20,140
S_ m0,s52,16384
Ta 36,v1,24584
Ta 38,v1,25096
Mvaa 180,24,180
L_ v6,28,s65,0
Mvaa 188,20,144
S_ s65,s59,7176192
Mvaa 188,24,184
S_ m0,s52,17408
Ta 224,v1,28680
Ta 226,v1,29192
Mvaa 196,20,148
U_ v248,s48,v248
U_ v249,s48,v249
Mvaa 196,24,188
L_ v6,28,s65,0
Mvaa 204,20,152
S_ s65,s59,7274496
U_ v250,s48,v250
U_ v251,s48,v251
Mvaa 204,24,192
S_ m0,s52,18432
U_ v252,s48,v252
U_ v253,s48,v253
Mvaa 212,20,156
U_ v254,s48,v254
U_ v255,s48,v255
Mvaa 212,24,196
C_ 255,254,255
C_ 254,252,253
Mvaa 208,32,196
s_nop 0
Ta 20,v1,24576
Ta 22,v1,25088
Mvaa 208,28,156
L_ v6,28,s65,0
Mvaa 200,32,192
S_ s65,s59,7372800
C_ 253,250,251
C_ 252,248,249
Mvaa 200,28,152
S_ m0,s52,19456
Ta 24,v1,28672
Ta 26,v1,29184
Mvaa 192,32,188
U_ v240,s48,v240
U_ v241,s48,v241
Mvaa 192,28,148
L_ v6,28,s65,0
Mvaa 184,32,184
U_ v242,s48,v242
U_ v243,s48,v243
Mvaa 184,28,144
U_ v244,s48,v244
U_ v245,s48,v245
Mvaa 176,32,180
U_ v246,s48,v246
U_ v247,s48,v247
Mvaa 176,28,140
W_ 4
C_ 251,246,247
C_ 250,244,245
Mvaa 140,36,60
s_nop 0
Ta 28,%0,24584
Ta 30,%0,25096
Mvaa 140,224,100
C_ 249,242,243
)A"
R"A(C_ 248,240,241
Mvaa 148,36,64
U_ v232,s48,v232
U_ v233,s48,v233
Mvaa 148,224,104
s_nop 0
Ta 32,%0,28680
Ta 34,%0,29192
Mvaa 156,36,68
U_ v234,s48,v234
U_ v235,s48,v235
Mvaa 156,224,108
U_ v236,s48,v236
U_ v237,s48,v237
Mvaa 164,36,72
U_ v238,s48,v238
U_ v239,s48,v239
Mvaa 164,224,112
C_ 247,238,239
C_ 246,236,237
Mvaa 172,36,76
C_ 245,234,235
C_ 244,232,233
Mvaa 172,224,116
W_ 4
U_ v224,s48,v224
U_ v225,s48,v225
Mvaa 168,24,116
s_nop 0
Ta 36,%0,24576
Ta 38,%0,25088
Mvaa 168,20,76
U_ v226,s48,v226
U_ v227,s48,v227
Mvaa 160,24,112
U_ v228,s48,v228
U_ v229,s48,v229
Mvaa 160,20,72
s_nop 0
Ta 224,%0,28672
Ta 226,%0,29184
Mvaa 152,24,108
U_ v230,s48,v230
U_ v231,s48,v231
Mvaa 152,20,68
C_ 243,230,231
C_ 242,228,229
Mvaa 144,24,104
C_ 241,226,227
C_ 240,224,225
Mvaa 144,20,64
U_ v216,s48,v216
U_ v217,s48,v217
Mvaa 136,24,100
s_waitcnt vmcnt(8) lgkmcnt(0)
s_barrier
U_ v218,s48,v218
Mvaa 136,20,60
S_ s65,s61,0
U_ v219,s48,v219
Mvaa 100,28,60
S_ m0,s51,24576
Ta 20,v1,8
Ta 22,v1,520
Mvaa 100,32,100
Ln v9,36,s65,0
Mvaa 108,28,64
U_ v220,s48,v220
U_ v221,s48,v221
Mvaa 108,32,104
s_nop 0
Ta 24,v1,4104
Ta 26,v1,4616
Mvaa 116,28,68
U_ v222,s48,v222
U_ v223,s48,v223
Mvaa 116,32,108
Ln v9,36,s65,3072
Mvaa 124,28,72
S_ m0,s51,25472
C_ 239,222,223
C_ 238,220,221
Mvaa 124,32,112
C_ 237,218,219
C_ 236,216,217
Mvaa 132,28,76
U_ v208,s48,v208
U_ v209,s48,v209
Mvaa 132,32,116
U_ v210,s48,v210
U_ v211,s48,v211
Mvaa 128,224,116
s_nop 0
Ta 28,v1,0
Ta 30,v1,512
Mvaa 128,36,76
Ln v9,36,s65,128
Mvaa 120,224,112
U_ v212,s48,v212
U_ v213,s48,v213
Mvaa 120,36,72
s_nop 0
Ta 32,v1,4096
Ta 34,v1,4608
Mvaa 112,224,108
U_ v214,s48,v214
U_ v215,s48,v215
Mvaa 112,36,68
Ln v9,36,s65,3200
Mvaa 104,224,104
S_ m0,s51,26368
C_ 235,214,215
C_ 234,212,213
Mvaa 104,36,64
C_ 233,210,211
C_ 232,208,209
Mvaa 96,224,100
U_ v200,s48,v200
U_ v201,s48,v201
Mvaa 96,36,60
W_ 4
U_ v202,s48,v202
U_ v203,s48,v203
Mvaa 140,20,140
s_nop 0
Ta 36,%0,8
Ta 38,%0,520
Mvaa 140,24,180
Ln v9,36,s65,256
Mvaa 148,20,144
U_ v204,s48,v204
U_ v205,s48,v205
Mvaa 148,24,184
s_nop 0
Ta 224,%0,4104
Ta 226,%0,4616
Mvaa 156,20,148
U_ v206,s48,v206
U_ v207,s48,v207
Mvaa 156,24,188
Ln v9,36,s65,3328
Mvaa 164,20,152
C_ 231,206,207
C_ 230,204,205
Mvaa 164,24,192
C_ 229,202,203
C_ 228,200,201
Mvaa 172,20,156
U_ v192,s48,v192
U_ v193,s48,v193
Mvaa 172,24,196
W_ 4
U_ v194,s48,v194
U_ v195,s48,v195
Mvaa 168,32,196
s_nop 0
Ta 20,%0,0
Ta 22,%0,512
Mvaa 168,28,156
U_ v196,s48,v196
U_ v197,s48,v197
Mvaa 160,32,192
U_ v198,s48,v198
U_ v199,s48,v199
Mvaa 160,28,152
s_nop 0
Ta 24,%0,4096
Ta 26,%0,4608
Mvaa 152,32,188
C_ 227,198,199
C_ 226,196,197
Mvaa 152,28,148
C_ 225,194,195
C_ 224,192,193
Mvaa 144,32,184
U_ v184,s48,v184
U_ v185,s48,v185
Mvaa 144,28,144
U_ v186,s48,v186
U_ v187,s48,v187
Mvaa 136,32,180
s_waitcnt vmcnt(10) lgkmcnt(0)
s_barrier
s_cmp_eq_u32 s11, 1
s_cbranch_scc1 .Lpace27
.Lpace27_back:
U_ v188,s48,v188
Mvaa 136,28,140
U_ v189,s48,v189
Mvaa 100,36,140
s_nop 0
Ta 28,v1,8200
Ta 30,v1,8712
Mvaa 100,224,180
U_ v190,s48,v190
U_ v191,s48,v191
Mvaa 108,36,144
C_ 223,190,191
C_ 222,188,189
Mvaa 108,224,184
s_nop 0
Ta 32,v1,12296
Ta 34,v1,12808
Mvaa 116,36,148
C_ 221,186,187
C_ 220,184,185
Mvaa 116,224,188
U_ v176,s48,v176
U_ v177,s48,v177
Mvaa 124,36,152
U_ v178,s48,v178
U_ v179,s48,v179
Mvaa 124,224,192
U_ v180,s48,v180
U_ v181,s48,v181
Mvaa 132,36,156
U_ v182,s48,v182
U_ v183,s48,v183
Mvaa 132,224,196
C_ 219,182,183
C_ 218,180,181
Mvaa 128,24,196
s_nop 0
Ta 36,v1,8192
Ta 38,v1,8704
Mvaa 128,20,156
C_ 217,178,179
C_ 216,176,177
Mvaa 120,24,192
U_ v168,s48,v168
U_ v169,s48,v169
Mvaa 120,20,152
s_nop 0
Ta 224,v1,12288
Ta 226,v1,12800
Mvaa 112,24,188
U_ v170,s48,v170
U_ v171,s48,v171
Mvaa 112,20,148
U_ v172,s48,v172
U_ v173,s48,v173
Mvaa 104,24,184
U_ v174,s48,v174
U_ v175,s48,v175
Mvaa 104,20,144
C_ 215,174,175
C_ 214,172,173
Mvaa 96,24,180
C_ 213,170,171
C_ 212,168,169
Mvaa 96,20,140
W_ 4
U_ v160,s48,v160
U_ v161,s48,v161
Mvaa 60,28,60
s_nop 0
Ta 20,%0,8200
Ta 22,%0,8712
Mvaa 60,32,100
U_ v162,s48,v162
U_ v163,s48,v163
Mvaa 68,28,64
U_ v164,s48,v164
U_ v165,s48,v165
Mvaa 68,32,104
s_nop 0
Ta 24,%0,12296
Ta 26,%0,12808
Mvaa 76,28,68
U_ v166,s48,v166
U_ v167,s48,v167
Mvaa 76,32,108
C_ 211,166,167
C_ 210,164,165
Mvaa 84,28,72
C_ 209,162,163
C_ 208,160,161
Mvaa 84,32,112
U_ v152,s48,v152
U_ v153,s48,v153
Mvaa 92,28,76
U_ v154,s48,v154
U_ v155,s48,v155
Mvaa 92,32,116
W_ 4
U_ v156,s48,v156
U_ v157,s48,v157
Mvaa 88,224,116
s_nop 0
Ta 28,%0,8192
Ta 30,%0,8704
Mvaa 88,36,76
U_ v158,s48,v158
U_ v159,s48,v159
Mvaa 80,224,112
C_ 207,158,159
C_ 206,156,157
Mvaa 80,36,72
s_nop 0
Ta 32,%0,12288
Ta 34,%0,12800
Mvaa 72,224,108
C_ 205,154,155
C_ 204,152,153
Mvaa 72,36,68
U_ v144,s48,v144
U_ v145,s48,v145
Mvaa 64,224,104
U_ v146,s48,v146
U_ v147,s48,v147
Mvaa 64,36,64
U_ v148,s48,v148
U_ v149,s48,v149
Mvaa 56,224,100
s_waitcnt vmcnt(6) lgkmcnt(0)
s_barrier
U_ v150,s48,v150
Mvaa 56,36,60
S_ s65,s61,24576
U_ v151,s48,v151
Mvaa 20,20,60
S_ m0,s51,0
Ta 36,v1,16392
Ta 38,v1,16904
Mvaa 20,24,100
Ln v7,32,s65,0
Mvaa 28,20,64
C_ 203,150,151
C_ 202,148,149
Mvaa 28,24,104
s_nop 0
Ta 224,v1,20488
Ta 226,v1,21000
Mvaa 36,20,68
C_ 201,146,147
C_ 200,144,145
Mvaa 36,24,108
Ln v8,32,s65,3072
Mvaa 44,20,72
S_ m0,s51,896
U_ v136,s48,v136
U_ v137,s48,v137
Mvaa 44,24,112
U_ v138,s48,v138
U_ v139,s48,v139
Mvaa 52,20,76
U_ v140,s48,v140
U_ v141,s48,v141
Mvaa 52,24,116
U_ v142,s48,v142
U_ v143,s48,v143
Mvaa 48,32,116
s_nop 0
Ta 20,v1,16384
Ta 22,v1,16896
Mvaa 48,28,76
Ln v7,32,s65,128
Mvaa 40,32,112
C_ 199,142,143
C_ 198,140,141
Mvaa 40,28,72
.Lpad_q2_6:
s_cbranch_execz .Lhop_q2_r
Ta 24,v1,20480
Ta 26,v1,20992
Mvaa 32,32,108
.Lpad_qx_4:
s_cbranch_execz .Lhop_qx_r
C_ 197,138,139
C_ 196,136,137
Mvaa 32,28,68
Ln v8,32,s65,3200
Mvaa 24,32,104
S_ m0,s51,1792
U_ v128,s48,v128
U_ v129,s48,v129
Mvaa 24,28,64
U_ v130,s48,v130
U_ v131,s48,v131
Mvaa 16,32,100
U_ v132,s48,v132
U_ v133,s48,v133
Mvaa 16,28,60
W_ 4
U_ v134,s48,v134
U_ v135,s48,v135
Mvaa 60,36,140
s_nop 0
Ta 28,%0,16392
Ta 30,%0,16904
Mvaa 60,224,180
Ln v7,32,s65,256
Mvaa 68,36,144
C_ 195,134,135
C_ 194,132,133
Mvaa 68,224,184
s_nop 0
Ta 32,%0,20488
Ta 34,%0,21000
Mvaa 76,36,148
C_ 193,130,131
C_ 192,128,129
Mvaa 76,224,188
Ln v8,32,s65,3328
Mvaa 84,36,152
U_ v120,s48,v120
U_ v121,s48,v121
Mvaa 84,224,192
U_ v122,s48,v122
U_ v123,s48,v123
Mvaa 92,36,156
U_ v124,s48,v124
U_ v125,s48,v125
Mvaa 92,224,196
W_ 4
U_ v126,s48,v126
U_ v127,s48,v127
Mvaa 88,24,196
s_nop 0
Ta 36,%0,16384
Ta 38,%0,16896
Mvaa 88,20,156
C_ 191,126,127
C_ 190,124,125
Mvaa 80,24,192
C_ 189,122,123
C_ 188,120,121
Mvaa 80,20,152
s_nop 0
Ta 224,%0,20480
Ta 226,%0,20992
Mvaa 72,24,188
U_ v112,s48,v112
U_ v113,s48,v113
Mvaa 72,20,148
U_ v114,s48,v114
U_ v115,s48,v115
Mvaa 64,24,184
U_ v116,s48,v116
U_ v117,s48,v117
Mvaa 64,20,144
U_ v118,s48,v118
U_ v119,s48,v119
Mvaa 56,24,180
s_waitcnt vmcnt(51) lgkmcnt(0)
s_barrier
s_cmp_eq_u32 s11, 1
s_cbranch_scc1 .Lpace29
.Lpace29_back:
C_ 187,118,119
Mvaa 56,20,140
S_ s65,s61,12288
C_ 186,116,117
Mvaa 20,28,140
S_ m0,s51,12288
C_ 185,114,115
C_ 184,112,113
Mvaa 20,32,180
Ln v9,36,s65,0
Mvaa 28,28,144
U_ v104,s48,v104
U_ v105,s48,v105
Mvaa 28,32,184
U_ v106,s48,v106
U_ v107,s48,v107
Mvaa 36,28,148
U_ v108,s48,v108
U_ v109,s48,v109
Mvaa 36,32,188
Ln v9,36,s65,3072
Mvaa 44,28,152
S_ m0,s51,13184
U_ v110,s48,v110
U_ v111,s48,v111
Mvaa 44,32,192
C_ 183,110,111
C_ 182,108,109
Mvaa 52,28,156
C_ 181,106,107
C_ 180,104,105
Mvaa 52,32,196
U_ v96,s48,v96
U_ v97,s48,v97
Mvaa 48,224,196
Ln v9,36,s65,128
Mvaa 48,36,156
U_ v98,s48,v98
U_ v99,s48,v99
Mvaa 40,224,192
U_ v100,s48,v100
U_ v101,s48,v101
Mvaa 40,36,152
U_ v102,s48,v102
U_ v103,s48,v103
Mvaa 32,224,188
Ln v9,36,s65,3200
Mvaa 32,36,148
S_ m0,s51,14080
C_ 179,102,103
C_ 178,100,101
Mvaa 24,224,184
C_ 177,98,99
C_ 176,96,97
Mvaa 24,36,144
v_pk_mul_f32 v[88:89], v[88:89], s[48:49]
v_pk_mul_f32 v[90:91], v[90:91], s[48:49]
Mvaa 16,224,180
Ln v9,36,s65,256
Mvaa 16,36,140
v_pk_mul_f32 v[92:93], v[92:93], s[48:49]
v_pk_mul_f32 v[94:95], v[94:95], s[48:49]
Ln v9,36,s65,3328
C_ 175,94,95
C_ 174,92,93
C_ 173,90,91
C_ 172,88,89
v_pk_mul_f32 v[80:81], v[80:81], s[48:49]
v_pk_mul_f32 v[82:83], v[82:83], s[48:49]
v_pk_mul_f32 v[84:85], v[84:85], s[48:49]
v_pk_mul_f32 v[86:87], v[86:87], s[48:49]
C_ 171,86,87
C_ 170,84,85
C_ 169,82,83
C_ 168,80,81
v_pk_mul_f32 v[72:73], v[72:73], s[48:49]
v_pk_mul_f32 v[74:75], v[74:75], s[48:49]
v_pk_mul_f32 v[76:77], v[76:77], s[48:49]
v_pk_mul_f32 v[78:79], v[78:79], s[48:49]
C_ 167,78,79
C_ 166,76,77
C_ 165,74,75
C_ 164,72,73
v_pk_mul_f32 v[40:41], v[40:41], s[48:49]
v_pk_mul_f32 v[42:43], v[42:43], s[48:49]
v_pk_mul_f32 v[44:45], v[44:45], s[48:49]
v_pk_mul_f32 v[46:47], v[46:47], s[48:49]
C_ 151,46,47
C_ 150,44,45
C_ 149,42,43
C_ 148,40,41
v_pk_mul_f32 v[32:33], v[32:33], s[48:49]
v_pk_mul_f32 v[34:35], v[34:35], s[48:49]
v_pk_mul_f32 v[36:37], v[36:37], s[48:49]
v_pk_mul_f32 v[38:39], v[38:39], s[48:49]
C_ 147,38,39
C_ 146,36,37
C_ 145,34,35
C_ 144,32,33
v_pk_mul_f32 v[24:25], v[24:25], s[48:49]
v_pk_mul_f32 v[26:27], v[26:27], s[48:49]
v_pk_mul_f32 v[28:29], v[28:29], s[48:49]
v_pk_mul_f32 v[30:31], v[30:31], s[48:49]
C_ 143,30,31
C_ 142,28,29
C_ 141,26,27
C_ 140,24,25
R_ %0,a244
v_xor_b32 v1, 64, %0
Dv 30,%0,43008
Dv 34,v1,43008
Dv 38,%0,44032
v_pk_mul_f32 v[64:65], v[64:65], s[48:49]
v_pk_mul_f32 v[66:67], v[66:67], s[48:49]
v_pk_mul_f32 v[68:69], v[68:69], s[48:49]
v_pk_mul_f32 v[70:71], v[70:71], s[48:49]
C_ 163,70,71
C_ 162,68,69
C_ 161,66,67
C_ 160,64,65
v_pk_mul_f32 v[56:57], v[56:57], s[48:49]
v_pk_mul_f32 v[58:59], v[58:59], s[48:49]
v_pk_mul_f32 v[60:61], v[60:61], s[48:49]
v_pk_mul_f32 v[62:63], v[62:63], s[48:49]
C_ 159,62,63
C_ 158,60,61
C_ 157,58,59
C_ 156,56,57
v_pk_mul_f32 v[48:49], v[48:49], s[48:49]
v_pk_mul_f32 v[50:51], v[50:51], s[48:49]
v_pk_mul_f32 v[52:53], v[52:53], s[48:49]
v_pk_mul_f32 v[54:55], v[54:55], s[48:49]
C_ 155,54,55
C_ 154,52,53
C_ 153,50,51
C_ 152,48,49
v_pk_mul_f32 v[16:17], v[16:17], s[48:49]
v_pk_mul_f32 v[18:19], v[18:19], s[48:49]
v_pk_mul_f32 v[20:21], v[20:21], s[48:49]
v_pk_mul_f32 v[22:23], v[22:23], s[48:49]
C_ 139,22,23
C_ 138,20,21
C_ 137,18,19
C_ 136,16,17
s_cmp_eq_u32 s58, 1
s_cbranch_scc1 .Latt1
s_cmp_eq_u32 s58, 2
s_cbranch_scc1 .Latt2
.Latt0:
s_cmp_eq_u32 s4, 0
s_cbranch_scc0 .Lhs_tok0
R_ v11,a241
buffer_load_dword a255, v11, s[92:95], 0 offen offset:0 sc1
.Lhs_tok0:
S_ s59,s59,768
W_ 2
Zvvv 96,30,136
Dv 42,v1,44032
R_ v2,a245
Zvvv 100,30,140
v_xor_b32 v3, 32, v2
v_xor_b32 v4, 64, v2
Zvvv 104,30,144
v_xor_b32 v5, 96, v2
Zvvv 108,30,148
s_cmp_eq_u32 s4, 0
s_cselect_b32 s67, 13, 0
Zvvv 112,30,152
W_ 2
s_or_b32 s67, s67, 8192
Mvvv 96,34,156
Dv 30,%0,45056
Mvvv 100,34,160
Mvvv 104,34,164
Mvvv 108,34,168
Mvvv 112,34,172
W_ 2
Mvvv 96,38,176
Dv 34,v1,45056
Mvvv 100,38,180
Mvvv 104,38,184
Mvvv 108,38,188
Mvvv 112,38,192
W_ 2
Mvvv 96,42,196
Dv 38,%0,36864
Mvvv 100,42,200
Mvvv 104,42,204
Mvvv 108,42,208
Mvvv 112,42,212
W_ 2
Mvvv 96,30,216
Dv 42,v1,36864
Mvvv 100,30,220
Mvvv 104,30,224
Mvvv 108,30,228
Mvvv 112,30,232
W_ 2
Mvvv 96,34,236
Dv 30,%0,37888
Mvvv 100,34,240
Mvvv 104,34,244
Mvvv 108,34,248
Mvvv 112,34,252
W_ 2
s_nop 1
v_max3_f32 v56, v96, v97, v98
v_max_f32 v56, v56, v99
Zvvv 76,38,136
s_memtime s[12:13]
Dv 34,v1,37888
v_max3_f32 v60, v100, v101, v102
Zvvv 80,38,140
s_memrealtime s[100:101]
v_max_f32 v60, v60, v103
Zvvv 84,38,144
v_max3_f32 v64, v104, v105, v106
v_max_f32 v64, v64, v107
Zvvv 88,38,148
v_max3_f32 v68, v108, v109, v110
v_max_f32 v68, v68, v111
Zvvv 92,38,152
W_ 2
v_max3_f32 v72, v112, v113, v114
v_max_f32 v72, v72, v115
Mvvv 76,42,156
Dv 38,%0,38912
Mvvv 80,42,160
Mvvv 84,42,164
Mvvv 88,42,168
Mvvv 92,42,172
W_ 2
Mvvv 76,30,176
Dv 42,v1,38912
Mvvv 80,30,180
Mvvv 84,30,184
Mvvv 88,30,188
Mvvv 92,30,192
s_waitcnt vmcnt(30) lgkmcnt(0)
s_barrier
Mvvv 76,34,196
S_ s65,s61,184320
Dv 30,%0,55296
Mvvv 76,38,216
S_ m0,s51,36864
s_sub_u32 s12, s12, s90
Mvvv 76,42,236
L_ v9,36,s65,0
Mvvv 80,34,200
s_sub_u32 s100, s100, s68
Mvvv 80,38,220
L_ v9,36,s65,3072
Mvvv 80,42,240
S_ m0,s51,37760
s_sub_u32 s101, s68, s98
Mvvv 84,34,204
L_ v9,36,s65,128
Mvvv 84,38,224
s_mul_i32 s12, s12, s101
v_max3_f32 v56, v56, v76, v77
v_max3_f32 v56, v56, v78, v79
Mvvv 84,42,244
L_ v9,36,s65,3200
Mvvv 88,34,208
S_ m0,s51,38656
v_max3_f32 v60, v60, v80, v81
s_sub_u32 s101, s90, s88
Mvvv 88,38,228
L_ v9,36,s65,256
Mvvv 88,42,248
s_mul_i32 s100, s100, s101
v_max3_f32 v60, v60, v82, v83
Mvvv 92,34,212
L_ v9,36,s65,3328
Mvvv 92,38,232
s_lshr_b32 s100, s100, 6
v_max3_f32 v64, v64, v84, v85
v_max3_f32 v64, v64, v86, v87
Mvvv 92,42,252
s_mul_i32 s100, s100, 47
s_cmp_gt_u32 s101, 4000
s_cselect_b32 s13, 2, 0
s_cmp_gt_u32 s12, s100
s_cselect_b32 s13, s13, 0
Dv 34,v1,55296
.p2align 3
v_mov_b32 v11, 0x3f803f80
v_permlane32_swap_b32 v56, v60
v_max3_f32 v68, v68, v88, v89
v_max3_f32 v68, v68, v90, v91
v_max_f32 v56, v56, v60
v_max3_f32 v72, v72, v92, v93
v_max3_f32 v72, v72, v94, v95
v_mov_b32 v73, v72
v_permlane32_swap_b32 v64, v68
v_max_f32 v64, v64, v68
v_permlane32_swap_b32 v72, v73
v_max_f32 v72, v72, v73
v_mov_b32 v73, v72
v_permlane16_swap_b32 v56, v64
v_max_f32 v56, v56, v64
v_permlane16_swap_b32 v72, v73
B_ v56,0xc0e00000,v56
v_max_f32 v72, v72, v73
v_mov_b32 v64, v56
B_ v72,0xc0e00000,v72
v_mov_b32 v73, v72
v_permlane16_swap_b32 v56, v64
v_mov_b32 v60, v56
v_mov_b32 v68, v64
v_mov_b64 v[74:75], v[72:73]
v_permlane32_swap_b32 v56, v60
v_permlane32_swap_b32 v64, v68
v_mov_b32 v57, v56
v_mov_b32 v61, v60
v_mov_b32 v65, v64
v_mov_b32 v69, v68
v_pk_add_f32 v[96:97], v[96:97], v[56:57]
v_pk_add_f32 v[98:99], v[98:99], v[56:57]
v_pk_add_f32 v[76:77], v[76:77], v[56:57]
v_pk_add_f32 v[78:79], v[78:79], v[56:57]
v_mov_b64 v[58:59], v[56:57]
v_pk_add_f32 v[100:101], v[100:101], v[60:61]
v_pk_add_f32 v[102:103], v[102:103], v[60:61]
v_pk_add_f32 v[80:81], v[80:81], v[60:61]
v_pk_add_f32 v[82:83], v[82:83], v[60:61]
v_mov_b64 v[62:63], v[60:61]
v_pk_add_f32 v[104:105], v[104:105], v[64:65]
v_pk_add_f32 v[106:107], v[106:107], v[64:65]
v_pk_add_f32 v[84:85], v[84:85], v[64:65]
v_pk_add_f32 v[86:87], v[86:87], v[64:65]
v_mov_b64 v[66:67], v[64:65]
v_pk_add_f32 v[108:109], v[108:109], v[68:69]
v_pk_add_f32 v[110:111], v[110:111], v[68:69]
v_pk_add_f32 v[88:89], v[88:89], v[68:69]
v_pk_add_f32 v[90:91], v[90:91], v[68:69]
v_mov_b64 v[70:71], v[68:69]
v_pk_add_f32 v[112:113], v[112:113], v[72:73]
v_pk_add_f32 v[114:115], v[114:115], v[72:73]
v_pk_add_f32 v[92:93], v[92:93], v[72:73]
v_pk_add_f32 v[94:95], v[94:95], v[72:73]
E_ 96
E_ 97
E_ 98
E_ 99
E_ 76
E_ 77
E_ 78
E_ 79
C_ 99,98,99
C_ 98,96,97
C_ 96,76,77
C_ 97,78,79
E_ 100
E_ 101
E_ 102
E_ 103
E_ 80
E_ 81
E_ 82
E_ 83
C_ 103,102,103
C_ 102,100,101
C_ 100,80,81
C_ 101,82,83
.Lstep0_1:
W_ 1
Nvvv 116,30,136,56
s_nop 0
Dv 38,%0,56320
Dv 42,v1,56320
W_ 2
Mvvv 116,34,156
E_ 104
Nvvv 120,30,140,60
E_ 105
Mvvv 120,34,160
E_ 106
Nvvv 124,30,144,64
E_ 107
Mvvv 124,34,164
E_ 84
Nvvv 128,30,148,68
E_ 85
Mvvv 128,34,168
E_ 86
Nvvv 132,30,152,72
E_ 87
Mvvv 132,34,172
W_ 0
C_ 107,106,107
C_ 106,104,105
Mvvv 132,38,192
s_nop 0
Dv 30,%0,57344
Dv 34,v1,57344
Mvvv 132,42,212
C_ 104,84,85
C_ 105,86,87
Mvvv 128,38,188
E_ 108
Mvvv 128,42,208
E_ 109
Mvvv 124,38,184
E_ 110
Mvvv 124,42,204
E_ 111
Mvvv 120,38,180
E_ 88
Mvvv 120,42,200
E_ 89
Mvvv 116,38,176
E_ 90
Mvvv 116,42,196
W_ 0
E_ 91
Mvvv 116,30,216
s_nop 0
Dv 38,%0,49152
Dv 42,v1,49152
Mvvv 116,34,236
C_ 111,110,111
C_ 110,108,109
Mvvv 120,30,220
C_ 108,88,89
C_ 109,90,91
Mvvv 120,34,240
E_ 112
Mvvv 124,30,224
E_ 113
Mvvv 124,34,244
E_ 114
Mvvv 128,30,228
E_ 115
Mvvv 128,34,248
E_ 92
Mvvv 132,30,232
E_ 93
Mvvv 132,34,252
W_ 0
E_ 94
Nvvv 76,38,136,56
s_nop 0
Dv 30,%0,50176
Dv 34,v1,50176
Mvvv 76,42,156
E_ 95
Nvvv 80,38,140,60
C_ 115,114,115
C_ 114,112,113
Mvvv 80,42,160
C_ 112,92,93
C_ 113,94,95
Nvvv 84,38,144,64
E_ 116
Mvvv 84,42,164
E_ 117
Nvvv 88,38,148,68
E_ 118
Mvvv 88,42,168
E_ 119
Nvvv 92,38,152,72
C_ 119,118,119
C_ 118,116,117
Mvvv 92,42,172
W_ 0
E_ 120
Mvvv 92,30,192
s_nop 0
Dv 38,%0,51200
Dv 42,v1,51200
Mvvv 92,34,212
X_ a180,v11
X_ a181,v11
Mvvv 88,30,188
X_ a182,v11
X_ a183,v11
Mvvv 88,34,208
E_ 121
Mvvv 84,30,184
E_ 122
Mvvv 84,34,204
E_ 123
Mvvv 80,30,180
C_ 123,122,123
C_ 122,120,121
Mvvv 80,34,200
E_ 124
Mvvv 76,30,176
E_ 125
Mvvv 76,34,196
S_ s65,s61,36864
E_ 126
W_ 1
Mvvv 76,38,216
S_ m0,s51,49152
E_ 127
W_ 0
Mvvv 76,42,236
s_waitcnt vmcnt(18) lgkmcnt(0)
s_barrier
C_ 127,126,127
Mvvv 80,38,220
Ln v7,32,s65,0
Mvvv 80,42,240
C_ 126,124,125
Mvvv 84,38,224
Ln v8,32,s65,3072
S_ m0,s51,50048
Mvvv 84,42,244
s_nop 0
Tv 30,v2,24576
Tv 32,v2,30720
Mvvv 88,38,228
Ln v7,32,s65,128
Mvvv 88,42,248
s_nop 0
Tv 34,v3,24576
Tv 36,v3,30720
Mvvv 92,38,232
Ln v8,32,s65,3200
S_ m0,s51,50944
Mvvv 92,42,252
.Lpad_a0_0:
s_cbranch_execz .Lhop_a0_r
E_ 128
Zaav 220,180,96
Ln v7,32,s65,256
Zaav 224,180,100
E_ 129
Zaav 228,180,104
Ln v8,32,s65,3328
Zaav 232,180,108
E_ 130
Zaav 236,180,112
W_ 0
E_ 131
Zavv 0,30,96
s_nop 0
Tv 38,v4,24576
Tv 40,v4,30720
Zavv 20,34,96
C_ 131,130,131
C_ 130,128,129
Zavv 24,34,100
s_nop 0
Tv 42,v5,24576
Tv 44,v5,30720
Zavv 4,30,100
E_ 132
Zavv 8,30,104
E_ 133
Zavv 28,34,104
R_ v46,a220
R_ v47,a224
Zavv 32,34,108
R_ v48,a228
R_ v49,a232
Zavv 12,30,108
R_ v50,a236
Zavv 16,30,112
E_ 134
Zavv 36,34,112
W_ 0
E_ 135
Zavv 56,38,112
s_nop 0
Tv 30,v2,25600
Tv 32,v2,31744
Zavv 76,42,112
C_ 135,134,135
C_ 134,132,133
Zavv 72,42,108
s_nop 0
Tv 34,v3,25600
Tv 36,v3,31744
Zavv 52,38,108
E_ 76
Zavv 48,38,104
E_ 77
Zavv 68,42,104
E_ 78
Zavv 64,42,100
E_ 79
Zavv 44,38,100
C_ 116,76,77
C_ 117,78,79
Zavv 40,38,96
E_ 80
Zavv 60,42,96
W_ 0
E_ 81
Zavv 80,30,96
s_nop 0
Tv 38,v4,25600
Tv 40,v4,31744
Zavv 100,34,96
E_ 82
Zavv 104,34,100
s_nop 0
Tv 42,v5,25600
Tv 44,v5,31744
Zavv 84,30,100
E_ 83
Zavv 88,30,104
C_ 120,80,81
C_ 121,82,83
Zavv 108,34,104
E_ 84
Zavv 112,34,108
E_ 85
Zavv 92,30,108
E_ 86
Zavv 96,30,112
E_ 87
Zavv 116,34,112
W_ 0
C_ 124,84,85
C_ 125,86,87
Zavv 136,38,112
s_nop 0
Tv 30,v2,26624
Tv 32,v2,32768
Zavv 156,42,112
E_ 88
Zavv 152,42,108
s_nop 0
Tv 34,v3,26624
Tv 36,v3,32768
Zavv 132,38,108
E_ 89
Zavv 128,38,104
E_ 90
Zavv 148,42,104
E_ 91
Zavv 144,42,100
C_ 128,88,89
C_ 129,90,91
Zavv 124,38,100
E_ 92
Zavv 120,38,96
E_ 93
Zavv 140,42,96
W_ 0
E_ 94
Zavv 160,30,96
s_nop 0
Tv 38,v4,26624
Tv 40,v4,32768
Zavv 180,34,96
E_ 95
Zavv 184,34,100
s_nop 0
Tv 42,v5,26624
Tv 44,v5,32768
Zavv 164,30,100
C_ 132,92,93
C_ 133,94,95
Zavv 168,30,104
v_mov_b32 v92, 0x3f803f80
v_mov_b32 v93, 0x3f803f80
Zavv 188,34,104
S_ s65,s61,24576
v_mov_b32 v94, 0x3f803f80
v_mov_b32 v95, 0x3f803f80
Zavv 192,34,108
S_ m0,s51,24576
Zavv 172,30,108
s_waitcnt vmcnt(18) lgkmcnt(0)
s_barrier
Zavv 176,30,112
L_ v9,36,s65,0
Zavv 196,34,112
Zavv 216,38,112
Dv 30,%0,6144
Zavv 236,42,112
Dv 34,v1,6144
Zavv 232,42,108
L_ v9,36,s65,3072
S_ m0,s51,25472
Zavv 212,38,108
Zavv 208,38,104
L_ v9,36,s65,128
Zavv 228,42,104
Zavv 224,42,100
L_ v9,36,s65,3200
S_ m0,s51,26368
Zavv 204,38,100
Zavv 200,38,96
L_ v9,36,s65,256
Zavv 220,42,96
.Lstep0_2:
Zvvv 76,92,116
Dv 38,%0,7168
Zvvv 80,92,120
Dv 42,v1,7168
Zvvv 84,92,124
Zvvv 88,92,128
Zvvv 92,92,132
W_ 2
Nvvv 96,30,136,56
Mvvv 96,34,156
Nvvv 100,30,140,60
s_nop 1
v_max3_f32 v10, v76, v80, v84
s_nop 0
v_max3_f32 v10, v10, v88, v92
Mvvv 100,34,160
v_cmp_lt_f32 vcc, 0x42000000, v10
s_cbranch_vccnz .Ltramp0_1
Nvvv 104,30,144,64
A_ v46,v46,v76
A_ v47,v47,v80
Mvvv 104,34,164
A_ v48,v48,v84
A_ v49,v49,v88
Nvvv 108,30,148,68
A_ v50,v50,v92
Mvvv 108,34,168
L_ v9,36,s65,3328
Nvvv 112,30,152,72
R_ v11,a248
Mvvv 112,34,172
W_ 0
v_min_u32 v11, 240, v11
Mvvv 112,38,192
Dv 30,%0,8192
v_lshlrev_b32 v11, 3, v11
Mvvv 112,42,212
Dv 34,v1,8192
Mvvv 108,38,188
buffer_load_dword a246, v11, s[44:47], s66 offen
Mvvv 108,42,208
Mvvv 104,38,184
Mvvv 104,42,204
Mvvv 100,38,180
Mvvv 100,42,200
Mvvv 96,38,176
Mvvv 96,42,196
W_ 0
Mvvv 96,30,216
Dv 38,%0,0
Mvvv 96,34,236
Dv 42,v1,0
Mvvv 100,30,220
Mvvv 100,34,240
Mvvv 104,30,224
Mvvv 104,34,244
Mvvv 108,30,228
Mvvv 108,34,248
s_nop 0
E_ 96
Mvvv 112,30,232
E_ 97
Mvvv 112,34,252
W_ 0
E_ 98
Nvvv 76,38,136,56
Dv 30,%0,1024
Mvvv 76,42,156
Dv 34,v1,1024
Nvvv 80,38,140,60
E_ 99
Mvvv 80,42,160
C_ 99,98,99
C_ 98,96,97
Nvvv 84,38,144,64
E_ 100
Mvvv 84,42,164
E_ 101
Nvvv 88,38,148,68
E_ 102
Mvvv 88,42,168
E_ 103
Nvvv 92,38,152,72
C_ 103,102,103
C_ 102,100,101
Mvvv 92,42,172
W_ 0
E_ 104
Mvvv 92,30,192
Dv 38,%0,2048
Mvvv 92,34,212
Dv 42,v1,2048
Mvvv 88,30,188
E_ 105
Mvvv 88,34,208
E_ 106
Mvvv 84,30,184
S_ s65,s61,49152
E_ 107
Mvvv 84,34,204
S_ m0,s51,0
C_ 107,106,107
C_ 106,104,105
Mvvv 80,30,180
s_waitcnt vmcnt(13) lgkmcnt(0)
s_barrier
s_cmp_eq_u32 s11, 2
s_cbranch_scc1 .Lxs2m
.Lxs2m_back:
Mvvv 80,34,200
Ln v7,32,s65,0
Mvvv 76,30,176
E_ 108
Mvvv 76,34,196
Ln v8,32,s65,3072
S_ m0,s51,896
Mvvv 76,38,216
Tv 30,v2,12288
Tv 32,v2,18432
Mvvv 76,42,236
Ln v7,32,s65,128
Mvvv 80,38,220
s_nop 0
Tv 34,v3,12288
Tv 36,v3,18432
Mvvv 80,42,240
Ln v8,32,s65,3200
S_ m0,s51,1792
Mvvv 84,38,224
E_ 109
Mvvv 84,42,244
Ln v7,32,s65,256
Mvvv 88,38,228
E_ 110
Mvvv 88,42,248
Ln v8,32,s65,3328
Mvvv 92,38,232
E_ 111
Mvvv 92,42,252
W_ 0
C_ 111,110,111
C_ 110,108,109
Mavv 0,30,116
s_nop 0
Tv 38,v4,12288
Tv 40,v4,18432
Mavv 20,34,116
E_ 112
Mavv 24,34,120
s_nop 0
Tv 42,v5,12288
Tv 44,v5,18432
Mavv 4,30,120
E_ 113
Mavv 8,30,124
E_ 114
Mavv 28,34,124
E_ 115
Mavv 32,34,128
C_ 115,114,115
C_ 114,112,113
Mavv 12,30,128
E_ 76
Mavv 16,30,132
E_ 77
Mavv 36,34,132
W_ 0
E_ 78
Mavv 56,38,132
s_nop 0
Tv 30,v2,13312
Tv 32,v2,19456
Mavv 76,42,132
E_ 79
Mavv 72,42,128
s_nop 0
Tv 34,v3,13312
Tv 36,v3,19456
Mavv 52,38,128
C_ 96,76,77
C_ 97,78,79
Mavv 48,38,124
E_ 80
Mavv 68,42,124
E_ 81
Mavv 64,42,120
E_ 82
Mavv 44,38,120
E_ 83
Mavv 40,38,116
C_ 100,80,81
C_ 101,82,83
Mavv 60,42,116
W_ 0
E_ 84
Mavv 80,30,116
s_nop 0
Tv 38,v4,13312
Tv 40,v4,19456
Mavv 100,34,116
E_ 85
Mavv 104,34,120
s_nop 0
Tv 42,v5,13312
Tv 44,v5,19456
Mavv 84,30,120
E_ 86
Mavv 88,30,124
E_ 87
Mavv 108,34,124
C_ 104,84,85
C_ 105,86,87
Mavv 112,34,128
E_ 88
Mavv 92,30,128
E_ 89
Mavv 96,30,132
E_ 90
Mavv 116,34,132
W_ 0
E_ 91
Mavv 136,38,132
s_nop 0
Tv 30,v2,14336
Tv 32,v2,20480
Mavv 156,42,132
C_ 108,88,89
C_ 109,90,91
Mavv 152,42,128
s_nop 0
Tv 34,v3,14336
Tv 36,v3,20480
Mavv 132,38,128
E_ 92
Mavv 128,38,124
E_ 93
Mavv 148,42,124
E_ 94
Mavv 144,42,120
E_ 95
Mavv 124,38,120
C_ 112,92,93
C_ 113,94,95
Mavv 120,38,116
v_mov_b32 v92, 0x3f803f80
v_mov_b32 v93, 0x3f803f80
Mavv 140,42,116
W_ 0
v_mov_b32 v94, 0x3f803f80
v_mov_b32 v95, 0x3f803f80
Mavv 160,30,116
s_nop 0
Tv 38,v4,14336
Tv 40,v4,20480
Mavv 180,34,116
Mavv 184,34,120
s_nop 0
Tv 42,v5,14336
Tv 44,v5,20480
Mavv 164,30,120
Mavv 168,30,124
Mavv 188,34,124
S_ s65,s61,36864
Mavv 192,34,128
S_ m0,s51,12288
Mavv 172,30,128
s_waitcnt vmcnt(13) lgkmcnt(0)
s_barrier
s_cmp_eq_u32 s11, 2
s_cbranch_scc1 .Lxs2e
.Lxs2e_back:
Mavv 176,30,132
L_ v9,36,s65,0
Mavv 196,34,132
Mavv 216,38,132
Dv 30,%0,55296
Mavv 236,42,132
Dv 34,v1,55296
Mavv 232,42,128
L_ v9,36,s65,3072
S_ m0,s51,13184
Mavv 212,38,128
Mavv 208,38,124
L_ v9,36,s65,128
Mavv 228,42,124
Mavv 224,42,120
L_ v9,36,s65,3200
S_ m0,s51,14080
Mavv 204,38,120
Mavv 200,38,116
L_ v9,36,s65,256
Mavv 220,42,116
s_bitcmp1_b32 s67, 0
s_cbranch_scc1 .Lhsd0_0
.Lhsd0_0_back:
.Lstep0_3:
Zvvv 76,92,96
Dv 38,%0,56320
Zvvv 80,92,100
Dv 42,v1,56320
Zvvv 84,92,104
Zvvv 88,92,108
Zvvv 92,92,112
W_ 2
Nvvv 116,30,136,56
Mvvv 116,34,156
Nvvv 120,30,140,60
s_nop 1
v_max3_f32 v10, v76, v80, v84
s_nop 0
v_max3_f32 v10, v10, v88, v92
Mvvv 120,34,160
v_cmp_lt_f32 vcc, 0x42000000, v10
s_cbranch_vccnz .Ltramp0_2
Nvvv 124,30,144,64
A_ v46,v46,v76
A_ v47,v47,v80
Mvvv 124,34,164
A_ v48,v48,v84
A_ v49,v49,v88
Nvvv 128,30,148,68
A_ v50,v50,v92
Mvvv 128,34,168
L_ v9,36,s65,3328
Nvvv 132,30,152,72
Mvvv 132,34,172
W_ 0
Mvvv 132,38,192
Dv 30,%0,57344
Mvvv 132,42,212
Dv 34,v1,57344
Mvvv 128,38,188
Mvvv 128,42,208
Mvvv 124,38,184
Mvvv 124,42,204
Mvvv 120,38,180
Mvvv 120,42,200
Mvvv 116,38,176
Mvvv 116,42,196
W_ 0
Mvvv 116,30,216
Dv 38,%0,49152
Mvvv 116,34,236
Dv 42,v1,49152
Mvvv 120,30,220
Mvvv 120,34,240
Mvvv 124,30,224
Mvvv 124,34,244
Mvvv 128,30,228
Mvvv 128,34,248
s_nop 0
E_ 116
Mvvv 132,30,232
E_ 117
Mvvv 132,34,252
W_ 0
E_ 118
Nvvv 76,38,136,56
Dv 30,%0,50176
Mvvv 76,42,156
Dv 34,v1,50176
Nvvv 80,38,140,60
E_ 119
Mvvv 80,42,160
C_ 119,118,119
C_ 118,116,117
Nvvv 84,38,144,64
E_ 120
Mvvv 84,42,164
E_ 121
Nvvv 88,38,148,68
E_ 122
Mvvv 88,42,168
E_ 123
Nvvv 92,38,152,72
C_ 123,122,123
C_ 122,120,121
Mvvv 92,42,172
W_ 0
E_ 124
Mvvv 92,30,192
Dv 38,%0,51200
Mvvv 92,34,212
Dv 42,v1,51200
Mvvv 88,30,188
E_ 125
Mvvv 88,34,208
E_ 126
Mvvv 84,30,184
S_ s65,s61,61440
E_ 127
Mvvv 84,34,204
S_ m0,s51,49152
C_ 127,126,127
C_ 126,124,125
Mvvv 80,30,180
s_waitcnt vmcnt(13) lgkmcnt(0)
s_barrier
s_cmp_eq_u32 s11, 2
s_cbranch_scc1 .Lxs3m
.Lxs3m_back:
Mvvv 80,34,200
Ln v7,32,s65,0
Mvvv 76,30,176
E_ 128
Mvvv 76,34,196
Ln v8,32,s65,3072
S_ m0,s51,50048
Mvvv 76,38,216
Tv 30,v2,24576
Tv 32,v2,30720
Mvvv 76,42,236
Ln v7,32,s65,128
Mvvv 80,38,220
s_nop 0
Tv 34,v3,24576
Tv 36,v3,30720
Mvvv 80,42,240
Ln v8,32,s65,3200
S_ m0,s51,50944
Mvvv 84,38,224
E_ 129
Mvvv 84,42,244
Ln v7,32,s65,256
Mvvv 88,38,228
E_ 130
Mvvv 88,42,248
Ln v8,32,s65,3328
Mvvv 92,38,232
E_ 131
Mvvv 92,42,252
W_ 0
C_ 131,130,131
C_ 130,128,129
Mavv 0,30,96
.Lpad_a0_1:
s_cbranch_execz .Lhop_a0_r
Tv 38,v4,24576
Tv 40,v4,30720
Mavv 20,34,96
E_ 132
Mavv 24,34,100
s_nop 0
Tv 42,v5,24576
Tv 44,v5,30720
Mavv 4,30,100
E_ 133
Mavv 8,30,104
E_ 134
Mavv 28,34,104
E_ 135
Mavv 32,34,108
C_ 135,134,135
C_ 134,132,133
Mavv 12,30,108
E_ 76
Mavv 16,30,112
E_ 77
Mavv 36,34,112
W_ 0
E_ 78
Mavv 56,38,112
s_nop 0
Tv 30,v2,25600
Tv 32,v2,31744
Mavv 76,42,112
E_ 79
Mavv 72,42,108
s_nop 0
Tv 34,v3,25600
Tv 36,v3,31744
Mavv 52,38,108
C_ 116,76,77
C_ 117,78,79
Mavv 48,38,104
E_ 80
Mavv 68,42,104
E_ 81
Mavv 64,42,100
E_ 82
Mavv 44,38,100
E_ 83
Mavv 40,38,96
C_ 120,80,81
C_ 121,82,83
Mavv 60,42,96
W_ 0
E_ 84
Mavv 80,30,96
s_nop 0
Tv 38,v4,25600
Tv 40,v4,31744
Mavv 100,34,96
E_ 85
Mavv 104,34,100
s_nop 0
Tv 42,v5,25600
Tv 44,v5,31744
Mavv 84,30,100
E_ 86
Mavv 88,30,104
E_ 87
Mavv 108,34,104
C_ 124,84,85
C_ 125,86,87
Mavv 112,34,108
E_ 88
Mavv 92,30,108
E_ 89
Mavv 96,30,112
E_ 90
Mavv 116,34,112
W_ 0
E_ 91
Mavv 136,38,112
s_nop 0
Tv 30,v2,26624
Tv 32,v2,32768
Mavv 156,42,112
C_ 128,88,89
C_ 129,90,91
Mavv 152,42,108
s_nop 0
Tv 34,v3,26624
Tv 36,v3,32768
Mavv 132,38,108
E_ 92
Mavv 128,38,104
E_ 93
Mavv 148,42,104
E_ 94
Mavv 144,42,100
E_ 95
Mavv 124,38,100
C_ 132,92,93
C_ 133,94,95
Mavv 120,38,96
v_mov_b32 v92, 0x3f803f80
v_mov_b32 v93, 0x3f803f80
Mavv 140,42,96
W_ 0
v_mov_b32 v94, 0x3f803f80
v_mov_b32 v95, 0x3f803f80
Mavv 160,30,96
s_nop 0
Tv 38,v4,26624
Tv 40,v4,32768
Mavv 180,34,96
Mavv 184,34,100
s_nop 0
Tv 42,v5,26624
Tv 44,v5,32768
Mavv 164,30,100
Mavv 168,30,104
Mavv 188,34,104
S_ s65,s61,49152
Mavv 192,34,108
S_ m0,s51,24576
Mavv 172,30,108
s_waitcnt vmcnt(12) lgkmcnt(0)
s_barrier
s_cmp_eq_u32 s11, 2
s_cbranch_scc1 .Lxs3e
.Lxs3e_back:
Mavv 176,30,112
L_ v9,36,s65,0
Mavv 196,34,112
S_ s61,s61,24576
Mavv 216,38,112
s_lshr_b32 s67, s67, 1
Dv 30,%0,6144
Mavv 236,42,112
s_bitcmp1_b32 s67, 9
s_cselect_b32 s11, s13, s11
Dv 34,v1,6144
Mavv 232,42,108
L_ v9,36,s65,3072
S_ m0,s51,25472
Mavv 212,38,108
Mavv 208,38,104
L_ v9,36,s65,128
Mavv 228,42,104
Mavv 224,42,100
L_ v9,36,s65,3200
S_ m0,s51,26368
Mavv 204,38,100
Mavv 200,38,96
L_ v9,36,s65,256
Mavv 220,42,96
s_cmp_ge_u32 s67, 256
s_cbranch_scc1 .Lstep0_2
s_sub_u32 s61, s61, 147456
.Lstep0_14:
Zvvv 76,92,116
Dv 38,%0,7168
Zvvv 80,92,120
Dv 42,v1,7168
Zvvv 84,92,124
Zvvv 88,92,128
Zvvv 92,92,132
W_ 2
Nvvv 96,30,136,56
Mvvv 96,34,156
Nvvv 100,30,140,60
s_nop 1
v_max3_f32 v10, v76, v80, v84
s_nop 0
v_max3_f32 v10, v10, v88, v92
Mvvv 100,34,160
v_cmp_lt_f32 vcc, 0x42000000, v10
s_cbranch_vccnz .Ltramp0_13
Nvvv 104,30,144,64
A_ v46,v46,v76
A_ v47,v47,v80
Mvvv 104,34,164
A_ v48,v48,v84
A_ v49,v49,v88
Nvvv 108,30,148,68
A_ v50,v50,v92
Mvvv 108,34,168
L_ v9,36,s65,3328
Nvvv 112,30,152,72
R_ v20,a240
Mvvv 112,34,172
W_ 0
v_xor_b32 v21, 64, v20
Mvvv 112,38,192
S_ s65,s60,0
Dv 30,%0,8192
Mvvv 112,42,212
S_ m0,s50,0
Dv 34,v1,8192
Mvvv 108,38,188
L_ v20,24,s65,0
Mvvv 108,42,208
S_ s65,s60,10240
Mvvv 104,38,184
S_ m0,s50,1024
Mvvv 104,42,204
L_ v21,24,s65,0
Mvvv 100,38,180
S_ s65,s60,20480
Mvvv 100,42,200
S_ m0,s50,2048
Mvvv 96,38,176
L_ v20,24,s65,0
Mvvv 96,42,196
S_ s65,s60,30720
W_ 0
Mvvv 96,30,216
S_ m0,s50,3072
Dv 38,%0,0
Mvvv 96,34,236
Dv 42,v1,0
Mvvv 100,30,220
L_ v21,24,s65,0
Mvvv 100,34,240
S_ s65,s60,40960
Mvvv 104,30,224
S_ m0,s50,4096
Mvvv 104,34,244
L_ v20,24,s65,0
Mvvv 108,30,228
Mvvv 108,34,248
E_ 96
Mvvv 112,30,232
E_ 97
Mvvv 112,34,252
W_ 0
E_ 98
Nvvv 76,38,136,56
Dv 30,%0,1024
Mvvv 76,42,156
Dv 34,v1,1024
Nvvv 80,38,140,60
E_ 99
Mvvv 80,42,160
C_ 99,98,99
C_ 98,96,97
Nvvv 84,38,144,64
E_ 100
Mvvv 84,42,164
E_ 101
Nvvv 88,38,148,68
E_ 102
Mvvv 88,42,168
E_ 103
Nvvv 92,38,152,72
C_ 103,102,103
C_ 102,100,101
Mvvv 92,42,172
W_ 0
E_ 104
Mvvv 92,30,192
Dv 38,%0,2048
Mvvv 92,34,212
Dv 42,v1,2048
Mvvv 88,30,188
E_ 105
Mvvv 88,34,208
E_ 106
Mvvv 84,30,184
E_ 107
Mvvv 84,34,204
C_ 107,106,107
C_ 106,104,105
Mvvv 80,30,180
s_waitcnt vmcnt(17) lgkmcnt(0)
s_barrier
s_cmp_eq_u32 s11, 2
s_cbranch_scc1 .Lxs14m
.Lxs14m_back:
Mvvv 80,34,200
E_ 108
Mvvv 76,30,176
E_ 109
Mvvv 76,34,196
E_ 110
Mvvv 76,38,216
s_nop 0
Tv 30,v2,12288
Tv 32,v2,18432
Mvvv 76,42,236
E_ 111
Mvvv 80,38,220
s_nop 0
Tv 34,v3,12288
Tv 36,v3,18432
Mvvv 80,42,240
C_ 111,110,111
C_ 110,108,109
Mvvv 84,38,224
E_ 112
Mvvv 84,42,244
E_ 113
Mvvv 88,38,228
E_ 114
Mvvv 88,42,248
E_ 115
Mvvv 92,38,232
C_ 115,114,115
C_ 114,112,113
Mvvv 92,42,252
W_ 0
E_ 76
Mavv 0,30,116
s_nop 0
Tv 38,v4,12288
Tv 40,v4,18432
Mavv 20,34,116
E_ 77
Mavv 24,34,120
s_nop 0
Tv 42,v5,12288
Tv 44,v5,18432
Mavv 4,30,120
E_ 78
Mavv 8,30,124
E_ 79
Mavv 28,34,124
C_ 96,76,77
C_ 97,78,79
Mavv 32,34,128
E_ 80
Mavv 12,30,128
E_ 81
Mavv 16,30,132
E_ 82
Mavv 36,34,132
W_ 0
E_ 83
Mavv 56,38,132
s_nop 0
Tv 30,v2,13312
Tv 32,v2,19456
Mavv 76,42,132
C_ 100,80,81
C_ 101,82,83
Mavv 72,42,128
s_nop 0
Tv 34,v3,13312
Tv 36,v3,19456
Mavv 52,38,128
E_ 84
Mavv 48,38,124
E_ 85
Mavv 68,42,124
E_ 86
Mavv 64,42,120
E_ 87
Mavv 44,38,120
C_ 104,84,85
C_ 105,86,87
Mavv 40,38,116
E_ 88
Mavv 60,42,116
W_ 0
E_ 89
Mavv 80,30,116
s_nop 0
Tv 38,v4,13312
Tv 40,v4,19456
Mavv 100,34,116
E_ 90
Mavv 104,34,120
s_nop 0
Tv 42,v5,13312
Tv 44,v5,19456
Mavv 84,30,120
E_ 91
Mavv 88,30,124
C_ 108,88,89
C_ 109,90,91
Mavv 108,34,124
E_ 92
Mavv 112,34,128
E_ 93
Mavv 92,30,128
E_ 94
Mavv 96,30,132
E_ 95
Mavv 116,34,132
W_ 0
C_ 112,92,93
C_ 113,94,95
Mavv 136,38,132
s_nop 0
Tv 30,v2,14336
Tv 32,v2,20480
Mavv 156,42,132
v_mov_b32 v92, 0x3f803f80
v_mov_b32 v93, 0x3f803f80
Mavv 152,42,128
s_nop 0
Tv 34,v3,14336
Tv 36,v3,20480
Mavv 132,38,128
v_mov_b32 v94, 0x3f803f80
v_mov_b32 v95, 0x3f803f80
Mavv 128,38,124
Mavv 148,42,124
Mavv 144,42,120
Mavv 124,38,120
Mavv 120,38,116
Mavv 140,42,116
W_ 0
Mavv 160,30,116
s_nop 0
Tv 38,v4,14336
Tv 40,v4,20480
Mavv 180,34,116
Mavv 184,34,120
s_nop 0
Tv 42,v5,14336
Tv 44,v5,20480
Mavv 164,30,120
Mavv 168,30,124
Mavv 188,34,124
Mavv 192,34,128
)A"
R"A(Mavv 172,30,128
s_waitcnt vmcnt(11) lgkmcnt(0)
s_barrier
s_cmp_eq_u32 s11, 2
s_cbranch_scc1 .Lxs14e
.Lxs14e_back:
Mavv 176,30,132
S_ s65,s59,0
Mavv 196,34,132
S_ m0,s52,8192
Mavv 216,38,132
Dv 30,%0,55296
Mavv 236,42,132
Dv 34,v1,55296
Mavv 232,42,128
L_ v6,28,s65,0
Mavv 212,38,128
S_ s65,s59,98304
Mavv 208,38,124
S_ m0,s52,9216
Mavv 228,42,124
L_ v6,28,s65,0
Mavv 224,42,120
S_ s65,s59,196608
Mavv 204,38,120
S_ m0,s52,10240
Mavv 200,38,116
L_ v6,28,s65,0
Mavv 220,42,116
S_ s65,s59,294912
.Lstep0_15:
Zvvv 76,92,96
Dv 38,%0,56320
Zvvv 80,92,100
Dv 42,v1,56320
Zvvv 84,92,104
Zvvv 88,92,108
Zvvv 92,92,112
W_ 2
Nvvv 116,30,136,56
Mvvv 116,34,156
Nvvv 120,30,140,60
s_nop 1
v_max3_f32 v10, v76, v80, v84
s_nop 0
v_max3_f32 v10, v10, v88, v92
Mvvv 120,34,160
v_cmp_lt_f32 vcc, 0x42000000, v10
s_cbranch_vccnz .Ltramp0_14
Nvvv 124,30,144,64
S_ m0,s52,11264
A_ v46,v46,v76
A_ v47,v47,v80
Mvvv 124,34,164
A_ v48,v48,v84
A_ v49,v49,v88
Nvvv 128,30,148,68
A_ v50,v50,v92
Mvvv 128,34,168
L_ v6,28,s65,0
Nvvv 132,30,152,72
S_ s65,s59,0
Mvvv 132,34,172
S_ m0,s52,16256
W_ 0
Mvvv 132,38,192
Dv 30,%0,57344
Mvvv 132,42,212
Dv 34,v1,57344
Mvvv 128,38,188
L_ v6,28,s65,128
Mvvv 128,42,208
S_ s65,s59,98304
Mvvv 124,38,184
S_ m0,s52,17280
Mvvv 124,42,204
L_ v6,28,s65,128
Mvvv 120,38,180
S_ s65,s59,196608
Mvvv 120,42,200
S_ m0,s52,18304
Mvvv 116,38,176
L_ v6,28,s65,128
Mvvv 116,42,196
S_ s65,s59,294912
W_ 0
Mvvv 116,30,216
S_ m0,s52,19328
Dv 38,%0,49152
Mvvv 116,34,236
Dv 42,v1,49152
Mvvv 120,30,220
L_ v6,28,s65,128
Mvvv 120,34,240
S_ s65,s59,786432
Mvvv 124,30,224
S_ m0,s52,0
Mvvv 124,34,244
L_ v6,28,s65,0
Mvvv 128,30,228
S_ s65,s59,884736
Mvvv 128,34,248
S_ m0,s52,1024
E_ 116
Mvvv 132,30,232
L_ v6,28,s65,0
Mvvv 132,34,252
S_ s65,s59,983040
E_ 117
W_ 1
Nvvv 76,38,136,56
S_ m0,s52,2048
Dv 30,%0,50176
W_ 1
Mvvv 76,42,156
Dv 34,v1,50176
Nvvv 80,38,140,60
L_ v6,28,s65,0
Mvvv 80,42,160
S_ s65,s59,1081344
E_ 118
Nvvv 84,38,144,64
S_ m0,s52,3072
E_ 119
Mvvv 84,42,164
L_ v6,28,s65,0
Nvvv 88,38,148,68
S_ s65,s60,128
C_ 119,118,119
C_ 118,116,117
Mvvv 88,42,168
S_ m0,s50,20480
E_ 120
Nvvv 92,38,152,72
L_ v20,24,s65,0
Mvvv 92,42,172
S_ s65,s60,10368
E_ 121
W_ 1
Mvvv 92,30,192
S_ m0,s50,21504
Dv 38,%0,51200
W_ 1
Mvvv 92,34,212
Dv 42,v1,51200
Mvvv 88,30,188
L_ v21,24,s65,0
Mvvv 88,34,208
S_ s65,s60,20608
E_ 122
Mvvv 84,30,184
S_ m0,s50,22528
E_ 123
Mvvv 84,34,204
L_ v20,24,s65,0
Mvvv 80,30,180
s_waitcnt vmcnt(20) lgkmcnt(0)
s_barrier
s_cmp_eq_u32 s11, 2
s_cbranch_scc1 .Lxs15m
.Lxs15m_back:
S_ s65,s60,30848
Mvvv 80,34,200
S_ m0,s50,23552
C_ 123,122,123
C_ 122,120,121
Mvvv 76,30,176
L_ v21,24,s65,0
Mvvv 76,34,196
S_ s65,s60,41088
E_ 124
Mvvv 76,38,216
S_ m0,s50,24576
Tv 30,v2,24576
Tv 32,v2,30720
Mvvv 76,42,236
L_ v20,24,s65,0
Mvvv 80,38,220
.Lpad_a0_2:
s_cbranch_execz .Lhop_a0_r
Tv 34,v3,24576
Tv 36,v3,30720
Mvvv 80,42,240
E_ 125
Mvvv 84,38,224
E_ 126
Mvvv 84,42,244
E_ 127
Mvvv 88,38,228
C_ 127,126,127
C_ 126,124,125
Mvvv 88,42,248
E_ 128
Mvvv 92,38,232
E_ 129
Mvvv 92,42,252
W_ 0
E_ 130
Mavv 0,30,96
s_nop 0
Tv 38,v4,24576
Tv 40,v4,30720
Mavv 20,34,96
E_ 131
Mavv 24,34,100
s_nop 0
Tv 42,v5,24576
Tv 44,v5,30720
Mavv 4,30,100
C_ 131,130,131
C_ 130,128,129
Mavv 8,30,104
E_ 132
Mavv 28,34,104
E_ 133
Mavv 32,34,108
E_ 134
Mavv 12,30,108
E_ 135
Mavv 16,30,112
C_ 135,134,135
C_ 134,132,133
Mavv 36,34,112
W_ 0
E_ 76
Mavv 56,38,112
s_nop 0
Tv 30,v2,25600
Tv 32,v2,31744
Mavv 76,42,112
E_ 77
Mavv 72,42,108
s_nop 0
Tv 34,v3,25600
Tv 36,v3,31744
Mavv 52,38,108
E_ 78
Mavv 48,38,104
E_ 79
Mavv 68,42,104
C_ 116,76,77
C_ 117,78,79
Mavv 64,42,100
E_ 80
Mavv 44,38,100
E_ 81
Mavv 40,38,96
E_ 82
Mavv 60,42,96
W_ 0
E_ 83
Mavv 80,30,96
s_nop 0
Tv 38,v4,25600
Tv 40,v4,31744
Mavv 100,34,96
C_ 120,80,81
C_ 121,82,83
Mavv 104,34,100
s_nop 0
Tv 42,v5,25600
Tv 44,v5,31744
Mavv 84,30,100
E_ 84
Mavv 88,30,104
E_ 85
Mavv 108,34,104
E_ 86
Mavv 112,34,108
E_ 87
Mavv 92,30,108
C_ 124,84,85
C_ 125,86,87
Mavv 96,30,112
E_ 88
Mavv 116,34,112
W_ 0
E_ 89
Mavv 136,38,112
s_nop 0
Tv 30,v2,26624
Tv 32,v2,32768
Mavv 156,42,112
E_ 90
Mavv 152,42,108
s_nop 0
Tv 34,v3,26624
Tv 36,v3,32768
Mavv 132,38,108
E_ 91
Mavv 128,38,104
C_ 128,88,89
C_ 129,90,91
Mavv 148,42,104
E_ 92
Mavv 144,42,100
E_ 93
Mavv 124,38,100
E_ 94
Mavv 120,38,96
E_ 95
Mavv 140,42,96
W_ 0
C_ 132,92,93
C_ 133,94,95
Mavv 160,30,96
s_nop 0
Tv 38,v4,26624
Tv 40,v4,32768
Mavv 180,34,96
v_mov_b32 v92, 0x3f803f80
v_mov_b32 v93, 0x3f803f80
Mavv 184,34,100
s_nop 0
Tv 42,v5,26624
Tv 44,v5,32768
Mavv 164,30,100
v_mov_b32 v94, 0x3f803f80
v_mov_b32 v95, 0x3f803f80
Mavv 168,30,104
Mavv 188,34,104
Mavv 192,34,108
Mavv 172,30,108
s_waitcnt vmcnt(63) lgkmcnt(0)
s_barrier
s_cmp_eq_u32 s11, 2
s_cbranch_scc1 .Lxs15e
.Lxs15e_back:
Mavv 176,30,112
S_ s65,s59,0
Mavv 196,34,112
S_ m0,s52,24320
Mavv 216,38,112
s_nop 0
Tv 30,v2,36864
Tv 32,v2,43008
Zvvv 76,92,116
L_ v6,28,s65,256
Zvvv 80,92,120
S_ s65,s59,98304
Tv 34,v3,36864
Tv 36,v3,43008
Zvvv 84,92,124
S_ m0,s52,25344
Zvvv 88,92,128
L_ v6,28,s65,256
Zvvv 92,92,132
S_ s65,s59,196608
Mavv 236,42,112
S_ m0,s52,26368
Mavv 232,42,108
L_ v6,28,s65,256
Mavv 212,38,108
S_ s65,s59,294912
Mavv 208,38,104
S_ m0,s52,27392
v_max3_f32 v10, v76, v80, v84
Mavv 228,42,104
v_max3_f32 v10, v10, v88, v92
v_cmp_lt_f32 vcc, 0x42000000, v10
Mavv 224,42,100
L_ v6,28,s65,256
Mavv 204,38,100
Mavv 200,38,96
Mavv 220,42,96
s_cbranch_vccnz .Ltramp0_15
.Ljoin0:
W_ 2
Mavv 0,30,116
s_mov_b32 s58, 1
Tv 38,v4,36864
Tv 40,v4,43008
W_ 2
Mavv 20,34,116
S_ s61,s61,393216
R_ v12,a248
s_mov_b32 s85, 4026531840
Mavv 24,34,120
s_nop 0
Tv 42,v5,36864
Tv 44,v5,43008
Mavv 4,30,120
A_ v46,v46,v76
A_ v47,v47,v80
Mavv 8,30,124
A_ v48,v48,v84
A_ v49,v49,v88
Mavv 28,34,124
A_ v50,v50,v92
Mavv 32,34,128
v_rcp_f32 v46, v46
Mavv 12,30,128
v_rcp_f32 v47, v47
Mavv 16,30,132
v_rcp_f32 v48, v48
Mavv 36,34,132
W_ 0
v_rcp_f32 v49, v49
Mavv 56,38,132
s_nop 0
Tv 30,v2,37888
Tv 32,v2,44032
Mavv 76,42,132
v_rcp_f32 v50, v50
Mavv 72,42,128
s_nop 0
Tv 34,v3,37888
Tv 36,v3,44032
Mavv 52,38,128
R_ v96,a0
R_ v97,a1
Mavv 48,38,124
R_ v98,a2
R_ v99,a3
Mavv 68,42,124
R_ v100,a20
R_ v101,a21
Mavv 64,42,120
R_ v102,a22
R_ v103,a23
Mavv 44,38,120
U_ v96,v96,v46
U_ v97,v97,v46
Mavv 40,38,116
U_ v98,v98,v46
U_ v99,v99,v46
Mavv 60,42,116
W_ 0
U_ v100,v100,v46
U_ v101,v101,v46
Mavv 80,30,116
s_nop 0
Tv 38,v4,37888
Tv 40,v4,44032
Mavv 100,34,116
U_ v102,v102,v46
U_ v103,v103,v46
Mavv 104,34,120
s_nop 0
Tv 42,v5,37888
Tv 44,v5,44032
Mavv 84,30,120
C_ 76,96,97
C_ 77,98,99
Mavv 88,30,124
C_ 78,100,101
C_ 79,102,103
Mavv 108,34,124
X_ a0,v76
X_ a1,v77
Mavv 112,34,128
X_ a2,v78
X_ a3,v79
Mavv 92,30,128
R_ v104,a4
R_ v105,a5
Mavv 96,30,132
R_ v106,a6
R_ v107,a7
Mavv 116,34,132
W_ 0
R_ v108,a24
R_ v109,a25
Mavv 136,38,132
s_nop 0
Tv 30,v2,38912
Tv 32,v2,45056
Mavv 156,42,132
R_ v110,a26
R_ v111,a27
Mavv 152,42,128
s_nop 0
Tv 34,v3,38912
Tv 36,v3,45056
Mavv 132,38,128
U_ v104,v104,v47
U_ v105,v105,v47
Mavv 128,38,124
U_ v106,v106,v47
U_ v107,v107,v47
Mavv 148,42,124
U_ v108,v108,v47
U_ v109,v109,v47
Mavv 144,42,120
U_ v110,v110,v47
U_ v111,v111,v47
Mavv 124,38,120
C_ 80,104,105
C_ 81,106,107
Mavv 120,38,116
C_ 82,108,109
C_ 83,110,111
Mavv 140,42,116
W_ 0
X_ a4,v80
X_ a5,v81
Mavv 160,30,116
s_nop 0
Tv 38,v4,38912
Tv 40,v4,45056
Mavv 180,34,116
X_ a6,v82
X_ a7,v83
Mavv 184,34,120
s_nop 0
Tv 42,v5,38912
Tv 44,v5,45056
Mavv 164,30,120
R_ v96,a8
R_ v97,a9
Mavv 168,30,124
R_ v98,a10
R_ v99,a11
Mavv 188,34,124
R_ v100,a28
R_ v101,a29
Mavv 192,34,128
R_ v102,a30
R_ v103,a31
Mavv 172,30,128
U_ v96,v96,v48
U_ v97,v97,v48
Mavv 176,30,132
U_ v98,v98,v48
U_ v99,v99,v48
Mavv 196,34,132
W_ 0
U_ v100,v100,v48
U_ v101,v101,v48
Mavv 216,38,132
U_ v102,v102,v48
U_ v103,v103,v48
Mavv 236,42,132
C_ 84,96,97
C_ 85,98,99
Mavv 232,42,128
C_ 86,100,101
C_ 87,102,103
Mavv 212,38,128
X_ a8,v84
X_ a9,v85
Mavv 208,38,124
X_ a10,v86
X_ a11,v87
Mavv 228,42,124
Mavv 224,42,120
Mavv 204,38,120
Mavv 200,38,116
Mavv 220,42,116
.p2align 3
R_ v104,a12
R_ v105,a13
R_ v106,a14
R_ v107,a15
R_ v108,a32
R_ v109,a33
R_ v110,a34
R_ v111,a35
v_pk_mul_f32 v[104:105], v[104:105], v[48:49] op_sel:[0,1]
v_pk_mul_f32 v[106:107], v[106:107], v[48:49] op_sel:[0,1]
v_pk_mul_f32 v[108:109], v[108:109], v[48:49] op_sel:[0,1]
v_pk_mul_f32 v[110:111], v[110:111], v[48:49] op_sel:[0,1]
C_ 88,104,105
C_ 89,106,107
C_ 90,108,109
C_ 91,110,111
X_ a12,v88
X_ a13,v89
X_ a14,v90
X_ a15,v91
.p2align 3
R_ v96,a16
R_ v97,a17
R_ v98,a18
R_ v99,a19
R_ v100,a36
R_ v101,a37
R_ v102,a38
R_ v103,a39
v_pk_mul_f32 v[96:97], v[96:97], v[50:51] op_sel_hi:[1,0]
v_pk_mul_f32 v[98:99], v[98:99], v[50:51] op_sel_hi:[1,0]
v_pk_mul_f32 v[100:101], v[100:101], v[50:51] op_sel_hi:[1,0]
v_pk_mul_f32 v[102:103], v[102:103], v[50:51] op_sel_hi:[1,0]
C_ 92,96,97
C_ 93,98,99
C_ 94,100,101
C_ 95,102,103
X_ a16,v92
X_ a17,v93
X_ a18,v94
X_ a19,v95
.p2align 3
R_ v104,a40
R_ v105,a41
R_ v106,a42
R_ v107,a43
R_ v108,a60
R_ v109,a61
R_ v110,a62
R_ v111,a63
v_pk_mul_f32 v[104:105], v[104:105], v[46:47] op_sel_hi:[1,0]
v_pk_mul_f32 v[106:107], v[106:107], v[46:47] op_sel_hi:[1,0]
v_pk_mul_f32 v[108:109], v[108:109], v[46:47] op_sel_hi:[1,0]
v_pk_mul_f32 v[110:111], v[110:111], v[46:47] op_sel_hi:[1,0]
C_ 76,104,105
C_ 77,106,107
C_ 78,108,109
C_ 79,110,111
X_ a40,v76
X_ a41,v77
X_ a42,v78
X_ a43,v79
.p2align 3
R_ v96,a44
R_ v97,a45
R_ v98,a46
R_ v99,a47
R_ v100,a64
R_ v101,a65
R_ v102,a66
R_ v103,a67
v_pk_mul_f32 v[96:97], v[96:97], v[46:47] op_sel:[0,1]
v_pk_mul_f32 v[98:99], v[98:99], v[46:47] op_sel:[0,1]
v_pk_mul_f32 v[100:101], v[100:101], v[46:47] op_sel:[0,1]
v_pk_mul_f32 v[102:103], v[102:103], v[46:47] op_sel:[0,1]
C_ 80,96,97
C_ 81,98,99
C_ 82,100,101
C_ 83,102,103
X_ a44,v80
X_ a45,v81
X_ a46,v82
X_ a47,v83
.p2align 3
R_ v104,a48
R_ v105,a49
R_ v106,a50
R_ v107,a51
R_ v108,a68
R_ v109,a69
R_ v110,a70
R_ v111,a71
v_pk_mul_f32 v[104:105], v[104:105], v[48:49] op_sel_hi:[1,0]
v_pk_mul_f32 v[106:107], v[106:107], v[48:49] op_sel_hi:[1,0]
v_pk_mul_f32 v[108:109], v[108:109], v[48:49] op_sel_hi:[1,0]
v_pk_mul_f32 v[110:111], v[110:111], v[48:49] op_sel_hi:[1,0]
C_ 84,104,105
C_ 85,106,107
C_ 86,108,109
C_ 87,110,111
X_ a48,v84
X_ a49,v85
X_ a50,v86
X_ a51,v87
.p2align 3
R_ v96,a52
R_ v97,a53
R_ v98,a54
R_ v99,a55
R_ v100,a72
R_ v101,a73
R_ v102,a74
R_ v103,a75
v_pk_mul_f32 v[96:97], v[96:97], v[48:49] op_sel:[0,1]
v_pk_mul_f32 v[98:99], v[98:99], v[48:49] op_sel:[0,1]
v_pk_mul_f32 v[100:101], v[100:101], v[48:49] op_sel:[0,1]
v_pk_mul_f32 v[102:103], v[102:103], v[48:49] op_sel:[0,1]
C_ 88,96,97
C_ 89,98,99
C_ 90,100,101
C_ 91,102,103
X_ a52,v88
X_ a53,v89
X_ a54,v90
X_ a55,v91
.p2align 3
R_ v104,a56
R_ v105,a57
R_ v106,a58
R_ v107,a59
R_ v108,a76
R_ v109,a77
R_ v110,a78
R_ v111,a79
v_pk_mul_f32 v[104:105], v[104:105], v[50:51] op_sel_hi:[1,0]
v_pk_mul_f32 v[106:107], v[106:107], v[50:51] op_sel_hi:[1,0]
v_pk_mul_f32 v[108:109], v[108:109], v[50:51] op_sel_hi:[1,0]
v_pk_mul_f32 v[110:111], v[110:111], v[50:51] op_sel_hi:[1,0]
C_ 92,104,105
C_ 93,106,107
C_ 94,108,109
C_ 95,110,111
X_ a56,v92
X_ a57,v93
X_ a58,v94
X_ a59,v95
.p2align 3
R_ v96,a80
R_ v97,a81
R_ v98,a82
R_ v99,a83
R_ v100,a100
R_ v101,a101
R_ v102,a102
R_ v103,a103
v_pk_mul_f32 v[96:97], v[96:97], v[46:47] op_sel_hi:[1,0]
v_pk_mul_f32 v[98:99], v[98:99], v[46:47] op_sel_hi:[1,0]
v_pk_mul_f32 v[100:101], v[100:101], v[46:47] op_sel_hi:[1,0]
v_pk_mul_f32 v[102:103], v[102:103], v[46:47] op_sel_hi:[1,0]
C_ 76,96,97
C_ 77,98,99
C_ 78,100,101
C_ 79,102,103
X_ a80,v76
X_ a81,v77
X_ a82,v78
X_ a83,v79
.p2align 3
R_ v104,a84
R_ v105,a85
R_ v106,a86
R_ v107,a87
R_ v108,a104
R_ v109,a105
R_ v110,a106
R_ v111,a107
v_pk_mul_f32 v[104:105], v[104:105], v[46:47] op_sel:[0,1]
v_pk_mul_f32 v[106:107], v[106:107], v[46:47] op_sel:[0,1]
v_pk_mul_f32 v[108:109], v[108:109], v[46:47] op_sel:[0,1]
v_pk_mul_f32 v[110:111], v[110:111], v[46:47] op_sel:[0,1]
C_ 80,104,105
C_ 81,106,107
C_ 82,108,109
C_ 83,110,111
X_ a84,v80
X_ a85,v81
X_ a86,v82
X_ a87,v83
.p2align 3
R_ v96,a88
R_ v97,a89
R_ v98,a90
R_ v99,a91
R_ v100,a108
R_ v101,a109
R_ v102,a110
R_ v103,a111
v_pk_mul_f32 v[96:97], v[96:97], v[48:49] op_sel_hi:[1,0]
v_pk_mul_f32 v[98:99], v[98:99], v[48:49] op_sel_hi:[1,0]
v_pk_mul_f32 v[100:101], v[100:101], v[48:49] op_sel_hi:[1,0]
v_pk_mul_f32 v[102:103], v[102:103], v[48:49] op_sel_hi:[1,0]
C_ 84,96,97
C_ 85,98,99
C_ 86,100,101
C_ 87,102,103
.Lpad_a0_3:
s_cbranch_execz .Lhop_a0_r
X_ a88,v84
X_ a89,v85
X_ a90,v86
X_ a91,v87
.p2align 3
R_ v104,a92
R_ v105,a93
R_ v106,a94
R_ v107,a95
R_ v108,a112
R_ v109,a113
R_ v110,a114
R_ v111,a115
v_pk_mul_f32 v[104:105], v[104:105], v[48:49] op_sel:[0,1]
v_pk_mul_f32 v[106:107], v[106:107], v[48:49] op_sel:[0,1]
v_pk_mul_f32 v[108:109], v[108:109], v[48:49] op_sel:[0,1]
v_pk_mul_f32 v[110:111], v[110:111], v[48:49] op_sel:[0,1]
C_ 88,104,105
C_ 89,106,107
C_ 90,108,109
C_ 91,110,111
X_ a92,v88
X_ a93,v89
X_ a94,v90
X_ a95,v91
.p2align 3
R_ v96,a96
R_ v97,a97
R_ v98,a98
R_ v99,a99
R_ v100,a116
R_ v101,a117
R_ v102,a118
R_ v103,a119
v_pk_mul_f32 v[96:97], v[96:97], v[50:51] op_sel_hi:[1,0]
v_pk_mul_f32 v[98:99], v[98:99], v[50:51] op_sel_hi:[1,0]
v_pk_mul_f32 v[100:101], v[100:101], v[50:51] op_sel_hi:[1,0]
v_pk_mul_f32 v[102:103], v[102:103], v[50:51] op_sel_hi:[1,0]
C_ 92,96,97
C_ 93,98,99
C_ 94,100,101
C_ 95,102,103
X_ a96,v92
X_ a97,v93
X_ a98,v94
X_ a99,v95
.p2align 3
R_ v104,a120
R_ v105,a121
R_ v106,a122
R_ v107,a123
R_ v108,a140
R_ v109,a141
R_ v110,a142
R_ v111,a143
v_pk_mul_f32 v[104:105], v[104:105], v[46:47] op_sel_hi:[1,0]
v_pk_mul_f32 v[106:107], v[106:107], v[46:47] op_sel_hi:[1,0]
v_pk_mul_f32 v[108:109], v[108:109], v[46:47] op_sel_hi:[1,0]
v_pk_mul_f32 v[110:111], v[110:111], v[46:47] op_sel_hi:[1,0]
C_ 76,104,105
C_ 77,106,107
C_ 78,108,109
C_ 79,110,111
X_ a120,v76
X_ a121,v77
X_ a122,v78
X_ a123,v79
.p2align 3
R_ v96,a124
R_ v97,a125
R_ v98,a126
R_ v99,a127
R_ v100,a144
R_ v101,a145
R_ v102,a146
R_ v103,a147
v_pk_mul_f32 v[96:97], v[96:97], v[46:47] op_sel:[0,1]
v_pk_mul_f32 v[98:99], v[98:99], v[46:47] op_sel:[0,1]
v_pk_mul_f32 v[100:101], v[100:101], v[46:47] op_sel:[0,1]
v_pk_mul_f32 v[102:103], v[102:103], v[46:47] op_sel:[0,1]
C_ 80,96,97
C_ 81,98,99
C_ 82,100,101
C_ 83,102,103
X_ a124,v80
X_ a125,v81
X_ a126,v82
X_ a127,v83
.p2align 3
R_ v104,a128
R_ v105,a129
R_ v106,a130
R_ v107,a131
R_ v108,a148
R_ v109,a149
R_ v110,a150
R_ v111,a151
v_pk_mul_f32 v[104:105], v[104:105], v[48:49] op_sel_hi:[1,0]
v_pk_mul_f32 v[106:107], v[106:107], v[48:49] op_sel_hi:[1,0]
v_pk_mul_f32 v[108:109], v[108:109], v[48:49] op_sel_hi:[1,0]
v_pk_mul_f32 v[110:111], v[110:111], v[48:49] op_sel_hi:[1,0]
C_ 84,104,105
C_ 85,106,107
C_ 86,108,109
C_ 87,110,111
X_ a128,v84
X_ a129,v85
X_ a130,v86
X_ a131,v87
.p2align 3
R_ v96,a132
R_ v97,a133
R_ v98,a134
R_ v99,a135
R_ v100,a152
R_ v101,a153
R_ v102,a154
R_ v103,a155
v_pk_mul_f32 v[96:97], v[96:97], v[48:49] op_sel:[0,1]
v_pk_mul_f32 v[98:99], v[98:99], v[48:49] op_sel:[0,1]
v_pk_mul_f32 v[100:101], v[100:101], v[48:49] op_sel:[0,1]
v_pk_mul_f32 v[102:103], v[102:103], v[48:49] op_sel:[0,1]
C_ 88,96,97
C_ 89,98,99
C_ 90,100,101
C_ 91,102,103
X_ a132,v88
X_ a133,v89
X_ a134,v90
X_ a135,v91
.p2align 3
R_ v104,a136
R_ v105,a137
R_ v106,a138
R_ v107,a139
R_ v108,a156
R_ v109,a157
R_ v110,a158
R_ v111,a159
v_pk_mul_f32 v[104:105], v[104:105], v[50:51] op_sel_hi:[1,0]
v_pk_mul_f32 v[106:107], v[106:107], v[50:51] op_sel_hi:[1,0]
v_pk_mul_f32 v[108:109], v[108:109], v[50:51] op_sel_hi:[1,0]
v_pk_mul_f32 v[110:111], v[110:111], v[50:51] op_sel_hi:[1,0]
C_ 92,104,105
C_ 93,106,107
C_ 94,108,109
C_ 95,110,111
X_ a136,v92
X_ a137,v93
X_ a138,v94
X_ a139,v95
.p2align 3
R_ v96,a160
R_ v97,a161
R_ v98,a162
R_ v99,a163
R_ v100,a180
R_ v101,a181
R_ v102,a182
R_ v103,a183
v_pk_mul_f32 v[96:97], v[96:97], v[46:47] op_sel_hi:[1,0]
v_pk_mul_f32 v[98:99], v[98:99], v[46:47] op_sel_hi:[1,0]
v_pk_mul_f32 v[100:101], v[100:101], v[46:47] op_sel_hi:[1,0]
v_pk_mul_f32 v[102:103], v[102:103], v[46:47] op_sel_hi:[1,0]
C_ 76,96,97
C_ 77,98,99
C_ 78,100,101
C_ 79,102,103
X_ a160,v76
X_ a161,v77
X_ a162,v78
X_ a163,v79
.p2align 3
R_ v104,a164
R_ v105,a165
R_ v106,a166
R_ v107,a167
R_ v108,a184
R_ v109,a185
R_ v110,a186
R_ v111,a187
v_pk_mul_f32 v[104:105], v[104:105], v[46:47] op_sel:[0,1]
v_pk_mul_f32 v[106:107], v[106:107], v[46:47] op_sel:[0,1]
v_pk_mul_f32 v[108:109], v[108:109], v[46:47] op_sel:[0,1]
v_pk_mul_f32 v[110:111], v[110:111], v[46:47] op_sel:[0,1]
C_ 80,104,105
C_ 81,106,107
C_ 82,108,109
C_ 83,110,111
X_ a164,v80
X_ a165,v81
X_ a166,v82
X_ a167,v83
.p2align 3
R_ v96,a168
R_ v97,a169
R_ v98,a170
R_ v99,a171
R_ v100,a188
R_ v101,a189
R_ v102,a190
R_ v103,a191
v_pk_mul_f32 v[96:97], v[96:97], v[48:49] op_sel_hi:[1,0]
v_pk_mul_f32 v[98:99], v[98:99], v[48:49] op_sel_hi:[1,0]
v_pk_mul_f32 v[100:101], v[100:101], v[48:49] op_sel_hi:[1,0]
v_pk_mul_f32 v[102:103], v[102:103], v[48:49] op_sel_hi:[1,0]
C_ 84,96,97
C_ 85,98,99
C_ 86,100,101
C_ 87,102,103
X_ a168,v84
X_ a169,v85
X_ a170,v86
X_ a171,v87
.p2align 3
R_ v104,a172
R_ v105,a173
R_ v106,a174
R_ v107,a175
R_ v108,a192
R_ v109,a193
R_ v110,a194
R_ v111,a195
v_pk_mul_f32 v[104:105], v[104:105], v[48:49] op_sel:[0,1]
v_pk_mul_f32 v[106:107], v[106:107], v[48:49] op_sel:[0,1]
v_pk_mul_f32 v[108:109], v[108:109], v[48:49] op_sel:[0,1]
v_pk_mul_f32 v[110:111], v[110:111], v[48:49] op_sel:[0,1]
C_ 88,104,105
C_ 89,106,107
C_ 90,108,109
C_ 91,110,111
X_ a172,v88
X_ a173,v89
X_ a174,v90
X_ a175,v91
.p2align 3
R_ v96,a176
R_ v97,a177
R_ v98,a178
R_ v99,a179
R_ v100,a196
R_ v101,a197
R_ v102,a198
R_ v103,a199
v_pk_mul_f32 v[96:97], v[96:97], v[50:51] op_sel_hi:[1,0]
v_pk_mul_f32 v[98:99], v[98:99], v[50:51] op_sel_hi:[1,0]
v_pk_mul_f32 v[100:101], v[100:101], v[50:51] op_sel_hi:[1,0]
v_pk_mul_f32 v[102:103], v[102:103], v[50:51] op_sel_hi:[1,0]
C_ 92,96,97
C_ 93,98,99
C_ 94,100,101
C_ 95,102,103
X_ a176,v92
X_ a177,v93
X_ a178,v94
X_ a179,v95
.p2align 3
R_ v104,a200
R_ v105,a201
R_ v106,a202
R_ v107,a203
R_ v108,a220
R_ v109,a221
R_ v110,a222
R_ v111,a223
v_pk_mul_f32 v[104:105], v[104:105], v[46:47] op_sel_hi:[1,0]
v_pk_mul_f32 v[106:107], v[106:107], v[46:47] op_sel_hi:[1,0]
v_pk_mul_f32 v[108:109], v[108:109], v[46:47] op_sel_hi:[1,0]
v_pk_mul_f32 v[110:111], v[110:111], v[46:47] op_sel_hi:[1,0]
C_ 76,104,105
C_ 77,106,107
C_ 78,108,109
C_ 79,110,111
X_ a200,v76
X_ a201,v77
X_ a202,v78
X_ a203,v79
.p2align 3
R_ v96,a204
R_ v97,a205
R_ v98,a206
R_ v99,a207
R_ v100,a224
R_ v101,a225
R_ v102,a226
R_ v103,a227
v_pk_mul_f32 v[96:97], v[96:97], v[46:47] op_sel:[0,1]
v_pk_mul_f32 v[98:99], v[98:99], v[46:47] op_sel:[0,1]
v_pk_mul_f32 v[100:101], v[100:101], v[46:47] op_sel:[0,1]
v_pk_mul_f32 v[102:103], v[102:103], v[46:47] op_sel:[0,1]
C_ 80,96,97
C_ 81,98,99
C_ 82,100,101
C_ 83,102,103
X_ a204,v80
X_ a205,v81
X_ a206,v82
X_ a207,v83
v_mov_b32 v4, v20
v_mov_b32 v5, v21
R_ %0,a242
R_ v2,a243
v_xor_b32 v1, 64, %0
v_xor_b32 v3, 64, v2
s_waitcnt vmcnt(0) lgkmcnt(0)
s_barrier
Ta 20,%0,8192
Ta 22,%0,8704
Ta 24,%0,12288
Ta 26,%0,12800
Da 60,v2,0
Da 100,v3,0
Da 64,v2,2048
Da 104,v3,2048
Da 68,v2,4096
Da 108,v3,4096
Da 72,v2,6144
Da 112,v3,6144
Da 76,v2,8192
Da 116,v3,8192
Ta 28,%0,8200
Ta 30,%0,8712
Ta 32,%0,12296
Ta 34,%0,12808
.p2align 3
R_ v104,a208
R_ v105,a209
R_ v106,a210
R_ v107,a211
R_ v108,a228
R_ v109,a229
R_ v110,a230
R_ v111,a231
v_pk_mul_f32 v[104:105], v[104:105], v[48:49] op_sel_hi:[1,0]
v_pk_mul_f32 v[106:107], v[106:107], v[48:49] op_sel_hi:[1,0]
v_pk_mul_f32 v[108:109], v[108:109], v[48:49] op_sel_hi:[1,0]
v_pk_mul_f32 v[110:111], v[110:111], v[48:49] op_sel_hi:[1,0]
C_ 84,104,105
C_ 85,106,107
C_ 86,108,109
C_ 87,110,111
X_ a208,v84
X_ a209,v85
X_ a210,v86
X_ a211,v87
.p2align 3
R_ v96,a212
R_ v97,a213
R_ v98,a214
R_ v99,a215
R_ v100,a232
R_ v101,a233
R_ v102,a234
R_ v103,a235
v_pk_mul_f32 v[96:97], v[96:97], v[48:49] op_sel:[0,1]
v_pk_mul_f32 v[98:99], v[98:99], v[48:49] op_sel:[0,1]
v_pk_mul_f32 v[100:101], v[100:101], v[48:49] op_sel:[0,1]
v_pk_mul_f32 v[102:103], v[102:103], v[48:49] op_sel:[0,1]
C_ 88,96,97
C_ 89,98,99
C_ 90,100,101
C_ 91,102,103
X_ a212,v88
X_ a213,v89
X_ a214,v90
X_ a215,v91
.p2align 3
R_ v104,a216
R_ v105,a217
R_ v106,a218
R_ v107,a219
R_ v108,a236
R_ v109,a237
R_ v110,a238
R_ v111,a239
v_pk_mul_f32 v[104:105], v[104:105], v[50:51] op_sel_hi:[1,0]
v_pk_mul_f32 v[106:107], v[106:107], v[50:51] op_sel_hi:[1,0]
v_pk_mul_f32 v[108:109], v[108:109], v[50:51] op_sel_hi:[1,0]
v_pk_mul_f32 v[110:111], v[110:111], v[50:51] op_sel_hi:[1,0]
C_ 92,104,105
C_ 93,106,107
C_ 94,108,109
C_ 95,110,111
X_ a216,v92
X_ a217,v93
X_ a218,v94
X_ a219,v95
s_branch .Lq
.Ltramp0_1:
s_mov_b32 s63, 1
s_branch .Lslow1
.Ltramp0_2:
s_mov_b32 s63, 2
s_branch .Lslow0
.Ltramp0_13:
s_mov_b32 s63, 13
s_branch .Lslow1
.Ltramp0_14:
s_mov_b32 s63, 14
s_branch .Lslow0
.Ltramp0_15:
s_mov_b32 s63, 15
s_branch .Lslow1
.Lstep0_16:
Zvvv 76,92,116
Zvvv 80,92,120
Zvvv 84,92,124
Zvvv 88,92,128
Zvvv 92,92,132
s_nop 5
v_max3_f32 v10, v76, v80, v84
s_nop 0
v_max3_f32 v10, v10, v88, v92
v_cmp_lt_f32 vcc, 0x42000000, v10
s_cbranch_vccnz .Ltramp0_15
s_branch .Ljoin0
.Ltramp1_1:
s_mov_b32 s63, 1
s_branch .Lslow1
.Ltramp1_2:
s_mov_b32 s63, 2
s_branch .Lslow0
.Ltramp1_3:
s_mov_b32 s63, 3
s_branch .Lslow1
.Ltramp1_4:
s_mov_b32 s63, 4
s_branch .Lslow0
.Ltramp1_5:
s_mov_b32 s63, 5
s_branch .Lslow1
.Ltramp1_6:
s_mov_b32 s63, 6
s_branch .Lslow0
.Ltramp1_7:
s_mov_b32 s63, 7
s_branch .Lslow1
.Ltramp1_8:
s_mov_b32 s63, 8
s_branch .Lslow0
.Ltramp1_9:
s_mov_b32 s63, 9
s_branch .Lslow1
.Ltramp1_10:
s_mov_b32 s63, 10
s_branch .Lslow0
.Ltramp1_11:
s_mov_b32 s63, 11
s_branch .Lslow1
.Ltramp1_12:
s_mov_b32 s63, 12
s_branch .Lslow0
.Ltramp1_13:
s_mov_b32 s63, 13
s_branch .Lslow1
.Ltramp1_14:
s_mov_b32 s63, 14
s_branch .Lslow0
.Ltramp1_15:
s_mov_b32 s63, 15
s_branch .Lslow1
.Lstep1_16:
Zvvv 76,92,116
Zvvv 80,92,120
Zvvv 84,92,124
Zvvv 88,92,128
Zvvv 92,92,132
s_nop 5
v_max3_f32 v10, v76, v80, v84
s_nop 0
v_max3_f32 v10, v10, v88, v92
v_cmp_lt_f32 vcc, 0x42000000, v10
s_cbranch_vccnz .Ltramp1_15
s_branch .Ljoin1
.Ltramp2_1:
s_mov_b32 s63, 1
s_branch .Lslow1
.Ltramp2_2:
s_mov_b32 s63, 2
s_branch .Lslow0
.Ltramp2_3:
s_mov_b32 s63, 3
s_branch .Lslow1
.Ltramp2_4:
s_mov_b32 s63, 4
s_branch .Lslow0
.Ltramp2_5:
s_mov_b32 s63, 5
s_branch .Lslow1
.Ltramp2_6:
s_mov_b32 s63, 6
s_branch .Lslow0
.Ltramp2_7:
s_mov_b32 s63, 7
s_branch .Lslow1
.Ltramp2_8:
s_mov_b32 s63, 8
s_branch .Lslow0
.Ltramp2_9:
s_mov_b32 s63, 9
s_branch .Lslow1
.Ltramp2_10:
s_mov_b32 s63, 10
s_branch .Lslow0
.Ltramp2_11:
s_mov_b32 s63, 11
s_branch .Lslow1
.Ltramp2_12:
s_mov_b32 s63, 12
s_branch .Lslow0
.Ltramp2_13:
s_mov_b32 s63, 13
s_branch .Lslow1
.Ltramp2_14:
s_mov_b32 s63, 14
s_branch .Lslow0
.Ltramp2_15:
s_mov_b32 s63, 15
s_branch .Lslow1
.Lstep2_16:
Zvvv 76,92,116
Zvvv 80,92,120
Zvvv 84,92,124
Zvvv 88,92,128
Zvvv 92,92,132
s_nop 5
v_max3_f32 v10, v76, v80, v84
s_nop 0
v_max3_f32 v10, v10, v88, v92
v_cmp_lt_f32 vcc, 0x42000000, v10
s_cbranch_vccnz .Ltramp2_15
s_branch .Ljoin2
.Lslow0:
W_ 0
s_nop 7
s_nop 7
s_and_b32 s64, s61, 4294959104
s_mul_i32 s53, s63, 12288
S_ s64,s64,s53
R_ v11,a248
v_and_b32 v10, 0xf0, v11
v_mul_u32_u24 v10, 24, v10
v_lshrrev_b32 v11, 8, v11
v_lshl_add_u32 v10, v11, 4, v10
S_ s53,s64,6144
buffer_load_dwordx4 v[116:119], v10, s[32:35], s53 offen offset:0
S_ s53,s64,6144
buffer_load_dwordx4 v[120:123], v10, s[32:35], s53 offen offset:64
S_ s53,s64,6144
buffer_load_dwordx4 v[124:127], v10, s[32:35], s53 offen offset:128
S_ s53,s64,6144
buffer_load_dwordx4 v[128:131], v10, s[32:35], s53 offen offset:192
s_waitcnt vmcnt(0)
Nvvv 96,116,136,56
Nvvv 100,116,140,60
Nvvv 104,116,144,64
Nvvv 108,116,148,68
Nvvv 112,116,152,72
Mvvv 96,120,156
Mvvv 100,120,160
Mvvv 104,120,164
Mvvv 108,120,168
Mvvv 112,120,172
Mvvv 96,124,176
Mvvv 100,124,180
Mvvv 104,124,184
Mvvv 108,124,188
Mvvv 112,124,192
Mvvv 96,128,196
Mvvv 100,128,200
Mvvv 104,128,204
Mvvv 108,128,208
Mvvv 112,128,212
s_nop 7
S_ s53,s64,6144
buffer_load_dwordx4 v[116:119], v10, s[32:35], s53 offen offset:256
S_ s53,s64,6144
buffer_load_dwordx4 v[120:123], v10, s[32:35], s53 offen offset:320
S_ s53,s64,0
buffer_load_dwordx4 v[124:127], v10, s[32:35], s53 offen offset:0
S_ s53,s64,0
buffer_load_dwordx4 v[128:131], v10, s[32:35], s53 offen offset:64
s_waitcnt vmcnt(0)
Mvvv 96,116,216
Mvvv 100,116,220
Mvvv 104,116,224
Mvvv 108,116,228
Mvvv 112,116,232
Mvvv 96,120,236
Mvvv 100,120,240
Mvvv 104,120,244
Mvvv 108,120,248
Mvvv 112,120,252
Nvvv 76,124,136,56
Nvvv 80,124,140,60
Nvvv 84,124,144,64
Nvvv 88,124,148,68
Nvvv 92,124,152,72
Mvvv 76,128,156
Mvvv 80,128,160
Mvvv 84,128,164
Mvvv 88,128,168
Mvvv 92,128,172
s_nop 7
S_ s53,s64,0
buffer_load_dwordx4 v[116:119], v10, s[32:35], s53 offen offset:128
S_ s53,s64,0
buffer_load_dwordx4 v[120:123], v10, s[32:35], s53 offen offset:192
S_ s53,s64,0
buffer_load_dwordx4 v[124:127], v10, s[32:35], s53 offen offset:256
S_ s53,s64,0
buffer_load_dwordx4 v[128:131], v10, s[32:35], s53 offen offset:320
s_waitcnt vmcnt(0)
Mvvv 76,116,176
Mvvv 80,116,180
Mvvv 84,116,184
Mvvv 88,116,188
Mvvv 92,116,192
Mvvv 76,120,196
Mvvv 80,120,200
Mvvv 84,120,204
Mvvv 88,120,208
Mvvv 92,120,212
Mvvv 76,124,216
Mvvv 80,124,220
Mvvv 84,124,224
Mvvv 88,124,228
Mvvv 92,124,232
Mvvv 76,128,236
Mvvv 80,128,240
Mvvv 84,128,244
Mvvv 88,128,248
Mvvv 92,128,252
s_nop 7
s_nop 2
v_max3_f32 v10, v76, v77, v78
v_max3_f32 v10, v10, v79, v96
v_max3_f32 v10, v10, v97, v98
v_max_f32 v10, v10, v99
v_mov_b32 v11, v10
s_nop 1
v_permlane16_swap_b32 v10, v11
v_max_f32 v10, v10, v11
v_mov_b32 v11, v10
s_nop 1
v_permlane32_swap_b32 v10, v11
v_max_f32 v10, v10, v11
A_ v10,0x40e00000,v10
v_max_f32 v10, 0, v10
v_exp_f32 v11, -v10
s_nop 0
U_ v46,v46,v11
R_ v12,a0
U_ v12,v12,v11
X_ a0,v12
R_ v13,a1
U_ v13,v13,v11
X_ a1,v13
R_ v12,a2
U_ v12,v12,v11
X_ a2,v12
R_ v13,a3
U_ v13,v13,v11
X_ a3,v13
R_ v12,a20
U_ v12,v12,v11
X_ a20,v12
R_ v13,a21
U_ v13,v13,v11
X_ a21,v13
R_ v12,a22
U_ v12,v12,v11
X_ a22,v12
R_ v13,a23
U_ v13,v13,v11
X_ a23,v13
R_ v12,a40
U_ v12,v12,v11
X_ a40,v12
R_ v13,a41
U_ v13,v13,v11
X_ a41,v13
R_ v12,a42
U_ v12,v12,v11
X_ a42,v12
R_ v13,a43
U_ v13,v13,v11
X_ a43,v13
R_ v12,a60
U_ v12,v12,v11
X_ a60,v12
R_ v13,a61
U_ v13,v13,v11
X_ a61,v13
R_ v12,a62
U_ v12,v12,v11
X_ a62,v12
R_ v13,a63
U_ v13,v13,v11
X_ a63,v13
R_ v12,a80
U_ v12,v12,v11
X_ a80,v12
R_ v13,a81
U_ v13,v13,v11
X_ a81,v13
R_ v12,a82
U_ v12,v12,v11
X_ a82,v12
R_ v13,a83
U_ v13,v13,v11
X_ a83,v13
R_ v12,a100
U_ v12,v12,v11
X_ a100,v12
R_ v13,a101
U_ v13,v13,v11
X_ a101,v13
R_ v12,a102
U_ v12,v12,v11
X_ a102,v12
R_ v13,a103
U_ v13,v13,v11
X_ a103,v13
R_ v12,a120
U_ v12,v12,v11
X_ a120,v12
R_ v13,a121
U_ v13,v13,v11
X_ a121,v13
R_ v12,a122
U_ v12,v12,v11
X_ a122,v12
R_ v13,a123
U_ v13,v13,v11
X_ a123,v13
R_ v12,a140
U_ v12,v12,v11
X_ a140,v12
R_ v13,a141
U_ v13,v13,v11
X_ a141,v13
R_ v12,a142
U_ v12,v12,v11
X_ a142,v12
R_ v13,a143
U_ v13,v13,v11
X_ a143,v13
R_ v12,a160
U_ v12,v12,v11
X_ a160,v12
R_ v13,a161
U_ v13,v13,v11
X_ a161,v13
R_ v12,a162
U_ v12,v12,v11
X_ a162,v12
R_ v13,a163
U_ v13,v13,v11
X_ a163,v13
R_ v12,a180
U_ v12,v12,v11
X_ a180,v12
R_ v13,a181
U_ v13,v13,v11
X_ a181,v13
R_ v12,a182
U_ v12,v12,v11
X_ a182,v12
R_ v13,a183
U_ v13,v13,v11
X_ a183,v13
R_ v12,a200
U_ v12,v12,v11
X_ a200,v12
R_ v13,a201
U_ v13,v13,v11
X_ a201,v13
R_ v12,a202
U_ v12,v12,v11
X_ a202,v12
R_ v13,a203
U_ v13,v13,v11
X_ a203,v13
R_ v12,a220
U_ v12,v12,v11
X_ a220,v12
R_ v13,a221
U_ v13,v13,v11
X_ a221,v13
R_ v12,a222
U_ v12,v12,v11
X_ a222,v12
R_ v13,a223
U_ v13,v13,v11
X_ a223,v13
B_ v56,v56,v10
B_ v57,v57,v10
B_ v58,v58,v10
B_ v59,v59,v10
B_ v76,v76,v10
B_ v77,v77,v10
B_ v78,v78,v10
B_ v79,v79,v10
B_ v96,v96,v10
B_ v97,v97,v10
B_ v98,v98,v10
B_ v99,v99,v10
E_ 76
E_ 77
E_ 78
E_ 79
E_ 96
E_ 97
E_ 98
E_ 99
s_nop 0
C_ 99,98,99
C_ 98,96,97
C_ 96,76,77
C_ 97,78,79
v_max3_f32 v10, v80, v81, v82
v_max3_f32 v10, v10, v83, v100
v_max3_f32 v10, v10, v101, v102
v_max_f32 v10, v10, v103
v_mov_b32 v11, v10
s_nop 1
v_permlane16_swap_b32 v10, v11
v_max_f32 v10, v10, v11
v_mov_b32 v11, v10
s_nop 1
v_permlane32_swap_b32 v10, v11
v_max_f32 v10, v10, v11
A_ v10,0x40e00000,v10
v_max_f32 v10, 0, v10
v_exp_f32 v11, -v10
s_nop 0
U_ v47,v47,v11
R_ v12,a4
U_ v12,v12,v11
X_ a4,v12
R_ v13,a5
U_ v13,v13,v11
X_ a5,v13
R_ v12,a6
U_ v12,v12,v11
X_ a6,v12
R_ v13,a7
U_ v13,v13,v11
X_ a7,v13
R_ v12,a24
U_ v12,v12,v11
X_ a24,v12
R_ v13,a25
U_ v13,v13,v11
X_ a25,v13
R_ v12,a26
U_ v12,v12,v11
X_ a26,v12
R_ v13,a27
U_ v13,v13,v11
X_ a27,v13
R_ v12,a44
U_ v12,v12,v11
X_ a44,v12
R_ v13,a45
U_ v13,v13,v11
X_ a45,v13
R_ v12,a46
U_ v12,v12,v11
X_ a46,v12
R_ v13,a47
U_ v13,v13,v11
X_ a47,v13
R_ v12,a64
U_ v12,v12,v11
X_ a64,v12
R_ v13,a65
U_ v13,v13,v11
X_ a65,v13
R_ v12,a66
U_ v12,v12,v11
X_ a66,v12
R_ v13,a67
U_ v13,v13,v11
X_ a67,v13
R_ v12,a84
U_ v12,v12,v11
X_ a84,v12
R_ v13,a85
U_ v13,v13,v11
X_ a85,v13
R_ v12,a86
U_ v12,v12,v11
X_ a86,v12
R_ v13,a87
U_ v13,v13,v11
X_ a87,v13
R_ v12,a104
U_ v12,v12,v11
X_ a104,v12
R_ v13,a105
U_ v13,v13,v11
X_ a105,v13
R_ v12,a106
U_ v12,v12,v11
X_ a106,v12
R_ v13,a107
U_ v13,v13,v11
X_ a107,v13
R_ v12,a124
U_ v12,v12,v11
X_ a124,v12
R_ v13,a125
U_ v13,v13,v11
X_ a125,v13
R_ v12,a126
U_ v12,v12,v11
X_ a126,v12
R_ v13,a127
U_ v13,v13,v11
X_ a127,v13
R_ v12,a144
U_ v12,v12,v11
X_ a144,v12
R_ v13,a145
U_ v13,v13,v11
X_ a145,v13
R_ v12,a146
U_ v12,v12,v11
X_ a146,v12
R_ v13,a147
U_ v13,v13,v11
X_ a147,v13
R_ v12,a164
U_ v12,v12,v11
X_ a164,v12
R_ v13,a165
U_ v13,v13,v11
X_ a165,v13
R_ v12,a166
U_ v12,v12,v11
X_ a166,v12
R_ v13,a167
U_ v13,v13,v11
X_ a167,v13
R_ v12,a184
U_ v12,v12,v11
X_ a184,v12
R_ v13,a185
U_ v13,v13,v11
X_ a185,v13
R_ v12,a186
U_ v12,v12,v11
X_ a186,v12
R_ v13,a187
U_ v13,v13,v11
X_ a187,v13
R_ v12,a204
U_ v12,v12,v11
X_ a204,v12
R_ v13,a205
U_ v13,v13,v11
X_ a205,v13
R_ v12,a206
U_ v12,v12,v11
X_ a206,v12
R_ v13,a207
U_ v13,v13,v11
X_ a207,v13
R_ v12,a224
U_ v12,v12,v11
X_ a224,v12
R_ v13,a225
U_ v13,v13,v11
X_ a225,v13
R_ v12,a226
U_ v12,v12,v11
X_ a226,v12
R_ v13,a227
U_ v13,v13,v11
X_ a227,v13
B_ v60,v60,v10
B_ v61,v61,v10
B_ v62,v62,v10
B_ v63,v63,v10
B_ v80,v80,v10
B_ v81,v81,v10
B_ v82,v82,v10
B_ v83,v83,v10
B_ v100,v100,v10
B_ v101,v101,v10
B_ v102,v102,v10
B_ v103,v103,v10
E_ 80
E_ 81
E_ 82
E_ 83
E_ 100
E_ 101
E_ 102
E_ 103
s_nop 0
C_ 103,102,103
C_ 102,100,101
C_ 100,80,81
C_ 101,82,83
v_max3_f32 v10, v84, v85, v86
v_max3_f32 v10, v10, v87, v104
v_max3_f32 v10, v10, v105, v106
v_max_f32 v10, v10, v107
v_mov_b32 v11, v10
s_nop 1
v_permlane16_swap_b32 v10, v11
v_max_f32 v10, v10, v11
v_mov_b32 v11, v10
s_nop 1
v_permlane32_swap_b32 v10, v11
v_max_f32 v10, v10, v11
A_ v10,0x40e00000,v10
v_max_f32 v10, 0, v10
v_exp_f32 v11, -v10
s_nop 0
U_ v48,v48,v11
R_ v12,a8
U_ v12,v12,v11
X_ a8,v12
R_ v13,a9
U_ v13,v13,v11
X_ a9,v13
R_ v12,a10
U_ v12,v12,v11
X_ a10,v12
R_ v13,a11
U_ v13,v13,v11
X_ a11,v13
R_ v12,a28
U_ v12,v12,v11
X_ a28,v12
R_ v13,a29
U_ v13,v13,v11
X_ a29,v13
R_ v12,a30
U_ v12,v12,v11
X_ a30,v12
R_ v13,a31
U_ v13,v13,v11
X_ a31,v13
R_ v12,a48
U_ v12,v12,v11
X_ a48,v12
R_ v13,a49
U_ v13,v13,v11
X_ a49,v13
R_ v12,a50
U_ v12,v12,v11
X_ a50,v12
R_ v13,a51
U_ v13,v13,v11
X_ a51,v13
R_ v12,a68
U_ v12,v12,v11
X_ a68,v12
R_ v13,a69
U_ v13,v13,v11
X_ a69,v13
R_ v12,a70
U_ v12,v12,v11
X_ a70,v12
R_ v13,a71
U_ v13,v13,v11
X_ a71,v13
R_ v12,a88
U_ v12,v12,v11
X_ a88,v12
R_ v13,a89
U_ v13,v13,v11
X_ a89,v13
R_ v12,a90
U_ v12,v12,v11
X_ a90,v12
R_ v13,a91
U_ v13,v13,v11
X_ a91,v13
R_ v12,a108
U_ v12,v12,v11
X_ a108,v12
R_ v13,a109
U_ v13,v13,v11
X_ a109,v13
R_ v12,a110
U_ v12,v12,v11
X_ a110,v12
R_ v13,a111
U_ v13,v13,v11
X_ a111,v13
R_ v12,a128
U_ v12,v12,v11
X_ a128,v12
R_ v13,a129
U_ v13,v13,v11
X_ a129,v13
R_ v12,a130
U_ v12,v12,v11
X_ a130,v12
R_ v13,a131
U_ v13,v13,v11
X_ a131,v13
R_ v12,a148
U_ v12,v12,v11
X_ a148,v12
R_ v13,a149
U_ v13,v13,v11
X_ a149,v13
R_ v12,a150
U_ v12,v12,v11
X_ a150,v12
R_ v13,a151
U_ v13,v13,v11
X_ a151,v13
R_ v12,a168
U_ v12,v12,v11
X_ a168,v12
R_ v13,a169
U_ v13,v13,v11
X_ a169,v13
R_ v12,a170
U_ v12,v12,v11
X_ a170,v12
R_ v13,a171
U_ v13,v13,v11
X_ a171,v13
R_ v12,a188
U_ v12,v12,v11
X_ a188,v12
R_ v13,a189
U_ v13,v13,v11
X_ a189,v13
R_ v12,a190
U_ v12,v12,v11
X_ a190,v12
R_ v13,a191
U_ v13,v13,v11
X_ a191,v13
R_ v12,a208
U_ v12,v12,v11
X_ a208,v12
R_ v13,a209
U_ v13,v13,v11
X_ a209,v13
R_ v12,a210
U_ v12,v12,v11
X_ a210,v12
R_ v13,a211
U_ v13,v13,v11
X_ a211,v13
R_ v12,a228
U_ v12,v12,v11
X_ a228,v12
R_ v13,a229
U_ v13,v13,v11
X_ a229,v13
R_ v12,a230
U_ v12,v12,v11
X_ a230,v12
R_ v13,a231
U_ v13,v13,v11
X_ a231,v13
B_ v64,v64,v10
B_ v65,v65,v10
B_ v66,v66,v10
B_ v67,v67,v10
B_ v84,v84,v10
B_ v85,v85,v10
B_ v86,v86,v10
B_ v87,v87,v10
B_ v104,v104,v10
B_ v105,v105,v10
B_ v106,v106,v10
B_ v107,v107,v10
E_ 84
E_ 85
E_ 86
E_ 87
E_ 104
E_ 105
E_ 106
E_ 107
s_nop 0
C_ 107,106,107
C_ 106,104,105
C_ 104,84,85
C_ 105,86,87
v_max3_f32 v10, v88, v89, v90
v_max3_f32 v10, v10, v91, v108
v_max3_f32 v10, v10, v109, v110
v_max_f32 v10, v10, v111
v_mov_b32 v11, v10
s_nop 1
v_permlane16_swap_b32 v10, v11
v_max_f32 v10, v10, v11
v_mov_b32 v11, v10
s_nop 1
v_permlane32_swap_b32 v10, v11
v_max_f32 v10, v10, v11
A_ v10,0x40e00000,v10
v_max_f32 v10, 0, v10
v_exp_f32 v11, -v10
s_nop 0
U_ v49,v49,v11
R_ v12,a12
U_ v12,v12,v11
X_ a12,v12
R_ v13,a13
U_ v13,v13,v11
X_ a13,v13
R_ v12,a14
U_ v12,v12,v11
X_ a14,v12
R_ v13,a15
U_ v13,v13,v11
X_ a15,v13
R_ v12,a32
U_ v12,v12,v11
X_ a32,v12
R_ v13,a33
U_ v13,v13,v11
X_ a33,v13
R_ v12,a34
U_ v12,v12,v11
X_ a34,v12
R_ v13,a35
U_ v13,v13,v11
X_ a35,v13
R_ v12,a52
U_ v12,v12,v11
X_ a52,v12
R_ v13,a53
U_ v13,v13,v11
X_ a53,v13
R_ v12,a54
U_ v12,v12,v11
X_ a54,v12
)A"
R"A(R_ v13,a55
U_ v13,v13,v11
X_ a55,v13
R_ v12,a72
U_ v12,v12,v11
X_ a72,v12
R_ v13,a73
U_ v13,v13,v11
X_ a73,v13
R_ v12,a74
U_ v12,v12,v11
X_ a74,v12
R_ v13,a75
U_ v13,v13,v11
X_ a75,v13
R_ v12,a92
U_ v12,v12,v11
X_ a92,v12
R_ v13,a93
U_ v13,v13,v11
X_ a93,v13
R_ v12,a94
U_ v12,v12,v11
X_ a94,v12
R_ v13,a95
U_ v13,v13,v11
X_ a95,v13
R_ v12,a112
U_ v12,v12,v11
X_ a112,v12
R_ v13,a113
U_ v13,v13,v11
X_ a113,v13
R_ v12,a114
U_ v12,v12,v11
X_ a114,v12
R_ v13,a115
U_ v13,v13,v11
X_ a115,v13
R_ v12,a132
U_ v12,v12,v11
X_ a132,v12
R_ v13,a133
U_ v13,v13,v11
X_ a133,v13
R_ v12,a134
U_ v12,v12,v11
X_ a134,v12
R_ v13,a135
U_ v13,v13,v11
X_ a135,v13
R_ v12,a152
U_ v12,v12,v11
X_ a152,v12
R_ v13,a153
U_ v13,v13,v11
X_ a153,v13
R_ v12,a154
U_ v12,v12,v11
X_ a154,v12
R_ v13,a155
U_ v13,v13,v11
X_ a155,v13
R_ v12,a172
U_ v12,v12,v11
X_ a172,v12
R_ v13,a173
U_ v13,v13,v11
X_ a173,v13
R_ v12,a174
U_ v12,v12,v11
X_ a174,v12
R_ v13,a175
U_ v13,v13,v11
X_ a175,v13
R_ v12,a192
U_ v12,v12,v11
X_ a192,v12
R_ v13,a193
U_ v13,v13,v11
X_ a193,v13
R_ v12,a194
U_ v12,v12,v11
X_ a194,v12
R_ v13,a195
U_ v13,v13,v11
X_ a195,v13
R_ v12,a212
U_ v12,v12,v11
X_ a212,v12
R_ v13,a213
U_ v13,v13,v11
X_ a213,v13
R_ v12,a214
U_ v12,v12,v11
X_ a214,v12
R_ v13,a215
U_ v13,v13,v11
X_ a215,v13
R_ v12,a232
U_ v12,v12,v11
X_ a232,v12
R_ v13,a233
U_ v13,v13,v11
X_ a233,v13
R_ v12,a234
U_ v12,v12,v11
X_ a234,v12
R_ v13,a235
U_ v13,v13,v11
X_ a235,v13
B_ v68,v68,v10
B_ v69,v69,v10
B_ v70,v70,v10
B_ v71,v71,v10
B_ v88,v88,v10
B_ v89,v89,v10
B_ v90,v90,v10
B_ v91,v91,v10
B_ v108,v108,v10
B_ v109,v109,v10
B_ v110,v110,v10
B_ v111,v111,v10
E_ 88
E_ 89
E_ 90
E_ 91
E_ 108
E_ 109
E_ 110
E_ 111
s_nop 0
C_ 111,110,111
C_ 110,108,109
C_ 108,88,89
C_ 109,90,91
v_max3_f32 v10, v92, v93, v94
v_max3_f32 v10, v10, v95, v112
v_max3_f32 v10, v10, v113, v114
v_max_f32 v10, v10, v115
v_mov_b32 v11, v10
s_nop 1
v_permlane16_swap_b32 v10, v11
v_max_f32 v10, v10, v11
v_mov_b32 v11, v10
s_nop 1
v_permlane32_swap_b32 v10, v11
v_max_f32 v10, v10, v11
A_ v10,0x40e00000,v10
v_max_f32 v10, 0, v10
v_exp_f32 v11, -v10
s_nop 0
U_ v50,v50,v11
R_ v12,a16
U_ v12,v12,v11
X_ a16,v12
R_ v13,a17
U_ v13,v13,v11
X_ a17,v13
R_ v12,a18
U_ v12,v12,v11
X_ a18,v12
R_ v13,a19
U_ v13,v13,v11
X_ a19,v13
R_ v12,a36
U_ v12,v12,v11
X_ a36,v12
R_ v13,a37
U_ v13,v13,v11
X_ a37,v13
R_ v12,a38
U_ v12,v12,v11
X_ a38,v12
R_ v13,a39
U_ v13,v13,v11
X_ a39,v13
R_ v12,a56
U_ v12,v12,v11
X_ a56,v12
R_ v13,a57
U_ v13,v13,v11
X_ a57,v13
R_ v12,a58
U_ v12,v12,v11
X_ a58,v12
R_ v13,a59
U_ v13,v13,v11
X_ a59,v13
R_ v12,a76
U_ v12,v12,v11
X_ a76,v12
R_ v13,a77
U_ v13,v13,v11
X_ a77,v13
R_ v12,a78
U_ v12,v12,v11
X_ a78,v12
R_ v13,a79
U_ v13,v13,v11
X_ a79,v13
R_ v12,a96
U_ v12,v12,v11
X_ a96,v12
R_ v13,a97
U_ v13,v13,v11
X_ a97,v13
R_ v12,a98
U_ v12,v12,v11
X_ a98,v12
R_ v13,a99
U_ v13,v13,v11
X_ a99,v13
R_ v12,a116
U_ v12,v12,v11
X_ a116,v12
R_ v13,a117
U_ v13,v13,v11
X_ a117,v13
R_ v12,a118
U_ v12,v12,v11
X_ a118,v12
R_ v13,a119
U_ v13,v13,v11
X_ a119,v13
R_ v12,a136
U_ v12,v12,v11
X_ a136,v12
R_ v13,a137
U_ v13,v13,v11
X_ a137,v13
R_ v12,a138
U_ v12,v12,v11
X_ a138,v12
R_ v13,a139
U_ v13,v13,v11
X_ a139,v13
R_ v12,a156
U_ v12,v12,v11
X_ a156,v12
R_ v13,a157
U_ v13,v13,v11
X_ a157,v13
R_ v12,a158
U_ v12,v12,v11
X_ a158,v12
R_ v13,a159
U_ v13,v13,v11
X_ a159,v13
R_ v12,a176
U_ v12,v12,v11
X_ a176,v12
R_ v13,a177
U_ v13,v13,v11
X_ a177,v13
R_ v12,a178
U_ v12,v12,v11
X_ a178,v12
R_ v13,a179
U_ v13,v13,v11
X_ a179,v13
R_ v12,a196
U_ v12,v12,v11
X_ a196,v12
R_ v13,a197
U_ v13,v13,v11
X_ a197,v13
R_ v12,a198
U_ v12,v12,v11
X_ a198,v12
R_ v13,a199
U_ v13,v13,v11
X_ a199,v13
R_ v12,a216
U_ v12,v12,v11
X_ a216,v12
R_ v13,a217
U_ v13,v13,v11
X_ a217,v13
R_ v12,a218
U_ v12,v12,v11
X_ a218,v12
R_ v13,a219
U_ v13,v13,v11
X_ a219,v13
R_ v12,a236
U_ v12,v12,v11
X_ a236,v12
R_ v13,a237
U_ v13,v13,v11
X_ a237,v13
R_ v12,a238
U_ v12,v12,v11
X_ a238,v12
R_ v13,a239
U_ v13,v13,v11
X_ a239,v13
B_ v72,v72,v10
B_ v73,v73,v10
B_ v74,v74,v10
B_ v75,v75,v10
B_ v92,v92,v10
B_ v93,v93,v10
B_ v94,v94,v10
B_ v95,v95,v10
B_ v112,v112,v10
B_ v113,v113,v10
B_ v114,v114,v10
B_ v115,v115,v10
E_ 92
E_ 93
E_ 94
E_ 95
E_ 112
E_ 113
E_ 114
E_ 115
s_nop 0
C_ 115,114,115
C_ 114,112,113
C_ 112,92,93
C_ 113,94,95
v_mov_b32 v92, 0x3f803f80
v_mov_b32 v93, 0x3f803f80
v_mov_b32 v94, 0x3f803f80
v_mov_b32 v95, 0x3f803f80
s_nop 7
s_lshl_b32 s53, s58, 4
S_ s53,s53,s63
s_cmp_eq_u32 s53, 2
s_cbranch_scc1 .Lstep0_3
s_cmp_eq_u32 s53, 14
s_cbranch_scc1 .Lstep0_15
s_cmp_eq_u32 s53, 18
s_cbranch_scc1 .Lstep1_3
s_cmp_eq_u32 s53, 20
s_cbranch_scc1 .Lstep1_5
s_cmp_eq_u32 s53, 22
s_cbranch_scc1 .Lstep1_7
s_cmp_eq_u32 s53, 24
s_cbranch_scc1 .Lstep1_9
s_cmp_eq_u32 s53, 26
s_cbranch_scc1 .Lstep1_11
s_cmp_eq_u32 s53, 28
s_cbranch_scc1 .Lstep1_13
s_cmp_eq_u32 s53, 30
s_cbranch_scc1 .Lstep1_15
s_cmp_eq_u32 s53, 34
s_cbranch_scc1 .Lstep2_3
s_cmp_eq_u32 s53, 36
s_cbranch_scc1 .Lstep2_5
s_cmp_eq_u32 s53, 38
s_cbranch_scc1 .Lstep2_7
s_cmp_eq_u32 s53, 40
s_cbranch_scc1 .Lstep2_9
s_cmp_eq_u32 s53, 42
s_cbranch_scc1 .Lstep2_11
s_cmp_eq_u32 s53, 44
s_cbranch_scc1 .Lstep2_13
s_cmp_eq_u32 s53, 46
s_cbranch_scc1 .Lstep2_15
s_endpgm
.Lslow1:
W_ 0
s_nop 7
s_nop 7
s_and_b32 s64, s61, 4294959104
s_mul_i32 s53, s63, 12288
S_ s64,s64,s53
R_ v11,a248
v_and_b32 v10, 0xf0, v11
v_mul_u32_u24 v10, 24, v10
v_lshrrev_b32 v11, 8, v11
v_lshl_add_u32 v10, v11, 4, v10
S_ s53,s64,6144
buffer_load_dwordx4 v[96:99], v10, s[32:35], s53 offen offset:0
S_ s53,s64,6144
buffer_load_dwordx4 v[100:103], v10, s[32:35], s53 offen offset:64
S_ s53,s64,6144
buffer_load_dwordx4 v[104:107], v10, s[32:35], s53 offen offset:128
S_ s53,s64,6144
buffer_load_dwordx4 v[108:111], v10, s[32:35], s53 offen offset:192
s_waitcnt vmcnt(0)
Nvvv 116,96,136,56
Nvvv 120,96,140,60
Nvvv 124,96,144,64
Nvvv 128,96,148,68
Nvvv 132,96,152,72
Mvvv 116,100,156
Mvvv 120,100,160
Mvvv 124,100,164
Mvvv 128,100,168
Mvvv 132,100,172
Mvvv 116,104,176
Mvvv 120,104,180
Mvvv 124,104,184
Mvvv 128,104,188
Mvvv 132,104,192
Mvvv 116,108,196
Mvvv 120,108,200
Mvvv 124,108,204
Mvvv 128,108,208
Mvvv 132,108,212
s_nop 7
S_ s53,s64,6144
buffer_load_dwordx4 v[96:99], v10, s[32:35], s53 offen offset:256
S_ s53,s64,6144
buffer_load_dwordx4 v[100:103], v10, s[32:35], s53 offen offset:320
S_ s53,s64,0
buffer_load_dwordx4 v[104:107], v10, s[32:35], s53 offen offset:0
S_ s53,s64,0
buffer_load_dwordx4 v[108:111], v10, s[32:35], s53 offen offset:64
s_waitcnt vmcnt(0)
Mvvv 116,96,216
Mvvv 120,96,220
Mvvv 124,96,224
Mvvv 128,96,228
Mvvv 132,96,232
Mvvv 116,100,236
Mvvv 120,100,240
Mvvv 124,100,244
Mvvv 128,100,248
Mvvv 132,100,252
Nvvv 76,104,136,56
Nvvv 80,104,140,60
Nvvv 84,104,144,64
Nvvv 88,104,148,68
Nvvv 92,104,152,72
Mvvv 76,108,156
Mvvv 80,108,160
Mvvv 84,108,164
Mvvv 88,108,168
Mvvv 92,108,172
s_nop 7
S_ s53,s64,0
buffer_load_dwordx4 v[96:99], v10, s[32:35], s53 offen offset:128
S_ s53,s64,0
buffer_load_dwordx4 v[100:103], v10, s[32:35], s53 offen offset:192
S_ s53,s64,0
buffer_load_dwordx4 v[104:107], v10, s[32:35], s53 offen offset:256
S_ s53,s64,0
buffer_load_dwordx4 v[108:111], v10, s[32:35], s53 offen offset:320
s_waitcnt vmcnt(0)
Mvvv 76,96,176
Mvvv 80,96,180
Mvvv 84,96,184
Mvvv 88,96,188
Mvvv 92,96,192
Mvvv 76,100,196
Mvvv 80,100,200
Mvvv 84,100,204
Mvvv 88,100,208
Mvvv 92,100,212
Mvvv 76,104,216
Mvvv 80,104,220
Mvvv 84,104,224
Mvvv 88,104,228
Mvvv 92,104,232
Mvvv 76,108,236
Mvvv 80,108,240
Mvvv 84,108,244
Mvvv 88,108,248
Mvvv 92,108,252
s_nop 7
s_nop 2
v_max3_f32 v10, v76, v77, v78
v_max3_f32 v10, v10, v79, v116
v_max3_f32 v10, v10, v117, v118
v_max_f32 v10, v10, v119
v_mov_b32 v11, v10
s_nop 1
v_permlane16_swap_b32 v10, v11
v_max_f32 v10, v10, v11
v_mov_b32 v11, v10
s_nop 1
v_permlane32_swap_b32 v10, v11
v_max_f32 v10, v10, v11
A_ v10,0x40e00000,v10
v_max_f32 v10, 0, v10
v_exp_f32 v11, -v10
s_nop 0
U_ v46,v46,v11
R_ v12,a0
U_ v12,v12,v11
X_ a0,v12
R_ v13,a1
U_ v13,v13,v11
X_ a1,v13
R_ v12,a2
U_ v12,v12,v11
X_ a2,v12
R_ v13,a3
U_ v13,v13,v11
X_ a3,v13
R_ v12,a20
U_ v12,v12,v11
X_ a20,v12
R_ v13,a21
U_ v13,v13,v11
X_ a21,v13
R_ v12,a22
U_ v12,v12,v11
X_ a22,v12
R_ v13,a23
U_ v13,v13,v11
X_ a23,v13
R_ v12,a40
U_ v12,v12,v11
X_ a40,v12
R_ v13,a41
U_ v13,v13,v11
X_ a41,v13
R_ v12,a42
U_ v12,v12,v11
X_ a42,v12
R_ v13,a43
U_ v13,v13,v11
X_ a43,v13
R_ v12,a60
U_ v12,v12,v11
X_ a60,v12
R_ v13,a61
U_ v13,v13,v11
X_ a61,v13
R_ v12,a62
U_ v12,v12,v11
X_ a62,v12
R_ v13,a63
U_ v13,v13,v11
X_ a63,v13
R_ v12,a80
U_ v12,v12,v11
X_ a80,v12
R_ v13,a81
U_ v13,v13,v11
X_ a81,v13
R_ v12,a82
U_ v12,v12,v11
X_ a82,v12
R_ v13,a83
U_ v13,v13,v11
X_ a83,v13
R_ v12,a100
U_ v12,v12,v11
X_ a100,v12
R_ v13,a101
U_ v13,v13,v11
X_ a101,v13
R_ v12,a102
U_ v12,v12,v11
X_ a102,v12
R_ v13,a103
U_ v13,v13,v11
X_ a103,v13
R_ v12,a120
U_ v12,v12,v11
X_ a120,v12
R_ v13,a121
U_ v13,v13,v11
X_ a121,v13
R_ v12,a122
U_ v12,v12,v11
X_ a122,v12
R_ v13,a123
U_ v13,v13,v11
X_ a123,v13
R_ v12,a140
U_ v12,v12,v11
X_ a140,v12
R_ v13,a141
U_ v13,v13,v11
X_ a141,v13
R_ v12,a142
U_ v12,v12,v11
X_ a142,v12
R_ v13,a143
U_ v13,v13,v11
X_ a143,v13
R_ v12,a160
U_ v12,v12,v11
X_ a160,v12
R_ v13,a161
U_ v13,v13,v11
X_ a161,v13
R_ v12,a162
U_ v12,v12,v11
X_ a162,v12
R_ v13,a163
U_ v13,v13,v11
X_ a163,v13
R_ v12,a180
U_ v12,v12,v11
X_ a180,v12
R_ v13,a181
U_ v13,v13,v11
X_ a181,v13
R_ v12,a182
U_ v12,v12,v11
X_ a182,v12
R_ v13,a183
U_ v13,v13,v11
X_ a183,v13
R_ v12,a200
U_ v12,v12,v11
X_ a200,v12
R_ v13,a201
U_ v13,v13,v11
X_ a201,v13
R_ v12,a202
U_ v12,v12,v11
X_ a202,v12
R_ v13,a203
U_ v13,v13,v11
X_ a203,v13
R_ v12,a220
U_ v12,v12,v11
X_ a220,v12
R_ v13,a221
U_ v13,v13,v11
X_ a221,v13
R_ v12,a222
U_ v12,v12,v11
X_ a222,v12
R_ v13,a223
U_ v13,v13,v11
X_ a223,v13
B_ v56,v56,v10
B_ v57,v57,v10
B_ v58,v58,v10
B_ v59,v59,v10
B_ v76,v76,v10
B_ v77,v77,v10
B_ v78,v78,v10
B_ v79,v79,v10
B_ v116,v116,v10
B_ v117,v117,v10
B_ v118,v118,v10
B_ v119,v119,v10
E_ 76
E_ 77
E_ 78
E_ 79
E_ 116
E_ 117
E_ 118
E_ 119
s_nop 0
C_ 119,118,119
C_ 118,116,117
C_ 116,76,77
C_ 117,78,79
v_max3_f32 v10, v80, v81, v82
v_max3_f32 v10, v10, v83, v120
v_max3_f32 v10, v10, v121, v122
v_max_f32 v10, v10, v123
v_mov_b32 v11, v10
s_nop 1
v_permlane16_swap_b32 v10, v11
v_max_f32 v10, v10, v11
v_mov_b32 v11, v10
s_nop 1
v_permlane32_swap_b32 v10, v11
v_max_f32 v10, v10, v11
A_ v10,0x40e00000,v10
v_max_f32 v10, 0, v10
v_exp_f32 v11, -v10
s_nop 0
U_ v47,v47,v11
R_ v12,a4
U_ v12,v12,v11
X_ a4,v12
R_ v13,a5
U_ v13,v13,v11
X_ a5,v13
R_ v12,a6
U_ v12,v12,v11
X_ a6,v12
R_ v13,a7
U_ v13,v13,v11
X_ a7,v13
R_ v12,a24
U_ v12,v12,v11
X_ a24,v12
R_ v13,a25
U_ v13,v13,v11
X_ a25,v13
R_ v12,a26
U_ v12,v12,v11
X_ a26,v12
R_ v13,a27
U_ v13,v13,v11
X_ a27,v13
R_ v12,a44
U_ v12,v12,v11
X_ a44,v12
R_ v13,a45
U_ v13,v13,v11
X_ a45,v13
R_ v12,a46
U_ v12,v12,v11
X_ a46,v12
R_ v13,a47
U_ v13,v13,v11
X_ a47,v13
R_ v12,a64
U_ v12,v12,v11
X_ a64,v12
R_ v13,a65
U_ v13,v13,v11
X_ a65,v13
R_ v12,a66
U_ v12,v12,v11
X_ a66,v12
R_ v13,a67
U_ v13,v13,v11
X_ a67,v13
R_ v12,a84
U_ v12,v12,v11
X_ a84,v12
R_ v13,a85
U_ v13,v13,v11
X_ a85,v13
R_ v12,a86
U_ v12,v12,v11
X_ a86,v12
R_ v13,a87
U_ v13,v13,v11
X_ a87,v13
R_ v12,a104
U_ v12,v12,v11
X_ a104,v12
R_ v13,a105
U_ v13,v13,v11
X_ a105,v13
R_ v12,a106
U_ v12,v12,v11
X_ a106,v12
R_ v13,a107
U_ v13,v13,v11
X_ a107,v13
R_ v12,a124
U_ v12,v12,v11
X_ a124,v12
R_ v13,a125
U_ v13,v13,v11
X_ a125,v13
R_ v12,a126
U_ v12,v12,v11
X_ a126,v12
R_ v13,a127
U_ v13,v13,v11
X_ a127,v13
R_ v12,a144
U_ v12,v12,v11
X_ a144,v12
R_ v13,a145
U_ v13,v13,v11
X_ a145,v13
R_ v12,a146
U_ v12,v12,v11
X_ a146,v12
R_ v13,a147
U_ v13,v13,v11
X_ a147,v13
R_ v12,a164
U_ v12,v12,v11
X_ a164,v12
R_ v13,a165
U_ v13,v13,v11
X_ a165,v13
R_ v12,a166
U_ v12,v12,v11
X_ a166,v12
R_ v13,a167
U_ v13,v13,v11
X_ a167,v13
R_ v12,a184
U_ v12,v12,v11
X_ a184,v12
R_ v13,a185
U_ v13,v13,v11
X_ a185,v13
R_ v12,a186
U_ v12,v12,v11
X_ a186,v12
R_ v13,a187
U_ v13,v13,v11
X_ a187,v13
R_ v12,a204
U_ v12,v12,v11
X_ a204,v12
R_ v13,a205
U_ v13,v13,v11
X_ a205,v13
R_ v12,a206
U_ v12,v12,v11
X_ a206,v12
R_ v13,a207
U_ v13,v13,v11
X_ a207,v13
R_ v12,a224
U_ v12,v12,v11
X_ a224,v12
R_ v13,a225
U_ v13,v13,v11
X_ a225,v13
R_ v12,a226
U_ v12,v12,v11
X_ a226,v12
R_ v13,a227
U_ v13,v13,v11
X_ a227,v13
B_ v60,v60,v10
B_ v61,v61,v10
B_ v62,v62,v10
B_ v63,v63,v10
B_ v80,v80,v10
B_ v81,v81,v10
B_ v82,v82,v10
B_ v83,v83,v10
B_ v120,v120,v10
B_ v121,v121,v10
B_ v122,v122,v10
B_ v123,v123,v10
E_ 80
E_ 81
E_ 82
E_ 83
E_ 120
E_ 121
E_ 122
E_ 123
s_nop 0
C_ 123,122,123
C_ 122,120,121
C_ 120,80,81
C_ 121,82,83
v_max3_f32 v10, v84, v85, v86
v_max3_f32 v10, v10, v87, v124
v_max3_f32 v10, v10, v125, v126
v_max_f32 v10, v10, v127
v_mov_b32 v11, v10
s_nop 1
v_permlane16_swap_b32 v10, v11
v_max_f32 v10, v10, v11
v_mov_b32 v11, v10
s_nop 1
v_permlane32_swap_b32 v10, v11
v_max_f32 v10, v10, v11
A_ v10,0x40e00000,v10
v_max_f32 v10, 0, v10
v_exp_f32 v11, -v10
s_nop 0
U_ v48,v48,v11
R_ v12,a8
U_ v12,v12,v11
X_ a8,v12
R_ v13,a9
U_ v13,v13,v11
X_ a9,v13
R_ v12,a10
U_ v12,v12,v11
X_ a10,v12
R_ v13,a11
U_ v13,v13,v11
X_ a11,v13
R_ v12,a28
U_ v12,v12,v11
X_ a28,v12
R_ v13,a29
U_ v13,v13,v11
X_ a29,v13
R_ v12,a30
U_ v12,v12,v11
X_ a30,v12
R_ v13,a31
U_ v13,v13,v11
X_ a31,v13
R_ v12,a48
U_ v12,v12,v11
X_ a48,v12
R_ v13,a49
U_ v13,v13,v11
X_ a49,v13
R_ v12,a50
U_ v12,v12,v11
X_ a50,v12
R_ v13,a51
U_ v13,v13,v11
X_ a51,v13
R_ v12,a68
U_ v12,v12,v11
X_ a68,v12
R_ v13,a69
U_ v13,v13,v11
X_ a69,v13
R_ v12,a70
U_ v12,v12,v11
X_ a70,v12
R_ v13,a71
U_ v13,v13,v11
X_ a71,v13
R_ v12,a88
U_ v12,v12,v11
X_ a88,v12
R_ v13,a89
U_ v13,v13,v11
X_ a89,v13
R_ v12,a90
U_ v12,v12,v11
X_ a90,v12
R_ v13,a91
U_ v13,v13,v11
X_ a91,v13
R_ v12,a108
U_ v12,v12,v11
X_ a108,v12
R_ v13,a109
U_ v13,v13,v11
X_ a109,v13
R_ v12,a110
U_ v12,v12,v11
X_ a110,v12
R_ v13,a111
U_ v13,v13,v11
X_ a111,v13
R_ v12,a128
U_ v12,v12,v11
X_ a128,v12
R_ v13,a129
U_ v13,v13,v11
X_ a129,v13
R_ v12,a130
U_ v12,v12,v11
X_ a130,v12
R_ v13,a131
U_ v13,v13,v11
X_ a131,v13
R_ v12,a148
U_ v12,v12,v11
X_ a148,v12
R_ v13,a149
U_ v13,v13,v11
X_ a149,v13
R_ v12,a150
U_ v12,v12,v11
X_ a150,v12
R_ v13,a151
U_ v13,v13,v11
X_ a151,v13
R_ v12,a168
U_ v12,v12,v11
X_ a168,v12
R_ v13,a169
U_ v13,v13,v11
X_ a169,v13
R_ v12,a170
U_ v12,v12,v11
X_ a170,v12
R_ v13,a171
U_ v13,v13,v11
X_ a171,v13
R_ v12,a188
U_ v12,v12,v11
X_ a188,v12
R_ v13,a189
U_ v13,v13,v11
X_ a189,v13
R_ v12,a190
U_ v12,v12,v11
X_ a190,v12
R_ v13,a191
U_ v13,v13,v11
X_ a191,v13
R_ v12,a208
U_ v12,v12,v11
X_ a208,v12
R_ v13,a209
U_ v13,v13,v11
X_ a209,v13
R_ v12,a210
U_ v12,v12,v11
X_ a210,v12
R_ v13,a211
U_ v13,v13,v11
X_ a211,v13
R_ v12,a228
U_ v12,v12,v11
X_ a228,v12
R_ v13,a229
U_ v13,v13,v11
X_ a229,v13
R_ v12,a230
U_ v12,v12,v11
X_ a230,v12
R_ v13,a231
U_ v13,v13,v11
X_ a231,v13
B_ v64,v64,v10
B_ v65,v65,v10
B_ v66,v66,v10
B_ v67,v67,v10
B_ v84,v84,v10
B_ v85,v85,v10
B_ v86,v86,v10
B_ v87,v87,v10
B_ v124,v124,v10
B_ v125,v125,v10
B_ v126,v126,v10
B_ v127,v127,v10
E_ 84
E_ 85
E_ 86
E_ 87
E_ 124
E_ 125
E_ 126
E_ 127
s_nop 0
C_ 127,126,127
C_ 126,124,125
C_ 124,84,85
C_ 125,86,87
v_max3_f32 v10, v88, v89, v90
v_max3_f32 v10, v10, v91, v128
v_max3_f32 v10, v10, v129, v130
v_max_f32 v10, v10, v131
v_mov_b32 v11, v10
s_nop 1
v_permlane16_swap_b32 v10, v11
v_max_f32 v10, v10, v11
v_mov_b32 v11, v10
s_nop 1
v_permlane32_swap_b32 v10, v11
v_max_f32 v10, v10, v11
A_ v10,0x40e00000,v10
v_max_f32 v10, 0, v10
v_exp_f32 v11, -v10
s_nop 0
U_ v49,v49,v11
R_ v12,a12
U_ v12,v12,v11
X_ a12,v12
R_ v13,a13
U_ v13,v13,v11
X_ a13,v13
R_ v12,a14
U_ v12,v12,v11
X_ a14,v12
R_ v13,a15
U_ v13,v13,v11
X_ a15,v13
R_ v12,a32
U_ v12,v12,v11
X_ a32,v12
R_ v13,a33
U_ v13,v13,v11
X_ a33,v13
R_ v12,a34
U_ v12,v12,v11
X_ a34,v12
R_ v13,a35
U_ v13,v13,v11
X_ a35,v13
R_ v12,a52
U_ v12,v12,v11
X_ a52,v12
R_ v13,a53
U_ v13,v13,v11
X_ a53,v13
R_ v12,a54
U_ v12,v12,v11
X_ a54,v12
R_ v13,a55
U_ v13,v13,v11
X_ a55,v13
R_ v12,a72
U_ v12,v12,v11
X_ a72,v12
R_ v13,a73
U_ v13,v13,v11
X_ a73,v13
R_ v12,a74
U_ v12,v12,v11
X_ a74,v12
R_ v13,a75
U_ v13,v13,v11
X_ a75,v13
R_ v12,a92
U_ v12,v12,v11
X_ a92,v12
R_ v13,a93
U_ v13,v13,v11
X_ a93,v13
R_ v12,a94
U_ v12,v12,v11
X_ a94,v12
R_ v13,a95
U_ v13,v13,v11
X_ a95,v13
R_ v12,a112
U_ v12,v12,v11
X_ a112,v12
R_ v13,a113
U_ v13,v13,v11
X_ a113,v13
R_ v12,a114
U_ v12,v12,v11
X_ a114,v12
R_ v13,a115
U_ v13,v13,v11
X_ a115,v13
R_ v12,a132
U_ v12,v12,v11
X_ a132,v12
R_ v13,a133
U_ v13,v13,v11
X_ a133,v13
R_ v12,a134
U_ v12,v12,v11
X_ a134,v12
R_ v13,a135
U_ v13,v13,v11
X_ a135,v13
R_ v12,a152
U_ v12,v12,v11
X_ a152,v12
R_ v13,a153
U_ v13,v13,v11
X_ a153,v13
R_ v12,a154
U_ v12,v12,v11
X_ a154,v12
R_ v13,a155
U_ v13,v13,v11
X_ a155,v13
R_ v12,a172
U_ v12,v12,v11
X_ a172,v12
R_ v13,a173
U_ v13,v13,v11
X_ a173,v13
R_ v12,a174
U_ v12,v12,v11
X_ a174,v12
R_ v13,a175
U_ v13,v13,v11
X_ a175,v13
R_ v12,a192
U_ v12,v12,v11
X_ a192,v12
R_ v13,a193
U_ v13,v13,v11
X_ a193,v13
R_ v12,a194
U_ v12,v12,v11
X_ a194,v12
R_ v13,a195
U_ v13,v13,v11
X_ a195,v13
R_ v12,a212
U_ v12,v12,v11
X_ a212,v12
R_ v13,a213
U_ v13,v13,v11
X_ a213,v13
R_ v12,a214
U_ v12,v12,v11
X_ a214,v12
R_ v13,a215
U_ v13,v13,v11
X_ a215,v13
R_ v12,a232
U_ v12,v12,v11
X_ a232,v12
R_ v13,a233
U_ v13,v13,v11
X_ a233,v13
R_ v12,a234
U_ v12,v12,v11
X_ a234,v12
R_ v13,a235
U_ v13,v13,v11
X_ a235,v13
B_ v68,v68,v10
B_ v69,v69,v10
B_ v70,v70,v10
B_ v71,v71,v10
B_ v88,v88,v10
B_ v89,v89,v10
B_ v90,v90,v10
B_ v91,v91,v10
B_ v128,v128,v10
B_ v129,v129,v10
B_ v130,v130,v10
B_ v131,v131,v10
E_ 88
E_ 89
E_ 90
E_ 91
E_ 128
E_ 129
E_ 130
E_ 131
s_nop 0
C_ 131,130,131
C_ 130,128,129
C_ 128,88,89
C_ 129,90,91
v_max3_f32 v10, v92, v93, v94
v_max3_f32 v10, v10, v95, v132
v_max3_f32 v10, v10, v133, v134
v_max_f32 v10, v10, v135
v_mov_b32 v11, v10
s_nop 1
v_permlane16_swap_b32 v10, v11
v_max_f32 v10, v10, v11
v_mov_b32 v11, v10
s_nop 1
v_permlane32_swap_b32 v10, v11
v_max_f32 v10, v10, v11
A_ v10,0x40e00000,v10
v_max_f32 v10, 0, v10
v_exp_f32 v11, -v10
s_nop 0
U_ v50,v50,v11
R_ v12,a16
U_ v12,v12,v11
X_ a16,v12
R_ v13,a17
U_ v13,v13,v11
X_ a17,v13
R_ v12,a18
U_ v12,v12,v11
X_ a18,v12
R_ v13,a19
U_ v13,v13,v11
X_ a19,v13
R_ v12,a36
U_ v12,v12,v11
X_ a36,v12
R_ v13,a37
U_ v13,v13,v11
X_ a37,v13
R_ v12,a38
U_ v12,v12,v11
X_ a38,v12
R_ v13,a39
U_ v13,v13,v11
X_ a39,v13
R_ v12,a56
U_ v12,v12,v11
X_ a56,v12
R_ v13,a57
U_ v13,v13,v11
X_ a57,v13
R_ v12,a58
U_ v12,v12,v11
X_ a58,v12
R_ v13,a59
U_ v13,v13,v11
X_ a59,v13
R_ v12,a76
U_ v12,v12,v11
X_ a76,v12
R_ v13,a77
U_ v13,v13,v11
X_ a77,v13
R_ v12,a78
U_ v12,v12,v11
X_ a78,v12
R_ v13,a79
U_ v13,v13,v11
X_ a79,v13
R_ v12,a96
U_ v12,v12,v11
X_ a96,v12
R_ v13,a97
U_ v13,v13,v11
X_ a97,v13
R_ v12,a98
U_ v12,v12,v11
X_ a98,v12
R_ v13,a99
U_ v13,v13,v11
X_ a99,v13
R_ v12,a116
U_ v12,v12,v11
X_ a116,v12
R_ v13,a117
U_ v13,v13,v11
X_ a117,v13
R_ v12,a118
U_ v12,v12,v11
X_ a118,v12
R_ v13,a119
U_ v13,v13,v11
X_ a119,v13
R_ v12,a136
U_ v12,v12,v11
X_ a136,v12
R_ v13,a137
U_ v13,v13,v11
X_ a137,v13
R_ v12,a138
U_ v12,v12,v11
X_ a138,v12
R_ v13,a139
U_ v13,v13,v11
X_ a139,v13
R_ v12,a156
U_ v12,v12,v11
X_ a156,v12
R_ v13,a157
U_ v13,v13,v11
X_ a157,v13
R_ v12,a158
U_ v12,v12,v11
X_ a158,v12
R_ v13,a159
U_ v13,v13,v11
X_ a159,v13
R_ v12,a176
U_ v12,v12,v11
X_ a176,v12
R_ v13,a177
U_ v13,v13,v11
X_ a177,v13
R_ v12,a178
U_ v12,v12,v11
X_ a178,v12
R_ v13,a179
U_ v13,v13,v11
X_ a179,v13
R_ v12,a196
U_ v12,v12,v11
X_ a196,v12
R_ v13,a197
U_ v13,v13,v11
X_ a197,v13
R_ v12,a198
U_ v12,v12,v11
X_ a198,v12
R_ v13,a199
U_ v13,v13,v11
X_ a199,v13
R_ v12,a216
U_ v12,v12,v11
X_ a216,v12
R_ v13,a217
U_ v13,v13,v11
X_ a217,v13
R_ v12,a218
U_ v12,v12,v11
X_ a218,v12
R_ v13,a219
U_ v13,v13,v11
X_ a219,v13
R_ v12,a236
U_ v12,v12,v11
X_ a236,v12
R_ v13,a237
U_ v13,v13,v11
X_ a237,v13
R_ v12,a238
U_ v12,v12,v11
X_ a238,v12
R_ v13,a239
U_ v13,v13,v11
X_ a239,v13
B_ v72,v72,v10
B_ v73,v73,v10
B_ v74,v74,v10
B_ v75,v75,v10
B_ v92,v92,v10
B_ v93,v93,v10
B_ v94,v94,v10
B_ v95,v95,v10
B_ v132,v132,v10
B_ v133,v133,v10
B_ v134,v134,v10
B_ v135,v135,v10
E_ 92
E_ 93
E_ 94
E_ 95
E_ 132
E_ 133
E_ 134
E_ 135
s_nop 0
C_ 135,134,135
C_ 134,132,133
C_ 132,92,93
C_ 133,94,95
v_mov_b32 v92, 0x3f803f80
v_mov_b32 v93, 0x3f803f80
v_mov_b32 v94, 0x3f803f80
v_mov_b32 v95, 0x3f803f80
s_nop 7
s_lshl_b32 s53, s58, 4
S_ s53,s53,s63
s_cmp_eq_u32 s53, 1
s_cbranch_scc1 .Lstep0_2
s_cmp_eq_u32 s53, 13
s_cbranch_scc1 .Lstep0_14
s_cmp_eq_u32 s53, 15
s_cbranch_scc1 .Lstep0_16
s_cmp_eq_u32 s53, 17
s_cbranch_scc1 .Lstep1_2
s_cmp_eq_u32 s53, 19
s_cbranch_scc1 .Lstep1_4
s_cmp_eq_u32 s53, 21
s_cbranch_scc1 .Lstep1_6
s_cmp_eq_u32 s53, 23
s_cbranch_scc1 .Lstep1_8
s_cmp_eq_u32 s53, 25
s_cbranch_scc1 .Lstep1_10
s_cmp_eq_u32 s53, 27
s_cbranch_scc1 .Lstep1_12
s_cmp_eq_u32 s53, 29
s_cbranch_scc1 .Lstep1_14
s_cmp_eq_u32 s53, 31
s_cbranch_scc1 .Lstep1_16
s_cmp_eq_u32 s53, 33
s_cbranch_scc1 .Lstep2_2
s_cmp_eq_u32 s53, 35
s_cbranch_scc1 .Lstep2_4
s_cmp_eq_u32 s53, 37
s_cbranch_scc1 .Lstep2_6
s_cmp_eq_u32 s53, 39
s_cbranch_scc1 .Lstep2_8
s_cmp_eq_u32 s53, 41
s_cbranch_scc1 .Lstep2_10
s_cmp_eq_u32 s53, 43
s_cbranch_scc1 .Lstep2_12
s_cmp_eq_u32 s53, 45
s_cbranch_scc1 .Lstep2_14
s_cmp_eq_u32 s53, 47
s_cbranch_scc1 .Lstep2_16
s_endpgm
.Lxs2m:
s_sleep 5
s_branch .Lxs2m_back
.Lxs2e:
s_sleep 5
s_branch .Lxs2e_back
.Lxs3m:
s_sleep 5
s_branch .Lxs3m_back
.Lxs3e:
s_sleep 5
s_branch .Lxs3e_back
.Lxs14m:
s_sleep 7
s_branch .Lxs14m_back
.Lxs14e:
s_sleep 7
s_branch .Lxs14e_back
.Lxs15m:
s_sleep 7
s_branch .Lxs15m_back
.Lxs15e:
s_sleep 7
s_branch .Lxs15e_back
.Lpace1:
S_ s12,s12,s13
.Lpace1_l:
s_memrealtime s[100:101]
W_ 0
s_sub_u32 s100, s100, s98
s_cmp_ge_u32 s100, s12
s_cbranch_scc1 .Lpace1_back
s_sleep 2
s_branch .Lpace1_l
.Lpace3:
S_ s12,s12,s13
.Lpace3_l:
s_memrealtime s[100:101]
W_ 0
s_sub_u32 s100, s100, s98
s_cmp_ge_u32 s100, s12
s_cbranch_scc1 .Lpace3_back
s_sleep 2
s_branch .Lpace3_l
.Lpace5:
S_ s12,s12,s13
.Lpace5_l:
s_memrealtime s[100:101]
W_ 0
s_sub_u32 s100, s100, s98
s_cmp_ge_u32 s100, s12
s_cbranch_scc1 .Lpace5_back
s_sleep 2
s_branch .Lpace5_l
.Lpace7:
S_ s12,s12,s13
.Lpace7_l:
s_memrealtime s[100:101]
W_ 0
s_sub_u32 s100, s100, s98
s_cmp_ge_u32 s100, s12
s_cbranch_scc1 .Lpace7_back
s_sleep 2
s_branch .Lpace7_l
.Lpace9:
S_ s12,s12,s13
.Lpace9_l:
s_memrealtime s[100:101]
W_ 0
s_sub_u32 s100, s100, s98
s_cmp_ge_u32 s100, s12
s_cbranch_scc1 .Lpace9_back
s_sleep 2
s_branch .Lpace9_l
.Lpace11:
S_ s12,s12,s13
.Lpace11_l:
s_memrealtime s[100:101]
W_ 0
s_sub_u32 s100, s100, s98
s_cmp_ge_u32 s100, s12
s_cbranch_scc1 .Lpace11_back
s_sleep 2
s_branch .Lpace11_l
.Lpace13:
S_ s12,s12,s13
.Lpace13_l:
s_memrealtime s[100:101]
W_ 0
s_sub_u32 s100, s100, s98
s_cmp_ge_u32 s100, s12
s_cbranch_scc1 .Lpace13_back
s_sleep 2
s_branch .Lpace13_l
.Lpace15:
S_ s12,s12,s13
.Lpace15_l:
s_memrealtime s[100:101]
W_ 0
s_sub_u32 s100, s100, s98
s_cmp_ge_u32 s100, s12
s_cbranch_scc1 .Lpace15_back
s_sleep 2
s_branch .Lpace15_l
.Lpace17:
S_ s12,s12,s13
.Lpace17_l:
s_memrealtime s[100:101]
W_ 0
s_sub_u32 s100, s100, s98
s_cmp_ge_u32 s100, s12
s_cbranch_scc1 .Lpace17_back
s_sleep 2
s_branch .Lpace17_l
.Lpace19:
S_ s12,s12,s13
.Lpace19_l:
s_memrealtime s[100:101]
W_ 0
s_sub_u32 s100, s100, s98
s_cmp_ge_u32 s100, s12
s_cbranch_scc1 .Lpace19_back
s_sleep 2
s_branch .Lpace19_l
.Lpace21:
S_ s12,s12,s13
.Lpace21_l:
s_memrealtime s[100:101]
W_ 0
s_sub_u32 s100, s100, s98
s_cmp_ge_u32 s100, s12
s_cbranch_scc1 .Lpace21_back
s_sleep 2
s_branch .Lpace21_l
.Lpace23:
S_ s12,s12,s13
.Lpace23_l:
s_memrealtime s[100:101]
W_ 0
s_sub_u32 s100, s100, s98
s_cmp_ge_u32 s100, s12
s_cbranch_scc1 .Lpace23_back
s_sleep 2
s_branch .Lpace23_l
.Lpace25:
S_ s12,s12,s13
.Lpace25_l:
s_memrealtime s[100:101]
W_ 0
s_sub_u32 s100, s100, s98
s_cmp_ge_u32 s100, s12
s_cbranch_scc1 .Lpace25_back
s_sleep 2
s_branch .Lpace25_l
.Lpace27:
S_ s12,s12,s13
.Lpace27_l:
s_memrealtime s[100:101]
W_ 0
s_sub_u32 s100, s100, s98
s_cmp_ge_u32 s100, s12
s_cbranch_scc1 .Lpace27_back
s_sleep 2
s_branch .Lpace27_l
.Lpace29:
S_ s12,s12,s13
.Lpace29_l:
s_memrealtime s[100:101]
W_ 0
s_sub_u32 s100, s100, s98
s_cmp_ge_u32 s100, s12
s_cbranch_scc1 .Lpace29_back
s_sleep 2
s_branch .Lpace29_l
.Lhsd0_0:
s_bitcmp1_b32 s67, 13
s_cbranch_scc1 .Lhs_a0
s_bitcmp1_b32 s67, 11
s_cbranch_scc1 .Lhs_c0
s_branch .Lhs_d0
.Lhs_a0:
s_nop 4
s_waitcnt vmcnt(12)
R_ v10,a251
buffer_store_dword a255, v10, s[92:95], 0 offen offset:128 sc1
s_branch .Lhsd0_0_back
.Lhs_a1:
s_nop 4
R_ v11,a248
v_lshrrev_b32 v11, 2, v11
buffer_load_dword a254, v11, s[92:95], s96 offen offset:128 sc1
s_branch .Lhs_a1_back
.Lhs_a2:
s_nop 4
s_waitcnt vmcnt(12)
R_ v10,a251
buffer_store_dword a255, v10, s[92:95], 0 offen offset:128 sc1
s_branch .Lhs_a2_back
.Lhs_b1:
s_nop 4
s_waitcnt vmcnt(12)
R_ v11,a254
v_cmp_eq_u32 vcc, s62, v11
R_ v10,a251
v_mov_b32 v11, 0x7fff0000
v_cndmask_b32 v10, v11, v10, vcc
R_ v11,a253
v_add_u32 v11, 1, v11
buffer_store_dword v11, v10, s[92:95], 0 offen offset:256 sc1
s_and_b32 s53, vcc_lo, 255
s_cmp_eq_u32 s53, 0xFF
s_cselect_b32 s97, 1, 0
s_branch .Lhs_b1_back
.Lhs_c0:
s_nop 4
R_ v11,a248
v_lshrrev_b32 v11, 2, v11
buffer_load_dword a254, v11, s[92:95], s96 offen offset:128 sc1
s_branch .Lhsd0_0_back
.Lhs_c1:
s_nop 4
R_ v11,a248
v_lshrrev_b32 v11, 2, v11
buffer_load_dword a254, v11, s[92:95], s96 offen offset:256 sc1
s_branch .Lhs_c1_back
.Lhs_c2:
s_nop 4
R_ v11,a248
v_lshrrev_b32 v11, 2, v11
buffer_load_dword a254, v11, s[92:95], s96 offen offset:128 sc1
s_branch .Lhs_c2_back
.Lhs_d0:
s_nop 4
s_waitcnt vmcnt(12)
R_ v11,a254
v_cmp_eq_u32 vcc, s62, v11
R_ v10,a251
v_mov_b32 v11, 0x7fff0000
v_cndmask_b32 v10, v11, v10, vcc
R_ v11,a253
v_add_u32 v11, 1, v11
buffer_store_dword v11, v10, s[92:95], 0 offen offset:256 sc1
s_and_b32 s53, vcc_lo, 255
s_cmp_eq_u32 s53, 0xFF
s_cselect_b32 s97, 1, 0
s_branch .Lhsd0_0_back
.Lhs_d2:
s_nop 4
s_waitcnt vmcnt(12)
R_ v11,a254
v_cmp_eq_u32 vcc, s62, v11
R_ v10,a251
v_mov_b32 v11, 0x7fff0000
v_cndmask_b32 v10, v11, v10, vcc
R_ v11,a253
v_add_u32 v11, 1, v11
buffer_store_dword v11, v10, s[92:95], 0 offen offset:256 sc1
s_and_b32 s53, vcc_lo, 255
s_cmp_eq_u32 s53, 0xFF
s_cselect_b32 s97, 1, 0
s_branch .Lhs_d2_back
.Lhs_d1:
s_nop 4
s_waitcnt vmcnt(12)
R_ v11,a254
R_ v10,a252
v_cmp_ne_u32 vcc, v11, v10
s_nop 1
s_and_b32 s53, vcc_lo, 255
s_cmp_eq_u32 s53, 0xFF
s_cselect_b32 s53, s97, 0
s_cmp_eq_u32 s53, 1
s_cbranch_scc1 .Lhs_ok
.Lhs_spin:
s_cmp_eq_u32 s57, 0
s_cbranch_scc1 .Lhs_ok
s_sub_u32 s57, s57, 1
s_sleep 8
R_ v10,a251
s_lshl_b32 s53, s6, 2
v_subrev_u32 v10, s53, v10
buffer_load_dword v11, v10, s[92:95], 0 offen offset:0 sc1
R_ v10,a248
v_lshrrev_b32 v10, 2, v10
buffer_load_dword v12, v10, s[92:95], s96 offen offset:128 sc1
buffer_load_dword v13, v10, s[92:95], s96 offen offset:256 sc1
s_waitcnt vmcnt(0)
X_ a255,v11
R_ v10,a251
buffer_store_dword v11, v10, s[92:95], 0 offen offset:128 sc1
s_nop 1
v_cmp_eq_u32 vcc, s62, v12
R_ v10,a251
v_mov_b32 v12, 0x7fff0000
v_cndmask_b32 v10, v12, v10, vcc
R_ v12,a253
v_add_u32 v12, 1, v12
buffer_store_dword v12, v10, s[92:95], 0 offen offset:256 sc1
s_and_b32 s53, vcc_lo, 255
s_cmp_eq_u32 s53, 0xFF
s_cselect_b32 s97, 1, 0
s_nop 1
R_ v10,a252
v_cmp_ne_u32 vcc, v13, v10
s_nop 1
s_and_b32 s53, vcc_lo, 255
s_cmp_eq_u32 s53, 0xFF
s_cselect_b32 s53, s97, 0
s_cmp_eq_u32 s53, 1
s_cbranch_scc0 .Lhs_spin
.Lhs_ok:
s_branch .Lhs_d1_back
.Latt2:
R_ v13,a248
S_ s53,s51,36864
v_add_u32 v12, s53, v13
.Lpad_a2_0:
s_cbranch_execz .Lpad_a2_4
s_mul_i32 s53, s4, 10240
S_ s53,s53,0
v_add_u32 v13, s53, v13
s_cmp_eq_u32 s4, 0
s_cbranch_scc0 .Lhs_tok2
R_ v11,a241
buffer_load_dword a255, v11, s[92:95], 0 offen offset:0 sc1
.Lhs_tok2:
W_ 2
Zvvv 96,30,136
Dv 42,v1,44032
R_ v2,a245
Zvvv 100,30,140
ds_write_b128 v13, a[0:3] offset:0
Zvvv 104,30,144
v_xor_b32 v3, 32, v2
v_xor_b32 v4, 64, v2
Zvvv 108,30,148
ds_write_b128 v13, a[4:7] offset:1024
Zvvv 112,30,152
W_ 4
v_xor_b32 v5, 96, v2
s_mov_b64 s[24:25], s[16:17]
Mvvv 96,34,156
s_mov_b32 s26, 7864320
Dv 30,%0,45056
s_mov_b64 s[28:29], s[22:23]
Mvvv 100,34,160
ds_write_b128 v13, a[8:11] offset:2048
Mvvv 104,34,164
s_mov_b32 s30, 1638400
R_ v20,a80
s_mul_i32 s52, s4, 10240
Mvvv 108,34,168
ds_write_b128 v13, a[12:15] offset:3072
Mvvv 112,34,172
W_ 6
R_ v21,a81
R_ v22,a82
Mvvv 96,38,176
Dv 34,v1,45056
R_ v23,a83
Mvvv 100,38,180
ds_write_b128 v13, a[16:19] offset:4096
Mvvv 104,38,184
R_ v24,a84
R_ v25,a85
Mvvv 108,38,188
ds_write_b128 v13, a[40:43] offset:5120
Mvvv 112,38,192
W_ 8
R_ v26,a86
R_ v27,a87
Mvvv 96,42,196
Dv 38,%0,36864
R_ v52,a88
Mvvv 100,42,200
ds_write_b128 v13, a[44:47] offset:6144
Mvvv 104,42,204
R_ v53,a89
R_ v54,a90
Mvvv 108,42,208
ds_write_b128 v13, a[48:51] offset:7168
Mvvv 112,42,212
W_ 8
R_ v55,a91
v_accvgpr_mov_b32 a240, a92
Mvvv 96,30,216
Dv 42,v1,36864
v_accvgpr_mov_b32 a241, a93
Mvvv 100,30,220
ds_write_b128 v13, a[52:55] offset:8192
Mvvv 104,30,224
v_accvgpr_mov_b32 a242, a94
v_accvgpr_mov_b32 a243, a95
Mvvv 108,30,228
ds_write_b128 v13, a[56:59] offset:9216
Mvvv 112,30,232
W_ 8
v_accvgpr_mov_b32 a244, a96
v_accvgpr_mov_b32 a245, a97
Mvvv 96,34,236
Dv 30,%0,37888
v_accvgpr_mov_b32 a246, a98
Mvvv 100,34,240
v_accvgpr_mov_b32 a247, a99
R_ v16,a120
Mvvv 104,34,244
R_ v17,a121
R_ v18,a122
Mvvv 108,34,248
R_ v19,a123
R_ v28,a124
Mvvv 112,34,252
W_ 6
R_ v29,a125
R_ v51,a126
Zvvv 76,38,136
Dv 34,v1,37888
R_ v6,a127
Zvvv 80,38,140
R_ v14,a128
R_ v15,a129
Zvvv 84,38,144
v_accvgpr_mov_b32 a249, a130
v_accvgpr_mov_b32 a250, a131
Zvvv 88,38,148
v_max3_f32 v56, v96, v97, v98
v_max_f32 v56, v56, v99
Zvvv 92,38,152
W_ 4
v_max3_f32 v60, v100, v101, v102
v_max_f32 v60, v60, v103
Mvvv 76,42,156
Dv 38,%0,38912
v_max3_f32 v64, v104, v105, v106
Mvvv 80,42,160
v_max_f32 v64, v64, v107
Mvvv 84,42,164
v_max3_f32 v68, v108, v109, v110
v_max_f32 v68, v68, v111
Mvvv 88,42,168
v_max3_f32 v72, v112, v113, v114
v_max_f32 v72, v72, v115
Mvvv 92,42,172
W_ 2
Mvvv 76,30,176
Dv 42,v1,38912
Mvvv 80,30,180
Mvvv 84,30,184
Mvvv 88,30,188
Mvvv 92,30,192
s_waitcnt vmcnt(30) lgkmcnt(0)
s_barrier
Mvvv 76,34,196
Dv 30,%0,55296
Mvvv 76,38,216
Mvvv 76,42,236
Mvvv 80,34,200
Mvvv 80,38,220
Mvvv 80,42,240
Mvvv 84,34,204
s_nop 3
v_max3_f32 v56, v56, v76, v77
v_max3_f32 v56, v56, v78, v79
Mvvv 84,38,224
Mvvv 84,42,244
Mvvv 88,34,208
v_max3_f32 v60, v60, v80, v81
v_max3_f32 v60, v60, v82, v83
Mvvv 88,38,228
Mvvv 88,42,248
Mvvv 92,34,212
s_nop 1
v_max3_f32 v64, v64, v84, v85
v_max3_f32 v64, v64, v86, v87
Mvvv 92,38,232
Mvvv 92,42,252
Dv 34,v1,55296
.p2align 3
v_mov_b32 v11, 0x3f803f80
v_permlane32_swap_b32 v56, v60
v_max3_f32 v68, v68, v88, v89
v_max3_f32 v68, v68, v90, v91
v_max_f32 v56, v56, v60
s_nop 1
v_max3_f32 v72, v72, v92, v93
v_max3_f32 v72, v72, v94, v95
v_mov_b32 v73, v72
v_permlane32_swap_b32 v64, v68
v_max_f32 v64, v64, v68
v_permlane32_swap_b32 v72, v73
v_max_f32 v72, v72, v73
v_mov_b32 v73, v72
v_permlane16_swap_b32 v56, v64
v_max_f32 v56, v56, v64
v_permlane16_swap_b32 v72, v73
B_ v56,0xc0e00000,v56
v_max_f32 v72, v72, v73
v_mov_b32 v64, v56
B_ v72,0xc0e00000,v72
v_mov_b32 v73, v72
v_permlane16_swap_b32 v56, v64
v_mov_b32 v60, v56
v_mov_b32 v68, v64
v_mov_b64 v[74:75], v[72:73]
v_permlane32_swap_b32 v56, v60
v_permlane32_swap_b32 v64, v68
v_mov_b32 v57, v56
v_mov_b32 v61, v60
v_mov_b32 v65, v64
v_mov_b32 v69, v68
v_pk_add_f32 v[96:97], v[96:97], v[56:57]
v_pk_add_f32 v[98:99], v[98:99], v[56:57]
v_pk_add_f32 v[76:77], v[76:77], v[56:57]
v_pk_add_f32 v[78:79], v[78:79], v[56:57]
v_mov_b64 v[58:59], v[56:57]
v_pk_add_f32 v[100:101], v[100:101], v[60:61]
v_pk_add_f32 v[102:103], v[102:103], v[60:61]
v_pk_add_f32 v[80:81], v[80:81], v[60:61]
v_pk_add_f32 v[82:83], v[82:83], v[60:61]
v_mov_b64 v[62:63], v[60:61]
v_pk_add_f32 v[104:105], v[104:105], v[64:65]
v_pk_add_f32 v[106:107], v[106:107], v[64:65]
v_pk_add_f32 v[84:85], v[84:85], v[64:65]
v_pk_add_f32 v[86:87], v[86:87], v[64:65]
v_mov_b64 v[66:67], v[64:65]
v_pk_add_f32 v[108:109], v[108:109], v[68:69]
v_pk_add_f32 v[110:111], v[110:111], v[68:69]
v_pk_add_f32 v[88:89], v[88:89], v[68:69]
v_pk_add_f32 v[90:91], v[90:91], v[68:69]
v_mov_b64 v[70:71], v[68:69]
v_pk_add_f32 v[112:113], v[112:113], v[72:73]
v_pk_add_f32 v[114:115], v[114:115], v[72:73]
v_pk_add_f32 v[92:93], v[92:93], v[72:73]
v_pk_add_f32 v[94:95], v[94:95], v[72:73]
E_ 96
E_ 97
E_ 98
E_ 99
E_ 76
E_ 77
E_ 78
E_ 79
C_ 99,98,99
C_ 98,96,97
C_ 96,76,77
C_ 97,78,79
E_ 100
E_ 101
E_ 102
E_ 103
E_ 80
E_ 81
E_ 82
E_ 83
C_ 103,102,103
C_ 102,100,101
C_ 100,80,81
C_ 101,82,83
.Lstep2_1:
)A"
R"A(W_ 1
Nvvv 116,30,136,56
s_nop 0
Dv 38,%0,56320
Dv 42,v1,56320
W_ 2
Mvvv 116,34,156
E_ 104
Nvvv 120,30,140,60
E_ 105
Mvvv 120,34,160
E_ 106
Nvvv 124,30,144,64
E_ 107
Mvvv 124,34,164
E_ 84
Nvvv 128,30,148,68
E_ 85
Mvvv 128,34,168
E_ 86
Nvvv 132,30,152,72
E_ 87
Mvvv 132,34,172
W_ 0
C_ 107,106,107
C_ 106,104,105
Mvvv 132,38,192
s_nop 0
Dv 30,%0,57344
Dv 34,v1,57344
Mvvv 132,42,212
C_ 104,84,85
C_ 105,86,87
Mvvv 128,38,188
E_ 108
Mvvv 128,42,208
E_ 109
Mvvv 124,38,184
E_ 110
Mvvv 124,42,204
E_ 111
Mvvv 120,38,180
E_ 88
Mvvv 120,42,200
E_ 89
Mvvv 116,38,176
E_ 90
Mvvv 116,42,196
W_ 0
E_ 91
Mvvv 116,30,216
s_nop 0
Dv 38,%0,49152
Dv 42,v1,49152
Mvvv 116,34,236
C_ 111,110,111
C_ 110,108,109
Mvvv 120,30,220
C_ 108,88,89
C_ 109,90,91
Mvvv 120,34,240
E_ 112
Mvvv 124,30,224
E_ 113
Mvvv 124,34,244
E_ 114
Mvvv 128,30,228
E_ 115
Mvvv 128,34,248
E_ 92
Mvvv 132,30,232
E_ 93
Mvvv 132,34,252
W_ 0
E_ 94
Nvvv 76,38,136,56
s_nop 0
Dv 30,%0,50176
Dv 34,v1,50176
Mvvv 76,42,156
E_ 95
Nvvv 80,38,140,60
C_ 115,114,115
C_ 114,112,113
Mvvv 80,42,160
.Lpad_a2_1:
s_cbranch_execz .Lpad_a2_5
C_ 112,92,93
C_ 113,94,95
Nvvv 84,38,144,64
E_ 116
Mvvv 84,42,164
E_ 117
Nvvv 88,38,148,68
E_ 118
Mvvv 88,42,168
E_ 119
Nvvv 92,38,152,72
C_ 119,118,119
C_ 118,116,117
Mvvv 92,42,172
W_ 0
E_ 120
Mvvv 92,30,192
s_nop 0
Dv 38,%0,51200
Dv 42,v1,51200
Mvvv 92,34,212
X_ a180,v11
X_ a181,v11
Mvvv 88,30,188
X_ a182,v11
X_ a183,v11
Mvvv 88,34,208
ds_write_b128 v12, a[132:135] offset:0
Mvvv 84,30,184
E_ 121
Mvvv 84,34,204
E_ 122
Mvvv 80,30,180
ds_write_b128 v12, a[136:139] offset:1024
Mvvv 80,34,200
E_ 123
Mvvv 76,30,176
C_ 123,122,123
C_ 122,120,121
Mvvv 76,34,196
ds_write_b128 v12, a[160:163] offset:2048
W_ 4
Mvvv 76,38,216
W_ 3
E_ 124
Mvvv 76,42,236
s_waitcnt vmcnt(12) lgkmcnt(0)
s_barrier
Mvvv 80,38,220
ds_write_b128 v12, a[164:167] offset:3072
Mvvv 80,42,240
E_ 125
Mvvv 84,38,224
E_ 126
Mvvv 84,42,244
s_nop 0
Tv 30,v2,24576
Tv 32,v2,30720
Mvvv 88,38,228
ds_write_b128 v12, a[168:171] offset:4096
Mvvv 88,42,248
s_nop 0
Tv 34,v3,24576
Tv 36,v3,30720
Mvvv 92,38,232
E_ 127
Mvvv 92,42,252
ds_write_b128 v12, a[172:175] offset:5120
Zaav 220,180,96
C_ 127,126,127
C_ 126,124,125
Zaav 224,180,100
E_ 128
Zaav 228,180,104
ds_write_b128 v12, a[176:179] offset:12288
Zaav 232,180,108
E_ 129
Zaav 236,180,112
W_ 2
E_ 130
Zavv 0,30,96
s_nop 0
Tv 38,v4,24576
Tv 40,v4,30720
Zavv 20,34,96
ds_write_b128 v12, a[200:203] offset:13312
Zavv 24,34,100
s_nop 0
Tv 42,v5,24576
Tv 44,v5,30720
Zavv 4,30,100
E_ 131
Zavv 8,30,104
ds_write_b128 v12, a[204:207] offset:14336
Zavv 28,34,104
R_ v46,a220
R_ v47,a224
Zavv 32,34,108
R_ v48,a228
R_ v49,a232
Zavv 12,30,108
R_ v50,a236
C_ 131,130,131
Zavv 16,30,112
ds_write_b128 v12, a[208:211] offset:15360
Zavv 36,34,112
W_ 2
C_ 130,128,129
Zavv 56,38,112
s_nop 0
Tv 30,v2,25600
Tv 32,v2,31744
Zavv 76,42,112
ds_write_b128 v12, a[212:215] offset:16384
Zavv 72,42,108
s_nop 0
Tv 34,v3,25600
Tv 36,v3,31744
Zavv 52,38,108
E_ 132
Zavv 48,38,104
ds_write_b128 v12, a[216:219] offset:17408
Zavv 68,42,104
E_ 133
Zavv 64,42,100
E_ 134
Zavv 44,38,100
E_ 135
Zavv 40,38,96
C_ 135,134,135
C_ 134,132,133
Zavv 60,42,96
W_ 1
E_ 76
Zavv 80,30,96
s_nop 0
Tv 38,v4,25600
Tv 40,v4,31744
Zavv 100,34,96
E_ 77
Zavv 104,34,100
s_nop 0
Tv 42,v5,25600
Tv 44,v5,31744
Zavv 84,30,100
E_ 78
Zavv 88,30,104
E_ 79
Zavv 108,34,104
C_ 116,76,77
C_ 117,78,79
Zavv 112,34,108
E_ 80
Zavv 92,30,108
E_ 81
Zavv 96,30,112
E_ 82
Zavv 116,34,112
W_ 0
E_ 83
Zavv 136,38,112
s_nop 0
Tv 30,v2,26624
Tv 32,v2,32768
Zavv 156,42,112
C_ 120,80,81
C_ 121,82,83
Zavv 152,42,108
s_nop 0
Tv 34,v3,26624
Tv 36,v3,32768
Zavv 132,38,108
E_ 84
Zavv 128,38,104
E_ 85
Zavv 148,42,104
E_ 86
Zavv 144,42,100
E_ 87
Zavv 124,38,100
C_ 124,84,85
C_ 125,86,87
Zavv 120,38,96
E_ 88
Zavv 140,42,96
W_ 0
E_ 89
Zavv 160,30,96
s_nop 0
Tv 38,v4,26624
Tv 40,v4,32768
Zavv 180,34,96
E_ 90
Zavv 184,34,100
s_nop 0
Tv 42,v5,26624
Tv 44,v5,32768
Zavv 164,30,100
E_ 91
Zavv 168,30,104
C_ 128,88,89
C_ 129,90,91
Zavv 188,34,104
S_ s65,s61,36864
E_ 92
Zavv 192,34,108
S_ m0,s51,24576
E_ 93
Zavv 172,30,108
s_waitcnt vmcnt(6) lgkmcnt(0)
s_barrier
Zavv 176,30,112
Ln v7,32,s65,0
Zavv 196,34,112
E_ 94
Zavv 216,38,112
Dv 30,%0,6144
Zavv 236,42,112
Dv 34,v1,6144
Zavv 232,42,108
Ln v8,32,s65,3072
S_ m0,s51,25472
Zavv 212,38,108
E_ 95
Zavv 208,38,104
Ln v7,32,s65,128
Zavv 228,42,104
C_ 132,92,93
C_ 133,94,95
Zavv 224,42,100
Ln v8,32,s65,3200
S_ m0,s51,26368
Zavv 204,38,100
v_mov_b32 v92, 0x3f803f80
v_mov_b32 v93, 0x3f803f80
Zavv 200,38,96
Ln v7,32,s65,256
Zavv 220,42,96
v_mov_b32 v94, 0x3f803f80
v_mov_b32 v95, 0x3f803f80
Ln v8,32,s65,3328
.Lstep2_2:
s_nop 0
Zvvv 76,92,116
Dv 38,%0,7168
Zvvv 80,92,120
Dv 42,v1,7168
Zvvv 84,92,124
Zvvv 88,92,128
Zvvv 92,92,132
W_ 2
Nvvv 96,30,136,56
Mvvv 96,34,156
Nvvv 100,30,140,60
s_nop 1
v_max3_f32 v10, v76, v80, v84
s_nop 0
v_max3_f32 v10, v10, v88, v92
Mvvv 100,34,160
v_cmp_lt_f32 vcc, 0x42000000, v10
s_cbranch_vccnz .Ltramp2_1
Nvvv 104,30,144,64
A_ v46,v46,v76
A_ v47,v47,v80
Mvvv 104,34,164
A_ v48,v48,v84
A_ v49,v49,v88
Nvvv 108,30,148,68
A_ v50,v50,v92
Mvvv 108,34,168
Nvvv 112,30,152,72
Mvvv 112,34,172
W_ 0
Mvvv 112,38,192
Dv 30,%0,8192
Mvvv 112,42,212
Dv 34,v1,8192
Mvvv 108,38,188
Mvvv 108,42,208
Mvvv 104,38,184
Mvvv 104,42,204
Mvvv 100,38,180
Mvvv 100,42,200
Mvvv 96,38,176
Mvvv 96,42,196
W_ 0
Mvvv 96,30,216
Dv 38,%0,0
Mvvv 96,34,236
Dv 42,v1,0
Mvvv 100,30,220
Mvvv 100,34,240
Mvvv 104,30,224
Mvvv 104,34,244
Mvvv 108,30,228
Mvvv 108,34,248
s_nop 0
E_ 96
Mvvv 112,30,232
E_ 97
Mvvv 112,34,252
W_ 0
E_ 98
Nvvv 76,38,136,56
Dv 30,%0,1024
Mvvv 76,42,156
Dv 34,v1,1024
Nvvv 80,38,140,60
E_ 99
Mvvv 80,42,160
C_ 99,98,99
C_ 98,96,97
Nvvv 84,38,144,64
E_ 100
Mvvv 84,42,164
E_ 101
Nvvv 88,38,148,68
E_ 102
Mvvv 88,42,168
E_ 103
Nvvv 92,38,152,72
C_ 103,102,103
C_ 102,100,101
Mvvv 92,42,172
W_ 0
E_ 104
Mvvv 92,30,192
Dv 38,%0,2048
Mvvv 92,34,212
Dv 42,v1,2048
Mvvv 88,30,188
E_ 105
Mvvv 88,34,208
E_ 106
Mvvv 84,30,184
S_ s65,s61,24576
E_ 107
Mvvv 84,34,204
S_ m0,s51,0
C_ 107,106,107
C_ 106,104,105
Mvvv 80,30,180
s_waitcnt vmcnt(6) lgkmcnt(0)
s_barrier
Mvvv 80,34,200
L_ v9,36,s65,0
Mvvv 76,30,176
E_ 108
Mvvv 76,34,196
L_ v9,36,s65,3072
S_ m0,s51,896
Mvvv 76,38,216
Tv 30,v2,12288
Tv 32,v2,18432
Mvvv 76,42,236
L_ v9,36,s65,128
Mvvv 80,38,220
s_nop 0
Tv 34,v3,12288
Tv 36,v3,18432
Mvvv 80,42,240
L_ v9,36,s65,3200
S_ m0,s51,1792
Mvvv 84,38,224
E_ 109
Mvvv 84,42,244
L_ v9,36,s65,256
Mvvv 88,38,228
E_ 110
Mvvv 88,42,248
L_ v9,36,s65,3328
Mvvv 92,38,232
E_ 111
Mvvv 92,42,252
W_ 0
C_ 111,110,111
C_ 110,108,109
Mavv 0,30,116
s_nop 0
Tv 38,v4,12288
Tv 40,v4,18432
Mavv 20,34,116
E_ 112
Mavv 24,34,120
s_nop 0
Tv 42,v5,12288
Tv 44,v5,18432
Mavv 4,30,120
E_ 113
Mavv 8,30,124
E_ 114
Mavv 28,34,124
E_ 115
Mavv 32,34,128
C_ 115,114,115
C_ 114,112,113
Mavv 12,30,128
E_ 76
Mavv 16,30,132
E_ 77
Mavv 36,34,132
W_ 0
E_ 78
Mavv 56,38,132
s_nop 0
Tv 30,v2,13312
Tv 32,v2,19456
Mavv 76,42,132
E_ 79
Mavv 72,42,128
s_nop 0
Tv 34,v3,13312
Tv 36,v3,19456
Mavv 52,38,128
C_ 96,76,77
C_ 97,78,79
Mavv 48,38,124
E_ 80
Mavv 68,42,124
E_ 81
Mavv 64,42,120
E_ 82
Mavv 44,38,120
E_ 83
Mavv 40,38,116
C_ 100,80,81
C_ 101,82,83
Mavv 60,42,116
W_ 0
E_ 84
Mavv 80,30,116
s_nop 0
Tv 38,v4,13312
Tv 40,v4,19456
Mavv 100,34,116
E_ 85
Mavv 104,34,120
s_nop 0
Tv 42,v5,13312
Tv 44,v5,19456
Mavv 84,30,120
E_ 86
Mavv 88,30,124
E_ 87
Mavv 108,34,124
C_ 104,84,85
C_ 105,86,87
Mavv 112,34,128
E_ 88
Mavv 92,30,128
E_ 89
Mavv 96,30,132
E_ 90
Mavv 116,34,132
W_ 0
E_ 91
Mavv 136,38,132
s_nop 0
Tv 30,v2,14336
Tv 32,v2,20480
Mavv 156,42,132
C_ 108,88,89
C_ 109,90,91
Mavv 152,42,128
s_nop 0
Tv 34,v3,14336
Tv 36,v3,20480
Mavv 132,38,128
E_ 92
Mavv 128,38,124
E_ 93
Mavv 148,42,124
E_ 94
Mavv 144,42,120
E_ 95
Mavv 124,38,120
C_ 112,92,93
C_ 113,94,95
Mavv 120,38,116
v_mov_b32 v92, 0x3f803f80
v_mov_b32 v93, 0x3f803f80
Mavv 140,42,116
W_ 0
v_mov_b32 v94, 0x3f803f80
v_mov_b32 v95, 0x3f803f80
Mavv 160,30,116
s_nop 0
Tv 38,v4,14336
Tv 40,v4,20480
Mavv 180,34,116
Mavv 184,34,120
s_nop 0
Tv 42,v5,14336
Tv 44,v5,20480
Mavv 164,30,120
Mavv 168,30,124
Mavv 188,34,124
S_ s65,s61,49152
Mavv 192,34,128
S_ m0,s51,12288
Mavv 172,30,128
s_waitcnt vmcnt(6) lgkmcnt(0)
s_barrier
Mavv 176,30,132
Ln v7,32,s65,0
Mavv 196,34,132
Mavv 216,38,132
Dv 30,%0,30720
Mavv 236,42,132
Dv 34,v1,30720
Mavv 232,42,128
Ln v8,32,s65,3072
S_ m0,s51,13184
Mavv 212,38,128
Mavv 208,38,124
Ln v7,32,s65,128
Mavv 228,42,124
Mavv 224,42,120
Ln v8,32,s65,3200
S_ m0,s51,14080
Mavv 204,38,120
Mavv 200,38,116
Ln v7,32,s65,256
Mavv 220,42,116
s_cmp_eq_u32 s4, 0
s_cbranch_scc1 .Lhs_a2
.Lhs_a2_back:
.Lstep2_3:
Zvvv 76,92,96
Dv 38,%0,31744
Zvvv 80,92,100
Dv 42,v1,31744
Zvvv 84,92,104
Zvvv 88,92,108
Zvvv 92,92,112
W_ 2
Nvvv 116,30,136,56
Mvvv 116,34,156
Nvvv 120,30,140,60
s_nop 1
v_max3_f32 v10, v76, v80, v84
s_nop 0
v_max3_f32 v10, v10, v88, v92
Mvvv 120,34,160
v_cmp_lt_f32 vcc, 0x42000000, v10
s_cbranch_vccnz .Ltramp2_2
Nvvv 124,30,144,64
A_ v46,v46,v76
A_ v47,v47,v80
Mvvv 124,34,164
A_ v48,v48,v84
A_ v49,v49,v88
Nvvv 128,30,148,68
A_ v50,v50,v92
Mvvv 128,34,168
Ln v8,32,s65,3328
Nvvv 132,30,152,72
Mvvv 132,34,172
W_ 0
Mvvv 132,38,192
Dv 30,%0,32768
Mvvv 132,42,212
Dv 34,v1,32768
Mvvv 128,38,188
Mvvv 128,42,208
Mvvv 124,38,184
Mvvv 124,42,204
Mvvv 120,38,180
Mvvv 120,42,200
Mvvv 116,38,176
Mvvv 116,42,196
W_ 0
Mvvv 116,30,216
Dv 38,%0,24576
Mvvv 116,34,236
Dv 42,v1,24576
Mvvv 120,30,220
Mvvv 120,34,240
Mvvv 124,30,224
Mvvv 124,34,244
Mvvv 128,30,228
Mvvv 128,34,248
s_nop 0
E_ 116
Mvvv 132,30,232
E_ 117
Mvvv 132,34,252
W_ 0
E_ 118
Nvvv 76,38,136,56
Dv 30,%0,25600
Mvvv 76,42,156
.Lpad_a2_2:
s_cbranch_execz .Lpad_a2_6
Dv 34,v1,25600
Nvvv 80,38,140,60
E_ 119
Mvvv 80,42,160
C_ 119,118,119
C_ 118,116,117
Nvvv 84,38,144,64
E_ 120
Mvvv 84,42,164
E_ 121
Nvvv 88,38,148,68
E_ 122
Mvvv 88,42,168
E_ 123
Nvvv 92,38,152,72
C_ 123,122,123
C_ 122,120,121
Mvvv 92,42,172
W_ 0
E_ 124
Mvvv 92,30,192
Dv 38,%0,26624
Mvvv 92,34,212
Dv 42,v1,26624
Mvvv 88,30,188
E_ 125
Mvvv 88,34,208
E_ 126
Mvvv 84,30,184
S_ s65,s61,36864
E_ 127
Mvvv 84,34,204
S_ m0,s51,24576
C_ 127,126,127
C_ 126,124,125
Mvvv 80,30,180
s_waitcnt vmcnt(6) lgkmcnt(0)
s_barrier
Mvvv 80,34,200
L_ v9,36,s65,0
Mvvv 76,30,176
E_ 128
Mvvv 76,34,196
L_ v9,36,s65,3072
S_ m0,s51,25472
Mvvv 76,38,216
Tv 30,v2,0
Tv 32,v2,6144
Mvvv 76,42,236
L_ v9,36,s65,128
Mvvv 80,38,220
s_nop 0
Tv 34,v3,0
Tv 36,v3,6144
Mvvv 80,42,240
L_ v9,36,s65,3200
S_ m0,s51,26368
Mvvv 84,38,224
E_ 129
Mvvv 84,42,244
L_ v9,36,s65,256
Mvvv 88,38,228
E_ 130
Mvvv 88,42,248
L_ v9,36,s65,3328
Mvvv 92,38,232
E_ 131
Mvvv 92,42,252
W_ 0
C_ 131,130,131
C_ 130,128,129
Mavv 0,30,96
s_nop 0
Tv 38,v4,0
Tv 40,v4,6144
Mavv 20,34,96
E_ 132
Mavv 24,34,100
s_nop 0
Tv 42,v5,0
Tv 44,v5,6144
Mavv 4,30,100
E_ 133
Mavv 8,30,104
E_ 134
Mavv 28,34,104
E_ 135
Mavv 32,34,108
C_ 135,134,135
C_ 134,132,133
Mavv 12,30,108
E_ 76
Mavv 16,30,112
E_ 77
Mavv 36,34,112
W_ 0
E_ 78
Mavv 56,38,112
s_nop 0
Tv 30,v2,1024
Tv 32,v2,7168
Mavv 76,42,112
E_ 79
Mavv 72,42,108
s_nop 0
Tv 34,v3,1024
Tv 36,v3,7168
Mavv 52,38,108
C_ 116,76,77
C_ 117,78,79
Mavv 48,38,104
E_ 80
Mavv 68,42,104
E_ 81
Mavv 64,42,100
E_ 82
Mavv 44,38,100
E_ 83
Mavv 40,38,96
C_ 120,80,81
C_ 121,82,83
Mavv 60,42,96
W_ 0
E_ 84
Mavv 80,30,96
s_nop 0
Tv 38,v4,1024
Tv 40,v4,7168
Mavv 100,34,96
E_ 85
Mavv 104,34,100
s_nop 0
Tv 42,v5,1024
Tv 44,v5,7168
Mavv 84,30,100
E_ 86
Mavv 88,30,104
E_ 87
Mavv 108,34,104
C_ 124,84,85
C_ 125,86,87
Mavv 112,34,108
E_ 88
Mavv 92,30,108
E_ 89
Mavv 96,30,112
E_ 90
Mavv 116,34,112
W_ 0
E_ 91
Mavv 136,38,112
s_nop 0
Tv 30,v2,2048
Tv 32,v2,8192
Mavv 156,42,112
C_ 128,88,89
C_ 129,90,91
Mavv 152,42,108
s_nop 0
Tv 34,v3,2048
Tv 36,v3,8192
Mavv 132,38,108
E_ 92
Mavv 128,38,104
E_ 93
Mavv 148,42,104
E_ 94
Mavv 144,42,100
E_ 95
Mavv 124,38,100
C_ 132,92,93
C_ 133,94,95
Mavv 120,38,96
v_mov_b32 v92, 0x3f803f80
v_mov_b32 v93, 0x3f803f80
Mavv 140,42,96
W_ 0
v_mov_b32 v94, 0x3f803f80
v_mov_b32 v95, 0x3f803f80
Mavv 160,30,96
s_nop 0
Tv 38,v4,2048
Tv 40,v4,8192
Mavv 180,34,96
Mavv 184,34,100
s_nop 0
Tv 42,v5,2048
Tv 44,v5,8192
Mavv 164,30,100
Mavv 168,30,104
Mavv 188,34,104
S_ s65,s61,61440
Mavv 192,34,108
S_ m0,s51,0
Mavv 172,30,108
s_waitcnt vmcnt(6) lgkmcnt(0)
s_barrier
Mavv 176,30,112
Ln v7,32,s65,0
Mavv 196,34,112
Mavv 216,38,112
Dv 30,%0,18432
Mavv 236,42,112
Dv 34,v1,18432
Mavv 232,42,108
Ln v8,32,s65,3072
S_ m0,s51,896
Mavv 212,38,108
Mavv 208,38,104
Ln v7,32,s65,128
Mavv 228,42,104
Mavv 224,42,100
Ln v8,32,s65,3200
S_ m0,s51,1792
Mavv 204,38,100
Mavv 200,38,96
Ln v7,32,s65,256
Mavv 220,42,96
.Lstep2_4:
Zvvv 76,92,116
Dv 38,%0,19456
Zvvv 80,92,120
Dv 42,v1,19456
Zvvv 84,92,124
Zvvv 88,92,128
Zvvv 92,92,132
W_ 2
Nvvv 96,30,136,56
Mvvv 96,34,156
Nvvv 100,30,140,60
s_nop 1
v_max3_f32 v10, v76, v80, v84
s_nop 0
v_max3_f32 v10, v10, v88, v92
Mvvv 100,34,160
v_cmp_lt_f32 vcc, 0x42000000, v10
s_cbranch_vccnz .Ltramp2_3
Nvvv 104,30,144,64
A_ v46,v46,v76
A_ v47,v47,v80
Mvvv 104,34,164
A_ v48,v48,v84
A_ v49,v49,v88
Nvvv 108,30,148,68
A_ v50,v50,v92
Mvvv 108,34,168
Ln v8,32,s65,3328
Nvvv 112,30,152,72
Mvvv 112,34,172
W_ 0
Mvvv 112,38,192
Dv 30,%0,20480
Mvvv 112,42,212
Dv 34,v1,20480
Mvvv 108,38,188
Mvvv 108,42,208
Mvvv 104,38,184
Mvvv 104,42,204
Mvvv 100,38,180
Mvvv 100,42,200
Mvvv 96,38,176
Mvvv 96,42,196
W_ 0
Mvvv 96,30,216
Dv 38,%0,12288
Mvvv 96,34,236
Dv 42,v1,12288
Mvvv 100,30,220
Mvvv 100,34,240
Mvvv 104,30,224
Mvvv 104,34,244
Mvvv 108,30,228
Mvvv 108,34,248
s_nop 0
E_ 96
Mvvv 112,30,232
E_ 97
Mvvv 112,34,252
W_ 0
E_ 98
Nvvv 76,38,136,56
Dv 30,%0,13312
Mvvv 76,42,156
Dv 34,v1,13312
Nvvv 80,38,140,60
E_ 99
Mvvv 80,42,160
C_ 99,98,99
C_ 98,96,97
Nvvv 84,38,144,64
E_ 100
Mvvv 84,42,164
E_ 101
Nvvv 88,38,148,68
E_ 102
Mvvv 88,42,168
E_ 103
Nvvv 92,38,152,72
C_ 103,102,103
C_ 102,100,101
Mvvv 92,42,172
W_ 0
E_ 104
Mvvv 92,30,192
Dv 38,%0,14336
Mvvv 92,34,212
Dv 42,v1,14336
Mvvv 88,30,188
E_ 105
Mvvv 88,34,208
E_ 106
Mvvv 84,30,184
S_ s65,s61,49152
E_ 107
Mvvv 84,34,204
S_ m0,s51,12288
C_ 107,106,107
C_ 106,104,105
Mvvv 80,30,180
s_waitcnt vmcnt(6) lgkmcnt(0)
s_barrier
Mvvv 80,34,200
L_ v9,36,s65,0
Mvvv 76,30,176
E_ 108
Mvvv 76,34,196
L_ v9,36,s65,3072
S_ m0,s51,13184
Mvvv 76,38,216
Tv 30,v2,24576
Tv 32,v2,30720
Mvvv 76,42,236
L_ v9,36,s65,128
Mvvv 80,38,220
s_nop 0
Tv 34,v3,24576
Tv 36,v3,30720
Mvvv 80,42,240
L_ v9,36,s65,3200
S_ m0,s51,14080
Mvvv 84,38,224
E_ 109
Mvvv 84,42,244
L_ v9,36,s65,256
Mvvv 88,38,228
E_ 110
Mvvv 88,42,248
L_ v9,36,s65,3328
Mvvv 92,38,232
E_ 111
Mvvv 92,42,252
W_ 0
C_ 111,110,111
C_ 110,108,109
Mavv 0,30,116
s_nop 0
Tv 38,v4,24576
Tv 40,v4,30720
Mavv 20,34,116
E_ 112
Mavv 24,34,120
s_nop 0
Tv 42,v5,24576
Tv 44,v5,30720
Mavv 4,30,120
E_ 113
Mavv 8,30,124
E_ 114
Mavv 28,34,124
E_ 115
Mavv 32,34,128
C_ 115,114,115
C_ 114,112,113
Mavv 12,30,128
E_ 76
Mavv 16,30,132
E_ 77
Mavv 36,34,132
W_ 0
E_ 78
Mavv 56,38,132
s_nop 0
Tv 30,v2,25600
Tv 32,v2,31744
Mavv 76,42,132
E_ 79
Mavv 72,42,128
s_nop 0
Tv 34,v3,25600
Tv 36,v3,31744
Mavv 52,38,128
C_ 96,76,77
C_ 97,78,79
Mavv 48,38,124
E_ 80
Mavv 68,42,124
E_ 81
Mavv 64,42,120
E_ 82
Mavv 44,38,120
E_ 83
Mavv 40,38,116
C_ 100,80,81
C_ 101,82,83
Mavv 60,42,116
W_ 0
E_ 84
Mavv 80,30,116
s_nop 0
Tv 38,v4,25600
Tv 40,v4,31744
Mavv 100,34,116
E_ 85
Mavv 104,34,120
s_nop 0
Tv 42,v5,25600
Tv 44,v5,31744
Mavv 84,30,120
E_ 86
Mavv 88,30,124
E_ 87
Mavv 108,34,124
C_ 104,84,85
C_ 105,86,87
Mavv 112,34,128
E_ 88
Mavv 92,30,128
E_ 89
Mavv 96,30,132
E_ 90
Mavv 116,34,132
W_ 0
E_ 91
Mavv 136,38,132
s_nop 0
Tv 30,v2,26624
Tv 32,v2,32768
Mavv 156,42,132
C_ 108,88,89
C_ 109,90,91
Mavv 152,42,128
s_nop 0
Tv 34,v3,26624
Tv 36,v3,32768
Mavv 132,38,128
E_ 92
Mavv 128,38,124
E_ 93
Mavv 148,42,124
E_ 94
Mavv 144,42,120
E_ 95
Mavv 124,38,120
C_ 112,92,93
C_ 113,94,95
Mavv 120,38,116
v_mov_b32 v92, 0x3f803f80
v_mov_b32 v93, 0x3f803f80
Mavv 140,42,116
W_ 0
v_mov_b32 v94, 0x3f803f80
v_mov_b32 v95, 0x3f803f80
Mavv 160,30,116
s_nop 0
Tv 38,v4,26624
Tv 40,v4,32768
Mavv 180,34,116
Mavv 184,34,120
s_nop 0
Tv 42,v5,26624
Tv 44,v5,32768
Mavv 164,30,120
Mavv 168,30,124
Mavv 188,34,124
S_ s65,s61,73728
Mavv 192,34,128
S_ m0,s51,24576
Mavv 172,30,128
s_waitcnt vmcnt(6) lgkmcnt(0)
s_barrier
Mavv 176,30,132
Ln v7,32,s65,0
Mavv 196,34,132
Mavv 216,38,132
Dv 30,%0,6144
Mavv 236,42,132
Dv 34,v1,6144
Mavv 232,42,128
Ln v8,32,s65,3072
S_ m0,s51,25472
Mavv 212,38,128
Mavv 208,38,124
Ln v7,32,s65,128
Mavv 228,42,124
Mavv 224,42,120
Ln v8,32,s65,3200
S_ m0,s51,26368
Mavv 204,38,120
Mavv 200,38,116
Ln v7,32,s65,256
Mavv 220,42,116
.Lstep2_5:
Zvvv 76,92,96
Dv 38,%0,7168
Zvvv 80,92,100
Dv 42,v1,7168
Zvvv 84,92,104
Zvvv 88,92,108
Zvvv 92,92,112
W_ 2
Nvvv 116,30,136,56
Mvvv 116,34,156
Nvvv 120,30,140,60
s_nop 1
v_max3_f32 v10, v76, v80, v84
s_nop 0
v_max3_f32 v10, v10, v88, v92
Mvvv 120,34,160
v_cmp_lt_f32 vcc, 0x42000000, v10
s_cbranch_vccnz .Ltramp2_4
Nvvv 124,30,144,64
A_ v46,v46,v76
A_ v47,v47,v80
Mvvv 124,34,164
A_ v48,v48,v84
A_ v49,v49,v88
Nvvv 128,30,148,68
A_ v50,v50,v92
Mvvv 128,34,168
Ln v8,32,s65,3328
Nvvv 132,30,152,72
Mvvv 132,34,172
W_ 0
Mvvv 132,38,192
Dv 30,%0,8192
Mvvv 132,42,212
Dv 34,v1,8192
Mvvv 128,38,188
Mvvv 128,42,208
Mvvv 124,38,184
Mvvv 124,42,204
Mvvv 120,38,180
Mvvv 120,42,200
Mvvv 116,38,176
Mvvv 116,42,196
W_ 0
Mvvv 116,30,216
Dv 38,%0,0
Mvvv 116,34,236
Dv 42,v1,0
Mvvv 120,30,220
Mvvv 120,34,240
Mvvv 124,30,224
Mvvv 124,34,244
Mvvv 128,30,228
Mvvv 128,34,248
s_nop 0
E_ 116
Mvvv 132,30,232
E_ 117
Mvvv 132,34,252
W_ 0
E_ 118
Nvvv 76,38,136,56
Dv 30,%0,1024
Mvvv 76,42,156
Dv 34,v1,1024
Nvvv 80,38,140,60
E_ 119
Mvvv 80,42,160
C_ 119,118,119
C_ 118,116,117
Nvvv 84,38,144,64
E_ 120
Mvvv 84,42,164
E_ 121
Nvvv 88,38,148,68
E_ 122
Mvvv 88,42,168
E_ 123
Nvvv 92,38,152,72
C_ 123,122,123
C_ 122,120,121
Mvvv 92,42,172
W_ 0
E_ 124
Mvvv 92,30,192
Dv 38,%0,2048
Mvvv 92,34,212
Dv 42,v1,2048
Mvvv 88,30,188
E_ 125
Mvvv 88,34,208
.Lpad_a2_3:
s_cbranch_execz .Lpad_a2_7
E_ 126
Mvvv 84,30,184
S_ s65,s61,61440
E_ 127
Mvvv 84,34,204
S_ m0,s51,0
C_ 127,126,127
C_ 126,124,125
Mvvv 80,30,180
s_waitcnt vmcnt(6) lgkmcnt(0)
s_barrier
Mvvv 80,34,200
L_ v9,36,s65,0
Mvvv 76,30,176
E_ 128
Mvvv 76,34,196
L_ v9,36,s65,3072
S_ m0,s51,896
Mvvv 76,38,216
Tv 30,v2,12288
Tv 32,v2,18432
Mvvv 76,42,236
L_ v9,36,s65,128
Mvvv 80,38,220
s_nop 0
Tv 34,v3,12288
Tv 36,v3,18432
Mvvv 80,42,240
L_ v9,36,s65,3200
S_ m0,s51,1792
Mvvv 84,38,224
E_ 129
Mvvv 84,42,244
L_ v9,36,s65,256
Mvvv 88,38,228
E_ 130
Mvvv 88,42,248
L_ v9,36,s65,3328
Mvvv 92,38,232
E_ 131
Mvvv 92,42,252
W_ 0
C_ 131,130,131
C_ 130,128,129
Mavv 0,30,96
s_nop 0
Tv 38,v4,12288
Tv 40,v4,18432
Mavv 20,34,96
E_ 132
Mavv 24,34,100
s_nop 0
Tv 42,v5,12288
Tv 44,v5,18432
Mavv 4,30,100
E_ 133
Mavv 8,30,104
E_ 134
Mavv 28,34,104
E_ 135
Mavv 32,34,108
C_ 135,134,135
C_ 134,132,133
Mavv 12,30,108
E_ 76
Mavv 16,30,112
E_ 77
Mavv 36,34,112
W_ 0
E_ 78
Mavv 56,38,112
s_nop 0
Tv 30,v2,13312
Tv 32,v2,19456
Mavv 76,42,112
E_ 79
Mavv 72,42,108
s_nop 0
Tv 34,v3,13312
Tv 36,v3,19456
Mavv 52,38,108
C_ 116,76,77
C_ 117,78,79
Mavv 48,38,104
E_ 80
Mavv 68,42,104
E_ 81
Mavv 64,42,100
E_ 82
Mavv 44,38,100
E_ 83
Mavv 40,38,96
C_ 120,80,81
C_ 121,82,83
Mavv 60,42,96
W_ 0
E_ 84
Mavv 80,30,96
s_nop 0
Tv 38,v4,13312
Tv 40,v4,19456
Mavv 100,34,96
E_ 85
Mavv 104,34,100
s_nop 0
Tv 42,v5,13312
Tv 44,v5,19456
Mavv 84,30,100
E_ 86
Mavv 88,30,104
E_ 87
Mavv 108,34,104
C_ 124,84,85
C_ 125,86,87
Mavv 112,34,108
E_ 88
Mavv 92,30,108
E_ 89
Mavv 96,30,112
E_ 90
Mavv 116,34,112
W_ 0
E_ 91
Mavv 136,38,112
s_nop 0
Tv 30,v2,14336
Tv 32,v2,20480
Mavv 156,42,112
C_ 128,88,89
C_ 129,90,91
Mavv 152,42,108
s_nop 0
Tv 34,v3,14336
Tv 36,v3,20480
Mavv 132,38,108
E_ 92
Mavv 128,38,104
E_ 93
Mavv 148,42,104
E_ 94
Mavv 144,42,100
E_ 95
Mavv 124,38,100
C_ 132,92,93
C_ 133,94,95
Mavv 120,38,96
v_mov_b32 v92, 0x3f803f80
v_mov_b32 v93, 0x3f803f80
Mavv 140,42,96
W_ 0
v_mov_b32 v94, 0x3f803f80
v_mov_b32 v95, 0x3f803f80
Mavv 160,30,96
s_nop 0
Tv 38,v4,14336
Tv 40,v4,20480
Mavv 180,34,96
Mavv 184,34,100
s_nop 0
Tv 42,v5,14336
Tv 44,v5,20480
Mavv 164,30,100
Mavv 168,30,104
Mavv 188,34,104
S_ s65,s61,86016
Mavv 192,34,108
S_ m0,s51,12288
Mavv 172,30,108
s_waitcnt vmcnt(6) lgkmcnt(0)
s_barrier
Mavv 176,30,112
Ln v7,32,s65,0
Mavv 196,34,112
Mavv 216,38,112
Dv 30,%0,30720
Mavv 236,42,112
Dv 34,v1,30720
Mavv 232,42,108
Ln v8,32,s65,3072
S_ m0,s51,13184
Mavv 212,38,108
Mavv 208,38,104
Ln v7,32,s65,128
Mavv 228,42,104
Mavv 224,42,100
Ln v8,32,s65,3200
S_ m0,s51,14080
Mavv 204,38,100
Mavv 200,38,96
Ln v7,32,s65,256
Mavv 220,42,96
.Lstep2_6:
Zvvv 76,92,116
Dv 38,%0,31744
Zvvv 80,92,120
Dv 42,v1,31744
Zvvv 84,92,124
Zvvv 88,92,128
Zvvv 92,92,132
W_ 2
Nvvv 96,30,136,56
Mvvv 96,34,156
Nvvv 100,30,140,60
s_nop 1
v_max3_f32 v10, v76, v80, v84
s_nop 0
v_max3_f32 v10, v10, v88, v92
Mvvv 100,34,160
v_cmp_lt_f32 vcc, 0x42000000, v10
s_cbranch_vccnz .Ltramp2_5
Nvvv 104,30,144,64
A_ v46,v46,v76
A_ v47,v47,v80
Mvvv 104,34,164
A_ v48,v48,v84
A_ v49,v49,v88
Nvvv 108,30,148,68
A_ v50,v50,v92
Mvvv 108,34,168
Ln v8,32,s65,3328
Nvvv 112,30,152,72
R_ v11,a248
Mvvv 112,34,172
W_ 0
v_min_u32 v11, 240, v11
Mvvv 112,38,192
Dv 30,%0,32768
v_lshlrev_b32 v11, 3, v11
Mvvv 112,42,212
Dv 34,v1,32768
Mvvv 108,38,188
buffer_load_dword a254, v11, s[44:47], s66 offen
Mvvv 108,42,208
Mvvv 104,38,184
Mvvv 104,42,204
Mvvv 100,38,180
Mvvv 100,42,200
Mvvv 96,38,176
Mvvv 96,42,196
W_ 0
Mvvv 96,30,216
Dv 38,%0,24576
Mvvv 96,34,236
Dv 42,v1,24576
Mvvv 100,30,220
Mvvv 100,34,240
Mvvv 104,30,224
Mvvv 104,34,244
Mvvv 108,30,228
Mvvv 108,34,248
s_nop 0
E_ 96
Mvvv 112,30,232
E_ 97
Mvvv 112,34,252
W_ 0
E_ 98
Nvvv 76,38,136,56
Dv 30,%0,25600
Mvvv 76,42,156
Dv 34,v1,25600
Nvvv 80,38,140,60
E_ 99
Mvvv 80,42,160
C_ 99,98,99
C_ 98,96,97
Nvvv 84,38,144,64
E_ 100
Mvvv 84,42,164
E_ 101
Nvvv 88,38,148,68
E_ 102
Mvvv 88,42,168
E_ 103
Nvvv 92,38,152,72
C_ 103,102,103
C_ 102,100,101
Mvvv 92,42,172
W_ 0
E_ 104
Mvvv 92,30,192
Dv 38,%0,26624
Mvvv 92,34,212
Dv 42,v1,26624
Mvvv 88,30,188
E_ 105
Mvvv 88,34,208
E_ 106
Mvvv 84,30,184
S_ s65,s61,73728
E_ 107
Mvvv 84,34,204
S_ m0,s51,24576
C_ 107,106,107
C_ 106,104,105
Mvvv 80,30,180
s_waitcnt vmcnt(7) lgkmcnt(0)
s_barrier
Mvvv 80,34,200
L_ v9,36,s65,0
Mvvv 76,30,176
E_ 108
Mvvv 76,34,196
L_ v9,36,s65,3072
S_ m0,s51,25472
Mvvv 76,38,216
Tv 30,v2,0
Tv 32,v2,6144
Mvvv 76,42,236
L_ v9,36,s65,128
Mvvv 80,38,220
s_nop 0
Tv 34,v3,0
Tv 36,v3,6144
Mvvv 80,42,240
L_ v9,36,s65,3200
S_ m0,s51,26368
Mvvv 84,38,224
E_ 109
Mvvv 84,42,244
L_ v9,36,s65,256
Mvvv 88,38,228
E_ 110
Mvvv 88,42,248
L_ v9,36,s65,3328
Mvvv 92,38,232
E_ 111
Mvvv 92,42,252
W_ 0
C_ 111,110,111
C_ 110,108,109
Mavv 0,30,116
s_nop 0
Tv 38,v4,0
Tv 40,v4,6144
Mavv 20,34,116
E_ 112
Mavv 24,34,120
s_nop 0
Tv 42,v5,0
Tv 44,v5,6144
Mavv 4,30,120
E_ 113
Mavv 8,30,124
E_ 114
Mavv 28,34,124
E_ 115
Mavv 32,34,128
C_ 115,114,115
C_ 114,112,113
Mavv 12,30,128
E_ 76
Mavv 16,30,132
E_ 77
Mavv 36,34,132
W_ 0
E_ 78
Mavv 56,38,132
s_nop 0
Tv 30,v2,1024
Tv 32,v2,7168
Mavv 76,42,132
E_ 79
Mavv 72,42,128
s_nop 0
Tv 34,v3,1024
Tv 36,v3,7168
Mavv 52,38,128
C_ 96,76,77
C_ 97,78,79
Mavv 48,38,124
E_ 80
Mavv 68,42,124
E_ 81
Mavv 64,42,120
E_ 82
Mavv 44,38,120
E_ 83
Mavv 40,38,116
C_ 100,80,81
C_ 101,82,83
Mavv 60,42,116
W_ 0
E_ 84
Mavv 80,30,116
s_nop 0
Tv 38,v4,1024
Tv 40,v4,7168
Mavv 100,34,116
E_ 85
Mavv 104,34,120
s_nop 0
Tv 42,v5,1024
Tv 44,v5,7168
Mavv 84,30,120
E_ 86
Mavv 88,30,124
E_ 87
Mavv 108,34,124
C_ 104,84,85
C_ 105,86,87
Mavv 112,34,128
E_ 88
Mavv 92,30,128
E_ 89
Mavv 96,30,132
E_ 90
Mavv 116,34,132
W_ 0
E_ 91
Mavv 136,38,132
s_nop 0
Tv 30,v2,2048
Tv 32,v2,8192
Mavv 156,42,132
C_ 108,88,89
C_ 109,90,91
Mavv 152,42,128
s_nop 0
Tv 34,v3,2048
Tv 36,v3,8192
Mavv 132,38,128
E_ 92
Mavv 128,38,124
E_ 93
Mavv 148,42,124
E_ 94
Mavv 144,42,120
E_ 95
Mavv 124,38,120
C_ 112,92,93
C_ 113,94,95
Mavv 120,38,116
v_mov_b32 v92, 0x3f803f80
v_mov_b32 v93, 0x3f803f80
Mavv 140,42,116
W_ 0
v_mov_b32 v94, 0x3f803f80
v_mov_b32 v95, 0x3f803f80
Mavv 160,30,116
s_nop 0
Tv 38,v4,2048
Tv 40,v4,8192
Mavv 180,34,116
Mavv 184,34,120
s_nop 0
Tv 42,v5,2048
Tv 44,v5,8192
Mavv 164,30,120
Mavv 168,30,124
Mavv 188,34,124
S_ s65,s61,98304
Mavv 192,34,128
S_ m0,s51,0
Mavv 172,30,128
s_waitcnt vmcnt(7) lgkmcnt(0)
s_barrier
Mavv 176,30,132
Ln v7,32,s65,0
Mavv 196,34,132
Mavv 216,38,132
Dv 30,%0,18432
Mavv 236,42,132
Dv 34,v1,18432
Mavv 232,42,128
Ln v8,32,s65,3072
S_ m0,s51,896
Mavv 212,38,128
Mavv 208,38,124
Ln v7,32,s65,128
Mavv 228,42,124
Mavv 224,42,120
Ln v8,32,s65,3200
S_ m0,s51,1792
Mavv 204,38,120
Mavv 200,38,116
Ln v7,32,s65,256
Mavv 220,42,116
s_cmp_eq_u32 s4, 0
s_cbranch_scc1 .Lhs_c2
.Lhs_c2_back:
.Lstep2_7:
Zvvv 76,92,96
Dv 38,%0,19456
Zvvv 80,92,100
Dv 42,v1,19456
Zvvv 84,92,104
Zvvv 88,92,108
Zvvv 92,92,112
W_ 2
Nvvv 116,30,136,56
Mvvv 116,34,156
Nvvv 120,30,140,60
s_nop 1
v_max3_f32 v10, v76, v80, v84
s_nop 0
v_max3_f32 v10, v10, v88, v92
Mvvv 120,34,160
v_cmp_lt_f32 vcc, 0x42000000, v10
s_cbranch_vccnz .Ltramp2_6
Nvvv 124,30,144,64
A_ v46,v46,v76
A_ v47,v47,v80
Mvvv 124,34,164
A_ v48,v48,v84
A_ v49,v49,v88
Nvvv 128,30,148,68
A_ v50,v50,v92
Mvvv 128,34,168
Ln v8,32,s65,3328
Nvvv 132,30,152,72
Mvvv 132,34,172
W_ 0
Mvvv 132,38,192
Dv 30,%0,20480
Mvvv 132,42,212
Dv 34,v1,20480
Mvvv 128,38,188
Mvvv 128,42,208
Mvvv 124,38,184
Mvvv 124,42,204
Mvvv 120,38,180
Mvvv 120,42,200
Mvvv 116,38,176
Mvvv 116,42,196
W_ 0
Mvvv 116,30,216
Dv 38,%0,12288
Mvvv 116,34,236
Dv 42,v1,12288
Mvvv 120,30,220
Mvvv 120,34,240
Mvvv 124,30,224
Mvvv 124,34,244
Mvvv 128,30,228
Mvvv 128,34,248
s_nop 0
E_ 116
Mvvv 132,30,232
E_ 117
Mvvv 132,34,252
W_ 0
E_ 118
Nvvv 76,38,136,56
Dv 30,%0,13312
Mvvv 76,42,156
Dv 34,v1,13312
Nvvv 80,38,140,60
E_ 119
Mvvv 80,42,160
C_ 119,118,119
C_ 118,116,117
Nvvv 84,38,144,64
E_ 120
Mvvv 84,42,164
E_ 121
Nvvv 88,38,148,68
E_ 122
Mvvv 88,42,168
E_ 123
Nvvv 92,38,152,72
C_ 123,122,123
C_ 122,120,121
Mvvv 92,42,172
W_ 0
E_ 124
Mvvv 92,30,192
Dv 38,%0,14336
Mvvv 92,34,212
Dv 42,v1,14336
Mvvv 88,30,188
E_ 125
Mvvv 88,34,208
E_ 126
Mvvv 84,30,184
S_ s65,s61,86016
E_ 127
Mvvv 84,34,204
S_ m0,s51,12288
C_ 127,126,127
C_ 126,124,125
Mvvv 80,30,180
s_waitcnt vmcnt(6) lgkmcnt(0)
s_barrier
Mvvv 80,34,200
L_ v9,36,s65,0
Mvvv 76,30,176
E_ 128
Mvvv 76,34,196
L_ v9,36,s65,3072
S_ m0,s51,13184
Mvvv 76,38,216
Tv 30,v2,24576
Tv 32,v2,30720
Mvvv 76,42,236
L_ v9,36,s65,128
Mvvv 80,38,220
.Lpad_a2_4:
s_cbranch_execz .Lhop_a2_r
Tv 34,v3,24576
Tv 36,v3,30720
Mvvv 80,42,240
L_ v9,36,s65,3200
S_ m0,s51,14080
Mvvv 84,38,224
E_ 129
Mvvv 84,42,244
L_ v9,36,s65,256
Mvvv 88,38,228
E_ 130
Mvvv 88,42,248
L_ v9,36,s65,3328
Mvvv 92,38,232
E_ 131
Mvvv 92,42,252
W_ 0
C_ 131,130,131
C_ 130,128,129
Mavv 0,30,96
s_nop 0
Tv 38,v4,24576
Tv 40,v4,30720
Mavv 20,34,96
E_ 132
Mavv 24,34,100
s_nop 0
Tv 42,v5,24576
Tv 44,v5,30720
Mavv 4,30,100
E_ 133
Mavv 8,30,104
E_ 134
Mavv 28,34,104
E_ 135
Mavv 32,34,108
C_ 135,134,135
C_ 134,132,133
Mavv 12,30,108
E_ 76
Mavv 16,30,112
E_ 77
Mavv 36,34,112
W_ 0
E_ 78
Mavv 56,38,112
s_nop 0
Tv 30,v2,25600
Tv 32,v2,31744
Mavv 76,42,112
E_ 79
Mavv 72,42,108
s_nop 0
Tv 34,v3,25600
Tv 36,v3,31744
Mavv 52,38,108
C_ 116,76,77
C_ 117,78,79
Mavv 48,38,104
E_ 80
Mavv 68,42,104
E_ 81
Mavv 64,42,100
E_ 82
Mavv 44,38,100
E_ 83
Mavv 40,38,96
C_ 120,80,81
C_ 121,82,83
Mavv 60,42,96
W_ 0
E_ 84
Mavv 80,30,96
s_nop 0
Tv 38,v4,25600
Tv 40,v4,31744
Mavv 100,34,96
E_ 85
Mavv 104,34,100
s_nop 0
Tv 42,v5,25600
Tv 44,v5,31744
Mavv 84,30,100
E_ 86
Mavv 88,30,104
E_ 87
Mavv 108,34,104
C_ 124,84,85
C_ 125,86,87
Mavv 112,34,108
E_ 88
Mavv 92,30,108
E_ 89
Mavv 96,30,112
E_ 90
Mavv 116,34,112
W_ 0
E_ 91
Mavv 136,38,112
s_nop 0
Tv 30,v2,26624
Tv 32,v2,32768
Mavv 156,42,112
C_ 128,88,89
C_ 129,90,91
Mavv 152,42,108
s_nop 0
Tv 34,v3,26624
Tv 36,v3,32768
Mavv 132,38,108
E_ 92
Mavv 128,38,104
E_ 93
Mavv 148,42,104
E_ 94
Mavv 144,42,100
E_ 95
Mavv 124,38,100
C_ 132,92,93
C_ 133,94,95
Mavv 120,38,96
v_mov_b32 v92, 0x3f803f80
v_mov_b32 v93, 0x3f803f80
Mavv 140,42,96
W_ 0
)A"
R"A(v_mov_b32 v94, 0x3f803f80
v_mov_b32 v95, 0x3f803f80
Mavv 160,30,96
s_nop 0
Tv 38,v4,26624
Tv 40,v4,32768
Mavv 180,34,96
Mavv 184,34,100
s_nop 0
Tv 42,v5,26624
Tv 44,v5,32768
Mavv 164,30,100
Mavv 168,30,104
Mavv 188,34,104
S_ s65,s61,110592
Mavv 192,34,108
S_ m0,s51,24576
Mavv 172,30,108
s_waitcnt vmcnt(6) lgkmcnt(0)
s_barrier
Mavv 176,30,112
Ln v7,32,s65,0
Mavv 196,34,112
Mavv 216,38,112
Dv 30,%0,6144
Mavv 236,42,112
Dv 34,v1,6144
Mavv 232,42,108
Ln v8,32,s65,3072
S_ m0,s51,25472
Mavv 212,38,108
Mavv 208,38,104
Ln v7,32,s65,128
Mavv 228,42,104
Mavv 224,42,100
Ln v8,32,s65,3200
S_ m0,s51,26368
Mavv 204,38,100
Mavv 200,38,96
Ln v7,32,s65,256
Mavv 220,42,96
.Lstep2_8:
Zvvv 76,92,116
Dv 38,%0,7168
Zvvv 80,92,120
Dv 42,v1,7168
Zvvv 84,92,124
Zvvv 88,92,128
Zvvv 92,92,132
W_ 2
Nvvv 96,30,136,56
Mvvv 96,34,156
Nvvv 100,30,140,60
s_nop 1
v_max3_f32 v10, v76, v80, v84
s_nop 0
v_max3_f32 v10, v10, v88, v92
Mvvv 100,34,160
v_cmp_lt_f32 vcc, 0x42000000, v10
s_cbranch_vccnz .Ltramp2_7
Nvvv 104,30,144,64
A_ v46,v46,v76
A_ v47,v47,v80
Mvvv 104,34,164
A_ v48,v48,v84
A_ v49,v49,v88
Nvvv 108,30,148,68
A_ v50,v50,v92
Mvvv 108,34,168
Ln v8,32,s65,3328
Nvvv 112,30,152,72
Mvvv 112,34,172
W_ 0
Mvvv 112,38,192
Dv 30,%0,8192
Mvvv 112,42,212
Dv 34,v1,8192
Mvvv 108,38,188
Mvvv 108,42,208
Mvvv 104,38,184
Mvvv 104,42,204
Mvvv 100,38,180
Mvvv 100,42,200
Mvvv 96,38,176
Mvvv 96,42,196
W_ 0
Mvvv 96,30,216
Dv 38,%0,0
Mvvv 96,34,236
Dv 42,v1,0
Mvvv 100,30,220
Mvvv 100,34,240
Mvvv 104,30,224
Mvvv 104,34,244
Mvvv 108,30,228
Mvvv 108,34,248
s_nop 0
E_ 96
Mvvv 112,30,232
E_ 97
Mvvv 112,34,252
W_ 0
E_ 98
Nvvv 76,38,136,56
Dv 30,%0,1024
Mvvv 76,42,156
Dv 34,v1,1024
Nvvv 80,38,140,60
E_ 99
Mvvv 80,42,160
C_ 99,98,99
C_ 98,96,97
Nvvv 84,38,144,64
E_ 100
Mvvv 84,42,164
E_ 101
Nvvv 88,38,148,68
E_ 102
Mvvv 88,42,168
E_ 103
Nvvv 92,38,152,72
C_ 103,102,103
C_ 102,100,101
Mvvv 92,42,172
W_ 0
E_ 104
Mvvv 92,30,192
Dv 38,%0,2048
Mvvv 92,34,212
Dv 42,v1,2048
Mvvv 88,30,188
E_ 105
Mvvv 88,34,208
E_ 106
Mvvv 84,30,184
S_ s65,s61,98304
E_ 107
Mvvv 84,34,204
S_ m0,s51,0
C_ 107,106,107
C_ 106,104,105
Mvvv 80,30,180
s_waitcnt vmcnt(6) lgkmcnt(0)
s_barrier
Mvvv 80,34,200
L_ v9,36,s65,0
Mvvv 76,30,176
E_ 108
Mvvv 76,34,196
L_ v9,36,s65,3072
S_ m0,s51,896
Mvvv 76,38,216
Tv 30,v2,12288
Tv 32,v2,18432
Mvvv 76,42,236
L_ v9,36,s65,128
Mvvv 80,38,220
s_nop 0
Tv 34,v3,12288
Tv 36,v3,18432
Mvvv 80,42,240
L_ v9,36,s65,3200
S_ m0,s51,1792
Mvvv 84,38,224
E_ 109
Mvvv 84,42,244
L_ v9,36,s65,256
Mvvv 88,38,228
E_ 110
Mvvv 88,42,248
L_ v9,36,s65,3328
Mvvv 92,38,232
E_ 111
Mvvv 92,42,252
W_ 0
C_ 111,110,111
C_ 110,108,109
Mavv 0,30,116
s_nop 0
Tv 38,v4,12288
Tv 40,v4,18432
Mavv 20,34,116
E_ 112
Mavv 24,34,120
s_nop 0
Tv 42,v5,12288
Tv 44,v5,18432
Mavv 4,30,120
E_ 113
Mavv 8,30,124
E_ 114
Mavv 28,34,124
E_ 115
Mavv 32,34,128
C_ 115,114,115
C_ 114,112,113
Mavv 12,30,128
E_ 76
Mavv 16,30,132
E_ 77
Mavv 36,34,132
W_ 0
E_ 78
Mavv 56,38,132
s_nop 0
Tv 30,v2,13312
Tv 32,v2,19456
Mavv 76,42,132
E_ 79
Mavv 72,42,128
s_nop 0
Tv 34,v3,13312
Tv 36,v3,19456
Mavv 52,38,128
C_ 96,76,77
C_ 97,78,79
Mavv 48,38,124
E_ 80
Mavv 68,42,124
E_ 81
Mavv 64,42,120
E_ 82
Mavv 44,38,120
E_ 83
Mavv 40,38,116
C_ 100,80,81
C_ 101,82,83
Mavv 60,42,116
W_ 0
E_ 84
Mavv 80,30,116
s_nop 0
Tv 38,v4,13312
Tv 40,v4,19456
Mavv 100,34,116
E_ 85
Mavv 104,34,120
s_nop 0
Tv 42,v5,13312
Tv 44,v5,19456
Mavv 84,30,120
E_ 86
Mavv 88,30,124
E_ 87
Mavv 108,34,124
C_ 104,84,85
C_ 105,86,87
Mavv 112,34,128
E_ 88
Mavv 92,30,128
E_ 89
Mavv 96,30,132
E_ 90
Mavv 116,34,132
W_ 0
E_ 91
Mavv 136,38,132
s_nop 0
Tv 30,v2,14336
Tv 32,v2,20480
Mavv 156,42,132
C_ 108,88,89
C_ 109,90,91
Mavv 152,42,128
s_nop 0
Tv 34,v3,14336
Tv 36,v3,20480
Mavv 132,38,128
E_ 92
Mavv 128,38,124
E_ 93
Mavv 148,42,124
E_ 94
Mavv 144,42,120
E_ 95
Mavv 124,38,120
C_ 112,92,93
C_ 113,94,95
Mavv 120,38,116
v_mov_b32 v92, 0x3f803f80
v_mov_b32 v93, 0x3f803f80
Mavv 140,42,116
W_ 0
v_mov_b32 v94, 0x3f803f80
v_mov_b32 v95, 0x3f803f80
Mavv 160,30,116
s_nop 0
Tv 38,v4,14336
Tv 40,v4,20480
Mavv 180,34,116
Mavv 184,34,120
s_nop 0
Tv 42,v5,14336
Tv 44,v5,20480
Mavv 164,30,120
Mavv 168,30,124
Mavv 188,34,124
S_ s65,s61,122880
Mavv 192,34,128
S_ m0,s51,12288
Mavv 172,30,128
s_waitcnt vmcnt(6) lgkmcnt(0)
s_barrier
Mavv 176,30,132
Ln v7,32,s65,0
Mavv 196,34,132
Mavv 216,38,132
Dv 30,%0,30720
Mavv 236,42,132
Dv 34,v1,30720
Mavv 232,42,128
Ln v8,32,s65,3072
S_ m0,s51,13184
Mavv 212,38,128
Mavv 208,38,124
Ln v7,32,s65,128
Mavv 228,42,124
Mavv 224,42,120
Ln v8,32,s65,3200
S_ m0,s51,14080
Mavv 204,38,120
Mavv 200,38,116
Ln v7,32,s65,256
Mavv 220,42,116
s_cmp_eq_u32 s4, 0
s_cbranch_scc1 .Lhs_d2
.Lhs_d2_back:
.Lstep2_9:
Zvvv 76,92,96
Dv 38,%0,31744
Zvvv 80,92,100
Dv 42,v1,31744
Zvvv 84,92,104
Zvvv 88,92,108
Zvvv 92,92,112
W_ 2
Nvvv 116,30,136,56
Mvvv 116,34,156
Nvvv 120,30,140,60
s_nop 1
v_max3_f32 v10, v76, v80, v84
s_nop 0
v_max3_f32 v10, v10, v88, v92
Mvvv 120,34,160
v_cmp_lt_f32 vcc, 0x42000000, v10
s_cbranch_vccnz .Ltramp2_8
Nvvv 124,30,144,64
A_ v46,v46,v76
A_ v47,v47,v80
Mvvv 124,34,164
A_ v48,v48,v84
A_ v49,v49,v88
Nvvv 128,30,148,68
A_ v50,v50,v92
Mvvv 128,34,168
Ln v8,32,s65,3328
Nvvv 132,30,152,72
Mvvv 132,34,172
W_ 0
Mvvv 132,38,192
Dv 30,%0,32768
Mvvv 132,42,212
Dv 34,v1,32768
Mvvv 128,38,188
Mvvv 128,42,208
Mvvv 124,38,184
Mvvv 124,42,204
Mvvv 120,38,180
Mvvv 120,42,200
Mvvv 116,38,176
Mvvv 116,42,196
W_ 0
Mvvv 116,30,216
Dv 38,%0,24576
Mvvv 116,34,236
Dv 42,v1,24576
Mvvv 120,30,220
Mvvv 120,34,240
Mvvv 124,30,224
Mvvv 124,34,244
Mvvv 128,30,228
Mvvv 128,34,248
s_nop 0
E_ 116
Mvvv 132,30,232
E_ 117
Mvvv 132,34,252
W_ 0
E_ 118
Nvvv 76,38,136,56
Dv 30,%0,25600
Mvvv 76,42,156
Dv 34,v1,25600
Nvvv 80,38,140,60
E_ 119
Mvvv 80,42,160
C_ 119,118,119
C_ 118,116,117
Nvvv 84,38,144,64
E_ 120
Mvvv 84,42,164
E_ 121
Nvvv 88,38,148,68
E_ 122
Mvvv 88,42,168
E_ 123
Nvvv 92,38,152,72
C_ 123,122,123
C_ 122,120,121
Mvvv 92,42,172
W_ 0
E_ 124
Mvvv 92,30,192
Dv 38,%0,26624
Mvvv 92,34,212
Dv 42,v1,26624
Mvvv 88,30,188
E_ 125
Mvvv 88,34,208
E_ 126
Mvvv 84,30,184
S_ s65,s61,110592
E_ 127
Mvvv 84,34,204
S_ m0,s51,24576
C_ 127,126,127
C_ 126,124,125
Mvvv 80,30,180
s_waitcnt vmcnt(6) lgkmcnt(0)
s_barrier
Mvvv 80,34,200
Ln v9,36,s65,0
Mvvv 76,30,176
E_ 128
Mvvv 76,34,196
Ln v9,36,s65,3072
S_ m0,s51,25472
Mvvv 76,38,216
Tv 30,v2,0
Tv 32,v2,6144
Mvvv 76,42,236
Ln v9,36,s65,128
Mvvv 80,38,220
s_nop 0
Tv 34,v3,0
Tv 36,v3,6144
Mvvv 80,42,240
Ln v9,36,s65,3200
S_ m0,s51,26368
Mvvv 84,38,224
E_ 129
Mvvv 84,42,244
Ln v9,36,s65,256
Mvvv 88,38,228
E_ 130
Mvvv 88,42,248
Ln v9,36,s65,3328
Mvvv 92,38,232
E_ 131
Mvvv 92,42,252
W_ 0
C_ 131,130,131
C_ 130,128,129
Mavv 0,30,96
.Lpad_a2_5:
s_cbranch_execz .Lhop_a2_r
Tv 38,v4,0
Tv 40,v4,6144
Mavv 20,34,96
E_ 132
Mavv 24,34,100
s_nop 0
Tv 42,v5,0
Tv 44,v5,6144
Mavv 4,30,100
E_ 133
Mavv 8,30,104
E_ 134
Mavv 28,34,104
E_ 135
Mavv 32,34,108
C_ 135,134,135
C_ 134,132,133
Mavv 12,30,108
E_ 76
Mavv 16,30,112
E_ 77
Mavv 36,34,112
W_ 0
E_ 78
Mavv 56,38,112
s_nop 0
Tv 30,v2,1024
Tv 32,v2,7168
Mavv 76,42,112
E_ 79
Mavv 72,42,108
s_nop 0
Tv 34,v3,1024
Tv 36,v3,7168
Mavv 52,38,108
C_ 116,76,77
C_ 117,78,79
Mavv 48,38,104
E_ 80
Mavv 68,42,104
E_ 81
Mavv 64,42,100
E_ 82
Mavv 44,38,100
E_ 83
Mavv 40,38,96
C_ 120,80,81
C_ 121,82,83
Mavv 60,42,96
W_ 0
E_ 84
Mavv 80,30,96
s_nop 0
Tv 38,v4,1024
Tv 40,v4,7168
Mavv 100,34,96
E_ 85
Mavv 104,34,100
s_nop 0
Tv 42,v5,1024
Tv 44,v5,7168
Mavv 84,30,100
E_ 86
Mavv 88,30,104
E_ 87
Mavv 108,34,104
C_ 124,84,85
C_ 125,86,87
Mavv 112,34,108
E_ 88
Mavv 92,30,108
E_ 89
Mavv 96,30,112
E_ 90
Mavv 116,34,112
W_ 0
E_ 91
Mavv 136,38,112
s_nop 0
Tv 30,v2,2048
Tv 32,v2,8192
Mavv 156,42,112
C_ 128,88,89
C_ 129,90,91
Mavv 152,42,108
s_nop 0
Tv 34,v3,2048
Tv 36,v3,8192
Mavv 132,38,108
E_ 92
Mavv 128,38,104
E_ 93
Mavv 148,42,104
E_ 94
Mavv 144,42,100
E_ 95
Mavv 124,38,100
C_ 132,92,93
C_ 133,94,95
Mavv 120,38,96
v_mov_b32 v92, 0x3f803f80
v_mov_b32 v93, 0x3f803f80
Mavv 140,42,96
W_ 0
v_mov_b32 v94, 0x3f803f80
v_mov_b32 v95, 0x3f803f80
Mavv 160,30,96
s_nop 0
Tv 38,v4,2048
Tv 40,v4,8192
Mavv 180,34,96
Mavv 184,34,100
s_nop 0
Tv 42,v5,2048
Tv 44,v5,8192
Mavv 164,30,100
Mavv 168,30,104
Mavv 188,34,104
S_ s65,s61,135168
Mavv 192,34,108
S_ m0,s51,0
Mavv 172,30,108
s_waitcnt vmcnt(6) lgkmcnt(0)
s_barrier
Mavv 176,30,112
Ln v7,32,s65,0
Mavv 196,34,112
Mavv 216,38,112
Dv 30,%0,18432
Mavv 236,42,112
Dv 34,v1,18432
Mavv 232,42,108
Ln v8,32,s65,3072
S_ m0,s51,896
Mavv 212,38,108
Mavv 208,38,104
Ln v7,32,s65,128
Mavv 228,42,104
Mavv 224,42,100
Ln v8,32,s65,3200
S_ m0,s51,1792
Mavv 204,38,100
Mavv 200,38,96
Ln v7,32,s65,256
Mavv 220,42,96
.Lstep2_10:
Zvvv 76,92,116
Dv 38,%0,19456
Zvvv 80,92,120
Dv 42,v1,19456
Zvvv 84,92,124
Zvvv 88,92,128
Zvvv 92,92,132
W_ 2
Nvvv 96,30,136,56
Mvvv 96,34,156
Nvvv 100,30,140,60
s_nop 1
v_max3_f32 v10, v76, v80, v84
s_nop 0
v_max3_f32 v10, v10, v88, v92
Mvvv 100,34,160
v_cmp_lt_f32 vcc, 0x42000000, v10
s_cbranch_vccnz .Ltramp2_9
Nvvv 104,30,144,64
A_ v46,v46,v76
A_ v47,v47,v80
Mvvv 104,34,164
A_ v48,v48,v84
A_ v49,v49,v88
Nvvv 108,30,148,68
A_ v50,v50,v92
Mvvv 108,34,168
Ln v8,32,s65,3328
Nvvv 112,30,152,72
Mvvv 112,34,172
W_ 0
Mvvv 112,38,192
Dv 30,%0,20480
Mvvv 112,42,212
Dv 34,v1,20480
Mvvv 108,38,188
Mvvv 108,42,208
Mvvv 104,38,184
Mvvv 104,42,204
Mvvv 100,38,180
Mvvv 100,42,200
Mvvv 96,38,176
Mvvv 96,42,196
W_ 0
Mvvv 96,30,216
Dv 38,%0,12288
Mvvv 96,34,236
Dv 42,v1,12288
Mvvv 100,30,220
Mvvv 100,34,240
Mvvv 104,30,224
Mvvv 104,34,244
Mvvv 108,30,228
Mvvv 108,34,248
s_nop 0
E_ 96
Mvvv 112,30,232
E_ 97
Mvvv 112,34,252
W_ 0
E_ 98
Nvvv 76,38,136,56
Dv 30,%0,13312
Mvvv 76,42,156
Dv 34,v1,13312
Nvvv 80,38,140,60
E_ 99
Mvvv 80,42,160
C_ 99,98,99
C_ 98,96,97
Nvvv 84,38,144,64
E_ 100
Mvvv 84,42,164
E_ 101
Nvvv 88,38,148,68
E_ 102
Mvvv 88,42,168
E_ 103
Nvvv 92,38,152,72
C_ 103,102,103
C_ 102,100,101
Mvvv 92,42,172
W_ 0
E_ 104
Mvvv 92,30,192
Dv 38,%0,14336
Mvvv 92,34,212
Dv 42,v1,14336
Mvvv 88,30,188
E_ 105
Mvvv 88,34,208
E_ 106
Mvvv 84,30,184
S_ s65,s61,122880
E_ 107
Mvvv 84,34,204
S_ m0,s51,12288
C_ 107,106,107
C_ 106,104,105
Mvvv 80,30,180
s_waitcnt vmcnt(6) lgkmcnt(0)
s_barrier
Mvvv 80,34,200
Ln v9,36,s65,0
Mvvv 76,30,176
E_ 108
Mvvv 76,34,196
Ln v9,36,s65,3072
S_ m0,s51,13184
Mvvv 76,38,216
Tv 30,v2,24576
Tv 32,v2,30720
Mvvv 76,42,236
Ln v9,36,s65,128
Mvvv 80,38,220
s_nop 0
Tv 34,v3,24576
Tv 36,v3,30720
Mvvv 80,42,240
Ln v9,36,s65,3200
S_ m0,s51,14080
Mvvv 84,38,224
E_ 109
Mvvv 84,42,244
Ln v9,36,s65,256
Mvvv 88,38,228
E_ 110
Mvvv 88,42,248
Ln v9,36,s65,3328
Mvvv 92,38,232
E_ 111
Mvvv 92,42,252
W_ 0
C_ 111,110,111
C_ 110,108,109
Mavv 0,30,116
s_nop 0
Tv 38,v4,24576
Tv 40,v4,30720
Mavv 20,34,116
E_ 112
Mavv 24,34,120
s_nop 0
Tv 42,v5,24576
Tv 44,v5,30720
Mavv 4,30,120
E_ 113
Mavv 8,30,124
E_ 114
Mavv 28,34,124
E_ 115
Mavv 32,34,128
C_ 115,114,115
C_ 114,112,113
Mavv 12,30,128
E_ 76
Mavv 16,30,132
E_ 77
Mavv 36,34,132
W_ 0
E_ 78
Mavv 56,38,132
s_nop 0
Tv 30,v2,25600
Tv 32,v2,31744
Mavv 76,42,132
E_ 79
Mavv 72,42,128
s_nop 0
Tv 34,v3,25600
Tv 36,v3,31744
Mavv 52,38,128
C_ 96,76,77
C_ 97,78,79
Mavv 48,38,124
E_ 80
Mavv 68,42,124
E_ 81
Mavv 64,42,120
E_ 82
Mavv 44,38,120
E_ 83
Mavv 40,38,116
C_ 100,80,81
C_ 101,82,83
Mavv 60,42,116
W_ 0
E_ 84
Mavv 80,30,116
s_nop 0
Tv 38,v4,25600
Tv 40,v4,31744
Mavv 100,34,116
E_ 85
Mavv 104,34,120
s_nop 0
Tv 42,v5,25600
Tv 44,v5,31744
Mavv 84,30,120
E_ 86
Mavv 88,30,124
E_ 87
Mavv 108,34,124
C_ 104,84,85
C_ 105,86,87
Mavv 112,34,128
E_ 88
Mavv 92,30,128
E_ 89
Mavv 96,30,132
E_ 90
Mavv 116,34,132
W_ 0
E_ 91
Mavv 136,38,132
s_nop 0
Tv 30,v2,26624
Tv 32,v2,32768
Mavv 156,42,132
C_ 108,88,89
C_ 109,90,91
Mavv 152,42,128
s_nop 0
Tv 34,v3,26624
Tv 36,v3,32768
Mavv 132,38,128
E_ 92
Mavv 128,38,124
E_ 93
Mavv 148,42,124
E_ 94
Mavv 144,42,120
E_ 95
Mavv 124,38,120
C_ 112,92,93
C_ 113,94,95
Mavv 120,38,116
v_mov_b32 v92, 0x3f803f80
v_mov_b32 v93, 0x3f803f80
Mavv 140,42,116
W_ 0
v_mov_b32 v94, 0x3f803f80
v_mov_b32 v95, 0x3f803f80
Mavv 160,30,116
s_nop 0
Tv 38,v4,26624
Tv 40,v4,32768
Mavv 180,34,116
Mavv 184,34,120
s_nop 0
Tv 42,v5,26624
Tv 44,v5,32768
Mavv 164,30,120
Mavv 168,30,124
Mavv 188,34,124
S_ s65,s61,147456
Mavv 192,34,128
S_ m0,s51,24576
Mavv 172,30,128
s_waitcnt vmcnt(6) lgkmcnt(0)
s_barrier
Mavv 176,30,132
Ln v7,32,s65,0
Mavv 196,34,132
Mavv 216,38,132
Dv 30,%0,6144
Mavv 236,42,132
Dv 34,v1,6144
Mavv 232,42,128
Ln v8,32,s65,3072
S_ m0,s51,25472
Mavv 212,38,128
Mavv 208,38,124
Ln v7,32,s65,128
Mavv 228,42,124
Mavv 224,42,120
Ln v8,32,s65,3200
S_ m0,s51,26368
Mavv 204,38,120
Mavv 200,38,116
Ln v7,32,s65,256
Mavv 220,42,116
.Lstep2_11:
Zvvv 76,92,96
Dv 38,%0,7168
Zvvv 80,92,100
Dv 42,v1,7168
Zvvv 84,92,104
Zvvv 88,92,108
Zvvv 92,92,112
W_ 2
Nvvv 116,30,136,56
Mvvv 116,34,156
Nvvv 120,30,140,60
s_nop 1
v_max3_f32 v10, v76, v80, v84
s_nop 0
v_max3_f32 v10, v10, v88, v92
Mvvv 120,34,160
v_cmp_lt_f32 vcc, 0x42000000, v10
s_cbranch_vccnz .Ltramp2_10
Nvvv 124,30,144,64
A_ v46,v46,v76
A_ v47,v47,v80
Mvvv 124,34,164
A_ v48,v48,v84
A_ v49,v49,v88
Nvvv 128,30,148,68
A_ v50,v50,v92
Mvvv 128,34,168
Ln v8,32,s65,3328
Nvvv 132,30,152,72
Mvvv 132,34,172
W_ 0
Mvvv 132,38,192
Dv 30,%0,8192
Mvvv 132,42,212
Dv 34,v1,8192
Mvvv 128,38,188
Mvvv 128,42,208
Mvvv 124,38,184
Mvvv 124,42,204
Mvvv 120,38,180
Mvvv 120,42,200
Mvvv 116,38,176
Mvvv 116,42,196
W_ 0
Mvvv 116,30,216
Dv 38,%0,0
Mvvv 116,34,236
Dv 42,v1,0
Mvvv 120,30,220
Mvvv 120,34,240
Mvvv 124,30,224
Mvvv 124,34,244
Mvvv 128,30,228
Mvvv 128,34,248
s_nop 0
E_ 116
Mvvv 132,30,232
E_ 117
Mvvv 132,34,252
W_ 0
E_ 118
Nvvv 76,38,136,56
Dv 30,%0,1024
Mvvv 76,42,156
Dv 34,v1,1024
Nvvv 80,38,140,60
E_ 119
Mvvv 80,42,160
C_ 119,118,119
C_ 118,116,117
Nvvv 84,38,144,64
E_ 120
Mvvv 84,42,164
E_ 121
Nvvv 88,38,148,68
E_ 122
Mvvv 88,42,168
E_ 123
Nvvv 92,38,152,72
C_ 123,122,123
C_ 122,120,121
Mvvv 92,42,172
W_ 0
E_ 124
Mvvv 92,30,192
Dv 38,%0,2048
Mvvv 92,34,212
Dv 42,v1,2048
Mvvv 88,30,188
E_ 125
Mvvv 88,34,208
E_ 126
Mvvv 84,30,184
S_ s65,s61,135168
E_ 127
Mvvv 84,34,204
S_ m0,s51,0
C_ 127,126,127
C_ 126,124,125
Mvvv 80,30,180
s_waitcnt vmcnt(6) lgkmcnt(0)
s_barrier
Mvvv 80,34,200
Ln v9,36,s65,0
Mvvv 76,30,176
E_ 128
Mvvv 76,34,196
Ln v9,36,s65,3072
S_ m0,s51,896
Mvvv 76,38,216
Tv 30,v2,12288
Tv 32,v2,18432
Mvvv 76,42,236
Ln v9,36,s65,128
Mvvv 80,38,220
s_nop 0
Tv 34,v3,12288
Tv 36,v3,18432
Mvvv 80,42,240
Ln v9,36,s65,3200
S_ m0,s51,1792
Mvvv 84,38,224
E_ 129
Mvvv 84,42,244
Ln v9,36,s65,256
Mvvv 88,38,228
E_ 130
Mvvv 88,42,248
Ln v9,36,s65,3328
Mvvv 92,38,232
E_ 131
Mvvv 92,42,252
W_ 0
C_ 131,130,131
C_ 130,128,129
Mavv 0,30,96
s_nop 0
Tv 38,v4,12288
Tv 40,v4,18432
Mavv 20,34,96
E_ 132
Mavv 24,34,100
s_nop 0
Tv 42,v5,12288
Tv 44,v5,18432
Mavv 4,30,100
E_ 133
Mavv 8,30,104
E_ 134
Mavv 28,34,104
E_ 135
Mavv 32,34,108
C_ 135,134,135
C_ 134,132,133
Mavv 12,30,108
E_ 76
Mavv 16,30,112
E_ 77
Mavv 36,34,112
W_ 0
E_ 78
Mavv 56,38,112
s_nop 0
Tv 30,v2,13312
Tv 32,v2,19456
Mavv 76,42,112
.Lpad_a2_6:
s_cbranch_execz .Lhop_a2_r
E_ 79
Mavv 72,42,108
s_nop 0
Tv 34,v3,13312
Tv 36,v3,19456
Mavv 52,38,108
C_ 116,76,77
C_ 117,78,79
Mavv 48,38,104
E_ 80
Mavv 68,42,104
E_ 81
Mavv 64,42,100
E_ 82
Mavv 44,38,100
E_ 83
Mavv 40,38,96
C_ 120,80,81
C_ 121,82,83
Mavv 60,42,96
W_ 0
E_ 84
Mavv 80,30,96
s_nop 0
Tv 38,v4,13312
Tv 40,v4,19456
Mavv 100,34,96
E_ 85
Mavv 104,34,100
s_nop 0
Tv 42,v5,13312
Tv 44,v5,19456
Mavv 84,30,100
E_ 86
Mavv 88,30,104
E_ 87
Mavv 108,34,104
C_ 124,84,85
C_ 125,86,87
Mavv 112,34,108
E_ 88
Mavv 92,30,108
E_ 89
Mavv 96,30,112
E_ 90
Mavv 116,34,112
W_ 0
E_ 91
Mavv 136,38,112
s_nop 0
Tv 30,v2,14336
Tv 32,v2,20480
Mavv 156,42,112
C_ 128,88,89
C_ 129,90,91
Mavv 152,42,108
s_nop 0
Tv 34,v3,14336
Tv 36,v3,20480
Mavv 132,38,108
E_ 92
Mavv 128,38,104
E_ 93
Mavv 148,42,104
E_ 94
Mavv 144,42,100
E_ 95
Mavv 124,38,100
C_ 132,92,93
C_ 133,94,95
Mavv 120,38,96
v_mov_b32 v92, 0x3f803f80
v_mov_b32 v93, 0x3f803f80
Mavv 140,42,96
W_ 0
v_mov_b32 v94, 0x3f803f80
v_mov_b32 v95, 0x3f803f80
Mavv 160,30,96
s_nop 0
Tv 38,v4,14336
Tv 40,v4,20480
Mavv 180,34,96
Mavv 184,34,100
s_nop 0
Tv 42,v5,14336
Tv 44,v5,20480
Mavv 164,30,100
Mavv 168,30,104
Mavv 188,34,104
S_ s65,s61,159744
Mavv 192,34,108
S_ m0,s51,12288
Mavv 172,30,108
s_waitcnt vmcnt(6) lgkmcnt(0)
s_barrier
Mavv 176,30,112
Ln v7,32,s65,0
Mavv 196,34,112
Mavv 216,38,112
Dv 30,%0,30720
Mavv 236,42,112
Dv 34,v1,30720
Mavv 232,42,108
Ln v8,32,s65,3072
S_ m0,s51,13184
Mavv 212,38,108
Mavv 208,38,104
Ln v7,32,s65,128
Mavv 228,42,104
Mavv 224,42,100
Ln v8,32,s65,3200
S_ m0,s51,14080
Mavv 204,38,100
Mavv 200,38,96
Ln v7,32,s65,256
Mavv 220,42,96
.Lstep2_12:
Zvvv 76,92,116
Dv 38,%0,31744
Zvvv 80,92,120
Dv 42,v1,31744
Zvvv 84,92,124
Zvvv 88,92,128
Zvvv 92,92,132
W_ 2
Nvvv 96,30,136,56
Mvvv 96,34,156
Nvvv 100,30,140,60
s_nop 1
v_max3_f32 v10, v76, v80, v84
s_nop 0
v_max3_f32 v10, v10, v88, v92
Mvvv 100,34,160
v_cmp_lt_f32 vcc, 0x42000000, v10
s_cbranch_vccnz .Ltramp2_11
Nvvv 104,30,144,64
A_ v46,v46,v76
A_ v47,v47,v80
Mvvv 104,34,164
A_ v48,v48,v84
A_ v49,v49,v88
Nvvv 108,30,148,68
A_ v50,v50,v92
Mvvv 108,34,168
Ln v8,32,s65,3328
Nvvv 112,30,152,72
R_ v11,a248
Mvvv 112,34,172
W_ 0
v_min_u32 v11, 240, v11
Mvvv 112,38,192
Dv 30,%0,32768
v_lshlrev_b32 v11, 3, v11
Mvvv 112,42,212
Dv 34,v1,32768
Mvvv 108,38,188
buffer_load_dword a254, v11, s[44:47], s66 offen
Mvvv 108,42,208
Mvvv 104,38,184
Mvvv 104,42,204
Mvvv 100,38,180
Mvvv 100,42,200
Mvvv 96,38,176
Mvvv 96,42,196
W_ 0
Mvvv 96,30,216
Dv 38,%0,24576
Mvvv 96,34,236
Dv 42,v1,24576
Mvvv 100,30,220
Mvvv 100,34,240
Mvvv 104,30,224
Mvvv 104,34,244
Mvvv 108,30,228
Mvvv 108,34,248
s_nop 0
E_ 96
Mvvv 112,30,232
E_ 97
Mvvv 112,34,252
W_ 0
E_ 98
Nvvv 76,38,136,56
Dv 30,%0,25600
Mvvv 76,42,156
Dv 34,v1,25600
Nvvv 80,38,140,60
E_ 99
Mvvv 80,42,160
C_ 99,98,99
C_ 98,96,97
Nvvv 84,38,144,64
E_ 100
Mvvv 84,42,164
E_ 101
Nvvv 88,38,148,68
E_ 102
Mvvv 88,42,168
E_ 103
Nvvv 92,38,152,72
C_ 103,102,103
C_ 102,100,101
Mvvv 92,42,172
W_ 0
E_ 104
Mvvv 92,30,192
Dv 38,%0,26624
Mvvv 92,34,212
Dv 42,v1,26624
Mvvv 88,30,188
E_ 105
Mvvv 88,34,208
E_ 106
Mvvv 84,30,184
S_ s65,s61,147456
E_ 107
Mvvv 84,34,204
S_ m0,s51,24576
C_ 107,106,107
C_ 106,104,105
Mvvv 80,30,180
s_waitcnt vmcnt(7) lgkmcnt(0)
s_barrier
Mvvv 80,34,200
Ln v9,36,s65,0
Mvvv 76,30,176
E_ 108
Mvvv 76,34,196
Ln v9,36,s65,3072
S_ m0,s51,25472
Mvvv 76,38,216
Tv 30,v2,0
Tv 32,v2,6144
Mvvv 76,42,236
Ln v9,36,s65,128
Mvvv 80,38,220
s_nop 0
Tv 34,v3,0
Tv 36,v3,6144
Mvvv 80,42,240
Ln v9,36,s65,3200
S_ m0,s51,26368
Mvvv 84,38,224
E_ 109
Mvvv 84,42,244
Ln v9,36,s65,256
Mvvv 88,38,228
E_ 110
Mvvv 88,42,248
Ln v9,36,s65,3328
Mvvv 92,38,232
E_ 111
Mvvv 92,42,252
W_ 0
C_ 111,110,111
C_ 110,108,109
Mavv 0,30,116
s_nop 0
Tv 38,v4,0
Tv 40,v4,6144
Mavv 20,34,116
E_ 112
Mavv 24,34,120
s_nop 0
Tv 42,v5,0
Tv 44,v5,6144
Mavv 4,30,120
E_ 113
Mavv 8,30,124
E_ 114
Mavv 28,34,124
E_ 115
Mavv 32,34,128
C_ 115,114,115
C_ 114,112,113
Mavv 12,30,128
E_ 76
Mavv 16,30,132
E_ 77
Mavv 36,34,132
W_ 0
E_ 78
Mavv 56,38,132
s_nop 0
Tv 30,v2,1024
Tv 32,v2,7168
Mavv 76,42,132
E_ 79
Mavv 72,42,128
s_nop 0
Tv 34,v3,1024
Tv 36,v3,7168
Mavv 52,38,128
C_ 96,76,77
C_ 97,78,79
Mavv 48,38,124
E_ 80
Mavv 68,42,124
E_ 81
Mavv 64,42,120
E_ 82
Mavv 44,38,120
E_ 83
Mavv 40,38,116
C_ 100,80,81
C_ 101,82,83
Mavv 60,42,116
W_ 0
E_ 84
Mavv 80,30,116
s_nop 0
Tv 38,v4,1024
Tv 40,v4,7168
Mavv 100,34,116
E_ 85
Mavv 104,34,120
s_nop 0
Tv 42,v5,1024
Tv 44,v5,7168
Mavv 84,30,120
E_ 86
Mavv 88,30,124
E_ 87
Mavv 108,34,124
C_ 104,84,85
C_ 105,86,87
Mavv 112,34,128
E_ 88
Mavv 92,30,128
E_ 89
Mavv 96,30,132
E_ 90
Mavv 116,34,132
W_ 0
E_ 91
Mavv 136,38,132
s_nop 0
Tv 30,v2,2048
Tv 32,v2,8192
Mavv 156,42,132
C_ 108,88,89
C_ 109,90,91
Mavv 152,42,128
s_nop 0
Tv 34,v3,2048
Tv 36,v3,8192
Mavv 132,38,128
E_ 92
Mavv 128,38,124
E_ 93
Mavv 148,42,124
E_ 94
Mavv 144,42,120
E_ 95
Mavv 124,38,120
C_ 112,92,93
C_ 113,94,95
Mavv 120,38,116
v_mov_b32 v92, 0x3f803f80
v_mov_b32 v93, 0x3f803f80
Mavv 140,42,116
W_ 0
v_mov_b32 v94, 0x3f803f80
v_mov_b32 v95, 0x3f803f80
Mavv 160,30,116
s_nop 0
Tv 38,v4,2048
Tv 40,v4,8192
Mavv 180,34,116
Mavv 184,34,120
s_nop 0
Tv 42,v5,2048
Tv 44,v5,8192
Mavv 164,30,120
Mavv 168,30,124
Mavv 188,34,124
S_ s65,s61,172032
Mavv 192,34,128
S_ m0,s51,0
Mavv 172,30,128
s_waitcnt vmcnt(7) lgkmcnt(0)
s_barrier
Mavv 176,30,132
Ln v7,32,s65,0
Mavv 196,34,132
Mavv 216,38,132
Dv 30,%0,18432
Mavv 236,42,132
Dv 34,v1,18432
Mavv 232,42,128
Ln v8,32,s65,3072
S_ m0,s51,896
Mavv 212,38,128
Mavv 208,38,124
Ln v7,32,s65,128
Mavv 228,42,124
Mavv 224,42,120
Ln v8,32,s65,3200
S_ m0,s51,1792
Mavv 204,38,120
Mavv 200,38,116
Ln v7,32,s65,256
Mavv 220,42,116
.Lstep2_13:
Zvvv 76,92,96
Dv 38,%0,19456
Zvvv 80,92,100
Dv 42,v1,19456
Zvvv 84,92,104
Zvvv 88,92,108
Zvvv 92,92,112
W_ 2
Nvvv 116,30,136,56
Mvvv 116,34,156
Nvvv 120,30,140,60
s_nop 1
v_max3_f32 v10, v76, v80, v84
s_nop 0
v_max3_f32 v10, v10, v88, v92
Mvvv 120,34,160
v_cmp_lt_f32 vcc, 0x42000000, v10
s_cbranch_vccnz .Ltramp2_12
Nvvv 124,30,144,64
A_ v46,v46,v76
A_ v47,v47,v80
Mvvv 124,34,164
A_ v48,v48,v84
A_ v49,v49,v88
Nvvv 128,30,148,68
A_ v50,v50,v92
Mvvv 128,34,168
Ln v8,32,s65,3328
Nvvv 132,30,152,72
Mvvv 132,34,172
W_ 0
Mvvv 132,38,192
Dv 30,%0,20480
Mvvv 132,42,212
Dv 34,v1,20480
Mvvv 128,38,188
Mvvv 128,42,208
Mvvv 124,38,184
Mvvv 124,42,204
Mvvv 120,38,180
Mvvv 120,42,200
Mvvv 116,38,176
Mvvv 116,42,196
W_ 0
Mvvv 116,30,216
Dv 38,%0,12288
Mvvv 116,34,236
Dv 42,v1,12288
Mvvv 120,30,220
Mvvv 120,34,240
Mvvv 124,30,224
Mvvv 124,34,244
Mvvv 128,30,228
Mvvv 128,34,248
s_nop 0
E_ 116
Mvvv 132,30,232
E_ 117
Mvvv 132,34,252
W_ 0
E_ 118
Nvvv 76,38,136,56
Dv 30,%0,13312
Mvvv 76,42,156
Dv 34,v1,13312
Nvvv 80,38,140,60
E_ 119
Mvvv 80,42,160
C_ 119,118,119
C_ 118,116,117
Nvvv 84,38,144,64
E_ 120
Mvvv 84,42,164
E_ 121
Nvvv 88,38,148,68
E_ 122
Mvvv 88,42,168
E_ 123
Nvvv 92,38,152,72
C_ 123,122,123
C_ 122,120,121
Mvvv 92,42,172
W_ 0
E_ 124
Mvvv 92,30,192
Dv 38,%0,14336
Mvvv 92,34,212
Dv 42,v1,14336
Mvvv 88,30,188
E_ 125
Mvvv 88,34,208
E_ 126
Mvvv 84,30,184
S_ s65,s61,159744
E_ 127
Mvvv 84,34,204
S_ m0,s51,12288
C_ 127,126,127
C_ 126,124,125
Mvvv 80,30,180
s_waitcnt vmcnt(6) lgkmcnt(0)
s_barrier
Mvvv 80,34,200
Ln v9,36,s65,0
Mvvv 76,30,176
E_ 128
Mvvv 76,34,196
Ln v9,36,s65,3072
S_ m0,s51,13184
Mvvv 76,38,216
Tv 30,v2,24576
Tv 32,v2,30720
Mvvv 76,42,236
Ln v9,36,s65,128
Mvvv 80,38,220
s_nop 0
Tv 34,v3,24576
Tv 36,v3,30720
Mvvv 80,42,240
Ln v9,36,s65,3200
S_ m0,s51,14080
Mvvv 84,38,224
E_ 129
Mvvv 84,42,244
Ln v9,36,s65,256
Mvvv 88,38,228
E_ 130
Mvvv 88,42,248
Ln v9,36,s65,3328
Mvvv 92,38,232
E_ 131
Mvvv 92,42,252
W_ 0
C_ 131,130,131
C_ 130,128,129
Mavv 0,30,96
s_nop 0
Tv 38,v4,24576
Tv 40,v4,30720
Mavv 20,34,96
E_ 132
Mavv 24,34,100
s_nop 0
Tv 42,v5,24576
Tv 44,v5,30720
Mavv 4,30,100
E_ 133
Mavv 8,30,104
E_ 134
Mavv 28,34,104
E_ 135
Mavv 32,34,108
C_ 135,134,135
C_ 134,132,133
Mavv 12,30,108
E_ 76
Mavv 16,30,112
E_ 77
Mavv 36,34,112
W_ 0
E_ 78
Mavv 56,38,112
s_nop 0
Tv 30,v2,25600
Tv 32,v2,31744
Mavv 76,42,112
E_ 79
Mavv 72,42,108
s_nop 0
Tv 34,v3,25600
Tv 36,v3,31744
Mavv 52,38,108
C_ 116,76,77
C_ 117,78,79
Mavv 48,38,104
E_ 80
Mavv 68,42,104
E_ 81
Mavv 64,42,100
E_ 82
Mavv 44,38,100
E_ 83
Mavv 40,38,96
C_ 120,80,81
C_ 121,82,83
Mavv 60,42,96
W_ 0
E_ 84
Mavv 80,30,96
.Lpad_a2_7:
s_cbranch_execz .Lhop_a2_r
Tv 38,v4,25600
Tv 40,v4,31744
Mavv 100,34,96
E_ 85
Mavv 104,34,100
s_nop 0
Tv 42,v5,25600
Tv 44,v5,31744
Mavv 84,30,100
E_ 86
Mavv 88,30,104
E_ 87
Mavv 108,34,104
C_ 124,84,85
C_ 125,86,87
Mavv 112,34,108
E_ 88
Mavv 92,30,108
E_ 89
Mavv 96,30,112
E_ 90
Mavv 116,34,112
W_ 0
E_ 91
Mavv 136,38,112
s_nop 0
Tv 30,v2,26624
Tv 32,v2,32768
Mavv 156,42,112
C_ 128,88,89
C_ 129,90,91
Mavv 152,42,108
s_nop 0
Tv 34,v3,26624
Tv 36,v3,32768
Mavv 132,38,108
E_ 92
Mavv 128,38,104
E_ 93
Mavv 148,42,104
E_ 94
Mavv 144,42,100
E_ 95
Mavv 124,38,100
C_ 132,92,93
C_ 133,94,95
Mavv 120,38,96
v_mov_b32 v92, 0x3f803f80
v_mov_b32 v93, 0x3f803f80
Mavv 140,42,96
W_ 0
v_mov_b32 v94, 0x3f803f80
v_mov_b32 v95, 0x3f803f80
Mavv 160,30,96
s_nop 0
Tv 38,v4,26624
Tv 40,v4,32768
Mavv 180,34,96
Mavv 184,34,100
s_nop 0
Tv 42,v5,26624
Tv 44,v5,32768
Mavv 164,30,100
Mavv 168,30,104
Mavv 188,34,104
S_ s65,s61,184320
Mavv 192,34,108
S_ m0,s51,24576
Mavv 172,30,108
s_waitcnt vmcnt(6) lgkmcnt(0)
s_barrier
Mavv 176,30,112
Ln v7,32,s65,0
Mavv 196,34,112
Mavv 216,38,112
Dv 30,%0,6144
Mavv 236,42,112
Dv 34,v1,6144
Mavv 232,42,108
Ln v8,32,s65,3072
S_ m0,s51,25472
Mavv 212,38,108
Mavv 208,38,104
Ln v7,32,s65,128
Mavv 228,42,104
Mavv 224,42,100
Ln v8,32,s65,3200
S_ m0,s51,26368
Mavv 204,38,100
Mavv 200,38,96
Ln v7,32,s65,256
Mavv 220,42,96
.Lstep2_14:
Zvvv 76,92,116
Dv 38,%0,7168
Zvvv 80,92,120
Dv 42,v1,7168
Zvvv 84,92,124
Zvvv 88,92,128
Zvvv 92,92,132
W_ 2
Nvvv 96,30,136,56
Mvvv 96,34,156
Nvvv 100,30,140,60
s_nop 1
v_max3_f32 v10, v76, v80, v84
s_nop 0
v_max3_f32 v10, v10, v88, v92
Mvvv 100,34,160
v_cmp_lt_f32 vcc, 0x42000000, v10
s_cbranch_vccnz .Ltramp2_13
Nvvv 104,30,144,64
A_ v46,v46,v76
A_ v47,v47,v80
Mvvv 104,34,164
A_ v48,v48,v84
A_ v49,v49,v88
Nvvv 108,30,148,68
A_ v50,v50,v92
Mvvv 108,34,168
Ln v8,32,s65,3328
Nvvv 112,30,152,72
Mvvv 112,34,172
W_ 0
Mvvv 112,38,192
Dv 30,%0,8192
Mvvv 112,42,212
Dv 34,v1,8192
Mvvv 108,38,188
Mvvv 108,42,208
Mvvv 104,38,184
Mvvv 104,42,204
Mvvv 100,38,180
Mvvv 100,42,200
Mvvv 96,38,176
Mvvv 96,42,196
W_ 0
Mvvv 96,30,216
Dv 38,%0,0
Mvvv 96,34,236
Dv 42,v1,0
Mvvv 100,30,220
Mvvv 100,34,240
Mvvv 104,30,224
Mvvv 104,34,244
Mvvv 108,30,228
Mvvv 108,34,248
s_nop 0
E_ 96
Mvvv 112,30,232
E_ 97
Mvvv 112,34,252
W_ 0
E_ 98
Nvvv 76,38,136,56
Dv 30,%0,1024
Mvvv 76,42,156
Dv 34,v1,1024
Nvvv 80,38,140,60
E_ 99
Mvvv 80,42,160
C_ 99,98,99
C_ 98,96,97
Nvvv 84,38,144,64
E_ 100
Mvvv 84,42,164
E_ 101
Nvvv 88,38,148,68
E_ 102
Mvvv 88,42,168
E_ 103
Nvvv 92,38,152,72
C_ 103,102,103
C_ 102,100,101
Mvvv 92,42,172
W_ 0
E_ 104
Mvvv 92,30,192
Dv 38,%0,2048
Mvvv 92,34,212
Dv 42,v1,2048
Mvvv 88,30,188
E_ 105
Mvvv 88,34,208
E_ 106
Mvvv 84,30,184
S_ s65,s61,172032
E_ 107
Mvvv 84,34,204
S_ m0,s51,0
C_ 107,106,107
C_ 106,104,105
Mvvv 80,30,180
s_waitcnt vmcnt(6) lgkmcnt(0)
s_barrier
Mvvv 80,34,200
Ln v9,36,s65,0
Mvvv 76,30,176
E_ 108
Mvvv 76,34,196
Ln v9,36,s65,3072
S_ m0,s51,896
Mvvv 76,38,216
Tv 30,v2,12288
Tv 32,v2,18432
Mvvv 76,42,236
Ln v9,36,s65,128
Mvvv 80,38,220
s_nop 0
Tv 34,v3,12288
Tv 36,v3,18432
Mvvv 80,42,240
Ln v9,36,s65,3200
S_ m0,s51,1792
Mvvv 84,38,224
E_ 109
Mvvv 84,42,244
Ln v9,36,s65,256
Mvvv 88,38,228
E_ 110
Mvvv 88,42,248
Ln v9,36,s65,3328
Mvvv 92,38,232
E_ 111
Mvvv 92,42,252
W_ 0
C_ 111,110,111
C_ 110,108,109
Mavv 0,30,116
s_nop 0
Tv 38,v4,12288
Tv 40,v4,18432
Mavv 20,34,116
E_ 112
Mavv 24,34,120
s_nop 0
Tv 42,v5,12288
Tv 44,v5,18432
Mavv 4,30,120
E_ 113
Mavv 8,30,124
E_ 114
Mavv 28,34,124
E_ 115
Mavv 32,34,128
C_ 115,114,115
C_ 114,112,113
Mavv 12,30,128
E_ 76
Mavv 16,30,132
E_ 77
Mavv 36,34,132
W_ 0
E_ 78
Mavv 56,38,132
s_nop 0
Tv 30,v2,13312
Tv 32,v2,19456
Mavv 76,42,132
E_ 79
Mavv 72,42,128
s_nop 0
Tv 34,v3,13312
Tv 36,v3,19456
Mavv 52,38,128
C_ 96,76,77
C_ 97,78,79
Mavv 48,38,124
E_ 80
Mavv 68,42,124
E_ 81
Mavv 64,42,120
E_ 82
Mavv 44,38,120
E_ 83
Mavv 40,38,116
C_ 100,80,81
C_ 101,82,83
Mavv 60,42,116
W_ 0
E_ 84
Mavv 80,30,116
s_nop 0
Tv 38,v4,13312
Tv 40,v4,19456
Mavv 100,34,116
E_ 85
Mavv 104,34,120
s_nop 0
Tv 42,v5,13312
Tv 44,v5,19456
Mavv 84,30,120
E_ 86
Mavv 88,30,124
E_ 87
Mavv 108,34,124
C_ 104,84,85
C_ 105,86,87
Mavv 112,34,128
E_ 88
Mavv 92,30,128
E_ 89
Mavv 96,30,132
E_ 90
Mavv 116,34,132
W_ 0
E_ 91
Mavv 136,38,132
s_nop 0
Tv 30,v2,14336
Tv 32,v2,20480
Mavv 156,42,132
C_ 108,88,89
C_ 109,90,91
Mavv 152,42,128
s_nop 0
Tv 34,v3,14336
Tv 36,v3,20480
Mavv 132,38,128
E_ 92
Mavv 128,38,124
E_ 93
Mavv 148,42,124
E_ 94
Mavv 144,42,120
E_ 95
Mavv 124,38,120
C_ 112,92,93
C_ 113,94,95
Mavv 120,38,116
v_mov_b32 v92, 0x3f803f80
v_mov_b32 v93, 0x3f803f80
Mavv 140,42,116
W_ 0
v_mov_b32 v94, 0x3f803f80
v_mov_b32 v95, 0x3f803f80
Mavv 160,30,116
s_nop 0
Tv 38,v4,14336
Tv 40,v4,20480
Mavv 180,34,116
Mavv 184,34,120
)A"
R"A(s_nop 0
Tv 42,v5,14336
Tv 44,v5,20480
Mavv 164,30,120
Mavv 168,30,124
Mavv 188,34,124
S_ s65,s61,184320
Mavv 192,34,128
S_ m0,s51,12288
Mavv 172,30,128
s_waitcnt vmcnt(6) lgkmcnt(0)
s_barrier
Mavv 176,30,132
Ln v9,36,s65,0
Mavv 196,34,132
Mavv 216,38,132
Dv 30,%0,30720
Mavv 236,42,132
Dv 34,v1,30720
Mavv 232,42,128
Ln v9,36,s65,3072
S_ m0,s51,13184
Mavv 212,38,128
Mavv 208,38,124
Ln v9,36,s65,128
Mavv 228,42,124
Mavv 224,42,120
Ln v9,36,s65,3200
S_ m0,s51,14080
Mavv 204,38,120
Mavv 200,38,116
Ln v9,36,s65,256
Mavv 220,42,116
.Lstep2_15:
Zvvv 76,92,96
Dv 38,%0,31744
Zvvv 80,92,100
Dv 42,v1,31744
Zvvv 84,92,104
Zvvv 88,92,108
Zvvv 92,92,112
W_ 2
Nvvv 116,30,136,56
Mvvv 116,34,156
Nvvv 120,30,140,60
s_nop 1
v_max3_f32 v10, v76, v80, v84
s_nop 0
v_max3_f32 v10, v10, v88, v92
Mvvv 120,34,160
v_cmp_lt_f32 vcc, 0x42000000, v10
s_cbranch_vccnz .Ltramp2_14
Nvvv 124,30,144,64
A_ v46,v46,v76
A_ v47,v47,v80
Mvvv 124,34,164
A_ v48,v48,v84
A_ v49,v49,v88
Nvvv 128,30,148,68
A_ v50,v50,v92
Mvvv 128,34,168
Ln v9,36,s65,3328
Nvvv 132,30,152,72
Mvvv 132,34,172
W_ 0
Mvvv 132,38,192
Dv 30,%0,32768
Mvvv 132,42,212
Dv 34,v1,32768
Mvvv 128,38,188
Mvvv 128,42,208
Mvvv 124,38,184
Mvvv 124,42,204
Mvvv 120,38,180
Mvvv 120,42,200
Mvvv 116,38,176
Mvvv 116,42,196
W_ 0
Mvvv 116,30,216
Dv 38,%0,24576
Mvvv 116,34,236
Dv 42,v1,24576
Mvvv 120,30,220
Mvvv 120,34,240
Mvvv 124,30,224
Mvvv 124,34,244
Mvvv 128,30,228
Mvvv 128,34,248
s_nop 0
E_ 116
Mvvv 132,30,232
E_ 117
Mvvv 132,34,252
W_ 0
E_ 118
Nvvv 76,38,136,56
Dv 30,%0,25600
Mvvv 76,42,156
Dv 34,v1,25600
Nvvv 80,38,140,60
E_ 119
Mvvv 80,42,160
C_ 119,118,119
C_ 118,116,117
Nvvv 84,38,144,64
E_ 120
Mvvv 84,42,164
E_ 121
Nvvv 88,38,148,68
E_ 122
Mvvv 88,42,168
E_ 123
Nvvv 92,38,152,72
C_ 123,122,123
C_ 122,120,121
Mvvv 92,42,172
W_ 0
E_ 124
Mvvv 92,30,192
Dv 38,%0,26624
Mvvv 92,34,212
Dv 42,v1,26624
Mvvv 88,30,188
E_ 125
Mvvv 88,34,208
E_ 126
Mvvv 84,30,184
S_ s65,s61,393216
E_ 127
Mvvv 84,34,204
S_ m0,s51,24576
C_ 127,126,127
C_ 126,124,125
Mvvv 80,30,180
s_waitcnt vmcnt(6) lgkmcnt(0)
s_barrier
Mvvv 80,34,200
L_ v9,36,s65,0
Mvvv 76,30,176
E_ 128
Mvvv 76,34,196
L_ v9,36,s65,3072
S_ m0,s51,25472
Mvvv 76,38,216
Tv 30,v2,0
Tv 32,v2,6144
Mvvv 76,42,236
L_ v9,36,s65,128
Mvvv 80,38,220
s_nop 0
Tv 34,v3,0
Tv 36,v3,6144
Mvvv 80,42,240
L_ v9,36,s65,3200
S_ m0,s51,26368
Mvvv 84,38,224
E_ 129
Mvvv 84,42,244
L_ v9,36,s65,256
Mvvv 88,38,228
E_ 130
Mvvv 88,42,248
L_ v9,36,s65,3328
Mvvv 92,38,232
E_ 131
Mvvv 92,42,252
W_ 0
C_ 131,130,131
C_ 130,128,129
Mavv 0,30,96
s_nop 0
Tv 38,v4,0
Tv 40,v4,6144
Mavv 20,34,96
E_ 132
Mavv 24,34,100
s_nop 0
Tv 42,v5,0
Tv 44,v5,6144
Mavv 4,30,100
E_ 133
Mavv 8,30,104
E_ 134
Mavv 28,34,104
E_ 135
Mavv 32,34,108
C_ 135,134,135
C_ 134,132,133
Mavv 12,30,108
E_ 76
Mavv 16,30,112
E_ 77
Mavv 36,34,112
W_ 0
E_ 78
Mavv 56,38,112
s_nop 0
Tv 30,v2,1024
Tv 32,v2,7168
Mavv 76,42,112
E_ 79
Mavv 72,42,108
s_nop 0
Tv 34,v3,1024
Tv 36,v3,7168
Mavv 52,38,108
C_ 116,76,77
C_ 117,78,79
Mavv 48,38,104
E_ 80
Mavv 68,42,104
E_ 81
Mavv 64,42,100
E_ 82
Mavv 44,38,100
E_ 83
Mavv 40,38,96
C_ 120,80,81
C_ 121,82,83
Mavv 60,42,96
W_ 0
E_ 84
Mavv 80,30,96
s_nop 0
Tv 38,v4,1024
Tv 40,v4,7168
Mavv 100,34,96
E_ 85
Mavv 104,34,100
s_nop 0
Tv 42,v5,1024
Tv 44,v5,7168
Mavv 84,30,100
E_ 86
Mavv 88,30,104
E_ 87
Mavv 108,34,104
C_ 124,84,85
C_ 125,86,87
Mavv 112,34,108
E_ 88
Mavv 92,30,108
E_ 89
Mavv 96,30,112
E_ 90
Mavv 116,34,112
W_ 0
E_ 91
Mavv 136,38,112
s_nop 0
Tv 30,v2,2048
Tv 32,v2,8192
Mavv 156,42,112
C_ 128,88,89
C_ 129,90,91
Mavv 152,42,108
s_nop 0
Tv 34,v3,2048
Tv 36,v3,8192
Mavv 132,38,108
E_ 92
Mavv 128,38,104
E_ 93
Mavv 148,42,104
E_ 94
Mavv 144,42,100
E_ 95
Mavv 124,38,100
C_ 132,92,93
C_ 133,94,95
Mavv 120,38,96
v_mov_b32 v92, 0x3f803f80
v_mov_b32 v93, 0x3f803f80
Mavv 140,42,96
W_ 0
v_mov_b32 v94, 0x3f803f80
v_mov_b32 v95, 0x3f803f80
Mavv 160,30,96
s_nop 0
Tv 38,v4,2048
Tv 40,v4,8192
Mavv 180,34,96
Mavv 184,34,100
s_nop 0
Tv 42,v5,2048
Tv 44,v5,8192
Mavv 164,30,100
Mavv 168,30,104
Mavv 188,34,104
S_ s65,s61,393216
Mavv 192,34,108
S_ m0,s51,0
Mavv 172,30,108
s_waitcnt vmcnt(6) lgkmcnt(0)
s_barrier
Mavv 176,30,112
Ln v7,32,s65,0
Mavv 196,34,112
Mavv 216,38,112
s_nop 0
Tv 30,v2,12288
Tv 32,v2,18432
Zvvv 76,92,116
Ln v8,32,s65,3072
S_ m0,s51,896
Zvvv 80,92,120
s_nop 0
Tv 34,v3,12288
Tv 36,v3,18432
Zvvv 84,92,124
Ln v7,32,s65,128
Zvvv 88,92,128
Zvvv 92,92,132
Ln v8,32,s65,3200
S_ m0,s51,1792
Mavv 236,42,112
Mavv 232,42,108
Ln v7,32,s65,256
Mavv 212,38,108
Mavv 208,38,104
v_max3_f32 v10, v76, v80, v84
Mavv 228,42,104
v_max3_f32 v10, v10, v88, v92
v_cmp_lt_f32 vcc, 0x42000000, v10
Mavv 224,42,100
Ln v8,32,s65,3328
Mavv 204,38,100
Mavv 200,38,96
Mavv 220,42,96
s_cbranch_vccnz .Ltramp2_15
.Ljoin2:
W_ 2
Mavv 0,30,116
s_mov_b32 s58, 1
Tv 38,v4,12288
Tv 40,v4,18432
W_ 2
Mavv 20,34,116
R_ v11,a248
R_ v13,a248
Mavv 24,34,120
S_ s53,s51,36864
Tv 42,v5,12288
Tv 44,v5,18432
Mavv 4,30,120
v_add_u32 v12, s53, v13
s_mul_i32 s53, s4, 10240
Mavv 8,30,124
v_add_u32 v13, s53, v13
A_ v46,v46,v76
Mavv 28,34,124
A_ v47,v47,v80
Mavv 32,34,128
A_ v48,v48,v84
A_ v49,v49,v88
Mavv 12,30,128
A_ v50,v50,v92
Mavv 16,30,132
v_rcp_f32 v46, v46
Mavv 36,34,132
W_ 0
v_rcp_f32 v47, v47
Mavv 56,38,132
s_nop 0
Tv 30,v2,13312
Tv 32,v2,19456
Mavv 76,42,132
v_rcp_f32 v48, v48
Mavv 72,42,128
s_nop 0
Tv 34,v3,13312
Tv 36,v3,19456
Mavv 52,38,128
v_rcp_f32 v49, v49
Mavv 48,38,124
v_rcp_f32 v50, v50
Mavv 68,42,124
s_mov_b32 s56, 12288
v_mov_b32 v176, v20
v_mov_b32 v177, v21
Mavv 64,42,120
Dv 136,v13,0
v_mov_b32 v178, v22
Mavv 44,38,120
Dv 140,v13,1024
v_mov_b32 v179, v23
Mavv 40,38,116
Dv 144,v13,2048
v_mov_b32 v180, v24
Mavv 60,42,116
W_ 3
Dv 148,v13,3072
v_mov_b32 v181, v25
Mavv 80,30,116
s_nop 0
Tv 38,v4,13312
Tv 40,v4,19456
Mavv 100,34,116
Dv 152,v13,4096
v_mov_b32 v182, v26
Mavv 104,34,120
s_nop 0
Tv 42,v5,13312
Tv 44,v5,19456
Mavv 84,30,120
Dv 156,v13,5120
v_mov_b32 v183, v27
Mavv 88,30,124
Dv 160,v13,6144
v_mov_b32 v184, v52
Mavv 108,34,124
Dv 164,v13,7168
v_mov_b32 v185, v53
Mavv 112,34,128
Dv 168,v13,8192
v_mov_b32 v186, v54
Mavv 92,30,128
Dv 172,v13,9216
v_mov_b32 v187, v55
Mavv 96,30,132
Dv 208,v12,0
R_ v188,a240
Mavv 116,34,132
W_ 6
Dv 212,v12,1024
R_ v189,a241
Mavv 136,38,132
s_nop 0
Tv 30,v2,14336
Tv 32,v2,20480
Mavv 156,42,132
Dv 216,v12,2048
R_ v190,a242
Mavv 152,42,128
s_nop 0
Tv 34,v3,14336
Tv 36,v3,20480
Mavv 132,38,128
Dv 220,v12,3072
R_ v191,a243
Mavv 128,38,124
Dv 224,v12,4096
R_ v192,a244
Mavv 148,42,124
Dv 228,v12,5120
R_ v193,a245
Mavv 144,42,120
Dv 232,v12,12288
R_ v194,a246
Mavv 124,38,120
Dv 236,v12,13312
R_ v195,a247
Mavv 120,38,116
Dv 240,v12,14336
v_mov_b32 v196, v16
Mavv 140,42,116
W_ 6
Dv 244,v12,15360
v_mov_b32 v197, v17
Mavv 160,30,116
s_nop 0
Tv 38,v4,14336
Tv 40,v4,20480
Mavv 180,34,116
Dv 248,v12,16384
v_mov_b32 v198, v18
Mavv 184,34,120
s_nop 0
Tv 42,v5,14336
Tv 44,v5,20480
Mavv 164,30,120
Dv 252,v12,17408
v_mov_b32 v199, v19
Mavv 168,30,124
v_mov_b32 v200, v28
v_mov_b32 v201, v29
Mavv 188,34,124
v_mov_b32 v202, v51
v_mov_b32 v203, v6
Mavv 192,34,128
v_mov_b32 v204, v14
v_mov_b32 v205, v15
Mavv 172,30,128
S_ s65,s61,405504
R_ v206,a249
R_ v207,a250
Mavv 176,30,132
S_ m0,s51,12288
R_ v96,a0
R_ v97,a1
Mavv 196,34,132
s_waitcnt vmcnt(63) lgkmcnt(0)
s_barrier
R_ v98,a2
Mavv 216,38,132
Ln v7,32,s65,0
Mavv 236,42,132
R_ v99,a3
Mavv 232,42,128
Ln v8,32,s65,3072
S_ m0,s51,13184
Mavv 212,38,128
R_ v100,a20
R_ v101,a21
Mavv 208,38,124
Ln v7,32,s65,128
Mavv 228,42,124
R_ v102,a22
R_ v103,a23
Mavv 224,42,120
Ln v8,32,s65,3200
S_ m0,s51,14080
Mavv 204,38,120
U_ v96,v96,v46
U_ v97,v97,v46
Mavv 200,38,116
Ln v7,32,s65,256
Mavv 220,42,116
U_ v98,v98,v46
U_ v99,v99,v46
U_ v100,v100,v46
U_ v101,v101,v46
U_ v102,v102,v46
U_ v103,v103,v46
C_ 76,96,97
C_ 77,98,99
C_ 78,100,101
C_ 79,102,103
ds_write_b128 v13, v[76:79] offset:0
Ln v8,32,s65,3328
S_ s61,s61,393216
.p2align 3
R_ v104,a4
R_ v105,a5
R_ v106,a6
R_ v107,a7
R_ v108,a24
R_ v109,a25
R_ v110,a26
R_ v111,a27
v_pk_mul_f32 v[104:105], v[104:105], v[46:47] op_sel:[0,1]
v_pk_mul_f32 v[106:107], v[106:107], v[46:47] op_sel:[0,1]
v_pk_mul_f32 v[108:109], v[108:109], v[46:47] op_sel:[0,1]
v_pk_mul_f32 v[110:111], v[110:111], v[46:47] op_sel:[0,1]
C_ 80,104,105
C_ 81,106,107
C_ 82,108,109
C_ 83,110,111
ds_write_b128 v13, v[80:83] offset:1024
.p2align 3
R_ v96,a8
R_ v97,a9
R_ v98,a10
R_ v99,a11
R_ v100,a28
R_ v101,a29
R_ v102,a30
R_ v103,a31
v_pk_mul_f32 v[96:97], v[96:97], v[48:49] op_sel_hi:[1,0]
v_pk_mul_f32 v[98:99], v[98:99], v[48:49] op_sel_hi:[1,0]
v_pk_mul_f32 v[100:101], v[100:101], v[48:49] op_sel_hi:[1,0]
v_pk_mul_f32 v[102:103], v[102:103], v[48:49] op_sel_hi:[1,0]
C_ 84,96,97
C_ 85,98,99
C_ 86,100,101
C_ 87,102,103
ds_write_b128 v13, v[84:87] offset:2048
.p2align 3
R_ v104,a12
R_ v105,a13
R_ v106,a14
R_ v107,a15
R_ v108,a32
R_ v109,a33
R_ v110,a34
R_ v111,a35
v_pk_mul_f32 v[104:105], v[104:105], v[48:49] op_sel:[0,1]
v_pk_mul_f32 v[106:107], v[106:107], v[48:49] op_sel:[0,1]
v_pk_mul_f32 v[108:109], v[108:109], v[48:49] op_sel:[0,1]
v_pk_mul_f32 v[110:111], v[110:111], v[48:49] op_sel:[0,1]
C_ 88,104,105
C_ 89,106,107
C_ 90,108,109
C_ 91,110,111
ds_write_b128 v13, v[88:91] offset:3072
.p2align 3
R_ v96,a16
R_ v97,a17
R_ v98,a18
R_ v99,a19
R_ v100,a36
R_ v101,a37
R_ v102,a38
R_ v103,a39
v_pk_mul_f32 v[96:97], v[96:97], v[50:51] op_sel_hi:[1,0]
v_pk_mul_f32 v[98:99], v[98:99], v[50:51] op_sel_hi:[1,0]
v_pk_mul_f32 v[100:101], v[100:101], v[50:51] op_sel_hi:[1,0]
v_pk_mul_f32 v[102:103], v[102:103], v[50:51] op_sel_hi:[1,0]
C_ 92,96,97
C_ 93,98,99
C_ 94,100,101
C_ 95,102,103
ds_write_b128 v13, v[92:95] offset:4096
.p2align 3
R_ v104,a40
R_ v105,a41
R_ v106,a42
R_ v107,a43
R_ v108,a60
R_ v109,a61
R_ v110,a62
R_ v111,a63
v_pk_mul_f32 v[104:105], v[104:105], v[46:47] op_sel_hi:[1,0]
v_pk_mul_f32 v[106:107], v[106:107], v[46:47] op_sel_hi:[1,0]
v_pk_mul_f32 v[108:109], v[108:109], v[46:47] op_sel_hi:[1,0]
v_pk_mul_f32 v[110:111], v[110:111], v[46:47] op_sel_hi:[1,0]
C_ 76,104,105
C_ 77,106,107
C_ 78,108,109
C_ 79,110,111
ds_write_b128 v13, v[76:79] offset:5120
.p2align 3
R_ v96,a44
R_ v97,a45
R_ v98,a46
R_ v99,a47
R_ v100,a64
R_ v101,a65
R_ v102,a66
R_ v103,a67
v_pk_mul_f32 v[96:97], v[96:97], v[46:47] op_sel:[0,1]
v_pk_mul_f32 v[98:99], v[98:99], v[46:47] op_sel:[0,1]
v_pk_mul_f32 v[100:101], v[100:101], v[46:47] op_sel:[0,1]
v_pk_mul_f32 v[102:103], v[102:103], v[46:47] op_sel:[0,1]
C_ 80,96,97
C_ 81,98,99
C_ 82,100,101
C_ 83,102,103
ds_write_b128 v13, v[80:83] offset:6144
.p2align 3
R_ v104,a48
R_ v105,a49
R_ v106,a50
R_ v107,a51
R_ v108,a68
R_ v109,a69
R_ v110,a70
R_ v111,a71
v_pk_mul_f32 v[104:105], v[104:105], v[48:49] op_sel_hi:[1,0]
v_pk_mul_f32 v[106:107], v[106:107], v[48:49] op_sel_hi:[1,0]
v_pk_mul_f32 v[108:109], v[108:109], v[48:49] op_sel_hi:[1,0]
v_pk_mul_f32 v[110:111], v[110:111], v[48:49] op_sel_hi:[1,0]
C_ 84,104,105
C_ 85,106,107
C_ 86,108,109
C_ 87,110,111
ds_write_b128 v13, v[84:87] offset:7168
.p2align 3
R_ v96,a52
R_ v97,a53
R_ v98,a54
R_ v99,a55
R_ v100,a72
R_ v101,a73
R_ v102,a74
R_ v103,a75
v_pk_mul_f32 v[96:97], v[96:97], v[48:49] op_sel:[0,1]
v_pk_mul_f32 v[98:99], v[98:99], v[48:49] op_sel:[0,1]
v_pk_mul_f32 v[100:101], v[100:101], v[48:49] op_sel:[0,1]
v_pk_mul_f32 v[102:103], v[102:103], v[48:49] op_sel:[0,1]
C_ 88,96,97
C_ 89,98,99
C_ 90,100,101
C_ 91,102,103
ds_write_b128 v13, v[88:91] offset:8192
.p2align 3
R_ v104,a56
R_ v105,a57
R_ v106,a58
R_ v107,a59
R_ v108,a76
R_ v109,a77
R_ v110,a78
R_ v111,a79
v_pk_mul_f32 v[104:105], v[104:105], v[50:51] op_sel_hi:[1,0]
v_pk_mul_f32 v[106:107], v[106:107], v[50:51] op_sel_hi:[1,0]
v_pk_mul_f32 v[108:109], v[108:109], v[50:51] op_sel_hi:[1,0]
v_pk_mul_f32 v[110:111], v[110:111], v[50:51] op_sel_hi:[1,0]
C_ 92,104,105
C_ 93,106,107
C_ 94,108,109
C_ 95,110,111
ds_write_b128 v13, v[92:95] offset:9216
.p2align 3
R_ v104,a92
R_ v105,a93
R_ v106,a94
R_ v107,a95
R_ v108,a112
R_ v109,a113
R_ v110,a114
R_ v111,a115
v_pk_mul_f32 v[104:105], v[104:105], v[48:49] op_sel:[0,1]
v_pk_mul_f32 v[106:107], v[106:107], v[48:49] op_sel:[0,1]
v_pk_mul_f32 v[108:109], v[108:109], v[48:49] op_sel:[0,1]
v_pk_mul_f32 v[110:111], v[110:111], v[48:49] op_sel:[0,1]
C_ 88,104,105
C_ 89,106,107
C_ 90,108,109
C_ 91,110,111
X_ a240,v88
X_ a241,v89
X_ a242,v90
X_ a243,v91
.p2align 3
R_ v96,a96
R_ v97,a97
R_ v98,a98
R_ v99,a99
R_ v100,a116
R_ v101,a117
R_ v102,a118
R_ v103,a119
v_pk_mul_f32 v[96:97], v[96:97], v[50:51] op_sel_hi:[1,0]
v_pk_mul_f32 v[98:99], v[98:99], v[50:51] op_sel_hi:[1,0]
v_pk_mul_f32 v[100:101], v[100:101], v[50:51] op_sel_hi:[1,0]
v_pk_mul_f32 v[102:103], v[102:103], v[50:51] op_sel_hi:[1,0]
C_ 92,96,97
C_ 93,98,99
C_ 94,100,101
C_ 95,102,103
X_ a244,v92
X_ a245,v93
X_ a246,v94
X_ a247,v95
.p2align 3
R_ v96,a124
R_ v97,a125
R_ v98,a126
R_ v99,a127
R_ v100,a144
R_ v101,a145
R_ v102,a146
R_ v103,a147
v_pk_mul_f32 v[96:97], v[96:97], v[46:47] op_sel:[0,1]
v_pk_mul_f32 v[98:99], v[98:99], v[46:47] op_sel:[0,1]
v_pk_mul_f32 v[100:101], v[100:101], v[46:47] op_sel:[0,1]
v_pk_mul_f32 v[102:103], v[102:103], v[46:47] op_sel:[0,1]
C_ 80,96,97
C_ 81,98,99
C_ 82,100,101
C_ 83,102,103
v_mov_b32 v28, v80
v_mov_b32 v29, v81
v_mov_b32 v51, v82
v_mov_b32 v6, v83
.p2align 3
R_ v104,a128
R_ v105,a129
R_ v106,a130
R_ v107,a131
R_ v108,a148
R_ v109,a149
R_ v110,a150
R_ v111,a151
v_pk_mul_f32 v[104:105], v[104:105], v[48:49] op_sel_hi:[1,0]
v_pk_mul_f32 v[106:107], v[106:107], v[48:49] op_sel_hi:[1,0]
v_pk_mul_f32 v[108:109], v[108:109], v[48:49] op_sel_hi:[1,0]
v_pk_mul_f32 v[110:111], v[110:111], v[48:49] op_sel_hi:[1,0]
C_ 84,104,105
C_ 85,106,107
C_ 86,108,109
C_ 87,110,111
v_mov_b32 v14, v84
v_mov_b32 v15, v85
X_ a249,v86
X_ a250,v87
.p2align 3
R_ v96,a132
R_ v97,a133
R_ v98,a134
R_ v99,a135
R_ v100,a152
R_ v101,a153
R_ v102,a154
R_ v103,a155
v_pk_mul_f32 v[96:97], v[96:97], v[48:49] op_sel:[0,1]
v_pk_mul_f32 v[98:99], v[98:99], v[48:49] op_sel:[0,1]
v_pk_mul_f32 v[100:101], v[100:101], v[48:49] op_sel:[0,1]
v_pk_mul_f32 v[102:103], v[102:103], v[48:49] op_sel:[0,1]
C_ 88,96,97
C_ 89,98,99
C_ 90,100,101
C_ 91,102,103
ds_write_b128 v12, v[88:91] offset:0
.p2align 3
R_ v104,a136
R_ v105,a137
R_ v106,a138
R_ v107,a139
R_ v108,a156
R_ v109,a157
R_ v110,a158
R_ v111,a159
v_pk_mul_f32 v[104:105], v[104:105], v[50:51] op_sel_hi:[1,0]
v_pk_mul_f32 v[106:107], v[106:107], v[50:51] op_sel_hi:[1,0]
v_pk_mul_f32 v[108:109], v[108:109], v[50:51] op_sel_hi:[1,0]
v_pk_mul_f32 v[110:111], v[110:111], v[50:51] op_sel_hi:[1,0]
C_ 92,104,105
C_ 93,106,107
C_ 94,108,109
C_ 95,110,111
ds_write_b128 v12, v[92:95] offset:1024
.p2align 3
R_ v96,a160
R_ v97,a161
R_ v98,a162
R_ v99,a163
R_ v100,a180
R_ v101,a181
R_ v102,a182
R_ v103,a183
v_pk_mul_f32 v[96:97], v[96:97], v[46:47] op_sel_hi:[1,0]
v_pk_mul_f32 v[98:99], v[98:99], v[46:47] op_sel_hi:[1,0]
v_pk_mul_f32 v[100:101], v[100:101], v[46:47] op_sel_hi:[1,0]
v_pk_mul_f32 v[102:103], v[102:103], v[46:47] op_sel_hi:[1,0]
C_ 76,96,97
C_ 77,98,99
C_ 78,100,101
C_ 79,102,103
ds_write_b128 v12, v[76:79] offset:2048
.p2align 3
R_ v104,a164
R_ v105,a165
R_ v106,a166
R_ v107,a167
R_ v108,a184
R_ v109,a185
R_ v110,a186
R_ v111,a187
v_pk_mul_f32 v[104:105], v[104:105], v[46:47] op_sel:[0,1]
v_pk_mul_f32 v[106:107], v[106:107], v[46:47] op_sel:[0,1]
v_pk_mul_f32 v[108:109], v[108:109], v[46:47] op_sel:[0,1]
v_pk_mul_f32 v[110:111], v[110:111], v[46:47] op_sel:[0,1]
C_ 80,104,105
C_ 81,106,107
C_ 82,108,109
C_ 83,110,111
ds_write_b128 v12, v[80:83] offset:3072
.p2align 3
R_ v96,a168
R_ v97,a169
R_ v98,a170
R_ v99,a171
R_ v100,a188
R_ v101,a189
R_ v102,a190
R_ v103,a191
v_pk_mul_f32 v[96:97], v[96:97], v[48:49] op_sel_hi:[1,0]
v_pk_mul_f32 v[98:99], v[98:99], v[48:49] op_sel_hi:[1,0]
v_pk_mul_f32 v[100:101], v[100:101], v[48:49] op_sel_hi:[1,0]
v_pk_mul_f32 v[102:103], v[102:103], v[48:49] op_sel_hi:[1,0]
C_ 84,96,97
C_ 85,98,99
C_ 86,100,101
C_ 87,102,103
ds_write_b128 v12, v[84:87] offset:4096
.p2align 3
R_ v104,a172
R_ v105,a173
R_ v106,a174
R_ v107,a175
R_ v108,a192
R_ v109,a193
R_ v110,a194
R_ v111,a195
v_pk_mul_f32 v[104:105], v[104:105], v[48:49] op_sel:[0,1]
v_pk_mul_f32 v[106:107], v[106:107], v[48:49] op_sel:[0,1]
v_pk_mul_f32 v[108:109], v[108:109], v[48:49] op_sel:[0,1]
v_pk_mul_f32 v[110:111], v[110:111], v[48:49] op_sel:[0,1]
C_ 88,104,105
C_ 89,106,107
C_ 90,108,109
C_ 91,110,111
ds_write_b128 v12, v[88:91] offset:5120
.p2align 3
R_ v96,a176
R_ v97,a177
R_ v98,a178
R_ v99,a179
R_ v100,a196
R_ v101,a197
R_ v102,a198
R_ v103,a199
v_pk_mul_f32 v[96:97], v[96:97], v[50:51] op_sel_hi:[1,0]
v_pk_mul_f32 v[98:99], v[98:99], v[50:51] op_sel_hi:[1,0]
v_pk_mul_f32 v[100:101], v[100:101], v[50:51] op_sel_hi:[1,0]
v_pk_mul_f32 v[102:103], v[102:103], v[50:51] op_sel_hi:[1,0]
C_ 92,96,97
C_ 93,98,99
C_ 94,100,101
C_ 95,102,103
ds_write_b128 v12, v[92:95] offset:12288
.p2align 3
R_ v104,a200
R_ v105,a201
R_ v106,a202
R_ v107,a203
R_ v108,a220
R_ v109,a221
R_ v110,a222
R_ v111,a223
v_pk_mul_f32 v[104:105], v[104:105], v[46:47] op_sel_hi:[1,0]
v_pk_mul_f32 v[106:107], v[106:107], v[46:47] op_sel_hi:[1,0]
v_pk_mul_f32 v[108:109], v[108:109], v[46:47] op_sel_hi:[1,0]
v_pk_mul_f32 v[110:111], v[110:111], v[46:47] op_sel_hi:[1,0]
C_ 76,104,105
C_ 77,106,107
C_ 78,108,109
C_ 79,110,111
ds_write_b128 v12, v[76:79] offset:13312
.p2align 3
R_ v96,a204
R_ v97,a205
R_ v98,a206
R_ v99,a207
R_ v100,a224
R_ v101,a225
R_ v102,a226
R_ v103,a227
v_pk_mul_f32 v[96:97], v[96:97], v[46:47] op_sel:[0,1]
v_pk_mul_f32 v[98:99], v[98:99], v[46:47] op_sel:[0,1]
v_pk_mul_f32 v[100:101], v[100:101], v[46:47] op_sel:[0,1]
v_pk_mul_f32 v[102:103], v[102:103], v[46:47] op_sel:[0,1]
C_ 80,96,97
C_ 81,98,99
C_ 82,100,101
C_ 83,102,103
ds_write_b128 v12, v[80:83] offset:14336
.p2align 3
R_ v104,a208
R_ v105,a209
R_ v106,a210
R_ v107,a211
R_ v108,a228
R_ v109,a229
R_ v110,a230
R_ v111,a231
v_pk_mul_f32 v[104:105], v[104:105], v[48:49] op_sel_hi:[1,0]
v_pk_mul_f32 v[106:107], v[106:107], v[48:49] op_sel_hi:[1,0]
v_pk_mul_f32 v[108:109], v[108:109], v[48:49] op_sel_hi:[1,0]
v_pk_mul_f32 v[110:111], v[110:111], v[48:49] op_sel_hi:[1,0]
C_ 84,104,105
C_ 85,106,107
C_ 86,108,109
C_ 87,110,111
ds_write_b128 v12, v[84:87] offset:15360
s_waitcnt vmcnt(6) lgkmcnt(0)
s_barrier
Dv 30,%0,6144
Dv 34,v1,6144
Dv 38,%0,7168
.p2align 3
R_ v96,a212
R_ v97,a213
R_ v98,a214
R_ v99,a215
R_ v100,a232
R_ v101,a233
R_ v102,a234
R_ v103,a235
v_pk_mul_f32 v[96:97], v[96:97], v[48:49] op_sel:[0,1]
v_pk_mul_f32 v[98:99], v[98:99], v[48:49] op_sel:[0,1]
v_pk_mul_f32 v[100:101], v[100:101], v[48:49] op_sel:[0,1]
v_pk_mul_f32 v[102:103], v[102:103], v[48:49] op_sel:[0,1]
C_ 88,96,97
C_ 89,98,99
C_ 90,100,101
C_ 91,102,103
ds_write_b128 v12, v[88:91] offset:16384
.p2align 3
R_ v104,a216
R_ v105,a217
R_ v106,a218
R_ v107,a219
R_ v108,a236
R_ v109,a237
R_ v110,a238
R_ v111,a239
v_pk_mul_f32 v[104:105], v[104:105], v[50:51] op_sel_hi:[1,0]
v_pk_mul_f32 v[106:107], v[106:107], v[50:51] op_sel_hi:[1,0]
v_pk_mul_f32 v[108:109], v[108:109], v[50:51] op_sel_hi:[1,0]
v_pk_mul_f32 v[110:111], v[110:111], v[50:51] op_sel_hi:[1,0]
C_ 92,104,105
C_ 93,106,107
C_ 94,108,109
C_ 95,110,111
ds_write_b128 v12, v[92:95] offset:17408
.Latt3:
W_ 4
Zvvv 96,30,136
Dv 42,v1,7168
R_ v116,a80
Zvvv 100,30,140
R_ v117,a81
R_ v118,a82
Zvvv 104,30,144
R_ v119,a83
R_ v120,a100
Zvvv 108,30,148
R_ v121,a101
R_ v122,a102
Zvvv 112,30,152
W_ 4
R_ v123,a103
U_ v116,v116,v46
Mvvv 96,34,156
Dv 30,%0,8192
U_ v117,v117,v46
Mvvv 100,34,160
U_ v118,v118,v46
U_ v119,v119,v46
Mvvv 104,34,164
U_ v120,v120,v46
U_ v121,v121,v46
Mvvv 108,34,168
U_ v122,v122,v46
U_ v123,v123,v46
Mvvv 112,34,172
W_ 4
C_ 20,116,117
C_ 21,118,119
Mvvv 96,38,176
Dv 34,v1,8192
C_ 22,120,121
Mvvv 100,38,180
C_ 23,122,123
R_ v124,a84
Mvvv 104,38,184
R_ v125,a85
R_ v126,a86
Mvvv 108,38,188
R_ v127,a87
R_ v128,a104
Mvvv 112,38,192
W_ 2
R_ v129,a105
R_ v130,a106
Mvvv 96,42,196
Dv 38,%0,0
R_ v131,a107
Mvvv 100,42,200
U_ v124,v124,v47
U_ v125,v125,v47
Mvvv 104,42,204
U_ v126,v126,v47
U_ v127,v127,v47
Mvvv 108,42,208
U_ v128,v128,v47
U_ v129,v129,v47
Mvvv 112,42,212
W_ 2
U_ v130,v130,v47
U_ v131,v131,v47
Mvvv 96,30,216
Dv 42,v1,0
C_ 24,124,125
Mvvv 100,30,220
C_ 25,126,127
C_ 26,128,129
Mvvv 104,30,224
C_ 27,130,131
R_ v116,a88
Mvvv 108,30,228
R_ v117,a89
R_ v118,a90
Mvvv 112,30,232
W_ 2
R_ v119,a91
R_ v120,a108
Mvvv 96,34,236
Dv 30,%0,1024
R_ v121,a109
Mvvv 100,34,240
R_ v122,a110
R_ v123,a111
Mvvv 104,34,244
U_ v116,v116,v48
U_ v117,v117,v48
Mvvv 108,34,248
U_ v118,v118,v48
U_ v119,v119,v48
Mvvv 112,34,252
W_ 2
U_ v120,v120,v48
U_ v121,v121,v48
Zvvv 76,38,136
Dv 34,v1,1024
U_ v122,v122,v48
Zvvv 80,38,140
U_ v123,v123,v48
C_ 52,116,117
Zvvv 84,38,144
C_ 53,118,119
C_ 54,120,121
Zvvv 88,38,148
C_ 55,122,123
R_ v124,a120
Zvvv 92,38,152
W_ 2
R_ v125,a121
R_ v126,a122
Mvvv 76,42,156
Dv 38,%0,2048
R_ v127,a123
Mvvv 80,42,160
R_ v128,a140
R_ v129,a141
Mvvv 84,42,164
R_ v130,a142
R_ v131,a143
Mvvv 88,42,168
U_ v124,v124,v46
U_ v125,v125,v46
Mvvv 92,42,172
W_ 2
U_ v126,v126,v46
U_ v127,v127,v46
Mvvv 76,30,176
Dv 42,v1,2048
U_ v128,v128,v46
Mvvv 80,30,180
U_ v129,v129,v46
U_ v130,v130,v46
Mvvv 84,30,184
U_ v131,v131,v46
C_ 16,124,125
Mvvv 88,30,188
C_ 17,126,127
C_ 18,128,129
Mvvv 92,30,192
s_waitcnt vmcnt(0) lgkmcnt(0)
s_barrier
Mvvv 76,34,196
S_ s65,s61,24576
Dv 30,%0,18432
C_ 19,130,131
Mvvv 76,38,216
S_ m0,s51,0
v_max3_f32 v56, v96, v97, v98
v_max_f32 v56, v56, v99
Mvvv 76,42,236
Ln v7,32,s65,0
Mvvv 80,34,200
v_max3_f32 v60, v100, v101, v102
v_max_f32 v60, v60, v103
Mvvv 80,38,220
Ln v8,32,s65,3072
Mvvv 80,42,240
S_ m0,s51,896
v_max3_f32 v64, v104, v105, v106
v_max_f32 v64, v64, v107
Mvvv 84,34,204
Ln v7,32,s65,128
Mvvv 84,38,224
v_max3_f32 v68, v108, v109, v110
v_max_f32 v68, v68, v111
Mvvv 84,42,244
Ln v8,32,s65,3200
Mvvv 88,34,208
S_ m0,s51,1792
v_max3_f32 v72, v112, v113, v114
v_max_f32 v72, v72, v115
Mvvv 88,38,228
Ln v7,32,s65,256
Mvvv 88,42,248
v_max3_f32 v56, v56, v76, v77
v_max3_f32 v56, v56, v78, v79
Mvvv 92,34,212
Ln v8,32,s65,3328
Mvvv 92,38,232
v_max3_f32 v60, v60, v80, v81
v_max3_f32 v60, v60, v82, v83
Mvvv 92,42,252
v_max3_f32 v64, v64, v84, v85
v_max3_f32 v64, v64, v86, v87
Dv 34,v1,18432
.p2align 3
v_mov_b32 v11, 0x3f803f80
v_permlane32_swap_b32 v56, v60
v_max3_f32 v68, v68, v88, v89
v_max3_f32 v68, v68, v90, v91
v_max_f32 v56, v56, v60
v_max3_f32 v72, v72, v92, v93
v_max3_f32 v72, v72, v94, v95
v_mov_b32 v73, v72
v_permlane32_swap_b32 v64, v68
v_max_f32 v64, v64, v68
v_permlane32_swap_b32 v72, v73
v_max_f32 v72, v72, v73
v_mov_b32 v73, v72
v_permlane16_swap_b32 v56, v64
v_max_f32 v56, v56, v64
v_permlane16_swap_b32 v72, v73
B_ v56,0xc0e00000,v56
v_max_f32 v72, v72, v73
v_mov_b32 v64, v56
B_ v72,0xc0e00000,v72
v_mov_b32 v73, v72
v_permlane16_swap_b32 v56, v64
v_mov_b32 v60, v56
v_mov_b32 v68, v64
v_mov_b64 v[74:75], v[72:73]
v_permlane32_swap_b32 v56, v60
v_permlane32_swap_b32 v64, v68
v_mov_b32 v57, v56
v_mov_b32 v61, v60
v_mov_b32 v65, v64
v_mov_b32 v69, v68
v_pk_add_f32 v[96:97], v[96:97], v[56:57]
v_pk_add_f32 v[98:99], v[98:99], v[56:57]
v_pk_add_f32 v[76:77], v[76:77], v[56:57]
v_pk_add_f32 v[78:79], v[78:79], v[56:57]
v_mov_b64 v[58:59], v[56:57]
v_pk_add_f32 v[100:101], v[100:101], v[60:61]
v_pk_add_f32 v[102:103], v[102:103], v[60:61]
v_pk_add_f32 v[80:81], v[80:81], v[60:61]
v_pk_add_f32 v[82:83], v[82:83], v[60:61]
v_mov_b64 v[62:63], v[60:61]
v_pk_add_f32 v[104:105], v[104:105], v[64:65]
v_pk_add_f32 v[106:107], v[106:107], v[64:65]
v_pk_add_f32 v[84:85], v[84:85], v[64:65]
v_pk_add_f32 v[86:87], v[86:87], v[64:65]
v_mov_b64 v[66:67], v[64:65]
v_pk_add_f32 v[108:109], v[108:109], v[68:69]
v_pk_add_f32 v[110:111], v[110:111], v[68:69]
v_pk_add_f32 v[88:89], v[88:89], v[68:69]
v_pk_add_f32 v[90:91], v[90:91], v[68:69]
v_mov_b64 v[70:71], v[68:69]
v_pk_add_f32 v[112:113], v[112:113], v[72:73]
v_pk_add_f32 v[114:115], v[114:115], v[72:73]
v_pk_add_f32 v[92:93], v[92:93], v[72:73]
v_pk_add_f32 v[94:95], v[94:95], v[72:73]
E_ 96
E_ 97
E_ 98
E_ 99
E_ 76
E_ 77
E_ 78
E_ 79
C_ 99,98,99
C_ 98,96,97
C_ 96,76,77
C_ 97,78,79
E_ 100
E_ 101
E_ 102
E_ 103
E_ 80
E_ 81
E_ 82
E_ 83
C_ 103,102,103
C_ 102,100,101
C_ 100,80,81
C_ 101,82,83
.Lstep3_1:
W_ 1
Nvvv 116,30,136,56
s_nop 0
Dv 38,%0,19456
Dv 42,v1,19456
W_ 2
Mvvv 116,34,156
E_ 104
Nvvv 120,30,140,60
E_ 105
Mvvv 120,34,160
E_ 106
Nvvv 124,30,144,64
E_ 107
Mvvv 124,34,164
E_ 84
Nvvv 128,30,148,68
E_ 85
Mvvv 128,34,168
E_ 86
Nvvv 132,30,152,72
E_ 87
Mvvv 132,34,172
W_ 0
C_ 107,106,107
C_ 106,104,105
Mvvv 132,38,192
s_nop 0
Dv 30,%0,20480
Dv 34,v1,20480
Mvvv 132,42,212
C_ 104,84,85
C_ 105,86,87
Mvvv 128,38,188
E_ 108
Mvvv 128,42,208
E_ 109
Mvvv 124,38,184
E_ 110
Mvvv 124,42,204
E_ 111
Mvvv 120,38,180
E_ 88
Mvvv 120,42,200
E_ 89
Mvvv 116,38,176
E_ 90
Mvvv 116,42,196
W_ 0
E_ 91
Mvvv 116,30,216
s_nop 0
Dv 38,%0,12288
Dv 42,v1,12288
Mvvv 116,34,236
C_ 111,110,111
C_ 110,108,109
Mvvv 120,30,220
C_ 108,88,89
C_ 109,90,91
Mvvv 120,34,240
E_ 112
Mvvv 124,30,224
E_ 113
Mvvv 124,34,244
E_ 114
Mvvv 128,30,228
E_ 115
Mvvv 128,34,248
E_ 92
Mvvv 132,30,232
E_ 93
Mvvv 132,34,252
W_ 0
E_ 94
Nvvv 76,38,136,56
s_nop 0
Dv 30,%0,13312
Dv 34,v1,13312
Mvvv 76,42,156
E_ 95
Nvvv 80,38,140,60
C_ 115,114,115
C_ 114,112,113
Mvvv 80,42,160
C_ 112,92,93
C_ 113,94,95
Nvvv 84,38,144,64
E_ 116
Mvvv 84,42,164
E_ 117
Nvvv 88,38,148,68
E_ 118
Mvvv 88,42,168
E_ 119
Nvvv 92,38,152,72
C_ 119,118,119
C_ 118,116,117
Mvvv 92,42,172
W_ 0
E_ 120
Mvvv 92,30,192
s_nop 0
Dv 38,%0,14336
Dv 42,v1,14336
Mvvv 92,34,212
X_ a180,v11
X_ a181,v11
Mvvv 88,30,188
X_ a182,v11
X_ a183,v11
Mvvv 88,34,208
E_ 121
Mvvv 84,30,184
E_ 122
Mvvv 84,34,204
E_ 123
Mvvv 80,30,180
C_ 123,122,123
C_ 122,120,121
Mvvv 80,34,200
E_ 124
Mvvv 76,30,176
E_ 125
Mvvv 76,34,196
S_ s65,s61,12288
E_ 126
W_ 1
Mvvv 76,38,216
S_ m0,s51,12288
E_ 127
W_ 0
Mvvv 76,42,236
s_waitcnt vmcnt(18) lgkmcnt(0)
s_barrier
C_ 127,126,127
Mvvv 80,38,220
L_ v9,36,s65,0
Mvvv 80,42,240
C_ 126,124,125
Mvvv 84,38,224
L_ v9,36,s65,3072
S_ m0,s51,13184
Mvvv 84,42,244
s_nop 0
Tv 30,v2,24576
Tv 32,v2,30720
Mvvv 88,38,228
L_ v9,36,s65,128
Mvvv 88,42,248
s_nop 0
Tv 34,v3,24576
Tv 36,v3,30720
Mvvv 92,38,232
L_ v9,36,s65,3200
S_ m0,s51,14080
Mvvv 92,42,252
E_ 128
Zaav 220,180,96
L_ v9,36,s65,256
Zaav 224,180,100
E_ 129
Zaav 228,180,104
L_ v9,36,s65,3328
Zaav 232,180,108
E_ 130
Zaav 236,180,112
W_ 0
E_ 131
Zavv 0,30,96
s_nop 0
Tv 38,v4,24576
Tv 40,v4,30720
Zavv 20,34,96
C_ 131,130,131
C_ 130,128,129
Zavv 24,34,100
s_nop 0
Tv 42,v5,24576
Tv 44,v5,30720
Zavv 4,30,100
E_ 132
Zavv 8,30,104
E_ 133
Zavv 28,34,104
R_ v46,a220
R_ v47,a224
Zavv 32,34,108
R_ v48,a228
R_ v49,a232
Zavv 12,30,108
R_ v50,a236
Zavv 16,30,112
E_ 134
Zavv 36,34,112
W_ 0
E_ 135
Zavv 56,38,112
s_nop 0
Tv 30,v2,25600
Tv 32,v2,31744
Zavv 76,42,112
C_ 135,134,135
C_ 134,132,133
Zavv 72,42,108
s_nop 0
Tv 34,v3,25600
Tv 36,v3,31744
Zavv 52,38,108
E_ 76
Zavv 48,38,104
E_ 77
Zavv 68,42,104
E_ 78
Zavv 64,42,100
E_ 79
Zavv 44,38,100
C_ 116,76,77
C_ 117,78,79
Zavv 40,38,96
E_ 80
Zavv 60,42,96
W_ 0
E_ 81
Zavv 80,30,96
s_nop 0
Tv 38,v4,25600
Tv 40,v4,31744
Zavv 100,34,96
E_ 82
Zavv 104,34,100
s_nop 0
Tv 42,v5,25600
Tv 44,v5,31744
Zavv 84,30,100
E_ 83
Zavv 88,30,104
C_ 120,80,81
C_ 121,82,83
Zavv 108,34,104
E_ 84
Zavv 112,34,108
E_ 85
Zavv 92,30,108
E_ 86
Zavv 96,30,112
E_ 87
Zavv 116,34,112
W_ 0
C_ 124,84,85
C_ 125,86,87
Zavv 136,38,112
s_nop 0
Tv 30,v2,26624
Tv 32,v2,32768
Zavv 156,42,112
E_ 88
Zavv 152,42,108
s_nop 0
Tv 34,v3,26624
Tv 36,v3,32768
Zavv 132,38,108
E_ 89
Zavv 128,38,104
E_ 90
Zavv 148,42,104
E_ 91
Zavv 144,42,100
C_ 128,88,89
C_ 129,90,91
Zavv 124,38,100
E_ 92
Zavv 120,38,96
E_ 93
Zavv 140,42,96
W_ 0
E_ 94
Zavv 160,30,96
s_nop 0
Tv 38,v4,26624
Tv 40,v4,32768
Zavv 180,34,96
E_ 95
Zavv 184,34,100
s_nop 0
Tv 42,v5,26624
Tv 44,v5,32768
Zavv 164,30,100
C_ 132,92,93
C_ 133,94,95
Zavv 168,30,104
v_mov_b32 v92, 0x3f803f80
v_mov_b32 v93, 0x3f803f80
Zavv 188,34,104
S_ s65,s61,36864
v_mov_b32 v94, 0x3f803f80
v_mov_b32 v95, 0x3f803f80
Zavv 192,34,108
S_ m0,s51,24576
Zavv 172,30,108
s_waitcnt vmcnt(6) lgkmcnt(0)
s_barrier
Zavv 176,30,112
Ln v7,32,s65,0
Zavv 196,34,112
Zavv 216,38,112
Dv 30,%0,6144
Zavv 236,42,112
Dv 34,v1,6144
Zavv 232,42,108
Ln v8,32,s65,3072
S_ m0,s51,25472
Zavv 212,38,108
Zavv 208,38,104
Ln v7,32,s65,128
Zavv 228,42,104
Zavv 224,42,100
Ln v8,32,s65,3200
S_ m0,s51,26368
Zavv 204,38,100
Zavv 200,38,96
Ln v7,32,s65,256
Zavv 220,42,96
Ln v8,32,s65,3328
W_ 0
s_nop 7
s_branch .Lstep1_2
.Latt1:
R_ v13,a248
S_ s53,s51,36864
.Lpad_a1_0:
s_cbranch_execz .Lhop_a1_r
v_add_u32 v12, s53, v13
s_mul_i32 s53, s4, 10240
S_ s53,s53,0
v_add_u32 v13, s53, v13
W_ 2
Zvvv 96,30,136
Dv 42,v1,44032
R_ v2,a245
Zvvv 100,30,140
ds_write_b128 v13, a[0:3] offset:0
Zvvv 104,30,144
v_xor_b32 v3, 32, v2
v_xor_b32 v4, 64, v2
Zvvv 108,30,148
ds_write_b128 v13, a[4:7] offset:1024
Zvvv 112,30,152
W_ 4
v_xor_b32 v5, 96, v2
s_mov_b64 s[24:25], s[16:17]
Mvvv 96,34,156
s_mov_b32 s26, 7864320
Dv 30,%0,45056
s_mov_b64 s[28:29], s[22:23]
Mvvv 100,34,160
ds_write_b128 v13, a[8:11] offset:2048
Mvvv 104,34,164
s_mov_b32 s30, 1638400
R_ v20,a80
s_mul_i32 s52, s4, 10240
Mvvv 108,34,168
ds_write_b128 v13, a[12:15] offset:3072
Mvvv 112,34,172
W_ 6
R_ v21,a81
R_ v22,a82
Mvvv 96,38,176
Dv 34,v1,45056
R_ v23,a83
Mvvv 100,38,180
ds_write_b128 v13, a[16:19] offset:4096
Mvvv 104,38,184
R_ v24,a84
R_ v25,a85
Mvvv 108,38,188
ds_write_b128 v13, a[40:43] offset:5120
Mvvv 112,38,192
W_ 8
R_ v26,a86
R_ v27,a87
Mvvv 96,42,196
Dv 38,%0,36864
R_ v52,a88
Mvvv 100,42,200
ds_write_b128 v13, a[44:47] offset:6144
Mvvv 104,42,204
R_ v53,a89
R_ v54,a90
Mvvv 108,42,208
ds_write_b128 v13, a[48:51] offset:7168
Mvvv 112,42,212
W_ 8
R_ v55,a91
v_accvgpr_mov_b32 a240, a92
Mvvv 96,30,216
Dv 42,v1,36864
v_accvgpr_mov_b32 a241, a93
Mvvv 100,30,220
ds_write_b128 v13, a[52:55] offset:8192
Mvvv 104,30,224
v_accvgpr_mov_b32 a242, a94
v_accvgpr_mov_b32 a243, a95
Mvvv 108,30,228
ds_write_b128 v13, a[56:59] offset:9216
Mvvv 112,30,232
W_ 8
v_accvgpr_mov_b32 a244, a96
v_accvgpr_mov_b32 a245, a97
Mvvv 96,34,236
Dv 30,%0,37888
v_accvgpr_mov_b32 a246, a98
Mvvv 100,34,240
v_accvgpr_mov_b32 a247, a99
R_ v16,a120
Mvvv 104,34,244
R_ v17,a121
R_ v18,a122
Mvvv 108,34,248
R_ v19,a123
R_ v28,a124
Mvvv 112,34,252
W_ 6
R_ v29,a125
R_ v51,a126
Zvvv 76,38,136
Dv 34,v1,37888
R_ v6,a127
Zvvv 80,38,140
R_ v14,a128
R_ v15,a129
Zvvv 84,38,144
v_accvgpr_mov_b32 a249, a130
v_accvgpr_mov_b32 a250, a131
Zvvv 88,38,148
v_max3_f32 v56, v96, v97, v98
v_max_f32 v56, v56, v99
Zvvv 92,38,152
W_ 4
v_max3_f32 v60, v100, v101, v102
v_max_f32 v60, v60, v103
Mvvv 76,42,156
Dv 38,%0,38912
v_max3_f32 v64, v104, v105, v106
Mvvv 80,42,160
v_max_f32 v64, v64, v107
Mvvv 84,42,164
v_max3_f32 v68, v108, v109, v110
v_max_f32 v68, v68, v111
Mvvv 88,42,168
v_max3_f32 v72, v112, v113, v114
v_max_f32 v72, v72, v115
Mvvv 92,42,172
W_ 2
Mvvv 76,30,176
Dv 42,v1,38912
Mvvv 80,30,180
Mvvv 84,30,184
Mvvv 88,30,188
Mvvv 92,30,192
s_waitcnt vmcnt(30) lgkmcnt(0)
s_barrier
Mvvv 76,34,196
Dv 30,%0,55296
Mvvv 76,38,216
Mvvv 76,42,236
Mvvv 80,34,200
Mvvv 80,38,220
Mvvv 80,42,240
Mvvv 84,34,204
s_nop 3
v_max3_f32 v56, v56, v76, v77
v_max3_f32 v56, v56, v78, v79
Mvvv 84,38,224
Mvvv 84,42,244
Mvvv 88,34,208
v_max3_f32 v60, v60, v80, v81
v_max3_f32 v60, v60, v82, v83
Mvvv 88,38,228
Mvvv 88,42,248
Mvvv 92,34,212
s_nop 1
v_max3_f32 v64, v64, v84, v85
v_max3_f32 v64, v64, v86, v87
Mvvv 92,38,232
Mvvv 92,42,252
Dv 34,v1,55296
.p2align 3
v_mov_b32 v11, 0x3f803f80
v_permlane32_swap_b32 v56, v60
v_max3_f32 v68, v68, v88, v89
v_max3_f32 v68, v68, v90, v91
v_max_f32 v56, v56, v60
s_nop 1
v_max3_f32 v72, v72, v92, v93
v_max3_f32 v72, v72, v94, v95
v_mov_b32 v73, v72
v_permlane32_swap_b32 v64, v68
v_max_f32 v64, v64, v68
v_permlane32_swap_b32 v72, v73
v_max_f32 v72, v72, v73
v_mov_b32 v73, v72
v_permlane16_swap_b32 v56, v64
v_max_f32 v56, v56, v64
v_permlane16_swap_b32 v72, v73
B_ v56,0xc0e00000,v56
v_max_f32 v72, v72, v73
v_mov_b32 v64, v56
B_ v72,0xc0e00000,v72
v_mov_b32 v73, v72
v_permlane16_swap_b32 v56, v64
v_mov_b32 v60, v56
v_mov_b32 v68, v64
v_mov_b64 v[74:75], v[72:73]
v_permlane32_swap_b32 v56, v60
v_permlane32_swap_b32 v64, v68
v_mov_b32 v57, v56
v_mov_b32 v61, v60
v_mov_b32 v65, v64
v_mov_b32 v69, v68
v_pk_add_f32 v[96:97], v[96:97], v[56:57]
v_pk_add_f32 v[98:99], v[98:99], v[56:57]
v_pk_add_f32 v[76:77], v[76:77], v[56:57]
v_pk_add_f32 v[78:79], v[78:79], v[56:57]
v_mov_b64 v[58:59], v[56:57]
v_pk_add_f32 v[100:101], v[100:101], v[60:61]
v_pk_add_f32 v[102:103], v[102:103], v[60:61]
v_pk_add_f32 v[80:81], v[80:81], v[60:61]
v_pk_add_f32 v[82:83], v[82:83], v[60:61]
v_mov_b64 v[62:63], v[60:61]
v_pk_add_f32 v[104:105], v[104:105], v[64:65]
v_pk_add_f32 v[106:107], v[106:107], v[64:65]
v_pk_add_f32 v[84:85], v[84:85], v[64:65]
v_pk_add_f32 v[86:87], v[86:87], v[64:65]
v_mov_b64 v[66:67], v[64:65]
v_pk_add_f32 v[108:109], v[108:109], v[68:69]
v_pk_add_f32 v[110:111], v[110:111], v[68:69]
v_pk_add_f32 v[88:89], v[88:89], v[68:69]
v_pk_add_f32 v[90:91], v[90:91], v[68:69]
v_mov_b64 v[70:71], v[68:69]
v_pk_add_f32 v[112:113], v[112:113], v[72:73]
v_pk_add_f32 v[114:115], v[114:115], v[72:73]
v_pk_add_f32 v[92:93], v[92:93], v[72:73]
v_pk_add_f32 v[94:95], v[94:95], v[72:73]
E_ 96
E_ 97
E_ 98
E_ 99
E_ 76
E_ 77
E_ 78
E_ 79
C_ 99,98,99
C_ 98,96,97
C_ 96,76,77
C_ 97,78,79
E_ 100
E_ 101
E_ 102
E_ 103
E_ 80
E_ 81
E_ 82
E_ 83
C_ 103,102,103
C_ 102,100,101
C_ 100,80,81
C_ 101,82,83
.Lstep1_1:
W_ 1
Nvvv 116,30,136,56
s_nop 0
Dv 38,%0,56320
Dv 42,v1,56320
W_ 2
Mvvv 116,34,156
E_ 104
Nvvv 120,30,140,60
E_ 105
Mvvv 120,34,160
E_ 106
Nvvv 124,30,144,64
E_ 107
Mvvv 124,34,164
E_ 84
Nvvv 128,30,148,68
E_ 85
Mvvv 128,34,168
E_ 86
Nvvv 132,30,152,72
E_ 87
Mvvv 132,34,172
W_ 0
C_ 107,106,107
C_ 106,104,105
Mvvv 132,38,192
s_nop 0
Dv 30,%0,57344
Dv 34,v1,57344
Mvvv 132,42,212
C_ 104,84,85
C_ 105,86,87
Mvvv 128,38,188
E_ 108
Mvvv 128,42,208
E_ 109
Mvvv 124,38,184
E_ 110
Mvvv 124,42,204
E_ 111
Mvvv 120,38,180
E_ 88
Mvvv 120,42,200
E_ 89
Mvvv 116,38,176
E_ 90
Mvvv 116,42,196
W_ 0
E_ 91
Mvvv 116,30,216
s_nop 0
Dv 38,%0,49152
Dv 42,v1,49152
Mvvv 116,34,236
C_ 111,110,111
C_ 110,108,109
Mvvv 120,30,220
C_ 108,88,89
C_ 109,90,91
Mvvv 120,34,240
E_ 112
Mvvv 124,30,224
E_ 113
Mvvv 124,34,244
E_ 114
Mvvv 128,30,228
E_ 115
Mvvv 128,34,248
E_ 92
Mvvv 132,30,232
E_ 93
Mvvv 132,34,252
W_ 0
E_ 94
Nvvv 76,38,136,56
s_nop 0
Dv 30,%0,50176
Dv 34,v1,50176
Mvvv 76,42,156
E_ 95
Nvvv 80,38,140,60
C_ 115,114,115
C_ 114,112,113
Mvvv 80,42,160
C_ 112,92,93
C_ 113,94,95
Nvvv 84,38,144,64
E_ 116
Mvvv 84,42,164
E_ 117
Nvvv 88,38,148,68
E_ 118
Mvvv 88,42,168
E_ 119
Nvvv 92,38,152,72
C_ 119,118,119
C_ 118,116,117
Mvvv 92,42,172
W_ 0
E_ 120
Mvvv 92,30,192
s_nop 0
Dv 38,%0,51200
Dv 42,v1,51200
Mvvv 92,34,212
X_ a180,v11
X_ a181,v11
Mvvv 88,30,188
X_ a182,v11
X_ a183,v11
Mvvv 88,34,208
ds_write_b128 v12, a[132:135] offset:0
Mvvv 84,30,184
E_ 121
Mvvv 84,34,204
E_ 122
Mvvv 80,30,180
ds_write_b128 v12, a[136:139] offset:1024
Mvvv 80,34,200
E_ 123
Mvvv 76,30,176
C_ 123,122,123
C_ 122,120,121
Mvvv 76,34,196
ds_write_b128 v12, a[160:163] offset:2048
W_ 4
Mvvv 76,38,216
W_ 3
E_ 124
Mvvv 76,42,236
s_waitcnt vmcnt(12) lgkmcnt(0)
s_barrier
Mvvv 80,38,220
ds_write_b128 v12, a[164:167] offset:3072
Mvvv 80,42,240
E_ 125
Mvvv 84,38,224
E_ 126
Mvvv 84,42,244
s_nop 0
Tv 30,v2,24576
Tv 32,v2,30720
Mvvv 88,38,228
ds_write_b128 v12, a[168:171] offset:4096
Mvvv 88,42,248
s_nop 0
Tv 34,v3,24576
Tv 36,v3,30720
Mvvv 92,38,232
E_ 127
Mvvv 92,42,252
ds_write_b128 v12, a[172:175] offset:5120
Zaav 220,180,96
C_ 127,126,127
C_ 126,124,125
Zaav 224,180,100
E_ 128
Zaav 228,180,104
ds_write_b128 v12, a[176:179] offset:12288
Zaav 232,180,108
E_ 129
Zaav 236,180,112
)A"
R"A(W_ 2
E_ 130
Zavv 0,30,96
s_nop 0
Tv 38,v4,24576
Tv 40,v4,30720
Zavv 20,34,96
ds_write_b128 v12, a[200:203] offset:13312
Zavv 24,34,100
s_nop 0
Tv 42,v5,24576
Tv 44,v5,30720
Zavv 4,30,100
E_ 131
Zavv 8,30,104
ds_write_b128 v12, a[204:207] offset:14336
Zavv 28,34,104
R_ v46,a220
R_ v47,a224
Zavv 32,34,108
R_ v48,a228
R_ v49,a232
Zavv 12,30,108
R_ v50,a236
C_ 131,130,131
Zavv 16,30,112
ds_write_b128 v12, a[208:211] offset:15360
Zavv 36,34,112
W_ 2
C_ 130,128,129
Zavv 56,38,112
.Lpad_a1_1:
s_cbranch_execz .Lhop_a1_r
Tv 30,v2,25600
Tv 32,v2,31744
Zavv 76,42,112
ds_write_b128 v12, a[212:215] offset:16384
Zavv 72,42,108
s_nop 0
Tv 34,v3,25600
Tv 36,v3,31744
Zavv 52,38,108
E_ 132
Zavv 48,38,104
ds_write_b128 v12, a[216:219] offset:17408
Zavv 68,42,104
E_ 133
Zavv 64,42,100
E_ 134
Zavv 44,38,100
E_ 135
Zavv 40,38,96
C_ 135,134,135
C_ 134,132,133
Zavv 60,42,96
W_ 1
E_ 76
Zavv 80,30,96
s_nop 0
Tv 38,v4,25600
Tv 40,v4,31744
Zavv 100,34,96
E_ 77
Zavv 104,34,100
s_nop 0
Tv 42,v5,25600
Tv 44,v5,31744
Zavv 84,30,100
E_ 78
Zavv 88,30,104
E_ 79
Zavv 108,34,104
C_ 116,76,77
C_ 117,78,79
Zavv 112,34,108
E_ 80
Zavv 92,30,108
E_ 81
Zavv 96,30,112
E_ 82
Zavv 116,34,112
W_ 0
E_ 83
Zavv 136,38,112
s_nop 0
Tv 30,v2,26624
Tv 32,v2,32768
Zavv 156,42,112
C_ 120,80,81
C_ 121,82,83
Zavv 152,42,108
s_nop 0
Tv 34,v3,26624
Tv 36,v3,32768
Zavv 132,38,108
E_ 84
Zavv 128,38,104
E_ 85
Zavv 148,42,104
E_ 86
Zavv 144,42,100
E_ 87
Zavv 124,38,100
C_ 124,84,85
C_ 125,86,87
Zavv 120,38,96
E_ 88
Zavv 140,42,96
W_ 0
E_ 89
Zavv 160,30,96
s_nop 0
Tv 38,v4,26624
Tv 40,v4,32768
Zavv 180,34,96
E_ 90
Zavv 184,34,100
s_nop 0
Tv 42,v5,26624
Tv 44,v5,32768
Zavv 164,30,100
E_ 91
Zavv 168,30,104
C_ 128,88,89
C_ 129,90,91
Zavv 188,34,104
S_ s65,s61,36864
E_ 92
Zavv 192,34,108
S_ m0,s51,24576
E_ 93
Zavv 172,30,108
s_waitcnt vmcnt(6) lgkmcnt(0)
s_barrier
Zavv 176,30,112
Ln v7,32,s65,0
Zavv 196,34,112
E_ 94
Zavv 216,38,112
Dv 30,%0,6144
Zavv 236,42,112
Dv 34,v1,6144
Zavv 232,42,108
Ln v8,32,s65,3072
S_ m0,s51,25472
Zavv 212,38,108
E_ 95
Zavv 208,38,104
Ln v7,32,s65,128
Zavv 228,42,104
C_ 132,92,93
C_ 133,94,95
Zavv 224,42,100
Ln v8,32,s65,3200
S_ m0,s51,26368
Zavv 204,38,100
v_mov_b32 v92, 0x3f803f80
v_mov_b32 v93, 0x3f803f80
Zavv 200,38,96
Ln v7,32,s65,256
Zavv 220,42,96
v_mov_b32 v94, 0x3f803f80
v_mov_b32 v95, 0x3f803f80
Ln v8,32,s65,3328
.Lstep1_2:
s_nop 0
Zvvv 76,92,116
Dv 38,%0,7168
Zvvv 80,92,120
Dv 42,v1,7168
Zvvv 84,92,124
Zvvv 88,92,128
Zvvv 92,92,132
W_ 2
Nvvv 96,30,136,56
Mvvv 96,34,156
Nvvv 100,30,140,60
s_nop 1
v_max3_f32 v10, v76, v80, v84
s_nop 0
v_max3_f32 v10, v10, v88, v92
Mvvv 100,34,160
v_cmp_lt_f32 vcc, 0x42000000, v10
s_cbranch_vccnz .Ltramp1_1
Nvvv 104,30,144,64
A_ v46,v46,v76
A_ v47,v47,v80
Mvvv 104,34,164
A_ v48,v48,v84
A_ v49,v49,v88
Nvvv 108,30,148,68
A_ v50,v50,v92
Mvvv 108,34,168
Nvvv 112,30,152,72
Mvvv 112,34,172
W_ 0
Mvvv 112,38,192
Dv 30,%0,8192
Mvvv 112,42,212
Dv 34,v1,8192
Mvvv 108,38,188
Mvvv 108,42,208
Mvvv 104,38,184
Mvvv 104,42,204
Mvvv 100,38,180
Mvvv 100,42,200
Mvvv 96,38,176
Mvvv 96,42,196
W_ 0
Mvvv 96,30,216
Dv 38,%0,0
Mvvv 96,34,236
Dv 42,v1,0
Mvvv 100,30,220
Mvvv 100,34,240
Mvvv 104,30,224
Mvvv 104,34,244
Mvvv 108,30,228
Mvvv 108,34,248
s_nop 0
E_ 96
Mvvv 112,30,232
E_ 97
Mvvv 112,34,252
W_ 0
E_ 98
Nvvv 76,38,136,56
Dv 30,%0,1024
Mvvv 76,42,156
Dv 34,v1,1024
Nvvv 80,38,140,60
E_ 99
Mvvv 80,42,160
C_ 99,98,99
C_ 98,96,97
Nvvv 84,38,144,64
E_ 100
Mvvv 84,42,164
E_ 101
Nvvv 88,38,148,68
E_ 102
Mvvv 88,42,168
E_ 103
Nvvv 92,38,152,72
C_ 103,102,103
C_ 102,100,101
Mvvv 92,42,172
W_ 0
E_ 104
Mvvv 92,30,192
Dv 38,%0,2048
Mvvv 92,34,212
Dv 42,v1,2048
Mvvv 88,30,188
E_ 105
Mvvv 88,34,208
E_ 106
Mvvv 84,30,184
S_ s65,s61,24576
E_ 107
Mvvv 84,34,204
S_ m0,s51,0
C_ 107,106,107
C_ 106,104,105
Mvvv 80,30,180
s_waitcnt vmcnt(6) lgkmcnt(0)
s_barrier
Mvvv 80,34,200
L_ v9,36,s65,0
Mvvv 76,30,176
E_ 108
Mvvv 76,34,196
L_ v9,36,s65,3072
S_ m0,s51,896
Mvvv 76,38,216
Tv 30,v2,12288
Tv 32,v2,18432
Mvvv 76,42,236
L_ v9,36,s65,128
Mvvv 80,38,220
s_nop 0
Tv 34,v3,12288
Tv 36,v3,18432
Mvvv 80,42,240
L_ v9,36,s65,3200
S_ m0,s51,1792
Mvvv 84,38,224
E_ 109
Mvvv 84,42,244
L_ v9,36,s65,256
Mvvv 88,38,228
E_ 110
Mvvv 88,42,248
L_ v9,36,s65,3328
Mvvv 92,38,232
E_ 111
Mvvv 92,42,252
W_ 0
C_ 111,110,111
C_ 110,108,109
Mavv 0,30,116
s_nop 0
Tv 38,v4,12288
Tv 40,v4,18432
Mavv 20,34,116
E_ 112
Mavv 24,34,120
s_nop 0
Tv 42,v5,12288
Tv 44,v5,18432
Mavv 4,30,120
E_ 113
Mavv 8,30,124
E_ 114
Mavv 28,34,124
E_ 115
Mavv 32,34,128
C_ 115,114,115
C_ 114,112,113
Mavv 12,30,128
E_ 76
Mavv 16,30,132
E_ 77
Mavv 36,34,132
W_ 0
E_ 78
Mavv 56,38,132
s_nop 0
Tv 30,v2,13312
Tv 32,v2,19456
Mavv 76,42,132
E_ 79
Mavv 72,42,128
s_nop 0
Tv 34,v3,13312
Tv 36,v3,19456
Mavv 52,38,128
C_ 96,76,77
C_ 97,78,79
Mavv 48,38,124
E_ 80
Mavv 68,42,124
E_ 81
Mavv 64,42,120
E_ 82
Mavv 44,38,120
E_ 83
Mavv 40,38,116
C_ 100,80,81
C_ 101,82,83
Mavv 60,42,116
W_ 0
E_ 84
Mavv 80,30,116
s_nop 0
Tv 38,v4,13312
Tv 40,v4,19456
Mavv 100,34,116
E_ 85
Mavv 104,34,120
s_nop 0
Tv 42,v5,13312
Tv 44,v5,19456
Mavv 84,30,120
E_ 86
Mavv 88,30,124
E_ 87
Mavv 108,34,124
C_ 104,84,85
C_ 105,86,87
Mavv 112,34,128
E_ 88
Mavv 92,30,128
E_ 89
Mavv 96,30,132
E_ 90
Mavv 116,34,132
W_ 0
E_ 91
Mavv 136,38,132
s_nop 0
Tv 30,v2,14336
Tv 32,v2,20480
Mavv 156,42,132
C_ 108,88,89
C_ 109,90,91
Mavv 152,42,128
s_nop 0
Tv 34,v3,14336
Tv 36,v3,20480
Mavv 132,38,128
E_ 92
Mavv 128,38,124
E_ 93
Mavv 148,42,124
E_ 94
Mavv 144,42,120
E_ 95
Mavv 124,38,120
C_ 112,92,93
C_ 113,94,95
Mavv 120,38,116
v_mov_b32 v92, 0x3f803f80
v_mov_b32 v93, 0x3f803f80
Mavv 140,42,116
W_ 0
v_mov_b32 v94, 0x3f803f80
v_mov_b32 v95, 0x3f803f80
Mavv 160,30,116
s_nop 0
Tv 38,v4,14336
Tv 40,v4,20480
Mavv 180,34,116
Mavv 184,34,120
s_nop 0
Tv 42,v5,14336
Tv 44,v5,20480
Mavv 164,30,120
Mavv 168,30,124
Mavv 188,34,124
S_ s65,s61,49152
Mavv 192,34,128
S_ m0,s51,12288
Mavv 172,30,128
s_waitcnt vmcnt(6) lgkmcnt(0)
s_barrier
Mavv 176,30,132
Ln v7,32,s65,0
Mavv 196,34,132
Mavv 216,38,132
Dv 30,%0,30720
Mavv 236,42,132
Dv 34,v1,30720
Mavv 232,42,128
Ln v8,32,s65,3072
S_ m0,s51,13184
Mavv 212,38,128
Mavv 208,38,124
Ln v7,32,s65,128
Mavv 228,42,124
Mavv 224,42,120
Ln v8,32,s65,3200
S_ m0,s51,14080
Mavv 204,38,120
Mavv 200,38,116
Ln v7,32,s65,256
Mavv 220,42,116
s_cmp_eq_u32 s4, 0
s_cbranch_scc1 .Lhs_a1
.Lhs_a1_back:
.Lstep1_3:
Zvvv 76,92,96
Dv 38,%0,31744
Zvvv 80,92,100
Dv 42,v1,31744
Zvvv 84,92,104
Zvvv 88,92,108
Zvvv 92,92,112
W_ 2
Nvvv 116,30,136,56
Mvvv 116,34,156
Nvvv 120,30,140,60
s_nop 1
v_max3_f32 v10, v76, v80, v84
s_nop 0
v_max3_f32 v10, v10, v88, v92
Mvvv 120,34,160
v_cmp_lt_f32 vcc, 0x42000000, v10
s_cbranch_vccnz .Ltramp1_2
Nvvv 124,30,144,64
A_ v46,v46,v76
A_ v47,v47,v80
Mvvv 124,34,164
A_ v48,v48,v84
A_ v49,v49,v88
Nvvv 128,30,148,68
A_ v50,v50,v92
Mvvv 128,34,168
Ln v8,32,s65,3328
Nvvv 132,30,152,72
Mvvv 132,34,172
W_ 0
Mvvv 132,38,192
Dv 30,%0,32768
Mvvv 132,42,212
Dv 34,v1,32768
Mvvv 128,38,188
Mvvv 128,42,208
Mvvv 124,38,184
Mvvv 124,42,204
Mvvv 120,38,180
Mvvv 120,42,200
Mvvv 116,38,176
Mvvv 116,42,196
W_ 0
Mvvv 116,30,216
Dv 38,%0,24576
Mvvv 116,34,236
Dv 42,v1,24576
Mvvv 120,30,220
Mvvv 120,34,240
Mvvv 124,30,224
Mvvv 124,34,244
Mvvv 128,30,228
Mvvv 128,34,248
s_nop 0
E_ 116
Mvvv 132,30,232
E_ 117
Mvvv 132,34,252
W_ 0
E_ 118
Nvvv 76,38,136,56
Dv 30,%0,25600
Mvvv 76,42,156
Dv 34,v1,25600
Nvvv 80,38,140,60
E_ 119
Mvvv 80,42,160
C_ 119,118,119
C_ 118,116,117
Nvvv 84,38,144,64
E_ 120
Mvvv 84,42,164
E_ 121
Nvvv 88,38,148,68
E_ 122
Mvvv 88,42,168
E_ 123
Nvvv 92,38,152,72
C_ 123,122,123
C_ 122,120,121
Mvvv 92,42,172
W_ 0
E_ 124
Mvvv 92,30,192
Dv 38,%0,26624
Mvvv 92,34,212
Dv 42,v1,26624
Mvvv 88,30,188
E_ 125
Mvvv 88,34,208
E_ 126
Mvvv 84,30,184
S_ s65,s61,36864
E_ 127
Mvvv 84,34,204
S_ m0,s51,24576
C_ 127,126,127
C_ 126,124,125
Mvvv 80,30,180
s_waitcnt vmcnt(6) lgkmcnt(0)
s_barrier
Mvvv 80,34,200
L_ v9,36,s65,0
Mvvv 76,30,176
E_ 128
Mvvv 76,34,196
L_ v9,36,s65,3072
S_ m0,s51,25472
Mvvv 76,38,216
Tv 30,v2,0
Tv 32,v2,6144
Mvvv 76,42,236
L_ v9,36,s65,128
Mvvv 80,38,220
s_nop 0
Tv 34,v3,0
Tv 36,v3,6144
Mvvv 80,42,240
L_ v9,36,s65,3200
S_ m0,s51,26368
Mvvv 84,38,224
E_ 129
Mvvv 84,42,244
L_ v9,36,s65,256
Mvvv 88,38,228
E_ 130
Mvvv 88,42,248
L_ v9,36,s65,3328
Mvvv 92,38,232
E_ 131
Mvvv 92,42,252
W_ 0
C_ 131,130,131
C_ 130,128,129
Mavv 0,30,96
s_nop 0
Tv 38,v4,0
Tv 40,v4,6144
Mavv 20,34,96
E_ 132
Mavv 24,34,100
s_nop 0
Tv 42,v5,0
Tv 44,v5,6144
Mavv 4,30,100
E_ 133
Mavv 8,30,104
E_ 134
Mavv 28,34,104
E_ 135
Mavv 32,34,108
C_ 135,134,135
C_ 134,132,133
Mavv 12,30,108
E_ 76
Mavv 16,30,112
E_ 77
Mavv 36,34,112
W_ 0
E_ 78
Mavv 56,38,112
s_nop 0
Tv 30,v2,1024
Tv 32,v2,7168
Mavv 76,42,112
E_ 79
Mavv 72,42,108
s_nop 0
Tv 34,v3,1024
Tv 36,v3,7168
Mavv 52,38,108
.Lpad_a1_2:
s_cbranch_execz .Lhop_a1_r
C_ 116,76,77
C_ 117,78,79
Mavv 48,38,104
E_ 80
Mavv 68,42,104
E_ 81
Mavv 64,42,100
E_ 82
Mavv 44,38,100
E_ 83
Mavv 40,38,96
C_ 120,80,81
C_ 121,82,83
Mavv 60,42,96
W_ 0
E_ 84
Mavv 80,30,96
s_nop 0
Tv 38,v4,1024
Tv 40,v4,7168
Mavv 100,34,96
E_ 85
Mavv 104,34,100
s_nop 0
Tv 42,v5,1024
Tv 44,v5,7168
Mavv 84,30,100
E_ 86
Mavv 88,30,104
E_ 87
Mavv 108,34,104
C_ 124,84,85
C_ 125,86,87
Mavv 112,34,108
E_ 88
Mavv 92,30,108
E_ 89
Mavv 96,30,112
E_ 90
Mavv 116,34,112
W_ 0
E_ 91
Mavv 136,38,112
s_nop 0
Tv 30,v2,2048
Tv 32,v2,8192
Mavv 156,42,112
C_ 128,88,89
C_ 129,90,91
Mavv 152,42,108
s_nop 0
Tv 34,v3,2048
Tv 36,v3,8192
Mavv 132,38,108
E_ 92
Mavv 128,38,104
E_ 93
Mavv 148,42,104
E_ 94
Mavv 144,42,100
E_ 95
Mavv 124,38,100
C_ 132,92,93
C_ 133,94,95
Mavv 120,38,96
v_mov_b32 v92, 0x3f803f80
v_mov_b32 v93, 0x3f803f80
Mavv 140,42,96
W_ 0
v_mov_b32 v94, 0x3f803f80
v_mov_b32 v95, 0x3f803f80
Mavv 160,30,96
s_nop 0
Tv 38,v4,2048
Tv 40,v4,8192
Mavv 180,34,96
Mavv 184,34,100
s_nop 0
Tv 42,v5,2048
Tv 44,v5,8192
Mavv 164,30,100
Mavv 168,30,104
Mavv 188,34,104
S_ s65,s61,61440
Mavv 192,34,108
S_ m0,s51,0
Mavv 172,30,108
s_waitcnt vmcnt(6) lgkmcnt(0)
s_barrier
Mavv 176,30,112
Ln v7,32,s65,0
Mavv 196,34,112
Mavv 216,38,112
Dv 30,%0,18432
Mavv 236,42,112
Dv 34,v1,18432
Mavv 232,42,108
Ln v8,32,s65,3072
S_ m0,s51,896
Mavv 212,38,108
Mavv 208,38,104
Ln v7,32,s65,128
Mavv 228,42,104
Mavv 224,42,100
Ln v8,32,s65,3200
S_ m0,s51,1792
Mavv 204,38,100
Mavv 200,38,96
Ln v7,32,s65,256
Mavv 220,42,96
.Lstep1_4:
Zvvv 76,92,116
Dv 38,%0,19456
Zvvv 80,92,120
Dv 42,v1,19456
Zvvv 84,92,124
Zvvv 88,92,128
Zvvv 92,92,132
W_ 2
Nvvv 96,30,136,56
Mvvv 96,34,156
Nvvv 100,30,140,60
s_nop 1
v_max3_f32 v10, v76, v80, v84
s_nop 0
v_max3_f32 v10, v10, v88, v92
Mvvv 100,34,160
v_cmp_lt_f32 vcc, 0x42000000, v10
s_cbranch_vccnz .Ltramp1_3
Nvvv 104,30,144,64
A_ v46,v46,v76
A_ v47,v47,v80
Mvvv 104,34,164
A_ v48,v48,v84
A_ v49,v49,v88
Nvvv 108,30,148,68
A_ v50,v50,v92
Mvvv 108,34,168
Ln v8,32,s65,3328
Nvvv 112,30,152,72
Mvvv 112,34,172
W_ 0
Mvvv 112,38,192
Dv 30,%0,20480
Mvvv 112,42,212
Dv 34,v1,20480
Mvvv 108,38,188
Mvvv 108,42,208
Mvvv 104,38,184
Mvvv 104,42,204
Mvvv 100,38,180
Mvvv 100,42,200
Mvvv 96,38,176
Mvvv 96,42,196
W_ 0
Mvvv 96,30,216
Dv 38,%0,12288
Mvvv 96,34,236
Dv 42,v1,12288
Mvvv 100,30,220
Mvvv 100,34,240
Mvvv 104,30,224
Mvvv 104,34,244
Mvvv 108,30,228
Mvvv 108,34,248
s_nop 0
E_ 96
Mvvv 112,30,232
E_ 97
Mvvv 112,34,252
W_ 0
E_ 98
Nvvv 76,38,136,56
Dv 30,%0,13312
Mvvv 76,42,156
Dv 34,v1,13312
Nvvv 80,38,140,60
E_ 99
Mvvv 80,42,160
C_ 99,98,99
C_ 98,96,97
Nvvv 84,38,144,64
E_ 100
Mvvv 84,42,164
E_ 101
Nvvv 88,38,148,68
E_ 102
Mvvv 88,42,168
E_ 103
Nvvv 92,38,152,72
C_ 103,102,103
C_ 102,100,101
Mvvv 92,42,172
W_ 0
E_ 104
Mvvv 92,30,192
Dv 38,%0,14336
Mvvv 92,34,212
Dv 42,v1,14336
Mvvv 88,30,188
E_ 105
Mvvv 88,34,208
E_ 106
Mvvv 84,30,184
S_ s65,s61,49152
E_ 107
Mvvv 84,34,204
S_ m0,s51,12288
C_ 107,106,107
C_ 106,104,105
Mvvv 80,30,180
s_waitcnt vmcnt(6) lgkmcnt(0)
s_barrier
Mvvv 80,34,200
L_ v9,36,s65,0
Mvvv 76,30,176
E_ 108
Mvvv 76,34,196
L_ v9,36,s65,3072
S_ m0,s51,13184
Mvvv 76,38,216
Tv 30,v2,24576
Tv 32,v2,30720
Mvvv 76,42,236
L_ v9,36,s65,128
Mvvv 80,38,220
s_nop 0
Tv 34,v3,24576
Tv 36,v3,30720
Mvvv 80,42,240
L_ v9,36,s65,3200
S_ m0,s51,14080
Mvvv 84,38,224
E_ 109
Mvvv 84,42,244
L_ v9,36,s65,256
Mvvv 88,38,228
E_ 110
Mvvv 88,42,248
L_ v9,36,s65,3328
Mvvv 92,38,232
E_ 111
Mvvv 92,42,252
W_ 0
C_ 111,110,111
C_ 110,108,109
Mavv 0,30,116
s_nop 0
Tv 38,v4,24576
Tv 40,v4,30720
Mavv 20,34,116
E_ 112
Mavv 24,34,120
s_nop 0
Tv 42,v5,24576
Tv 44,v5,30720
Mavv 4,30,120
E_ 113
Mavv 8,30,124
E_ 114
Mavv 28,34,124
E_ 115
Mavv 32,34,128
C_ 115,114,115
C_ 114,112,113
Mavv 12,30,128
E_ 76
Mavv 16,30,132
E_ 77
Mavv 36,34,132
W_ 0
E_ 78
Mavv 56,38,132
s_nop 0
Tv 30,v2,25600
Tv 32,v2,31744
Mavv 76,42,132
E_ 79
Mavv 72,42,128
s_nop 0
Tv 34,v3,25600
Tv 36,v3,31744
Mavv 52,38,128
C_ 96,76,77
C_ 97,78,79
Mavv 48,38,124
E_ 80
Mavv 68,42,124
E_ 81
Mavv 64,42,120
E_ 82
Mavv 44,38,120
E_ 83
Mavv 40,38,116
C_ 100,80,81
C_ 101,82,83
Mavv 60,42,116
W_ 0
E_ 84
Mavv 80,30,116
s_nop 0
Tv 38,v4,25600
Tv 40,v4,31744
Mavv 100,34,116
E_ 85
Mavv 104,34,120
s_nop 0
Tv 42,v5,25600
Tv 44,v5,31744
Mavv 84,30,120
E_ 86
Mavv 88,30,124
E_ 87
Mavv 108,34,124
C_ 104,84,85
C_ 105,86,87
Mavv 112,34,128
E_ 88
Mavv 92,30,128
E_ 89
Mavv 96,30,132
E_ 90
Mavv 116,34,132
W_ 0
E_ 91
Mavv 136,38,132
s_nop 0
Tv 30,v2,26624
Tv 32,v2,32768
Mavv 156,42,132
C_ 108,88,89
C_ 109,90,91
Mavv 152,42,128
s_nop 0
Tv 34,v3,26624
Tv 36,v3,32768
Mavv 132,38,128
E_ 92
Mavv 128,38,124
E_ 93
Mavv 148,42,124
E_ 94
Mavv 144,42,120
E_ 95
Mavv 124,38,120
C_ 112,92,93
C_ 113,94,95
Mavv 120,38,116
v_mov_b32 v92, 0x3f803f80
v_mov_b32 v93, 0x3f803f80
Mavv 140,42,116
W_ 0
v_mov_b32 v94, 0x3f803f80
v_mov_b32 v95, 0x3f803f80
Mavv 160,30,116
s_nop 0
Tv 38,v4,26624
Tv 40,v4,32768
Mavv 180,34,116
Mavv 184,34,120
s_nop 0
Tv 42,v5,26624
Tv 44,v5,32768
Mavv 164,30,120
Mavv 168,30,124
Mavv 188,34,124
S_ s65,s61,73728
Mavv 192,34,128
S_ m0,s51,24576
Mavv 172,30,128
s_waitcnt vmcnt(6) lgkmcnt(0)
s_barrier
Mavv 176,30,132
Ln v7,32,s65,0
Mavv 196,34,132
Mavv 216,38,132
Dv 30,%0,6144
Mavv 236,42,132
Dv 34,v1,6144
Mavv 232,42,128
Ln v8,32,s65,3072
S_ m0,s51,25472
Mavv 212,38,128
Mavv 208,38,124
Ln v7,32,s65,128
Mavv 228,42,124
Mavv 224,42,120
Ln v8,32,s65,3200
S_ m0,s51,26368
Mavv 204,38,120
Mavv 200,38,116
Ln v7,32,s65,256
Mavv 220,42,116
s_cmp_eq_u32 s4, 0
s_cbranch_scc1 .Lhs_b1
.Lhs_b1_back:
.Lstep1_5:
Zvvv 76,92,96
Dv 38,%0,7168
Zvvv 80,92,100
Dv 42,v1,7168
Zvvv 84,92,104
Zvvv 88,92,108
Zvvv 92,92,112
W_ 2
Nvvv 116,30,136,56
Mvvv 116,34,156
Nvvv 120,30,140,60
s_nop 1
v_max3_f32 v10, v76, v80, v84
s_nop 0
v_max3_f32 v10, v10, v88, v92
Mvvv 120,34,160
v_cmp_lt_f32 vcc, 0x42000000, v10
s_cbranch_vccnz .Ltramp1_4
Nvvv 124,30,144,64
A_ v46,v46,v76
A_ v47,v47,v80
Mvvv 124,34,164
A_ v48,v48,v84
A_ v49,v49,v88
Nvvv 128,30,148,68
A_ v50,v50,v92
Mvvv 128,34,168
Ln v8,32,s65,3328
Nvvv 132,30,152,72
Mvvv 132,34,172
W_ 0
Mvvv 132,38,192
Dv 30,%0,8192
Mvvv 132,42,212
Dv 34,v1,8192
Mvvv 128,38,188
Mvvv 128,42,208
Mvvv 124,38,184
Mvvv 124,42,204
Mvvv 120,38,180
Mvvv 120,42,200
Mvvv 116,38,176
Mvvv 116,42,196
W_ 0
Mvvv 116,30,216
Dv 38,%0,0
Mvvv 116,34,236
Dv 42,v1,0
Mvvv 120,30,220
Mvvv 120,34,240
Mvvv 124,30,224
Mvvv 124,34,244
Mvvv 128,30,228
Mvvv 128,34,248
s_nop 0
E_ 116
Mvvv 132,30,232
E_ 117
Mvvv 132,34,252
W_ 0
E_ 118
Nvvv 76,38,136,56
Dv 30,%0,1024
Mvvv 76,42,156
Dv 34,v1,1024
Nvvv 80,38,140,60
E_ 119
Mvvv 80,42,160
C_ 119,118,119
C_ 118,116,117
Nvvv 84,38,144,64
E_ 120
Mvvv 84,42,164
E_ 121
Nvvv 88,38,148,68
E_ 122
Mvvv 88,42,168
E_ 123
Nvvv 92,38,152,72
C_ 123,122,123
C_ 122,120,121
Mvvv 92,42,172
W_ 0
E_ 124
Mvvv 92,30,192
Dv 38,%0,2048
Mvvv 92,34,212
Dv 42,v1,2048
Mvvv 88,30,188
E_ 125
Mvvv 88,34,208
E_ 126
Mvvv 84,30,184
S_ s65,s61,61440
E_ 127
Mvvv 84,34,204
S_ m0,s51,0
C_ 127,126,127
C_ 126,124,125
Mvvv 80,30,180
s_waitcnt vmcnt(6) lgkmcnt(0)
s_barrier
Mvvv 80,34,200
L_ v9,36,s65,0
Mvvv 76,30,176
E_ 128
Mvvv 76,34,196
L_ v9,36,s65,3072
S_ m0,s51,896
Mvvv 76,38,216
Tv 30,v2,12288
Tv 32,v2,18432
Mvvv 76,42,236
L_ v9,36,s65,128
Mvvv 80,38,220
s_nop 0
Tv 34,v3,12288
Tv 36,v3,18432
Mvvv 80,42,240
L_ v9,36,s65,3200
S_ m0,s51,1792
Mvvv 84,38,224
E_ 129
Mvvv 84,42,244
L_ v9,36,s65,256
Mvvv 88,38,228
E_ 130
Mvvv 88,42,248
L_ v9,36,s65,3328
Mvvv 92,38,232
E_ 131
Mvvv 92,42,252
W_ 0
C_ 131,130,131
C_ 130,128,129
Mavv 0,30,96
s_nop 0
Tv 38,v4,12288
Tv 40,v4,18432
Mavv 20,34,96
E_ 132
Mavv 24,34,100
s_nop 0
Tv 42,v5,12288
Tv 44,v5,18432
Mavv 4,30,100
E_ 133
Mavv 8,30,104
E_ 134
Mavv 28,34,104
E_ 135
Mavv 32,34,108
C_ 135,134,135
C_ 134,132,133
Mavv 12,30,108
E_ 76
Mavv 16,30,112
E_ 77
Mavv 36,34,112
W_ 0
E_ 78
Mavv 56,38,112
s_nop 0
Tv 30,v2,13312
Tv 32,v2,19456
Mavv 76,42,112
E_ 79
Mavv 72,42,108
s_nop 0
Tv 34,v3,13312
Tv 36,v3,19456
Mavv 52,38,108
C_ 116,76,77
C_ 117,78,79
Mavv 48,38,104
E_ 80
Mavv 68,42,104
E_ 81
Mavv 64,42,100
E_ 82
Mavv 44,38,100
E_ 83
Mavv 40,38,96
C_ 120,80,81
C_ 121,82,83
Mavv 60,42,96
W_ 0
E_ 84
Mavv 80,30,96
s_nop 0
Tv 38,v4,13312
Tv 40,v4,19456
Mavv 100,34,96
E_ 85
Mavv 104,34,100
s_nop 0
Tv 42,v5,13312
Tv 44,v5,19456
Mavv 84,30,100
.Lpad_a1_3:
s_cbranch_execz .Lhop_a1_r
E_ 86
Mavv 88,30,104
E_ 87
Mavv 108,34,104
C_ 124,84,85
C_ 125,86,87
Mavv 112,34,108
E_ 88
Mavv 92,30,108
E_ 89
Mavv 96,30,112
E_ 90
Mavv 116,34,112
W_ 0
E_ 91
Mavv 136,38,112
s_nop 0
Tv 30,v2,14336
Tv 32,v2,20480
Mavv 156,42,112
C_ 128,88,89
C_ 129,90,91
Mavv 152,42,108
s_nop 0
Tv 34,v3,14336
Tv 36,v3,20480
Mavv 132,38,108
E_ 92
Mavv 128,38,104
E_ 93
Mavv 148,42,104
E_ 94
Mavv 144,42,100
E_ 95
Mavv 124,38,100
C_ 132,92,93
C_ 133,94,95
Mavv 120,38,96
v_mov_b32 v92, 0x3f803f80
v_mov_b32 v93, 0x3f803f80
Mavv 140,42,96
W_ 0
v_mov_b32 v94, 0x3f803f80
v_mov_b32 v95, 0x3f803f80
Mavv 160,30,96
s_nop 0
Tv 38,v4,14336
Tv 40,v4,20480
Mavv 180,34,96
Mavv 184,34,100
s_nop 0
Tv 42,v5,14336
Tv 44,v5,20480
Mavv 164,30,100
Mavv 168,30,104
Mavv 188,34,104
S_ s65,s61,86016
Mavv 192,34,108
S_ m0,s51,12288
Mavv 172,30,108
s_waitcnt vmcnt(6) lgkmcnt(0)
s_barrier
Mavv 176,30,112
Ln v7,32,s65,0
Mavv 196,34,112
Mavv 216,38,112
Dv 30,%0,30720
Mavv 236,42,112
Dv 34,v1,30720
Mavv 232,42,108
Ln v8,32,s65,3072
S_ m0,s51,13184
Mavv 212,38,108
Mavv 208,38,104
Ln v7,32,s65,128
Mavv 228,42,104
Mavv 224,42,100
Ln v8,32,s65,3200
S_ m0,s51,14080
Mavv 204,38,100
Mavv 200,38,96
Ln v7,32,s65,256
Mavv 220,42,96
.Lstep1_6:
Zvvv 76,92,116
Dv 38,%0,31744
Zvvv 80,92,120
Dv 42,v1,31744
Zvvv 84,92,124
Zvvv 88,92,128
Zvvv 92,92,132
W_ 2
Nvvv 96,30,136,56
Mvvv 96,34,156
Nvvv 100,30,140,60
s_nop 1
v_max3_f32 v10, v76, v80, v84
s_nop 0
v_max3_f32 v10, v10, v88, v92
Mvvv 100,34,160
v_cmp_lt_f32 vcc, 0x42000000, v10
s_cbranch_vccnz .Ltramp1_5
Nvvv 104,30,144,64
A_ v46,v46,v76
A_ v47,v47,v80
Mvvv 104,34,164
A_ v48,v48,v84
A_ v49,v49,v88
Nvvv 108,30,148,68
A_ v50,v50,v92
Mvvv 108,34,168
Ln v8,32,s65,3328
Nvvv 112,30,152,72
R_ v11,a248
Mvvv 112,34,172
W_ 0
v_min_u32 v11, 240, v11
Mvvv 112,38,192
Dv 30,%0,32768
v_lshlrev_b32 v11, 3, v11
Mvvv 112,42,212
Dv 34,v1,32768
Mvvv 108,38,188
buffer_load_dword a254, v11, s[44:47], s66 offen
Mvvv 108,42,208
Mvvv 104,38,184
Mvvv 104,42,204
Mvvv 100,38,180
Mvvv 100,42,200
Mvvv 96,38,176
Mvvv 96,42,196
W_ 0
Mvvv 96,30,216
Dv 38,%0,24576
Mvvv 96,34,236
Dv 42,v1,24576
Mvvv 100,30,220
Mvvv 100,34,240
Mvvv 104,30,224
Mvvv 104,34,244
Mvvv 108,30,228
Mvvv 108,34,248
s_nop 0
E_ 96
Mvvv 112,30,232
E_ 97
Mvvv 112,34,252
W_ 0
E_ 98
Nvvv 76,38,136,56
Dv 30,%0,25600
Mvvv 76,42,156
Dv 34,v1,25600
Nvvv 80,38,140,60
E_ 99
Mvvv 80,42,160
C_ 99,98,99
C_ 98,96,97
Nvvv 84,38,144,64
E_ 100
Mvvv 84,42,164
E_ 101
Nvvv 88,38,148,68
E_ 102
Mvvv 88,42,168
E_ 103
Nvvv 92,38,152,72
C_ 103,102,103
C_ 102,100,101
Mvvv 92,42,172
W_ 0
E_ 104
Mvvv 92,30,192
Dv 38,%0,26624
Mvvv 92,34,212
Dv 42,v1,26624
Mvvv 88,30,188
E_ 105
Mvvv 88,34,208
E_ 106
Mvvv 84,30,184
S_ s65,s61,73728
E_ 107
Mvvv 84,34,204
S_ m0,s51,24576
C_ 107,106,107
C_ 106,104,105
Mvvv 80,30,180
s_waitcnt vmcnt(7) lgkmcnt(0)
s_barrier
Mvvv 80,34,200
L_ v9,36,s65,0
Mvvv 76,30,176
E_ 108
Mvvv 76,34,196
L_ v9,36,s65,3072
S_ m0,s51,25472
Mvvv 76,38,216
Tv 30,v2,0
Tv 32,v2,6144
Mvvv 76,42,236
L_ v9,36,s65,128
Mvvv 80,38,220
s_nop 0
Tv 34,v3,0
Tv 36,v3,6144
Mvvv 80,42,240
L_ v9,36,s65,3200
S_ m0,s51,26368
Mvvv 84,38,224
E_ 109
Mvvv 84,42,244
L_ v9,36,s65,256
Mvvv 88,38,228
E_ 110
Mvvv 88,42,248
L_ v9,36,s65,3328
Mvvv 92,38,232
E_ 111
Mvvv 92,42,252
W_ 0
C_ 111,110,111
C_ 110,108,109
Mavv 0,30,116
s_nop 0
Tv 38,v4,0
Tv 40,v4,6144
Mavv 20,34,116
E_ 112
Mavv 24,34,120
s_nop 0
Tv 42,v5,0
Tv 44,v5,6144
Mavv 4,30,120
E_ 113
Mavv 8,30,124
E_ 114
Mavv 28,34,124
E_ 115
Mavv 32,34,128
C_ 115,114,115
C_ 114,112,113
Mavv 12,30,128
E_ 76
Mavv 16,30,132
E_ 77
Mavv 36,34,132
W_ 0
E_ 78
Mavv 56,38,132
s_nop 0
Tv 30,v2,1024
Tv 32,v2,7168
Mavv 76,42,132
E_ 79
Mavv 72,42,128
s_nop 0
Tv 34,v3,1024
Tv 36,v3,7168
Mavv 52,38,128
C_ 96,76,77
C_ 97,78,79
Mavv 48,38,124
E_ 80
Mavv 68,42,124
E_ 81
Mavv 64,42,120
E_ 82
Mavv 44,38,120
E_ 83
Mavv 40,38,116
C_ 100,80,81
C_ 101,82,83
Mavv 60,42,116
W_ 0
E_ 84
Mavv 80,30,116
s_nop 0
Tv 38,v4,1024
Tv 40,v4,7168
Mavv 100,34,116
E_ 85
Mavv 104,34,120
s_nop 0
Tv 42,v5,1024
Tv 44,v5,7168
Mavv 84,30,120
E_ 86
Mavv 88,30,124
E_ 87
Mavv 108,34,124
C_ 104,84,85
C_ 105,86,87
Mavv 112,34,128
E_ 88
Mavv 92,30,128
E_ 89
Mavv 96,30,132
E_ 90
Mavv 116,34,132
W_ 0
E_ 91
Mavv 136,38,132
s_nop 0
Tv 30,v2,2048
Tv 32,v2,8192
Mavv 156,42,132
C_ 108,88,89
C_ 109,90,91
Mavv 152,42,128
s_nop 0
Tv 34,v3,2048
Tv 36,v3,8192
Mavv 132,38,128
E_ 92
Mavv 128,38,124
E_ 93
Mavv 148,42,124
E_ 94
Mavv 144,42,120
E_ 95
Mavv 124,38,120
C_ 112,92,93
C_ 113,94,95
Mavv 120,38,116
v_mov_b32 v92, 0x3f803f80
v_mov_b32 v93, 0x3f803f80
Mavv 140,42,116
W_ 0
v_mov_b32 v94, 0x3f803f80
v_mov_b32 v95, 0x3f803f80
Mavv 160,30,116
s_nop 0
Tv 38,v4,2048
Tv 40,v4,8192
Mavv 180,34,116
Mavv 184,34,120
s_nop 0
Tv 42,v5,2048
Tv 44,v5,8192
Mavv 164,30,120
Mavv 168,30,124
Mavv 188,34,124
S_ s65,s61,98304
Mavv 192,34,128
S_ m0,s51,0
Mavv 172,30,128
s_waitcnt vmcnt(7) lgkmcnt(0)
s_barrier
Mavv 176,30,132
Ln v7,32,s65,0
Mavv 196,34,132
Mavv 216,38,132
Dv 30,%0,18432
Mavv 236,42,132
Dv 34,v1,18432
Mavv 232,42,128
Ln v8,32,s65,3072
S_ m0,s51,896
Mavv 212,38,128
Mavv 208,38,124
Ln v7,32,s65,128
Mavv 228,42,124
Mavv 224,42,120
Ln v8,32,s65,3200
S_ m0,s51,1792
Mavv 204,38,120
Mavv 200,38,116
Ln v7,32,s65,256
Mavv 220,42,116
s_cmp_eq_u32 s4, 0
s_cbranch_scc1 .Lhs_c1
.Lhs_c1_back:
.Lstep1_7:
Zvvv 76,92,96
Dv 38,%0,19456
Zvvv 80,92,100
Dv 42,v1,19456
Zvvv 84,92,104
Zvvv 88,92,108
Zvvv 92,92,112
W_ 2
Nvvv 116,30,136,56
Mvvv 116,34,156
Nvvv 120,30,140,60
s_nop 1
v_max3_f32 v10, v76, v80, v84
s_nop 0
v_max3_f32 v10, v10, v88, v92
Mvvv 120,34,160
v_cmp_lt_f32 vcc, 0x42000000, v10
s_cbranch_vccnz .Ltramp1_6
Nvvv 124,30,144,64
A_ v46,v46,v76
A_ v47,v47,v80
Mvvv 124,34,164
A_ v48,v48,v84
A_ v49,v49,v88
Nvvv 128,30,148,68
A_ v50,v50,v92
Mvvv 128,34,168
Ln v8,32,s65,3328
Nvvv 132,30,152,72
Mvvv 132,34,172
W_ 0
Mvvv 132,38,192
Dv 30,%0,20480
Mvvv 132,42,212
Dv 34,v1,20480
Mvvv 128,38,188
Mvvv 128,42,208
Mvvv 124,38,184
Mvvv 124,42,204
Mvvv 120,38,180
Mvvv 120,42,200
Mvvv 116,38,176
Mvvv 116,42,196
W_ 0
Mvvv 116,30,216
Dv 38,%0,12288
Mvvv 116,34,236
Dv 42,v1,12288
Mvvv 120,30,220
Mvvv 120,34,240
Mvvv 124,30,224
Mvvv 124,34,244
Mvvv 128,30,228
Mvvv 128,34,248
s_nop 0
E_ 116
Mvvv 132,30,232
E_ 117
Mvvv 132,34,252
W_ 0
E_ 118
Nvvv 76,38,136,56
Dv 30,%0,13312
Mvvv 76,42,156
Dv 34,v1,13312
Nvvv 80,38,140,60
E_ 119
Mvvv 80,42,160
C_ 119,118,119
C_ 118,116,117
Nvvv 84,38,144,64
E_ 120
Mvvv 84,42,164
E_ 121
Nvvv 88,38,148,68
E_ 122
Mvvv 88,42,168
E_ 123
Nvvv 92,38,152,72
C_ 123,122,123
C_ 122,120,121
Mvvv 92,42,172
W_ 0
E_ 124
Mvvv 92,30,192
Dv 38,%0,14336
Mvvv 92,34,212
Dv 42,v1,14336
Mvvv 88,30,188
E_ 125
Mvvv 88,34,208
E_ 126
Mvvv 84,30,184
S_ s65,s61,86016
E_ 127
Mvvv 84,34,204
S_ m0,s51,12288
C_ 127,126,127
C_ 126,124,125
Mvvv 80,30,180
s_waitcnt vmcnt(6) lgkmcnt(0)
s_barrier
Mvvv 80,34,200
L_ v9,36,s65,0
Mvvv 76,30,176
E_ 128
Mvvv 76,34,196
L_ v9,36,s65,3072
S_ m0,s51,13184
Mvvv 76,38,216
Tv 30,v2,24576
Tv 32,v2,30720
Mvvv 76,42,236
L_ v9,36,s65,128
Mvvv 80,38,220
s_nop 0
Tv 34,v3,24576
Tv 36,v3,30720
Mvvv 80,42,240
L_ v9,36,s65,3200
S_ m0,s51,14080
Mvvv 84,38,224
E_ 129
Mvvv 84,42,244
L_ v9,36,s65,256
Mvvv 88,38,228
E_ 130
Mvvv 88,42,248
L_ v9,36,s65,3328
Mvvv 92,38,232
E_ 131
Mvvv 92,42,252
W_ 0
C_ 131,130,131
C_ 130,128,129
Mavv 0,30,96
s_nop 0
Tv 38,v4,24576
Tv 40,v4,30720
Mavv 20,34,96
E_ 132
Mavv 24,34,100
s_nop 0
Tv 42,v5,24576
Tv 44,v5,30720
Mavv 4,30,100
E_ 133
Mavv 8,30,104
E_ 134
Mavv 28,34,104
E_ 135
Mavv 32,34,108
C_ 135,134,135
C_ 134,132,133
Mavv 12,30,108
E_ 76
Mavv 16,30,112
E_ 77
Mavv 36,34,112
W_ 0
E_ 78
Mavv 56,38,112
s_nop 0
Tv 30,v2,25600
Tv 32,v2,31744
Mavv 76,42,112
E_ 79
Mavv 72,42,108
s_nop 0
Tv 34,v3,25600
Tv 36,v3,31744
Mavv 52,38,108
C_ 116,76,77
C_ 117,78,79
Mavv 48,38,104
E_ 80
Mavv 68,42,104
E_ 81
Mavv 64,42,100
E_ 82
Mavv 44,38,100
E_ 83
Mavv 40,38,96
C_ 120,80,81
C_ 121,82,83
Mavv 60,42,96
W_ 0
E_ 84
Mavv 80,30,96
s_nop 0
Tv 38,v4,25600
Tv 40,v4,31744
Mavv 100,34,96
E_ 85
Mavv 104,34,100
s_nop 0
Tv 42,v5,25600
Tv 44,v5,31744
Mavv 84,30,100
E_ 86
Mavv 88,30,104
E_ 87
Mavv 108,34,104
C_ 124,84,85
C_ 125,86,87
Mavv 112,34,108
E_ 88
Mavv 92,30,108
E_ 89
Mavv 96,30,112
E_ 90
Mavv 116,34,112
W_ 0
E_ 91
Mavv 136,38,112
s_nop 0
Tv 30,v2,26624
Tv 32,v2,32768
Mavv 156,42,112
C_ 128,88,89
C_ 129,90,91
Mavv 152,42,108
s_nop 0
Tv 34,v3,26624
Tv 36,v3,32768
Mavv 132,38,108
E_ 92
Mavv 128,38,104
E_ 93
Mavv 148,42,104
E_ 94
Mavv 144,42,100
E_ 95
Mavv 124,38,100
C_ 132,92,93
C_ 133,94,95
Mavv 120,38,96
v_mov_b32 v92, 0x3f803f80
v_mov_b32 v93, 0x3f803f80
Mavv 140,42,96
W_ 0
v_mov_b32 v94, 0x3f803f80
v_mov_b32 v95, 0x3f803f80
Mavv 160,30,96
s_nop 0
Tv 38,v4,26624
Tv 40,v4,32768
Mavv 180,34,96
Mavv 184,34,100
s_nop 0
Tv 42,v5,26624
Tv 44,v5,32768
Mavv 164,30,100
Mavv 168,30,104
Mavv 188,34,104
S_ s65,s61,110592
Mavv 192,34,108
S_ m0,s51,24576
Mavv 172,30,108
s_waitcnt vmcnt(6) lgkmcnt(0)
s_barrier
Mavv 176,30,112
Ln v7,32,s65,0
Mavv 196,34,112
Mavv 216,38,112
Dv 30,%0,6144
Mavv 236,42,112
Dv 34,v1,6144
Mavv 232,42,108
Ln v8,32,s65,3072
S_ m0,s51,25472
Mavv 212,38,108
Mavv 208,38,104
Ln v7,32,s65,128
Mavv 228,42,104
Mavv 224,42,100
Ln v8,32,s65,3200
S_ m0,s51,26368
Mavv 204,38,100
Mavv 200,38,96
Ln v7,32,s65,256
Mavv 220,42,96
.Lstep1_8:
Zvvv 76,92,116
Dv 38,%0,7168
Zvvv 80,92,120
Dv 42,v1,7168
Zvvv 84,92,124
Zvvv 88,92,128
Zvvv 92,92,132
W_ 2
Nvvv 96,30,136,56
Mvvv 96,34,156
Nvvv 100,30,140,60
s_nop 1
v_max3_f32 v10, v76, v80, v84
s_nop 0
v_max3_f32 v10, v10, v88, v92
Mvvv 100,34,160
v_cmp_lt_f32 vcc, 0x42000000, v10
s_cbranch_vccnz .Ltramp1_7
Nvvv 104,30,144,64
A_ v46,v46,v76
A_ v47,v47,v80
Mvvv 104,34,164
A_ v48,v48,v84
A_ v49,v49,v88
Nvvv 108,30,148,68
A_ v50,v50,v92
Mvvv 108,34,168
Ln v8,32,s65,3328
Nvvv 112,30,152,72
Mvvv 112,34,172
W_ 0
Mvvv 112,38,192
Dv 30,%0,8192
Mvvv 112,42,212
Dv 34,v1,8192
Mvvv 108,38,188
Mvvv 108,42,208
Mvvv 104,38,184
Mvvv 104,42,204
Mvvv 100,38,180
Mvvv 100,42,200
Mvvv 96,38,176
Mvvv 96,42,196
W_ 0
Mvvv 96,30,216
Dv 38,%0,0
Mvvv 96,34,236
Dv 42,v1,0
Mvvv 100,30,220
Mvvv 100,34,240
Mvvv 104,30,224
Mvvv 104,34,244
Mvvv 108,30,228
Mvvv 108,34,248
s_nop 0
E_ 96
Mvvv 112,30,232
E_ 97
Mvvv 112,34,252
W_ 0
E_ 98
Nvvv 76,38,136,56
Dv 30,%0,1024
Mvvv 76,42,156
Dv 34,v1,1024
Nvvv 80,38,140,60
E_ 99
Mvvv 80,42,160
C_ 99,98,99
C_ 98,96,97
Nvvv 84,38,144,64
E_ 100
Mvvv 84,42,164
E_ 101
Nvvv 88,38,148,68
E_ 102
Mvvv 88,42,168
E_ 103
Nvvv 92,38,152,72
C_ 103,102,103
C_ 102,100,101
Mvvv 92,42,172
W_ 0
E_ 104
Mvvv 92,30,192
Dv 38,%0,2048
Mvvv 92,34,212
Dv 42,v1,2048
Mvvv 88,30,188
E_ 105
Mvvv 88,34,208
E_ 106
Mvvv 84,30,184
S_ s65,s61,98304
E_ 107
Mvvv 84,34,204
S_ m0,s51,0
C_ 107,106,107
C_ 106,104,105
Mvvv 80,30,180
s_waitcnt vmcnt(6) lgkmcnt(0)
s_barrier
Mvvv 80,34,200
L_ v9,36,s65,0
Mvvv 76,30,176
E_ 108
Mvvv 76,34,196
L_ v9,36,s65,3072
S_ m0,s51,896
Mvvv 76,38,216
Tv 30,v2,12288
Tv 32,v2,18432
Mvvv 76,42,236
L_ v9,36,s65,128
Mvvv 80,38,220
s_nop 0
Tv 34,v3,12288
Tv 36,v3,18432
Mvvv 80,42,240
L_ v9,36,s65,3200
)A"
R"A(S_ m0,s51,1792
Mvvv 84,38,224
E_ 109
Mvvv 84,42,244
L_ v9,36,s65,256
Mvvv 88,38,228
E_ 110
Mvvv 88,42,248
L_ v9,36,s65,3328
Mvvv 92,38,232
E_ 111
Mvvv 92,42,252
W_ 0
C_ 111,110,111
C_ 110,108,109
Mavv 0,30,116
s_nop 0
Tv 38,v4,12288
Tv 40,v4,18432
Mavv 20,34,116
E_ 112
Mavv 24,34,120
s_nop 0
Tv 42,v5,12288
Tv 44,v5,18432
Mavv 4,30,120
E_ 113
Mavv 8,30,124
E_ 114
Mavv 28,34,124
E_ 115
Mavv 32,34,128
C_ 115,114,115
C_ 114,112,113
Mavv 12,30,128
E_ 76
Mavv 16,30,132
E_ 77
Mavv 36,34,132
W_ 0
E_ 78
Mavv 56,38,132
s_nop 0
Tv 30,v2,13312
Tv 32,v2,19456
Mavv 76,42,132
E_ 79
Mavv 72,42,128
s_nop 0
Tv 34,v3,13312
Tv 36,v3,19456
Mavv 52,38,128
C_ 96,76,77
C_ 97,78,79
Mavv 48,38,124
E_ 80
Mavv 68,42,124
E_ 81
Mavv 64,42,120
E_ 82
Mavv 44,38,120
E_ 83
Mavv 40,38,116
C_ 100,80,81
C_ 101,82,83
Mavv 60,42,116
W_ 0
E_ 84
Mavv 80,30,116
s_nop 0
Tv 38,v4,13312
Tv 40,v4,19456
Mavv 100,34,116
E_ 85
Mavv 104,34,120
s_nop 0
Tv 42,v5,13312
Tv 44,v5,19456
Mavv 84,30,120
E_ 86
Mavv 88,30,124
E_ 87
Mavv 108,34,124
C_ 104,84,85
C_ 105,86,87
Mavv 112,34,128
E_ 88
Mavv 92,30,128
E_ 89
Mavv 96,30,132
E_ 90
Mavv 116,34,132
W_ 0
E_ 91
Mavv 136,38,132
s_nop 0
Tv 30,v2,14336
Tv 32,v2,20480
Mavv 156,42,132
C_ 108,88,89
C_ 109,90,91
Mavv 152,42,128
s_nop 0
Tv 34,v3,14336
Tv 36,v3,20480
Mavv 132,38,128
E_ 92
Mavv 128,38,124
E_ 93
Mavv 148,42,124
E_ 94
Mavv 144,42,120
E_ 95
Mavv 124,38,120
C_ 112,92,93
C_ 113,94,95
Mavv 120,38,116
v_mov_b32 v92, 0x3f803f80
v_mov_b32 v93, 0x3f803f80
Mavv 140,42,116
W_ 0
v_mov_b32 v94, 0x3f803f80
v_mov_b32 v95, 0x3f803f80
Mavv 160,30,116
s_nop 0
Tv 38,v4,14336
Tv 40,v4,20480
Mavv 180,34,116
Mavv 184,34,120
s_nop 0
Tv 42,v5,14336
Tv 44,v5,20480
Mavv 164,30,120
Mavv 168,30,124
Mavv 188,34,124
S_ s65,s61,122880
Mavv 192,34,128
S_ m0,s51,12288
Mavv 172,30,128
s_waitcnt vmcnt(6) lgkmcnt(0)
s_barrier
Mavv 176,30,132
Ln v7,32,s65,0
Mavv 196,34,132
Mavv 216,38,132
Dv 30,%0,30720
Mavv 236,42,132
Dv 34,v1,30720
Mavv 232,42,128
Ln v8,32,s65,3072
S_ m0,s51,13184
Mavv 212,38,128
Mavv 208,38,124
Ln v7,32,s65,128
Mavv 228,42,124
Mavv 224,42,120
Ln v8,32,s65,3200
S_ m0,s51,14080
Mavv 204,38,120
Mavv 200,38,116
Ln v7,32,s65,256
Mavv 220,42,116
s_cmp_eq_u32 s4, 0
s_cbranch_scc1 .Lhs_d1
.Lhs_d1_back:
.Lstep1_9:
Zvvv 76,92,96
Dv 38,%0,31744
Zvvv 80,92,100
Dv 42,v1,31744
Zvvv 84,92,104
Zvvv 88,92,108
Zvvv 92,92,112
W_ 2
Nvvv 116,30,136,56
Mvvv 116,34,156
Nvvv 120,30,140,60
s_nop 1
v_max3_f32 v10, v76, v80, v84
s_nop 0
v_max3_f32 v10, v10, v88, v92
Mvvv 120,34,160
v_cmp_lt_f32 vcc, 0x42000000, v10
s_cbranch_vccnz .Ltramp1_8
Nvvv 124,30,144,64
A_ v46,v46,v76
A_ v47,v47,v80
Mvvv 124,34,164
A_ v48,v48,v84
A_ v49,v49,v88
Nvvv 128,30,148,68
A_ v50,v50,v92
Mvvv 128,34,168
Ln v8,32,s65,3328
Nvvv 132,30,152,72
Mvvv 132,34,172
W_ 0
Mvvv 132,38,192
Dv 30,%0,32768
Mvvv 132,42,212
Dv 34,v1,32768
Mvvv 128,38,188
Mvvv 128,42,208
Mvvv 124,38,184
Mvvv 124,42,204
Mvvv 120,38,180
Mvvv 120,42,200
Mvvv 116,38,176
Mvvv 116,42,196
W_ 0
Mvvv 116,30,216
Dv 38,%0,24576
Mvvv 116,34,236
Dv 42,v1,24576
Mvvv 120,30,220
Mvvv 120,34,240
Mvvv 124,30,224
Mvvv 124,34,244
Mvvv 128,30,228
Mvvv 128,34,248
s_nop 0
E_ 116
Mvvv 132,30,232
E_ 117
Mvvv 132,34,252
W_ 0
E_ 118
Nvvv 76,38,136,56
Dv 30,%0,25600
Mvvv 76,42,156
Dv 34,v1,25600
Nvvv 80,38,140,60
E_ 119
Mvvv 80,42,160
C_ 119,118,119
C_ 118,116,117
Nvvv 84,38,144,64
E_ 120
Mvvv 84,42,164
E_ 121
Nvvv 88,38,148,68
E_ 122
Mvvv 88,42,168
E_ 123
Nvvv 92,38,152,72
C_ 123,122,123
C_ 122,120,121
Mvvv 92,42,172
W_ 0
E_ 124
Mvvv 92,30,192
Dv 38,%0,26624
Mvvv 92,34,212
Dv 42,v1,26624
Mvvv 88,30,188
E_ 125
Mvvv 88,34,208
E_ 126
Mvvv 84,30,184
S_ s65,s61,110592
E_ 127
Mvvv 84,34,204
S_ m0,s51,24576
C_ 127,126,127
C_ 126,124,125
Mvvv 80,30,180
s_waitcnt vmcnt(6) lgkmcnt(0)
s_barrier
Mvvv 80,34,200
L_ v9,36,s65,0
Mvvv 76,30,176
E_ 128
Mvvv 76,34,196
L_ v9,36,s65,3072
S_ m0,s51,25472
Mvvv 76,38,216
Tv 30,v2,0
Tv 32,v2,6144
Mvvv 76,42,236
L_ v9,36,s65,128
Mvvv 80,38,220
s_nop 0
Tv 34,v3,0
Tv 36,v3,6144
Mvvv 80,42,240
L_ v9,36,s65,3200
S_ m0,s51,26368
Mvvv 84,38,224
E_ 129
Mvvv 84,42,244
L_ v9,36,s65,256
Mvvv 88,38,228
E_ 130
Mvvv 88,42,248
L_ v9,36,s65,3328
Mvvv 92,38,232
E_ 131
Mvvv 92,42,252
W_ 0
C_ 131,130,131
C_ 130,128,129
Mavv 0,30,96
s_nop 0
Tv 38,v4,0
Tv 40,v4,6144
Mavv 20,34,96
E_ 132
Mavv 24,34,100
s_nop 0
Tv 42,v5,0
Tv 44,v5,6144
Mavv 4,30,100
E_ 133
Mavv 8,30,104
E_ 134
Mavv 28,34,104
E_ 135
Mavv 32,34,108
C_ 135,134,135
C_ 134,132,133
Mavv 12,30,108
E_ 76
Mavv 16,30,112
E_ 77
Mavv 36,34,112
W_ 0
E_ 78
Mavv 56,38,112
s_nop 0
Tv 30,v2,1024
Tv 32,v2,7168
Mavv 76,42,112
E_ 79
Mavv 72,42,108
s_nop 0
Tv 34,v3,1024
Tv 36,v3,7168
Mavv 52,38,108
C_ 116,76,77
C_ 117,78,79
Mavv 48,38,104
E_ 80
Mavv 68,42,104
E_ 81
Mavv 64,42,100
E_ 82
Mavv 44,38,100
E_ 83
Mavv 40,38,96
C_ 120,80,81
C_ 121,82,83
Mavv 60,42,96
W_ 0
E_ 84
Mavv 80,30,96
s_nop 0
Tv 38,v4,1024
Tv 40,v4,7168
Mavv 100,34,96
E_ 85
Mavv 104,34,100
s_nop 0
Tv 42,v5,1024
Tv 44,v5,7168
Mavv 84,30,100
E_ 86
Mavv 88,30,104
E_ 87
Mavv 108,34,104
C_ 124,84,85
C_ 125,86,87
Mavv 112,34,108
E_ 88
Mavv 92,30,108
E_ 89
Mavv 96,30,112
E_ 90
Mavv 116,34,112
W_ 0
E_ 91
Mavv 136,38,112
s_nop 0
Tv 30,v2,2048
Tv 32,v2,8192
Mavv 156,42,112
C_ 128,88,89
C_ 129,90,91
Mavv 152,42,108
s_nop 0
Tv 34,v3,2048
Tv 36,v3,8192
Mavv 132,38,108
E_ 92
Mavv 128,38,104
E_ 93
Mavv 148,42,104
E_ 94
Mavv 144,42,100
E_ 95
Mavv 124,38,100
C_ 132,92,93
C_ 133,94,95
Mavv 120,38,96
v_mov_b32 v92, 0x3f803f80
v_mov_b32 v93, 0x3f803f80
Mavv 140,42,96
W_ 0
v_mov_b32 v94, 0x3f803f80
v_mov_b32 v95, 0x3f803f80
Mavv 160,30,96
s_nop 0
Tv 38,v4,2048
Tv 40,v4,8192
Mavv 180,34,96
Mavv 184,34,100
s_nop 0
Tv 42,v5,2048
Tv 44,v5,8192
Mavv 164,30,100
Mavv 168,30,104
Mavv 188,34,104
S_ s65,s61,135168
Mavv 192,34,108
S_ m0,s51,0
Mavv 172,30,108
s_waitcnt vmcnt(6) lgkmcnt(0)
s_barrier
Mavv 176,30,112
Ln v7,32,s65,0
Mavv 196,34,112
Mavv 216,38,112
Dv 30,%0,18432
Mavv 236,42,112
Dv 34,v1,18432
Mavv 232,42,108
Ln v8,32,s65,3072
S_ m0,s51,896
Mavv 212,38,108
Mavv 208,38,104
Ln v7,32,s65,128
Mavv 228,42,104
Mavv 224,42,100
Ln v8,32,s65,3200
S_ m0,s51,1792
Mavv 204,38,100
Mavv 200,38,96
Ln v7,32,s65,256
Mavv 220,42,96
.Lstep1_10:
Zvvv 76,92,116
Dv 38,%0,19456
Zvvv 80,92,120
Dv 42,v1,19456
Zvvv 84,92,124
Zvvv 88,92,128
Zvvv 92,92,132
W_ 2
Nvvv 96,30,136,56
Mvvv 96,34,156
Nvvv 100,30,140,60
s_nop 1
v_max3_f32 v10, v76, v80, v84
s_nop 0
v_max3_f32 v10, v10, v88, v92
Mvvv 100,34,160
v_cmp_lt_f32 vcc, 0x42000000, v10
s_cbranch_vccnz .Ltramp1_9
Nvvv 104,30,144,64
A_ v46,v46,v76
A_ v47,v47,v80
Mvvv 104,34,164
A_ v48,v48,v84
A_ v49,v49,v88
Nvvv 108,30,148,68
A_ v50,v50,v92
Mvvv 108,34,168
Ln v8,32,s65,3328
Nvvv 112,30,152,72
Mvvv 112,34,172
W_ 0
Mvvv 112,38,192
Dv 30,%0,20480
Mvvv 112,42,212
Dv 34,v1,20480
Mvvv 108,38,188
Mvvv 108,42,208
Mvvv 104,38,184
Mvvv 104,42,204
Mvvv 100,38,180
Mvvv 100,42,200
Mvvv 96,38,176
Mvvv 96,42,196
W_ 0
Mvvv 96,30,216
Dv 38,%0,12288
Mvvv 96,34,236
Dv 42,v1,12288
Mvvv 100,30,220
Mvvv 100,34,240
Mvvv 104,30,224
Mvvv 104,34,244
Mvvv 108,30,228
Mvvv 108,34,248
s_nop 0
E_ 96
Mvvv 112,30,232
E_ 97
Mvvv 112,34,252
W_ 0
E_ 98
Nvvv 76,38,136,56
Dv 30,%0,13312
Mvvv 76,42,156
Dv 34,v1,13312
Nvvv 80,38,140,60
E_ 99
Mvvv 80,42,160
C_ 99,98,99
C_ 98,96,97
Nvvv 84,38,144,64
E_ 100
Mvvv 84,42,164
E_ 101
Nvvv 88,38,148,68
E_ 102
Mvvv 88,42,168
E_ 103
Nvvv 92,38,152,72
C_ 103,102,103
C_ 102,100,101
Mvvv 92,42,172
W_ 0
E_ 104
Mvvv 92,30,192
Dv 38,%0,14336
Mvvv 92,34,212
Dv 42,v1,14336
Mvvv 88,30,188
E_ 105
Mvvv 88,34,208
E_ 106
Mvvv 84,30,184
S_ s65,s61,122880
E_ 107
Mvvv 84,34,204
S_ m0,s51,12288
C_ 107,106,107
C_ 106,104,105
Mvvv 80,30,180
s_waitcnt vmcnt(6) lgkmcnt(0)
s_barrier
Mvvv 80,34,200
L_ v9,36,s65,0
Mvvv 76,30,176
E_ 108
Mvvv 76,34,196
L_ v9,36,s65,3072
S_ m0,s51,13184
Mvvv 76,38,216
Tv 30,v2,24576
Tv 32,v2,30720
Mvvv 76,42,236
L_ v9,36,s65,128
Mvvv 80,38,220
s_nop 0
Tv 34,v3,24576
Tv 36,v3,30720
Mvvv 80,42,240
L_ v9,36,s65,3200
S_ m0,s51,14080
Mvvv 84,38,224
E_ 109
Mvvv 84,42,244
L_ v9,36,s65,256
Mvvv 88,38,228
E_ 110
Mvvv 88,42,248
L_ v9,36,s65,3328
Mvvv 92,38,232
E_ 111
Mvvv 92,42,252
W_ 0
C_ 111,110,111
C_ 110,108,109
Mavv 0,30,116
s_nop 0
Tv 38,v4,24576
Tv 40,v4,30720
Mavv 20,34,116
E_ 112
Mavv 24,34,120
s_nop 0
Tv 42,v5,24576
Tv 44,v5,30720
Mavv 4,30,120
E_ 113
Mavv 8,30,124
E_ 114
Mavv 28,34,124
E_ 115
Mavv 32,34,128
C_ 115,114,115
C_ 114,112,113
Mavv 12,30,128
E_ 76
Mavv 16,30,132
E_ 77
Mavv 36,34,132
W_ 0
E_ 78
Mavv 56,38,132
s_nop 0
Tv 30,v2,25600
Tv 32,v2,31744
Mavv 76,42,132
E_ 79
Mavv 72,42,128
s_nop 0
Tv 34,v3,25600
Tv 36,v3,31744
Mavv 52,38,128
C_ 96,76,77
C_ 97,78,79
Mavv 48,38,124
E_ 80
Mavv 68,42,124
E_ 81
Mavv 64,42,120
E_ 82
Mavv 44,38,120
E_ 83
Mavv 40,38,116
C_ 100,80,81
C_ 101,82,83
Mavv 60,42,116
W_ 0
E_ 84
Mavv 80,30,116
s_nop 0
Tv 38,v4,25600
Tv 40,v4,31744
Mavv 100,34,116
E_ 85
Mavv 104,34,120
s_nop 0
Tv 42,v5,25600
Tv 44,v5,31744
Mavv 84,30,120
E_ 86
Mavv 88,30,124
E_ 87
Mavv 108,34,124
C_ 104,84,85
C_ 105,86,87
Mavv 112,34,128
E_ 88
Mavv 92,30,128
E_ 89
Mavv 96,30,132
E_ 90
Mavv 116,34,132
W_ 0
E_ 91
Mavv 136,38,132
s_nop 0
Tv 30,v2,26624
Tv 32,v2,32768
Mavv 156,42,132
C_ 108,88,89
C_ 109,90,91
Mavv 152,42,128
s_nop 0
Tv 34,v3,26624
Tv 36,v3,32768
Mavv 132,38,128
E_ 92
Mavv 128,38,124
E_ 93
Mavv 148,42,124
E_ 94
Mavv 144,42,120
E_ 95
Mavv 124,38,120
C_ 112,92,93
C_ 113,94,95
Mavv 120,38,116
v_mov_b32 v92, 0x3f803f80
v_mov_b32 v93, 0x3f803f80
Mavv 140,42,116
W_ 0
v_mov_b32 v94, 0x3f803f80
v_mov_b32 v95, 0x3f803f80
Mavv 160,30,116
s_nop 0
Tv 38,v4,26624
Tv 40,v4,32768
Mavv 180,34,116
Mavv 184,34,120
s_nop 0
Tv 42,v5,26624
Tv 44,v5,32768
Mavv 164,30,120
Mavv 168,30,124
Mavv 188,34,124
S_ s65,s61,147456
Mavv 192,34,128
S_ m0,s51,24576
Mavv 172,30,128
s_waitcnt vmcnt(6) lgkmcnt(0)
s_barrier
Mavv 176,30,132
Ln v7,32,s65,0
Mavv 196,34,132
Mavv 216,38,132
Dv 30,%0,6144
Mavv 236,42,132
Dv 34,v1,6144
Mavv 232,42,128
Ln v8,32,s65,3072
S_ m0,s51,25472
Mavv 212,38,128
Mavv 208,38,124
Ln v7,32,s65,128
Mavv 228,42,124
Mavv 224,42,120
Ln v8,32,s65,3200
S_ m0,s51,26368
Mavv 204,38,120
Mavv 200,38,116
Ln v7,32,s65,256
Mavv 220,42,116
.Lstep1_11:
Zvvv 76,92,96
Dv 38,%0,7168
Zvvv 80,92,100
Dv 42,v1,7168
Zvvv 84,92,104
Zvvv 88,92,108
Zvvv 92,92,112
W_ 2
Nvvv 116,30,136,56
Mvvv 116,34,156
Nvvv 120,30,140,60
s_nop 1
v_max3_f32 v10, v76, v80, v84
s_nop 0
v_max3_f32 v10, v10, v88, v92
Mvvv 120,34,160
v_cmp_lt_f32 vcc, 0x42000000, v10
s_cbranch_vccnz .Ltramp1_10
Nvvv 124,30,144,64
A_ v46,v46,v76
A_ v47,v47,v80
Mvvv 124,34,164
A_ v48,v48,v84
A_ v49,v49,v88
Nvvv 128,30,148,68
A_ v50,v50,v92
Mvvv 128,34,168
Ln v8,32,s65,3328
Nvvv 132,30,152,72
Mvvv 132,34,172
W_ 0
Mvvv 132,38,192
Dv 30,%0,8192
Mvvv 132,42,212
Dv 34,v1,8192
Mvvv 128,38,188
Mvvv 128,42,208
Mvvv 124,38,184
Mvvv 124,42,204
Mvvv 120,38,180
Mvvv 120,42,200
Mvvv 116,38,176
Mvvv 116,42,196
W_ 0
Mvvv 116,30,216
Dv 38,%0,0
Mvvv 116,34,236
Dv 42,v1,0
Mvvv 120,30,220
Mvvv 120,34,240
Mvvv 124,30,224
Mvvv 124,34,244
Mvvv 128,30,228
Mvvv 128,34,248
s_nop 0
E_ 116
Mvvv 132,30,232
E_ 117
Mvvv 132,34,252
W_ 0
E_ 118
Nvvv 76,38,136,56
Dv 30,%0,1024
Mvvv 76,42,156
Dv 34,v1,1024
Nvvv 80,38,140,60
E_ 119
Mvvv 80,42,160
C_ 119,118,119
C_ 118,116,117
Nvvv 84,38,144,64
E_ 120
Mvvv 84,42,164
E_ 121
Nvvv 88,38,148,68
E_ 122
Mvvv 88,42,168
E_ 123
Nvvv 92,38,152,72
C_ 123,122,123
C_ 122,120,121
Mvvv 92,42,172
W_ 0
E_ 124
Mvvv 92,30,192
Dv 38,%0,2048
Mvvv 92,34,212
Dv 42,v1,2048
Mvvv 88,30,188
E_ 125
Mvvv 88,34,208
E_ 126
Mvvv 84,30,184
S_ s65,s61,135168
E_ 127
Mvvv 84,34,204
S_ m0,s51,0
C_ 127,126,127
C_ 126,124,125
Mvvv 80,30,180
s_waitcnt vmcnt(6) lgkmcnt(0)
s_barrier
Mvvv 80,34,200
L_ v9,36,s65,0
Mvvv 76,30,176
E_ 128
Mvvv 76,34,196
L_ v9,36,s65,3072
S_ m0,s51,896
Mvvv 76,38,216
Tv 30,v2,12288
Tv 32,v2,18432
Mvvv 76,42,236
L_ v9,36,s65,128
Mvvv 80,38,220
s_nop 0
Tv 34,v3,12288
Tv 36,v3,18432
Mvvv 80,42,240
L_ v9,36,s65,3200
S_ m0,s51,1792
Mvvv 84,38,224
E_ 129
Mvvv 84,42,244
L_ v9,36,s65,256
Mvvv 88,38,228
E_ 130
Mvvv 88,42,248
L_ v9,36,s65,3328
Mvvv 92,38,232
E_ 131
Mvvv 92,42,252
W_ 0
C_ 131,130,131
C_ 130,128,129
Mavv 0,30,96
s_nop 0
Tv 38,v4,12288
Tv 40,v4,18432
Mavv 20,34,96
E_ 132
Mavv 24,34,100
s_nop 0
Tv 42,v5,12288
Tv 44,v5,18432
Mavv 4,30,100
E_ 133
Mavv 8,30,104
E_ 134
Mavv 28,34,104
E_ 135
Mavv 32,34,108
C_ 135,134,135
C_ 134,132,133
Mavv 12,30,108
E_ 76
Mavv 16,30,112
E_ 77
Mavv 36,34,112
W_ 0
E_ 78
Mavv 56,38,112
s_nop 0
Tv 30,v2,13312
Tv 32,v2,19456
Mavv 76,42,112
E_ 79
Mavv 72,42,108
s_nop 0
Tv 34,v3,13312
Tv 36,v3,19456
Mavv 52,38,108
C_ 116,76,77
C_ 117,78,79
Mavv 48,38,104
E_ 80
Mavv 68,42,104
E_ 81
Mavv 64,42,100
E_ 82
Mavv 44,38,100
E_ 83
Mavv 40,38,96
C_ 120,80,81
C_ 121,82,83
Mavv 60,42,96
W_ 0
E_ 84
Mavv 80,30,96
s_nop 0
Tv 38,v4,13312
Tv 40,v4,19456
Mavv 100,34,96
E_ 85
Mavv 104,34,100
s_nop 0
Tv 42,v5,13312
Tv 44,v5,19456
Mavv 84,30,100
E_ 86
Mavv 88,30,104
E_ 87
Mavv 108,34,104
C_ 124,84,85
C_ 125,86,87
Mavv 112,34,108
E_ 88
Mavv 92,30,108
E_ 89
Mavv 96,30,112
E_ 90
Mavv 116,34,112
W_ 0
E_ 91
Mavv 136,38,112
s_nop 0
Tv 30,v2,14336
Tv 32,v2,20480
Mavv 156,42,112
C_ 128,88,89
C_ 129,90,91
Mavv 152,42,108
s_nop 0
Tv 34,v3,14336
Tv 36,v3,20480
Mavv 132,38,108
E_ 92
Mavv 128,38,104
E_ 93
Mavv 148,42,104
E_ 94
Mavv 144,42,100
E_ 95
Mavv 124,38,100
C_ 132,92,93
C_ 133,94,95
Mavv 120,38,96
v_mov_b32 v92, 0x3f803f80
v_mov_b32 v93, 0x3f803f80
Mavv 140,42,96
W_ 0
v_mov_b32 v94, 0x3f803f80
v_mov_b32 v95, 0x3f803f80
Mavv 160,30,96
s_nop 0
Tv 38,v4,14336
Tv 40,v4,20480
Mavv 180,34,96
Mavv 184,34,100
s_nop 0
Tv 42,v5,14336
Tv 44,v5,20480
Mavv 164,30,100
Mavv 168,30,104
Mavv 188,34,104
S_ s65,s61,159744
Mavv 192,34,108
S_ m0,s51,12288
Mavv 172,30,108
s_waitcnt vmcnt(6) lgkmcnt(0)
s_barrier
Mavv 176,30,112
Ln v7,32,s65,0
Mavv 196,34,112
Mavv 216,38,112
Dv 30,%0,30720
Mavv 236,42,112
Dv 34,v1,30720
Mavv 232,42,108
Ln v8,32,s65,3072
S_ m0,s51,13184
Mavv 212,38,108
Mavv 208,38,104
Ln v7,32,s65,128
Mavv 228,42,104
Mavv 224,42,100
Ln v8,32,s65,3200
S_ m0,s51,14080
Mavv 204,38,100
Mavv 200,38,96
Ln v7,32,s65,256
Mavv 220,42,96
.Lstep1_12:
Zvvv 76,92,116
Dv 38,%0,31744
Zvvv 80,92,120
Dv 42,v1,31744
Zvvv 84,92,124
Zvvv 88,92,128
Zvvv 92,92,132
W_ 2
Nvvv 96,30,136,56
Mvvv 96,34,156
Nvvv 100,30,140,60
s_nop 1
v_max3_f32 v10, v76, v80, v84
s_nop 0
v_max3_f32 v10, v10, v88, v92
Mvvv 100,34,160
v_cmp_lt_f32 vcc, 0x42000000, v10
s_cbranch_vccnz .Ltramp1_11
Nvvv 104,30,144,64
A_ v46,v46,v76
A_ v47,v47,v80
Mvvv 104,34,164
A_ v48,v48,v84
A_ v49,v49,v88
Nvvv 108,30,148,68
A_ v50,v50,v92
Mvvv 108,34,168
Ln v8,32,s65,3328
Nvvv 112,30,152,72
R_ v11,a248
Mvvv 112,34,172
W_ 0
v_min_u32 v11, 240, v11
Mvvv 112,38,192
Dv 30,%0,32768
v_lshlrev_b32 v11, 3, v11
Mvvv 112,42,212
Dv 34,v1,32768
Mvvv 108,38,188
buffer_load_dword a254, v11, s[44:47], s66 offen
Mvvv 108,42,208
Mvvv 104,38,184
Mvvv 104,42,204
Mvvv 100,38,180
Mvvv 100,42,200
Mvvv 96,38,176
Mvvv 96,42,196
W_ 0
Mvvv 96,30,216
Dv 38,%0,24576
Mvvv 96,34,236
Dv 42,v1,24576
Mvvv 100,30,220
Mvvv 100,34,240
Mvvv 104,30,224
Mvvv 104,34,244
Mvvv 108,30,228
Mvvv 108,34,248
s_nop 0
E_ 96
Mvvv 112,30,232
E_ 97
Mvvv 112,34,252
W_ 0
E_ 98
Nvvv 76,38,136,56
Dv 30,%0,25600
Mvvv 76,42,156
Dv 34,v1,25600
Nvvv 80,38,140,60
E_ 99
Mvvv 80,42,160
C_ 99,98,99
C_ 98,96,97
Nvvv 84,38,144,64
E_ 100
Mvvv 84,42,164
E_ 101
Nvvv 88,38,148,68
E_ 102
Mvvv 88,42,168
E_ 103
Nvvv 92,38,152,72
C_ 103,102,103
C_ 102,100,101
Mvvv 92,42,172
W_ 0
E_ 104
Mvvv 92,30,192
Dv 38,%0,26624
Mvvv 92,34,212
Dv 42,v1,26624
Mvvv 88,30,188
E_ 105
Mvvv 88,34,208
E_ 106
Mvvv 84,30,184
S_ s65,s61,147456
E_ 107
Mvvv 84,34,204
S_ m0,s51,24576
C_ 107,106,107
C_ 106,104,105
Mvvv 80,30,180
s_waitcnt vmcnt(7) lgkmcnt(0)
s_barrier
Mvvv 80,34,200
Ln v9,36,s65,0
Mvvv 76,30,176
E_ 108
Mvvv 76,34,196
Ln v9,36,s65,3072
S_ m0,s51,25472
Mvvv 76,38,216
Tv 30,v2,0
Tv 32,v2,6144
Mvvv 76,42,236
Ln v9,36,s65,128
Mvvv 80,38,220
s_nop 0
Tv 34,v3,0
Tv 36,v3,6144
Mvvv 80,42,240
Ln v9,36,s65,3200
S_ m0,s51,26368
Mvvv 84,38,224
E_ 109
Mvvv 84,42,244
Ln v9,36,s65,256
Mvvv 88,38,228
E_ 110
Mvvv 88,42,248
Ln v9,36,s65,3328
Mvvv 92,38,232
E_ 111
Mvvv 92,42,252
W_ 0
C_ 111,110,111
C_ 110,108,109
Mavv 0,30,116
s_nop 0
Tv 38,v4,0
Tv 40,v4,6144
Mavv 20,34,116
E_ 112
Mavv 24,34,120
s_nop 0
Tv 42,v5,0
Tv 44,v5,6144
Mavv 4,30,120
E_ 113
Mavv 8,30,124
E_ 114
Mavv 28,34,124
E_ 115
Mavv 32,34,128
C_ 115,114,115
C_ 114,112,113
Mavv 12,30,128
E_ 76
Mavv 16,30,132
E_ 77
Mavv 36,34,132
W_ 0
E_ 78
Mavv 56,38,132
s_nop 0
Tv 30,v2,1024
Tv 32,v2,7168
Mavv 76,42,132
E_ 79
Mavv 72,42,128
s_nop 0
Tv 34,v3,1024
Tv 36,v3,7168
Mavv 52,38,128
C_ 96,76,77
C_ 97,78,79
Mavv 48,38,124
E_ 80
Mavv 68,42,124
E_ 81
Mavv 64,42,120
E_ 82
Mavv 44,38,120
E_ 83
Mavv 40,38,116
C_ 100,80,81
C_ 101,82,83
Mavv 60,42,116
W_ 0
E_ 84
Mavv 80,30,116
s_nop 0
Tv 38,v4,1024
Tv 40,v4,7168
Mavv 100,34,116
E_ 85
Mavv 104,34,120
s_nop 0
Tv 42,v5,1024
Tv 44,v5,7168
Mavv 84,30,120
E_ 86
Mavv 88,30,124
E_ 87
Mavv 108,34,124
C_ 104,84,85
C_ 105,86,87
Mavv 112,34,128
E_ 88
Mavv 92,30,128
E_ 89
Mavv 96,30,132
E_ 90
Mavv 116,34,132
W_ 0
E_ 91
Mavv 136,38,132
s_nop 0
Tv 30,v2,2048
Tv 32,v2,8192
Mavv 156,42,132
C_ 108,88,89
C_ 109,90,91
Mavv 152,42,128
s_nop 0
Tv 34,v3,2048
Tv 36,v3,8192
Mavv 132,38,128
E_ 92
Mavv 128,38,124
E_ 93
Mavv 148,42,124
E_ 94
Mavv 144,42,120
E_ 95
Mavv 124,38,120
C_ 112,92,93
C_ 113,94,95
Mavv 120,38,116
v_mov_b32 v92, 0x3f803f80
v_mov_b32 v93, 0x3f803f80
Mavv 140,42,116
W_ 0
v_mov_b32 v94, 0x3f803f80
v_mov_b32 v95, 0x3f803f80
Mavv 160,30,116
s_nop 0
Tv 38,v4,2048
Tv 40,v4,8192
Mavv 180,34,116
Mavv 184,34,120
s_nop 0
Tv 42,v5,2048
Tv 44,v5,8192
Mavv 164,30,120
Mavv 168,30,124
Mavv 188,34,124
S_ s65,s61,172032
Mavv 192,34,128
S_ m0,s51,0
Mavv 172,30,128
s_waitcnt vmcnt(7) lgkmcnt(0)
s_barrier
Mavv 176,30,132
Ln v7,32,s65,0
Mavv 196,34,132
Mavv 216,38,132
Dv 30,%0,18432
Mavv 236,42,132
Dv 34,v1,18432
Mavv 232,42,128
Ln v8,32,s65,3072
S_ m0,s51,896
Mavv 212,38,128
Mavv 208,38,124
Ln v7,32,s65,128
Mavv 228,42,124
Mavv 224,42,120
Ln v8,32,s65,3200
S_ m0,s51,1792
Mavv 204,38,120
Mavv 200,38,116
Ln v7,32,s65,256
Mavv 220,42,116
.Lstep1_13:
Zvvv 76,92,96
Dv 38,%0,19456
Zvvv 80,92,100
Dv 42,v1,19456
Zvvv 84,92,104
Zvvv 88,92,108
Zvvv 92,92,112
W_ 2
Nvvv 116,30,136,56
Mvvv 116,34,156
Nvvv 120,30,140,60
s_nop 1
v_max3_f32 v10, v76, v80, v84
s_nop 0
v_max3_f32 v10, v10, v88, v92
Mvvv 120,34,160
v_cmp_lt_f32 vcc, 0x42000000, v10
s_cbranch_vccnz .Ltramp1_12
Nvvv 124,30,144,64
A_ v46,v46,v76
A_ v47,v47,v80
Mvvv 124,34,164
A_ v48,v48,v84
A_ v49,v49,v88
Nvvv 128,30,148,68
A_ v50,v50,v92
Mvvv 128,34,168
Ln v8,32,s65,3328
Nvvv 132,30,152,72
Mvvv 132,34,172
W_ 0
Mvvv 132,38,192
Dv 30,%0,20480
Mvvv 132,42,212
Dv 34,v1,20480
Mvvv 128,38,188
Mvvv 128,42,208
Mvvv 124,38,184
Mvvv 124,42,204
Mvvv 120,38,180
Mvvv 120,42,200
Mvvv 116,38,176
Mvvv 116,42,196
W_ 0
Mvvv 116,30,216
Dv 38,%0,12288
Mvvv 116,34,236
Dv 42,v1,12288
Mvvv 120,30,220
Mvvv 120,34,240
Mvvv 124,30,224
Mvvv 124,34,244
Mvvv 128,30,228
Mvvv 128,34,248
s_nop 0
E_ 116
Mvvv 132,30,232
E_ 117
Mvvv 132,34,252
W_ 0
E_ 118
Nvvv 76,38,136,56
Dv 30,%0,13312
Mvvv 76,42,156
Dv 34,v1,13312
Nvvv 80,38,140,60
E_ 119
Mvvv 80,42,160
C_ 119,118,119
C_ 118,116,117
Nvvv 84,38,144,64
E_ 120
Mvvv 84,42,164
E_ 121
Nvvv 88,38,148,68
E_ 122
Mvvv 88,42,168
E_ 123
Nvvv 92,38,152,72
C_ 123,122,123
C_ 122,120,121
Mvvv 92,42,172
W_ 0
E_ 124
Mvvv 92,30,192
Dv 38,%0,14336
Mvvv 92,34,212
Dv 42,v1,14336
Mvvv 88,30,188
E_ 125
Mvvv 88,34,208
E_ 126
Mvvv 84,30,184
S_ s65,s61,159744
E_ 127
Mvvv 84,34,204
S_ m0,s51,12288
C_ 127,126,127
C_ 126,124,125
Mvvv 80,30,180
s_waitcnt vmcnt(6) lgkmcnt(0)
s_barrier
Mvvv 80,34,200
Ln v9,36,s65,0
Mvvv 76,30,176
E_ 128
Mvvv 76,34,196
Ln v9,36,s65,3072
S_ m0,s51,13184
Mvvv 76,38,216
Tv 30,v2,24576
Tv 32,v2,30720
Mvvv 76,42,236
Ln v9,36,s65,128
Mvvv 80,38,220
s_nop 0
Tv 34,v3,24576
Tv 36,v3,30720
Mvvv 80,42,240
Ln v9,36,s65,3200
S_ m0,s51,14080
Mvvv 84,38,224
E_ 129
Mvvv 84,42,244
Ln v9,36,s65,256
Mvvv 88,38,228
E_ 130
Mvvv 88,42,248
Ln v9,36,s65,3328
Mvvv 92,38,232
E_ 131
Mvvv 92,42,252
W_ 0
C_ 131,130,131
C_ 130,128,129
Mavv 0,30,96
s_nop 0
Tv 38,v4,24576
Tv 40,v4,30720
Mavv 20,34,96
E_ 132
Mavv 24,34,100
s_nop 0
Tv 42,v5,24576
Tv 44,v5,30720
Mavv 4,30,100
E_ 133
Mavv 8,30,104
E_ 134
Mavv 28,34,104
E_ 135
Mavv 32,34,108
C_ 135,134,135
C_ 134,132,133
Mavv 12,30,108
E_ 76
Mavv 16,30,112
E_ 77
Mavv 36,34,112
W_ 0
E_ 78
Mavv 56,38,112
s_nop 0
Tv 30,v2,25600
Tv 32,v2,31744
Mavv 76,42,112
E_ 79
Mavv 72,42,108
s_nop 0
Tv 34,v3,25600
Tv 36,v3,31744
Mavv 52,38,108
C_ 116,76,77
C_ 117,78,79
Mavv 48,38,104
E_ 80
Mavv 68,42,104
E_ 81
Mavv 64,42,100
E_ 82
Mavv 44,38,100
E_ 83
Mavv 40,38,96
C_ 120,80,81
C_ 121,82,83
Mavv 60,42,96
W_ 0
E_ 84
Mavv 80,30,96
s_nop 0
Tv 38,v4,25600
Tv 40,v4,31744
Mavv 100,34,96
E_ 85
Mavv 104,34,100
s_nop 0
Tv 42,v5,25600
Tv 44,v5,31744
Mavv 84,30,100
E_ 86
Mavv 88,30,104
E_ 87
Mavv 108,34,104
C_ 124,84,85
C_ 125,86,87
Mavv 112,34,108
E_ 88
Mavv 92,30,108
E_ 89
Mavv 96,30,112
E_ 90
Mavv 116,34,112
W_ 0
E_ 91
Mavv 136,38,112
s_nop 0
Tv 30,v2,26624
Tv 32,v2,32768
Mavv 156,42,112
C_ 128,88,89
C_ 129,90,91
Mavv 152,42,108
s_nop 0
Tv 34,v3,26624
Tv 36,v3,32768
Mavv 132,38,108
E_ 92
Mavv 128,38,104
E_ 93
Mavv 148,42,104
E_ 94
Mavv 144,42,100
E_ 95
Mavv 124,38,100
C_ 132,92,93
C_ 133,94,95
Mavv 120,38,96
v_mov_b32 v92, 0x3f803f80
v_mov_b32 v93, 0x3f803f80
Mavv 140,42,96
W_ 0
v_mov_b32 v94, 0x3f803f80
v_mov_b32 v95, 0x3f803f80
Mavv 160,30,96
s_nop 0
Tv 38,v4,26624
Tv 40,v4,32768
Mavv 180,34,96
Mavv 184,34,100
s_nop 0
Tv 42,v5,26624
Tv 44,v5,32768
Mavv 164,30,100
Mavv 168,30,104
Mavv 188,34,104
S_ s65,s61,184320
Mavv 192,34,108
S_ m0,s51,24576
Mavv 172,30,108
s_waitcnt vmcnt(6) lgkmcnt(0)
s_barrier
Mavv 176,30,112
Ln v7,32,s65,0
Mavv 196,34,112
Mavv 216,38,112
Dv 30,%0,6144
Mavv 236,42,112
Dv 34,v1,6144
Mavv 232,42,108
Ln v8,32,s65,3072
S_ m0,s51,25472
Mavv 212,38,108
Mavv 208,38,104
Ln v7,32,s65,128
Mavv 228,42,104
Mavv 224,42,100
Ln v8,32,s65,3200
S_ m0,s51,26368
Mavv 204,38,100
Mavv 200,38,96
Ln v7,32,s65,256
Mavv 220,42,96
.Lstep1_14:
Zvvv 76,92,116
Dv 38,%0,7168
Zvvv 80,92,120
Dv 42,v1,7168
Zvvv 84,92,124
Zvvv 88,92,128
Zvvv 92,92,132
W_ 2
Nvvv 96,30,136,56
Mvvv 96,34,156
Nvvv 100,30,140,60
s_nop 1
v_max3_f32 v10, v76, v80, v84
s_nop 0
v_max3_f32 v10, v10, v88, v92
Mvvv 100,34,160
v_cmp_lt_f32 vcc, 0x42000000, v10
s_cbranch_vccnz .Ltramp1_13
Nvvv 104,30,144,64
A_ v46,v46,v76
A_ v47,v47,v80
Mvvv 104,34,164
A_ v48,v48,v84
A_ v49,v49,v88
Nvvv 108,30,148,68
A_ v50,v50,v92
Mvvv 108,34,168
Ln v8,32,s65,3328
Nvvv 112,30,152,72
Mvvv 112,34,172
W_ 0
Mvvv 112,38,192
Dv 30,%0,8192
Mvvv 112,42,212
Dv 34,v1,8192
Mvvv 108,38,188
Mvvv 108,42,208
Mvvv 104,38,184
Mvvv 104,42,204
Mvvv 100,38,180
Mvvv 100,42,200
Mvvv 96,38,176
Mvvv 96,42,196
W_ 0
Mvvv 96,30,216
Dv 38,%0,0
Mvvv 96,34,236
Dv 42,v1,0
Mvvv 100,30,220
Mvvv 100,34,240
Mvvv 104,30,224
Mvvv 104,34,244
Mvvv 108,30,228
Mvvv 108,34,248
s_nop 0
E_ 96
Mvvv 112,30,232
E_ 97
Mvvv 112,34,252
W_ 0
E_ 98
Nvvv 76,38,136,56
Dv 30,%0,1024
Mvvv 76,42,156
Dv 34,v1,1024
Nvvv 80,38,140,60
E_ 99
Mvvv 80,42,160
C_ 99,98,99
C_ 98,96,97
Nvvv 84,38,144,64
E_ 100
Mvvv 84,42,164
E_ 101
Nvvv 88,38,148,68
E_ 102
Mvvv 88,42,168
E_ 103
Nvvv 92,38,152,72
C_ 103,102,103
C_ 102,100,101
Mvvv 92,42,172
W_ 0
E_ 104
Mvvv 92,30,192
Dv 38,%0,2048
Mvvv 92,34,212
Dv 42,v1,2048
Mvvv 88,30,188
E_ 105
Mvvv 88,34,208
E_ 106
Mvvv 84,30,184
S_ s65,s61,172032
E_ 107
Mvvv 84,34,204
S_ m0,s51,0
C_ 107,106,107
C_ 106,104,105
Mvvv 80,30,180
s_mov_b64 exec, 0
s_bitcmp1_b32 s4, 1
s_cbranch_scc1 .Lhop_o_b
s_bitcmp1_b32 s4, 0
s_cbranch_scc1 .Lpad_o_1
s_branch .Lpad_o_0
.Lhop_o_b:
s_bitcmp1_b32 s4, 0
s_cbranch_scc1 .Lpad_o_3
s_branch .Lpad_o_2
.Lhop_o_r:
s_mov_b64 exec, -1
s_nop 1
s_waitcnt vmcnt(6) lgkmcnt(0)
s_barrier
Mvvv 80,34,200
Ln v9,36,s65,0
Mvvv 76,30,176
E_ 108
Mvvv 76,34,196
Ln v9,36,s65,3072
S_ m0,s51,896
Mvvv 76,38,216
Tv 30,v2,12288
Tv 32,v2,18432
Mvvv 76,42,236
Ln v9,36,s65,128
Mvvv 80,38,220
s_nop 0
Tv 34,v3,12288
Tv 36,v3,18432
Mvvv 80,42,240
Ln v9,36,s65,3200
S_ m0,s51,1792
Mvvv 84,38,224
E_ 109
Mvvv 84,42,244
Ln v9,36,s65,256
Mvvv 88,38,228
E_ 110
Mvvv 88,42,248
Ln v9,36,s65,3328
Mvvv 92,38,232
E_ 111
Mvvv 92,42,252
W_ 0
C_ 111,110,111
C_ 110,108,109
Mavv 0,30,116
s_nop 0
Tv 38,v4,12288
Tv 40,v4,18432
Mavv 20,34,116
E_ 112
Mavv 24,34,120
s_nop 0
Tv 42,v5,12288
Tv 44,v5,18432
Mavv 4,30,120
E_ 113
Mavv 8,30,124
E_ 114
Mavv 28,34,124
E_ 115
Mavv 32,34,128
C_ 115,114,115
C_ 114,112,113
Mavv 12,30,128
E_ 76
Mavv 16,30,132
E_ 77
Mavv 36,34,132
W_ 0
E_ 78
Mavv 56,38,132
s_nop 0
Tv 30,v2,13312
Tv 32,v2,19456
Mavv 76,42,132
E_ 79
Mavv 72,42,128
s_nop 0
Tv 34,v3,13312
Tv 36,v3,19456
Mavv 52,38,128
C_ 96,76,77
C_ 97,78,79
Mavv 48,38,124
E_ 80
Mavv 68,42,124
E_ 81
Mavv 64,42,120
E_ 82
Mavv 44,38,120
E_ 83
Mavv 40,38,116
C_ 100,80,81
C_ 101,82,83
Mavv 60,42,116
W_ 0
E_ 84
Mavv 80,30,116
s_nop 0
Tv 38,v4,13312
Tv 40,v4,19456
Mavv 100,34,116
E_ 85
Mavv 104,34,120
s_nop 0
Tv 42,v5,13312
Tv 44,v5,19456
Mavv 84,30,120
E_ 86
Mavv 88,30,124
E_ 87
Mavv 108,34,124
C_ 104,84,85
C_ 105,86,87
Mavv 112,34,128
E_ 88
Mavv 92,30,128
E_ 89
Mavv 96,30,132
E_ 90
Mavv 116,34,132
W_ 0
E_ 91
Mavv 136,38,132
s_nop 0
Tv 30,v2,14336
Tv 32,v2,20480
Mavv 156,42,132
C_ 108,88,89
C_ 109,90,91
Mavv 152,42,128
s_nop 0
Tv 34,v3,14336
Tv 36,v3,20480
Mavv 132,38,128
E_ 92
Mavv 128,38,124
E_ 93
Mavv 148,42,124
E_ 94
Mavv 144,42,120
E_ 95
Mavv 124,38,120
C_ 112,92,93
C_ 113,94,95
Mavv 120,38,116
v_mov_b32 v92, 0x3f803f80
v_mov_b32 v93, 0x3f803f80
Mavv 140,42,116
W_ 0
v_mov_b32 v94, 0x3f803f80
v_mov_b32 v95, 0x3f803f80
Mavv 160,30,116
s_nop 0
Tv 38,v4,14336
Tv 40,v4,20480
Mavv 180,34,116
Mavv 184,34,120
s_nop 0
Tv 42,v5,14336
Tv 44,v5,20480
Mavv 164,30,120
Mavv 168,30,124
Mavv 188,34,124
S_ s65,s61,184320
Mavv 192,34,128
S_ m0,s51,12288
Mavv 172,30,128
s_waitcnt vmcnt(6) lgkmcnt(0)
s_barrier
Mavv 176,30,132
Ln v9,36,s65,0
Mavv 196,34,132
Mavv 216,38,132
Dv 30,%0,30720
Mavv 236,42,132
Dv 34,v1,30720
Mavv 232,42,128
Ln v9,36,s65,3072
S_ m0,s51,13184
Mavv 212,38,128
Mavv 208,38,124
Ln v9,36,s65,128
Mavv 228,42,124
Mavv 224,42,120
Ln v9,36,s65,3200
S_ m0,s51,14080
Mavv 204,38,120
Mavv 200,38,116
Ln v9,36,s65,256
Mavv 220,42,116
.Lstep1_15:
Zvvv 76,92,96
Dv 38,%0,31744
Zvvv 80,92,100
Dv 42,v1,31744
Zvvv 84,92,104
Zvvv 88,92,108
Zvvv 92,92,112
W_ 2
Nvvv 116,30,136,56
Mvvv 116,34,156
Nvvv 120,30,140,60
s_nop 1
v_max3_f32 v10, v76, v80, v84
s_nop 0
v_max3_f32 v10, v10, v88, v92
Mvvv 120,34,160
v_cmp_lt_f32 vcc, 0x42000000, v10
s_cbranch_vccnz .Ltramp1_14
Nvvv 124,30,144,64
A_ v46,v46,v76
A_ v47,v47,v80
Mvvv 124,34,164
A_ v48,v48,v84
A_ v49,v49,v88
Nvvv 128,30,148,68
A_ v50,v50,v92
Mvvv 128,34,168
Ln v9,36,s65,3328
Nvvv 132,30,152,72
Mvvv 132,34,172
W_ 0
Mvvv 132,38,192
Dv 30,%0,32768
Mvvv 132,42,212
Dv 34,v1,32768
Mvvv 128,38,188
Mvvv 128,42,208
Mvvv 124,38,184
Mvvv 124,42,204
Mvvv 120,38,180
Mvvv 120,42,200
Mvvv 116,38,176
Mvvv 116,42,196
W_ 0
Mvvv 116,30,216
Dv 38,%0,24576
Mvvv 116,34,236
Dv 42,v1,24576
Mvvv 120,30,220
Mvvv 120,34,240
Mvvv 124,30,224
Mvvv 124,34,244
Mvvv 128,30,228
Mvvv 128,34,248
s_nop 0
E_ 116
Mvvv 132,30,232
E_ 117
Mvvv 132,34,252
W_ 0
E_ 118
Nvvv 76,38,136,56
Dv 30,%0,25600
Mvvv 76,42,156
Dv 34,v1,25600
Nvvv 80,38,140,60
E_ 119
Mvvv 80,42,160
C_ 119,118,119
C_ 118,116,117
Nvvv 84,38,144,64
E_ 120
Mvvv 84,42,164
E_ 121
Nvvv 88,38,148,68
E_ 122
Mvvv 88,42,168
E_ 123
Nvvv 92,38,152,72
C_ 123,122,123
C_ 122,120,121
Mvvv 92,42,172
W_ 0
E_ 124
Mvvv 92,30,192
Dv 38,%0,26624
Mvvv 92,34,212
Dv 42,v1,26624
Mvvv 88,30,188
E_ 125
Mvvv 88,34,208
E_ 126
Mvvv 84,30,184
E_ 127
Mvvv 84,34,204
C_ 127,126,127
C_ 126,124,125
Mvvv 80,30,180
s_waitcnt vmcnt(6) lgkmcnt(0)
s_barrier
Mvvv 80,34,200
R_ v7,a248
Mvvv 76,30,176
v_lshrrev_b32 v7, 7, v7
Mvvv 76,34,196
v_mul_u32_u24 v7, 896, v7
Mvvv 76,38,216
s_nop 0
Tv 30,v2,0
Tv 32,v2,6144
Mvvv 76,42,236
v_add_u32 v7, v7, v9
Mvvv 80,38,220
S_ s71,s72,1024
Tv 34,v3,0
Tv 36,v3,6144
Mvvv 80,42,240
S_ s55,s71,491520
E_ 128
)A"
R"A(Mvvv 84,38,224
S_ m0,s51,24576
E_ 129
Mvvv 84,42,244
L_ v7,24,s55,0
Mvvv 88,38,228
S_ s55,s71,501760
E_ 130
Mvvv 88,42,248
S_ m0,s51,25600
E_ 131
Mvvv 92,38,232
L_ v7,24,s55,0
Mvvv 92,42,252
S_ s55,s71,512000
C_ 131,130,131
C_ 130,128,129
W_ 2
Mavv 0,30,96
S_ m0,s51,26624
Tv 38,v4,0
Tv 40,v4,6144
W_ 2
Mavv 20,34,96
L_ v7,24,s55,0
Mavv 24,34,100
S_ s55,s71,522240
Tv 42,v5,0
Tv 44,v5,6144
Mavv 4,30,100
S_ m0,s51,27648
E_ 132
Mavv 8,30,104
L_ v7,24,s55,0
Mavv 28,34,104
S_ s55,s71,532480
E_ 133
Mavv 32,34,108
S_ m0,s51,28672
E_ 134
Mavv 12,30,108
L_ v7,24,s55,0
Mavv 16,30,112
S_ s55,s71,542720
E_ 135
Mavv 36,34,112
S_ m0,s51,29696
C_ 135,134,135
C_ 134,132,133
W_ 2
Mavv 56,38,112
Tv 30,v2,1024
Tv 32,v2,7168
W_ 2
Mavv 76,42,112
L_ v7,24,s55,0
Mavv 72,42,108
Tv 34,v3,1024
Tv 36,v3,7168
Mavv 52,38,108
E_ 76
Mavv 48,38,104
E_ 77
Mavv 68,42,104
E_ 78
Mavv 64,42,100
E_ 79
Mavv 44,38,100
C_ 116,76,77
C_ 117,78,79
Mavv 40,38,96
E_ 80
Mavv 60,42,96
W_ 0
E_ 81
Mavv 80,30,96
s_nop 0
Tv 38,v4,1024
Tv 40,v4,7168
Mavv 100,34,96
E_ 82
Mavv 104,34,100
s_nop 0
Tv 42,v5,1024
Tv 44,v5,7168
Mavv 84,30,100
E_ 83
Mavv 88,30,104
C_ 120,80,81
C_ 121,82,83
Mavv 108,34,104
E_ 84
Mavv 112,34,108
E_ 85
Mavv 92,30,108
E_ 86
Mavv 96,30,112
E_ 87
Mavv 116,34,112
W_ 0
C_ 124,84,85
C_ 125,86,87
Mavv 136,38,112
s_nop 0
Tv 30,v2,2048
Tv 32,v2,8192
Mavv 156,42,112
E_ 88
Mavv 152,42,108
s_nop 0
Tv 34,v3,2048
Tv 36,v3,8192
Mavv 132,38,108
E_ 89
Mavv 128,38,104
E_ 90
Mavv 148,42,104
E_ 91
Mavv 144,42,100
C_ 128,88,89
C_ 129,90,91
Mavv 124,38,100
E_ 92
Mavv 120,38,96
E_ 93
Mavv 140,42,96
W_ 0
E_ 94
Mavv 160,30,96
s_nop 0
Tv 38,v4,2048
Tv 40,v4,8192
Mavv 180,34,96
E_ 95
Mavv 184,34,100
s_nop 0
Tv 42,v5,2048
Tv 44,v5,8192
Mavv 164,30,100
C_ 132,92,93
C_ 133,94,95
Mavv 168,30,104
v_mov_b32 v92, 0x3f803f80
v_mov_b32 v93, 0x3f803f80
Mavv 188,34,104
v_mov_b32 v94, 0x3f803f80
v_mov_b32 v95, 0x3f803f80
Mavv 192,34,108
Mavv 172,30,108
s_waitcnt vmcnt(6) lgkmcnt(0)
s_barrier
Mavv 176,30,112
S_ s55,s71,614400
Mavv 196,34,112
S_ m0,s51,0
Mavv 216,38,112
s_nop 0
Tv 30,v2,12288
Tv 32,v2,18432
Zvvv 76,92,116
L_ v7,24,s55,0
Zvvv 80,92,120
S_ s55,s71,624640
Tv 34,v3,12288
Tv 36,v3,18432
Zvvv 84,92,124
S_ m0,s51,1024
R_ v11,a248
Zvvv 88,92,128
L_ v7,24,s55,0
Zvvv 92,92,132
S_ s55,s71,634880
v_mul_u32_u24 v11, 80, v11
Mavv 236,42,112
S_ m0,s51,2048
v_min_u32 v11, 29440, v11
Mavv 232,42,108
L_ v7,24,s55,0
Mavv 212,38,108
S_ s55,s71,645120
S_ s85,s87,1024
Mavv 208,38,104
S_ m0,s51,3072
v_max3_f32 v10, v76, v80, v84
Mavv 228,42,104
v_max3_f32 v10, v10, v88, v92
v_cmp_lt_f32 vcc, 0x42000000, v10
Mavv 224,42,100
L_ v7,24,s55,0
Mavv 204,38,100
S_ s55,s71,655360
Mavv 200,38,96
buffer_load_dword a254, v11, s[24:27], s85 offen
Mavv 220,42,96
S_ m0,s51,4096
S_ s85,s87,1152
buffer_load_dword a254, v11, s[24:27], s85 offen
s_cbranch_vccnz .Ltramp1_15
.Ljoin1:
W_ 2
Mavv 0,30,116
s_nop 0
Tv 38,v4,12288
Tv 40,v4,18432
W_ 2
Mavv 20,34,116
L_ v7,24,s55,0
Mavv 24,34,120
S_ s55,s71,665600
Tv 42,v5,12288
Tv 44,v5,18432
Mavv 4,30,120
S_ m0,s51,5120
R_ v13,a248
S_ s53,s51,36864
Mavv 8,30,124
L_ v7,24,s55,0
Mavv 28,34,124
v_add_u32 v12, s53, v13
s_mul_i32 s53, s4, 10240
Mavv 32,34,128
v_add_u32 v13, s53, v13
R_ v10,a248
Mavv 12,30,128
buffer_load_dword v11, off, s[92:95], s96 offset:0 sc1
Mavv 16,30,132
A_ v46,v46,v76
A_ v47,v47,v80
Mavv 36,34,132
W_ 0
A_ v48,v48,v84
A_ v49,v49,v88
Mavv 56,38,132
s_nop 0
Tv 30,v2,13312
Tv 32,v2,19456
Mavv 76,42,132
A_ v50,v50,v92
Mavv 72,42,128
s_nop 0
Tv 34,v3,13312
Tv 36,v3,19456
Mavv 52,38,128
v_rcp_f32 v46, v46
Mavv 48,38,124
v_rcp_f32 v47, v47
Mavv 68,42,124
v_rcp_f32 v48, v48
Mavv 64,42,120
v_rcp_f32 v49, v49
Mavv 44,38,120
v_rcp_f32 v50, v50
Mavv 40,38,116
R_ v96,a0
R_ v97,a1
Mavv 60,42,116
W_ 0
R_ v98,a2
R_ v99,a3
Mavv 80,30,116
s_nop 0
Tv 38,v4,13312
Tv 40,v4,19456
Mavv 100,34,116
R_ v100,a20
R_ v101,a21
Mavv 104,34,120
s_nop 0
Tv 42,v5,13312
Tv 44,v5,19456
Mavv 84,30,120
R_ v102,a22
R_ v103,a23
Mavv 88,30,124
U_ v96,v96,v46
U_ v97,v97,v46
Mavv 108,34,124
U_ v98,v98,v46
U_ v99,v99,v46
Mavv 112,34,128
U_ v100,v100,v46
U_ v101,v101,v46
Mavv 92,30,128
U_ v102,v102,v46
U_ v103,v103,v46
Mavv 96,30,132
C_ 136,96,97
C_ 137,98,99
Mavv 116,34,132
W_ 0
C_ 138,100,101
C_ 139,102,103
Mavv 136,38,132
s_nop 0
Tv 30,v2,14336
Tv 32,v2,20480
Mavv 156,42,132
Da 0,v13,0
Mavv 152,42,128
s_nop 0
Tv 34,v3,14336
Tv 36,v3,20480
Mavv 132,38,128
R_ v104,a4
R_ v105,a5
Mavv 128,38,124
R_ v106,a6
R_ v107,a7
Mavv 148,42,124
R_ v108,a24
R_ v109,a25
Mavv 144,42,120
R_ v110,a26
R_ v111,a27
Mavv 124,38,120
U_ v104,v104,v47
U_ v105,v105,v47
Mavv 120,38,116
U_ v106,v106,v47
U_ v107,v107,v47
Mavv 140,42,116
W_ 0
U_ v108,v108,v47
U_ v109,v109,v47
Mavv 160,30,116
s_nop 0
Tv 38,v4,14336
Tv 40,v4,20480
Mavv 180,34,116
U_ v110,v110,v47
U_ v111,v111,v47
Mavv 184,34,120
s_nop 0
Tv 42,v5,14336
Tv 44,v5,20480
Mavv 164,30,120
C_ 140,104,105
C_ 141,106,107
Mavv 168,30,124
C_ 142,108,109
C_ 143,110,111
Mavv 188,34,124
Da 4,v13,1024
Mavv 192,34,128
R_ v96,a8
R_ v97,a9
Mavv 172,30,128
S_ s55,s71,0
R_ v98,a10
R_ v99,a11
Mavv 176,30,132
S_ m0,s51,12288
R_ v100,a28
R_ v101,a29
Mavv 196,34,132
s_waitcnt vmcnt(63) lgkmcnt(0)
s_barrier
R_ v102,a30
Mavv 216,38,132
L_ v7,24,s55,0
S_ s55,s71,10240
S_ m0,s51,13312
Mavv 236,42,132
R_ v103,a31
Mavv 232,42,128
L_ v7,24,s55,0
S_ s55,s71,20480
S_ m0,s51,14336
Mavv 212,38,128
U_ v96,v96,v48
U_ v97,v97,v48
Mavv 208,38,124
L_ v7,24,s55,0
S_ s55,s71,30720
S_ m0,s51,15360
Mavv 228,42,124
U_ v98,v98,v48
U_ v99,v99,v48
Mavv 224,42,120
L_ v7,24,s55,0
S_ s55,s71,40960
S_ m0,s51,16384
Mavv 204,38,120
U_ v100,v100,v48
U_ v101,v101,v48
Mavv 200,38,116
L_ v7,24,s55,0
S_ s55,s71,51200
S_ m0,s51,17408
Mavv 220,42,116
U_ v102,v102,v48
U_ v103,v103,v48
C_ 144,96,97
C_ 145,98,99
C_ 146,100,101
C_ 147,102,103
Da 8,v13,2048
L_ v7,24,s55,0
.p2align 3
R_ v104,a12
R_ v105,a13
R_ v106,a14
R_ v107,a15
R_ v108,a32
R_ v109,a33
R_ v110,a34
R_ v111,a35
v_pk_mul_f32 v[104:105], v[104:105], v[48:49] op_sel:[0,1]
v_pk_mul_f32 v[106:107], v[106:107], v[48:49] op_sel:[0,1]
v_pk_mul_f32 v[108:109], v[108:109], v[48:49] op_sel:[0,1]
v_pk_mul_f32 v[110:111], v[110:111], v[48:49] op_sel:[0,1]
C_ 148,104,105
C_ 149,106,107
C_ 150,108,109
C_ 151,110,111
Da 12,v13,3072
.p2align 3
R_ v96,a16
R_ v97,a17
R_ v98,a18
R_ v99,a19
R_ v100,a36
R_ v101,a37
R_ v102,a38
R_ v103,a39
v_pk_mul_f32 v[96:97], v[96:97], v[50:51] op_sel_hi:[1,0]
v_pk_mul_f32 v[98:99], v[98:99], v[50:51] op_sel_hi:[1,0]
v_pk_mul_f32 v[100:101], v[100:101], v[50:51] op_sel_hi:[1,0]
v_pk_mul_f32 v[102:103], v[102:103], v[50:51] op_sel_hi:[1,0]
C_ 152,96,97
C_ 153,98,99
C_ 154,100,101
C_ 155,102,103
Da 16,v13,4096
.p2align 3
R_ v104,a40
R_ v105,a41
R_ v106,a42
R_ v107,a43
R_ v108,a60
R_ v109,a61
R_ v110,a62
R_ v111,a63
v_pk_mul_f32 v[104:105], v[104:105], v[46:47] op_sel_hi:[1,0]
v_pk_mul_f32 v[106:107], v[106:107], v[46:47] op_sel_hi:[1,0]
v_pk_mul_f32 v[108:109], v[108:109], v[46:47] op_sel_hi:[1,0]
v_pk_mul_f32 v[110:111], v[110:111], v[46:47] op_sel_hi:[1,0]
C_ 156,104,105
C_ 157,106,107
C_ 158,108,109
C_ 159,110,111
Da 40,v13,5120
.p2align 3
R_ v96,a44
R_ v97,a45
R_ v98,a46
R_ v99,a47
R_ v100,a64
R_ v101,a65
R_ v102,a66
R_ v103,a67
v_pk_mul_f32 v[96:97], v[96:97], v[46:47] op_sel:[0,1]
v_pk_mul_f32 v[98:99], v[98:99], v[46:47] op_sel:[0,1]
v_pk_mul_f32 v[100:101], v[100:101], v[46:47] op_sel:[0,1]
v_pk_mul_f32 v[102:103], v[102:103], v[46:47] op_sel:[0,1]
C_ 160,96,97
C_ 161,98,99
C_ 162,100,101
C_ 163,102,103
Da 44,v13,6144
.p2align 3
R_ v104,a48
R_ v105,a49
R_ v106,a50
R_ v107,a51
R_ v108,a68
R_ v109,a69
R_ v110,a70
R_ v111,a71
v_pk_mul_f32 v[104:105], v[104:105], v[48:49] op_sel_hi:[1,0]
v_pk_mul_f32 v[106:107], v[106:107], v[48:49] op_sel_hi:[1,0]
v_pk_mul_f32 v[108:109], v[108:109], v[48:49] op_sel_hi:[1,0]
v_pk_mul_f32 v[110:111], v[110:111], v[48:49] op_sel_hi:[1,0]
C_ 164,104,105
C_ 165,106,107
C_ 166,108,109
C_ 167,110,111
Da 48,v13,7168
.p2align 3
R_ v96,a52
R_ v97,a53
R_ v98,a54
R_ v99,a55
R_ v100,a72
R_ v101,a73
R_ v102,a74
R_ v103,a75
v_pk_mul_f32 v[96:97], v[96:97], v[48:49] op_sel:[0,1]
v_pk_mul_f32 v[98:99], v[98:99], v[48:49] op_sel:[0,1]
v_pk_mul_f32 v[100:101], v[100:101], v[48:49] op_sel:[0,1]
v_pk_mul_f32 v[102:103], v[102:103], v[48:49] op_sel:[0,1]
C_ 168,96,97
C_ 169,98,99
C_ 170,100,101
C_ 171,102,103
Da 52,v13,8192
.p2align 3
R_ v104,a56
R_ v105,a57
R_ v106,a58
R_ v107,a59
R_ v108,a76
R_ v109,a77
R_ v110,a78
R_ v111,a79
v_pk_mul_f32 v[104:105], v[104:105], v[50:51] op_sel_hi:[1,0]
v_pk_mul_f32 v[106:107], v[106:107], v[50:51] op_sel_hi:[1,0]
v_pk_mul_f32 v[108:109], v[108:109], v[50:51] op_sel_hi:[1,0]
v_pk_mul_f32 v[110:111], v[110:111], v[50:51] op_sel_hi:[1,0]
C_ 172,104,105
C_ 173,106,107
C_ 174,108,109
C_ 175,110,111
Da 56,v13,9216
.p2align 3
R_ v96,a80
R_ v97,a81
R_ v98,a82
R_ v99,a83
R_ v100,a100
R_ v101,a101
R_ v102,a102
R_ v103,a103
v_pk_mul_f32 v[96:97], v[96:97], v[46:47] op_sel_hi:[1,0]
v_pk_mul_f32 v[98:99], v[98:99], v[46:47] op_sel_hi:[1,0]
v_pk_mul_f32 v[100:101], v[100:101], v[46:47] op_sel_hi:[1,0]
v_pk_mul_f32 v[102:103], v[102:103], v[46:47] op_sel_hi:[1,0]
C_ 176,96,97
C_ 177,98,99
C_ 178,100,101
C_ 179,102,103
X_ a80,v20
X_ a81,v21
X_ a82,v22
X_ a83,v23
.p2align 3
R_ v104,a84
R_ v105,a85
R_ v106,a86
R_ v107,a87
R_ v108,a104
R_ v109,a105
R_ v110,a106
R_ v111,a107
v_pk_mul_f32 v[104:105], v[104:105], v[46:47] op_sel:[0,1]
v_pk_mul_f32 v[106:107], v[106:107], v[46:47] op_sel:[0,1]
v_pk_mul_f32 v[108:109], v[108:109], v[46:47] op_sel:[0,1]
v_pk_mul_f32 v[110:111], v[110:111], v[46:47] op_sel:[0,1]
C_ 180,104,105
C_ 181,106,107
C_ 182,108,109
C_ 183,110,111
X_ a84,v24
X_ a85,v25
X_ a86,v26
X_ a87,v27
.p2align 3
R_ v96,a88
R_ v97,a89
R_ v98,a90
R_ v99,a91
R_ v100,a108
R_ v101,a109
R_ v102,a110
R_ v103,a111
v_pk_mul_f32 v[96:97], v[96:97], v[48:49] op_sel_hi:[1,0]
v_pk_mul_f32 v[98:99], v[98:99], v[48:49] op_sel_hi:[1,0]
v_pk_mul_f32 v[100:101], v[100:101], v[48:49] op_sel_hi:[1,0]
v_pk_mul_f32 v[102:103], v[102:103], v[48:49] op_sel_hi:[1,0]
C_ 184,96,97
C_ 185,98,99
C_ 186,100,101
C_ 187,102,103
X_ a88,v52
X_ a89,v53
X_ a90,v54
X_ a91,v55
.p2align 3
R_ v104,a92
R_ v105,a93
R_ v106,a94
R_ v107,a95
R_ v108,a112
R_ v109,a113
R_ v110,a114
R_ v111,a115
v_pk_mul_f32 v[104:105], v[104:105], v[48:49] op_sel:[0,1]
v_pk_mul_f32 v[106:107], v[106:107], v[48:49] op_sel:[0,1]
v_pk_mul_f32 v[108:109], v[108:109], v[48:49] op_sel:[0,1]
v_pk_mul_f32 v[110:111], v[110:111], v[48:49] op_sel:[0,1]
C_ 188,104,105
C_ 189,106,107
C_ 190,108,109
C_ 191,110,111
.p2align 3
R_ v96,a96
R_ v97,a97
R_ v98,a98
R_ v99,a99
R_ v100,a116
R_ v101,a117
R_ v102,a118
R_ v103,a119
v_pk_mul_f32 v[96:97], v[96:97], v[50:51] op_sel_hi:[1,0]
v_pk_mul_f32 v[98:99], v[98:99], v[50:51] op_sel_hi:[1,0]
v_pk_mul_f32 v[100:101], v[100:101], v[50:51] op_sel_hi:[1,0]
v_pk_mul_f32 v[102:103], v[102:103], v[50:51] op_sel_hi:[1,0]
C_ 192,96,97
C_ 193,98,99
C_ 194,100,101
C_ 195,102,103
.p2align 3
R_ v104,a120
R_ v105,a121
R_ v106,a122
R_ v107,a123
R_ v108,a140
R_ v109,a141
R_ v110,a142
R_ v111,a143
v_pk_mul_f32 v[104:105], v[104:105], v[46:47] op_sel_hi:[1,0]
v_pk_mul_f32 v[106:107], v[106:107], v[46:47] op_sel_hi:[1,0]
v_pk_mul_f32 v[108:109], v[108:109], v[46:47] op_sel_hi:[1,0]
v_pk_mul_f32 v[110:111], v[110:111], v[46:47] op_sel_hi:[1,0]
C_ 196,104,105
C_ 197,106,107
C_ 198,108,109
C_ 199,110,111
X_ a120,v16
X_ a121,v17
X_ a122,v18
X_ a123,v19
.p2align 3
R_ v96,a124
R_ v97,a125
R_ v98,a126
R_ v99,a127
R_ v100,a144
R_ v101,a145
R_ v102,a146
R_ v103,a147
v_pk_mul_f32 v[96:97], v[96:97], v[46:47] op_sel:[0,1]
v_pk_mul_f32 v[98:99], v[98:99], v[46:47] op_sel:[0,1]
v_pk_mul_f32 v[100:101], v[100:101], v[46:47] op_sel:[0,1]
v_pk_mul_f32 v[102:103], v[102:103], v[46:47] op_sel:[0,1]
C_ 200,96,97
C_ 201,98,99
C_ 202,100,101
C_ 203,102,103
X_ a124,v28
X_ a125,v29
X_ a126,v51
X_ a127,v6
.p2align 3
R_ v104,a128
R_ v105,a129
R_ v106,a130
R_ v107,a131
R_ v108,a148
R_ v109,a149
R_ v110,a150
R_ v111,a151
v_pk_mul_f32 v[104:105], v[104:105], v[48:49] op_sel_hi:[1,0]
v_pk_mul_f32 v[106:107], v[106:107], v[48:49] op_sel_hi:[1,0]
v_pk_mul_f32 v[108:109], v[108:109], v[48:49] op_sel_hi:[1,0]
v_pk_mul_f32 v[110:111], v[110:111], v[48:49] op_sel_hi:[1,0]
C_ 204,104,105
C_ 205,106,107
C_ 206,108,109
C_ 207,110,111
X_ a128,v14
X_ a129,v15
v_accvgpr_mov_b32 a130, a249
v_accvgpr_mov_b32 a131, a250
.p2align 3
R_ v96,a132
R_ v97,a133
R_ v98,a134
R_ v99,a135
R_ v100,a152
R_ v101,a153
R_ v102,a154
R_ v103,a155
v_pk_mul_f32 v[96:97], v[96:97], v[48:49] op_sel:[0,1]
v_pk_mul_f32 v[98:99], v[98:99], v[48:49] op_sel:[0,1]
v_pk_mul_f32 v[100:101], v[100:101], v[48:49] op_sel:[0,1]
v_pk_mul_f32 v[102:103], v[102:103], v[48:49] op_sel:[0,1]
C_ 208,96,97
C_ 209,98,99
C_ 210,100,101
C_ 211,102,103
Da 132,v12,0
.p2align 3
R_ v104,a136
R_ v105,a137
R_ v106,a138
R_ v107,a139
R_ v108,a156
R_ v109,a157
R_ v110,a158
R_ v111,a159
v_pk_mul_f32 v[104:105], v[104:105], v[50:51] op_sel_hi:[1,0]
v_pk_mul_f32 v[106:107], v[106:107], v[50:51] op_sel_hi:[1,0]
v_pk_mul_f32 v[108:109], v[108:109], v[50:51] op_sel_hi:[1,0]
v_pk_mul_f32 v[110:111], v[110:111], v[50:51] op_sel_hi:[1,0]
C_ 212,104,105
C_ 213,106,107
C_ 214,108,109
C_ 215,110,111
Da 136,v12,1024
.p2align 3
R_ v96,a160
R_ v97,a161
R_ v98,a162
R_ v99,a163
R_ v100,a180
R_ v101,a181
R_ v102,a182
R_ v103,a183
v_pk_mul_f32 v[96:97], v[96:97], v[46:47] op_sel_hi:[1,0]
v_pk_mul_f32 v[98:99], v[98:99], v[46:47] op_sel_hi:[1,0]
v_pk_mul_f32 v[100:101], v[100:101], v[46:47] op_sel_hi:[1,0]
v_pk_mul_f32 v[102:103], v[102:103], v[46:47] op_sel_hi:[1,0]
C_ 216,96,97
C_ 217,98,99
C_ 218,100,101
C_ 219,102,103
Da 160,v12,2048
.p2align 3
R_ v104,a164
R_ v105,a165
R_ v106,a166
R_ v107,a167
R_ v108,a184
R_ v109,a185
R_ v110,a186
R_ v111,a187
v_pk_mul_f32 v[104:105], v[104:105], v[46:47] op_sel:[0,1]
v_pk_mul_f32 v[106:107], v[106:107], v[46:47] op_sel:[0,1]
v_pk_mul_f32 v[108:109], v[108:109], v[46:47] op_sel:[0,1]
v_pk_mul_f32 v[110:111], v[110:111], v[46:47] op_sel:[0,1]
C_ 220,104,105
.Lpad_o_0:
s_cbranch_execz .Lpad_o_4
C_ 221,106,107
C_ 222,108,109
C_ 223,110,111
Da 164,v12,3072
.p2align 3
R_ v96,a168
R_ v97,a169
R_ v98,a170
R_ v99,a171
R_ v100,a188
R_ v101,a189
R_ v102,a190
R_ v103,a191
v_pk_mul_f32 v[96:97], v[96:97], v[48:49] op_sel_hi:[1,0]
v_pk_mul_f32 v[98:99], v[98:99], v[48:49] op_sel_hi:[1,0]
v_pk_mul_f32 v[100:101], v[100:101], v[48:49] op_sel_hi:[1,0]
v_pk_mul_f32 v[102:103], v[102:103], v[48:49] op_sel_hi:[1,0]
C_ 224,96,97
C_ 225,98,99
C_ 226,100,101
C_ 227,102,103
Da 168,v12,4096
.p2align 3
R_ v104,a172
R_ v105,a173
R_ v106,a174
R_ v107,a175
R_ v108,a192
R_ v109,a193
R_ v110,a194
R_ v111,a195
v_pk_mul_f32 v[104:105], v[104:105], v[48:49] op_sel:[0,1]
v_pk_mul_f32 v[106:107], v[106:107], v[48:49] op_sel:[0,1]
v_pk_mul_f32 v[108:109], v[108:109], v[48:49] op_sel:[0,1]
v_pk_mul_f32 v[110:111], v[110:111], v[48:49] op_sel:[0,1]
C_ 228,104,105
C_ 229,106,107
C_ 230,108,109
C_ 231,110,111
Da 172,v12,5120
.p2align 3
R_ v96,a176
R_ v97,a177
R_ v98,a178
R_ v99,a179
R_ v100,a196
R_ v101,a197
R_ v102,a198
R_ v103,a199
v_pk_mul_f32 v[96:97], v[96:97], v[50:51] op_sel_hi:[1,0]
v_pk_mul_f32 v[98:99], v[98:99], v[50:51] op_sel_hi:[1,0]
v_pk_mul_f32 v[100:101], v[100:101], v[50:51] op_sel_hi:[1,0]
v_pk_mul_f32 v[102:103], v[102:103], v[50:51] op_sel_hi:[1,0]
C_ 232,96,97
C_ 233,98,99
C_ 234,100,101
C_ 235,102,103
Da 176,v12,12288
.p2align 3
R_ v104,a200
R_ v105,a201
R_ v106,a202
R_ v107,a203
R_ v108,a220
R_ v109,a221
R_ v110,a222
R_ v111,a223
v_pk_mul_f32 v[104:105], v[104:105], v[46:47] op_sel_hi:[1,0]
v_pk_mul_f32 v[106:107], v[106:107], v[46:47] op_sel_hi:[1,0]
v_pk_mul_f32 v[108:109], v[108:109], v[46:47] op_sel_hi:[1,0]
v_pk_mul_f32 v[110:111], v[110:111], v[46:47] op_sel_hi:[1,0]
C_ 236,104,105
C_ 237,106,107
C_ 238,108,109
C_ 239,110,111
Da 200,v12,13312
.p2align 3
R_ v96,a204
R_ v97,a205
R_ v98,a206
R_ v99,a207
R_ v100,a224
R_ v101,a225
R_ v102,a226
R_ v103,a227
v_pk_mul_f32 v[96:97], v[96:97], v[46:47] op_sel:[0,1]
v_pk_mul_f32 v[98:99], v[98:99], v[46:47] op_sel:[0,1]
v_pk_mul_f32 v[100:101], v[100:101], v[46:47] op_sel:[0,1]
v_pk_mul_f32 v[102:103], v[102:103], v[46:47] op_sel:[0,1]
C_ 240,96,97
C_ 241,98,99
C_ 242,100,101
C_ 243,102,103
Da 204,v12,14336
v_mov_b32 v6, v10
v_lshrrev_b32 v10, 9, v6
v_lshlrev_b32 v10, 11, v10
v_sub_u32 %0, v2, v10
v_xor_b32 %0, s83, %0
s_waitcnt vmcnt(15) lgkmcnt(0)
s_barrier
Ta 180,%0,24576
Ta 182,%0,26624
Ta 184,%0,28672
Ta 186,%0,30720
Ta 188,%0,32768
Ta 190,%0,34816
.p2align 3
R_ v104,a208
R_ v105,a209
R_ v106,a210
R_ v107,a211
R_ v108,a228
R_ v109,a229
R_ v110,a230
R_ v111,a231
v_pk_mul_f32 v[104:105], v[104:105], v[48:49] op_sel_hi:[1,0]
v_pk_mul_f32 v[106:107], v[106:107], v[48:49] op_sel_hi:[1,0]
v_pk_mul_f32 v[108:109], v[108:109], v[48:49] op_sel_hi:[1,0]
v_pk_mul_f32 v[110:111], v[110:111], v[48:49] op_sel_hi:[1,0]
C_ 244,104,105
C_ 245,106,107
C_ 246,108,109
C_ 247,110,111
Da 208,v12,15360
.p2align 3
R_ v96,a212
R_ v97,a213
R_ v98,a214
R_ v99,a215
R_ v100,a232
R_ v101,a233
R_ v102,a234
R_ v103,a235
v_pk_mul_f32 v[96:97], v[96:97], v[48:49] op_sel:[0,1]
v_pk_mul_f32 v[98:99], v[98:99], v[48:49] op_sel:[0,1]
v_pk_mul_f32 v[100:101], v[100:101], v[48:49] op_sel:[0,1]
v_pk_mul_f32 v[102:103], v[102:103], v[48:49] op_sel:[0,1]
C_ 248,96,97
C_ 249,98,99
C_ 250,100,101
C_ 251,102,103
Da 212,v12,16384
.p2align 3
R_ v104,a216
R_ v105,a217
R_ v106,a218
R_ v107,a219
R_ v108,a236
R_ v109,a237
R_ v110,a238
R_ v111,a239
v_pk_mul_f32 v[104:105], v[104:105], v[50:51] op_sel_hi:[1,0]
v_pk_mul_f32 v[106:107], v[106:107], v[50:51] op_sel_hi:[1,0]
v_pk_mul_f32 v[108:109], v[108:109], v[50:51] op_sel_hi:[1,0]
v_pk_mul_f32 v[110:111], v[110:111], v[50:51] op_sel_hi:[1,0]
C_ 252,104,105
C_ 253,106,107
C_ 254,108,109
C_ 255,110,111
Da 216,v12,17408
v_xor_b32 v1, 32, %0
W_ 7
Zvav 56,180,136
s_mul_i32 s76, s6, 204800
Ta 192,v1,24576
Ta 194,v1,26624
W_ 7
Mvav 56,184,156
v_xor_b32 v2, 64, %0
v_xor_b32 v3, 96, %0
W_ 5
Mvav 56,188,176
s_nop 0
Ta 196,v1,28672
Ta 198,v1,30720
Zvav 60,180,140
W_ 4
R_ v4,a248
v_mul_u32_u24 v4, 80, v4
Mvav 60,184,160
S_ s55,s71,122880
Ta 220,v1,32768
Ta 222,v1,34816
Mvav 60,188,180
S_ m0,s51,49152
v_min_u32 v4, 29440, v4
s_waitcnt vmcnt(6)
Zvav 64,180,144
v_readfirstlane_b32 s62, v11
v_lshrrev_b32 v8, 2, v6
Mvav 64,184,164
v_add_u32 v5, s75, v8
R_ v9,a251
Mvav 64,188,184
v_or_b32 v9, s73, v9
v_lshrrev_b32 v10, 8, v6
Zvav 68,180,148
v_bfe_u32 v12, v6, 6, 2
v_cmp_eq_u32 vcc, v10, v12
Mvav 68,184,168
v_and_b32 v10, 16, v6
v_mov_b32 v12, 0x3f80
Mvav 68,188,188
v_lshlrev_b32 v12, v10, v12
v_cndmask_b32 v12, 0, v12, vcc
Zvav 72,180,152
v_and_b32 v10, 32, v6
v_cmp_eq_u32 vcc, 0, v10
Mvav 72,184,172
X_ a68,0
X_ a69,0
Mvav 72,188,192
W_ 0
X_ a72,0
X_ a73,0
Zvav 92,220,192
s_nop 0
Ta 180,v2,24576
Ta 182,v2,26624
Mvav 92,196,172
v_cndmask_b32 v10, 0, v12, vcc
v_sub_u32 v12, v12, v10
Mvav 92,192,152
s_nop 0
Ta 184,v2,28672
Ta 186,v2,30720
Zvav 88,220,188
X_ a70,v10
X_ a71,v12
Mvav 88,196,168
s_mov_b64 s[32:33], s[20:21]
Ta 188,v2,32768
Ta 190,v2,34816
Mvav 88,192,148
s_mov_b32 s34, 409600
S_ s85,s6,1
Zvav 84,220,184
L_ v7,24,s55,0
Mvav 84,196,164
S_ s55,s71,133120
s_and_b32 s85, s85, 7
Mvav 84,192,144
S_ m0,s51,50176
s_lshl_b32 s85, s85, 7
Zvav 80,220,180
S_ s85,s85,s87
Mvav 80,196,160
L_ v7,24,s55,0
Mvav 80,192,140
S_ s55,s71,143360
Zvav 76,220,176
S_ m0,s51,51200
Mvav 76,196,156
Mvav 76,192,136
W_ 0
Zvav 16,180,136
s_nop 0
Ta 192,v3,24576
Ta 194,v3,26624
Mvav 16,184,156
L_ v7,24,s55,0
Mvav 16,188,176
S_ s55,s71,153600
Ta 196,v3,28672
Ta 198,v3,30720
Zvav 20,180,140
S_ m0,s51,52224
Mvav 20,184,160
s_nop 0
Ta 220,v3,32768
Ta 222,v3,34816
Mvav 20,188,180
L_ v7,24,s55,0
Zvav 24,180,144
S_ s55,s71,163840
Mvav 24,184,164
S_ m0,s51,53248
Mvav 24,188,184
Zvav 28,180,148
L_ v7,24,s55,0
Mvav 28,184,168
S_ s55,s71,174080
Mvav 28,188,188
S_ m0,s51,54272
Zvav 32,180,152
Mvav 32,184,172
L_ v7,24,s55,0
Mvav 32,188,192
s_waitcnt vmcnt(13) lgkmcnt(0)
s_barrier
S_ s71,s72,1152
Zvav 52,220,192
S_ s55,s71,491520
Ta 180,%0,0
Ta 182,%0,2048
Mvav 52,196,172
S_ m0,s51,36864
Mvav 52,192,152
s_nop 0
Ta 184,%0,4096
Ta 186,%0,6144
Zvav 48,220,188
L_ v7,24,s55,0
Mvav 48,196,168
S_ s55,s71,501760
Ta 188,%0,8192
Ta 190,%0,10240
Mvav 48,192,148
S_ m0,s51,37888
Zvav 44,220,184
Mvav 44,196,164
L_ v7,24,s55,0
Mvav 44,192,144
S_ s55,s71,512000
Zvav 40,220,180
S_ m0,s51,38912
Mvav 40,196,160
Mvav 40,192,140
L_ v7,24,s55,0
Zvav 36,220,176
S_ s55,s71,522240
Mvav 36,196,156
S_ m0,s51,39936
Mvav 36,192,136
W_ 0
Mvav 56,180,196
s_nop 0
Ta 192,v1,0
Ta 194,v1,2048
Mvav 56,184,216
L_ v7,24,s55,0
Mvav 56,188,236
S_ s55,s71,532480
Ta 196,v1,4096
Ta 198,v1,6144
Mvav 60,180,200
S_ m0,s51,40960
Mvav 60,184,220
s_nop 0
Ta 220,v1,8192
Ta 222,v1,10240
Mvav 60,188,240
L_ v7,24,s55,0
Mvav 64,180,204
S_ s55,s71,542720
Mvav 64,184,224
S_ m0,s51,41984
Mvav 64,188,244
Mvav 68,180,208
L_ v7,24,s55,0
Mvav 68,184,228
S_ s55,s71,614400
Mvav 68,188,248
S_ m0,s51,24576
Mvav 72,180,212
Mvav 72,184,232
L_ v7,24,s55,0
Mvav 72,188,252
W_ 0
S_ s55,s71,624640
Mvav 92,220,252
S_ m0,s51,25600
Ta 180,v2,0
Ta 182,v2,2048
Mvav 92,196,232
Mvav 92,192,212
s_nop 0
Ta 184,v2,4096
Ta 186,v2,6144
Mvav 88,220,248
L_ v7,24,s55,0
Mvav 88,196,228
S_ s55,s71,634880
Ta 188,v2,8192
Ta 190,v2,10240
Mvav 88,192,208
S_ m0,s51,26624
Mvav 84,220,244
Mvav 84,196,224
L_ v7,24,s55,0
Mvav 84,192,204
S_ s55,s71,645120
Mvav 80,220,240
S_ m0,s51,27648
Mvav 80,196,220
Mvav 80,192,200
L_ v7,24,s55,0
Mvav 76,220,236
S_ s55,s71,655360
Mvav 76,196,216
S_ m0,s51,28672
Mvav 76,192,196
W_ 0
Mvav 16,180,196
s_nop 0
Ta 192,v3,0
Ta 194,v3,2048
Mvav 16,184,216
L_ v7,24,s55,0
Mvav 16,188,236
S_ s55,s71,665600
Ta 196,v3,4096
Ta 198,v3,6144
Mvav 20,180,200
S_ m0,s51,29696
Mvav 20,184,220
s_nop 0
Ta 220,v3,8192
Ta 222,v3,10240
Mvav 20,188,240
L_ v7,24,s55,0
Mvav 24,180,204
Mvav 24,184,224
Mvav 24,188,244
Mvav 28,180,208
buffer_load_dword a74, v4, s[24:27], s85 offen
Mvav 28,184,228
S_ s85,s6,2
Mvav 28,188,248
s_and_b32 s85, s85, 7
Mvav 32,180,212
s_lshl_b32 s85, s85, 7
Mvav 32,184,232
S_ s85,s85,s87
Mvav 32,188,252
s_waitcnt vmcnt(19) lgkmcnt(0)
s_barrier
Mvav 52,220,252
S_ s55,s71,0
Ta 180,%0,12288
Ta 182,%0,14336
Mvav 52,196,232
buffer_load_dword a74, v4, s[24:27], s85 offen
Mvav 52,192,212
S_ m0,s51,0
Ta 184,%0,16384
Ta 186,%0,18432
Mvav 48,220,248
Mvav 48,196,228
s_nop 0
Ta 188,%0,20480
Ta 190,%0,22528
Mvav 48,192,208
L_ v7,24,s55,0
Mvav 44,220,244
S_ s55,s71,10240
Mvav 44,196,224
S_ m0,s51,1024
Mvav 44,192,204
Mvav 40,220,240
L_ v7,24,s55,0
Mvav 40,196,220
S_ s55,s71,20480
Mvav 40,192,200
S_ m0,s51,2048
Mvav 36,220,236
Mvav 36,196,216
L_ v7,24,s55,0
Mvav 36,192,196
W_ 0
S_ s55,s71,30720
Mvaa 56,180,0
S_ m0,s51,3072
Ta 192,v1,12288
Ta 194,v1,14336
Mvaa 56,184,40
Mvaa 56,188,80
s_nop 0
Ta 196,v1,16384
Ta 198,v1,18432
Mvaa 60,180,4
L_ v7,24,s55,0
Mvaa 60,184,44
S_ s55,s71,40960
Ta 220,v1,20480
Ta 222,v1,22528
Mvaa 60,188,84
S_ m0,s51,4096
Mvaa 64,180,8
Mvaa 64,184,48
L_ v7,24,s55,0
Mvaa 64,188,88
S_ s55,s71,51200
Mvaa 68,180,12
S_ m0,s51,5120
Mvaa 68,184,52
Mvaa 68,188,240
L_ v7,24,s55,0
Mvaa 72,180,16
Mvaa 72,184,56
Mvaa 72,188,244
W_ 0
Mvaa 92,220,244
s_nop 0
Ta 180,v2,12288
Ta 182,v2,14336
Mvaa 92,196,56
Mvaa 92,192,16
s_nop 0
Ta 184,v2,16384
Ta 186,v2,18432
Mvaa 88,220,240
Mvaa 88,196,52
s_nop 0
Ta 188,v2,20480
Ta 190,v2,22528
Mvaa 88,192,12
Mvaa 84,220,88
Mvaa 84,196,48
Mvaa 84,192,8
Mvaa 80,220,84
Mvaa 80,196,44
Mvaa 80,192,4
Mvaa 76,220,80
Mvaa 76,196,40
Mvaa 76,192,0
W_ 0
Mvaa 16,180,0
s_nop 0
Ta 192,v3,12288
Ta 194,v3,14336
Mvaa 16,184,40
Mvaa 16,188,80
s_nop 0
Ta 196,v3,16384
Ta 198,v3,18432
Mvaa 20,180,4
Mvaa 20,184,44
s_nop 0
Ta 220,v3,20480
Ta 222,v3,22528
Mvaa 20,188,84
Mvaa 24,180,8
Mvaa 24,184,48
Mvaa 24,188,88
Mvaa 28,180,12
Mvaa 28,184,52
Mvaa 28,188,240
Mvaa 32,180,16
Mvaa 32,184,56
Mvaa 32,188,244
s_waitcnt vmcnt(20) lgkmcnt(0)
s_barrier
Mvaa 52,220,244
s_nop 0
Ta 180,%0,49152
Ta 182,%0,51200
Mvaa 52,196,56
.Lpad_o_1:
s_cbranch_execz .Lpad_o_5
Mvaa 52,192,16
s_nop 0
Ta 184,%0,53248
Ta 186,%0,55296
Mvaa 48,220,240
Mvaa 48,196,52
s_nop 0
Ta 188,%0,57344
Ta 190,%0,59392
Mvaa 48,192,12
Mvaa 44,220,88
Mvaa 44,196,48
Mvaa 44,192,8
Mvaa 40,220,84
Mvaa 40,196,44
Mvaa 40,192,4
Mvaa 36,220,80
Mvaa 36,196,40
Mvaa 36,192,0
W_ 0
Mvaa 56,180,120
S_ s73,s78,163840
Ta 192,v1,49152
Ta 194,v1,51200
Mvaa 56,184,160
S_ s73,s73,4096
Mvaa 56,188,200
s_nop 0
Ta 196,v1,53248
Ta 198,v1,55296
Mvaa 60,180,124
s_mov_b32 m0, s52
Mvaa 60,184,164
s_nop 0
Ta 220,v1,57344
Ta 222,v1,59392
Mvaa 60,188,204
s_nop 0
Y_ 56,0
Y_ 57,256
Mvaa 64,180,128
s_nop 0
Y_ 58,512
Y_ 59,768
Mvaa 64,184,168
Mvaa 64,188,208
s_nop 0
Y_ 60,1024
Y_ 61,1280
Mvaa 68,180,132
s_nop 0
Y_ 62,1536
Y_ 63,1792
Mvaa 68,184,172
Mvaa 68,188,212
s_nop 0
Y_ 64,2048
Y_ 65,2304
Mvaa 72,180,136
s_nop 0
Y_ 66,2560
Y_ 67,2816
Mvaa 72,184,176
Mvaa 72,188,216
W_ 12
Y_ 68,3072
Y_ 69,3328
Mvaa 92,220,216
s_nop 0
Ta 180,v2,49152
Ta 182,v2,51200
Mvaa 92,196,176
s_nop 0
Y_ 70,3584
Y_ 71,3840
Mvaa 92,192,136
s_nop 0
Ta 184,v2,53248
Ta 186,v2,55296
Mvaa 88,220,212
s_nop 0
Y_ 72,4096
Y_ 73,4352
Mvaa 88,196,172
s_nop 0
Ta 188,v2,57344
Ta 190,v2,59392
Mvaa 88,192,132
s_nop 0
Y_ 74,4608
Y_ 75,4864
Mvaa 84,220,208
s_nop 0
Y_ 92,9216
Y_ 93,9472
Mvaa 84,196,168
s_nop 0
Y_ 94,9728
Y_ 95,9984
Mvaa 84,192,128
s_nop 0
Y_ 88,8192
Y_ 89,8448
Mvaa 80,220,204
s_nop 0
Y_ 90,8704
Y_ 91,8960
Mvaa 80,196,164
Mvaa 80,192,124
s_nop 0
Y_ 84,7168
Y_ 85,7424
Mvaa 76,220,200
s_nop 0
Y_ 86,7680
Y_ 87,7936
Mvaa 76,196,160
Mvaa 76,192,120
W_ 14
Y_ 80,6144
Y_ 81,6400
Mvaa 16,180,120
s_nop 0
Ta 192,v3,49152
Ta 194,v3,51200
Mvaa 16,184,160
s_nop 0
Y_ 82,6656
Y_ 83,6912
Mvaa 16,188,200
s_nop 0
Ta 196,v3,53248
Ta 198,v3,55296
Mvaa 20,180,124
s_nop 0
Y_ 76,5120
Y_ 77,5376
Mvaa 20,184,164
s_nop 0
Ta 220,v3,57344
Ta 222,v3,59392
Mvaa 20,188,204
s_nop 0
Y_ 78,5632
Y_ 79,5888
Mvaa 24,180,128
Mvaa 24,184,168
Mvaa 24,188,208
s_waitcnt vmcnt(14) lgkmcnt(0)
s_barrier
Mvaa 28,180,132
S_ s55,s71,122880
ds_read2st64_b32 v[96:97], v5 offset0:0 offset1:1
Mvaa 28,184,172
S_ m0,s51,12288
ds_read2st64_b32 v[98:99], v5 offset0:2 offset1:3
Mvaa 28,188,212
L_ v7,24,s55,0
Mvaa 32,180,136
S_ s55,s71,133120
ds_read2st64_b32 v[104:105], v5 offset0:4 offset1:5
Mvaa 32,184,176
S_ m0,s51,13312
ds_read2st64_b32 v[106:107], v5 offset0:6 offset1:7
Mvaa 32,188,216
ds_read2st64_b32 v[112:113], v5 offset0:8 offset1:9
Mvaa 52,220,216
s_nop 0
Ta 180,%0,36864
Ta 182,%0,38912
Mvaa 52,196,176
L_ v7,24,s55,0
Mvaa 52,192,136
W_ 5
Ta 184,%0,40960
Ta 186,%0,43008
Mvaa 48,220,212
S_ s55,s71,143360
ds_read2st64_b32 v[114:115], v5 offset0:10 offset1:11
A_ v96,v16,v96
Mvaa 48,196,172
S_ m0,s51,14336
Ta 188,%0,45056
Ta 190,%0,47104
Mvaa 48,192,132
L_ v7,24,s55,0
Mvaa 44,220,208
S_ s55,s71,153600
ds_read2st64_b32 v[120:121], v5 offset0:12 offset1:13
A_ v97,v17,v97
Mvaa 44,196,168
S_ m0,s51,15360
ds_read2st64_b32 v[122:123], v5 offset0:14 offset1:15
A_ v98,v18,v98
Mvaa 44,192,128
ds_read2st64_b32 v[128:129], v5 offset0:16 offset1:17
A_ v99,v19,v99
Mvaa 40,220,204
L_ v7,24,s55,0
Mvaa 40,196,164
W_ 11
ds_read2st64_b32 v[130:131], v5 offset0:18 offset1:19
A_ v104,v20,v104
Mvaa 40,192,124
S_ s55,s71,163840
ds_read2st64_b32 v[132:133], v5 offset0:36 offset1:37
A_ v105,v21,v105
Mvaa 36,220,200
S_ m0,s51,16384
ds_read2st64_b32 v[134:135], v5 offset0:38 offset1:39
A_ v106,v22,v106
Mvaa 36,196,160
L_ v7,24,s55,0
Mvaa 36,192,120
W_ 6
ds_read2st64_b32 v[124:125], v5 offset0:32 offset1:33
A_ v107,v23,v107
Zvav 56,180,136
Ta 192,v1,36864
Ta 194,v1,38912
Mvav 56,184,156
S_ s55,s71,174080
ds_read2st64_b32 v[126:127], v5 offset0:34 offset1:35
A_ v112,v24,v112
Mvav 56,188,176
S_ m0,s51,17408
Ta 196,v1,40960
Ta 198,v1,43008
Zvav 60,180,140
L_ v7,24,s55,0
Mvav 60,184,160
S_ s79,s6,1
Ta 220,v1,45056
Ta 222,v1,47104
Mvav 60,188,180
s_and_b32 s79, s79, 7
ds_read2st64_b32 v[116:117], v5 offset0:28 offset1:29
A_ v113,v25,v113
Zvav 64,180,144
s_lshl_b32 s79, s79, 7
ds_read2st64_b32 v[118:119], v5 offset0:30 offset1:31
A_ v114,v26,v114
Mvav 64,184,164
S_ s71,s72,s79
ds_read2st64_b32 v[108:109], v5 offset0:24 offset1:25
A_ v115,v27,v115
Mvav 64,188,184
W_ 14
ds_read2st64_b32 v[110:111], v5 offset0:26 offset1:27
A_ v120,v28,v120
Zvav 68,180,148
S_ s55,s71,491520
ds_read2st64_b32 v[100:101], v5 offset0:20 offset1:21
A_ v121,v29,v121
Mvav 68,184,168
S_ m0,s51,49152
ds_read2st64_b32 v[102:103], v5 offset0:22 offset1:23
A_ v122,v30,v122
Mvav 68,188,188
L_ v7,24,s55,0
Zvav 72,180,152
S_ s55,s71,501760
A_ v123,v31,v123
Mvav 72,184,172
W_ 14
A_ v128,v32,v128
A_ v129,v33,v129
Mvav 72,188,192
W_ 6
A_ v130,v34,v130
A_ v131,v35,v131
Zvav 92,220,192
Ta 180,v2,36864
Ta 182,v2,38912
Mvav 92,196,172
S_ m0,s51,50176
A_ v132,v52,v132
A_ v133,v53,v133
Mvav 92,192,152
s_nop 0
Ta 184,v2,40960
Ta 186,v2,43008
Zvav 88,220,188
L_ v7,24,s55,0
Mvav 88,196,168
S_ s55,s71,512000
Ta 188,v2,45056
Ta 190,v2,47104
Mvav 88,192,148
S_ m0,s51,51200
A_ v134,v54,v134
A_ v135,v55,v135
Zvav 84,220,184
A_ v124,v48,v124
A_ v125,v49,v125
Mvav 84,196,164
L_ v7,24,s55,0
Mvav 84,192,144
S_ s55,s71,522240
A_ v126,v50,v126
A_ v127,v51,v127
Zvav 80,220,180
W_ 10
A_ v116,v44,v116
A_ v117,v45,v117
Mvav 80,196,160
S_ m0,s51,52224
A_ v118,v46,v118
A_ v119,v47,v119
Mvav 80,192,140
L_ v7,24,s55,0
Zvav 76,220,176
W_ 8
A_ v108,v40,v108
A_ v109,v41,v109
Mvav 76,196,156
S_ s55,s71,532480
A_ v110,v42,v110
A_ v111,v43,v111
Mvav 76,192,136
W_ 0
A_ v100,v36,v100
Zvav 16,180,136
S_ m0,s51,53248
Ta 192,v3,36864
Ta 194,v3,38912
Mvav 16,184,156
L_ v7,24,s55,0
Mvav 16,188,176
S_ s55,s71,542720
Ta 196,v3,40960
Ta 198,v3,43008
Zvav 20,180,140
S_ m0,s51,54272
A_ v101,v37,v101
A_ v102,v38,v102
Mvav 20,184,160
S_ s85,s6,3
Ta 220,v3,45056
Ta 222,v3,47104
Mvav 20,188,180
L_ v7,24,s55,0
Zvav 24,180,144
s_and_b32 s85, s85, 7
A_ v103,v39,v103
C_ 131,130,131
Mvav 24,184,164
s_lshl_b32 s85, s85, 7
C_ 130,128,129
C_ 132,132,133
Mvav 24,188,184
S_ s85,s85,s87
C_ 133,134,135
Zvav 28,180,148
buffer_store_dwordx4 v[130:133], v6, s[28:31], s73 offen offset:0 sc1
Mvav 28,184,168
s_sub_u32 s73, s73, 4096
Mvav 28,188,188
C_ 123,122,123
C_ 122,120,121
Zvav 32,180,152
C_ 124,124,125
C_ 125,126,127
Mvav 32,184,172
Mvav 32,188,192
s_waitcnt vmcnt(21) lgkmcnt(0)
s_barrier
Zvav 52,220,192
S_ s55,s71,614400
Ta 180,%0,24576
Ta 182,%0,26624
Mvav 52,196,172
S_ m0,s51,36864
Mvav 52,192,152
s_nop 0
Ta 184,%0,28672
Ta 186,%0,30720
Zvav 48,220,188
buffer_store_dwordx4 v[122:125], v6, s[28:31], s73 offen offset:3072 sc1
Mvav 48,196,168
s_nop 0
Ta 188,%0,32768
Ta 190,%0,34816
Mvav 48,192,148
C_ 115,114,115
C_ 114,112,113
Zvav 44,220,184
C_ 116,116,117
C_ 117,118,119
Mvav 44,196,164
Mvav 44,192,144
Zvav 40,220,180
Mvav 40,196,160
Mvav 40,192,140
buffer_store_dwordx4 v[114:117], v6, s[28:31], s73 offen offset:2048 sc1
Zvav 36,220,176
C_ 107,106,107
C_ 106,104,105
Mvav 36,196,156
C_ 108,108,109
C_ 109,110,111
Mvav 36,192,136
W_ 0
Mvav 56,180,196
s_nop 0
Ta 192,v1,24576
Ta 194,v1,26624
Mvav 56,184,216
Mvav 56,188,236
s_nop 0
Ta 196,v1,28672
Ta 198,v1,30720
Mvav 60,180,200
Mvav 60,184,220
s_nop 0
Ta 220,v1,32768
Ta 222,v1,34816
Mvav 60,188,240
buffer_store_dwordx4 v[106:109], v6, s[28:31], s73 offen offset:1024 sc1
Mvav 64,180,204
C_ 99,98,99
C_ 98,96,97
Mvav 64,184,224
C_ 100,100,101
C_ 101,102,103
Mvav 64,188,244
Mvav 68,180,208
Mvav 68,184,228
Mvav 68,188,248
Mvav 72,180,212
Mvav 72,184,232
buffer_store_dwordx4 v[98:101], v6, s[28:31], s73 offen offset:0 sc1
Mvav 72,188,252
W_ 0
Mvav 92,220,252
s_nop 0
Ta 180,v2,24576
Ta 182,v2,26624
Mvav 92,196,232
Mvav 92,192,212
s_nop 0
Ta 184,v2,28672
Ta 186,v2,30720
Mvav 88,220,248
Mvav 88,196,228
s_nop 0
Ta 188,v2,32768
Ta 190,v2,34816
Mvav 88,192,208
Mvav 84,220,244
L_ v7,24,s55,0
Mvav 84,196,224
S_ s55,s71,624640
Mvav 84,192,204
S_ m0,s51,37888
Mvav 80,220,240
Mvav 80,196,220
L_ v7,24,s55,0
Mvav 80,192,200
S_ s55,s71,634880
Mvav 76,220,236
S_ m0,s51,38912
Mvav 76,196,216
Mvav 76,192,196
W_ 0
Mvav 16,180,196
s_nop 0
Ta 192,v3,24576
Ta 194,v3,26624
Mvav 16,184,216
L_ v7,24,s55,0
Mvav 16,188,236
S_ s55,s71,645120
Ta 196,v3,28672
Ta 198,v3,30720
Mvav 20,180,200
S_ m0,s51,39936
Mvav 20,184,220
s_nop 0
Ta 220,v3,32768
Ta 222,v3,34816
Mvav 20,188,240
L_ v7,24,s55,0
Mvav 24,180,204
S_ s55,s71,655360
Mvav 24,184,224
S_ m0,s51,40960
Mvav 24,188,244
Mvav 28,180,208
L_ v7,24,s55,0
Mvav 28,184,228
S_ s55,s71,665600
Mvav 28,188,248
S_ m0,s51,41984
Mvav 32,180,212
Mvav 32,184,232
L_ v7,24,s55,0
Mvav 32,188,252
s_waitcnt vmcnt(23) lgkmcnt(0)
s_barrier
Mvav 52,220,252
S_ s55,s71,0
Ta 180,%0,0
Ta 182,%0,2048
Mvav 52,196,232
S_ m0,s51,24576
Mvav 52,192,212
s_nop 0
Ta 184,%0,4096
Ta 186,%0,6144
Mvav 48,220,248
L_ v7,24,s55,0
Mvav 48,196,228
S_ s55,s71,10240
Ta 188,%0,8192
Ta 190,%0,10240
Mvav 48,192,208
S_ m0,s51,25600
Mvav 44,220,244
Mvav 44,196,224
L_ v7,24,s55,0
Mvav 44,192,204
S_ s55,s71,20480
Mvav 40,220,240
S_ m0,s51,26624
Mvav 40,196,220
Mvav 40,192,200
L_ v7,24,s55,0
Mvav 36,220,236
S_ s55,s71,30720
Mvav 36,196,216
S_ m0,s51,27648
Mvav 36,192,196
W_ 0
Mvaa 56,180,0
s_nop 0
Ta 192,v1,0
Ta 194,v1,2048
Mvaa 56,184,40
L_ v7,24,s55,0
Mvaa 56,188,80
S_ s55,s71,40960
Ta 196,v1,4096
Ta 198,v1,6144
Mvaa 60,180,4
S_ m0,s51,28672
Mvaa 60,184,44
s_nop 0
Ta 220,v1,8192
Ta 222,v1,10240
Mvaa 60,188,84
L_ v7,24,s55,0
Mvaa 64,180,8
S_ s55,s71,51200
Mvaa 64,184,48
S_ m0,s51,29696
Mvaa 64,188,88
Mvaa 68,180,12
L_ v7,24,s55,0
Mvaa 68,184,52
Mvaa 68,188,240
Mvaa 72,180,16
Mvaa 72,184,56
buffer_load_dword a74, v4, s[24:27], s85 offen
Mvaa 72,188,244
W_ 0
Mvaa 92,220,244
s_nop 0
Ta 180,v2,0
Ta 182,v2,2048
Mvaa 92,196,56
.Lpad_o_2:
s_cbranch_execz .Lpad_o_6
Mvaa 92,192,16
s_nop 0
Ta 184,v2,4096
Ta 186,v2,6144
Mvaa 88,220,240
Mvaa 88,196,52
s_nop 0
Ta 188,v2,8192
Ta 190,v2,10240
Mvaa 88,192,12
Mvaa 84,220,88
Mvaa 84,196,48
Mvaa 84,192,8
Mvaa 80,220,84
Mvaa 80,196,44
Mvaa 80,192,4
Mvaa 76,220,80
Mvaa 76,196,40
X_ a20,-1
X_ a21,-1
Mvaa 76,192,0
W_ 0
X_ a22,-1
X_ a23,-1
Mvaa 16,180,0
S_ s81,s6,7
Ta 192,v3,0
Ta 194,v3,2048
Mvaa 16,184,40
s_and_b32 s81, s81, 7
Mvaa 16,188,80
s_mul_i32 s81, s81, 20480
Ta 196,v3,4096
Ta 198,v3,6144
Mvaa 20,180,4
S_ s81,s81,s78
Mvaa 20,184,44
s_nop 0
Ta 220,v3,8192
Ta 222,v3,10240
Mvaa 20,188,84
buffer_store_dwordx4 a[20:23], v6, s[28:31], s81 offen offset:0 sc1
Mvaa 24,180,8
Mvaa 24,184,48
Mvaa 24,188,88
Mvaa 28,180,12
buffer_store_dwordx4 a[20:23], v6, s[28:31], s81 offen offset:1024 sc1
Mvaa 28,184,52
Mvaa 28,188,240
Mvaa 32,180,16
Mvaa 32,184,56
Mvaa 32,188,244
s_waitcnt vmcnt(26) lgkmcnt(0)
s_barrier
Mvaa 52,220,244
s_nop 0
Ta 180,%0,12288
Ta 182,%0,14336
Mvaa 52,196,56
buffer_store_dwordx4 a[20:23], v6, s[28:31], s81 offen offset:2048 sc1
Mvaa 52,192,16
s_nop 0
Ta 184,%0,16384
Ta 186,%0,18432
Mvaa 48,220,240
Mvaa 48,196,52
s_nop 0
Ta 188,%0,20480
Ta 190,%0,22528
Mvaa 48,192,12
Mvaa 44,220,88
Mvaa 44,196,48
buffer_store_dwordx4 a[20:23], v6, s[28:31], s81 offen offset:3072 sc1
Mvaa 44,192,8
S_ s81,s81,4096
Mvaa 40,220,84
Mvaa 40,196,44
Mvaa 40,192,4
Mvaa 36,220,80
Mvaa 36,196,40
Mvaa 36,192,0
W_ 0
Mvaa 56,180,120
s_nop 0
Ta 192,v1,12288
Ta 194,v1,14336
Mvaa 56,184,160
buffer_store_dwordx4 a[20:23], v6, s[28:31], s81 offen offset:0 sc1
Mvaa 56,188,200
S_ s73,s78,184320
Ta 196,v1,16384
Ta 198,v1,18432
Mvaa 60,180,124
s_mov_b32 m0, s52
S_ s73,s73,4096
Mvaa 60,184,164
s_nop 0
Ta 220,v1,20480
Ta 222,v1,22528
Mvaa 60,188,204
s_nop 0
Y_ 56,0
Y_ 57,256
Mvaa 64,180,128
s_nop 0
Y_ 58,512
Y_ 59,768
Mvaa 64,184,168
Mvaa 64,188,208
s_nop 0
Y_ 60,1024
Y_ 61,1280
Mvaa 68,180,132
s_nop 0
Y_ 62,1536
Y_ 63,1792
Mvaa 68,184,172
Mvaa 68,188,212
s_nop 0
Y_ 64,2048
Y_ 65,2304
Mvaa 72,180,136
)A"
R"A(s_nop 0
Y_ 66,2560
Y_ 67,2816
Mvaa 72,184,176
Mvaa 72,188,216
W_ 12
Y_ 68,3072
Y_ 69,3328
Mvaa 92,220,216
s_nop 0
Ta 180,v2,12288
Ta 182,v2,14336
Mvaa 92,196,176
s_nop 0
Y_ 70,3584
Y_ 71,3840
Mvaa 92,192,136
s_nop 0
Ta 184,v2,16384
Ta 186,v2,18432
Mvaa 88,220,212
s_nop 0
Y_ 72,4096
Y_ 73,4352
Mvaa 88,196,172
s_nop 0
Ta 188,v2,20480
Ta 190,v2,22528
Mvaa 88,192,132
s_nop 0
Y_ 74,4608
Y_ 75,4864
Mvaa 84,220,208
s_nop 0
Y_ 92,9216
Y_ 93,9472
Mvaa 84,196,168
s_nop 0
Y_ 94,9728
Y_ 95,9984
Mvaa 84,192,128
s_nop 0
Y_ 88,8192
Y_ 89,8448
Mvaa 80,220,204
s_nop 0
Y_ 90,8704
Y_ 91,8960
Mvaa 80,196,164
Mvaa 80,192,124
s_nop 0
Y_ 84,7168
Y_ 85,7424
Mvaa 76,220,200
s_nop 0
Y_ 86,7680
Y_ 87,7936
Mvaa 76,196,160
Mvaa 76,192,120
W_ 14
Y_ 80,6144
Y_ 81,6400
Mvaa 16,180,120
s_nop 0
Ta 192,v3,12288
Ta 194,v3,14336
Mvaa 16,184,160
s_nop 0
Y_ 82,6656
Y_ 83,6912
Mvaa 16,188,200
s_nop 0
Ta 196,v3,16384
Ta 198,v3,18432
Mvaa 20,180,124
s_nop 0
Y_ 76,5120
Y_ 77,5376
Mvaa 20,184,164
s_nop 0
Ta 220,v3,20480
Ta 222,v3,22528
Mvaa 20,188,204
s_nop 0
Y_ 78,5632
Y_ 79,5888
Mvaa 24,180,128
Mvaa 24,184,168
Mvaa 24,188,208
s_waitcnt vmcnt(23) lgkmcnt(0)
s_barrier
Mvaa 28,180,132
S_ s55,s71,122880
ds_read2st64_b32 v[96:97], v5 offset0:0 offset1:1
Mvaa 28,184,172
S_ m0,s51,0
ds_read2st64_b32 v[98:99], v5 offset0:2 offset1:3
Mvaa 28,188,212
L_ v7,24,s55,0
Mvaa 32,180,136
S_ s55,s71,133120
ds_read2st64_b32 v[104:105], v5 offset0:4 offset1:5
Mvaa 32,184,176
S_ m0,s51,1024
ds_read2st64_b32 v[106:107], v5 offset0:6 offset1:7
Mvaa 32,188,216
ds_read2st64_b32 v[112:113], v5 offset0:8 offset1:9
Mvaa 52,220,216
s_nop 0
Ta 180,%0,49152
Ta 182,%0,51200
Mvaa 52,196,176
L_ v7,24,s55,0
Mvaa 52,192,136
W_ 5
Ta 184,%0,53248
Ta 186,%0,55296
Mvaa 48,220,212
S_ s55,s71,143360
ds_read2st64_b32 v[114:115], v5 offset0:10 offset1:11
A_ v96,v16,v96
Mvaa 48,196,172
S_ m0,s51,2048
Ta 188,%0,57344
Ta 190,%0,59392
Mvaa 48,192,132
L_ v7,24,s55,0
Mvaa 44,220,208
S_ s55,s71,153600
ds_read2st64_b32 v[120:121], v5 offset0:12 offset1:13
A_ v97,v17,v97
Mvaa 44,196,168
S_ m0,s51,3072
ds_read2st64_b32 v[122:123], v5 offset0:14 offset1:15
A_ v98,v18,v98
Mvaa 44,192,128
ds_read2st64_b32 v[128:129], v5 offset0:16 offset1:17
A_ v99,v19,v99
Mvaa 40,220,204
L_ v7,24,s55,0
Mvaa 40,196,164
W_ 11
ds_read2st64_b32 v[130:131], v5 offset0:18 offset1:19
A_ v104,v20,v104
Mvaa 40,192,124
S_ s55,s71,163840
ds_read2st64_b32 v[132:133], v5 offset0:36 offset1:37
A_ v105,v21,v105
Mvaa 36,220,200
S_ m0,s51,4096
ds_read2st64_b32 v[134:135], v5 offset0:38 offset1:39
A_ v106,v22,v106
Mvaa 36,196,160
L_ v7,24,s55,0
Mvaa 36,192,120
W_ 6
ds_read2st64_b32 v[124:125], v5 offset0:32 offset1:33
A_ v107,v23,v107
Zvav 56,180,136
Ta 192,v1,49152
Ta 194,v1,51200
Mvav 56,184,156
S_ s55,s71,174080
ds_read2st64_b32 v[126:127], v5 offset0:34 offset1:35
A_ v112,v24,v112
Mvav 56,188,176
S_ m0,s51,5120
Ta 196,v1,53248
Ta 198,v1,55296
Zvav 60,180,140
L_ v7,24,s55,0
Mvav 60,184,160
S_ s79,s6,2
Ta 220,v1,57344
Ta 222,v1,59392
Mvav 60,188,180
s_and_b32 s79, s79, 7
ds_read2st64_b32 v[116:117], v5 offset0:28 offset1:29
A_ v113,v25,v113
Zvav 64,180,144
s_lshl_b32 s79, s79, 7
ds_read2st64_b32 v[118:119], v5 offset0:30 offset1:31
A_ v114,v26,v114
Mvav 64,184,164
S_ s71,s72,s79
ds_read2st64_b32 v[108:109], v5 offset0:24 offset1:25
A_ v115,v27,v115
Mvav 64,188,184
W_ 14
ds_read2st64_b32 v[110:111], v5 offset0:26 offset1:27
A_ v120,v28,v120
Zvav 68,180,148
S_ s55,s71,491520
ds_read2st64_b32 v[100:101], v5 offset0:20 offset1:21
A_ v121,v29,v121
Mvav 68,184,168
S_ m0,s51,12288
ds_read2st64_b32 v[102:103], v5 offset0:22 offset1:23
A_ v122,v30,v122
Mvav 68,188,188
L_ v7,24,s55,0
Zvav 72,180,152
S_ s55,s71,501760
A_ v123,v31,v123
R_ v12,a248
Mvav 72,184,172
W_ 14
A_ v128,v32,v128
A_ v129,v33,v129
Mvav 72,188,192
W_ 6
A_ v130,v34,v130
A_ v131,v35,v131
Zvav 92,220,192
Ta 180,v2,49152
Ta 182,v2,51200
Mvav 92,196,172
S_ m0,s51,13312
A_ v132,v52,v132
A_ v133,v53,v133
Mvav 92,192,152
s_nop 0
Ta 184,v2,53248
Ta 186,v2,55296
Zvav 88,220,188
L_ v7,24,s55,0
Mvav 88,196,168
S_ s55,s71,512000
Ta 188,v2,57344
Ta 190,v2,59392
Mvav 88,192,148
S_ m0,s51,14336
A_ v134,v54,v134
A_ v135,v55,v135
Zvav 84,220,184
A_ v124,v48,v124
A_ v125,v49,v125
Mvav 84,196,164
L_ v7,24,s55,0
Mvav 84,192,144
S_ s55,s71,522240
A_ v126,v50,v126
A_ v127,v51,v127
Zvav 80,220,180
W_ 10
A_ v116,v44,v116
A_ v117,v45,v117
Mvav 80,196,160
S_ m0,s51,15360
A_ v118,v46,v118
A_ v119,v47,v119
Mvav 80,192,140
L_ v7,24,s55,0
Zvav 76,220,176
W_ 8
A_ v108,v40,v108
A_ v109,v41,v109
Mvav 76,196,156
S_ s55,s71,532480
A_ v110,v42,v110
A_ v111,v43,v111
Mvav 76,192,136
W_ 0
A_ v100,v36,v100
Zvav 16,180,136
S_ m0,s51,16384
Ta 192,v3,49152
Ta 194,v3,51200
Mvav 16,184,156
L_ v7,24,s55,0
Mvav 16,188,176
S_ s55,s71,542720
Ta 196,v3,53248
Ta 198,v3,55296
Zvav 20,180,140
S_ m0,s51,17408
A_ v101,v37,v101
A_ v102,v38,v102
Mvav 20,184,160
S_ s85,s6,4
Ta 220,v3,57344
Ta 222,v3,59392
Mvav 20,188,180
L_ v7,24,s55,0
Zvav 24,180,144
s_and_b32 s85, s85, 7
A_ v103,v39,v103
C_ 131,130,131
Mvav 24,184,164
s_lshl_b32 s85, s85, 7
C_ 130,128,129
C_ 132,132,133
Mvav 24,188,184
S_ s85,s85,s87
C_ 133,134,135
v_min_u32 v12, 240, v12
Zvav 28,180,148
buffer_store_dwordx4 v[130:133], v6, s[28:31], s73 offen offset:0 sc1
Mvav 28,184,168
s_sub_u32 s73, s73, 4096
v_lshlrev_b32 v12, 3, v12
Mvav 28,188,188
C_ 123,122,123
C_ 122,120,121
Zvav 32,180,152
C_ 124,124,125
C_ 125,126,127
Mvav 32,184,172
Mvav 32,188,192
s_waitcnt vmcnt(25) lgkmcnt(0)
s_barrier
Zvav 52,220,192
S_ s55,s71,614400
Ta 180,%0,36864
Ta 182,%0,38912
Mvav 52,196,172
S_ m0,s51,49152
Mvav 52,192,152
s_nop 0
Ta 184,%0,40960
Ta 186,%0,43008
Zvav 48,220,188
buffer_store_dwordx4 v[122:125], v6, s[28:31], s73 offen offset:3072 sc1
Mvav 48,196,168
s_nop 0
Ta 188,%0,45056
Ta 190,%0,47104
Mvav 48,192,148
C_ 115,114,115
C_ 114,112,113
Zvav 44,220,184
C_ 116,116,117
C_ 117,118,119
Mvav 44,196,164
Mvav 44,192,144
Zvav 40,220,180
Mvav 40,196,160
Mvav 40,192,140
buffer_store_dwordx4 v[114:117], v6, s[28:31], s73 offen offset:2048 sc1
Zvav 36,220,176
C_ 107,106,107
C_ 106,104,105
Mvav 36,196,156
C_ 108,108,109
C_ 109,110,111
Mvav 36,192,136
W_ 0
Mvav 56,180,196
s_nop 0
Ta 192,v1,36864
Ta 194,v1,38912
Mvav 56,184,216
Mvav 56,188,236
s_nop 0
Ta 196,v1,40960
Ta 198,v1,43008
Mvav 60,180,200
Mvav 60,184,220
s_nop 0
Ta 220,v1,45056
Ta 222,v1,47104
Mvav 60,188,240
buffer_store_dwordx4 v[106:109], v6, s[28:31], s73 offen offset:1024 sc1
Mvav 64,180,204
C_ 99,98,99
C_ 98,96,97
Mvav 64,184,224
C_ 100,100,101
C_ 101,102,103
Mvav 64,188,244
Mvav 68,180,208
Mvav 68,184,228
Mvav 68,188,248
Mvav 72,180,212
Mvav 72,184,232
buffer_store_dwordx4 v[98:101], v6, s[28:31], s73 offen offset:0 sc1
Mvav 72,188,252
W_ 0
Mvav 92,220,252
s_nop 0
Ta 180,v2,36864
Ta 182,v2,38912
Mvav 92,196,232
Mvav 92,192,212
s_nop 0
Ta 184,v2,40960
Ta 186,v2,43008
Mvav 88,220,248
Mvav 88,196,228
s_nop 0
Ta 188,v2,45056
Ta 190,v2,47104
Mvav 88,192,208
Mvav 84,220,244
L_ v7,24,s55,0
Mvav 84,196,224
S_ s55,s71,624640
Mvav 84,192,204
S_ m0,s51,50176
Mvav 80,220,240
Mvav 80,196,220
L_ v7,24,s55,0
Mvav 80,192,200
S_ s55,s71,634880
Mvav 76,220,236
S_ m0,s51,51200
Mvav 76,196,216
Mvav 76,192,196
W_ 0
Mvav 16,180,196
s_nop 0
Ta 192,v3,36864
Ta 194,v3,38912
Mvav 16,184,216
L_ v7,24,s55,0
Mvav 16,188,236
S_ s55,s71,645120
Ta 196,v3,40960
Ta 198,v3,43008
Mvav 20,180,200
S_ m0,s51,52224
Mvav 20,184,220
s_nop 0
Ta 220,v3,45056
Ta 222,v3,47104
Mvav 20,188,240
L_ v7,24,s55,0
Mvav 24,180,204
S_ s55,s71,655360
Mvav 24,184,224
S_ m0,s51,53248
Mvav 24,188,244
Mvav 28,180,208
L_ v7,24,s55,0
Mvav 28,184,228
S_ s55,s71,665600
Mvav 28,188,248
S_ m0,s51,54272
Mvav 32,180,212
Mvav 32,184,232
L_ v7,24,s55,0
Mvav 32,188,252
s_waitcnt vmcnt(29) lgkmcnt(0)
s_barrier
Mvav 52,220,252
S_ s55,s71,0
Ta 180,%0,24576
Ta 182,%0,26624
Mvav 52,196,232
S_ m0,s51,36864
Mvav 52,192,212
.Lpad_o_3:
s_cbranch_execz .Lpad_o_7
Ta 184,%0,28672
Ta 186,%0,30720
Mvav 48,220,248
L_ v7,24,s55,0
Mvav 48,196,228
S_ s55,s71,10240
Ta 188,%0,32768
Ta 190,%0,34816
Mvav 48,192,208
S_ m0,s51,37888
Mvav 44,220,244
Mvav 44,196,224
L_ v7,24,s55,0
Mvav 44,192,204
S_ s55,s71,20480
Mvav 40,220,240
S_ m0,s51,38912
Mvav 40,196,220
Mvav 40,192,200
L_ v7,24,s55,0
Mvav 36,220,236
S_ s55,s71,30720
Mvav 36,196,216
S_ m0,s51,39936
Mvav 36,192,196
W_ 0
Mvaa 56,180,0
s_nop 0
Ta 192,v1,24576
Ta 194,v1,26624
Mvaa 56,184,40
L_ v7,24,s55,0
Mvaa 56,188,80
S_ s55,s71,40960
Ta 196,v1,28672
Ta 198,v1,30720
Mvaa 60,180,4
S_ m0,s51,40960
Mvaa 60,184,44
s_nop 0
Ta 220,v1,32768
Ta 222,v1,34816
Mvaa 60,188,84
L_ v7,24,s55,0
Mvaa 64,180,8
S_ s55,s71,51200
Mvaa 64,184,48
S_ m0,s51,41984
Mvaa 64,188,88
Mvaa 68,180,12
L_ v7,24,s55,0
Mvaa 68,184,52
Mvaa 68,188,240
Mvaa 72,180,16
Mvaa 72,184,56
buffer_load_dword a74, v12, s[44:47], s66 offen
Mvaa 72,188,244
W_ 0
Mvaa 92,220,244
s_nop 0
Ta 180,v2,24576
Ta 182,v2,26624
Mvaa 92,196,56
Mvaa 92,192,16
s_nop 0
Ta 184,v2,28672
Ta 186,v2,30720
Mvaa 88,220,240
buffer_load_dword a74, v4, s[24:27], s85 offen
Mvaa 88,196,52
s_nop 0
Ta 188,v2,32768
Ta 190,v2,34816
Mvaa 88,192,12
Mvaa 84,220,88
Mvaa 84,196,48
Mvaa 84,192,8
Mvaa 80,220,84
Mvaa 80,196,44
Mvaa 80,192,4
Mvaa 76,220,80
Mvaa 76,196,40
Mvaa 76,192,0
W_ 0
Mvaa 16,180,0
s_nop 0
Ta 192,v3,24576
Ta 194,v3,26624
Mvaa 16,184,40
Mvaa 16,188,80
s_nop 0
Ta 196,v3,28672
Ta 198,v3,30720
Mvaa 20,180,4
Mvaa 20,184,44
s_nop 0
Ta 220,v3,32768
Ta 222,v3,34816
Mvaa 20,188,84
Mvaa 24,180,8
Mvaa 24,184,48
Mvaa 24,188,88
Mvaa 28,180,12
Mvaa 28,184,52
Mvaa 28,188,240
Mvaa 32,180,16
Mvaa 32,184,56
Mvaa 32,188,244
s_waitcnt vmcnt(25) lgkmcnt(0)
s_barrier
Mvaa 52,220,244
s_nop 0
Ta 180,%0,0
Ta 182,%0,2048
Mvaa 52,196,56
Mvaa 52,192,16
s_nop 0
Ta 184,%0,4096
Ta 186,%0,6144
Mvaa 48,220,240
Mvaa 48,196,52
s_nop 0
Ta 188,%0,8192
Ta 190,%0,10240
Mvaa 48,192,12
Mvaa 44,220,88
Mvaa 44,196,48
Mvaa 44,192,8
Mvaa 40,220,84
Mvaa 40,196,44
Mvaa 40,192,4
Mvaa 36,220,80
Mvaa 36,196,40
Mvaa 36,192,0
W_ 0
Mvaa 56,180,120
S_ s81,s6,1
Ta 192,v1,0
Ta 194,v1,2048
Mvaa 56,184,160
s_and_b32 s81, s81, 7
Mvaa 56,188,200
s_mul_i32 s81, s81, 20480
Ta 196,v1,4096
Ta 198,v1,6144
Mvaa 60,180,124
s_mov_b32 m0, s52
S_ s73,s81,s78
Mvaa 60,184,164
S_ s73,s73,4096
Ta 220,v1,8192
Ta 222,v1,10240
Mvaa 60,188,204
s_nop 0
Y_ 56,0
Y_ 57,256
Mvaa 64,180,128
s_nop 0
Y_ 58,512
Y_ 59,768
Mvaa 64,184,168
Mvaa 64,188,208
s_nop 0
Y_ 60,1024
Y_ 61,1280
Mvaa 68,180,132
s_nop 0
Y_ 62,1536
Y_ 63,1792
Mvaa 68,184,172
Mvaa 68,188,212
s_nop 0
Y_ 64,2048
Y_ 65,2304
Mvaa 72,180,136
s_nop 0
Y_ 66,2560
Y_ 67,2816
Mvaa 72,184,176
Mvaa 72,188,216
W_ 12
Y_ 68,3072
Y_ 69,3328
Mvaa 92,220,216
s_nop 0
Ta 180,v2,0
Ta 182,v2,2048
Mvaa 92,196,176
s_nop 0
Y_ 70,3584
Y_ 71,3840
Mvaa 92,192,136
s_nop 0
Ta 184,v2,4096
Ta 186,v2,6144
Mvaa 88,220,212
s_nop 0
Y_ 72,4096
Y_ 73,4352
Mvaa 88,196,172
s_nop 0
Ta 188,v2,8192
Ta 190,v2,10240
Mvaa 88,192,132
s_nop 0
Y_ 74,4608
Y_ 75,4864
Mvaa 84,220,208
s_nop 0
Y_ 92,9216
Y_ 93,9472
Mvaa 84,196,168
s_nop 0
Y_ 94,9728
Y_ 95,9984
Mvaa 84,192,128
s_nop 0
Y_ 88,8192
Y_ 89,8448
Mvaa 80,220,204
s_nop 0
Y_ 90,8704
Y_ 91,8960
Mvaa 80,196,164
Mvaa 80,192,124
s_nop 0
Y_ 84,7168
Y_ 85,7424
Mvaa 76,220,200
s_nop 0
Y_ 86,7680
Y_ 87,7936
Mvaa 76,196,160
Mvaa 76,192,120
W_ 14
Y_ 80,6144
Y_ 81,6400
Mvaa 16,180,120
s_nop 0
Ta 192,v3,0
Ta 194,v3,2048
Mvaa 16,184,160
s_nop 0
Y_ 82,6656
Y_ 83,6912
Mvaa 16,188,200
s_nop 0
Ta 196,v3,4096
Ta 198,v3,6144
Mvaa 20,180,124
s_nop 0
Y_ 76,5120
Y_ 77,5376
Mvaa 20,184,164
s_nop 0
Ta 220,v3,8192
Ta 222,v3,10240
Mvaa 20,188,204
s_nop 0
Y_ 78,5632
Y_ 79,5888
Mvaa 24,180,128
Mvaa 24,184,168
Mvaa 24,188,208
s_waitcnt vmcnt(19) lgkmcnt(0)
s_barrier
Mvaa 28,180,132
S_ s55,s71,122880
ds_read2st64_b32 v[96:97], v5 offset0:0 offset1:1
Mvaa 28,184,172
S_ m0,s51,24576
ds_read2st64_b32 v[98:99], v5 offset0:2 offset1:3
Mvaa 28,188,212
L_ v7,24,s55,0
Mvaa 32,180,136
S_ s55,s71,133120
ds_read2st64_b32 v[104:105], v5 offset0:4 offset1:5
Mvaa 32,184,176
S_ m0,s51,25600
ds_read2st64_b32 v[106:107], v5 offset0:6 offset1:7
Mvaa 32,188,216
ds_read2st64_b32 v[112:113], v5 offset0:8 offset1:9
Mvaa 52,220,216
s_nop 0
Ta 180,%0,12288
Ta 182,%0,14336
Mvaa 52,196,176
L_ v7,24,s55,0
Mvaa 52,192,136
W_ 5
Ta 184,%0,16384
Ta 186,%0,18432
Mvaa 48,220,212
S_ s55,s71,143360
ds_read2st64_b32 v[114:115], v5 offset0:10 offset1:11
A_ v96,v16,v96
Mvaa 48,196,172
S_ m0,s51,26624
Ta 188,%0,20480
Ta 190,%0,22528
Mvaa 48,192,132
L_ v7,24,s55,0
Mvaa 44,220,208
S_ s55,s71,153600
ds_read2st64_b32 v[120:121], v5 offset0:12 offset1:13
A_ v97,v17,v97
Mvaa 44,196,168
S_ m0,s51,27648
ds_read2st64_b32 v[122:123], v5 offset0:14 offset1:15
A_ v98,v18,v98
Mvaa 44,192,128
ds_read2st64_b32 v[128:129], v5 offset0:16 offset1:17
A_ v99,v19,v99
Mvaa 40,220,204
L_ v7,24,s55,0
Mvaa 40,196,164
W_ 11
ds_read2st64_b32 v[130:131], v5 offset0:18 offset1:19
A_ v104,v20,v104
Mvaa 40,192,124
S_ s55,s71,163840
ds_read2st64_b32 v[132:133], v5 offset0:36 offset1:37
A_ v105,v21,v105
Mvaa 36,220,200
S_ m0,s51,28672
ds_read2st64_b32 v[134:135], v5 offset0:38 offset1:39
A_ v106,v22,v106
Mvaa 36,196,160
L_ v7,24,s55,0
Mvaa 36,192,120
W_ 6
ds_read2st64_b32 v[124:125], v5 offset0:32 offset1:33
A_ v107,v23,v107
Zvav 56,180,136
Ta 192,v1,12288
Ta 194,v1,14336
Mvav 56,184,156
S_ s55,s71,174080
ds_read2st64_b32 v[126:127], v5 offset0:34 offset1:35
A_ v112,v24,v112
Mvav 56,188,176
S_ m0,s51,29696
Ta 196,v1,16384
Ta 198,v1,18432
Zvav 60,180,140
L_ v7,24,s55,0
Mvav 60,184,160
S_ s79,s6,3
Ta 220,v1,20480
Ta 222,v1,22528
Mvav 60,188,180
s_and_b32 s79, s79, 7
ds_read2st64_b32 v[116:117], v5 offset0:28 offset1:29
A_ v113,v25,v113
Zvav 64,180,144
s_lshl_b32 s79, s79, 7
ds_read2st64_b32 v[118:119], v5 offset0:30 offset1:31
A_ v114,v26,v114
Mvav 64,184,164
S_ s71,s72,s79
ds_read2st64_b32 v[108:109], v5 offset0:24 offset1:25
A_ v115,v27,v115
Mvav 64,188,184
W_ 14
ds_read2st64_b32 v[110:111], v5 offset0:26 offset1:27
A_ v120,v28,v120
Zvav 68,180,148
S_ s55,s71,491520
ds_read2st64_b32 v[100:101], v5 offset0:20 offset1:21
A_ v121,v29,v121
Mvav 68,184,168
S_ m0,s51,0
ds_read2st64_b32 v[102:103], v5 offset0:22 offset1:23
A_ v122,v30,v122
Mvav 68,188,188
L_ v7,24,s55,0
Zvav 72,180,152
S_ s55,s71,501760
A_ v123,v31,v123
Mvav 72,184,172
W_ 14
A_ v128,v32,v128
A_ v129,v33,v129
Mvav 72,188,192
W_ 6
A_ v130,v34,v130
A_ v131,v35,v131
Zvav 92,220,192
Ta 180,v2,12288
Ta 182,v2,14336
Mvav 92,196,172
S_ m0,s51,1024
A_ v132,v52,v132
A_ v133,v53,v133
Mvav 92,192,152
s_nop 0
Ta 184,v2,16384
Ta 186,v2,18432
Zvav 88,220,188
L_ v7,24,s55,0
Mvav 88,196,168
S_ s55,s71,512000
Ta 188,v2,20480
Ta 190,v2,22528
Mvav 88,192,148
S_ m0,s51,2048
A_ v134,v54,v134
A_ v135,v55,v135
Zvav 84,220,184
A_ v124,v48,v124
A_ v125,v49,v125
Mvav 84,196,164
L_ v7,24,s55,0
Mvav 84,192,144
S_ s55,s71,522240
A_ v126,v50,v126
A_ v127,v51,v127
Zvav 80,220,180
W_ 10
A_ v116,v44,v116
A_ v117,v45,v117
Mvav 80,196,160
S_ m0,s51,3072
A_ v118,v46,v118
A_ v119,v47,v119
Mvav 80,192,140
L_ v7,24,s55,0
Zvav 76,220,176
W_ 8
A_ v108,v40,v108
A_ v109,v41,v109
Mvav 76,196,156
S_ s55,s71,532480
A_ v110,v42,v110
A_ v111,v43,v111
Mvav 76,192,136
W_ 0
A_ v100,v36,v100
Zvav 16,180,136
S_ m0,s51,4096
Ta 192,v3,12288
Ta 194,v3,14336
Mvav 16,184,156
L_ v7,24,s55,0
Mvav 16,188,176
S_ s55,s71,542720
Ta 196,v3,16384
Ta 198,v3,18432
Zvav 20,180,140
S_ m0,s51,5120
A_ v101,v37,v101
A_ v102,v38,v102
Mvav 20,184,160
S_ s85,s6,5
Ta 220,v3,20480
Ta 222,v3,22528
Mvav 20,188,180
L_ v7,24,s55,0
Zvav 24,180,144
s_and_b32 s85, s85, 7
A_ v103,v39,v103
C_ 131,130,131
Mvav 24,184,164
s_lshl_b32 s85, s85, 7
C_ 130,128,129
C_ 132,132,133
Mvav 24,188,184
S_ s85,s85,s87
C_ 133,134,135
Zvav 28,180,148
buffer_store_dwordx4 v[130:133], v6, s[28:31], s73 offen offset:0 sc1
Mvav 28,184,168
s_sub_u32 s73, s73, 4096
Mvav 28,188,188
C_ 123,122,123
C_ 122,120,121
Zvav 32,180,152
C_ 124,124,125
C_ 125,126,127
Mvav 32,184,172
Mvav 32,188,192
s_waitcnt vmcnt(21) lgkmcnt(0)
s_barrier
Zvav 52,220,192
S_ s56,s76,s74
Ta 180,%0,49152
Ta 182,%0,51200
Mvav 52,196,172
S_ s55,s71,614400
Mvav 52,192,152
S_ m0,s51,12288
Ta 184,%0,53248
Ta 186,%0,55296
Zvav 48,220,188
buffer_load_dwordx4 a[60:63], v6, s[28:31], s56 offen offset:0 sc1
Mvav 48,196,168
S_ s56,s76,s82
Ta 188,%0,57344
Ta 190,%0,59392
Mvav 48,192,148
Zvav 44,220,184
Mvav 44,196,164
buffer_load_dwordx4 a[64:67], v6, s[28:31], s56 offen offset:0 sc1
Mvav 44,192,144
Zvav 40,220,180
Mvav 40,196,160
Mvav 40,192,140
buffer_store_dwordx4 v[122:125], v6, s[28:31], s73 offen offset:3072 sc1
Zvav 36,220,176
C_ 115,114,115
C_ 114,112,113
Mvav 36,196,156
C_ 116,116,117
C_ 117,118,119
Mvav 36,192,136
W_ 0
Mvav 56,180,196
s_nop 0
Ta 192,v1,49152
Ta 194,v1,51200
Mvav 56,184,216
Mvav 56,188,236
s_nop 0
Ta 196,v1,53248
Ta 198,v1,55296
Mvav 60,180,200
Mvav 60,184,220
s_nop 0
Ta 220,v1,57344
Ta 222,v1,59392
Mvav 60,188,240
buffer_store_dwordx4 v[114:117], v6, s[28:31], s73 offen offset:2048 sc1
Mvav 64,180,204
C_ 107,106,107
C_ 106,104,105
Mvav 64,184,224
C_ 108,108,109
C_ 109,110,111
Mvav 64,188,244
Mvav 68,180,208
Mvav 68,184,228
Mvav 68,188,248
Mvav 72,180,212
Mvav 72,184,232
buffer_store_dwordx4 v[106:109], v6, s[28:31], s73 offen offset:1024 sc1
Mvav 72,188,252
W_ 0
C_ 99,98,99
C_ 98,96,97
Mvav 92,220,252
s_nop 0
Ta 180,v2,49152
Ta 182,v2,51200
Mvav 92,196,232
C_ 100,100,101
C_ 101,102,103
Mvav 92,192,212
s_nop 0
Ta 184,v2,53248
Ta 186,v2,55296
Mvav 88,220,248
Mvav 88,196,228
s_nop 0
Ta 188,v2,57344
Ta 190,v2,59392
Mvav 88,192,208
Mvav 84,220,244
buffer_store_dwordx4 v[98:101], v6, s[28:31], s73 offen offset:0 sc1
Mvav 84,196,224
Mvav 84,192,204
.Lpad_o_4:
s_cbranch_execz .Lpad_o_8
Mvav 80,220,240
Mvav 80,196,220
S_ s83,s6,7
Mvav 80,192,200
s_and_b32 s83, s83, 7
Mvav 76,220,236
s_lshl_b32 s83, s83, 2
Mvav 76,196,216
v_cmp_eq_u32_e64 s[36:37], s83, v8
Mvav 76,192,196
W_ 0
v_mov_b32 v10, 0x7fff0000
Mvav 16,180,196
s_nop 0
Ta 192,v3,49152
Ta 194,v3,51200
Mvav 16,184,216
L_ v7,24,s55,0
Mvav 16,188,236
S_ s55,s71,624640
Ta 196,v3,53248
Ta 198,v3,55296
Mvav 20,180,200
S_ m0,s51,13312
v_cndmask_b32_e64 v10, v10, v9, s[36:37]
Mvav 20,184,220
s_nop 0
Ta 220,v3,57344
Ta 222,v3,59392
Mvav 20,188,240
L_ v7,24,s55,0
Mvav 24,180,204
S_ s55,s71,634880
Mvav 24,184,224
S_ m0,s51,14336
Mvav 24,188,244
Mvav 28,180,208
L_ v7,24,s55,0
Mvav 28,184,228
S_ s55,s71,645120
Mvav 28,188,248
S_ m0,s51,15360
Mvav 32,180,212
Mvav 32,184,232
L_ v7,24,s55,0
Mvav 32,188,252
s_waitcnt vmcnt(25) lgkmcnt(0)
s_barrier
S_ s55,s71,655360
Mvav 52,220,252
S_ m0,s51,16384
Ta 180,%0,36864
Ta 182,%0,38912
Mvav 52,196,232
Mvav 52,192,212
s_nop 0
Ta 184,%0,40960
Ta 186,%0,43008
Mvav 48,220,248
buffer_store_dword a255, v10, s[92:95], 0 offen offset:384 sc1
Mvav 48,196,228
s_nop 0
Ta 188,%0,45056
Ta 190,%0,47104
Mvav 48,192,208
Mvav 44,220,244
Mvav 44,196,224
L_ v7,24,s55,0
Mvav 44,192,204
S_ s55,s71,665600
Mvav 40,220,240
S_ m0,s51,17408
Mvav 40,196,220
Mvav 40,192,200
L_ v7,24,s55,0
Mvav 36,220,236
S_ s55,s71,0
Mvav 36,196,216
S_ m0,s51,49152
Mvav 36,192,196
W_ 0
Mvaa 56,180,0
s_nop 0
Ta 192,v1,36864
Ta 194,v1,38912
Mvaa 56,184,40
L_ v7,24,s55,0
Mvaa 56,188,80
S_ s55,s71,10240
Ta 196,v1,40960
Ta 198,v1,43008
Mvaa 60,180,4
S_ m0,s51,50176
Mvaa 60,184,44
s_nop 0
Ta 220,v1,45056
Ta 222,v1,47104
Mvaa 60,188,84
L_ v7,24,s55,0
Mvaa 64,180,8
S_ s55,s71,20480
Mvaa 64,184,48
S_ m0,s51,51200
Mvaa 64,188,88
Mvaa 68,180,12
L_ v7,24,s55,0
Mvaa 68,184,52
S_ s55,s71,30720
Mvaa 68,188,240
S_ m0,s51,52224
Mvaa 72,180,16
Mvaa 72,184,56
L_ v7,24,s55,0
Mvaa 72,188,244
W_ 0
S_ s55,s71,40960
Mvaa 92,220,244
S_ m0,s51,53248
Ta 180,v2,36864
Ta 182,v2,38912
Mvaa 92,196,56
Mvaa 92,192,16
s_nop 0
Ta 184,v2,40960
Ta 186,v2,43008
Mvaa 88,220,240
L_ v7,24,s55,0
Mvaa 88,196,52
S_ s55,s71,51200
Ta 188,v2,45056
Ta 190,v2,47104
Mvaa 88,192,12
S_ m0,s51,54272
Mvaa 84,220,88
Mvaa 84,196,48
L_ v7,24,s55,0
Mvaa 84,192,8
Mvaa 80,220,84
Mvaa 80,196,44
Mvaa 80,192,4
buffer_load_dword a74, v4, s[24:27], s85 offen
Mvaa 76,220,80
Mvaa 76,196,40
Mvaa 76,192,0
W_ 0
Mvaa 16,180,0
s_nop 0
Ta 192,v3,36864
Ta 194,v3,38912
Mvaa 16,184,40
Mvaa 16,188,80
s_nop 0
Ta 196,v3,40960
Ta 198,v3,43008
Mvaa 20,180,4
Mvaa 20,184,44
s_nop 0
Ta 220,v3,45056
Ta 222,v3,47104
Mvaa 20,188,84
Mvaa 24,180,8
Mvaa 24,184,48
Mvaa 24,188,88
Mvaa 28,180,12
Mvaa 28,184,52
Mvaa 28,188,240
Mvaa 32,180,16
Mvaa 32,184,56
Mvaa 32,188,244
s_waitcnt vmcnt(27) lgkmcnt(0)
s_barrier
Mvaa 52,220,244
s_nop 0
Ta 180,%0,24576
Ta 182,%0,26624
Mvaa 52,196,56
Mvaa 52,192,16
s_nop 0
Ta 184,%0,28672
Ta 186,%0,30720
Mvaa 48,220,240
Mvaa 48,196,52
s_nop 0
Ta 188,%0,32768
Ta 190,%0,34816
Mvaa 48,192,12
Mvaa 44,220,88
Mvaa 44,196,48
Mvaa 44,192,8
Mvaa 40,220,84
Mvaa 40,196,44
Mvaa 40,192,4
Mvaa 36,220,80
Mvaa 36,196,40
Mvaa 36,192,0
W_ 0
Mvaa 56,180,120
S_ s81,s6,2
Ta 192,v1,24576
Ta 194,v1,26624
Mvaa 56,184,160
s_and_b32 s81, s81, 7
Mvaa 56,188,200
s_mul_i32 s81, s81, 20480
Ta 196,v1,28672
Ta 198,v1,30720
Mvaa 60,180,124
s_mov_b32 m0, s52
S_ s73,s81,s78
Mvaa 60,184,164
S_ s73,s73,4096
Ta 220,v1,32768
Ta 222,v1,34816
Mvaa 60,188,204
s_nop 0
Y_ 56,0
Y_ 57,256
Mvaa 64,180,128
s_nop 0
Y_ 58,512
Y_ 59,768
Mvaa 64,184,168
Mvaa 64,188,208
s_nop 0
Y_ 60,1024
Y_ 61,1280
Mvaa 68,180,132
s_nop 0
Y_ 62,1536
Y_ 63,1792
Mvaa 68,184,172
Mvaa 68,188,212
s_nop 0
Y_ 64,2048
Y_ 65,2304
Mvaa 72,180,136
s_nop 0
Y_ 66,2560
Y_ 67,2816
Mvaa 72,184,176
Mvaa 72,188,216
W_ 12
Y_ 68,3072
Y_ 69,3328
Mvaa 92,220,216
s_nop 0
Ta 180,v2,24576
Ta 182,v2,26624
Mvaa 92,196,176
s_nop 0
Y_ 70,3584
Y_ 71,3840
Mvaa 92,192,136
s_nop 0
Ta 184,v2,28672
Ta 186,v2,30720
Mvaa 88,220,212
s_nop 0
Y_ 72,4096
Y_ 73,4352
Mvaa 88,196,172
S_ s83,s6,1
Ta 188,v2,32768
Ta 190,v2,34816
Mvaa 88,192,132
s_and_b32 s83, s83, 7
Y_ 74,4608
Y_ 75,4864
Mvaa 84,220,208
s_lshl_b32 s83, s83, 2
Y_ 92,9216
Y_ 93,9472
Mvaa 84,196,168
s_nop 0
Y_ 94,9728
Y_ 95,9984
Mvaa 84,192,128
s_nop 0
Y_ 88,8192
Y_ 89,8448
Mvaa 80,220,204
s_nop 0
Y_ 90,8704
Y_ 91,8960
Mvaa 80,196,164
v_cmp_eq_u32_e64 s[36:37], s83, v8
Mvaa 80,192,124
s_nop 0
Y_ 84,7168
Y_ 85,7424
Mvaa 76,220,200
s_nop 0
Y_ 86,7680
Y_ 87,7936
Mvaa 76,196,160
v_mov_b32 v10, 0x7fff0000
Mvaa 76,192,120
W_ 14
Y_ 80,6144
Y_ 81,6400
Mvaa 16,180,120
s_nop 0
Ta 192,v3,24576
Ta 194,v3,26624
Mvaa 16,184,160
s_nop 0
Y_ 82,6656
Y_ 83,6912
Mvaa 16,188,200
s_nop 0
Ta 196,v3,28672
Ta 198,v3,30720
Mvaa 20,180,124
s_nop 0
Y_ 76,5120
Y_ 77,5376
Mvaa 20,184,164
s_nop 0
Ta 220,v3,32768
Ta 222,v3,34816
Mvaa 20,188,204
s_nop 0
Y_ 78,5632
Y_ 79,5888
Mvaa 24,180,128
v_cndmask_b32_e64 v10, v10, v9, s[36:37]
Mvaa 24,184,168
Mvaa 24,188,208
s_waitcnt vmcnt(14) lgkmcnt(0)
s_barrier
Mvaa 28,180,132
buffer_store_dword a255, v10, s[92:95], 0 offen offset:384 sc1
Mvaa 28,184,172
S_ s55,s71,122880
ds_read2st64_b32 v[96:97], v5 offset0:0 offset1:1
Mvaa 28,188,212
S_ m0,s51,36864
ds_read2st64_b32 v[98:99], v5 offset0:2 offset1:3
Mvaa 32,180,136
ds_read2st64_b32 v[104:105], v5 offset0:4 offset1:5
Mvaa 32,184,176
L_ v7,24,s55,0
Mvaa 32,188,216
S_ s55,s71,133120
ds_read2st64_b32 v[106:107], v5 offset0:6 offset1:7
Mvaa 52,220,216
S_ m0,s51,37888
Ta 180,%0,0
Ta 182,%0,2048
Mvaa 52,196,176
ds_read2st64_b32 v[112:113], v5 offset0:8 offset1:9
Mvaa 52,192,136
W_ 5
Ta 184,%0,4096
Ta 186,%0,6144
Mvaa 48,220,212
L_ v7,24,s55,0
Mvaa 48,196,172
S_ s55,s71,143360
Ta 188,%0,8192
Ta 190,%0,10240
Mvaa 48,192,132
S_ m0,s51,38912
ds_read2st64_b32 v[114:115], v5 offset0:10 offset1:11
A_ v96,v16,v96
Mvaa 44,220,208
ds_read2st64_b32 v[120:121], v5 offset0:12 offset1:13
A_ v97,v17,v97
Mvaa 44,196,168
L_ v7,24,s55,0
Mvaa 44,192,128
S_ s55,s71,153600
ds_read2st64_b32 v[122:123], v5 offset0:14 offset1:15
A_ v98,v18,v98
Mvaa 40,220,204
S_ m0,s51,39936
ds_read2st64_b32 v[128:129], v5 offset0:16 offset1:17
A_ v99,v19,v99
Mvaa 40,196,164
W_ 11
ds_read2st64_b32 v[130:131], v5 offset0:18 offset1:19
A_ v104,v20,v104
Mvaa 40,192,124
L_ v7,24,s55,0
Mvaa 36,220,200
S_ s55,s71,163840
ds_read2st64_b32 v[132:133], v5 offset0:36 offset1:37
A_ v105,v21,v105
Mvaa 36,196,160
S_ m0,s51,40960
ds_read2st64_b32 v[134:135], v5 offset0:38 offset1:39
A_ v106,v22,v106
Mvaa 36,192,120
ds_read2st64_b32 v[124:125], v5 offset0:32 offset1:33
A_ v107,v23,v107
s_waitcnt vmcnt(23)
Zaaa 224,70,60
L_ v7,24,s55,0
Zaaa 228,68,60
W_ 7
ds_read2st64_b32 v[126:127], v5 offset0:34 offset1:35
A_ v112,v24,v112
Zaaa 232,70,64
S_ s55,s71,174080
ds_read2st64_b32 v[116:117], v5 offset0:28 offset1:29
A_ v113,v25,v113
Zaaa 236,68,64
ds_read2st64_b32 v[118:119], v5 offset0:30 offset1:31
A_ v114,v26,v114
Zvav 56,180,136
S_ m0,s51,41984
Ta 192,v1,0
Ta 194,v1,2048
Mvav 56,184,156
L_ v7,24,s55,0
Mvav 56,188,176
S_ s79,s6,4
Ta 196,v1,4096
Ta 198,v1,6144
Zvav 60,180,140
s_and_b32 s79, s79, 7
ds_read2st64_b32 v[108:109], v5 offset0:24 offset1:25
A_ v115,v27,v115
Mvav 60,184,160
W_ 13
Ta 220,v1,8192
Ta 222,v1,10240
Mvav 60,188,180
s_lshl_b32 s79, s79, 7
ds_read2st64_b32 v[110:111], v5 offset0:26 offset1:27
A_ v120,v28,v120
Zvav 64,180,144
S_ s71,s72,s79
ds_read2st64_b32 v[100:101], v5 offset0:20 offset1:21
A_ v121,v29,v121
Mvav 64,184,164
S_ s55,s71,491520
ds_read2st64_b32 v[102:103], v5 offset0:22 offset1:23
A_ v122,v30,v122
Mvav 64,188,184
S_ m0,s51,24576
A_ v123,v31,v123
Zvav 68,180,148
L_ v7,24,s55,0
Mvav 68,184,168
W_ 14
A_ v128,v32,v128
A_ v129,v33,v129
Mvav 68,188,188
S_ s55,s71,501760
A_ v130,v34,v130
A_ v131,v35,v131
Zvav 72,180,152
A_ v132,v52,v132
A_ v133,v53,v133
Mvav 72,184,172
S_ m0,s51,25600
A_ v134,v54,v134
A_ v135,v55,v135
Mvav 72,188,192
W_ 3
A_ v124,v48,v124
Zvav 92,220,192
s_nop 0
Ta 180,v2,0
Ta 182,v2,2048
Mvav 92,196,172
L_ v7,24,s55,0
Mvav 92,192,152
S_ s55,s71,512000
Ta 184,v2,4096
Ta 186,v2,6144
Zvav 88,220,188
S_ m0,s51,26624
A_ v125,v49,v125
A_ v126,v50,v126
Mvav 88,196,168
s_nop 0
Ta 188,v2,8192
Ta 190,v2,10240
Mvav 88,192,148
L_ v7,24,s55,0
Zvav 84,220,184
S_ s55,s71,522240
A_ v127,v51,v127
C_ 131,130,131
Mvav 84,196,164
A_ v116,v44,v116
A_ v117,v45,v117
Mvav 84,192,144
S_ m0,s51,27648
A_ v118,v46,v118
A_ v119,v47,v119
Zvav 80,220,180
L_ v7,24,s55,0
Mvav 80,196,160
W_ 8
A_ v108,v40,v108
A_ v109,v41,v109
Mvav 80,192,140
S_ s55,s71,532480
A_ v110,v42,v110
A_ v111,v43,v111
Zvav 76,220,176
W_ 6
A_ v100,v36,v100
A_ v101,v37,v101
Mvav 76,196,156
S_ m0,s51,28672
A_ v102,v38,v102
A_ v103,v39,v103
Mvav 76,192,136
W_ 0
C_ 130,128,129
C_ 132,132,133
Zvav 16,180,136
S_ s85,s6,6
Ta 192,v3,0
Ta 194,v3,2048
Mvav 16,184,156
L_ v7,24,s55,0
Mvav 16,188,176
S_ s55,s71,542720
Ta 196,v3,4096
Ta 198,v3,6144
Zvav 20,180,140
S_ m0,s51,29696
C_ 133,134,135
s_and_b32 s85, s85, 7
Mvav 20,184,160
s_lshl_b32 s85, s85, 7
Ta 220,v3,8192
Ta 222,v3,10240
Mvav 20,188,180
buffer_store_dwordx4 v[130:133], v6, s[28:31], s73 offen offset:0 sc1
Zvav 24,180,144
s_sub_u32 s73, s73, 4096
S_ s85,s85,s87
Mvav 24,184,164
C_ 123,122,123
C_ 122,120,121
Mvav 24,188,184
C_ 124,124,125
C_ 125,126,127
Zvav 28,180,148
Mvav 28,184,168
Mvav 28,188,188
Zvav 32,180,152
Mvav 32,184,172
buffer_store_dwordx4 v[122:125], v6, s[28:31], s73 offen offset:3072 sc1
Mvav 32,188,192
s_waitcnt vmcnt(21) lgkmcnt(0)
s_barrier
C_ 115,114,115
Zvav 52,220,192
s_nop 0
Ta 180,%0,12288
Ta 182,%0,14336
Mvav 52,196,172
C_ 114,112,113
C_ 116,116,117
Mvav 52,192,152
s_nop 0
Ta 184,%0,16384
Ta 186,%0,18432
Zvav 48,220,188
C_ 117,118,119
Mvav 48,196,168
s_nop 0
Ta 188,%0,20480
Ta 190,%0,22528
Mvav 48,192,148
Zvav 44,220,184
buffer_store_dwordx4 v[114:117], v6, s[28:31], s73 offen offset:2048 sc1
Mvav 44,196,164
.Lpad_o_5:
s_cbranch_execz .Lpad_o_9
C_ 107,106,107
C_ 106,104,105
Mvav 44,192,144
C_ 108,108,109
C_ 109,110,111
Zvav 40,220,180
Mvav 40,196,160
Mvav 40,192,140
Zvav 36,220,176
Mvav 36,196,156
Mvav 36,192,136
W_ 0
Mvav 56,180,196
s_nop 0
Ta 192,v1,12288
Ta 194,v1,14336
Mvav 56,184,216
buffer_store_dwordx4 v[106:109], v6, s[28:31], s73 offen offset:1024 sc1
Mvav 56,188,236
s_nop 0
Ta 196,v1,16384
Ta 198,v1,18432
Mvav 60,180,200
C_ 99,98,99
C_ 98,96,97
Mvav 60,184,220
s_nop 0
Ta 220,v1,20480
Ta 222,v1,22528
Mvav 60,188,240
C_ 100,100,101
C_ 101,102,103
Mvav 64,180,204
Mvav 64,184,224
Mvav 64,188,244
Mvav 68,180,208
buffer_store_dwordx4 v[98:101], v6, s[28:31], s73 offen offset:0 sc1
Mvav 68,184,228
Mvav 68,188,248
Mvav 72,180,212
Mvav 72,184,232
Mvav 72,188,252
W_ 0
Mvav 92,220,252
s_nop 0
Ta 180,v2,12288
Ta 182,v2,14336
Mvav 92,196,232
Mvav 92,192,212
s_nop 0
Ta 184,v2,16384
Ta 186,v2,18432
Mvav 88,220,248
L_ v7,24,s55,0
Mvav 88,196,228
S_ s55,s71,614400
Ta 188,v2,20480
Ta 190,v2,22528
Mvav 88,192,208
S_ m0,s51,0
Mvav 84,220,244
Mvav 84,196,224
L_ v7,24,s55,0
Mvav 84,192,204
S_ s55,s71,624640
Mvav 80,220,240
S_ m0,s51,1024
Mvav 80,196,220
Mvav 80,192,200
L_ v7,24,s55,0
Mvav 76,220,236
S_ s55,s71,634880
Mvav 76,196,216
S_ m0,s51,2048
Mvav 76,192,196
W_ 0
Mvav 16,180,196
s_nop 0
Ta 192,v3,12288
Ta 194,v3,14336
Mvav 16,184,216
L_ v7,24,s55,0
Mvav 16,188,236
S_ s55,s71,645120
Ta 196,v3,16384
Ta 198,v3,18432
Mvav 20,180,200
S_ m0,s51,3072
Mvav 20,184,220
s_nop 0
Ta 220,v3,20480
Ta 222,v3,22528
Mvav 20,188,240
L_ v7,24,s55,0
Mvav 24,180,204
S_ s55,s71,655360
Mvav 24,184,224
S_ m0,s51,4096
Mvav 24,188,244
Mvav 28,180,208
L_ v7,24,s55,0
Mvav 28,184,228
S_ s55,s71,665600
Mvav 28,188,248
S_ m0,s51,5120
Mvav 32,180,212
Mvav 32,184,232
L_ v7,24,s55,0
Mvav 32,188,252
s_waitcnt vmcnt(25) lgkmcnt(0)
s_barrier
Mvav 52,220,252
S_ s55,s71,0
Ta 180,%0,49152
Ta 182,%0,51200
Mvav 52,196,232
S_ m0,s51,12288
Mvav 52,192,212
s_nop 0
Ta 184,%0,53248
Ta 186,%0,55296
Mvav 48,220,248
L_ v7,24,s55,0
Mvav 48,196,228
S_ s55,s71,10240
Ta 188,%0,57344
Ta 190,%0,59392
Mvav 48,192,208
S_ m0,s51,13312
Mvav 44,220,244
Mvav 44,196,224
L_ v7,24,s55,0
Mvav 44,192,204
S_ s55,s71,20480
Mvav 40,220,240
S_ m0,s51,14336
Mvav 40,196,220
Mvav 40,192,200
L_ v7,24,s55,0
Mvav 36,220,236
S_ s55,s71,30720
Mvav 36,196,216
S_ m0,s51,15360
Mvav 36,192,196
W_ 0
Mvaa 56,180,0
s_nop 0
Ta 192,v1,49152
Ta 194,v1,51200
Mvaa 56,184,40
L_ v7,24,s55,0
Mvaa 56,188,80
S_ s55,s71,40960
Ta 196,v1,53248
Ta 198,v1,55296
Mvaa 60,180,4
S_ m0,s51,16384
Mvaa 60,184,44
s_nop 0
Ta 220,v1,57344
Ta 222,v1,59392
Mvaa 60,188,84
L_ v7,24,s55,0
Mvaa 64,180,8
S_ s55,s71,51200
Mvaa 64,184,48
S_ m0,s51,17408
Mvaa 64,188,88
Mvaa 68,180,12
L_ v7,24,s55,0
Mvaa 68,184,52
Mvaa 68,188,240
Mvaa 72,180,16
Mvaa 72,184,56
buffer_load_dword a74, v4, s[24:27], s85 offen
Mvaa 72,188,244
W_ 0
Mvaa 92,220,244
s_nop 0
Ta 180,v2,49152
Ta 182,v2,51200
Mvaa 92,196,56
Mvaa 92,192,16
s_nop 0
Ta 184,v2,53248
Ta 186,v2,55296
Mvaa 88,220,240
Mvaa 88,196,52
s_nop 0
Ta 188,v2,57344
Ta 190,v2,59392
Mvaa 88,192,12
Mvaa 84,220,88
Mvaa 84,196,48
Mvaa 84,192,8
Mvaa 80,220,84
Mvaa 80,196,44
Mvaa 80,192,4
Mvaa 76,220,80
Mvaa 76,196,40
Mvaa 76,192,0
W_ 0
Mvaa 16,180,0
s_nop 0
Ta 192,v3,49152
Ta 194,v3,51200
Mvaa 16,184,40
Mvaa 16,188,80
s_nop 0
Ta 196,v3,53248
Ta 198,v3,55296
Mvaa 20,180,4
Mvaa 20,184,44
s_nop 0
Ta 220,v3,57344
Ta 222,v3,59392
Mvaa 20,188,84
Mvaa 24,180,8
Mvaa 24,184,48
Mvaa 24,188,88
Mvaa 28,180,12
Mvaa 28,184,52
Mvaa 28,188,240
Mvaa 32,180,16
Mvaa 32,184,56
Mvaa 32,188,244
s_waitcnt vmcnt(24) lgkmcnt(0)
s_barrier
Mvaa 52,220,244
s_nop 0
Ta 180,%0,36864
Ta 182,%0,38912
Mvaa 52,196,56
Mvaa 52,192,16
s_nop 0
Ta 184,%0,40960
Ta 186,%0,43008
Mvaa 48,220,240
Mvaa 48,196,52
s_nop 0
Ta 188,%0,45056
Ta 190,%0,47104
Mvaa 48,192,12
Mvaa 44,220,88
Mvaa 44,196,48
Mvaa 44,192,8
Mvaa 40,220,84
Mvaa 40,196,44
Mvaa 40,192,4
Mvaa 36,220,80
Mvaa 36,196,40
Mvaa 36,192,0
W_ 0
Mvaa 56,180,120
S_ s81,s6,3
Ta 192,v1,36864
Ta 194,v1,38912
Mvaa 56,184,160
s_and_b32 s81, s81, 7
Mvaa 56,188,200
s_mul_i32 s81, s81, 20480
Ta 196,v1,40960
Ta 198,v1,43008
Mvaa 60,180,124
s_mov_b32 m0, s52
S_ s73,s81,s78
Mvaa 60,184,164
S_ s73,s73,4096
Ta 220,v1,45056
Ta 222,v1,47104
Mvaa 60,188,204
s_nop 0
Y_ 56,0
Y_ 57,256
Mvaa 64,180,128
s_nop 0
Y_ 58,512
Y_ 59,768
Mvaa 64,184,168
Mvaa 64,188,208
s_nop 0
Y_ 60,1024
Y_ 61,1280
Mvaa 68,180,132
s_nop 0
Y_ 62,1536
Y_ 63,1792
Mvaa 68,184,172
Mvaa 68,188,212
s_nop 0
Y_ 64,2048
Y_ 65,2304
Mvaa 72,180,136
s_nop 0
Y_ 66,2560
Y_ 67,2816
Mvaa 72,184,176
Mvaa 72,188,216
W_ 12
Y_ 68,3072
Y_ 69,3328
Mvaa 92,220,216
s_nop 0
Ta 180,v2,36864
Ta 182,v2,38912
Mvaa 92,196,176
s_nop 0
Y_ 70,3584
Y_ 71,3840
Mvaa 92,192,136
s_nop 0
Ta 184,v2,40960
Ta 186,v2,43008
Mvaa 88,220,212
s_nop 0
Y_ 72,4096
Y_ 73,4352
Mvaa 88,196,172
S_ s83,s6,2
Ta 188,v2,45056
Ta 190,v2,47104
Mvaa 88,192,132
s_and_b32 s83, s83, 7
Y_ 74,4608
Y_ 75,4864
Mvaa 84,220,208
s_lshl_b32 s83, s83, 2
Y_ 92,9216
Y_ 93,9472
Mvaa 84,196,168
s_nop 0
Y_ 94,9728
Y_ 95,9984
Mvaa 84,192,128
s_nop 0
Y_ 88,8192
Y_ 89,8448
Mvaa 80,220,204
s_nop 0
Y_ 90,8704
Y_ 91,8960
Mvaa 80,196,164
v_cmp_eq_u32_e64 s[36:37], s83, v8
Mvaa 80,192,124
s_sub_u32 s75, s6, 1
Y_ 84,7168
Y_ 85,7424
Mvaa 76,220,200
s_and_b32 s75, s75, 7
Y_ 86,7680
Y_ 87,7936
Mvaa 76,196,160
v_mov_b32 v10, 0x7fff0000
Mvaa 76,192,120
W_ 14
Y_ 80,6144
Y_ 81,6400
Mvaa 16,180,120
s_nop 0
Ta 192,v3,36864
Ta 194,v3,38912
Mvaa 16,184,160
s_nop 0
Y_ 82,6656
Y_ 83,6912
Mvaa 16,188,200
s_nop 0
Ta 196,v3,40960
Ta 198,v3,43008
Mvaa 20,180,124
s_nop 0
Y_ 76,5120
Y_ 77,5376
Mvaa 20,184,164
s_nop 0
Ta 220,v3,45056
Ta 222,v3,47104
Mvaa 20,188,204
s_nop 0
Y_ 78,5632
Y_ 79,5888
Mvaa 24,180,128
buffer_load_dword v11, v8, s[92:95], s96 offen offset:384 sc1
Mvaa 24,184,168
v_cndmask_b32_e64 v10, v10, v9, s[36:37]
Mvaa 24,188,208
s_waitcnt vmcnt(14) lgkmcnt(0)
s_barrier
Mvaa 28,180,132
S_ s55,s71,122880
ds_read2st64_b32 v[96:97], v5 offset0:0 offset1:1
Mvaa 28,184,172
buffer_store_dword a255, v10, s[92:95], 0 offen offset:384 sc1
Mvaa 28,188,212
S_ m0,s51,49152
ds_read2st64_b32 v[98:99], v5 offset0:2 offset1:3
Mvaa 32,180,136
ds_read2st64_b32 v[104:105], v5 offset0:4 offset1:5
)A"
R"A(Mvaa 32,184,176
ds_read2st64_b32 v[106:107], v5 offset0:6 offset1:7
Mvaa 32,188,216
L_ v7,24,s55,0
Mvaa 52,220,216
S_ s55,s71,133120
Ta 180,%0,24576
Ta 182,%0,26624
Mvaa 52,196,176
S_ m0,s51,50176
ds_read2st64_b32 v[112:113], v5 offset0:8 offset1:9
Mvaa 52,192,136
W_ 5
Ta 184,%0,28672
Ta 186,%0,30720
Mvaa 48,220,212
L_ v7,24,s55,0
Mvaa 48,196,172
S_ s55,s71,143360
Ta 188,%0,32768
Ta 190,%0,34816
Mvaa 48,192,132
S_ m0,s51,51200
ds_read2st64_b32 v[114:115], v5 offset0:10 offset1:11
A_ v96,v16,v96
Mvaa 44,220,208
ds_read2st64_b32 v[120:121], v5 offset0:12 offset1:13
A_ v97,v17,v97
Mvaa 44,196,168
L_ v7,24,s55,0
Mvaa 44,192,128
S_ s55,s71,153600
ds_read2st64_b32 v[122:123], v5 offset0:14 offset1:15
A_ v98,v18,v98
Mvaa 40,220,204
S_ m0,s51,52224
ds_read2st64_b32 v[128:129], v5 offset0:16 offset1:17
A_ v99,v19,v99
Mvaa 40,196,164
W_ 11
ds_read2st64_b32 v[130:131], v5 offset0:18 offset1:19
A_ v104,v20,v104
Mvaa 40,192,124
L_ v7,24,s55,0
Mvaa 36,220,200
S_ s55,s71,163840
ds_read2st64_b32 v[132:133], v5 offset0:36 offset1:37
A_ v105,v21,v105
Mvaa 36,196,160
S_ m0,s51,53248
ds_read2st64_b32 v[134:135], v5 offset0:38 offset1:39
A_ v106,v22,v106
Mvaa 36,192,120
W_ 7
ds_read2st64_b32 v[124:125], v5 offset0:32 offset1:33
A_ v107,v23,v107
Zvav 56,180,136
W_ 7
Ta 192,v1,24576
Ta 194,v1,26624
Mvav 56,184,156
L_ v7,24,s55,0
Mvav 56,188,176
S_ s55,s71,174080
Ta 196,v1,28672
Ta 198,v1,30720
Zvav 60,180,140
S_ m0,s51,54272
ds_read2st64_b32 v[126:127], v5 offset0:34 offset1:35
A_ v112,v24,v112
Mvav 60,184,160
s_nop 0
Ta 220,v1,32768
Ta 222,v1,34816
Mvav 60,188,180
L_ v7,24,s55,0
Zvav 64,180,144
S_ s79,s6,5
ds_read2st64_b32 v[116:117], v5 offset0:28 offset1:29
A_ v113,v25,v113
Mvav 64,184,164
s_and_b32 s79, s79, 7
ds_read2st64_b32 v[118:119], v5 offset0:30 offset1:31
A_ v114,v26,v114
Mvav 64,188,184
s_lshl_b32 s79, s79, 7
ds_read2st64_b32 v[108:109], v5 offset0:24 offset1:25
A_ v115,v27,v115
Zvav 68,180,148
W_ 14
ds_read2st64_b32 v[110:111], v5 offset0:26 offset1:27
A_ v120,v28,v120
Mvav 68,184,168
S_ s71,s72,s79
ds_read2st64_b32 v[100:101], v5 offset0:20 offset1:21
A_ v121,v29,v121
Mvav 68,188,188
S_ s55,s71,491520
ds_read2st64_b32 v[102:103], v5 offset0:22 offset1:23
A_ v122,v30,v122
Zvav 72,180,152
s_waitcnt vmcnt(7)
A_ v123,v31,v123
S_ m0,s51,36864
Mvav 72,184,172
L_ v7,24,s55,0
Mvav 72,188,192
W_ 6
v_cmp_eq_u32 vcc, s62, v11
Zvav 92,220,192
s_mul_i32 s76, s75, 204800
Ta 180,v2,24576
Ta 182,v2,26624
Mvav 92,196,172
s_bitcmp1_b32 vcc_lo, s75
s_cbranch_scc0 .Lspin0
.Lspun0:
A_ v128,v32,v128
Mvav 92,192,152
S_ s56,s76,s84
Ta 184,v2,28672
Ta 186,v2,30720
Zvav 88,220,188
buffer_load_dwordx4 a[20:23], v6, s[28:31], s56 offen offset:0 sc1
Mvav 88,196,168
S_ s55,s71,501760
Ta 188,v2,32768
Ta 190,v2,34816
Mvav 88,192,148
S_ m0,s51,37888
A_ v129,v33,v129
A_ v130,v34,v130
Zvav 84,220,184
S_ s85,s6,7
A_ v131,v35,v131
Mvav 84,196,164
buffer_load_dwordx4 a[24:27], v6, s[28:31], s56 offen offset:1024 sc1
Mvav 84,192,144
A_ v132,v52,v132
A_ v133,v53,v133
Zvav 80,220,180
s_and_b32 s85, s85, 7
A_ v134,v54,v134
A_ v135,v55,v135
Mvav 80,196,160
A_ v124,v48,v124
A_ v125,v49,v125
Mvav 80,192,140
buffer_load_dwordx4 a[28:31], v6, s[28:31], s56 offen offset:2048 sc1
Zvav 76,220,176
s_lshl_b32 s85, s85, 7
A_ v126,v50,v126
A_ v127,v51,v127
Mvav 76,196,156
W_ 10
A_ v116,v44,v116
A_ v117,v45,v117
Mvav 76,192,136
W_ 0
A_ v118,v46,v118
A_ v119,v47,v119
Zvav 16,180,136
Ta 192,v3,24576
Ta 194,v3,26624
Mvav 16,184,156
buffer_load_dwordx4 a[32:35], v6, s[28:31], s56 offen offset:3072 sc1
Mvav 16,188,176
S_ s56,s56,4096
Ta 196,v3,28672
Ta 198,v3,30720
Zvav 20,180,140
S_ s85,s85,s87
A_ v108,v40,v108
A_ v109,v41,v109
Mvav 20,184,160
.Lpad_o_6:
s_cbranch_execz .Lpad_o_10
Ta 220,v3,32768
Ta 222,v3,34816
Mvav 20,188,180
buffer_load_dwordx4 a[36:39], v6, s[28:31], s56 offen offset:0 sc1
Zvav 24,180,144
S_ s56,s76,s74
A_ v110,v42,v110
A_ v111,v43,v111
Mvav 24,184,164
A_ v100,v36,v100
A_ v101,v37,v101
Mvav 24,188,184
A_ v102,v38,v102
A_ v103,v39,v103
Zvav 28,180,148
buffer_load_dwordx4 a[60:63], v6, s[28:31], s56 offen offset:0 sc1
Mvav 28,184,168
S_ s56,s76,s82
C_ 131,130,131
C_ 130,128,129
Mvav 28,188,188
C_ 132,132,133
C_ 133,134,135
Zvav 32,180,152
Mvav 32,184,172
buffer_load_dwordx4 a[64:67], v6, s[28:31], s56 offen offset:0 sc1
Mvav 32,188,192
s_waitcnt vmcnt(23) lgkmcnt(0)
s_barrier
Zvav 52,220,192
s_nop 0
Ta 180,%0,0
Ta 182,%0,2048
Mvav 52,196,172
Mvav 52,192,152
s_nop 0
Ta 184,%0,4096
Ta 186,%0,6144
Zvav 48,220,188
buffer_store_dwordx4 v[130:133], v6, s[28:31], s73 offen offset:0 sc1
Mvav 48,196,168
s_sub_u32 s73, s73, 4096
Ta 188,%0,8192
Ta 190,%0,10240
Mvav 48,192,148
C_ 123,122,123
C_ 122,120,121
Zvav 44,220,184
C_ 124,124,125
C_ 125,126,127
Mvav 44,196,164
Mvav 44,192,144
Zvav 40,220,180
Mvav 40,196,160
Mvav 40,192,140
buffer_store_dwordx4 v[122:125], v6, s[28:31], s73 offen offset:3072 sc1
Zvav 36,220,176
C_ 115,114,115
C_ 114,112,113
Mvav 36,196,156
C_ 116,116,117
C_ 117,118,119
Mvav 36,192,136
W_ 0
Mvav 56,180,196
s_nop 0
Ta 192,v1,0
Ta 194,v1,2048
Mvav 56,184,216
Mvav 56,188,236
s_nop 0
Ta 196,v1,4096
Ta 198,v1,6144
Mvav 60,180,200
Mvav 60,184,220
s_nop 0
Ta 220,v1,8192
Ta 222,v1,10240
Mvav 60,188,240
buffer_store_dwordx4 v[114:117], v6, s[28:31], s73 offen offset:2048 sc1
Mvav 64,180,204
C_ 107,106,107
C_ 106,104,105
Mvav 64,184,224
C_ 108,108,109
C_ 109,110,111
Mvav 64,188,244
Mvav 68,180,208
Mvav 68,184,228
Mvav 68,188,248
Mvav 72,180,212
Mvav 72,184,232
buffer_store_dwordx4 v[106:109], v6, s[28:31], s73 offen offset:1024 sc1
Mvav 72,188,252
W_ 0
C_ 99,98,99
C_ 98,96,97
Mvav 92,220,252
s_nop 0
Ta 180,v2,0
Ta 182,v2,2048
Mvav 92,196,232
C_ 100,100,101
C_ 101,102,103
Mvav 92,192,212
s_nop 0
Ta 184,v2,4096
Ta 186,v2,6144
Mvav 88,220,248
Mvav 88,196,228
s_nop 0
Ta 188,v2,8192
Ta 190,v2,10240
Mvav 88,192,208
Mvav 84,220,244
buffer_store_dwordx4 v[98:101], v6, s[28:31], s73 offen offset:0 sc1
Mvav 84,196,224
Mvav 84,192,204
Mvav 80,220,240
Mvav 80,196,220
Mvav 80,192,200
Mvav 76,220,236
Mvav 76,196,216
Mvav 76,192,196
W_ 0
Mvav 16,180,196
s_nop 0
Ta 192,v3,0
Ta 194,v3,2048
Mvav 16,184,216
L_ v7,24,s55,0
Mvav 16,188,236
S_ s55,s71,512000
Ta 196,v3,4096
Ta 198,v3,6144
Mvav 20,180,200
S_ m0,s51,38912
Mvav 20,184,220
s_nop 0
Ta 220,v3,8192
Ta 222,v3,10240
Mvav 20,188,240
L_ v7,24,s55,0
Mvav 24,180,204
S_ s55,s71,522240
Mvav 24,184,224
S_ m0,s51,39936
Mvav 24,188,244
Mvav 28,180,208
L_ v7,24,s55,0
Mvav 28,184,228
S_ s55,s71,532480
Mvav 28,188,248
S_ m0,s51,40960
Mvav 32,180,212
Mvav 32,184,232
L_ v7,24,s55,0
Mvav 32,188,252
s_waitcnt vmcnt(26) lgkmcnt(0)
s_barrier
S_ s55,s71,542720
Mvav 52,220,252
S_ m0,s51,41984
Ta 180,%0,12288
Ta 182,%0,14336
Mvav 52,196,232
Mvav 52,192,212
s_nop 0
Ta 184,%0,16384
Ta 186,%0,18432
Mvav 48,220,248
L_ v7,24,s55,0
Mvav 48,196,228
S_ s55,s71,614400
Ta 188,%0,20480
Ta 190,%0,22528
Mvav 48,192,208
S_ m0,s51,24576
Mvav 44,220,244
Mvav 44,196,224
L_ v7,24,s55,0
Mvav 44,192,204
S_ s55,s71,624640
Mvav 40,220,240
S_ m0,s51,25600
Mvav 40,196,220
Mvav 40,192,200
L_ v7,24,s55,0
Mvav 36,220,236
S_ s55,s71,634880
Mvav 36,196,216
S_ m0,s51,26624
Mvav 36,192,196
W_ 0
Mvaa 56,180,0
s_nop 0
Ta 192,v1,12288
Ta 194,v1,14336
Mvaa 56,184,40
L_ v7,24,s55,0
Mvaa 56,188,80
S_ s55,s71,645120
Ta 196,v1,16384
Ta 198,v1,18432
Mvaa 60,180,4
S_ m0,s51,27648
Mvaa 60,184,44
s_nop 0
Ta 220,v1,20480
Ta 222,v1,22528
Mvaa 60,188,84
L_ v7,24,s55,0
Mvaa 64,180,8
S_ s55,s71,655360
Mvaa 64,184,48
S_ m0,s51,28672
Mvaa 64,188,88
Mvaa 68,180,12
L_ v7,24,s55,0
Mvaa 68,184,52
S_ s55,s71,665600
Mvaa 68,188,240
S_ m0,s51,29696
Mvaa 72,180,16
Mvaa 72,184,56
L_ v7,24,s55,0
Mvaa 72,188,244
W_ 0
S_ s55,s71,0
Mvaa 92,220,244
S_ m0,s51,0
Ta 180,v2,12288
Ta 182,v2,14336
Mvaa 92,196,56
Mvaa 92,192,16
s_nop 0
Ta 184,v2,16384
Ta 186,v2,18432
Mvaa 88,220,240
L_ v7,24,s55,0
Mvaa 88,196,52
S_ s55,s71,10240
Ta 188,v2,20480
Ta 190,v2,22528
Mvaa 88,192,12
S_ m0,s51,1024
Mvaa 84,220,88
Mvaa 84,196,48
L_ v7,24,s55,0
Mvaa 84,192,8
S_ s55,s71,20480
Mvaa 80,220,84
S_ m0,s51,2048
Mvaa 80,196,44
Mvaa 80,192,4
L_ v7,24,s55,0
Mvaa 76,220,80
S_ s55,s71,30720
Mvaa 76,196,40
S_ m0,s51,3072
Mvaa 76,192,0
W_ 0
Mvaa 16,180,0
s_nop 0
Ta 192,v3,12288
Ta 194,v3,14336
Mvaa 16,184,40
L_ v7,24,s55,0
Mvaa 16,188,80
S_ s55,s71,40960
Ta 196,v3,16384
Ta 198,v3,18432
Mvaa 20,180,4
S_ m0,s51,4096
Mvaa 20,184,44
s_nop 0
Ta 220,v3,20480
Ta 222,v3,22528
Mvaa 20,188,84
L_ v7,24,s55,0
Mvaa 24,180,8
S_ s55,s71,51200
Mvaa 24,184,48
S_ m0,s51,5120
Mvaa 24,188,88
Mvaa 28,180,12
L_ v7,24,s55,0
Mvaa 28,184,52
Mvaa 28,188,240
Mvaa 32,180,16
Mvaa 32,184,56
buffer_load_dword a74, v4, s[24:27], s85 offen
Mvaa 32,188,244
s_waitcnt vmcnt(31) lgkmcnt(0)
s_barrier
Mvaa 52,220,244
s_nop 0
Ta 180,%0,49152
Ta 182,%0,51200
Mvaa 52,196,56
Mvaa 52,192,16
s_nop 0
Ta 184,%0,53248
Ta 186,%0,55296
Mvaa 48,220,240
Mvaa 48,196,52
s_nop 0
Ta 188,%0,57344
Ta 190,%0,59392
Mvaa 48,192,12
Mvaa 44,220,88
Mvaa 44,196,48
Mvaa 44,192,8
Mvaa 40,220,84
Mvaa 40,196,44
Mvaa 40,192,4
Mvaa 36,220,80
Mvaa 36,196,40
Mvaa 36,192,0
W_ 0
Mvaa 56,180,120
S_ s81,s6,4
Ta 192,v1,49152
Ta 194,v1,51200
Mvaa 56,184,160
s_and_b32 s81, s81, 7
Mvaa 56,188,200
s_mul_i32 s81, s81, 20480
Ta 196,v1,53248
Ta 198,v1,55296
Mvaa 60,180,124
s_mov_b32 m0, s52
S_ s73,s81,s78
Mvaa 60,184,164
S_ s73,s73,4096
Ta 220,v1,57344
Ta 222,v1,59392
Mvaa 60,188,204
s_nop 0
Y_ 56,0
Y_ 57,256
Mvaa 64,180,128
s_nop 0
Y_ 58,512
Y_ 59,768
Mvaa 64,184,168
Mvaa 64,188,208
s_nop 0
Y_ 60,1024
Y_ 61,1280
Mvaa 68,180,132
s_nop 0
Y_ 62,1536
Y_ 63,1792
Mvaa 68,184,172
Mvaa 68,188,212
s_nop 0
Y_ 64,2048
Y_ 65,2304
Mvaa 72,180,136
s_nop 0
Y_ 66,2560
Y_ 67,2816
Mvaa 72,184,176
Mvaa 72,188,216
W_ 12
Y_ 68,3072
Y_ 69,3328
Mvaa 92,220,216
s_nop 0
Ta 180,v2,49152
Ta 182,v2,51200
Mvaa 92,196,176
s_nop 0
Y_ 70,3584
Y_ 71,3840
Mvaa 92,192,136
s_nop 0
Ta 184,v2,53248
Ta 186,v2,55296
Mvaa 88,220,212
s_nop 0
Y_ 72,4096
Y_ 73,4352
Mvaa 88,196,172
S_ s83,s6,3
Ta 188,v2,57344
Ta 190,v2,59392
Mvaa 88,192,132
s_and_b32 s83, s83, 7
Y_ 74,4608
Y_ 75,4864
Mvaa 84,220,208
s_lshl_b32 s83, s83, 2
Y_ 92,9216
Y_ 93,9472
Mvaa 84,196,168
s_nop 0
Y_ 94,9728
Y_ 95,9984
Mvaa 84,192,128
s_nop 0
Y_ 88,8192
Y_ 89,8448
Mvaa 80,220,204
s_nop 0
Y_ 90,8704
Y_ 91,8960
Mvaa 80,196,164
v_cmp_eq_u32_e64 s[36:37], s83, v8
Mvaa 80,192,124
s_sub_u32 s75, s6, 2
Y_ 84,7168
Y_ 85,7424
Mvaa 76,220,200
s_and_b32 s75, s75, 7
Y_ 86,7680
Y_ 87,7936
Mvaa 76,196,160
v_mov_b32 v10, 0x7fff0000
Mvaa 76,192,120
W_ 14
Y_ 80,6144
Y_ 81,6400
Mvaa 16,180,120
s_nop 0
Ta 192,v3,49152
Ta 194,v3,51200
Mvaa 16,184,160
s_nop 0
Y_ 82,6656
Y_ 83,6912
Mvaa 16,188,200
s_nop 0
Ta 196,v3,53248
Ta 198,v3,55296
Mvaa 20,180,124
s_nop 0
Y_ 76,5120
Y_ 77,5376
Mvaa 20,184,164
s_nop 0
Ta 220,v3,57344
Ta 222,v3,59392
Mvaa 20,188,204
s_nop 0
Y_ 78,5632
Y_ 79,5888
Mvaa 24,180,128
buffer_load_dword v11, v8, s[92:95], s96 offen offset:384 sc1
Mvaa 24,184,168
v_cndmask_b32_e64 v10, v10, v9, s[36:37]
Mvaa 24,188,208
s_waitcnt vmcnt(14) lgkmcnt(0)
s_barrier
Mvaa 28,180,132
S_ s55,s71,122880
ds_read2st64_b32 v[96:97], v5 offset0:0 offset1:1
Mvaa 28,184,172
buffer_store_dword a255, v10, s[92:95], 0 offen offset:384 sc1
Mvaa 28,188,212
S_ m0,s51,12288
ds_read2st64_b32 v[98:99], v5 offset0:2 offset1:3
Mvaa 32,180,136
ds_read2st64_b32 v[104:105], v5 offset0:4 offset1:5
Mvaa 32,184,176
ds_read2st64_b32 v[106:107], v5 offset0:6 offset1:7
Mvaa 32,188,216
L_ v7,24,s55,0
Mvaa 52,220,216
S_ s55,s71,133120
Ta 180,%0,36864
Ta 182,%0,38912
Mvaa 52,196,176
S_ m0,s51,13312
ds_read2st64_b32 v[112:113], v5 offset0:8 offset1:9
Mvaa 52,192,136
W_ 5
Ta 184,%0,40960
Ta 186,%0,43008
Mvaa 48,220,212
L_ v7,24,s55,0
Mvaa 48,196,172
S_ s55,s71,143360
Ta 188,%0,45056
Ta 190,%0,47104
Mvaa 48,192,132
S_ m0,s51,14336
ds_read2st64_b32 v[114:115], v5 offset0:10 offset1:11
A_ v96,v16,v96
Mvaa 44,220,208
ds_read2st64_b32 v[120:121], v5 offset0:12 offset1:13
A_ v97,v17,v97
Mvaa 44,196,168
L_ v7,24,s55,0
Mvaa 44,192,128
S_ s55,s71,153600
ds_read2st64_b32 v[122:123], v5 offset0:14 offset1:15
A_ v98,v18,v98
Mvaa 40,220,204
S_ m0,s51,15360
ds_read2st64_b32 v[128:129], v5 offset0:16 offset1:17
A_ v99,v19,v99
Mvaa 40,196,164
W_ 11
ds_read2st64_b32 v[130:131], v5 offset0:18 offset1:19
A_ v104,v20,v104
Mvaa 40,192,124
L_ v7,24,s55,0
Mvaa 36,220,200
S_ s55,s71,163840
ds_read2st64_b32 v[132:133], v5 offset0:36 offset1:37
A_ v105,v21,v105
Mvaa 36,196,160
S_ m0,s51,16384
ds_read2st64_b32 v[134:135], v5 offset0:38 offset1:39
A_ v106,v22,v106
Mvaa 36,192,120
ds_read2st64_b32 v[124:125], v5 offset0:32 offset1:33
A_ v107,v23,v107
s_waitcnt vmcnt(29)
Zaaa 100,70,20
L_ v7,24,s55,0
Zaaa 140,68,20
W_ 7
ds_read2st64_b32 v[126:127], v5 offset0:34 offset1:35
A_ v112,v24,v112
Zaaa 104,70,24
S_ s55,s71,174080
ds_read2st64_b32 v[116:117], v5 offset0:28 offset1:29
A_ v113,v25,v113
Zaaa 144,68,24
S_ m0,s51,17408
ds_read2st64_b32 v[118:119], v5 offset0:30 offset1:31
A_ v114,v26,v114
Zaaa 108,70,28
L_ v7,24,s55,0
Zaaa 148,68,28
S_ s79,s6,6
ds_read2st64_b32 v[108:109], v5 offset0:24 offset1:25
A_ v115,v27,v115
Zaaa 112,70,32
W_ 9
ds_read2st64_b32 v[110:111], v5 offset0:26 offset1:27
A_ v120,v28,v120
Zaaa 152,68,32
s_and_b32 s79, s79, 7
ds_read2st64_b32 v[100:101], v5 offset0:20 offset1:21
A_ v121,v29,v121
Zaaa 116,70,36
s_lshl_b32 s79, s79, 7
ds_read2st64_b32 v[102:103], v5 offset0:22 offset1:23
A_ v122,v30,v122
Zaaa 156,68,36
S_ s71,s72,s79
A_ v123,v31,v123
Maaa 224,70,60
W_ 10
A_ v128,v32,v128
A_ v129,v33,v129
Maaa 228,68,60
S_ s55,s71,491520
A_ v130,v34,v130
A_ v131,v35,v131
Maaa 232,70,64
W_ 8
A_ v132,v52,v132
A_ v133,v53,v133
Maaa 236,68,64
.Lpad_o_7:
s_cbranch_execz .Lpad_o_11
A_ v134,v54,v134
A_ v135,v55,v135
Zvav 56,180,136
W_ 6
Ta 192,v1,36864
Ta 194,v1,38912
Mvav 56,184,156
S_ m0,s51,49152
A_ v124,v48,v124
A_ v125,v49,v125
Mvav 56,188,176
s_nop 0
Ta 196,v1,40960
Ta 198,v1,43008
Zvav 60,180,140
L_ v7,24,s55,0
Mvav 60,184,160
S_ s55,s71,501760
Ta 220,v1,45056
Ta 222,v1,47104
Mvav 60,188,180
S_ m0,s51,50176
A_ v126,v50,v126
A_ v127,v51,v127
Zvav 64,180,144
W_ 10
A_ v116,v44,v116
A_ v117,v45,v117
Mvav 64,184,164
L_ v7,24,s55,0
Mvav 64,188,184
S_ s55,s71,512000
A_ v118,v46,v118
A_ v119,v47,v119
Zvav 68,180,148
W_ 8
A_ v108,v40,v108
A_ v109,v41,v109
Mvav 68,184,168
S_ m0,s51,51200
A_ v110,v42,v110
A_ v111,v43,v111
Mvav 68,188,188
L_ v7,24,s55,0
Zvav 72,180,152
s_waitcnt vmcnt(10)
W_ 7
A_ v100,v36,v100
W_ 6
Mvav 72,184,172
S_ s55,s71,522240
v_cmp_eq_u32 vcc, s62, v11
A_ v101,v37,v101
Mvav 72,188,192
W_ 0
A_ v102,v38,v102
s_mul_i32 s76, s75, 204800
Zvav 92,220,192
S_ m0,s51,52224
Ta 180,v2,36864
Ta 182,v2,38912
Mvav 92,196,172
L_ v7,24,s55,0
Mvav 92,192,152
S_ s55,s71,532480
Ta 184,v2,40960
Ta 186,v2,43008
Zvav 88,220,188
s_bitcmp1_b32 vcc_lo, s75
s_cbranch_scc0 .Lspin1
.Lspun1:
A_ v103,v39,v103
Mvav 88,196,168
S_ s56,s76,s84
Ta 188,v2,45056
Ta 190,v2,47104
Mvav 88,192,148
buffer_load_dwordx4 a[20:23], v6, s[28:31], s56 offen offset:0 sc1
Zvav 84,220,184
S_ m0,s51,53248
C_ 131,130,131
C_ 130,128,129
Mvav 84,196,164
S_ s85,s6,8
C_ 132,132,133
C_ 133,134,135
Mvav 84,192,144
s_and_b32 s85, s85, 7
Zvav 80,220,180
buffer_load_dwordx4 a[24:27], v6, s[28:31], s56 offen offset:1024 sc1
Mvav 80,196,160
s_lshl_b32 s85, s85, 7
Mvav 80,192,140
S_ s85,s85,s87
Zvav 76,220,176
Mvav 76,196,156
buffer_load_dwordx4 a[28:31], v6, s[28:31], s56 offen offset:2048 sc1
Mvav 76,192,136
W_ 0
Zvav 16,180,136
s_nop 0
Ta 192,v3,36864
Ta 194,v3,38912
Mvav 16,184,156
Mvav 16,188,176
s_nop 0
Ta 196,v3,40960
Ta 198,v3,43008
Zvav 20,180,140
buffer_load_dwordx4 a[32:35], v6, s[28:31], s56 offen offset:3072 sc1
Mvav 20,184,160
S_ s56,s56,4096
Ta 220,v3,45056
Ta 222,v3,47104
Mvav 20,188,180
Zvav 24,180,144
Mvav 24,184,164
buffer_load_dwordx4 a[36:39], v6, s[28:31], s56 offen offset:0 sc1
Mvav 24,188,184
S_ s56,s76,s74
Zvav 28,180,148
Mvav 28,184,168
Mvav 28,188,188
buffer_load_dwordx4 a[60:63], v6, s[28:31], s56 offen offset:0 sc1
Zvav 32,180,152
S_ s56,s76,s82
Mvav 32,184,172
Mvav 32,188,192
s_waitcnt vmcnt(25) lgkmcnt(0)
s_barrier
Zvav 52,220,192
s_nop 0
Ta 180,%0,24576
Ta 182,%0,26624
Mvav 52,196,172
buffer_load_dwordx4 a[64:67], v6, s[28:31], s56 offen offset:0 sc1
Mvav 52,192,152
s_nop 0
Ta 184,%0,28672
Ta 186,%0,30720
Zvav 48,220,188
Mvav 48,196,168
s_nop 0
Ta 188,%0,32768
Ta 190,%0,34816
Mvav 48,192,148
buffer_store_dwordx4 v[130:133], v6, s[28:31], s73 offen offset:0 sc1
Zvav 44,220,184
s_sub_u32 s73, s73, 4096
Mvav 44,196,164
C_ 123,122,123
C_ 122,120,121
Mvav 44,192,144
C_ 124,124,125
C_ 125,126,127
Zvav 40,220,180
Mvav 40,196,160
Mvav 40,192,140
Zvav 36,220,176
Mvav 36,196,156
buffer_store_dwordx4 v[122:125], v6, s[28:31], s73 offen offset:3072 sc1
Mvav 36,192,136
W_ 0
C_ 115,114,115
C_ 114,112,113
Mvav 56,180,196
s_nop 0
Ta 192,v1,24576
Ta 194,v1,26624
Mvav 56,184,216
C_ 116,116,117
C_ 117,118,119
Mvav 56,188,236
s_nop 0
Ta 196,v1,28672
Ta 198,v1,30720
Mvav 60,180,200
Mvav 60,184,220
s_nop 0
Ta 220,v1,32768
Ta 222,v1,34816
Mvav 60,188,240
Mvav 64,180,204
buffer_store_dwordx4 v[114:117], v6, s[28:31], s73 offen offset:2048 sc1
Mvav 64,184,224
C_ 107,106,107
C_ 106,104,105
Mvav 64,188,244
C_ 108,108,109
C_ 109,110,111
Mvav 68,180,208
Mvav 68,184,228
Mvav 68,188,248
Mvav 72,180,212
Mvav 72,184,232
Mvav 72,188,252
W_ 0
Mvav 92,220,252
s_nop 0
Ta 180,v2,24576
Ta 182,v2,26624
Mvav 92,196,232
buffer_store_dwordx4 v[106:109], v6, s[28:31], s73 offen offset:1024 sc1
Mvav 92,192,212
s_nop 0
Ta 184,v2,28672
Ta 186,v2,30720
Mvav 88,220,248
C_ 99,98,99
C_ 98,96,97
Mvav 88,196,228
s_nop 0
Ta 188,v2,32768
Ta 190,v2,34816
Mvav 88,192,208
C_ 100,100,101
C_ 101,102,103
Mvav 84,220,244
Mvav 84,196,224
Mvav 84,192,204
Mvav 80,220,240
buffer_store_dwordx4 v[98:101], v6, s[28:31], s73 offen offset:0 sc1
Mvav 80,196,220
Mvav 80,192,200
Mvav 76,220,236
Mvav 76,196,216
Mvav 76,192,196
W_ 0
Mvav 16,180,196
s_nop 0
Ta 192,v3,24576
Ta 194,v3,26624
Mvav 16,184,216
Mvav 16,188,236
s_nop 0
Ta 196,v3,28672
Ta 198,v3,30720
Mvav 20,180,200
L_ v7,24,s55,0
Mvav 20,184,220
S_ s55,s71,542720
Ta 220,v3,32768
Ta 222,v3,34816
Mvav 20,188,240
S_ m0,s51,54272
Mvav 24,180,204
Mvav 24,184,224
L_ v7,24,s55,0
Mvav 24,188,244
S_ s55,s71,614400
Mvav 28,180,208
S_ m0,s51,36864
Mvav 28,184,228
Mvav 28,188,248
L_ v7,24,s55,0
Mvav 32,180,212
S_ s55,s71,624640
Mvav 32,184,232
S_ m0,s51,37888
Mvav 32,188,252
s_waitcnt vmcnt(28) lgkmcnt(0)
s_barrier
Mvav 52,220,252
s_nop 0
Ta 180,%0,0
Ta 182,%0,2048
Mvav 52,196,232
L_ v7,24,s55,0
Mvav 52,192,212
S_ s55,s71,634880
Ta 184,%0,4096
Ta 186,%0,6144
Mvav 48,220,248
S_ m0,s51,38912
Mvav 48,196,228
s_nop 0
Ta 188,%0,8192
Ta 190,%0,10240
Mvav 48,192,208
L_ v7,24,s55,0
Mvav 44,220,244
S_ s55,s71,645120
Mvav 44,196,224
S_ m0,s51,39936
Mvav 44,192,204
Mvav 40,220,240
L_ v7,24,s55,0
Mvav 40,196,220
S_ s55,s71,655360
Mvav 40,192,200
S_ m0,s51,40960
Mvav 36,220,236
Mvav 36,196,216
L_ v7,24,s55,0
Mvav 36,192,196
W_ 0
S_ s55,s71,665600
Mvaa 56,180,0
S_ m0,s51,41984
Ta 192,v1,0
Ta 194,v1,2048
Mvaa 56,184,40
Mvaa 56,188,80
s_nop 0
Ta 196,v1,4096
Ta 198,v1,6144
Mvaa 60,180,4
L_ v7,24,s55,0
Mvaa 60,184,44
S_ s55,s71,0
Ta 220,v1,8192
Ta 222,v1,10240
Mvaa 60,188,84
S_ m0,s51,24576
Mvaa 64,180,8
Mvaa 64,184,48
L_ v7,24,s55,0
Mvaa 64,188,88
S_ s55,s71,10240
Mvaa 68,180,12
S_ m0,s51,25600
Mvaa 68,184,52
Mvaa 68,188,240
L_ v7,24,s55,0
Mvaa 72,180,16
S_ s55,s71,20480
Mvaa 72,184,56
S_ m0,s51,26624
Mvaa 72,188,244
W_ 0
Mvaa 92,220,244
s_nop 0
Ta 180,v2,0
Ta 182,v2,2048
Mvaa 92,196,56
L_ v7,24,s55,0
Mvaa 92,192,16
S_ s55,s71,30720
Ta 184,v2,4096
Ta 186,v2,6144
Mvaa 88,220,240
S_ m0,s51,27648
Mvaa 88,196,52
s_nop 0
Ta 188,v2,8192
Ta 190,v2,10240
Mvaa 88,192,12
L_ v7,24,s55,0
Mvaa 84,220,88
S_ s55,s71,40960
Mvaa 84,196,48
S_ m0,s51,28672
Mvaa 84,192,8
Mvaa 80,220,84
L_ v7,24,s55,0
Mvaa 80,196,44
S_ s55,s71,51200
Mvaa 80,192,4
S_ m0,s51,29696
Mvaa 76,220,80
Mvaa 76,196,40
L_ v7,24,s55,0
Mvaa 76,192,0
W_ 0
Mvaa 16,180,0
s_nop 0
Ta 192,v3,0
Ta 194,v3,2048
Mvaa 16,184,40
Mvaa 16,188,80
s_nop 0
Ta 196,v3,4096
Ta 198,v3,6144
Mvaa 20,180,4
buffer_load_dword a74, v4, s[24:27], s85 offen
Mvaa 20,184,44
s_nop 0
Ta 220,v3,8192
Ta 222,v3,10240
Mvaa 20,188,84
Mvaa 24,180,8
Mvaa 24,184,48
Mvaa 24,188,88
Mvaa 28,180,12
Mvaa 28,184,52
Mvaa 28,188,240
Mvaa 32,180,16
Mvaa 32,184,56
Mvaa 32,188,244
s_waitcnt vmcnt(31) lgkmcnt(0)
s_barrier
Mvaa 52,220,244
s_nop 0
Ta 180,%0,12288
Ta 182,%0,14336
Mvaa 52,196,56
Mvaa 52,192,16
s_nop 0
Ta 184,%0,16384
Ta 186,%0,18432
Mvaa 48,220,240
Mvaa 48,196,52
s_nop 0
Ta 188,%0,20480
Ta 190,%0,22528
Mvaa 48,192,12
Mvaa 44,220,88
Mvaa 44,196,48
Mvaa 44,192,8
Mvaa 40,220,84
Mvaa 40,196,44
Mvaa 40,192,4
Mvaa 36,220,80
Mvaa 36,196,40
Mvaa 36,192,0
W_ 0
Mvaa 56,180,120
S_ s81,s6,5
Ta 192,v1,12288
Ta 194,v1,14336
Mvaa 56,184,160
s_and_b32 s81, s81, 7
Mvaa 56,188,200
s_mul_i32 s81, s81, 20480
Ta 196,v1,16384
Ta 198,v1,18432
Mvaa 60,180,124
s_mov_b32 m0, s52
S_ s73,s81,s78
Mvaa 60,184,164
S_ s73,s73,4096
Ta 220,v1,20480
Ta 222,v1,22528
Mvaa 60,188,204
s_nop 0
Y_ 56,0
Y_ 57,256
Mvaa 64,180,128
s_nop 0
Y_ 58,512
Y_ 59,768
Mvaa 64,184,168
Mvaa 64,188,208
s_nop 0
Y_ 60,1024
Y_ 61,1280
Mvaa 68,180,132
s_nop 0
Y_ 62,1536
Y_ 63,1792
Mvaa 68,184,172
Mvaa 68,188,212
s_nop 0
Y_ 64,2048
Y_ 65,2304
Mvaa 72,180,136
s_nop 0
Y_ 66,2560
Y_ 67,2816
Mvaa 72,184,176
Mvaa 72,188,216
W_ 12
Y_ 68,3072
Y_ 69,3328
Mvaa 92,220,216
s_nop 0
Ta 180,v2,12288
Ta 182,v2,14336
Mvaa 92,196,176
s_nop 0
Y_ 70,3584
Y_ 71,3840
Mvaa 92,192,136
s_nop 0
Ta 184,v2,16384
Ta 186,v2,18432
Mvaa 88,220,212
s_nop 0
Y_ 72,4096
Y_ 73,4352
Mvaa 88,196,172
S_ s83,s6,4
Ta 188,v2,20480
Ta 190,v2,22528
Mvaa 88,192,132
s_and_b32 s83, s83, 7
Y_ 74,4608
Y_ 75,4864
Mvaa 84,220,208
s_lshl_b32 s83, s83, 2
Y_ 92,9216
Y_ 93,9472
Mvaa 84,196,168
s_nop 0
Y_ 94,9728
Y_ 95,9984
Mvaa 84,192,128
s_nop 0
Y_ 88,8192
Y_ 89,8448
Mvaa 80,220,204
s_nop 0
Y_ 90,8704
Y_ 91,8960
Mvaa 80,196,164
v_cmp_eq_u32_e64 s[36:37], s83, v8
Mvaa 80,192,124
s_sub_u32 s75, s6, 3
Y_ 84,7168
Y_ 85,7424
Mvaa 76,220,200
s_and_b32 s75, s75, 7
Y_ 86,7680
Y_ 87,7936
Mvaa 76,196,160
v_mov_b32 v10, 0x7fff0000
Mvaa 76,192,120
W_ 14
Y_ 80,6144
Y_ 81,6400
Mvaa 16,180,120
s_nop 0
Ta 192,v3,12288
Ta 194,v3,14336
Mvaa 16,184,160
s_nop 0
Y_ 82,6656
Y_ 83,6912
Mvaa 16,188,200
s_nop 0
Ta 196,v3,16384
Ta 198,v3,18432
Mvaa 20,180,124
s_nop 0
Y_ 76,5120
Y_ 77,5376
Mvaa 20,184,164
s_nop 0
Ta 220,v3,20480
Ta 222,v3,22528
Mvaa 20,188,204
s_nop 0
Y_ 78,5632
Y_ 79,5888
Mvaa 24,180,128
buffer_load_dword v11, v8, s[92:95], s96 offen offset:384 sc1
Mvaa 24,184,168
v_cndmask_b32_e64 v10, v10, v9, s[36:37]
Mvaa 24,188,208
s_waitcnt vmcnt(14) lgkmcnt(0)
s_barrier
Mvaa 28,180,132
S_ s55,s71,122880
ds_read2st64_b32 v[96:97], v5 offset0:0 offset1:1
Mvaa 28,184,172
buffer_store_dword a255, v10, s[92:95], 0 offen offset:384 sc1
Mvaa 28,188,212
S_ m0,s51,0
ds_read2st64_b32 v[98:99], v5 offset0:2 offset1:3
Mvaa 32,180,136
.Lpad_o_8:
s_cbranch_execz .Lhop_o_r
ds_read2st64_b32 v[104:105], v5 offset0:4 offset1:5
Mvaa 32,184,176
ds_read2st64_b32 v[106:107], v5 offset0:6 offset1:7
Mvaa 32,188,216
L_ v7,24,s55,0
Mvaa 52,220,216
S_ s55,s71,133120
Ta 180,%0,49152
Ta 182,%0,51200
Mvaa 52,196,176
S_ m0,s51,1024
ds_read2st64_b32 v[112:113], v5 offset0:8 offset1:9
Mvaa 52,192,136
W_ 5
Ta 184,%0,53248
Ta 186,%0,55296
Mvaa 48,220,212
L_ v7,24,s55,0
Mvaa 48,196,172
S_ s55,s71,143360
Ta 188,%0,57344
Ta 190,%0,59392
Mvaa 48,192,132
S_ m0,s51,2048
ds_read2st64_b32 v[114:115], v5 offset0:10 offset1:11
A_ v96,v16,v96
Mvaa 44,220,208
ds_read2st64_b32 v[120:121], v5 offset0:12 offset1:13
A_ v97,v17,v97
Mvaa 44,196,168
L_ v7,24,s55,0
Mvaa 44,192,128
S_ s55,s71,153600
ds_read2st64_b32 v[122:123], v5 offset0:14 offset1:15
A_ v98,v18,v98
Mvaa 40,220,204
S_ m0,s51,3072
ds_read2st64_b32 v[128:129], v5 offset0:16 offset1:17
A_ v99,v19,v99
Mvaa 40,196,164
W_ 11
ds_read2st64_b32 v[130:131], v5 offset0:18 offset1:19
A_ v104,v20,v104
Mvaa 40,192,124
L_ v7,24,s55,0
Mvaa 36,220,200
S_ s55,s71,163840
ds_read2st64_b32 v[132:133], v5 offset0:36 offset1:37
A_ v105,v21,v105
Mvaa 36,196,160
S_ m0,s51,4096
ds_read2st64_b32 v[134:135], v5 offset0:38 offset1:39
A_ v106,v22,v106
Mvaa 36,192,120
ds_read2st64_b32 v[124:125], v5 offset0:32 offset1:33
A_ v107,v23,v107
s_waitcnt vmcnt(26)
Maaa 100,70,20
L_ v7,24,s55,0
Maaa 140,68,20
W_ 7
ds_read2st64_b32 v[126:127], v5 offset0:34 offset1:35
A_ v112,v24,v112
Maaa 104,70,24
S_ s55,s71,174080
ds_read2st64_b32 v[116:117], v5 offset0:28 offset1:29
A_ v113,v25,v113
Maaa 144,68,24
S_ m0,s51,5120
ds_read2st64_b32 v[118:119], v5 offset0:30 offset1:31
A_ v114,v26,v114
Maaa 108,70,28
L_ v7,24,s55,0
Maaa 148,68,28
S_ s79,s6,7
ds_read2st64_b32 v[108:109], v5 offset0:24 offset1:25
A_ v115,v27,v115
Maaa 112,70,32
W_ 9
ds_read2st64_b32 v[110:111], v5 offset0:26 offset1:27
A_ v120,v28,v120
Maaa 152,68,32
s_and_b32 s79, s79, 7
ds_read2st64_b32 v[100:101], v5 offset0:20 offset1:21
A_ v121,v29,v121
Maaa 116,70,36
s_lshl_b32 s79, s79, 7
ds_read2st64_b32 v[102:103], v5 offset0:22 offset1:23
A_ v122,v30,v122
Maaa 156,68,36
S_ s71,s72,s79
A_ v123,v31,v123
Maaa 224,70,60
W_ 10
A_ v128,v32,v128
A_ v129,v33,v129
Maaa 228,68,60
S_ s55,s71,491520
A_ v130,v34,v130
A_ v131,v35,v131
Maaa 232,70,64
W_ 8
A_ v132,v52,v132
A_ v133,v53,v133
Maaa 236,68,64
A_ v134,v54,v134
A_ v135,v55,v135
Zvav 56,180,136
W_ 6
Ta 192,v1,49152
Ta 194,v1,51200
Mvav 56,184,156
S_ m0,s51,12288
A_ v124,v48,v124
A_ v125,v49,v125
Mvav 56,188,176
s_nop 0
Ta 196,v1,53248
Ta 198,v1,55296
Zvav 60,180,140
L_ v7,24,s55,0
Mvav 60,184,160
S_ s55,s71,501760
Ta 220,v1,57344
Ta 222,v1,59392
Mvav 60,188,180
S_ m0,s51,13312
A_ v126,v50,v126
A_ v127,v51,v127
Zvav 64,180,144
W_ 10
A_ v116,v44,v116
A_ v117,v45,v117
Mvav 64,184,164
L_ v7,24,s55,0
Mvav 64,188,184
S_ s55,s71,512000
A_ v118,v46,v118
A_ v119,v47,v119
Zvav 68,180,148
W_ 8
A_ v108,v40,v108
A_ v109,v41,v109
Mvav 68,184,168
S_ m0,s51,14336
A_ v110,v42,v110
A_ v111,v43,v111
Mvav 68,188,188
L_ v7,24,s55,0
Zvav 72,180,152
s_waitcnt vmcnt(10)
W_ 7
A_ v100,v36,v100
W_ 6
Mvav 72,184,172
S_ s55,s71,522240
v_cmp_eq_u32 vcc, s62, v11
A_ v101,v37,v101
Mvav 72,188,192
W_ 0
A_ v102,v38,v102
s_mul_i32 s76, s75, 204800
Zvav 92,220,192
S_ m0,s51,15360
Ta 180,v2,49152
Ta 182,v2,51200
Mvav 92,196,172
L_ v7,24,s55,0
Mvav 92,192,152
S_ s55,s71,532480
Ta 184,v2,53248
Ta 186,v2,55296
Zvav 88,220,188
s_bitcmp1_b32 vcc_lo, s75
s_cbranch_scc0 .Lspin2
.Lspun2:
A_ v103,v39,v103
Mvav 88,196,168
S_ s56,s76,s84
Ta 188,v2,57344
Ta 190,v2,59392
Mvav 88,192,148
buffer_load_dwordx4 a[20:23], v6, s[28:31], s56 offen offset:0 sc1
Zvav 84,220,184
S_ m0,s51,16384
C_ 131,130,131
C_ 130,128,129
Mvav 84,196,164
C_ 132,132,133
C_ 133,134,135
Mvav 84,192,144
Zvav 80,220,180
buffer_load_dwordx4 a[24:27], v6, s[28:31], s56 offen offset:1024 sc1
Mvav 80,196,160
Mvav 80,192,140
Zvav 76,220,176
Mvav 76,196,156
buffer_load_dwordx4 a[28:31], v6, s[28:31], s56 offen offset:2048 sc1
Mvav 76,192,136
W_ 0
Zvav 16,180,136
s_nop 0
Ta 192,v3,49152
Ta 194,v3,51200
Mvav 16,184,156
Mvav 16,188,176
s_nop 0
Ta 196,v3,53248
Ta 198,v3,55296
Zvav 20,180,140
buffer_load_dwordx4 a[32:35], v6, s[28:31], s56 offen offset:3072 sc1
Mvav 20,184,160
S_ s56,s56,4096
Ta 220,v3,57344
Ta 222,v3,59392
Mvav 20,188,180
Zvav 24,180,144
Mvav 24,184,164
buffer_load_dwordx4 a[36:39], v6, s[28:31], s56 offen offset:0 sc1
Mvav 24,188,184
S_ s56,s76,s74
Zvav 28,180,148
Mvav 28,184,168
Mvav 28,188,188
buffer_load_dwordx4 a[60:63], v6, s[28:31], s56 offen offset:0 sc1
Zvav 32,180,152
S_ s56,s76,s82
Mvav 32,184,172
Mvav 32,188,192
s_waitcnt vmcnt(25) lgkmcnt(0)
s_barrier
Zvav 52,220,192
s_nop 0
Ta 180,%0,36864
Ta 182,%0,38912
Mvav 52,196,172
buffer_load_dwordx4 a[64:67], v6, s[28:31], s56 offen offset:0 sc1
Mvav 52,192,152
s_nop 0
Ta 184,%0,40960
Ta 186,%0,43008
Zvav 48,220,188
Mvav 48,196,168
s_nop 0
Ta 188,%0,45056
Ta 190,%0,47104
Mvav 48,192,148
buffer_store_dwordx4 v[130:133], v6, s[28:31], s73 offen offset:0 sc1
Zvav 44,220,184
s_sub_u32 s73, s73, 4096
Mvav 44,196,164
C_ 123,122,123
C_ 122,120,121
Mvav 44,192,144
C_ 124,124,125
C_ 125,126,127
Zvav 40,220,180
Mvav 40,196,160
Mvav 40,192,140
Zvav 36,220,176
Mvav 36,196,156
buffer_store_dwordx4 v[122:125], v6, s[28:31], s73 offen offset:3072 sc1
Mvav 36,192,136
W_ 0
C_ 115,114,115
C_ 114,112,113
Mvav 56,180,196
s_nop 0
Ta 192,v1,36864
Ta 194,v1,38912
Mvav 56,184,216
C_ 116,116,117
C_ 117,118,119
Mvav 56,188,236
s_nop 0
Ta 196,v1,40960
Ta 198,v1,43008
Mvav 60,180,200
Mvav 60,184,220
s_nop 0
Ta 220,v1,45056
Ta 222,v1,47104
Mvav 60,188,240
Mvav 64,180,204
buffer_store_dwordx4 v[114:117], v6, s[28:31], s73 offen offset:2048 sc1
Mvav 64,184,224
C_ 107,106,107
C_ 106,104,105
Mvav 64,188,244
C_ 108,108,109
C_ 109,110,111
Mvav 68,180,208
Mvav 68,184,228
Mvav 68,188,248
Mvav 72,180,212
Mvav 72,184,232
Mvav 72,188,252
W_ 0
Mvav 92,220,252
s_nop 0
Ta 180,v2,36864
Ta 182,v2,38912
Mvav 92,196,232
buffer_store_dwordx4 v[106:109], v6, s[28:31], s73 offen offset:1024 sc1
Mvav 92,192,212
s_nop 0
Ta 184,v2,40960
Ta 186,v2,43008
Mvav 88,220,248
C_ 99,98,99
C_ 98,96,97
Mvav 88,196,228
s_nop 0
Ta 188,v2,45056
Ta 190,v2,47104
Mvav 88,192,208
C_ 100,100,101
C_ 101,102,103
Mvav 84,220,244
Mvav 84,196,224
Mvav 84,192,204
Mvav 80,220,240
buffer_store_dwordx4 v[98:101], v6, s[28:31], s73 offen offset:0 sc1
Mvav 80,196,220
Mvav 80,192,200
Mvav 76,220,236
Mvav 76,196,216
Mvav 76,192,196
W_ 0
Mvav 16,180,196
s_nop 0
Ta 192,v3,36864
Ta 194,v3,38912
Mvav 16,184,216
Mvav 16,188,236
s_nop 0
Ta 196,v3,40960
Ta 198,v3,43008
Mvav 20,180,200
L_ v7,24,s55,0
Mvav 20,184,220
S_ s55,s71,542720
Ta 220,v3,45056
Ta 222,v3,47104
Mvav 20,188,240
S_ m0,s51,17408
Mvav 24,180,204
Mvav 24,184,224
L_ v7,24,s55,0
Mvav 24,188,244
S_ s55,s71,614400
Mvav 28,180,208
S_ m0,s51,49152
Mvav 28,184,228
Mvav 28,188,248
L_ v7,24,s55,0
Mvav 32,180,212
S_ s55,s71,624640
Mvav 32,184,232
S_ m0,s51,50176
Mvav 32,188,252
s_waitcnt vmcnt(28) lgkmcnt(0)
s_barrier
Mvav 52,220,252
s_nop 0
Ta 180,%0,24576
Ta 182,%0,26624
Mvav 52,196,232
L_ v7,24,s55,0
Mvav 52,192,212
S_ s55,s71,634880
Ta 184,%0,28672
Ta 186,%0,30720
Mvav 48,220,248
S_ m0,s51,51200
Mvav 48,196,228
s_nop 0
Ta 188,%0,32768
Ta 190,%0,34816
Mvav 48,192,208
L_ v7,24,s55,0
Mvav 44,220,244
S_ s55,s71,645120
Mvav 44,196,224
S_ m0,s51,52224
Mvav 44,192,204
Mvav 40,220,240
L_ v7,24,s55,0
Mvav 40,196,220
S_ s55,s71,655360
Mvav 40,192,200
S_ m0,s51,53248
Mvav 36,220,236
Mvav 36,196,216
L_ v7,24,s55,0
Mvav 36,192,196
W_ 0
S_ s55,s71,665600
Mvaa 56,180,0
S_ m0,s51,54272
Ta 192,v1,24576
Ta 194,v1,26624
Mvaa 56,184,40
Mvaa 56,188,80
s_nop 0
Ta 196,v1,28672
Ta 198,v1,30720
Mvaa 60,180,4
L_ v7,24,s55,0
Mvaa 60,184,44
S_ s55,s71,0
Ta 220,v1,32768
Ta 222,v1,34816
Mvaa 60,188,84
S_ m0,s51,36864
Mvaa 64,180,8
Mvaa 64,184,48
L_ v7,24,s55,0
Mvaa 64,188,88
S_ s55,s71,10240
Mvaa 68,180,12
S_ m0,s51,37888
Mvaa 68,184,52
Mvaa 68,188,240
L_ v7,24,s55,0
Mvaa 72,180,16
S_ s55,s71,20480
Mvaa 72,184,56
S_ m0,s51,38912
Mvaa 72,188,244
W_ 0
Mvaa 92,220,244
s_nop 0
Ta 180,v2,24576
Ta 182,v2,26624
Mvaa 92,196,56
L_ v7,24,s55,0
Mvaa 92,192,16
S_ s55,s71,30720
Ta 184,v2,28672
Ta 186,v2,30720
Mvaa 88,220,240
S_ m0,s51,39936
Mvaa 88,196,52
s_nop 0
Ta 188,v2,32768
Ta 190,v2,34816
Mvaa 88,192,12
L_ v7,24,s55,0
Mvaa 84,220,88
S_ s55,s71,40960
Mvaa 84,196,48
S_ m0,s51,40960
Mvaa 84,192,8
Mvaa 80,220,84
L_ v7,24,s55,0
Mvaa 80,196,44
S_ s55,s71,51200
Mvaa 80,192,4
S_ m0,s51,41984
Mvaa 76,220,80
Mvaa 76,196,40
L_ v7,24,s55,0
Mvaa 76,192,0
W_ 0
Mvaa 16,180,0
s_nop 0
Ta 192,v3,24576
Ta 194,v3,26624
Mvaa 16,184,40
Mvaa 16,188,80
s_nop 0
Ta 196,v3,28672
Ta 198,v3,30720
Mvaa 20,180,4
Mvaa 20,184,44
s_nop 0
Ta 220,v3,32768
Ta 222,v3,34816
Mvaa 20,188,84
Mvaa 24,180,8
Mvaa 24,184,48
Mvaa 24,188,88
Mvaa 28,180,12
Mvaa 28,184,52
Mvaa 28,188,240
Mvaa 32,180,16
Mvaa 32,184,56
Mvaa 32,188,244
s_waitcnt vmcnt(30) lgkmcnt(0)
s_barrier
Mvaa 52,220,244
s_nop 0
Ta 180,%0,0
Ta 182,%0,2048
Mvaa 52,196,56
Mvaa 52,192,16
s_nop 0
Ta 184,%0,4096
Ta 186,%0,6144
Mvaa 48,220,240
Mvaa 48,196,52
s_nop 0
Ta 188,%0,8192
Ta 190,%0,10240
Mvaa 48,192,12
Mvaa 44,220,88
Mvaa 44,196,48
Mvaa 44,192,8
Mvaa 40,220,84
Mvaa 40,196,44
Mvaa 40,192,4
Mvaa 36,220,80
Mvaa 36,196,40
Mvaa 36,192,0
W_ 0
Mvaa 56,180,120
S_ s81,s6,6
Ta 192,v1,0
Ta 194,v1,2048
Mvaa 56,184,160
s_and_b32 s81, s81, 7
Mvaa 56,188,200
s_mul_i32 s81, s81, 20480
Ta 196,v1,4096
Ta 198,v1,6144
Mvaa 60,180,124
s_mov_b32 m0, s52
S_ s73,s81,s78
Mvaa 60,184,164
S_ s73,s73,4096
Ta 220,v1,8192
Ta 222,v1,10240
Mvaa 60,188,204
s_nop 0
Y_ 56,0
Y_ 57,256
Mvaa 64,180,128
s_nop 0
Y_ 58,512
Y_ 59,768
Mvaa 64,184,168
Mvaa 64,188,208
s_nop 0
Y_ 60,1024
Y_ 61,1280
Mvaa 68,180,132
s_nop 0
Y_ 62,1536
Y_ 63,1792
Mvaa 68,184,172
Mvaa 68,188,212
s_nop 0
Y_ 64,2048
Y_ 65,2304
Mvaa 72,180,136
s_nop 0
Y_ 66,2560
Y_ 67,2816
Mvaa 72,184,176
Mvaa 72,188,216
W_ 12
Y_ 68,3072
Y_ 69,3328
Mvaa 92,220,216
s_nop 0
Ta 180,v2,0
Ta 182,v2,2048
Mvaa 92,196,176
s_nop 0
Y_ 70,3584
Y_ 71,3840
Mvaa 92,192,136
.Lpad_o_9:
s_cbranch_execz .Lhop_o_r
Ta 184,v2,4096
Ta 186,v2,6144
Mvaa 88,220,212
s_nop 0
Y_ 72,4096
Y_ 73,4352
Mvaa 88,196,172
S_ s83,s6,5
Ta 188,v2,8192
Ta 190,v2,10240
Mvaa 88,192,132
s_and_b32 s83, s83, 7
Y_ 74,4608
Y_ 75,4864
Mvaa 84,220,208
s_lshl_b32 s83, s83, 2
Y_ 92,9216
Y_ 93,9472
Mvaa 84,196,168
s_nop 0
Y_ 94,9728
Y_ 95,9984
Mvaa 84,192,128
s_nop 0
Y_ 88,8192
Y_ 89,8448
Mvaa 80,220,204
s_nop 0
Y_ 90,8704
Y_ 91,8960
Mvaa 80,196,164
v_cmp_eq_u32_e64 s[36:37], s83, v8
Mvaa 80,192,124
s_sub_u32 s75, s6, 4
Y_ 84,7168
Y_ 85,7424
Mvaa 76,220,200
s_and_b32 s75, s75, 7
Y_ 86,7680
Y_ 87,7936
Mvaa 76,196,160
v_mov_b32 v10, 0x7fff0000
Mvaa 76,192,120
W_ 14
Y_ 80,6144
Y_ 81,6400
Mvaa 16,180,120
s_nop 0
Ta 192,v3,0
Ta 194,v3,2048
Mvaa 16,184,160
s_nop 0
Y_ 82,6656
Y_ 83,6912
Mvaa 16,188,200
s_nop 0
Ta 196,v3,4096
Ta 198,v3,6144
Mvaa 20,180,124
s_nop 0
Y_ 76,5120
Y_ 77,5376
Mvaa 20,184,164
s_nop 0
Ta 220,v3,8192
Ta 222,v3,10240
Mvaa 20,188,204
s_nop 0
Y_ 78,5632
Y_ 79,5888
Mvaa 24,180,128
buffer_load_dword v11, v8, s[92:95], s96 offen offset:384 sc1
Mvaa 24,184,168
v_cndmask_b32_e64 v10, v10, v9, s[36:37]
Mvaa 24,188,208
s_waitcnt vmcnt(13) lgkmcnt(0)
s_barrier
Mvaa 28,180,132
S_ s55,s71,122880
ds_read2st64_b32 v[96:97], v5 offset0:0 offset1:1
Mvaa 28,184,172
buffer_store_dword a255, v10, s[92:95], 0 offen offset:384 sc1
Mvaa 28,188,212
S_ m0,s51,24576
ds_read2st64_b32 v[98:99], v5 offset0:2 offset1:3
Mvaa 32,180,136
ds_read2st64_b32 v[104:105], v5 offset0:4 offset1:5
Mvaa 32,184,176
ds_read2st64_b32 v[106:107], v5 offset0:6 offset1:7
Mvaa 32,188,216
L_ v7,24,s55,0
Mvaa 52,220,216
S_ s55,s71,133120
Ta 180,%0,12288
Ta 182,%0,14336
Mvaa 52,196,176
S_ m0,s51,25600
ds_read2st64_b32 v[112:113], v5 offset0:8 offset1:9
Mvaa 52,192,136
W_ 5
Ta 184,%0,16384
Ta 186,%0,18432
Mvaa 48,220,212
L_ v7,24,s55,0
Mvaa 48,196,172
S_ s55,s71,143360
Ta 188,%0,20480
Ta 190,%0,22528
Mvaa 48,192,132
S_ m0,s51,26624
ds_read2st64_b32 v[114:115], v5 offset0:10 offset1:11
A_ v96,v16,v96
Mvaa 44,220,208
ds_read2st64_b32 v[120:121], v5 offset0:12 offset1:13
A_ v97,v17,v97
)A"
R"A(Mvaa 44,196,168
L_ v7,24,s55,0
Mvaa 44,192,128
S_ s55,s71,153600
ds_read2st64_b32 v[122:123], v5 offset0:14 offset1:15
A_ v98,v18,v98
Mvaa 40,220,204
S_ m0,s51,27648
ds_read2st64_b32 v[128:129], v5 offset0:16 offset1:17
A_ v99,v19,v99
Mvaa 40,196,164
W_ 11
ds_read2st64_b32 v[130:131], v5 offset0:18 offset1:19
A_ v104,v20,v104
Mvaa 40,192,124
L_ v7,24,s55,0
Mvaa 36,220,200
S_ s55,s71,163840
ds_read2st64_b32 v[132:133], v5 offset0:36 offset1:37
A_ v105,v21,v105
Mvaa 36,196,160
S_ m0,s51,28672
ds_read2st64_b32 v[134:135], v5 offset0:38 offset1:39
A_ v106,v22,v106
Mvaa 36,192,120
ds_read2st64_b32 v[124:125], v5 offset0:32 offset1:33
A_ v107,v23,v107
s_waitcnt vmcnt(25)
Maaa 100,70,20
L_ v7,24,s55,0
Maaa 140,68,20
W_ 7
ds_read2st64_b32 v[126:127], v5 offset0:34 offset1:35
A_ v112,v24,v112
Maaa 104,70,24
S_ s55,s71,174080
ds_read2st64_b32 v[116:117], v5 offset0:28 offset1:29
A_ v113,v25,v113
Maaa 144,68,24
S_ m0,s51,29696
ds_read2st64_b32 v[118:119], v5 offset0:30 offset1:31
A_ v114,v26,v114
Maaa 108,70,28
L_ v7,24,s55,0
Maaa 148,68,28
S_ s79,s6,8
ds_read2st64_b32 v[108:109], v5 offset0:24 offset1:25
A_ v115,v27,v115
Maaa 112,70,32
W_ 9
ds_read2st64_b32 v[110:111], v5 offset0:26 offset1:27
A_ v120,v28,v120
Maaa 152,68,32
s_and_b32 s79, s79, 7
ds_read2st64_b32 v[100:101], v5 offset0:20 offset1:21
A_ v121,v29,v121
Maaa 116,70,36
s_lshl_b32 s79, s79, 7
ds_read2st64_b32 v[102:103], v5 offset0:22 offset1:23
A_ v122,v30,v122
Maaa 156,68,36
S_ s71,s72,s79
A_ v123,v31,v123
Maaa 224,70,60
W_ 10
A_ v128,v32,v128
A_ v129,v33,v129
Maaa 228,68,60
S_ s55,s71,491520
A_ v130,v34,v130
A_ v131,v35,v131
Maaa 232,70,64
W_ 8
A_ v132,v52,v132
A_ v133,v53,v133
Maaa 236,68,64
A_ v134,v54,v134
A_ v135,v55,v135
Zvav 56,180,136
W_ 6
Ta 192,v1,12288
Ta 194,v1,14336
Mvav 56,184,156
S_ m0,s51,0
A_ v124,v48,v124
A_ v125,v49,v125
Mvav 56,188,176
s_nop 0
Ta 196,v1,16384
Ta 198,v1,18432
Zvav 60,180,140
L_ v7,24,s55,0
Mvav 60,184,160
S_ s55,s71,501760
Ta 220,v1,20480
Ta 222,v1,22528
Mvav 60,188,180
S_ m0,s51,1024
A_ v126,v50,v126
A_ v127,v51,v127
Zvav 64,180,144
W_ 10
A_ v116,v44,v116
A_ v117,v45,v117
Mvav 64,184,164
L_ v7,24,s55,0
Mvav 64,188,184
S_ s55,s71,512000
A_ v118,v46,v118
A_ v119,v47,v119
Zvav 68,180,148
W_ 8
A_ v108,v40,v108
A_ v109,v41,v109
Mvav 68,184,168
S_ m0,s51,2048
A_ v110,v42,v110
A_ v111,v43,v111
Mvav 68,188,188
L_ v7,24,s55,0
Zvav 72,180,152
s_waitcnt vmcnt(10)
W_ 7
A_ v100,v36,v100
W_ 6
Mvav 72,184,172
S_ s55,s71,522240
v_cmp_eq_u32 vcc, s62, v11
A_ v101,v37,v101
Mvav 72,188,192
W_ 0
A_ v102,v38,v102
s_mul_i32 s76, s75, 204800
Zvav 92,220,192
S_ m0,s51,3072
Ta 180,v2,12288
Ta 182,v2,14336
Mvav 92,196,172
L_ v7,24,s55,0
Mvav 92,192,152
S_ s55,s71,532480
Ta 184,v2,16384
Ta 186,v2,18432
Zvav 88,220,188
s_bitcmp1_b32 vcc_lo, s75
s_cbranch_scc0 .Lspin3
.Lspun3:
A_ v103,v39,v103
Mvav 88,196,168
S_ s56,s76,s84
Ta 188,v2,20480
Ta 190,v2,22528
Mvav 88,192,148
buffer_load_dwordx4 a[20:23], v6, s[28:31], s56 offen offset:0 sc1
Zvav 84,220,184
S_ m0,s51,4096
C_ 131,130,131
C_ 130,128,129
Mvav 84,196,164
C_ 132,132,133
C_ 133,134,135
Mvav 84,192,144
Zvav 80,220,180
buffer_load_dwordx4 a[24:27], v6, s[28:31], s56 offen offset:1024 sc1
Mvav 80,196,160
Mvav 80,192,140
Zvav 76,220,176
Mvav 76,196,156
buffer_load_dwordx4 a[28:31], v6, s[28:31], s56 offen offset:2048 sc1
Mvav 76,192,136
W_ 0
Zvav 16,180,136
s_nop 0
Ta 192,v3,12288
Ta 194,v3,14336
Mvav 16,184,156
Mvav 16,188,176
s_nop 0
Ta 196,v3,16384
Ta 198,v3,18432
Zvav 20,180,140
buffer_load_dwordx4 a[32:35], v6, s[28:31], s56 offen offset:3072 sc1
Mvav 20,184,160
S_ s56,s56,4096
Ta 220,v3,20480
Ta 222,v3,22528
Mvav 20,188,180
Zvav 24,180,144
Mvav 24,184,164
buffer_load_dwordx4 a[36:39], v6, s[28:31], s56 offen offset:0 sc1
Mvav 24,188,184
S_ s56,s76,s74
Zvav 28,180,148
Mvav 28,184,168
Mvav 28,188,188
buffer_load_dwordx4 a[60:63], v6, s[28:31], s56 offen offset:0 sc1
Zvav 32,180,152
S_ s56,s76,s82
Mvav 32,184,172
Mvav 32,188,192
s_waitcnt vmcnt(24) lgkmcnt(0)
s_barrier
Zvav 52,220,192
s_nop 0
Ta 180,%0,49152
Ta 182,%0,51200
Mvav 52,196,172
buffer_load_dwordx4 a[64:67], v6, s[28:31], s56 offen offset:0 sc1
Mvav 52,192,152
s_nop 0
Ta 184,%0,53248
Ta 186,%0,55296
Zvav 48,220,188
Mvav 48,196,168
s_nop 0
Ta 188,%0,57344
Ta 190,%0,59392
Mvav 48,192,148
buffer_store_dwordx4 v[130:133], v6, s[28:31], s73 offen offset:0 sc1
Zvav 44,220,184
s_sub_u32 s73, s73, 4096
Mvav 44,196,164
C_ 123,122,123
C_ 122,120,121
Mvav 44,192,144
C_ 124,124,125
C_ 125,126,127
Zvav 40,220,180
Mvav 40,196,160
Mvav 40,192,140
Zvav 36,220,176
Mvav 36,196,156
buffer_store_dwordx4 v[122:125], v6, s[28:31], s73 offen offset:3072 sc1
Mvav 36,192,136
W_ 0
C_ 115,114,115
C_ 114,112,113
Mvav 56,180,196
s_nop 0
Ta 192,v1,49152
Ta 194,v1,51200
Mvav 56,184,216
C_ 116,116,117
C_ 117,118,119
Mvav 56,188,236
s_nop 0
Ta 196,v1,53248
Ta 198,v1,55296
Mvav 60,180,200
Mvav 60,184,220
s_nop 0
Ta 220,v1,57344
Ta 222,v1,59392
Mvav 60,188,240
Mvav 64,180,204
buffer_store_dwordx4 v[114:117], v6, s[28:31], s73 offen offset:2048 sc1
Mvav 64,184,224
C_ 107,106,107
C_ 106,104,105
Mvav 64,188,244
C_ 108,108,109
C_ 109,110,111
Mvav 68,180,208
Mvav 68,184,228
Mvav 68,188,248
Mvav 72,180,212
Mvav 72,184,232
Mvav 72,188,252
W_ 0
Mvav 92,220,252
s_nop 0
Ta 180,v2,49152
Ta 182,v2,51200
Mvav 92,196,232
buffer_store_dwordx4 v[106:109], v6, s[28:31], s73 offen offset:1024 sc1
Mvav 92,192,212
s_nop 0
Ta 184,v2,53248
Ta 186,v2,55296
Mvav 88,220,248
C_ 99,98,99
C_ 98,96,97
Mvav 88,196,228
s_nop 0
Ta 188,v2,57344
Ta 190,v2,59392
Mvav 88,192,208
C_ 100,100,101
C_ 101,102,103
Mvav 84,220,244
Mvav 84,196,224
Mvav 84,192,204
Mvav 80,220,240
buffer_store_dwordx4 v[98:101], v6, s[28:31], s73 offen offset:0 sc1
Mvav 80,196,220
Mvav 80,192,200
Mvav 76,220,236
Mvav 76,196,216
Mvav 76,192,196
W_ 0
Mvav 16,180,196
s_nop 0
Ta 192,v3,49152
Ta 194,v3,51200
Mvav 16,184,216
Mvav 16,188,236
s_nop 0
Ta 196,v3,53248
Ta 198,v3,55296
Mvav 20,180,200
L_ v7,24,s55,0
Mvav 20,184,220
S_ s55,s71,542720
Ta 220,v3,57344
Ta 222,v3,59392
Mvav 20,188,240
S_ m0,s51,5120
Mvav 24,180,204
Mvav 24,184,224
L_ v7,24,s55,0
Mvav 24,188,244
S_ s55,s71,614400
Mvav 28,180,208
S_ m0,s51,12288
Mvav 28,184,228
Mvav 28,188,248
L_ v7,24,s55,0
Mvav 32,180,212
S_ s55,s71,624640
Mvav 32,184,232
S_ m0,s51,13312
Mvav 32,188,252
s_waitcnt vmcnt(27) lgkmcnt(0)
s_barrier
Mvav 52,220,252
s_nop 0
Ta 180,%0,36864
Ta 182,%0,38912
Mvav 52,196,232
L_ v7,24,s55,0
Mvav 52,192,212
S_ s55,s71,634880
Ta 184,%0,40960
Ta 186,%0,43008
Mvav 48,220,248
S_ m0,s51,14336
Mvav 48,196,228
s_nop 0
Ta 188,%0,45056
Ta 190,%0,47104
Mvav 48,192,208
L_ v7,24,s55,0
Mvav 44,220,244
s_sub_u32 s75, s6, 5
S_ s55,s71,645120
Mvav 44,196,224
s_and_b32 s75, s75, 7
S_ m0,s51,15360
Mvav 44,192,204
Mvav 40,220,240
buffer_load_dword v11, v8, s[92:95], s96 offen offset:384 sc1
Mvav 40,196,220
Mvav 40,192,200
Mvav 36,220,236
Mvav 36,196,216
L_ v7,24,s55,0
Mvav 36,192,196
W_ 0
S_ s55,s71,655360
Mvaa 56,180,0
S_ m0,s51,16384
Ta 192,v1,36864
Ta 194,v1,38912
Mvaa 56,184,40
Mvaa 56,188,80
s_nop 0
Ta 196,v1,40960
Ta 198,v1,43008
Mvaa 60,180,4
L_ v7,24,s55,0
Mvaa 60,184,44
S_ s55,s71,665600
Ta 220,v1,45056
Ta 222,v1,47104
Mvaa 60,188,84
S_ m0,s51,17408
Mvaa 64,180,8
Mvaa 64,184,48
L_ v7,24,s55,0
Mvaa 64,188,88
S_ s55,s71,0
Mvaa 68,180,12
S_ m0,s51,49152
Mvaa 68,184,52
Mvaa 68,188,240
L_ v7,24,s55,0
Mvaa 72,180,16
S_ s55,s71,10240
Mvaa 72,184,56
S_ m0,s51,50176
Mvaa 72,188,244
W_ 0
Mvaa 92,220,244
s_nop 0
Ta 180,v2,36864
Ta 182,v2,38912
Mvaa 92,196,56
L_ v7,24,s55,0
Mvaa 92,192,16
S_ s55,s71,20480
Ta 184,v2,40960
Ta 186,v2,43008
Mvaa 88,220,240
S_ m0,s51,51200
Mvaa 88,196,52
s_nop 0
Ta 188,v2,45056
Ta 190,v2,47104
Mvaa 88,192,12
L_ v7,24,s55,0
Mvaa 84,220,88
S_ s55,s71,30720
Mvaa 84,196,48
S_ m0,s51,52224
Mvaa 84,192,8
Mvaa 80,220,84
L_ v7,24,s55,0
Mvaa 80,196,44
S_ s83,s6,6
S_ s55,s71,40960
Mvaa 80,192,4
s_and_b32 s83, s83, 7
S_ m0,s51,53248
Mvaa 76,220,80
s_lshl_b32 s83, s83, 2
Mvaa 76,196,40
L_ v7,24,s55,0
Mvaa 76,192,0
W_ 0
v_cmp_eq_u32_e64 s[36:37], s83, v8
S_ s55,s71,51200
Mvaa 16,180,0
S_ m0,s51,54272
Ta 192,v3,36864
Ta 194,v3,38912
Mvaa 16,184,40
v_mov_b32 v10, 0x7fff0000
Mvaa 16,188,80
s_nop 0
Ta 196,v3,40960
Ta 198,v3,43008
Mvaa 20,180,4
L_ v7,24,s55,0
Mvaa 20,184,44
s_nop 0
Ta 220,v3,45056
Ta 222,v3,47104
Mvaa 20,188,84
.Lpad_o_10:
s_cbranch_execz .Lhop_o_r
v_cndmask_b32_e64 v10, v10, v9, s[36:37]
Mvaa 24,180,8
Mvaa 24,184,48
Mvaa 24,188,88
Mvaa 28,180,12
Mvaa 28,184,52
Mvaa 28,188,240
Mvaa 32,180,16
Mvaa 32,184,56
Mvaa 32,188,244
s_waitcnt vmcnt(15) lgkmcnt(0)
s_barrier
Mvaa 52,220,244
s_nop 0
Ta 180,%0,24576
Ta 182,%0,26624
Mvaa 52,196,56
buffer_store_dword a255, v10, s[92:95], 0 offen offset:384 sc1
Mvaa 52,192,16
s_nop 0
Ta 184,%0,28672
Ta 186,%0,30720
Mvaa 48,220,240
Mvaa 48,196,52
s_nop 0
Ta 188,%0,32768
Ta 190,%0,34816
Mvaa 48,192,12
Mvaa 44,220,88
Mvaa 44,196,48
Mvaa 44,192,8
Mvaa 40,220,84
Mvaa 40,196,44
Mvaa 40,192,4
Mvaa 36,220,80
Mvaa 36,196,40
Mvaa 36,192,0
W_ 0
Mvaa 56,180,120
S_ s81,s6,7
Ta 192,v1,24576
Ta 194,v1,26624
Mvaa 56,184,160
s_and_b32 s81, s81, 7
Mvaa 56,188,200
s_mul_i32 s81, s81, 20480
Ta 196,v1,28672
Ta 198,v1,30720
Mvaa 60,180,124
s_mov_b32 m0, s52
S_ s73,s81,s78
Mvaa 60,184,164
S_ s73,s73,4096
Ta 220,v1,32768
Ta 222,v1,34816
Mvaa 60,188,204
s_nop 0
Y_ 56,0
Y_ 57,256
Mvaa 64,180,128
s_nop 0
Y_ 58,512
Y_ 59,768
Mvaa 64,184,168
Mvaa 64,188,208
s_nop 0
Y_ 60,1024
Y_ 61,1280
Mvaa 68,180,132
s_nop 0
Y_ 62,1536
Y_ 63,1792
Mvaa 68,184,172
Mvaa 68,188,212
s_nop 0
Y_ 64,2048
Y_ 65,2304
Mvaa 72,180,136
s_nop 0
Y_ 66,2560
Y_ 67,2816
Mvaa 72,184,176
Mvaa 72,188,216
W_ 12
Y_ 68,3072
Y_ 69,3328
Mvaa 92,220,216
s_nop 0
Ta 180,v2,24576
Ta 182,v2,26624
Mvaa 92,196,176
s_nop 0
Y_ 70,3584
Y_ 71,3840
Mvaa 92,192,136
s_nop 0
Ta 184,v2,28672
Ta 186,v2,30720
Mvaa 88,220,212
s_nop 0
Y_ 72,4096
Y_ 73,4352
Mvaa 88,196,172
s_nop 0
Ta 188,v2,32768
Ta 190,v2,34816
Mvaa 88,192,132
s_nop 0
Y_ 74,4608
Y_ 75,4864
Mvaa 84,220,208
s_nop 0
Y_ 92,9216
Y_ 93,9472
Mvaa 84,196,168
s_nop 0
Y_ 94,9728
Y_ 95,9984
Mvaa 84,192,128
s_nop 0
Y_ 88,8192
Y_ 89,8448
Mvaa 80,220,204
s_nop 0
Y_ 90,8704
Y_ 91,8960
Mvaa 80,196,164
Mvaa 80,192,124
s_nop 0
Y_ 84,7168
Y_ 85,7424
Mvaa 76,220,200
s_nop 0
Y_ 86,7680
Y_ 87,7936
Mvaa 76,196,160
Mvaa 76,192,120
W_ 14
Y_ 80,6144
Y_ 81,6400
Mvaa 16,180,120
s_nop 0
Ta 192,v3,24576
Ta 194,v3,26624
Mvaa 16,184,160
s_nop 0
Y_ 82,6656
Y_ 83,6912
Mvaa 16,188,200
s_nop 0
Ta 196,v3,28672
Ta 198,v3,30720
Mvaa 20,180,124
s_nop 0
Y_ 76,5120
Y_ 77,5376
Mvaa 20,184,164
s_nop 0
Ta 220,v3,32768
Ta 222,v3,34816
Mvaa 20,188,204
s_nop 0
Y_ 78,5632
Y_ 79,5888
Mvaa 24,180,128
Mvaa 24,184,168
Mvaa 24,188,208
s_waitcnt vmcnt(14) lgkmcnt(0)
s_barrier
Mvaa 28,180,132
S_ s55,s71,122880
ds_read2st64_b32 v[96:97], v5 offset0:0 offset1:1
Mvaa 28,184,172
S_ m0,s51,36864
ds_read2st64_b32 v[98:99], v5 offset0:2 offset1:3
Mvaa 28,188,212
L_ v7,24,s55,0
Mvaa 32,180,136
S_ s55,s71,133120
ds_read2st64_b32 v[104:105], v5 offset0:4 offset1:5
Mvaa 32,184,176
s_waitcnt vmcnt(11)
ds_read2st64_b32 v[106:107], v5 offset0:6 offset1:7
S_ m0,s51,37888
Mvaa 32,188,216
ds_read2st64_b32 v[112:113], v5 offset0:8 offset1:9
v_cmp_eq_u32 vcc, s62, v11
Mvaa 52,220,216
s_mul_i32 s76, s75, 204800
Ta 180,%0,0
Ta 182,%0,2048
Mvaa 52,196,176
L_ v7,24,s55,0
Mvaa 52,192,136
W_ 5
Ta 184,%0,4096
Ta 186,%0,6144
Mvaa 48,220,212
s_bitcmp1_b32 vcc_lo, s75
s_cbranch_scc0 .Lspin4
.Lspun4:
ds_read2st64_b32 v[114:115], v5 offset0:10 offset1:11
Mvaa 48,196,172
S_ s55,s71,143360
Ta 188,%0,8192
Ta 190,%0,10240
Mvaa 48,192,132
S_ m0,s51,38912
ds_read2st64_b32 v[120:121], v5 offset0:12 offset1:13
A_ v96,v16,v96
Mvaa 44,220,208
L_ v7,24,s55,0
Mvaa 44,196,168
S_ s55,s71,153600
ds_read2st64_b32 v[122:123], v5 offset0:14 offset1:15
A_ v97,v17,v97
Mvaa 44,192,128
S_ m0,s51,39936
ds_read2st64_b32 v[128:129], v5 offset0:16 offset1:17
A_ v98,v18,v98
Mvaa 40,220,204
ds_read2st64_b32 v[130:131], v5 offset0:18 offset1:19
A_ v99,v19,v99
Mvaa 40,196,164
L_ v7,24,s55,0
Mvaa 40,192,124
W_ 12
ds_read2st64_b32 v[132:133], v5 offset0:36 offset1:37
A_ v104,v20,v104
Mvaa 36,220,200
S_ s55,s71,163840
ds_read2st64_b32 v[134:135], v5 offset0:38 offset1:39
A_ v105,v21,v105
Mvaa 36,196,160
S_ m0,s51,40960
ds_read2st64_b32 v[124:125], v5 offset0:32 offset1:33
A_ v106,v22,v106
Mvaa 36,192,120
L_ v7,24,s55,0
s_waitcnt vmcnt(26)
Maaa 100,70,20
S_ s55,s71,174080
ds_read2st64_b32 v[126:127], v5 offset0:34 offset1:35
A_ v107,v23,v107
Maaa 140,68,20
W_ 10
ds_read2st64_b32 v[116:117], v5 offset0:28 offset1:29
A_ v112,v24,v112
Maaa 104,70,24
S_ m0,s51,41984
ds_read2st64_b32 v[118:119], v5 offset0:30 offset1:31
A_ v113,v25,v113
Maaa 144,68,24
L_ v7,24,s55,0
Maaa 108,70,28
ds_read2st64_b32 v[108:109], v5 offset0:24 offset1:25
A_ v114,v26,v114
Maaa 148,68,28
ds_read2st64_b32 v[110:111], v5 offset0:26 offset1:27
A_ v115,v27,v115
Maaa 112,70,32
W_ 10
ds_read2st64_b32 v[100:101], v5 offset0:20 offset1:21
A_ v120,v28,v120
Maaa 152,68,32
ds_read2st64_b32 v[102:103], v5 offset0:22 offset1:23
A_ v121,v29,v121
Maaa 116,70,36
A_ v122,v30,v122
A_ v123,v31,v123
Maaa 156,68,36
W_ 10
A_ v128,v32,v128
A_ v129,v33,v129
Maaa 224,70,60
A_ v130,v34,v130
A_ v131,v35,v131
Maaa 228,68,60
W_ 8
A_ v132,v52,v132
A_ v133,v53,v133
Maaa 232,70,64
A_ v134,v54,v134
A_ v135,v55,v135
Maaa 236,68,64
W_ 7
A_ v124,v48,v124
W_ 6
Zvav 56,180,136
s_nop 0
Ta 192,v1,0
Ta 194,v1,2048
Mvav 56,184,156
A_ v125,v49,v125
A_ v126,v50,v126
Mvav 56,188,176
S_ s56,s76,s84
Ta 196,v1,4096
Ta 198,v1,6144
Zvav 60,180,140
buffer_load_dwordx4 a[20:23], v6, s[28:31], s56 offen offset:0 sc1
Mvav 60,184,160
s_nop 0
Ta 220,v1,8192
Ta 222,v1,10240
Mvav 60,188,180
A_ v127,v51,v127
C_ 131,130,131
Zvav 64,180,144
W_ 10
A_ v116,v44,v116
A_ v117,v45,v117
Mvav 64,184,164
buffer_load_dwordx4 a[24:27], v6, s[28:31], s56 offen offset:1024 sc1
Mvav 64,188,184
A_ v118,v46,v118
A_ v119,v47,v119
Zvav 68,180,148
W_ 8
A_ v108,v40,v108
A_ v109,v41,v109
Mvav 68,184,168
A_ v110,v42,v110
A_ v111,v43,v111
Mvav 68,188,188
buffer_load_dwordx4 a[28:31], v6, s[28:31], s56 offen offset:2048 sc1
Zvav 72,180,152
W_ 6
A_ v100,v36,v100
A_ v101,v37,v101
Mvav 72,184,172
A_ v102,v38,v102
A_ v103,v39,v103
Mvav 72,188,192
W_ 0
C_ 130,128,129
C_ 132,132,133
Zvav 92,220,192
s_nop 0
Ta 180,v2,0
Ta 182,v2,2048
Mvav 92,196,172
buffer_load_dwordx4 a[32:35], v6, s[28:31], s56 offen offset:3072 sc1
Mvav 92,192,152
S_ s56,s56,4096
Ta 184,v2,4096
Ta 186,v2,6144
Zvav 88,220,188
C_ 133,134,135
Mvav 88,196,168
s_nop 0
Ta 188,v2,8192
Ta 190,v2,10240
Mvav 88,192,148
buffer_load_dwordx4 a[36:39], v6, s[28:31], s56 offen offset:0 sc1
Zvav 84,220,184
S_ s56,s76,s74
Mvav 84,196,164
Mvav 84,192,144
Zvav 80,220,180
buffer_load_dwordx4 a[60:63], v6, s[28:31], s56 offen offset:0 sc1
Mvav 80,196,160
S_ s56,s76,s82
Mvav 80,192,140
Zvav 76,220,176
Mvav 76,196,156
buffer_load_dwordx4 a[64:67], v6, s[28:31], s56 offen offset:0 sc1
Mvav 76,192,136
W_ 0
s_sub_u32 s75, s6, 6
Maav 100,180,136
s_and_b32 s75, s75, 7
Ta 192,v3,0
Ta 194,v3,2048
Maav 100,184,156
Maav 100,188,176
s_nop 0
Ta 196,v3,4096
Ta 198,v3,6144
Maav 104,180,140
buffer_load_dword v11, v8, s[92:95], s96 offen offset:384 sc1
Maav 104,184,160
s_nop 0
Ta 220,v3,8192
Ta 222,v3,10240
Maav 104,188,180
Maav 108,180,144
Maav 108,184,164
buffer_store_dwordx4 v[130:133], v6, s[28:31], s73 offen offset:0 sc1
Maav 108,188,184
s_sub_u32 s73, s73, 4096
Maav 112,180,148
C_ 123,122,123
C_ 122,120,121
Maav 112,184,168
C_ 124,124,125
C_ 125,126,127
Maav 112,188,188
buffer_store_dwordx4 v[122:125], v6, s[28:31], s73 offen offset:3072 sc1
Maav 116,180,152
C_ 115,114,115
C_ 114,112,113
Maav 116,184,172
C_ 116,116,117
C_ 117,118,119
Maav 116,188,192
s_waitcnt vmcnt(23) lgkmcnt(0)
s_barrier
Maav 156,220,192
s_nop 0
Ta 180,%0,12288
Ta 182,%0,14336
Maav 156,196,172
buffer_store_dwordx4 v[114:117], v6, s[28:31], s73 offen offset:2048 sc1
Maav 156,192,152
s_nop 0
Ta 184,%0,16384
Ta 186,%0,18432
Maav 152,220,188
C_ 107,106,107
C_ 106,104,105
Maav 152,196,168
s_nop 0
Ta 188,%0,20480
Ta 190,%0,22528
Maav 152,192,148
C_ 108,108,109
C_ 109,110,111
Maav 148,220,184
buffer_store_dwordx4 v[106:109], v6, s[28:31], s73 offen offset:1024 sc1
Maav 148,196,164
C_ 99,98,99
C_ 98,96,97
Maav 148,192,144
C_ 100,100,101
C_ 101,102,103
Maav 144,220,180
Maav 144,196,160
buffer_store_dwordx4 v[98:101], v6, s[28:31], s73 offen offset:0 sc1
Maav 144,192,140
Maav 140,220,176
Maav 140,196,156
Maav 140,192,136
W_ 0
Mvav 56,180,196
s_nop 0
Ta 192,v1,12288
Ta 194,v1,14336
Mvav 56,184,216
Mvav 56,188,236
s_nop 0
Ta 196,v1,16384
Ta 198,v1,18432
Mvav 60,180,200
Mvav 60,184,220
s_nop 0
Ta 220,v1,20480
Ta 222,v1,22528
Mvav 60,188,240
v_bfe_u32 v4, v6, 4, 4
Mvav 64,180,204
v_mul_u32_u24 v4, 2560, v4
Mvav 64,184,224
v_bfe_u32 v10, v6, 8, 1
Mvav 64,188,244
v_lshl_add_u32 v4, v10, 5, v4
Mvav 68,180,208
v_lshrrev_b32 v10, 9, v6
Mvav 68,184,228
v_lshl_add_u32 v4, v10, 4, v4
Mvav 68,188,248
Mvav 72,180,212
Mvav 72,184,232
Mvav 72,188,252
W_ 0
Mvav 92,220,252
s_nop 0
Ta 180,v2,12288
Ta 182,v2,14336
Mvav 92,196,232
Mvav 92,192,212
s_nop 0
Ta 184,v2,16384
Ta 186,v2,18432
Mvav 88,220,248
Mvav 88,196,228
s_nop 0
Ta 188,v2,20480
Ta 190,v2,22528
Mvav 88,192,208
Mvav 84,220,244
Mvav 84,196,224
Mvav 84,192,204
Mvav 80,220,240
Mvav 80,196,220
Mvav 80,192,200
Mvav 76,220,236
Mvav 76,196,216
Mvav 76,192,196
W_ 0
Maav 100,180,196
s_nop 0
Ta 192,v3,12288
Ta 194,v3,14336
Maav 100,184,216
Maav 100,188,236
s_nop 0
Ta 196,v3,16384
Ta 198,v3,18432
Maav 104,180,200
Maav 104,184,220
s_nop 0
Ta 220,v3,20480
Ta 222,v3,22528
Maav 104,188,240
Maav 108,180,204
S_ s86,s6,1
Maav 108,184,224
s_and_b32 s86, s86, 7
Maav 108,188,244
s_waitcnt vmcnt(5)
Maav 112,180,208
v_cmp_eq_u32 vcc, s62, v11
Maav 112,184,228
s_mul_i32 s76, s75, 204800
Maav 112,188,248
s_bitcmp1_b32 vcc_lo, s75
s_cbranch_scc0 .Lspin5
.Lspun5:
Maav 116,180,212
s_bitcmp1_b32 vcc_lo, s86
s_cbranch_scc0 .Lspin6
.Lspun6:
Maav 116,184,232
S_ s56,s76,s84
Maav 116,188,252
s_waitcnt vmcnt(20) lgkmcnt(0)
s_barrier
Maav 156,220,252
s_nop 0
Ta 180,%0,49152
Ta 182,%0,51200
Maav 156,196,232
buffer_load_dwordx4 v[16:19], v6, s[28:31], s56 offen offset:0 sc1
Maav 156,192,212
s_nop 0
Ta 184,%0,53248
Ta 186,%0,55296
Maav 152,220,248
Maav 152,196,228
s_nop 0
Ta 188,%0,57344
Ta 190,%0,59392
Maav 152,192,208
buffer_load_dwordx4 v[20:23], v6, s[28:31], s56 offen offset:1024 sc1
Maav 148,220,244
Maav 148,196,224
Maav 148,192,204
Maav 144,220,240
buffer_load_dwordx4 v[24:27], v6, s[28:31], s56 offen offset:2048 sc1
Maav 144,196,220
Maav 144,192,200
Maav 140,220,236
Maav 140,196,216
buffer_load_dwordx4 v[28:31], v6, s[28:31], s56 offen offset:3072 sc1
Maav 140,192,196
W_ 0
S_ s56,s56,4096
Mvaa 56,180,0
s_nop 0
Ta 192,v1,49152
Ta 194,v1,51200
Mvaa 56,184,40
R_ v164,a224
R_ v165,a225
Mvaa 56,188,80
s_nop 0
Ta 196,v1,53248
Ta 198,v1,55296
Mvaa 60,180,4
buffer_load_dwordx4 v[32:35], v6, s[28:31], s56 offen offset:0 sc1
Mvaa 60,184,44
S_ s56,s76,s74
Ta 220,v1,57344
Ta 222,v1,59392
Mvaa 60,188,84
.Lpad_o_11:
s_cbranch_execz .Lhop_o_r
R_ v166,a226
R_ v167,a227
Mvaa 64,180,8
R_ v168,a228
R_ v169,a229
Mvaa 64,184,48
buffer_load_dwordx4 v[36:39], v6, s[28:31], s56 offen offset:0 sc1
Mvaa 64,188,88
S_ s56,s76,s82
R_ v170,a230
R_ v171,a231
Mvaa 68,180,12
R_ v172,a232
R_ v173,a233
Mvaa 68,184,52
R_ v174,a234
R_ v175,a235
Mvaa 68,188,240
buffer_load_dwordx4 v[40:43], v6, s[28:31], s56 offen offset:0 sc1
Mvaa 72,180,16
s_mul_i32 s76, s86, 204800
R_ v176,a236
R_ v177,a237
Mvaa 72,184,56
S_ s80,s76,s84
R_ v178,a238
R_ v179,a239
Mvaa 72,188,244
W_ 0
S_ s56,s76,s74
Mvaa 92,220,244
s_nop 0
Ta 180,v2,49152
Ta 182,v2,51200
Mvaa 92,196,56
buffer_load_dwordx4 v[156:159], v6, s[28:31], s56 offen offset:0 sc1
Mvaa 92,192,16
S_ s56,s76,s82
Ta 184,v2,53248
Ta 186,v2,55296
Mvaa 88,220,240
Mvaa 88,196,52
s_nop 0
Ta 188,v2,57344
Ta 190,v2,59392
Mvaa 88,192,12
buffer_load_dwordx4 v[160:163], v6, s[28:31], s56 offen offset:0 sc1
Mvaa 84,220,88
Mvaa 84,196,48
Mvaa 84,192,8
Mvaa 80,220,84
Mvaa 80,196,44
Mvaa 80,192,4
Mvaa 76,220,80
Mvaa 76,196,40
Mvaa 76,192,0
W_ 0
Maaa 100,180,0
s_nop 0
Ta 192,v3,49152
Ta 194,v3,51200
Maaa 100,184,40
Maaa 100,188,80
s_nop 0
Ta 196,v3,53248
Ta 198,v3,55296
Maaa 104,180,4
Maaa 104,184,44
s_nop 0
Ta 220,v3,57344
Ta 222,v3,59392
Maaa 104,188,84
Maaa 108,180,8
R_ v180,a100
R_ v181,a101
Maaa 108,184,48
R_ v182,a102
R_ v183,a103
Maaa 108,188,88
Maaa 112,180,12
R_ v184,a104
R_ v185,a105
Maaa 112,184,52
R_ v186,a106
R_ v187,a107
Maaa 112,188,240
Maaa 116,180,16
R_ v188,a108
R_ v189,a109
Maaa 116,184,56
R_ v190,a110
R_ v191,a111
Maaa 116,188,244
s_waitcnt vmcnt(22) lgkmcnt(0)
s_barrier
Maaa 156,220,244
s_nop 0
Ta 180,%0,36864
Ta 182,%0,38912
Maaa 156,196,56
R_ v192,a112
R_ v193,a113
Maaa 156,192,16
s_nop 0
Ta 184,%0,40960
Ta 186,%0,43008
Maaa 152,220,240
R_ v194,a114
R_ v195,a115
Maaa 152,196,52
s_nop 0
Ta 188,%0,45056
Ta 190,%0,47104
Maaa 152,192,12
R_ v196,a116
R_ v197,a117
Maaa 148,220,88
R_ v198,a118
R_ v199,a119
Maaa 148,196,48
R_ v216,a156
R_ v217,a157
Maaa 148,192,8
R_ v218,a158
R_ v219,a159
Maaa 144,220,84
R_ v212,a152
R_ v213,a153
Maaa 144,196,44
R_ v214,a154
R_ v215,a155
Maaa 144,192,4
Maaa 140,220,80
R_ v208,a148
R_ v209,a149
Maaa 140,196,40
R_ v210,a150
R_ v211,a151
Maaa 140,192,0
s_waitcnt vmcnt(15)
Mvaa 164,70,60
R_ v204,a144
R_ v205,a145
Mvaa 168,68,60
R_ v206,a146
R_ v207,a147
Mvaa 172,70,64
Mvaa 176,68,64
R_ v200,a140
R_ v201,a141
Mvaa 180,70,20
R_ v202,a142
R_ v203,a143
Mvaa 184,70,24
Mvaa 188,70,28
Mvaa 192,70,32
Mvaa 196,70,36
Mvaa 216,68,36
Mvaa 212,68,32
Mvaa 208,68,28
Mvaa 204,68,24
Mvaa 200,68,20
W_ 0
Mvaa 56,180,120
s_nop 0
Ta 192,v1,36864
Ta 194,v1,38912
Mvaa 56,184,160
Mvaa 56,188,200
s_nop 0
Ta 196,v1,40960
Ta 198,v1,43008
Mvaa 60,180,124
s_mov_b32 m0, s52
Mvaa 60,184,164
s_nop 0
Ta 220,v1,45056
Ta 222,v1,47104
Mvaa 60,188,204
s_nop 0
Y_ 56,0
Y_ 57,256
Mvaa 64,180,128
s_nop 0
Y_ 58,512
Y_ 59,768
Mvaa 64,184,168
Mvaa 64,188,208
s_nop 0
Y_ 60,1024
Y_ 61,1280
Mvaa 68,180,132
s_nop 0
Y_ 62,1536
Y_ 63,1792
Mvaa 68,184,172
Mvaa 68,188,212
s_nop 0
Y_ 64,2048
Y_ 65,2304
Mvaa 72,180,136
s_nop 0
Y_ 66,2560
Y_ 67,2816
Mvaa 72,184,176
Mvaa 72,188,216
W_ 12
Y_ 68,3072
Y_ 69,3328
Mvaa 92,220,216
s_nop 0
Ta 180,v2,36864
Ta 182,v2,38912
Mvaa 92,196,176
s_nop 0
Y_ 70,3584
Y_ 71,3840
Mvaa 92,192,136
s_nop 0
Ta 184,v2,40960
Ta 186,v2,43008
Mvaa 88,220,212
s_nop 0
Y_ 72,4096
Y_ 73,4352
Mvaa 88,196,172
s_nop 0
Ta 188,v2,45056
Ta 190,v2,47104
Mvaa 88,192,132
s_nop 0
Y_ 74,4608
Y_ 75,4864
Mvaa 84,220,208
s_nop 0
Y_ 92,9216
Y_ 93,9472
Mvaa 84,196,168
s_nop 0
Y_ 94,9728
Y_ 95,9984
Mvaa 84,192,128
s_nop 0
Y_ 88,8192
Y_ 89,8448
Mvaa 80,220,204
s_nop 0
Y_ 90,8704
Y_ 91,8960
Mvaa 80,196,164
Mvaa 80,192,124
s_nop 0
Y_ 84,7168
Y_ 85,7424
Mvaa 76,220,200
s_nop 0
Y_ 86,7680
Y_ 87,7936
Mvaa 76,196,160
Mvaa 76,192,120
W_ 14
Y_ 80,6144
Y_ 81,6400
Mvaa 180,180,120
S_ s56,s76,s84
Ta 192,v3,36864
Ta 194,v3,38912
Mvaa 180,184,160
s_nop 0
Y_ 82,6656
Y_ 83,6912
Mvaa 180,188,200
s_nop 0
Ta 196,v3,40960
Ta 198,v3,43008
Mvaa 184,180,124
s_nop 0
Y_ 76,5120
Y_ 77,5376
Mvaa 184,184,164
s_nop 0
Ta 220,v3,45056
Ta 222,v3,47104
Mvaa 184,188,204
s_nop 0
Y_ 78,5632
Y_ 79,5888
Mvaa 188,180,128
buffer_load_dwordx4 v[136:139], v6, s[28:31], s56 offen offset:0 sc1
Mvaa 188,184,168
Mvaa 188,188,208
s_waitcnt vmcnt(63) lgkmcnt(0)
s_barrier
Mvaa 192,180,132
ds_read2st64_b32 v[96:97], v5 offset0:0 offset1:1
Mvaa 192,184,172
buffer_load_dwordx4 v[140:143], v6, s[28:31], s56 offen offset:1024 sc1
Mvaa 192,188,212
ds_read2st64_b32 v[98:99], v5 offset0:2 offset1:3
Mvaa 196,180,136
ds_read2st64_b32 v[104:105], v5 offset0:4 offset1:5
Mvaa 196,184,176
ds_read2st64_b32 v[106:107], v5 offset0:6 offset1:7
Mvaa 196,188,216
buffer_load_dwordx4 v[144:147], v6, s[28:31], s56 offen offset:2048 sc1
Mvaa 216,220,216
ds_read2st64_b32 v[112:113], v5 offset0:8 offset1:9
Mvaa 216,196,176
ds_read2st64_b32 v[114:115], v5 offset0:10 offset1:11
W_ 5
A_ v96,v96,v180
Mvaa 216,192,136
ds_read2st64_b32 v[120:121], v5 offset0:12 offset1:13
A_ v97,v97,v181
Mvaa 212,220,212
buffer_load_dwordx4 v[148:151], v6, s[28:31], s56 offen offset:3072 sc1
Mvaa 212,196,172
S_ s56,s56,4096
ds_read2st64_b32 v[122:123], v5 offset0:14 offset1:15
W_ 6
A_ v98,v98,v182
Mvaa 212,192,132
ds_read2st64_b32 v[128:129], v5 offset0:16 offset1:17
A_ v99,v99,v183
Mvaa 208,220,208
ds_read2st64_b32 v[130:131], v5 offset0:18 offset1:19
W_ 7
A_ v104,v104,v184
Mvaa 208,196,168
buffer_load_dwordx4 v[152:155], v6, s[28:31], s56 offen offset:0 sc1
Mvaa 208,192,128
ds_read2st64_b32 v[132:133], v5 offset0:36 offset1:37
A_ v105,v105,v185
Mvaa 204,220,204
ds_read2st64_b32 v[134:135], v5 offset0:38 offset1:39
W_ 8
A_ v106,v106,v186
Mvaa 204,196,164
ds_read2st64_b32 v[124:125], v5 offset0:32 offset1:33
A_ v107,v107,v187
Mvaa 204,192,124
ds_read2st64_b32 v[126:127], v5 offset0:34 offset1:35
W_ 9
A_ v112,v112,v188
Mvaa 200,220,200
ds_read2st64_b32 v[116:117], v5 offset0:28 offset1:29
A_ v113,v113,v189
Mvaa 200,196,160
ds_read2st64_b32 v[118:119], v5 offset0:30 offset1:31
W_ 10
A_ v114,v114,v190
Mvaa 200,192,120
ds_read2st64_b32 v[108:109], v5 offset0:24 offset1:25
A_ v115,v115,v191
ds_read2st64_b32 v[110:111], v5 offset0:26 offset1:27
ds_read2st64_b32 v[100:101], v5 offset0:20 offset1:21
ds_read2st64_b32 v[102:103], v5 offset0:22 offset1:23
W_ 13
A_ v120,v120,v192
A_ v121,v121,v193
W_ 12
A_ v122,v122,v194
A_ v123,v123,v195
W_ 11
A_ v128,v128,v196
A_ v129,v129,v197
W_ 10
A_ v130,v130,v198
A_ v131,v131,v199
s_waitcnt vmcnt(5)
Mvav 164,70,36
W_ 9
A_ v132,v132,v216
A_ v133,v133,v217
Mvav 168,68,36
W_ 8
A_ v134,v134,v218
A_ v135,v135,v219
Mvav 172,70,40
W_ 7
A_ v124,v124,v212
A_ v125,v125,v213
Mvav 176,68,40
W_ 6
A_ v126,v126,v214
A_ v127,v127,v215
Mvav 164,70,156
W_ 5
A_ v116,v116,v208
A_ v117,v117,v209
Mvav 168,68,156
W_ 4
A_ v118,v118,v210
A_ v119,v119,v211
Mvav 172,70,160
W_ 3
A_ v108,v108,v204
A_ v109,v109,v205
Mvav 176,68,160
W_ 2
A_ v110,v110,v206
A_ v111,v111,v207
Mvav 96,70,16
W_ 1
A_ v100,v100,v200
A_ v101,v101,v201
Mvav 104,70,20
W_ 0
A_ v102,v102,v202
A_ v103,v103,v203
Mvav 112,70,24
C_ 220,164,165
C_ 221,166,167
Mvav 120,70,28
C_ 222,168,169
C_ 223,170,171
Mvav 128,70,32
v_permlane16_swap_b32 v220, v222
v_permlane16_swap_b32 v221, v223
Mvav 132,68,32
buffer_store_dwordx4 v[220:223], v4, s[32:35], s9 offen sc1
Mvav 124,68,28
C_ 228,172,173
C_ 229,174,175
Mvav 116,68,24
C_ 230,176,177
C_ 231,178,179
Mvav 108,68,20
v_permlane16_swap_b32 v228, v230
v_permlane16_swap_b32 v229, v231
Mvav 100,68,16
buffer_store_dwordx4 v[228:231], v4, s[32:35], s10 offen sc1
s_waitcnt vmcnt(2)
.Lsel6:
Nvav 180,70,136,96
Nvav 188,70,140,104
Nvav 196,70,144,112
Nvav 204,70,148,120
Nvav 212,70,152,128
Nvav 184,68,136,100
s_nop 3
A_ v10,v180,v188
Nvav 192,68,140,108
A_ v11,v196,v204
Nvav 200,68,144,116
A_ v10,v10,v212
v_cmp_u_f32 vcc, v10, v11
Nvav 208,68,148,124
s_cbranch_vccnz .Lretry
.Lgood:
Nvav 216,68,152,132
C_ 180,180,181
C_ 181,182,183
C_ 182,184,185
C_ 183,186,187
s_nop 0
v_permlane16_swap_b32 v180, v182
v_permlane16_swap_b32 v181, v183
buffer_store_dwordx4 v[180:183], v4, s[32:35], s8 offen sc1
C_ 188,188,189
C_ 189,190,191
C_ 190,192,193
C_ 191,194,195
S_ s8,s8,40960
v_permlane16_swap_b32 v188, v190
v_permlane16_swap_b32 v189, v191
buffer_store_dwordx4 v[188:191], v4, s[32:35], s8 offen sc1
C_ 196,196,197
C_ 197,198,199
C_ 198,200,201
C_ 199,202,203
S_ s8,s8,40960
v_permlane16_swap_b32 v196, v198
v_permlane16_swap_b32 v197, v199
buffer_store_dwordx4 v[196:199], v4, s[32:35], s8 offen sc1
C_ 204,204,205
C_ 205,206,207
C_ 206,208,209
C_ 207,210,211
S_ s8,s8,40960
v_permlane16_swap_b32 v204, v206
v_permlane16_swap_b32 v205, v207
buffer_store_dwordx4 v[204:207], v4, s[32:35], s8 offen sc1
C_ 212,212,213
C_ 213,214,215
C_ 214,216,217
C_ 215,218,219
S_ s8,s8,40960
v_permlane16_swap_b32 v212, v214
v_permlane16_swap_b32 v213, v215
buffer_store_dwordx4 v[212:215], v4, s[32:35], s8 offen sc1
s_endpgm
.Lspin0:
s_cmp_eq_u32 s57, 0
s_cbranch_scc1 .Lspun0
s_sub_u32 s57, s57, 1
s_sleep 4
buffer_load_dword v11, v8, s[92:95], s96 offen offset:384 sc1
s_waitcnt vmcnt(0)
v_cmp_eq_u32 vcc, s62, v11
s_nop 1
s_bitcmp1_b32 vcc_lo, s75
s_cbranch_scc0 .Lspin0
s_branch .Lspun0
.Lspin1:
s_cmp_eq_u32 s57, 0
s_cbranch_scc1 .Lspun1
s_sub_u32 s57, s57, 1
s_sleep 4
buffer_load_dword v11, v8, s[92:95], s96 offen offset:384 sc1
s_waitcnt vmcnt(0)
v_cmp_eq_u32 vcc, s62, v11
s_nop 1
s_bitcmp1_b32 vcc_lo, s75
s_cbranch_scc0 .Lspin1
s_branch .Lspun1
.Lspin2:
s_cmp_eq_u32 s57, 0
s_cbranch_scc1 .Lspun2
s_sub_u32 s57, s57, 1
s_sleep 4
buffer_load_dword v11, v8, s[92:95], s96 offen offset:384 sc1
s_waitcnt vmcnt(0)
v_cmp_eq_u32 vcc, s62, v11
s_nop 1
s_bitcmp1_b32 vcc_lo, s75
s_cbranch_scc0 .Lspin2
s_branch .Lspun2
.Lspin3:
s_cmp_eq_u32 s57, 0
s_cbranch_scc1 .Lspun3
s_sub_u32 s57, s57, 1
s_sleep 4
buffer_load_dword v11, v8, s[92:95], s96 offen offset:384 sc1
s_waitcnt vmcnt(0)
v_cmp_eq_u32 vcc, s62, v11
s_nop 1
s_bitcmp1_b32 vcc_lo, s75
s_cbranch_scc0 .Lspin3
s_branch .Lspun3
.Lspin4:
s_cmp_eq_u32 s57, 0
s_cbranch_scc1 .Lspun4
s_sub_u32 s57, s57, 1
s_sleep 4
buffer_load_dword v11, v8, s[92:95], s96 offen offset:384 sc1
s_waitcnt vmcnt(0)
v_cmp_eq_u32 vcc, s62, v11
s_nop 1
s_bitcmp1_b32 vcc_lo, s75
s_cbranch_scc0 .Lspin4
s_branch .Lspun4
.Lspin5:
s_cmp_eq_u32 s57, 0
s_cbranch_scc1 .Lspun5
s_sub_u32 s57, s57, 1
s_sleep 4
buffer_load_dword v11, v8, s[92:95], s96 offen offset:384 sc1
s_waitcnt vmcnt(0)
v_cmp_eq_u32 vcc, s62, v11
s_nop 1
s_bitcmp1_b32 vcc_lo, s75
s_cbranch_scc0 .Lspin5
s_branch .Lspun5
.Lspin6:
s_cmp_eq_u32 s57, 0
s_cbranch_scc1 .Lspun6
s_sub_u32 s57, s57, 1
s_sleep 4
buffer_load_dword v11, v8, s[92:95], s96 offen offset:384 sc1
s_waitcnt vmcnt(0)
v_cmp_eq_u32 vcc, s62, v11
s_nop 1
s_bitcmp1_b32 vcc_lo, s86
s_cbranch_scc0 .Lspin6
s_branch .Lspun6
.Lretry:
s_cmp_eq_u32 s57, 0
s_cbranch_scc1 .Lgood
s_sub_u32 s57, s57, 1
v_max_u32 v10, v136, v137
v_max3_u32 v10, v10, v138, v139
v_max3_u32 v10, v10, v140, v141
v_max3_u32 v10, v10, v142, v143
v_max3_u32 v10, v10, v144, v145
v_max3_u32 v10, v10, v146, v147
v_max3_u32 v10, v10, v148, v149
v_max3_u32 v10, v10, v150, v151
v_max3_u32 v10, v10, v152, v153
v_max3_u32 v10, v10, v154, v155
v_cmp_eq_u32 vcc, -1, v10
s_nop 4
s_cbranch_vccz .Lgood
s_nop 7
s_nop 7
S_ s53,s80,4096
buffer_load_dwordx4 v[136:139], v6, s[28:31], s80 offen offset:0 sc1
buffer_load_dwordx4 v[140:143], v6, s[28:31], s80 offen offset:1024 sc1
buffer_load_dwordx4 v[144:147], v6, s[28:31], s80 offen offset:2048 sc1
buffer_load_dwordx4 v[148:151], v6, s[28:31], s80 offen offset:3072 sc1
buffer_load_dwordx4 v[152:155], v6, s[28:31], s53 offen offset:0 sc1
s_waitcnt vmcnt(0)
s_branch .Lsel6
.Ldone:
s_waitcnt vmcnt(0)
.purgem S_
.purgem W_
.purgem L_
.purgem X_
.purgem R_
.purgem Ta
.purgem Da
.purgem Zvaa
.purgem Mvaa
.purgem U_
.purgem C_
.purgem Ln
.purgem Dv
.purgem Zvvv
.purgem Mvvv
.purgem B_
.purgem E_
.purgem Nvvv
.purgem Tv
.purgem Zaav
.purgem Zavv
.purgem A_
.purgem Mavv
.purgem Zvav
.purgem Mvav
.purgem Y_
.purgem Zaaa
.purgem Maaa
.purgem Maav
.purgem Nvav
)A"
    : "+v"(tid)
    : "s"(__builtin_amdgcn_kernarg_segment_ptr()), "s"(blockIdx.x)
    : "v1", "v2", "v3", "v4", "v5", "v6", "v7", "v8", "v9", "v10", "v11", "v12", "v13", "v14", "v15", "v16", "v17", "v18", "v19", "v20", "v21", "v22", "v23", "v24", "v25", "v26", "v27", "v28", "v29", "v30", "v31", "v32", "v33", "v34", "v35", "v36", "v37", "v38", "v39", "v40", "v41", "v42", "v43", "v44", "v45", "v46", "v47", "v48", "v49", "v50", "v51", "v52", "v53", "v54", "v55", "v56", "v57", "v58", "v59", "v60", "v61", "v62", "v63", "v64", "v65", "v66", "v67", "v68", "v69", "v70", "v71", "v72", "v73", "v74", "v75", "v76", "v77", "v78", "v79", "v80", "v81", "v82", "v83", "v84", "v85", "v86", "v87", "v88", "v89", "v90", "v91", "v92", "v93", "v94", "v95", "v96", "v97", "v98", "v99", "v100", "v101", "v102", "v103", "v104", "v105", "v106", "v107", "v108", "v109", "v110", "v111", "v112", "v113", "v114", "v115", "v116", "v117", "v118", "v119", "v120", "v121", "v122", "v123", "v124", "v125", "v126", "v127", "v128", "v129", "v130", "v131", "v132", "v133", "v134", "v135", "v136", "v137", "v138", "v139", "v140", "v141", "v142", "v143", "v144", "v145", "v146", "v147", "v148", "v149", "v150", "v151", "v152", "v153", "v154", "v155", "v156", "v157", "v158", "v159", "v160", "v161", "v162", "v163", "v164", "v165", "v166", "v167", "v168", "v169", "v170", "v171", "v172", "v173", "v174", "v175", "v176", "v177", "v178", "v179", "v180", "v181", "v182", "v183", "v184", "v185", "v186", "v187", "v188", "v189", "v190", "v191", "v192", "v193", "v194", "v195", "v196", "v197", "v198", "v199", "v200", "v201", "v202", "v203", "v204", "v205", "v206", "v207", "v208", "v209", "v210", "v211", "v212", "v213", "v214", "v215", "v216", "v217", "v218", "v219", "v220", "v221", "v222", "v223", "v224", "v225", "v226", "v227", "v228", "v229", "v230", "v231", "v232", "v233", "v234", "v235", "v236", "v237", "v238", "v239", "v240", "v241", "v242", "v243", "v244", "v245", "v246", "v247", "v248", "v249", "v250", "v251", "v252", "v253", "v254", "v255", "a0", "a1", "a2", "a3", "a4", "a5", "a6", "a7", "a8", "a9", "a10", "a11", "a12", "a13", "a14", "a15", "a16", "a17", "a18", "a19", "a20", "a21", "a22", "a23", "a24", "a25", "a26", "a27", "a28", "a29", "a30", "a31", "a32", "a33", "a34", "a35", "a36", "a37", "a38", "a39", "a40", "a41", "a42", "a43", "a44", "a45", "a46", "a47", "a48", "a49", "a50", "a51", "a52", "a53", "a54", "a55", "a56", "a57", "a58", "a59", "a60", "a61", "a62", "a63", "a64", "a65", "a66", "a67", "a68", "a69", "a70", "a71", "a72", "a73", "a74", "a75", "a76", "a77", "a78", "a79", "a80", "a81", "a82", "a83", "a84", "a85", "a86", "a87", "a88", "a89", "a90", "a91", "a92", "a93", "a94", "a95", "a96", "a97", "a98", "a99", "a100", "a101", "a102", "a103", "a104", "a105", "a106", "a107", "a108", "a109", "a110", "a111", "a112", "a113", "a114", "a115", "a116", "a117", "a118", "a119", "a120", "a121", "a122", "a123", "a124", "a125", "a126", "a127", "a128", "a129", "a130", "a131", "a132", "a133", "a134", "a135", "a136", "a137", "a138", "a139", "a140", "a141", "a142", "a143", "a144", "a145", "a146", "a147", "a148", "a149", "a150", "a151", "a152", "a153", "a154", "a155", "a156", "a157", "a158", "a159", "a160", "a161", "a162", "a163", "a164", "a165", "a166", "a167", "a168", "a169", "a170", "a171", "a172", "a173", "a174", "a175", "a176", "a177", "a178", "a179", "a180", "a181", "a182", "a183", "a184", "a185", "a186", "a187", "a188", "a189", "a190", "a191", "a192", "a193", "a194", "a195", "a196", "a197", "a198", "a199", "a200", "a201", "a202", "a203", "a204", "a205", "a206", "a207", "a208", "a209", "a210", "a211", "a212", "a213", "a214", "a215", "a216", "a217", "a218", "a219", "a220", "a221", "a222", "a223", "a224", "a225", "a226", "a227", "a228", "a229", "a230", "a231", "a232", "a233", "a234", "a235", "a236", "a237", "a238", "a239", "a240", "a241", "a242", "a243", "a244", "a245", "a246", "a247", "a248", "a249", "a250", "a251", "a252", "a253", "a254", "a255", "s3", "s4", "s5", "s6", "s7", "s8", "s9", "s10", "s11", "s12", "s13", "s14", "s15", "s16", "s17", "s18", "s19", "s20", "s21", "s22", "s23", "s24", "s25", "s26", "s27", "s28", "s29", "s30", "s31", "s32", "s33", "s34", "s35", "s36", "s37", "s38", "s39", "s40", "s41", "s42", "s43", "s44", "s45", "s46", "s47", "s48", "s49", "s50", "s51", "s52", "s53", "s54", "s55", "s56", "s57", "s58", "s59", "s60", "s61", "s62", "s63", "s64", "s65", "s66", "s67", "s68", "s69", "s70", "s71", "s72", "s73", "s74", "s75", "s76", "s77", "s78", "s79", "s80", "s81", "s82", "s83", "s84", "s85", "s86", "s87", "s88", "s89", "s90", "s91", "s92", "s93", "s94", "s95", "s96", "s97", "s98", "s99", "s100", "s101", "vcc", "scc", "memory");
}

extern "C" void kernel_launch(void* const* d_in, const int* in_sizes, int n_in, void* d_out, int out_size, void* d_ws, size_t ws_size, hipStream_t stream) {
  Args P;
  for (int br = 0; br < 2; ++br) {
    P.p[br][0] = d_in[5 * br]; P.p[br][1] = d_in[5 * br + 3]; P.p[br][2] = d_in[5 * br + 1]; P.p[br][3] = d_in[5 * br + 2]; P.p[br][4] = d_in[5 * br + 4];
    P.p[br][5] = d_ws; P.p[br][6] = (char*)d_out + br * 1280; P.p[br][7] = d_ws;
  }
  (void)hipFuncSetAttribute((const void*)fused_kernel, hipFuncAttributeMaxDynamicSharedMemorySize, LDS1);
  fused_kernel<<<256, 256, LDS1, stream>>>(P);
}
```

```cpp
#include <hip/hip_runtime.h>
#include <hip/hip_bf16.h>

constexpr unsigned LDS1 = 160 * 1024;

struct Args { const void* p[2][8]; };

__global__ __launch_bounds__(256, 1) void fused_kernel(Args P) {
  unsigned tid = threadIdx.x;
  asm volatile(
R"A(.macro S_ a,b,c
s_add_u32 \a, \b, \c
.endm
.macro W_ a
s_waitcnt lgkmcnt(\a)
.endm
.macro L_ x,r,s,o
buffer_load_dwordx4 \x, s[\r:\r+3], \s offen offset:\o lds
.endm
.macro X_ a,b
v_accvgpr_write_b32 \a, \b
.endm
.macro R_ a,b
v_accvgpr_read_b32 \a, \b
.endm
.macro Ta d,x,o
ds_read_b64_tr_b16 a[\d:\d+1], \x offset:\o
.endm
.macro Da d,x,o
ds_read_b128 a[\d:\d+3], \x offset:\o
.endm
.macro Zvaa d,a,b
v_mfma_f32_16x16x32_bf16 v[\d:\d+3], a[\a:\a+3], a[\b:\b+3], 0
.endm
.macro Mvaa d,a,b
v_mfma_f32_16x16x32_bf16 v[\d:\d+3], a[\a:\a+3], a[\b:\b+3], v[\d:\d+3]
.endm
.macro U_ a,b,c
v_mul_f32 \a, \b, \c
.endm
.macro C_ d,a,b
v_cvt_pk_bf16_f32 v\d, v\a, v\b
.endm
.macro Ln x,r,s,o
buffer_load_dwordx4 \x, s[\r:\r+3], \s offen offset:\o nt lds
.endm
.macro Dv d,x,o
ds_read_b128 v[\d:\d+3], \x offset:\o
.endm
.macro Zvvv d,a,b
v_mfma_f32_16x16x32_bf16 v[\d:\d+3], v[\a:\a+3], v[\b:\b+3], 0
.endm
.macro Mvvv d,a,b
v_mfma_f32_16x16x32_bf16 v[\d:\d+3], v[\a:\a+3], v[\b:\b+3], v[\d:\d+3]
.endm
.macro B_ a,b,c
v_sub_f32 \a, \b, \c
.endm
.macro E_ r
v_exp_f32 v\r, v\r
.endm
.macro Nvvv d,a,b,c
v_mfma_f32_16x16x32_bf16 v[\d:\d+3], v[\a:\a+3], v[\b:\b+3], v[\c:\c+3]
.endm
.macro Tv d,x,o
ds_read_b64_tr_b16 v[\d:\d+1], \x offset:\o
.endm
.macro Zaav d,a,b
v_mfma_f32_16x16x32_bf16 a[\d:\d+3], a[\a:\a+3], v[\b:\b+3], 0
.endm
.macro Zavv d,a,b
v_mfma_f32_16x16x32_bf16 a[\d:\d+3], v[\a:\a+3], v[\b:\b+3], 0
.endm
.macro A_ a,b,c
v_add_f32 \a, \b, \c
.endm
.macro Mavv d,a,b
v_mfma_f32_16x16x32_bf16 a[\d:\d+3], v[\a:\a+3], v[\b:\b+3], a[\d:\d+3]
.endm
.macro Zvav d,a,b
v_mfma_f32_16x16x32_bf16 v[\d:\d+3], a[\a:\a+3], v[\b:\b+3], 0
.endm
.macro Mvav d,a,b
v_mfma_f32_16x16x32_bf16 v[\d:\d+3], a[\a:\a+3], v[\b:\b+3], v[\d:\d+3]
.endm
.macro Y_ r,o
ds_write_addtid_b32 v\r offset:\o
.endm
.macro Zaaa d,a,b
v_mfma_f32_16x16x32_bf16 a[\d:\d+3], a[\a:\a+3], a[\b:\b+3], 0
.endm
.macro Maaa d,a,b
v_mfma_f32_16x16x32_bf16 a[\d:\d+3], a[\a:\a+3], a[\b:\b+3], a[\d:\d+3]
.endm
.macro Maav d,a,b
v_mfma_f32_16x16x32_bf16 a[\d:\d+3], a[\a:\a+3], v[\b:\b+3], a[\d:\d+3]
.endm
.macro Nvav d,a,b,c
v_mfma_f32_16x16x32_bf16 v[\d:\d+3], a[\a:\a+3], v[\b:\b+3], v[\c:\c+3]
.endm
.p2align 12
s_and_b32 s3, %2, 4
s_lshl_b32 s3, s3, 4
s_load_dwordx16 s[8:23], %1, s3
s_memrealtime s[100:101]
s_memtime s[90:91]
v_readfirstlane_b32 s4, %0
v_and_b32 v16, 63, %0
v_lshlrev_b32 v10, 4, v16
s_mov_b32 s7, %2
s_lshr_b32 s4, s4, 6
.Lcode0:
s_getpc_b64 s[44:45]
s_mov_b32 s46, .Ldone-.Lcode0
s_mov_b32 s47, 0x00020000
s_lshr_b32 s66, s7, 3
s_lshl_b32 s66, s66, 2
S_ s66,s66,s4
s_lshl_b32 s66, s66, 11
v_min_u32 v40, 15, v16
v_lshlrev_b32 v40, 7, v40
buffer_load_dword v247, v40, s[44:47], s66 offen
s_and_b32 s3, s7, 1
s_lshl_b32 s3, s3, 3
s_lshr_b32 s53, s7, 5
S_ s3,s3,s53
s_and_b32 s6, s7, 2
s_lshl_b32 s6, s6, 1
s_bfe_u32 s53, s7, 0x20003
S_ s6,s6,s53
s_and_b32 s5, s7, 4
s_lshl_b32 s5, s5, 2
S_ s5,s5,s3
s_mul_i32 s50, s4, 5120
s_mul_i32 s60, s4, 51200
s_lshr_b32 s77, s4, 1
s_and_b32 s53, s4, 1
s_mul_i32 s59, s53, 393216
s_lshl_b32 s52, s53, 11
s_mul_i32 s61, s53, 6144
s_mul_i32 s51, s77, 61440
S_ s55,s51,40960
S_ s51,s55,s61
s_sub_u32 s52, s51, s52
s_lshl_b32 s86, s53, 2
s_mul_i32 s56, s53, 10240
v_lshrrev_b32 v17, 3, v16
v_and_b32 v18, 7, v16
v_lshrrev_b32 v23, 4, v16
v_and_b32 v24, 1, v23
v_lshlrev_b32 v24, 2, v24
v_xor_b32 v24, v18, v24
v_mul_u32_u24 v6, 12288, v17
v_lshl_add_u32 v6, v24, 4, v6
v_add_u32 v24, s86, v23
v_xor_b32 v24, v18, v24
v_mul_u32_u24 v4, 1280, v17
v_lshl_add_u32 v4, v24, 4, v4
v_xor_b32 v5, 64, v4
s_getreg_b32 s53, hwreg(HW_REG_XCC_ID, 0, 4)
s_bitcmp1_b32 0xaaaa, s53
s_cselect_b32 s58, 2, 0
s_cselect_b32 s89, 1, 0
s_cselect_b32 s53, 768, 0
S_ s59,s59,s53
s_lshl_b32 s54, s6, 2
S_ s54,s54,s77
s_mul_i32 s86, s54, 384
s_mul_i32 s87, s3, 204800
s_lshl_b32 s53, s3, 5
S_ s53,s53,s54
s_mul_i32 s85, s53, 196608
s_mov_b32 s30, 7853184
s_mov_b32 s31, 0x00020000
s_mov_b32 s26, 204800
s_mov_b32 s27, 0x00020000
W_ 0
s_mov_b32 s98, s100
s_mov_b32 s99, 0
s_mov_b32 s88, s90
S_ s28,s10,s86
s_addc_u32 s29, s11, 0
S_ s24,s8,s87
s_addc_u32 s25, s9, 0
s_buffer_load_dword s67, s[24:27], s60
s_mul_i32 s64, s66, 30
s_buffer_load_dword s67, s[28:31], s64
s_mov_b32 s11, 0
S_ s65,s59,0
S_ m0,s52,8192
s_mov_b64 s[92:93], s[18:19]
s_mov_b32 s53, 251658240
L_ v6,28,s65,0
S_ s65,s59,98304
S_ m0,s52,9216
S_ s92,s92,s53
s_addc_u32 s93, s93, 0
L_ v6,28,s65,0
S_ s65,s59,196608
S_ m0,s52,10240
s_mov_b32 s94, 131072
s_mov_b32 s95, 0x00020000
L_ v6,28,s65,0
S_ s65,s59,294912
S_ m0,s52,11264
s_lshl_b32 s96, s5, 3
S_ s96,s96,s6
L_ v6,28,s65,0
S_ s65,s60,0
S_ m0,s50,0
s_lshl_b32 s96, s96, 9
v_cmp_gt_u32 vcc, 8, v16
L_ v4,24,s65,0
S_ s65,s60,10240
S_ m0,s50,1024
s_lshl_b32 s54, s5, 12
v_lshl_add_u32 v24, v16, 9, s54
L_ v5,24,s65,0
S_ s65,s60,20480
S_ m0,s50,2048
v_mov_b32 v12, 0x7fff0000
v_cndmask_b32 v12, v12, v24, vcc
L_ v4,24,s65,0
S_ s65,s60,30720
S_ m0,s50,3072
s_lshl_b32 s54, s6, 2
v_add_u32 v13, s54, v12
L_ v5,24,s65,0
S_ s65,s60,40960
S_ m0,s50,4096
v_lshrrev_b32 v19, 2, v10
v_mul_u32_u24 v25, 384, v17
L_ v4,24,s65,0
s_load_dword s67, s[12:13], s85
s_load_dword s67, s[14:15], s85
S_ s64,s85,393216
s_load_dword s67, s[12:13], s64
s_load_dword s67, s[14:15], s64
s_lshl_b32 s64, s6, 2
S_ s64,s64,s4
s_mul_i32 s64, s64, 245760
s_load_dword s67, s[16:17], s64
s_cmp_eq_u32 s4, 0
s_cbranch_scc0 .Lhs0a
buffer_load_dword v20, v19, s[92:95], s96 offen offset:128 sc1
buffer_load_dword v21, v19, s[92:95], s96 offen offset:384 sc1
buffer_load_dword a252, v19, s[92:95], s96 offen offset:256 sc1
buffer_load_dword a253, v13, s[92:95], 0 offen offset:256 sc1
buffer_load_dword v22, off, s[92:95], s96 offset:0 sc1
.Lhs0a:
S_ s65,s59,0
S_ m0,s52,16256
v_xor_b32 v24, v18, v23
v_lshl_add_u32 v7, v24, 4, v25
v_lshlrev_b32 v24, 1, v23
v_xor_b32 v24, v18, v24
v_lshl_add_u32 v9, v24, 4, v25
v_and_b32 v26, 15, v16
v_lshrrev_b32 v27, 2, v26
v_and_b32 v28, 3, v16
v_lshrrev_b32 v24, 1, v26
v_xor_b32 v24, v23, v24
L_ v6,28,s65,128
S_ s65,s59,98304
S_ m0,s52,17280
v_lshlrev_b32 v24, 4, v24
v_lshl_add_u32 v24, v18, 7, v24
v_lshrrev_b32 v25, 3, v26
v_lshl_add_u32 v2, v25, 10, v24
v_add_u32 v2, s56, v2
v_mul_u32_u24 v1, 3072, v25
v_add3_u32 v1, v1, v24, s55
v_lshlrev_b32 v24, 2, v25
v_xor_b32 v24, v28, v24
v_lshlrev_b32 v24, 4, v24
L_ v6,28,s65,128
S_ s65,s59,196608
S_ m0,s52,18304
v_lshl_add_u32 v24, v27, 7, v24
v_lshl_add_u32 %0, v23, 10, v24
v_add_u32 %0, s55, %0
v_and_b32 v29, 1, v23
v_lshl_add_u32 v24, v29, 1, v25
v_lshlrev_b32 v24, 1, v24
v_lshrrev_b32 v30, 1, v28
v_xor_b32 v24, v30, v24
v_lshlrev_b32 v24, 4, v24
v_lshl_add_u32 v29, v29, 2, v27
L_ v6,28,s65,128
S_ s65,s59,294912
S_ m0,s52,19328
v_lshl_add_u32 v24, v29, 7, v24
v_and_b32 v29, 1, v28
v_lshl_add_u32 v24, v29, 3, v24
v_lshrrev_b32 v29, 1, v23
v_mul_u32_u24 v3, 3072, v29
v_add3_u32 v3, v3, v24, s55
X_ a242,%0
X_ a244,v1
X_ a243,v2
X_ a245,v3
L_ v6,28,s65,128
S_ s65,s59,0
S_ m0,s52,24320
X_ a240,v4
X_ a248,v10
X_ a241,v12
X_ a251,v13
v_xor_b32 v1, 64, %0
v_xor_b32 v3, 64, v2
v_xor_b32 v8, 64, v7
S_ s32,s12,s85
s_addc_u32 s33, s13, 0
s_mov_b32 s34, 589824
L_ v6,28,s65,256
S_ s65,s59,98304
S_ m0,s52,25344
s_mov_b32 s35, 0x00020000
S_ s36,s14,s85
s_addc_u32 s37, s15, 0
s_mov_b32 s38, 589824
s_mov_b32 s39, 0x00020000
s_lshl_b32 s87, s87, 1
S_ s20,s20,s87
s_addc_u32 s21, s21, 0
s_mov_b32 s57, 20000
s_mov_b32 s48, 1037384597
L_ v6,28,s65,256
S_ s65,s59,196608
S_ m0,s52,26368
s_mov_b32 s49, 1037384597
s_mul_i32 s53, s5, 1638400
S_ s53,s53,134217728
S_ s22,s18,s53
s_addc_u32 s23, s19, 0
s_lshl_b32 s53, s6, 3
s_lshr_b32 s54, s7, 5
S_ s53,s53,s54
s_lshl_b32 s53, s53, 2
S_ s53,s53,s4
L_ v6,28,s65,256
S_ s65,s59,294912
S_ m0,s52,27392
s_mul_i32 s87, s53, 30720
s_lshl_b32 s53, s6, 2
S_ s53,s53,s77
s_mul_i32 s72, s53, 245760
s_and_b32 s54, s4, 1
s_mul_i32 s53, s54, 61440
S_ s72,s72,s53
s_lshl_b32 s53, s77, 6
s_xor_b32 s83, s53, 64
s_xor_b32 s53, s4, 2
L_ v6,28,s65,256
S_ s65,s60,128
S_ m0,s50,20480
s_mul_i32 s75, s53, 10240
s_cmp_eq_u32 s4, 0
s_cselect_b32 s73, 0, 2147418112
s_mul_i32 s53, s4, 5120
s_mul_i32 s78, s6, 204800
S_ s78,s78,s53
s_and_b32 s84, s6, 7
s_mul_i32 s84, s84, 20480
S_ s84,s84,s53
s_mul_i32 s53, s6, 5120
L_ v4,24,s65,0
S_ s65,s60,10368
S_ m0,s50,21504
S_ s53,s53,163840
s_lshl_b32 s54, s4, 10
S_ s74,s53,s54
S_ s53,s53,4096
s_cmp_eq_u32 s4, 0
s_cselect_b32 s82, s53, 4026531840
s_and_b32 s53, s4, 1
s_mul_i32 s53, s53, 204800
s_lshl_b32 s54, s6, 7
S_ s53,s53,s54
L_ v5,24,s65,0
S_ s65,s60,20608
S_ m0,s50,22528
s_lshl_b32 s54, s77, 6
S_ s8,s53,s54
s_and_b32 s53, s6, 1
s_mul_i32 s53, s53, 204800
s_lshr_b32 s54, s6, 1
s_lshl_b32 s54, s54, 6
S_ s53,s53,s54
S_ s53,s53,1024
s_mul_i32 s54, s4, 40960
S_ s9,s53,s54
L_ v4,24,s65,0
S_ s65,s60,30848
S_ m0,s50,23552
S_ s53,s53,163840
s_cmp_eq_u32 s4, 0
s_cselect_b32 s10, s53, 4026531840
R_ v12,a248
v_lshrrev_b32 v12, 4, v12
v_min_u32 v10, 11, v12
s_mul_i32 s56, s4, 12
v_add_u32 v10, s56, v10
v_mul_u32_u24 v11, 43691, v10
v_lshrrev_b32 v11, 18, v11
L_ v5,24,s65,0
S_ s65,s60,41088
S_ m0,s50,24576
v_lshlrev_b32 v10, 7, v10
v_mul_u32_u24 v11, 11520, v11
v_add_u32 v10, v10, v11
v_min_u32 v12, 9, v12
s_and_b32 s56, s6, 3
s_lshl_b32 s56, s56, 2
S_ s56,s56,s4
s_mul_i32 s56, s56, 10
v_add_u32 v12, s56, v12
v_mul_u32_u24 v12, 1280, v12
L_ v4,24,s65,0
S_ s65,s59,786432
S_ m0,s52,0
s_lshr_b32 s56, s7, 5
s_mul_i32 s56, s56, 98304
s_and_b32 s55, s59, 4095
S_ s56,s56,s55
s_mul_i32 s55, s77, 384
s_sub_u32 s56, s56, s55
s_mov_b32 s63, 0
L_ v6,28,s65,0
S_ s65,s59,884736
S_ m0,s52,1024
s_nop 0
L_ v6,28,s65,0
S_ s65,s59,983040
S_ m0,s52,2048
s_nop 0
L_ v6,28,s65,0
S_ s65,s59,1081344
S_ m0,s52,3072
s_nop 0
L_ v6,28,s65,0
S_ s55,s56,786432
buffer_load_dword a254, v10, s[28:31], s55 offen
S_ s55,s56,1572864
buffer_load_dword a254, v10, s[28:31], s55 offen
buffer_load_dword a254, v12, s[24:27], s63 offen offset:256
S_ s55,s56,2359296
buffer_load_dword a254, v10, s[28:31], s55 offen
buffer_load_dword a254, v12, s[24:27], s63 offen offset:384
s_waitcnt vmcnt(22) lgkmcnt(0)
s_barrier
s_memtime s[90:91]
s_memrealtime s[68:69]
Ta 20,%0,8192
Ta 22,%0,8704
Ta 24,%0,12288
Ta 26,%0,12800
Da 60,v2,0
Da 100,v3,0
Da 64,v2,2048
Da 104,v3,2048
Da 68,v2,4096
Da 108,v3,4096
Da 72,v2,6144
Da 112,v3,6144
Da 76,v2,8192
Da 116,v3,8192
Ta 28,%0,8200
Ta 30,%0,8712
Ta 32,%0,12296
Ta 34,%0,12808
s_cmp_eq_u32 s4, 0
s_cbranch_scc0 .Lhs0b
v_readfirstlane_b32 s62, v22
.Lhs0l:
s_nop 3
S_ s62,s62,1
v_cmp_eq_u32 vcc, s62, v20
s_nop 1
s_or_b32 s53, vcc_lo, vcc_hi
v_cmp_eq_u32 vcc, s62, v21
s_nop 1
s_or_b32 s54, vcc_lo, vcc_hi
s_or_b32 s53, s53, s54
s_cbranch_scc1 .Lhs0l
v_mov_b32 v22, s62
s_mov_b64 exec, 1
s_nop 1
buffer_store_dword v22, off, s[92:95], s96 sc1
s_mov_b64 exec, -1
s_nop 1
.Lhs0b:
s_cmp_eq_u32 s58, 0
s_cbranch_scc1 .Lq
.Lqy:
W_ 13
Zvaa 16,20,60
s_nop 0
Ta 36,v1,8192
Ta 38,v1,8704
W_ 14
Mvaa 16,24,100
R_ v12,a248
W_ 13
Zvaa 24,20,64
v_lshrrev_b32 v12, 4, v12
W_ 12
Mvaa 24,24,104
s_nop 0
Ta 224,v1,12288
Ta 226,v1,12800
W_ 13
Zvaa 32,20,68
v_min_u32 v10, 11, v12
W_ 12
Mvaa 32,24,108
s_mul_i32 s56, s4, 12
W_ 11
Zvaa 40,20,72
v_add_u32 v10, s56, v10
W_ 10
Mvaa 40,24,112
v_mul_u32_u24 v11, 43691, v10
W_ 9
Zvaa 48,20,76
v_lshrrev_b32 v11, 18, v11
W_ 8
Mvaa 48,24,116
W_ 4
v_lshlrev_b32 v10, 7, v10
Zvaa 52,32,116
s_nop 0
Ta 20,v1,8200
Ta 22,v1,8712
Mvaa 52,28,76
v_mul_u32_u24 v11, 11520, v11
Zvaa 44,32,112
v_add_u32 v10, v10, v11
Mvaa 44,28,72
s_nop 0
Ta 24,v1,12296
Ta 26,v1,12808
Zvaa 36,32,108
v_min_u32 v12, 9, v12
Mvaa 36,28,68
s_and_b32 s56, s6, 3
Zvaa 28,32,104
s_lshl_b32 s56, s56, 2
Mvaa 28,28,64
S_ s56,s56,s4
Zvaa 20,32,100
s_waitcnt vmcnt(18) lgkmcnt(0)
s_barrier
s_mul_i32 s56, s56, 10
Mvaa 20,28,60
S_ s65,s59,786432
v_add_u32 v12, s56, v12
Zvaa 56,36,60
S_ m0,s52,8064
Ta 28,%0,16384
Ta 30,%0,16896
Mvaa 56,224,100
L_ v6,28,s65,128
Zvaa 64,36,64
S_ s65,s59,884736
v_mul_u32_u24 v12, 1280, v12
Mvaa 64,224,104
S_ m0,s52,9088
Ta 32,%0,20480
Ta 34,%0,20992
Zvaa 72,36,68
s_lshr_b32 s56, s7, 5
Mvaa 72,224,108
L_ v6,28,s65,128
Zvaa 80,36,72
S_ s65,s59,983040
s_mul_i32 s56, s56, 98304
Mvaa 80,224,112
S_ m0,s52,10112
s_and_b32 s55, s59, 4095
Zvaa 88,36,76
S_ s56,s56,s55
Mvaa 88,224,116
s_mul_i32 s55, s77, 384
Zvaa 92,24,116
s_sub_u32 s56, s56, s55
Ta 36,%0,16392
Ta 38,%0,16904
Mvaa 92,20,76
L_ v6,28,s65,128
Zvaa 84,24,112
S_ s65,s59,1081344
s_mov_b32 s63, 0
Mvaa 84,20,72
S_ m0,s52,11136
Ta 224,%0,20488
Ta 226,%0,21000
Zvaa 76,24,108
S_ s55,s56,3145728
Mvaa 76,20,68
L_ v6,28,s65,128
Zvaa 68,24,104
S_ s65,s60,256
Mvaa 68,20,64
S_ m0,s50,0
Zvaa 60,24,100
Mvaa 60,20,60
W_ 4
Zvaa 96,28,60
s_nop 0
Ta 20,v1,16384
Ta 22,v1,16896
Mvaa 96,32,100
L_ v4,24,s65,0
Zvaa 104,28,64
S_ s65,s60,10496
Mvaa 104,32,104
S_ m0,s50,1024
Ta 24,v1,20480
Ta 26,v1,20992
Zvaa 112,28,68
Mvaa 112,32,108
L_ v5,24,s65,0
Zvaa 120,28,72
S_ s65,s60,20736
Mvaa 120,32,112
S_ m0,s50,2048
Zvaa 128,28,76
Mvaa 128,32,116
W_ 4
Zvaa 132,224,116
s_nop 0
Ta 28,v1,16392
Ta 30,v1,16904
Mvaa 132,36,76
L_ v4,24,s65,0
Zvaa 124,224,112
S_ s65,s60,30976
Mvaa 124,36,72
S_ m0,s50,3072
Ta 32,v1,20488
Ta 34,v1,21000
Zvaa 116,224,108
Mvaa 116,36,68
L_ v5,24,s65,0
Zvaa 108,224,104
S_ s65,s60,41216
Mvaa 108,36,64
S_ m0,s50,4096
Zvaa 100,224,100
s_mov_b64 exec, 0
s_bitcmp1_b32 s4, 1
s_cbranch_scc1 .Lhop_q1_b
s_bitcmp1_b32 s4, 0
s_cbranch_scc1 .Lpad_q1_1
s_branch .Lpad_q1_0
.Lhop_q1_b:
s_bitcmp1_b32 s4, 0
s_cbranch_scc1 .Lpad_q1_3
s_branch .Lpad_q1_2
.Lhop_q1_r:
s_mov_b64 exec, -1
s_nop 1
.Lqy_early:
s_waitcnt vmcnt(17) lgkmcnt(0)
s_barrier
Mvaa 100,36,60
Da 140,v2,20480
Zvaa 136,20,60
s_nop 0
Ta 36,%0,24576
Ta 38,%0,25088
Mvaa 136,24,100
L_ v4,24,s65,0
Zvaa 144,20,64
S_ s65,s59,786432
Da 180,v3,20480
Mvaa 144,24,104
S_ m0,s52,16128
Ta 224,%0,28672
Ta 226,%0,29184
Zvaa 152,20,68
Mvaa 152,24,108
Da 144,v2,22528
Zvaa 160,20,72
L_ v6,28,s65,256
Mvaa 160,24,112
S_ s65,s59,884736
Zvaa 168,20,76
S_ m0,s52,17152
Da 184,v3,22528
Mvaa 168,24,116
Zvaa 172,32,116
s_nop 0
Ta 20,%0,24584
Ta 22,%0,25096
Mvaa 172,28,76
Da 148,v2,24576
Zvaa 164,32,112
L_ v6,28,s65,256
Mvaa 164,28,72
S_ s65,s59,983040
Ta 24,%0,28680
Ta 26,%0,29192
Zvaa 156,32,108
S_ m0,s52,18176
Da 188,v3,24576
Mvaa 156,28,68
Zvaa 148,32,104
L_ v6,28,s65,256
Mvaa 148,28,64
S_ s65,s59,1081344
Da 152,v2,26624
Zvaa 140,32,100
S_ m0,s52,19200
Mvaa 140,28,60
W_ 9
Zvaa 176,36,60
s_nop 0
Ta 28,v1,24576
Ta 30,v1,25088
Mvaa 176,224,100
Da 192,v3,26624
Zvaa 184,36,64
L_ v6,28,s65,256
Mvaa 184,224,104
s_nop 0
Ta 32,v1,28672
Ta 34,v1,29184
Zvaa 192,36,68
Da 156,v2,28672
Mvaa 192,224,108
Zvaa 200,36,72
Da 196,v3,28672
Mvaa 200,224,112
buffer_load_dword a254, v10, s[28:31], s55 offen
Zvaa 208,36,76
Mvaa 208,224,116
W_ 9
Zvaa 212,24,116
s_nop 0
Ta 36,v1,24584
Ta 38,v1,25096
Mvaa 212,20,76
buffer_load_dword a254, v12, s[24:27], s63 offen offset:512
Zvaa 204,24,112
.Lpad_q1_0:
s_cbranch_execz .Lhop_q1_r
Mvaa 204,20,72
s_nop 0
Ta 224,v1,28680
Ta 226,v1,29192
Zvaa 196,24,108
Mvaa 196,20,68
Zvaa 188,24,104
Mvaa 188,20,64
Zvaa 180,24,100
s_waitcnt vmcnt(20) lgkmcnt(0)
s_barrier
Mvaa 180,20,60
S_ s65,s59,1572864
Zvaa 216,28,60
S_ m0,s52,24576
Ta 20,%0,0
Ta 22,%0,512
Mvaa 216,32,100
L_ v6,28,s65,0
Zvaa 224,28,64
S_ s65,s59,1671168
Mvaa 224,32,104
S_ m0,s52,25600
Ta 24,%0,4096
Ta 26,%0,4608
Zvaa 232,28,68
Mvaa 232,32,108
L_ v6,28,s65,0
Zvaa 240,28,72
S_ s65,s59,1769472
Mvaa 240,32,112
S_ m0,s52,26624
Zvaa 248,28,76
Mvaa 248,32,116
Zvaa 252,224,116
s_nop 0
Ta 28,%0,8
Ta 30,%0,520
Mvaa 252,36,76
L_ v6,28,s65,0
Zvaa 244,224,112
S_ s65,s59,1867776
Mvaa 244,36,72
S_ m0,s52,27648
Ta 32,%0,4104
Ta 34,%0,4616
Zvaa 236,224,108
Mvaa 236,36,68
L_ v6,28,s65,0
Zvaa 228,224,104
S_ s65,s60,384
Mvaa 228,36,64
S_ m0,s50,20480
Zvaa 220,224,100
Mvaa 220,36,60
W_ 4
Mvaa 16,20,140
s_nop 0
Ta 36,v1,0
Ta 38,v1,512
Mvaa 16,24,180
L_ v4,24,s65,0
Mvaa 24,20,144
S_ s65,s60,10624
Mvaa 24,24,184
S_ m0,s50,21504
Ta 224,v1,4096
Ta 226,v1,4608
Mvaa 32,20,148
Mvaa 32,24,188
L_ v5,24,s65,0
Mvaa 40,20,152
S_ s65,s60,20864
Mvaa 40,24,192
S_ m0,s50,22528
Mvaa 48,20,156
Mvaa 48,24,196
W_ 4
Mvaa 52,32,196
s_nop 0
Ta 20,v1,8
Ta 22,v1,520
Mvaa 52,28,156
L_ v4,24,s65,0
Mvaa 44,32,192
S_ s65,s60,31104
Mvaa 44,28,152
S_ m0,s50,23552
Ta 24,v1,4104
Ta 26,v1,4616
Mvaa 36,32,188
Mvaa 36,28,148
L_ v5,24,s65,0
Mvaa 28,32,184
S_ s65,s60,41344
Mvaa 28,28,144
S_ m0,s50,24576
Mvaa 20,32,180
s_waitcnt vmcnt(19) lgkmcnt(0)
s_barrier
Mvaa 20,28,140
L_ v4,24,s65,0
Mvaa 56,36,140
S_ s65,s59,1572864
Ta 28,%0,8192
Ta 30,%0,8704
Mvaa 56,224,180
S_ m0,s52,-128
Mvaa 64,36,144
Mvaa 64,224,184
s_nop 0
Ta 32,%0,12288
Ta 34,%0,12800
Mvaa 72,36,148
L_ v6,28,s65,128
Mvaa 72,224,188
S_ s65,s59,1671168
Mvaa 80,36,152
S_ m0,s52,896
Mvaa 80,224,192
Mvaa 88,36,156
L_ v6,28,s65,128
Mvaa 88,224,196
S_ s65,s59,1769472
Mvaa 92,24,196
S_ m0,s52,1920
Ta 36,%0,8200
Ta 38,%0,8712
Mvaa 92,20,156
S_ s55,s56,3932160
Mvaa 84,24,192
L_ v6,28,s65,128
Mvaa 84,20,152
S_ s65,s59,1867776
Ta 224,%0,12296
Ta 226,%0,12808
Mvaa 76,24,188
S_ m0,s52,2944
Mvaa 76,20,148
Mvaa 68,24,184
L_ v6,28,s65,128
Mvaa 68,20,144
Mvaa 60,24,180
Mvaa 60,20,140
W_ 4
Mvaa 96,28,140
s_nop 0
Ta 20,v1,8192
Ta 22,v1,8704
Mvaa 96,32,180
buffer_load_dword a254, v10, s[28:31], s55 offen
Mvaa 104,28,144
Mvaa 104,32,184
s_nop 0
Ta 24,v1,12288
Ta 26,v1,12800
Mvaa 112,28,148
Mvaa 112,32,188
buffer_load_dword a254, v12, s[24:27], s63 offen offset:640
Mvaa 120,28,152
Mvaa 120,32,192
Mvaa 128,28,156
Mvaa 128,32,196
W_ 4
Mvaa 132,224,196
s_nop 0
Ta 28,v1,8200
Ta 30,v1,8712
Mvaa 132,36,156
Mvaa 124,224,192
Mvaa 124,36,152
s_nop 0
Ta 32,v1,12296
Ta 34,v1,12808
Mvaa 116,224,188
Mvaa 116,36,148
Mvaa 108,224,184
Mvaa 108,36,144
Mvaa 100,224,180
s_waitcnt vmcnt(17) lgkmcnt(0)
s_barrier
Mvaa 100,36,140
S_ s65,s59,1572864
Da 60,v2,0
Mvaa 136,20,140
S_ m0,s52,7936
Ta 36,%0,16384
Ta 38,%0,16896
Mvaa 136,24,180
L_ v6,28,s65,256
Mvaa 144,20,144
S_ s65,s59,1671168
Da 100,v3,0
Mvaa 144,24,184
S_ m0,s52,8960
Ta 224,%0,20480
Ta 226,%0,20992
Mvaa 152,20,148
Mvaa 152,24,188
Da 64,v2,2048
Mvaa 160,20,152
L_ v6,28,s65,256
Mvaa 160,24,192
S_ s65,s59,1769472
Mvaa 168,20,156
S_ m0,s52,9984
Da 104,v3,2048
Mvaa 168,24,196
Mvaa 172,32,196
s_nop 0
Ta 20,%0,16392
Ta 22,%0,16904
Mvaa 172,28,156
Da 68,v2,4096
Mvaa 164,32,192
L_ v6,28,s65,256
Mvaa 164,28,152
S_ s65,s59,1867776
Ta 24,%0,20488
Ta 26,%0,21000
Mvaa 156,32,188
S_ m0,s52,11008
Da 108,v3,4096
Mvaa 156,28,148
Mvaa 148,32,184
L_ v6,28,s65,256
Mvaa 148,28,144
Da 72,v2,6144
Mvaa 140,32,180
Mvaa 140,28,140
W_ 9
Mvaa 176,36,140
s_nop 0
Ta 28,v1,16384
Ta 30,v1,16896
Mvaa 176,224,180
Da 112,v3,6144
Mvaa 184,36,144
Mvaa 184,224,184
s_nop 0
Ta 32,v1,20480
Ta 34,v1,20992
Mvaa 192,36,148
Da 76,v2,8192
Mvaa 192,224,188
Mvaa 200,36,152
Da 116,v3,8192
Mvaa 200,224,192
Mvaa 208,36,156
Mvaa 208,224,196
W_ 9
Mvaa 212,24,196
s_nop 0
Ta 36,v1,16392
Ta 38,v1,16904
Mvaa 212,20,156
Mvaa 204,24,192
Mvaa 204,20,152
s_nop 0
Ta 224,v1,20488
Ta 226,v1,21000
Mvaa 196,24,188
Mvaa 196,20,148
Mvaa 188,24,184
Mvaa 188,20,144
Mvaa 180,24,180
s_waitcnt vmcnt(15) lgkmcnt(0)
s_barrier
Mvaa 180,20,140
S_ s65,s59,2359296
Mvaa 216,28,140
S_ m0,s52,16384
Ta 20,%0,24576
Ta 22,%0,25088
Mvaa 216,32,180
L_ v6,28,s65,0
Mvaa 224,28,144
S_ s65,s59,2457600
Mvaa 224,32,184
S_ m0,s52,17408
Ta 24,%0,28672
Ta 26,%0,29184
Mvaa 232,28,148
Mvaa 232,32,188
L_ v6,28,s65,0
Mvaa 240,28,152
S_ s65,s59,2555904
Mvaa 240,32,192
S_ m0,s52,18432
Mvaa 248,28,156
Mvaa 248,32,196
Mvaa 252,224,196
s_nop 0
Ta 28,%0,24584
Ta 30,%0,25096
Mvaa 252,36,156
L_ v6,28,s65,0
Mvaa 244,224,192
S_ s65,s59,2654208
Mvaa 244,36,152
S_ m0,s52,19456
Ta 32,%0,28680
Ta 34,%0,29192
Mvaa 236,224,188
Mvaa 236,36,148
L_ v6,28,s65,0
Mvaa 228,224,184
S_ s65,s60,512
Mvaa 228,36,144
S_ m0,s50,0
Mvaa 220,224,180
Mvaa 220,36,140
W_ 4
Mvaa 16,20,60
s_nop 0
Ta 36,v1,24576
Ta 38,v1,25088
Mvaa 16,24,100
L_ v4,24,s65,0
Mvaa 24,20,64
S_ s65,s60,10752
Mvaa 24,24,104
S_ m0,s50,1024
Ta 224,v1,28672
Ta 226,v1,29184
Mvaa 32,20,68
Mvaa 32,24,108
L_ v5,24,s65,0
Mvaa 40,20,72
S_ s65,s60,20992
Mvaa 40,24,112
S_ m0,s50,2048
Mvaa 48,20,76
Mvaa 48,24,116
W_ 4
Mvaa 52,32,116
s_nop 0
Ta 20,v1,24584
Ta 22,v1,25096
Mvaa 52,28,76
L_ v4,24,s65,0
Mvaa 44,32,112
S_ s65,s60,31232
Mvaa 44,28,72
S_ m0,s50,3072
Ta 24,v1,28680
Ta 26,v1,29192
Mvaa 36,32,108
Mvaa 36,28,68
L_ v5,24,s65,0
Mvaa 28,32,104
S_ s65,s60,41472
Mvaa 28,28,64
S_ m0,s50,4096
Mvaa 20,32,100
s_waitcnt vmcnt(14) lgkmcnt(0)
s_barrier
Mvaa 20,28,60
L_ v4,24,s65,0
Mvaa 56,36,60
S_ s65,s59,2359296
Ta 28,%0,0
Ta 30,%0,512
Mvaa 56,224,100
S_ m0,s52,24448
Mvaa 64,36,64
Mvaa 64,224,104
s_nop 0
Ta 32,%0,4096
Ta 34,%0,4608
Mvaa 72,36,68
L_ v6,28,s65,128
Mvaa 72,224,108
S_ s65,s59,2457600
Mvaa 80,36,72
S_ m0,s52,25472
Mvaa 80,224,112
Mvaa 88,36,76
L_ v6,28,s65,128
Mvaa 88,224,116
S_ s65,s59,2555904
Mvaa 92,24,116
S_ m0,s52,26496
Ta 36,%0,8
Ta 38,%0,520
Mvaa 92,20,76
S_ s55,s56,4718592
Mvaa 84,24,112
L_ v6,28,s65,128
Mvaa 84,20,72
S_ s65,s59,2654208
Ta 224,%0,4104
Ta 226,%0,4616
Mvaa 76,24,108
S_ m0,s52,27520
Mvaa 76,20,68
Mvaa 68,24,104
L_ v6,28,s65,128
Mvaa 68,20,64
Mvaa 60,24,100
Mvaa 60,20,60
W_ 4
Mvaa 96,28,60
s_nop 0
Ta 20,v1,0
Ta 22,v1,512
Mvaa 96,32,100
buffer_load_dword a254, v10, s[28:31], s55 offen
Mvaa 104,28,64
Mvaa 104,32,104
s_nop 0
Ta 24,v1,4096
Ta 26,v1,4608
Mvaa 112,28,68
Mvaa 112,32,108
buffer_load_dword a254, v12, s[24:27], s63 offen offset:768
Mvaa 120,28,72
Mvaa 120,32,112
Mvaa 128,28,76
Mvaa 128,32,116
W_ 4
Mvaa 132,224,116
s_nop 0
Ta 28,v1,8
Ta 30,v1,520
Mvaa 132,36,76
Mvaa 124,224,112
Mvaa 124,36,72
s_nop 0
Ta 32,v1,4104
Ta 34,v1,4616
Mvaa 116,224,108
Mvaa 116,36,68
Mvaa 108,224,104
Mvaa 108,36,64
Mvaa 100,224,100
s_waitcnt vmcnt(15) lgkmcnt(0)
s_barrier
Mvaa 100,36,60
S_ s65,s59,2359296
Da 140,v2,20480
Mvaa 136,20,60
S_ m0,s52,-256
Ta 36,%0,8192
Ta 38,%0,8704
Mvaa 136,24,100
L_ v6,28,s65,256
Mvaa 144,20,64
S_ s65,s59,2457600
Da 180,v3,20480
Mvaa 144,24,104
S_ m0,s52,768
Ta 224,%0,12288
Ta 226,%0,12800
Mvaa 152,20,68
Mvaa 152,24,108
Da 144,v2,22528
Mvaa 160,20,72
L_ v6,28,s65,256
Mvaa 160,24,112
S_ s65,s59,2555904
Mvaa 168,20,76
S_ m0,s52,1792
Da 184,v3,22528
Mvaa 168,24,116
Mvaa 172,32,116
s_nop 0
Ta 20,%0,8200
Ta 22,%0,8712
Mvaa 172,28,76
Da 148,v2,24576
Mvaa 164,32,112
L_ v6,28,s65,256
Mvaa 164,28,72
S_ s65,s59,2654208
Ta 24,%0,12296
Ta 26,%0,12808
Mvaa 156,32,108
S_ m0,s52,2816
Da 188,v3,24576
Mvaa 156,28,68
Mvaa 148,32,104
L_ v6,28,s65,256
Mvaa 148,28,64
Da 152,v2,26624
Mvaa 140,32,100
Mvaa 140,28,60
W_ 9
Mvaa 176,36,60
s_nop 0
Ta 28,v1,8192
Ta 30,v1,8704
Mvaa 176,224,100
Da 192,v3,26624
Mvaa 184,36,64
Mvaa 184,224,104
s_nop 0
Ta 32,v1,12288
Ta 34,v1,12800
Mvaa 192,36,68
Da 156,v2,28672
Mvaa 192,224,108
Mvaa 200,36,72
Da 196,v3,28672
Mvaa 200,224,112
Mvaa 208,36,76
Mvaa 208,224,116
W_ 9
Mvaa 212,24,116
s_nop 0
Ta 36,v1,8200
Ta 38,v1,8712
Mvaa 212,20,76
Mvaa 204,24,112
Mvaa 204,20,72
s_nop 0
Ta 224,v1,12296
Ta 226,v1,12808
Mvaa 196,24,108
Mvaa 196,20,68
Mvaa 188,24,104
Mvaa 188,20,64
Mvaa 180,24,100
s_waitcnt vmcnt(15) lgkmcnt(0)
s_barrier
Mvaa 180,20,60
S_ s65,s59,3145728
Mvaa 216,28,60
S_ m0,s52,8192
Ta 20,%0,16384
Ta 22,%0,16896
Mvaa 216,32,100
L_ v6,28,s65,0
Mvaa 224,28,64
S_ s65,s59,3244032
Mvaa 224,32,104
S_ m0,s52,9216
Ta 24,%0,20480
Ta 26,%0,20992
Mvaa 232,28,68
.Lpad_q1_1:
s_cbranch_execz .Lhop_q1_r
Mvaa 232,32,108
L_ v6,28,s65,0
Mvaa 240,28,72
S_ s65,s59,3342336
Mvaa 240,32,112
S_ m0,s52,10240
Mvaa 248,28,76
Mvaa 248,32,116
Mvaa 252,224,116
s_nop 0
Ta 28,%0,16392
Ta 30,%0,16904
Mvaa 252,36,76
L_ v6,28,s65,0
Mvaa 244,224,112
S_ s65,s59,3440640
Mvaa 244,36,72
S_ m0,s52,11264
Ta 32,%0,20488
Ta 34,%0,21000
Mvaa 236,224,108
Mvaa 236,36,68
L_ v6,28,s65,0
Mvaa 228,224,104
S_ s65,s60,640
Mvaa 228,36,64
S_ m0,s50,20480
Mvaa 220,224,100
Mvaa 220,36,60
W_ 4
Mvaa 16,20,140
s_nop 0
Ta 36,v1,16384
Ta 38,v1,16896
Mvaa 16,24,180
L_ v4,24,s65,0
Mvaa 24,20,144
S_ s65,s60,10880
Mvaa 24,24,184
S_ m0,s50,21504
Ta 224,v1,20480
Ta 226,v1,20992
Mvaa 32,20,148
Mvaa 32,24,188
L_ v5,24,s65,0
Mvaa 40,20,152
S_ s65,s60,21120
Mvaa 40,24,192
S_ m0,s50,22528
Mvaa 48,20,156
Mvaa 48,24,196
W_ 4
Mvaa 52,32,196
s_nop 0
Ta 20,v1,16392
Ta 22,v1,16904
Mvaa 52,28,156
L_ v4,24,s65,0
Mvaa 44,32,192
S_ s65,s60,31360
Mvaa 44,28,152
S_ m0,s50,23552
Ta 24,v1,20488
Ta 26,v1,21000
Mvaa 36,32,188
Mvaa 36,28,148
L_ v5,24,s65,0
Mvaa 28,32,184
S_ s65,s60,41600
Mvaa 28,28,144
S_ m0,s50,24576
Mvaa 20,32,180
s_waitcnt vmcnt(14) lgkmcnt(0)
s_barrier
Mvaa 20,28,140
L_ v4,24,s65,0
Mvaa 56,36,140
S_ s65,s59,3145728
Ta 28,%0,24576
Ta 30,%0,25088
Mvaa 56,224,180
S_ m0,s52,16256
Mvaa 64,36,144
Mvaa 64,224,184
s_nop 0
Ta 32,%0,28672
Ta 34,%0,29184
Mvaa 72,36,148
L_ v6,28,s65,128
Mvaa 72,224,188
S_ s65,s59,3244032
Mvaa 80,36,152
S_ m0,s52,17280
Mvaa 80,224,192
Mvaa 88,36,156
L_ v6,28,s65,128
Mvaa 88,224,196
S_ s65,s59,3342336
Mvaa 92,24,196
S_ m0,s52,18304
Ta 36,%0,24584
Ta 38,%0,25096
Mvaa 92,20,156
S_ s55,s56,5505024
Mvaa 84,24,192
L_ v6,28,s65,128
Mvaa 84,20,152
S_ s65,s59,3440640
Ta 224,%0,28680
Ta 226,%0,29192
Mvaa 76,24,188
S_ m0,s52,19328
Mvaa 76,20,148
Mvaa 68,24,184
L_ v6,28,s65,128
Mvaa 68,20,144
Mvaa 60,24,180
Mvaa 60,20,140
W_ 4
Mvaa 96,28,140
s_nop 0
Ta 20,v1,24576
Ta 22,v1,25088
Mvaa 96,32,180
buffer_load_dword a254, v10, s[28:31], s55 offen
Mvaa 104,28,144
Mvaa 104,32,184
s_nop 0
Ta 24,v1,28672
Ta 26,v1,29184
Mvaa 112,28,148
Mvaa 112,32,188
buffer_load_dword a254, v12, s[24:27], s63 offen offset:896
Mvaa 120,28,152
Mvaa 120,32,192
Mvaa 128,28,156
Mvaa 128,32,196
W_ 4
Mvaa 132,224,196
s_nop 0
Ta 28,v1,24584
Ta 30,v1,25096
Mvaa 132,36,156
Mvaa 124,224,192
Mvaa 124,36,152
s_nop 0
Ta 32,v1,28680
Ta 34,v1,29192
Mvaa 116,224,188
Mvaa 116,36,148
Mvaa 108,224,184
Mvaa 108,36,144
Mvaa 100,224,180
s_waitcnt vmcnt(15) lgkmcnt(0)
s_barrier
Mvaa 100,36,140
S_ s65,s59,3145728
Da 60,v2,0
Mvaa 136,20,140
S_ m0,s52,24320
Ta 36,%0,0
Ta 38,%0,512
Mvaa 136,24,180
L_ v6,28,s65,256
Mvaa 144,20,144
S_ s65,s59,3244032
Da 100,v3,0
Mvaa 144,24,184
S_ m0,s52,25344
Ta 224,%0,4096
Ta 226,%0,4608
Mvaa 152,20,148
Mvaa 152,24,188
Da 64,v2,2048
Mvaa 160,20,152
L_ v6,28,s65,256
Mvaa 160,24,192
S_ s65,s59,3342336
Mvaa 168,20,156
S_ m0,s52,26368
Da 104,v3,2048
Mvaa 168,24,196
Mvaa 172,32,196
s_nop 0
Ta 20,%0,8
Ta 22,%0,520
Mvaa 172,28,156
Da 68,v2,4096
Mvaa 164,32,192
L_ v6,28,s65,256
Mvaa 164,28,152
S_ s65,s59,3440640
Ta 24,%0,4104
Ta 26,%0,4616
Mvaa 156,32,188
S_ m0,s52,27392
Da 108,v3,4096
Mvaa 156,28,148
Mvaa 148,32,184
L_ v6,28,s65,256
Mvaa 148,28,144
Da 72,v2,6144
Mvaa 140,32,180
Mvaa 140,28,140
W_ 9
Mvaa 176,36,140
s_nop 0
Ta 28,v1,0
Ta 30,v1,512
Mvaa 176,224,180
Da 112,v3,6144
Mvaa 184,36,144
Mvaa 184,224,184
s_nop 0
Ta 32,v1,4096
Ta 34,v1,4608
Mvaa 192,36,148
Da 76,v2,8192
Mvaa 192,224,188
Mvaa 200,36,152
Da 116,v3,8192
Mvaa 200,224,192
Mvaa 208,36,156
Mvaa 208,224,196
W_ 9
Mvaa 212,24,196
s_nop 0
Ta 36,v1,8
Ta 38,v1,520
Mvaa 212,20,156
Mvaa 204,24,192
Mvaa 204,20,152
s_nop 0
Ta 224,v1,4104
Ta 226,v1,4616
Mvaa 196,24,188
Mvaa 196,20,148
Mvaa 188,24,184
Mvaa 188,20,144
Mvaa 180,24,180
s_waitcnt vmcnt(15) lgkmcnt(0)
s_barrier
Mvaa 180,20,140
S_ s65,s59,3932160
Mvaa 216,28,140
S_ m0,s52,0
Ta 20,%0,8192
Ta 22,%0,8704
Mvaa 216,32,180
L_ v6,28,s65,0
Mvaa 224,28,144
S_ s65,s59,4030464
Mvaa 224,32,184
S_ m0,s52,1024
Ta 24,%0,12288
Ta 26,%0,12800
Mvaa 232,28,148
Mvaa 232,32,188
L_ v6,28,s65,0
Mvaa 240,28,152
S_ s65,s59,4128768
Mvaa 240,32,192
S_ m0,s52,2048
Mvaa 248,28,156
Mvaa 248,32,196
Mvaa 252,224,196
s_nop 0
Ta 28,%0,8200
Ta 30,%0,8712
Mvaa 252,36,156
L_ v6,28,s65,0
Mvaa 244,224,192
S_ s65,s59,4227072
Mvaa 244,36,152
S_ m0,s52,3072
Ta 32,%0,12296
Ta 34,%0,12808
Mvaa 236,224,188
Mvaa 236,36,148
L_ v6,28,s65,0
Mvaa 228,224,184
S_ s65,s60,768
Mvaa 228,36,144
S_ m0,s50,0
Mvaa 220,224,180
Mvaa 220,36,140
W_ 4
Mvaa 16,20,60
s_nop 0
Ta 36,v1,8192
Ta 38,v1,8704
Mvaa 16,24,100
L_ v4,24,s65,0
Mvaa 24,20,64
S_ s65,s60,11008
R_ v13,a248
Mvaa 24,24,104
S_ m0,s50,1024
Ta 224,v1,12288
Ta 226,v1,12800
Mvaa 32,20,68
v_min_u32 v13, 240, v13
Mvaa 32,24,108
L_ v5,24,s65,0
Mvaa 40,20,72
S_ s65,s60,21248
v_lshlrev_b32 v13, 3, v13
Mvaa 40,24,112
S_ m0,s50,2048
Mvaa 48,20,76
Mvaa 48,24,116
W_ 4
Mvaa 52,32,116
s_nop 0
Ta 20,v1,8200
Ta 22,v1,8712
Mvaa 52,28,76
L_ v4,24,s65,0
Mvaa 44,32,112
S_ s65,s60,31488
Mvaa 44,28,72
S_ m0,s50,3072
Ta 24,v1,12296
Ta 26,v1,12808
Mvaa 36,32,108
Mvaa 36,28,68
L_ v5,24,s65,0
Mvaa 28,32,104
S_ s65,s60,41728
Mvaa 28,28,64
S_ m0,s50,4096
Mvaa 20,32,100
s_waitcnt vmcnt(14) lgkmcnt(0)
s_barrier
Mvaa 20,28,60
L_ v4,24,s65,0
Mvaa 56,36,60
S_ s65,s59,3932160
Ta 28,%0,16384
Ta 30,%0,16896
Mvaa 56,224,100
S_ m0,s52,8064
Mvaa 64,36,64
Mvaa 64,224,104
s_nop 0
Ta 32,%0,20480
Ta 34,%0,20992
Mvaa 72,36,68
L_ v6,28,s65,128
Mvaa 72,224,108
S_ s65,s59,4030464
Mvaa 80,36,72
S_ m0,s52,9088
Mvaa 80,224,112
Mvaa 88,36,76
L_ v6,28,s65,128
Mvaa 88,224,116
S_ s65,s59,4128768
Mvaa 92,24,116
S_ m0,s52,10112
Ta 36,%0,16392
Ta 38,%0,16904
Mvaa 92,20,76
S_ s55,s56,6291456
Mvaa 84,24,112
L_ v6,28,s65,128
Mvaa 84,20,72
S_ s65,s59,4227072
Ta 224,%0,20488
Ta 226,%0,21000
Mvaa 76,24,108
S_ m0,s52,11136
Mvaa 76,20,68
Mvaa 68,24,104
L_ v6,28,s65,128
Mvaa 68,20,64
Mvaa 60,24,100
Mvaa 60,20,60
W_ 4
Mvaa 96,28,60
s_nop 0
Ta 20,v1,16384
Ta 22,v1,16896
Mvaa 96,32,100
buffer_load_dword a220, v13, s[44:47], s66 offen
Mvaa 104,28,64
Mvaa 104,32,104
s_nop 0
Ta 24,v1,20480
Ta 26,v1,20992
Mvaa 112,28,68
Mvaa 112,32,108
buffer_load_dword a254, v10, s[28:31], s55 offen
Mvaa 120,28,72
Mvaa 120,32,112
Mvaa 128,28,76
Mvaa 128,32,116
W_ 4
Mvaa 132,224,116
s_nop 0
Ta 28,v1,16392
Ta 30,v1,16904
Mvaa 132,36,76
buffer_load_dword a254, v12, s[24:27], s63 offen offset:1024
Mvaa 124,224,112
Mvaa 124,36,72
s_nop 0
Ta 32,v1,20488
Ta 34,v1,21000
Mvaa 116,224,108
Mvaa 116,36,68
Mvaa 108,224,104
Mvaa 108,36,64
Mvaa 100,224,100
s_waitcnt vmcnt(16) lgkmcnt(0)
s_barrier
Mvaa 100,36,60
S_ s65,s59,3932160
Da 140,v2,20480
Mvaa 136,20,60
S_ m0,s52,16128
Ta 36,%0,24576
Ta 38,%0,25088
Mvaa 136,24,100
L_ v6,28,s65,256
Mvaa 144,20,64
S_ s65,s59,4030464
Da 180,v3,20480
Mvaa 144,24,104
S_ m0,s52,17152
Ta 224,%0,28672
Ta 226,%0,29184
Mvaa 152,20,68
Mvaa 152,24,108
Da 144,v2,22528
Mvaa 160,20,72
L_ v6,28,s65,256
Mvaa 160,24,112
S_ s65,s59,4128768
Mvaa 168,20,76
S_ m0,s52,18176
Da 184,v3,22528
Mvaa 168,24,116
Mvaa 172,32,116
s_nop 0
Ta 20,%0,24584
Ta 22,%0,25096
Mvaa 172,28,76
Da 148,v2,24576
Mvaa 164,32,112
L_ v6,28,s65,256
Mvaa 164,28,72
S_ s65,s59,4227072
Ta 24,%0,28680
Ta 26,%0,29192
Mvaa 156,32,108
S_ m0,s52,19200
Da 188,v3,24576
Mvaa 156,28,68
Mvaa 148,32,104
L_ v6,28,s65,256
Mvaa 148,28,64
Da 152,v2,26624
Mvaa 140,32,100
Mvaa 140,28,60
W_ 9
Mvaa 176,36,60
s_nop 0
Ta 28,v1,24576
Ta 30,v1,25088
Mvaa 176,224,100
Da 192,v3,26624
Mvaa 184,36,64
Mvaa 184,224,104
s_nop 0
Ta 32,v1,28672
Ta 34,v1,29184
Mvaa 192,36,68
Da 156,v2,28672
Mvaa 192,224,108
Mvaa 200,36,72
Da 196,v3,28672
Mvaa 200,224,112
Mvaa 208,36,76
Mvaa 208,224,116
W_ 9
Mvaa 212,24,116
s_nop 0
Ta 36,v1,24584
Ta 38,v1,25096
Mvaa 212,20,76
Mvaa 204,24,112
Mvaa 204,20,72
s_nop 0
Ta 224,v1,28680
Ta 226,v1,29192
Mvaa 196,24,108
Mvaa 196,20,68
Mvaa 188,24,104
Mvaa 188,20,64
Mvaa 180,24,100
s_waitcnt vmcnt(16) lgkmcnt(0)
s_barrier
Mvaa 180,20,60
S_ s65,s59,4718592
Mvaa 216,28,60
S_ m0,s52,24576
Ta 20,%0,0
Ta 22,%0,512
Mvaa 216,32,100
L_ v6,28,s65,0
Mvaa 224,28,64
S_ s65,s59,4816896
Mvaa 224,32,104
S_ m0,s52,25600
Ta 24,%0,4096
Ta 26,%0,4608
Mvaa 232,28,68
Mvaa 232,32,108
L_ v6,28,s65,0
Mvaa 240,28,72
S_ s65,s59,4915200
Mvaa 240,32,112
S_ m0,s52,26624
Mvaa 248,28,76
Mvaa 248,32,116
Mvaa 252,224,116
s_nop 0
Ta 28,%0,8
Ta 30,%0,520
Mvaa 252,36,76
L_ v6,28,s65,0
Mvaa 244,224,112
S_ s65,s59,5013504
Mvaa 244,36,72
S_ m0,s52,27648
Ta 32,%0,4104
Ta 34,%0,4616
Mvaa 236,224,108
.Lpad_q1_2:
s_cbranch_execz .Lhop_q1_r
Mvaa 236,36,68
L_ v6,28,s65,0
Mvaa 228,224,104
S_ s65,s60,896
Mvaa 228,36,64
S_ m0,s50,20480
Mvaa 220,224,100
Mvaa 220,36,60
W_ 4
Mvaa 16,20,140
s_nop 0
Ta 36,v1,0
Ta 38,v1,512
Mvaa 16,24,180
L_ v4,24,s65,0
Mvaa 24,20,144
S_ s65,s60,11136
Mvaa 24,24,184
S_ m0,s50,21504
Ta 224,v1,4096
Ta 226,v1,4608
Mvaa 32,20,148
Mvaa 32,24,188
L_ v5,24,s65,0
Mvaa 40,20,152
S_ s65,s60,21376
Mvaa 40,24,192
S_ m0,s50,22528
Mvaa 48,20,156
Mvaa 48,24,196
W_ 4
Mvaa 52,32,196
s_nop 0
Ta 20,v1,8
Ta 22,v1,520
Mvaa 52,28,156
L_ v4,24,s65,0
Mvaa 44,32,192
S_ s65,s60,31616
Mvaa 44,28,152
S_ m0,s50,23552
Ta 24,v1,4104
Ta 26,v1,4616
Mvaa 36,32,188
Mvaa 36,28,148
L_ v5,24,s65,0
Mvaa 28,32,184
S_ s65,s60,41856
Mvaa 28,28,144
S_ m0,s50,24576
Mvaa 20,32,180
s_waitcnt vmcnt(15) lgkmcnt(0)
s_barrier
Mvaa 20,28,140
L_ v4,24,s65,0
Mvaa 56,36,140
S_ s65,s59,4718592
Ta 28,%0,8192
Ta 30,%0,8704
Mvaa 56,224,180
S_ m0,s52,-128
Mvaa 64,36,144
Mvaa 64,224,184
s_nop 0
Ta 32,%0,12288
Ta 34,%0,12800
Mvaa 72,36,148
L_ v6,28,s65,128
Mvaa 72,224,188
S_ s65,s59,4816896
Mvaa 80,36,152
S_ m0,s52,896
Mvaa 80,224,192
Mvaa 88,36,156
L_ v6,28,s65,128
Mvaa 88,224,196
S_ s65,s59,4915200
Mvaa 92,24,196
S_ m0,s52,1920
Ta 36,%0,8200
Ta 38,%0,8712
Mvaa 92,20,156
S_ s55,s56,7077888
Mvaa 84,24,192
L_ v6,28,s65,128
Mvaa 84,20,152
S_ s65,s59,5013504
Ta 224,%0,12296
Ta 226,%0,12808
Mvaa 76,24,188
S_ m0,s52,2944
Mvaa 76,20,148
Mvaa 68,24,184
L_ v6,28,s65,128
Mvaa 68,20,144
Mvaa 60,24,180
Mvaa 60,20,140
W_ 4
Mvaa 96,28,140
s_nop 0
Ta 20,v1,8192
Ta 22,v1,8704
Mvaa 96,32,180
buffer_load_dword a254, v10, s[28:31], s55 offen
Mvaa 104,28,144
Mvaa 104,32,184
s_nop 0
Ta 24,v1,12288
Ta 26,v1,12800
Mvaa 112,28,148
Mvaa 112,32,188
buffer_load_dword a254, v12, s[24:27], s63 offen offset:1152
Mvaa 120,28,152
Mvaa 120,32,192
Mvaa 128,28,156
Mvaa 128,32,196
W_ 4
Mvaa 132,224,196
s_nop 0
Ta 28,v1,8200
Ta 30,v1,8712
Mvaa 132,36,156
Mvaa 124,224,192
Mvaa 124,36,152
s_nop 0
Ta 32,v1,12296
Ta 34,v1,12808
Mvaa 116,224,188
Mvaa 116,36,148
Mvaa 108,224,184
Mvaa 108,36,144
Mvaa 100,224,180
s_waitcnt vmcnt(15) lgkmcnt(0)
s_barrier
Mvaa 100,36,140
S_ s65,s59,4718592
Da 60,v2,0
Mvaa 136,20,140
S_ m0,s52,7936
Ta 36,%0,16384
Ta 38,%0,16896
Mvaa 136,24,180
L_ v6,28,s65,256
Mvaa 144,20,144
S_ s65,s59,4816896
Da 100,v3,0
Mvaa 144,24,184
S_ m0,s52,8960
Ta 224,%0,20480
Ta 226,%0,20992
Mvaa 152,20,148
Mvaa 152,24,188
Da 64,v2,2048
Mvaa 160,20,152
L_ v6,28,s65,256
Mvaa 160,24,192
S_ s65,s59,4915200
Mvaa 168,20,156
S_ m0,s52,9984
Da 104,v3,2048
Mvaa 168,24,196
Mvaa 172,32,196
s_nop 0
Ta 20,%0,16392
Ta 22,%0,16904
Mvaa 172,28,156
Da 68,v2,4096
Mvaa 164,32,192
L_ v6,28,s65,256
Mvaa 164,28,152
S_ s65,s59,5013504
Ta 24,%0,20488
Ta 26,%0,21000
Mvaa 156,32,188
S_ m0,s52,11008
Da 108,v3,4096
Mvaa 156,28,148
Mvaa 148,32,184
L_ v6,28,s65,256
)A"
R"A(Mvaa 148,28,144
Da 72,v2,6144
Mvaa 140,32,180
Mvaa 140,28,140
W_ 9
Mvaa 176,36,140
s_nop 0
Ta 28,v1,16384
Ta 30,v1,16896
Mvaa 176,224,180
Da 112,v3,6144
Mvaa 184,36,144
Mvaa 184,224,184
s_nop 0
Ta 32,v1,20480
Ta 34,v1,20992
Mvaa 192,36,148
Da 76,v2,8192
Mvaa 192,224,188
Mvaa 200,36,152
Da 116,v3,8192
Mvaa 200,224,192
Mvaa 208,36,156
Mvaa 208,224,196
W_ 9
Mvaa 212,24,196
s_nop 0
Ta 36,v1,16392
Ta 38,v1,16904
Mvaa 212,20,156
Mvaa 204,24,192
Mvaa 204,20,152
s_nop 0
Ta 224,v1,20488
Ta 226,v1,21000
Mvaa 196,24,188
Mvaa 196,20,148
Mvaa 188,24,184
Mvaa 188,20,144
Mvaa 180,24,180
s_waitcnt vmcnt(15) lgkmcnt(0)
s_barrier
Mvaa 180,20,140
S_ s65,s59,5505024
Mvaa 216,28,140
S_ m0,s52,16384
Ta 20,%0,24576
Ta 22,%0,25088
Mvaa 216,32,180
L_ v6,28,s65,0
Mvaa 224,28,144
S_ s65,s59,5603328
Mvaa 224,32,184
S_ m0,s52,17408
Ta 24,%0,28672
Ta 26,%0,29184
Mvaa 232,28,148
Mvaa 232,32,188
L_ v6,28,s65,0
Mvaa 240,28,152
S_ s65,s59,5701632
Mvaa 240,32,192
S_ m0,s52,18432
Mvaa 248,28,156
Mvaa 248,32,196
Mvaa 252,224,196
s_nop 0
Ta 28,%0,24584
Ta 30,%0,25096
Mvaa 252,36,156
L_ v6,28,s65,0
Mvaa 244,224,192
S_ s65,s59,5799936
Mvaa 244,36,152
S_ m0,s52,19456
Ta 32,%0,28680
Ta 34,%0,29192
Mvaa 236,224,188
Mvaa 236,36,148
L_ v6,28,s65,0
Mvaa 228,224,184
S_ s65,s60,1024
Mvaa 228,36,144
S_ m0,s50,0
Mvaa 220,224,180
Mvaa 220,36,140
W_ 4
Mvaa 16,20,60
s_nop 0
Ta 36,v1,24576
Ta 38,v1,25088
Mvaa 16,24,100
L_ v4,24,s65,0
Mvaa 24,20,64
S_ s65,s60,11264
Mvaa 24,24,104
S_ m0,s50,1024
Ta 224,v1,28672
Ta 226,v1,29184
Mvaa 32,20,68
Mvaa 32,24,108
L_ v5,24,s65,0
Mvaa 40,20,72
S_ s65,s60,21504
Mvaa 40,24,112
S_ m0,s50,2048
Mvaa 48,20,76
Mvaa 48,24,116
W_ 4
Mvaa 52,32,116
s_nop 0
Ta 20,v1,24584
Ta 22,v1,25096
Mvaa 52,28,76
L_ v4,24,s65,0
Mvaa 44,32,112
S_ s65,s60,31744
Mvaa 44,28,72
S_ m0,s50,3072
Ta 24,v1,28680
Ta 26,v1,29192
Mvaa 36,32,108
Mvaa 36,28,68
L_ v5,24,s65,0
Mvaa 28,32,104
S_ s65,s60,41984
Mvaa 28,28,64
S_ m0,s50,4096
Mvaa 20,32,100
s_waitcnt vmcnt(14) lgkmcnt(0)
s_barrier
Mvaa 20,28,60
L_ v4,24,s65,0
Mvaa 56,36,60
S_ s65,s59,5505024
Ta 28,%0,0
Ta 30,%0,512
Mvaa 56,224,100
S_ m0,s52,24448
Mvaa 64,36,64
Mvaa 64,224,104
s_nop 0
Ta 32,%0,4096
Ta 34,%0,4608
Mvaa 72,36,68
L_ v6,28,s65,128
Mvaa 72,224,108
S_ s65,s59,5603328
Mvaa 80,36,72
S_ m0,s52,25472
Mvaa 80,224,112
Mvaa 88,36,76
L_ v6,28,s65,128
Mvaa 88,224,116
S_ s65,s59,5701632
Mvaa 92,24,116
S_ m0,s52,26496
Ta 36,%0,8
Ta 38,%0,520
Mvaa 92,20,76
Mvaa 84,24,112
L_ v6,28,s65,128
Mvaa 84,20,72
S_ s65,s59,5799936
Ta 224,%0,4104
Ta 226,%0,4616
Mvaa 76,24,108
S_ m0,s52,27520
Mvaa 76,20,68
Mvaa 68,24,104
L_ v6,28,s65,128
Mvaa 68,20,64
Mvaa 60,24,100
Mvaa 60,20,60
W_ 4
Mvaa 96,28,60
s_nop 0
Ta 20,v1,0
Ta 22,v1,512
Mvaa 96,32,100
Mvaa 104,28,64
Mvaa 104,32,104
s_nop 0
Ta 24,v1,4096
Ta 26,v1,4608
Mvaa 112,28,68
Mvaa 112,32,108
Mvaa 120,28,72
Mvaa 120,32,112
Mvaa 128,28,76
Mvaa 128,32,116
W_ 4
Mvaa 132,224,116
s_nop 0
Ta 28,v1,8
Ta 30,v1,520
Mvaa 132,36,76
Mvaa 124,224,112
Mvaa 124,36,72
s_nop 0
Ta 32,v1,4104
Ta 34,v1,4616
Mvaa 116,224,108
Mvaa 116,36,68
Mvaa 108,224,104
Mvaa 108,36,64
Mvaa 100,224,100
s_waitcnt vmcnt(13) lgkmcnt(0)
s_barrier
Mvaa 100,36,60
S_ s65,s59,5505024
Da 140,v2,20480
Mvaa 136,20,60
S_ m0,s52,-256
Ta 36,%0,8192
Ta 38,%0,8704
Mvaa 136,24,100
L_ v6,28,s65,256
Mvaa 144,20,64
S_ s65,s59,5603328
Da 180,v3,20480
Mvaa 144,24,104
S_ m0,s52,768
Ta 224,%0,12288
Ta 226,%0,12800
Mvaa 152,20,68
Mvaa 152,24,108
Da 144,v2,22528
Mvaa 160,20,72
L_ v6,28,s65,256
Mvaa 160,24,112
S_ s65,s59,5701632
Mvaa 168,20,76
S_ m0,s52,1792
Da 184,v3,22528
Mvaa 168,24,116
Mvaa 172,32,116
s_nop 0
Ta 20,%0,8200
Ta 22,%0,8712
Mvaa 172,28,76
Da 148,v2,24576
Mvaa 164,32,112
L_ v6,28,s65,256
Mvaa 164,28,72
S_ s65,s59,5799936
Ta 24,%0,12296
Ta 26,%0,12808
Mvaa 156,32,108
S_ m0,s52,2816
Da 188,v3,24576
Mvaa 156,28,68
Mvaa 148,32,104
L_ v6,28,s65,256
Mvaa 148,28,64
Da 152,v2,26624
Mvaa 140,32,100
Mvaa 140,28,60
W_ 9
Mvaa 176,36,60
s_nop 0
Ta 28,v1,8192
Ta 30,v1,8704
Mvaa 176,224,100
Da 192,v3,26624
Mvaa 184,36,64
Mvaa 184,224,104
s_nop 0
Ta 32,v1,12288
Ta 34,v1,12800
Mvaa 192,36,68
Da 156,v2,28672
Mvaa 192,224,108
Mvaa 200,36,72
Da 196,v3,28672
Mvaa 200,224,112
Mvaa 208,36,76
Mvaa 208,224,116
W_ 9
Mvaa 212,24,116
s_nop 0
Ta 36,v1,8200
Ta 38,v1,8712
Mvaa 212,20,76
Mvaa 204,24,112
Mvaa 204,20,72
s_nop 0
Ta 224,v1,12296
Ta 226,v1,12808
Mvaa 196,24,108
Mvaa 196,20,68
Mvaa 188,24,104
Mvaa 188,20,64
Mvaa 180,24,100
s_waitcnt vmcnt(13) lgkmcnt(0)
s_barrier
Mvaa 180,20,60
S_ s65,s59,6291456
Mvaa 216,28,60
S_ m0,s52,7936
Ta 20,%0,16384
Ta 22,%0,16896
Mvaa 216,32,100
L_ v6,28,s65,256
Mvaa 224,28,64
S_ s65,s59,6389760
Mvaa 224,32,104
S_ m0,s52,8960
Ta 24,%0,20480
Ta 26,%0,20992
Mvaa 232,28,68
Mvaa 232,32,108
L_ v6,28,s65,256
Mvaa 240,28,72
S_ s65,s59,6488064
Mvaa 240,32,112
S_ m0,s52,9984
Mvaa 248,28,76
Mvaa 248,32,116
Mvaa 252,224,116
s_nop 0
Ta 28,%0,16392
Ta 30,%0,16904
Mvaa 252,36,76
L_ v6,28,s65,256
Mvaa 244,224,112
S_ s65,s59,6586368
Mvaa 244,36,72
S_ m0,s52,11008
Ta 32,%0,20488
Ta 34,%0,21000
Mvaa 236,224,108
Mvaa 236,36,68
L_ v6,28,s65,256
Mvaa 228,224,104
S_ s65,s60,1152
Mvaa 228,36,64
S_ m0,s50,20480
Mvaa 220,224,100
Mvaa 220,36,60
W_ 4
Mvaa 16,20,140
s_nop 0
Ta 36,v1,16384
Ta 38,v1,16896
Mvaa 16,24,180
L_ v4,24,s65,0
Mvaa 24,20,144
S_ s65,s60,11392
Mvaa 24,24,184
S_ m0,s50,21504
Ta 224,v1,20480
Ta 226,v1,20992
Mvaa 32,20,148
.Lpad_q1_3:
s_cbranch_execz .Lhop_q1_r
Mvaa 32,24,188
L_ v5,24,s65,0
Mvaa 40,20,152
S_ s65,s60,21632
Mvaa 40,24,192
S_ m0,s50,22528
Mvaa 48,20,156
Mvaa 48,24,196
W_ 4
Mvaa 52,32,196
s_nop 0
Ta 20,v1,16392
Ta 22,v1,16904
Mvaa 52,28,156
L_ v4,24,s65,0
Mvaa 44,32,192
S_ s65,s60,31872
Mvaa 44,28,152
S_ m0,s50,23552
Ta 24,v1,20488
Ta 26,v1,21000
Mvaa 36,32,188
Mvaa 36,28,148
L_ v5,24,s65,0
Mvaa 28,32,184
S_ s65,s60,42112
Mvaa 28,28,144
S_ m0,s50,24576
Mvaa 20,32,180
s_waitcnt vmcnt(12) lgkmcnt(0)
s_barrier
Mvaa 20,28,140
L_ v4,24,s65,0
Mvaa 56,36,140
S_ s65,s59,7077888
Ta 28,%0,24576
Ta 30,%0,25088
Mvaa 56,224,180
S_ m0,s52,16128
Mvaa 64,36,144
Mvaa 64,224,184
s_nop 0
Ta 32,%0,28672
Ta 34,%0,29184
Mvaa 72,36,148
L_ v6,28,s65,256
Mvaa 72,224,188
S_ s65,s59,7176192
Mvaa 80,36,152
S_ m0,s52,17152
Mvaa 80,224,192
Mvaa 88,36,156
L_ v6,28,s65,256
Mvaa 88,224,196
S_ s65,s59,7274496
Mvaa 92,24,196
S_ m0,s52,18176
Ta 36,%0,24584
Ta 38,%0,25096
Mvaa 92,20,156
Mvaa 84,24,192
L_ v6,28,s65,256
Mvaa 84,20,152
S_ s65,s59,7372800
Ta 224,%0,28680
Ta 226,%0,29192
Mvaa 76,24,188
S_ m0,s52,19200
Mvaa 76,20,148
Mvaa 68,24,184
L_ v6,28,s65,256
Mvaa 68,20,144
Mvaa 60,24,180
Mvaa 60,20,140
W_ 4
Mvaa 96,28,140
s_nop 0
Ta 20,v1,24576
Ta 22,v1,25088
Mvaa 96,32,180
Mvaa 104,28,144
Mvaa 104,32,184
s_nop 0
Ta 24,v1,28672
Ta 26,v1,29184
Mvaa 112,28,148
Mvaa 112,32,188
Mvaa 120,28,152
Mvaa 120,32,192
Mvaa 128,28,156
Mvaa 128,32,196
W_ 4
Mvaa 132,224,196
s_nop 0
Ta 28,v1,24584
Ta 30,v1,25096
Mvaa 132,36,156
Mvaa 124,224,192
Mvaa 124,36,152
s_nop 0
Ta 32,v1,28680
Ta 34,v1,29192
Mvaa 116,224,188
Mvaa 116,36,148
Mvaa 108,224,184
Mvaa 108,36,144
Mvaa 100,224,180
s_waitcnt vmcnt(13) lgkmcnt(0)
s_barrier
Mvaa 100,36,140
S_ s65,s59,6291456
Da 60,v2,0
Mvaa 136,20,140
S_ m0,s52,24448
Ta 36,%0,0
Ta 38,%0,512
Mvaa 136,24,180
L_ v6,28,s65,128
Mvaa 144,20,144
S_ s65,s59,6389760
Da 100,v3,0
Mvaa 144,24,184
S_ m0,s52,25472
Ta 224,%0,4096
Ta 226,%0,4608
Mvaa 152,20,148
Mvaa 152,24,188
Da 64,v2,2048
Mvaa 160,20,152
L_ v6,28,s65,128
Mvaa 160,24,192
S_ s65,s59,6488064
Mvaa 168,20,156
S_ m0,s52,26496
Da 104,v3,2048
Mvaa 168,24,196
Mvaa 172,32,196
s_nop 0
Ta 20,%0,8
Ta 22,%0,520
Mvaa 172,28,156
Da 68,v2,4096
Mvaa 164,32,192
L_ v6,28,s65,128
Mvaa 164,28,152
S_ s65,s59,6586368
Ta 24,%0,4104
Ta 26,%0,4616
Mvaa 156,32,188
S_ m0,s52,27520
Da 108,v3,4096
Mvaa 156,28,148
Mvaa 148,32,184
L_ v6,28,s65,128
Mvaa 148,28,144
Da 72,v2,6144
Mvaa 140,32,180
Mvaa 140,28,140
W_ 9
Mvaa 176,36,140
s_nop 0
Ta 28,v1,0
Ta 30,v1,512
Mvaa 176,224,180
Da 112,v3,6144
Mvaa 184,36,144
Mvaa 184,224,184
s_nop 0
Ta 32,v1,4096
Ta 34,v1,4608
Mvaa 192,36,148
Da 76,v2,8192
Mvaa 192,224,188
Mvaa 200,36,152
Da 116,v3,8192
Mvaa 200,224,192
Mvaa 208,36,156
Mvaa 208,224,196
W_ 9
Mvaa 212,24,196
s_nop 0
Ta 36,v1,8
Ta 38,v1,520
Mvaa 212,20,156
Mvaa 204,24,192
Mvaa 204,20,152
s_nop 0
Ta 224,v1,4104
Ta 226,v1,4616
Mvaa 196,24,188
Mvaa 196,20,148
Mvaa 188,24,184
Mvaa 188,20,144
Mvaa 180,24,180
s_waitcnt vmcnt(8) lgkmcnt(0)
s_barrier
Mvaa 180,20,140
S_ s65,s59,7077888
Mvaa 216,28,140
S_ m0,s52,-128
Ta 20,v1,8200
Ta 22,v1,8712
Mvaa 216,32,180
L_ v6,28,s65,128
Mvaa 224,28,144
S_ s65,s59,7176192
Mvaa 224,32,184
S_ m0,s52,896
Ta 24,v1,12296
Ta 26,v1,12808
Mvaa 232,28,148
Mvaa 232,32,188
L_ v6,28,s65,128
Mvaa 240,28,152
S_ s65,s59,7274496
Mvaa 240,32,192
S_ m0,s52,1920
Mvaa 248,28,156
Mvaa 248,32,196
Mvaa 252,224,196
s_nop 0
Ta 28,v1,8192
Ta 30,v1,8704
Mvaa 252,36,156
L_ v6,28,s65,128
Mvaa 244,224,192
S_ s65,s59,7372800
Mvaa 244,36,152
S_ m0,s52,2944
Ta 32,v1,12288
Ta 34,v1,12800
Mvaa 236,224,188
Mvaa 236,36,148
L_ v6,28,s65,128
Mvaa 228,224,184
S_ s65,s60,0
Mvaa 228,36,144
S_ m0,s50,0
Mvaa 220,224,180
Mvaa 220,36,140
W_ 4
Mvaa 220,20,60
s_nop 0
Ta 36,%0,8200
Ta 38,%0,8712
Mvaa 220,24,100
Da 140,v2,20480
Mvaa 228,20,64
L_ v4,24,s65,0
Mvaa 228,24,104
S_ s65,s60,10240
Ta 224,%0,12296
Ta 226,%0,12808
Mvaa 236,20,68
S_ m0,s50,1024
Da 180,v3,20480
Mvaa 236,24,108
Mvaa 244,20,72
Da 144,v2,22528
Mvaa 244,24,112
L_ v5,24,s65,0
Mvaa 252,20,76
S_ s65,s60,20480
Mvaa 252,24,116
W_ 7
Da 184,v3,22528
S_ m0,s50,2048
Mvaa 248,32,116
s_nop 0
Ta 20,%0,8192
Ta 22,%0,8704
Mvaa 248,28,76
L_ v4,24,s65,0
Mvaa 240,32,112
S_ s65,s60,30720
Da 148,v2,24576
Mvaa 240,28,72
S_ m0,s50,3072
Ta 24,%0,12288
Ta 26,%0,12800
Mvaa 232,32,108
Mvaa 232,28,68
Da 188,v3,24576
Mvaa 224,32,104
L_ v5,24,s65,0
Mvaa 224,28,64
S_ s65,s60,40960
Mvaa 216,32,100
s_mov_b64 exec, 0
s_bitcmp1_b32 s4, 1
s_cbranch_scc1 .Lhop_q2_b
s_bitcmp1_b32 s4, 0
s_cbranch_scc1 .Lpad_q2_1
s_branch .Lpad_q2_0
.Lhop_q2_b:
s_bitcmp1_b32 s4, 0
s_cbranch_scc1 .Lpad_q2_3
s_branch .Lpad_q2_2
.Lhop_q2_r:
s_mov_b64 exec, -1
s_nop 1
.Lqy_late:
s_waitcnt vmcnt(12) lgkmcnt(0)
s_barrier
Da 152,v2,26624
Mvaa 216,28,60
S_ m0,s50,4096
Mvaa 180,36,60
s_nop 0
Ta 28,v1,16392
Ta 30,v1,16904
Mvaa 180,224,100
Da 192,v3,26624
Mvaa 188,36,64
L_ v4,24,s65,0
Mvaa 188,224,104
S_ s65,s59,6291456
Ta 32,v1,20488
Ta 34,v1,21000
Mvaa 196,36,68
S_ m0,s52,8192
Da 156,v2,28672
Mvaa 196,224,108
Mvaa 204,36,72
L_ v6,28,s65,0
Mvaa 204,224,112
S_ s65,s59,6389760
Da 196,v3,28672
Mvaa 212,36,76
S_ m0,s52,9216
Mvaa 212,224,116
Mvaa 208,24,116
s_nop 0
Ta 36,v1,16384
Ta 38,v1,16896
Mvaa 208,20,76
L_ v6,28,s65,0
Mvaa 200,24,112
S_ s65,s59,6488064
Mvaa 200,20,72
S_ m0,s52,10240
Ta 224,v1,20480
Ta 226,v1,20992
Mvaa 192,24,108
Mvaa 192,20,68
L_ v6,28,s65,0
Mvaa 184,24,104
S_ s65,s59,6586368
Mvaa 184,20,64
S_ m0,s52,11264
Mvaa 176,24,100
Mvaa 176,20,60
W_ 6
Mvaa 220,28,140
s_nop 0
Ta 20,%0,16392
Ta 22,%0,16904
Mvaa 220,32,180
L_ v6,28,s65,0
Mvaa 228,28,144
Mvaa 228,32,184
s_nop 0
Ta 24,%0,20488
Ta 26,%0,21000
Mvaa 236,28,148
Mvaa 236,32,188
Mvaa 244,28,152
Mvaa 244,32,192
W_ 9
Mvaa 252,28,156
W_ 8
Mvaa 252,32,196
W_ 4
Mvaa 248,224,196
s_nop 0
Ta 28,%0,16384
Ta 30,%0,16896
Mvaa 248,36,156
Mvaa 240,224,192
Mvaa 240,36,152
s_nop 0
Ta 32,%0,20480
Ta 34,%0,20992
Mvaa 232,224,188
Mvaa 232,36,148
Mvaa 224,224,184
Mvaa 224,36,144
Mvaa 216,224,180
s_waitcnt vmcnt(13) lgkmcnt(0)
s_barrier
Mvaa 216,36,140
S_ s65,s59,7077888
Mvaa 180,20,140
S_ m0,s52,16384
Ta 36,v1,24584
Ta 38,v1,25096
Mvaa 180,24,180
L_ v6,28,s65,0
Mvaa 188,20,144
S_ s65,s59,7176192
Mvaa 188,24,184
S_ m0,s52,17408
Ta 224,v1,28680
Ta 226,v1,29192
Mvaa 196,20,148
U_ v248,s48,v248
U_ v249,s48,v249
Mvaa 196,24,188
L_ v6,28,s65,0
Mvaa 204,20,152
S_ s65,s59,7274496
U_ v250,s48,v250
U_ v251,s48,v251
Mvaa 204,24,192
S_ m0,s52,18432
U_ v252,s48,v252
U_ v253,s48,v253
Mvaa 212,20,156
U_ v254,s48,v254
U_ v255,s48,v255
Mvaa 212,24,196
C_ 255,254,255
C_ 254,252,253
Mvaa 208,32,196
s_nop 0
Ta 20,v1,24576
Ta 22,v1,25088
Mvaa 208,28,156
L_ v6,28,s65,0
Mvaa 200,32,192
S_ s65,s59,7372800
C_ 253,250,251
C_ 252,248,249
Mvaa 200,28,152
S_ m0,s52,19456
Ta 24,v1,28672
Ta 26,v1,29184
Mvaa 192,32,188
X_ a216,v252
X_ a217,v253
Mvaa 192,28,148
L_ v6,28,s65,0
Mvaa 184,32,184
X_ a218,v254
X_ a219,v255
Mvaa 184,28,144
U_ v240,s48,v240
U_ v241,s48,v241
Mvaa 176,32,180
U_ v242,s48,v242
U_ v243,s48,v243
Mvaa 176,28,140
W_ 4
U_ v244,s48,v244
U_ v245,s48,v245
Mvaa 140,36,60
s_nop 0
Ta 28,%0,24584
Ta 30,%0,25096
Mvaa 140,224,100
U_ v246,s48,v246
U_ v247,s48,v247
Mvaa 148,36,64
C_ 251,246,247
C_ 250,244,245
Mvaa 148,224,104
s_nop 0
Ta 32,%0,28680
Ta 34,%0,29192
Mvaa 156,36,68
C_ 249,242,243
C_ 248,240,241
Mvaa 156,224,108
X_ a212,v248
X_ a213,v249
Mvaa 164,36,72
X_ a214,v250
X_ a215,v251
Mvaa 164,224,112
U_ v232,s48,v232
U_ v233,s48,v233
Mvaa 172,36,76
U_ v234,s48,v234
U_ v235,s48,v235
Mvaa 172,224,116
W_ 4
U_ v236,s48,v236
U_ v237,s48,v237
Mvaa 168,24,116
s_nop 0
Ta 36,%0,24576
Ta 38,%0,25088
Mvaa 168,20,76
U_ v238,s48,v238
U_ v239,s48,v239
Mvaa 160,24,112
C_ 247,238,239
C_ 246,236,237
Mvaa 160,20,72
s_nop 0
Ta 224,%0,28672
Ta 226,%0,29184
Mvaa 152,24,108
C_ 245,234,235
C_ 244,232,233
Mvaa 152,20,68
X_ a208,v244
X_ a209,v245
Mvaa 144,24,104
X_ a210,v246
X_ a211,v247
Mvaa 144,20,64
U_ v224,s48,v224
U_ v225,s48,v225
Mvaa 136,24,100
s_waitcnt vmcnt(13) lgkmcnt(0)
s_barrier
U_ v226,s48,v226
Mvaa 136,20,60
S_ s65,s59,-768
U_ v227,s48,v227
Mvaa 100,28,60
S_ m0,s52,24320
Ta 20,v1,8
Ta 22,v1,520
Mvaa 100,32,100
L_ v6,28,s65,256
Mvaa 108,28,64
S_ s65,s59,97536
U_ v228,s48,v228
U_ v229,s48,v229
Mvaa 108,32,104
S_ m0,s52,25344
Ta 24,v1,4104
Ta 26,v1,4616
Mvaa 116,28,68
U_ v230,s48,v230
U_ v231,s48,v231
Mvaa 116,32,108
L_ v6,28,s65,256
Mvaa 124,28,72
S_ s65,s59,195840
C_ 243,230,231
C_ 242,228,229
Mvaa 124,32,112
S_ m0,s52,26368
C_ 241,226,227
C_ 240,224,225
Mvaa 132,28,76
X_ a204,v240
X_ a205,v241
Mvaa 132,32,116
X_ a206,v242
X_ a207,v243
Mvaa 128,224,116
s_nop 0
Ta 28,v1,0
Ta 30,v1,512
Mvaa 128,36,76
L_ v6,28,s65,256
Mvaa 120,224,112
S_ s65,s59,294144
U_ v216,s48,v216
U_ v217,s48,v217
Mvaa 120,36,72
S_ m0,s52,27392
Ta 32,v1,4096
Ta 34,v1,4608
Mvaa 112,224,108
.Lpad_q2_0:
s_cbranch_execz .Lpad_q2_4
U_ v218,s48,v218
U_ v219,s48,v219
Mvaa 112,36,68
L_ v6,28,s65,256
Mvaa 104,224,104
S_ s65,s60,128
U_ v220,s48,v220
U_ v221,s48,v221
Mvaa 104,36,64
S_ m0,s50,20480
U_ v222,s48,v222
U_ v223,s48,v223
Mvaa 96,224,100
C_ 239,222,223
C_ 238,220,221
Mvaa 96,36,60
W_ 4
C_ 237,218,219
C_ 236,216,217
Mvaa 140,20,140
s_nop 0
Ta 36,%0,8
Ta 38,%0,520
Mvaa 140,24,180
L_ v4,24,s65,0
Mvaa 148,20,144
S_ s65,s60,10368
X_ a200,v236
X_ a201,v237
Mvaa 148,24,184
S_ m0,s50,21504
Ta 224,%0,4104
Ta 226,%0,4616
Mvaa 156,20,148
X_ a202,v238
X_ a203,v239
Mvaa 156,24,188
L_ v5,24,s65,0
Mvaa 164,20,152
S_ s65,s60,20608
U_ v208,s48,v208
U_ v209,s48,v209
Mvaa 164,24,192
S_ m0,s50,22528
U_ v210,s48,v210
U_ v211,s48,v211
Mvaa 172,20,156
U_ v212,s48,v212
U_ v213,s48,v213
Mvaa 172,24,196
W_ 4
U_ v214,s48,v214
U_ v215,s48,v215
Mvaa 168,32,196
s_nop 0
Ta 20,%0,0
Ta 22,%0,512
Mvaa 168,28,156
L_ v4,24,s65,0
Mvaa 160,32,192
S_ s65,s60,30848
C_ 235,214,215
C_ 234,212,213
Mvaa 160,28,152
S_ m0,s50,23552
Ta 24,%0,4096
Ta 26,%0,4608
Mvaa 152,32,188
C_ 233,210,211
C_ 232,208,209
Mvaa 152,28,148
L_ v5,24,s65,0
Mvaa 144,32,184
S_ s65,s60,41088
X_ a176,v232
X_ a177,v233
Mvaa 144,28,144
S_ m0,s50,24576
X_ a178,v234
X_ a179,v235
Mvaa 136,32,180
s_waitcnt vmcnt(12) lgkmcnt(0)
s_barrier
U_ v200,s48,v200
Mvaa 136,28,140
L_ v4,24,s65,0
Mvaa 100,36,140
S_ s65,s59,785664
Ta 28,v1,8200
Ta 30,v1,8712
Mvaa 100,224,180
S_ m0,s52,0
U_ v201,s48,v201
Mvaa 108,36,144
U_ v202,s48,v202
U_ v203,s48,v203
Mvaa 108,224,184
s_nop 0
Ta 32,v1,12296
Ta 34,v1,12808
Mvaa 116,36,148
L_ v6,28,s65,0
Mvaa 116,224,188
S_ s65,s59,883968
U_ v204,s48,v204
U_ v205,s48,v205
Mvaa 124,36,152
S_ m0,s52,1024
U_ v206,s48,v206
U_ v207,s48,v207
Mvaa 124,224,192
C_ 231,206,207
C_ 230,204,205
Mvaa 132,36,156
L_ v6,28,s65,0
Mvaa 132,224,196
C_ 229,202,203
S_ s65,s59,982272
Mvaa 128,24,196
S_ m0,s52,2048
Ta 36,v1,8192
Ta 38,v1,8704
Mvaa 128,20,156
C_ 228,200,201
Mvaa 120,24,192
L_ v6,28,s65,0
Mvaa 120,20,152
S_ s65,s59,1080576
Ta 224,v1,12288
Ta 226,v1,12800
Mvaa 112,24,188
S_ m0,s52,3072
X_ a172,v228
X_ a173,v229
Mvaa 112,20,148
X_ a174,v230
X_ a175,v231
Mvaa 104,24,184
L_ v6,28,s65,0
Mvaa 104,20,144
U_ v192,s48,v192
U_ v193,s48,v193
Mvaa 96,24,180
U_ v194,s48,v194
U_ v195,s48,v195
Mvaa 96,20,140
W_ 4
U_ v196,s48,v196
U_ v197,s48,v197
Mvaa 60,28,60
s_nop 0
Ta 20,%0,8200
Ta 22,%0,8712
Mvaa 60,32,100
U_ v198,s48,v198
U_ v199,s48,v199
Mvaa 68,28,64
C_ 227,198,199
C_ 226,196,197
Mvaa 68,32,104
s_nop 0
Ta 24,%0,12296
Ta 26,%0,12808
Mvaa 76,28,68
C_ 225,194,195
C_ 224,192,193
Mvaa 76,32,108
X_ a168,v224
X_ a169,v225
Mvaa 84,28,72
X_ a170,v226
X_ a171,v227
Mvaa 84,32,112
U_ v184,s48,v184
U_ v185,s48,v185
Mvaa 92,28,76
U_ v186,s48,v186
U_ v187,s48,v187
Mvaa 92,32,116
W_ 4
U_ v188,s48,v188
U_ v189,s48,v189
Mvaa 88,224,116
s_nop 0
Ta 28,%0,8192
Ta 30,%0,8704
Mvaa 88,36,76
U_ v190,s48,v190
U_ v191,s48,v191
Mvaa 80,224,112
C_ 223,190,191
C_ 222,188,189
Mvaa 80,36,72
s_nop 0
Ta 32,%0,12288
Ta 34,%0,12800
Mvaa 72,224,108
C_ 221,186,187
C_ 220,184,185
Mvaa 72,36,68
X_ a164,v220
X_ a165,v221
Mvaa 64,224,104
X_ a166,v222
X_ a167,v223
Mvaa 64,36,64
U_ v176,s48,v176
U_ v177,s48,v177
Mvaa 56,224,100
s_waitcnt vmcnt(13) lgkmcnt(0)
s_barrier
U_ v178,s48,v178
Mvaa 56,36,60
S_ s65,s59,-768
U_ v179,s48,v179
Mvaa 20,20,60
S_ m0,s52,8192
Ta 36,v1,16392
Ta 38,v1,16904
Mvaa 20,24,100
L_ v6,28,s65,0
Mvaa 28,20,64
S_ s65,s59,97536
U_ v180,s48,v180
U_ v181,s48,v181
Mvaa 28,24,104
S_ m0,s52,9216
Ta 224,v1,20488
Ta 226,v1,21000
Mvaa 36,20,68
U_ v182,s48,v182
U_ v183,s48,v183
Mvaa 36,24,108
L_ v6,28,s65,0
Mvaa 44,20,72
S_ s65,s59,195840
C_ 219,182,183
C_ 218,180,181
Mvaa 44,24,112
S_ m0,s52,10240
C_ 217,178,179
C_ 216,176,177
Mvaa 52,20,76
X_ a160,v216
X_ a161,v217
Mvaa 52,24,116
X_ a162,v218
X_ a163,v219
Mvaa 48,32,116
s_nop 0
Ta 20,v1,16384
Ta 22,v1,16896
Mvaa 48,28,76
L_ v6,28,s65,0
Mvaa 40,32,112
S_ s65,s59,294144
U_ v168,s48,v168
U_ v169,s48,v169
Mvaa 40,28,72
S_ m0,s52,11264
Ta 24,v1,20480
Ta 26,v1,20992
Mvaa 32,32,108
U_ v170,s48,v170
U_ v171,s48,v171
Mvaa 32,28,68
L_ v6,28,s65,0
Mvaa 24,32,104
U_ v172,s48,v172
U_ v173,s48,v173
Mvaa 24,28,64
U_ v174,s48,v174
U_ v175,s48,v175
Mvaa 16,32,100
C_ 215,174,175
C_ 214,172,173
Mvaa 16,28,60
W_ 4
C_ 213,170,171
C_ 212,168,169
Mvaa 60,36,140
s_nop 0
Ta 28,%0,16392
Ta 30,%0,16904
Mvaa 60,224,180
X_ a136,v212
X_ a137,v213
Mvaa 68,36,144
X_ a138,v214
X_ a139,v215
Mvaa 68,224,184
s_nop 0
Ta 32,%0,20488
Ta 34,%0,21000
Mvaa 76,36,148
U_ v160,s48,v160
U_ v161,s48,v161
Mvaa 76,224,188
U_ v162,s48,v162
U_ v163,s48,v163
Mvaa 84,36,152
U_ v164,s48,v164
U_ v165,s48,v165
Mvaa 84,224,192
U_ v166,s48,v166
U_ v167,s48,v167
Mvaa 92,36,156
C_ 211,166,167
C_ 210,164,165
Mvaa 92,224,196
W_ 4
C_ 209,162,163
C_ 208,160,161
Mvaa 88,24,196
s_nop 0
Ta 36,%0,16384
Ta 38,%0,16896
Mvaa 88,20,156
X_ a132,v208
X_ a133,v209
Mvaa 80,24,192
X_ a134,v210
X_ a135,v211
Mvaa 80,20,152
s_nop 0
Ta 224,%0,20480
Ta 226,%0,20992
Mvaa 72,24,188
U_ v152,s48,v152
U_ v153,s48,v153
Mvaa 72,20,148
U_ v154,s48,v154
U_ v155,s48,v155
Mvaa 64,24,184
U_ v156,s48,v156
U_ v157,s48,v157
Mvaa 64,20,144
U_ v158,s48,v158
U_ v159,s48,v159
Mvaa 56,24,180
s_waitcnt vmcnt(63) lgkmcnt(0)
s_barrier
C_ 207,158,159
Mvaa 56,20,140
S_ s65,s59,-768
C_ 206,156,157
Mvaa 20,28,140
S_ m0,s52,16256
C_ 205,154,155
C_ 204,152,153
Mvaa 20,32,180
L_ v6,28,s65,128
Mvaa 28,28,144
S_ s65,s59,97536
X_ a128,v204
X_ a129,v205
Mvaa 28,32,184
S_ m0,s52,17280
X_ a130,v206
X_ a131,v207
Mvaa 36,28,148
U_ v144,s48,v144
U_ v145,s48,v145
Mvaa 36,32,188
L_ v6,28,s65,128
Mvaa 44,28,152
S_ s65,s59,195840
U_ v146,s48,v146
U_ v147,s48,v147
Mvaa 44,32,192
S_ m0,s52,18304
U_ v148,s48,v148
U_ v149,s48,v149
Mvaa 52,28,156
U_ v150,s48,v150
U_ v151,s48,v151
Mvaa 52,32,196
C_ 203,150,151
C_ 202,148,149
Mvaa 48,224,196
L_ v6,28,s65,128
Mvaa 48,36,156
S_ s65,s59,294144
C_ 201,146,147
C_ 200,144,145
Mvaa 40,224,192
S_ m0,s52,19328
X_ a124,v200
X_ a125,v201
Mvaa 40,36,152
X_ a126,v202
X_ a127,v203
Mvaa 32,224,188
L_ v6,28,s65,128
Mvaa 32,36,148
U_ v136,s48,v136
U_ v137,s48,v137
Mvaa 24,224,184
U_ v138,s48,v138
U_ v139,s48,v139
Mvaa 24,36,144
U_ v140,s48,v140
U_ v141,s48,v141
Mvaa 16,224,180
U_ v142,s48,v142
U_ v143,s48,v143
Mvaa 16,36,140
C_ 199,142,143
C_ 198,140,141
C_ 197,138,139
C_ 196,136,137
X_ a120,v196
X_ a121,v197
X_ a122,v198
X_ a123,v199
U_ v128,s48,v128
U_ v129,s48,v129
U_ v130,s48,v130
U_ v131,s48,v131
U_ v132,s48,v132
U_ v133,s48,v133
U_ v134,s48,v134
U_ v135,s48,v135
C_ 195,134,135
C_ 194,132,133
C_ 193,130,131
C_ 192,128,129
X_ a96,v192
X_ a97,v193
X_ a98,v194
X_ a99,v195
U_ v120,s48,v120
U_ v121,s48,v121
U_ v122,s48,v122
U_ v123,s48,v123
U_ v124,s48,v124
U_ v125,s48,v125
U_ v126,s48,v126
U_ v127,s48,v127
C_ 191,126,127
C_ 190,124,125
C_ 189,122,123
C_ 188,120,121
X_ a92,v188
X_ a93,v189
X_ a94,v190
X_ a95,v191
U_ v112,s48,v112
U_ v113,s48,v113
U_ v114,s48,v114
U_ v115,s48,v115
U_ v116,s48,v116
U_ v117,s48,v117
U_ v118,s48,v118
U_ v119,s48,v119
C_ 187,118,119
C_ 186,116,117
C_ 185,114,115
C_ 184,112,113
X_ a88,v184
X_ a89,v185
X_ a90,v186
X_ a91,v187
U_ v104,s48,v104
U_ v105,s48,v105
U_ v106,s48,v106
U_ v107,s48,v107
U_ v108,s48,v108
U_ v109,s48,v109
U_ v110,s48,v110
U_ v111,s48,v111
C_ 183,110,111
C_ 182,108,109
C_ 181,106,107
C_ 180,104,105
X_ a84,v180
X_ a85,v181
X_ a86,v182
X_ a87,v183
U_ v96,s48,v96
U_ v97,s48,v97
U_ v98,s48,v98
U_ v99,s48,v99
U_ v100,s48,v100
U_ v101,s48,v101
U_ v102,s48,v102
U_ v103,s48,v103
C_ 179,102,103
C_ 178,100,101
C_ 177,98,99
C_ 176,96,97
X_ a80,v176
X_ a81,v177
X_ a82,v178
X_ a83,v179
v_pk_mul_f32 v[88:89], v[88:89], s[48:49]
v_pk_mul_f32 v[90:91], v[90:91], s[48:49]
v_pk_mul_f32 v[92:93], v[92:93], s[48:49]
v_pk_mul_f32 v[94:95], v[94:95], s[48:49]
C_ 175,94,95
C_ 174,92,93
C_ 173,90,91
C_ 172,88,89
X_ a56,v172
X_ a57,v173
X_ a58,v174
X_ a59,v175
v_pk_mul_f32 v[80:81], v[80:81], s[48:49]
v_pk_mul_f32 v[82:83], v[82:83], s[48:49]
v_pk_mul_f32 v[84:85], v[84:85], s[48:49]
v_pk_mul_f32 v[86:87], v[86:87], s[48:49]
C_ 171,86,87
C_ 170,84,85
C_ 169,82,83
C_ 168,80,81
X_ a52,v168
X_ a53,v169
X_ a54,v170
X_ a55,v171
v_pk_mul_f32 v[72:73], v[72:73], s[48:49]
v_pk_mul_f32 v[74:75], v[74:75], s[48:49]
v_pk_mul_f32 v[76:77], v[76:77], s[48:49]
v_pk_mul_f32 v[78:79], v[78:79], s[48:49]
C_ 167,78,79
C_ 166,76,77
C_ 165,74,75
C_ 164,72,73
X_ a48,v164
X_ a49,v165
X_ a50,v166
X_ a51,v167
v_pk_mul_f32 v[64:65], v[64:65], s[48:49]
v_pk_mul_f32 v[66:67], v[66:67], s[48:49]
v_pk_mul_f32 v[68:69], v[68:69], s[48:49]
v_pk_mul_f32 v[70:71], v[70:71], s[48:49]
C_ 163,70,71
C_ 162,68,69
C_ 161,66,67
C_ 160,64,65
X_ a44,v160
X_ a45,v161
X_ a46,v162
X_ a47,v163
v_pk_mul_f32 v[56:57], v[56:57], s[48:49]
v_pk_mul_f32 v[58:59], v[58:59], s[48:49]
v_pk_mul_f32 v[60:61], v[60:61], s[48:49]
v_pk_mul_f32 v[62:63], v[62:63], s[48:49]
C_ 159,62,63
C_ 158,60,61
C_ 157,58,59
C_ 156,56,57
X_ a40,v156
X_ a41,v157
X_ a42,v158
X_ a43,v159
v_pk_mul_f32 v[48:49], v[48:49], s[48:49]
v_pk_mul_f32 v[50:51], v[50:51], s[48:49]
v_pk_mul_f32 v[52:53], v[52:53], s[48:49]
v_pk_mul_f32 v[54:55], v[54:55], s[48:49]
C_ 155,54,55
C_ 154,52,53
C_ 153,50,51
C_ 152,48,49
X_ a16,v152
X_ a17,v153
X_ a18,v154
X_ a19,v155
v_pk_mul_f32 v[40:41], v[40:41], s[48:49]
v_pk_mul_f32 v[42:43], v[42:43], s[48:49]
v_pk_mul_f32 v[44:45], v[44:45], s[48:49]
v_pk_mul_f32 v[46:47], v[46:47], s[48:49]
C_ 151,46,47
C_ 150,44,45
C_ 149,42,43
C_ 148,40,41
X_ a12,v148
X_ a13,v149
X_ a14,v150
X_ a15,v151
s_sub_u32 s59, s59, 768
s_waitcnt vmcnt(0) lgkmcnt(0)
s_barrier
Ta 20,%0,8192
Ta 22,%0,8704
Ta 24,%0,12288
Ta 26,%0,12800
Da 60,v2,0
Da 100,v3,0
Da 64,v2,2048
Da 104,v3,2048
.Lpad_q2_1:
s_cbranch_execz .Lpad_q2_5
Da 68,v2,4096
Da 108,v3,4096
Da 72,v2,6144
Da 112,v3,6144
Da 76,v2,8192
Da 116,v3,8192
Ta 28,%0,8200
Ta 30,%0,8712
Ta 32,%0,12296
Ta 34,%0,12808
v_pk_mul_f32 v[32:33], v[32:33], s[48:49]
v_pk_mul_f32 v[34:35], v[34:35], s[48:49]
v_pk_mul_f32 v[36:37], v[36:37], s[48:49]
v_pk_mul_f32 v[38:39], v[38:39], s[48:49]
C_ 147,38,39
C_ 146,36,37
C_ 145,34,35
C_ 144,32,33
X_ a8,v144
X_ a9,v145
X_ a10,v146
X_ a11,v147
v_pk_mul_f32 v[24:25], v[24:25], s[48:49]
v_pk_mul_f32 v[26:27], v[26:27], s[48:49]
v_pk_mul_f32 v[28:29], v[28:29], s[48:49]
v_pk_mul_f32 v[30:31], v[30:31], s[48:49]
C_ 143,30,31
C_ 142,28,29
C_ 141,26,27
C_ 140,24,25
X_ a4,v140
X_ a5,v141
X_ a6,v142
X_ a7,v143
v_pk_mul_f32 v[16:17], v[16:17], s[48:49]
v_pk_mul_f32 v[18:19], v[18:19], s[48:49]
v_pk_mul_f32 v[20:21], v[20:21], s[48:49]
v_pk_mul_f32 v[22:23], v[22:23], s[48:49]
C_ 139,22,23
C_ 138,20,21
C_ 137,18,19
C_ 136,16,17
X_ a0,v136
X_ a1,v137
X_ a2,v138
X_ a3,v139
s_sub_u32 s90, s90, s88
s_cmp_gt_u32 s90, 4000
s_cselect_b32 s11, 1, 0
s_sub_u32 s12, s68, s98
S_ s12,s12,1252
s_mov_b32 s13, 110
s_nop 1
.p2align 3
.Lq:
W_ 13
Zvaa 16,20,60
s_nop 0
Ta 36,v1,8192
Ta 38,v1,8704
W_ 14
Mvaa 16,24,100
R_ v12,a248
W_ 13
Zvaa 24,20,64
v_lshrrev_b32 v12, 4, v12
W_ 12
Mvaa 24,24,104
s_nop 0
Ta 224,v1,12288
Ta 226,v1,12800
W_ 13
Zvaa 32,20,68
v_min_u32 v10, 11, v12
W_ 12
Mvaa 32,24,108
s_mul_i32 s56, s4, 12
W_ 11
Zvaa 40,20,72
v_add_u32 v10, s56, v10
W_ 10
Mvaa 40,24,112
v_mul_u32_u24 v11, 43691, v10
W_ 9
Zvaa 48,20,76
v_lshrrev_b32 v11, 18, v11
W_ 8
Mvaa 48,24,116
W_ 4
v_lshlrev_b32 v10, 7, v10
Zvaa 52,32,116
s_nop 0
Ta 20,v1,8200
Ta 22,v1,8712
Mvaa 52,28,76
v_mul_u32_u24 v11, 11520, v11
Zvaa 44,32,112
v_add_u32 v10, v10, v11
Mvaa 44,28,72
s_nop 0
Ta 24,v1,12296
Ta 26,v1,12808
Zvaa 36,32,108
v_min_u32 v12, 9, v12
Mvaa 36,28,68
s_and_b32 s56, s6, 3
Zvaa 28,32,104
s_lshl_b32 s56, s56, 2
Mvaa 28,28,64
S_ s56,s56,s4
Zvaa 20,32,100
s_waitcnt vmcnt(18) lgkmcnt(0)
s_barrier
s_mul_i32 s56, s56, 10
Mvaa 20,28,60
S_ s65,s59,786432
v_add_u32 v12, s56, v12
Zvaa 56,36,60
S_ m0,s52,8064
Ta 28,%0,16384
Ta 30,%0,16896
Mvaa 56,224,100
L_ v6,28,s65,128
Zvaa 64,36,64
S_ s65,s59,884736
v_mul_u32_u24 v12, 1280, v12
Mvaa 64,224,104
S_ m0,s52,9088
Ta 32,%0,20480
Ta 34,%0,20992
Zvaa 72,36,68
s_lshr_b32 s56, s7, 5
Mvaa 72,224,108
L_ v6,28,s65,128
Zvaa 80,36,72
S_ s65,s59,983040
s_mul_i32 s56, s56, 98304
Mvaa 80,224,112
S_ m0,s52,10112
s_and_b32 s55, s59, 4095
Zvaa 88,36,76
S_ s56,s56,s55
Mvaa 88,224,116
s_mul_i32 s55, s77, 384
Zvaa 92,24,116
s_sub_u32 s56, s56, s55
Ta 36,%0,16392
Ta 38,%0,16904
Mvaa 92,20,76
L_ v6,28,s65,128
Zvaa 84,24,112
S_ s65,s59,1081344
s_mov_b32 s63, 0
Mvaa 84,20,72
S_ m0,s52,11136
Ta 224,%0,20488
Ta 226,%0,21000
Zvaa 76,24,108
s_bitcmp1_b32 1, s58
s_cselect_b32 s63, 0, 4026531840
Mvaa 76,20,68
L_ v6,28,s65,128
Zvaa 68,24,104
S_ s65,s60,256
S_ s56,s56,s63
Mvaa 68,20,64
S_ m0,s50,0
S_ s55,s56,3145728
Zvaa 60,24,100
Mvaa 60,20,60
W_ 4
Zvaa 96,28,60
s_nop 0
Ta 20,v1,16384
Ta 22,v1,16896
Mvaa 96,32,100
L_ v4,24,s65,0
Zvaa 104,28,64
S_ s65,s60,10496
Mvaa 104,32,104
S_ m0,s50,1024
Ta 24,v1,20480
Ta 26,v1,20992
Zvaa 112,28,68
Mvaa 112,32,108
L_ v5,24,s65,0
Zvaa 120,28,72
S_ s65,s60,20736
Mvaa 120,32,112
S_ m0,s50,2048
Zvaa 128,28,76
Mvaa 128,32,116
W_ 4
Zvaa 132,224,116
s_nop 0
Ta 28,v1,16392
Ta 30,v1,16904
Mvaa 132,36,76
L_ v4,24,s65,0
Zvaa 124,224,112
S_ s65,s60,30976
Mvaa 124,36,72
S_ m0,s50,3072
Ta 32,v1,20488
Ta 34,v1,21000
Zvaa 116,224,108
Mvaa 116,36,68
L_ v5,24,s65,0
Zvaa 108,224,104
S_ s65,s60,41216
Mvaa 108,36,64
S_ m0,s50,4096
Zvaa 100,224,100
s_cmp_eq_u32 s58, 0
s_cbranch_scc0 .Lq_early
s_mov_b64 exec, 0
s_bitcmp1_b32 s4, 1
s_cbranch_scc1 .Lhop_qx_b
s_bitcmp1_b32 s4, 0
s_cbranch_scc1 .Lpad_qx_1
s_branch .Lpad_qx_0
.Lhop_qx_b:
s_bitcmp1_b32 s4, 0
s_cbranch_scc1 .Lpad_qx_3
s_branch .Lpad_qx_2
.Lhop_qx_r:
s_mov_b64 exec, -1
s_nop 1
.Lq_early:
s_waitcnt vmcnt(17) lgkmcnt(0)
s_barrier
s_cmp_eq_u32 s11, 1
s_cbranch_scc1 .Lpace1
.Lpace1_back:
Mvaa 100,36,60
Da 140,v2,20480
Zvaa 136,20,60
s_nop 0
Ta 36,%0,24576
Ta 38,%0,25088
Mvaa 136,24,100
L_ v4,24,s65,0
Zvaa 144,20,64
S_ s65,s59,786432
Da 180,v3,20480
Mvaa 144,24,104
S_ m0,s52,16128
Ta 224,%0,28672
Ta 226,%0,29184
Zvaa 152,20,68
Mvaa 152,24,108
Da 144,v2,22528
Zvaa 160,20,72
L_ v6,28,s65,256
Mvaa 160,24,112
S_ s65,s59,884736
Zvaa 168,20,76
S_ m0,s52,17152
Da 184,v3,22528
Mvaa 168,24,116
Zvaa 172,32,116
s_nop 0
Ta 20,%0,24584
Ta 22,%0,25096
Mvaa 172,28,76
Da 148,v2,24576
Zvaa 164,32,112
L_ v6,28,s65,256
Mvaa 164,28,72
S_ s65,s59,983040
Ta 24,%0,28680
Ta 26,%0,29192
Zvaa 156,32,108
S_ m0,s52,18176
Da 188,v3,24576
Mvaa 156,28,68
Zvaa 148,32,104
L_ v6,28,s65,256
Mvaa 148,28,64
S_ s65,s59,1081344
Da 152,v2,26624
Zvaa 140,32,100
S_ m0,s52,19200
Mvaa 140,28,60
W_ 9
Zvaa 176,36,60
s_nop 0
Ta 28,v1,24576
Ta 30,v1,25088
Mvaa 176,224,100
Da 192,v3,26624
Zvaa 184,36,64
L_ v6,28,s65,256
Mvaa 184,224,104
s_nop 0
Ta 32,v1,28672
Ta 34,v1,29184
Zvaa 192,36,68
Da 156,v2,28672
Mvaa 192,224,108
Zvaa 200,36,72
Da 196,v3,28672
Mvaa 200,224,112
buffer_load_dword a254, v10, s[28:31], s55 offen
Zvaa 208,36,76
Mvaa 208,224,116
W_ 9
Zvaa 212,24,116
s_nop 0
Ta 36,v1,24584
Ta 38,v1,25096
Mvaa 212,20,76
buffer_load_dword a254, v12, s[24:27], s63 offen offset:512
Zvaa 204,24,112
Mvaa 204,20,72
s_nop 0
Ta 224,v1,28680
Ta 226,v1,29192
Zvaa 196,24,108
Mvaa 196,20,68
Zvaa 188,24,104
Mvaa 188,20,64
Zvaa 180,24,100
s_waitcnt vmcnt(20) lgkmcnt(0)
s_barrier
s_cmp_eq_u32 s11, 2
s_cbranch_scc1 .Lqsl2
.Lqsl2_back:
Mvaa 180,20,60
S_ s65,s59,1572864
Zvaa 216,28,60
S_ m0,s52,24576
Ta 20,%0,0
Ta 22,%0,512
Mvaa 216,32,100
L_ v6,28,s65,0
Zvaa 224,28,64
S_ s65,s59,1671168
Mvaa 224,32,104
S_ m0,s52,25600
Ta 24,%0,4096
Ta 26,%0,4608
Zvaa 232,28,68
Mvaa 232,32,108
L_ v6,28,s65,0
Zvaa 240,28,72
S_ s65,s59,1769472
Mvaa 240,32,112
S_ m0,s52,26624
Zvaa 248,28,76
Mvaa 248,32,116
Zvaa 252,224,116
s_nop 0
Ta 28,%0,8
Ta 30,%0,520
Mvaa 252,36,76
L_ v6,28,s65,0
Zvaa 244,224,112
S_ s65,s59,1867776
Mvaa 244,36,72
S_ m0,s52,27648
Ta 32,%0,4104
Ta 34,%0,4616
Zvaa 236,224,108
Mvaa 236,36,68
L_ v6,28,s65,0
Zvaa 228,224,104
S_ s65,s60,384
Mvaa 228,36,64
S_ m0,s50,20480
Zvaa 220,224,100
Mvaa 220,36,60
W_ 4
Mvaa 16,20,140
s_nop 0
Ta 36,v1,0
Ta 38,v1,512
Mvaa 16,24,180
L_ v4,24,s65,0
Mvaa 24,20,144
S_ s65,s60,10624
Mvaa 24,24,184
S_ m0,s50,21504
Ta 224,v1,4096
Ta 226,v1,4608
Mvaa 32,20,148
Mvaa 32,24,188
L_ v5,24,s65,0
Mvaa 40,20,152
S_ s65,s60,20864
Mvaa 40,24,192
S_ m0,s50,22528
Mvaa 48,20,156
Mvaa 48,24,196
W_ 4
Mvaa 52,32,196
s_nop 0
Ta 20,v1,8
Ta 22,v1,520
Mvaa 52,28,156
L_ v4,24,s65,0
Mvaa 44,32,192
S_ s65,s60,31104
Mvaa 44,28,152
S_ m0,s50,23552
Ta 24,v1,4104
Ta 26,v1,4616
Mvaa 36,32,188
Mvaa 36,28,148
L_ v5,24,s65,0
Mvaa 28,32,184
S_ s65,s60,41344
Mvaa 28,28,144
S_ m0,s50,24576
Mvaa 20,32,180
s_waitcnt vmcnt(19) lgkmcnt(0)
s_barrier
s_cmp_eq_u32 s11, 1
s_cbranch_scc1 .Lpace3
.Lpace3_back:
Mvaa 20,28,140
L_ v4,24,s65,0
Mvaa 56,36,140
S_ s65,s59,1572864
Ta 28,%0,8192
Ta 30,%0,8704
Mvaa 56,224,180
S_ m0,s52,-128
Mvaa 64,36,144
Mvaa 64,224,184
s_nop 0
Ta 32,%0,12288
Ta 34,%0,12800
Mvaa 72,36,148
L_ v6,28,s65,128
Mvaa 72,224,188
S_ s65,s59,1671168
Mvaa 80,36,152
S_ m0,s52,896
Mvaa 80,224,192
Mvaa 88,36,156
L_ v6,28,s65,128
Mvaa 88,224,196
S_ s65,s59,1769472
Mvaa 92,24,196
S_ m0,s52,1920
Ta 36,%0,8200
Ta 38,%0,8712
Mvaa 92,20,156
S_ s55,s56,3932160
Mvaa 84,24,192
L_ v6,28,s65,128
Mvaa 84,20,152
S_ s65,s59,1867776
Ta 224,%0,12296
Ta 226,%0,12808
Mvaa 76,24,188
S_ m0,s52,2944
Mvaa 76,20,148
Mvaa 68,24,184
L_ v6,28,s65,128
Mvaa 68,20,144
Mvaa 60,24,180
Mvaa 60,20,140
W_ 4
Mvaa 96,28,140
s_nop 0
Ta 20,v1,8192
Ta 22,v1,8704
Mvaa 96,32,180
buffer_load_dword a254, v10, s[28:31], s55 offen
Mvaa 104,28,144
Mvaa 104,32,184
s_nop 0
Ta 24,v1,12288
Ta 26,v1,12800
Mvaa 112,28,148
Mvaa 112,32,188
buffer_load_dword a254, v12, s[24:27], s63 offen offset:640
Mvaa 120,28,152
Mvaa 120,32,192
)A"
R"A(Mvaa 128,28,156
Mvaa 128,32,196
W_ 4
Mvaa 132,224,196
s_nop 0
Ta 28,v1,8200
Ta 30,v1,8712
Mvaa 132,36,156
Mvaa 124,224,192
Mvaa 124,36,152
s_nop 0
Ta 32,v1,12296
Ta 34,v1,12808
Mvaa 116,224,188
Mvaa 116,36,148
Mvaa 108,224,184
Mvaa 108,36,144
Mvaa 100,224,180
s_waitcnt vmcnt(17) lgkmcnt(0)
s_barrier
s_cmp_eq_u32 s11, 2
s_cbranch_scc1 .Lqsl4
.Lqsl4_back:
Mvaa 100,36,140
S_ s65,s59,1572864
Da 60,v2,0
Mvaa 136,20,140
S_ m0,s52,7936
Ta 36,%0,16384
Ta 38,%0,16896
Mvaa 136,24,180
L_ v6,28,s65,256
Mvaa 144,20,144
S_ s65,s59,1671168
Da 100,v3,0
Mvaa 144,24,184
S_ m0,s52,8960
Ta 224,%0,20480
Ta 226,%0,20992
Mvaa 152,20,148
Mvaa 152,24,188
Da 64,v2,2048
Mvaa 160,20,152
L_ v6,28,s65,256
Mvaa 160,24,192
S_ s65,s59,1769472
Mvaa 168,20,156
S_ m0,s52,9984
Da 104,v3,2048
Mvaa 168,24,196
Mvaa 172,32,196
s_nop 0
Ta 20,%0,16392
Ta 22,%0,16904
Mvaa 172,28,156
Da 68,v2,4096
Mvaa 164,32,192
L_ v6,28,s65,256
Mvaa 164,28,152
S_ s65,s59,1867776
Ta 24,%0,20488
Ta 26,%0,21000
Mvaa 156,32,188
S_ m0,s52,11008
Da 108,v3,4096
Mvaa 156,28,148
Mvaa 148,32,184
L_ v6,28,s65,256
Mvaa 148,28,144
Da 72,v2,6144
Mvaa 140,32,180
Mvaa 140,28,140
W_ 9
Mvaa 176,36,140
s_nop 0
Ta 28,v1,16384
Ta 30,v1,16896
Mvaa 176,224,180
Da 112,v3,6144
Mvaa 184,36,144
Mvaa 184,224,184
.Lpad_q2_2:
s_cbranch_execz .Lpad_q2_6
Ta 32,v1,20480
Ta 34,v1,20992
Mvaa 192,36,148
.Lpad_qx_0:
s_cbranch_execz .Lpad_qx_4
Da 76,v2,8192
Mvaa 192,224,188
Mvaa 200,36,152
Da 116,v3,8192
Mvaa 200,224,192
Mvaa 208,36,156
Mvaa 208,224,196
W_ 9
Mvaa 212,24,196
s_nop 0
Ta 36,v1,16392
Ta 38,v1,16904
Mvaa 212,20,156
Mvaa 204,24,192
Mvaa 204,20,152
s_nop 0
Ta 224,v1,20488
Ta 226,v1,21000
Mvaa 196,24,188
Mvaa 196,20,148
Mvaa 188,24,184
Mvaa 188,20,144
Mvaa 180,24,180
s_waitcnt vmcnt(15) lgkmcnt(0)
s_barrier
s_cmp_eq_u32 s11, 1
s_cbranch_scc1 .Lpace5
.Lpace5_back:
Mvaa 180,20,140
S_ s65,s59,2359296
Mvaa 216,28,140
S_ m0,s52,16384
Ta 20,%0,24576
Ta 22,%0,25088
Mvaa 216,32,180
L_ v6,28,s65,0
Mvaa 224,28,144
S_ s65,s59,2457600
Mvaa 224,32,184
S_ m0,s52,17408
Ta 24,%0,28672
Ta 26,%0,29184
Mvaa 232,28,148
Mvaa 232,32,188
L_ v6,28,s65,0
Mvaa 240,28,152
S_ s65,s59,2555904
Mvaa 240,32,192
S_ m0,s52,18432
Mvaa 248,28,156
Mvaa 248,32,196
Mvaa 252,224,196
s_nop 0
Ta 28,%0,24584
Ta 30,%0,25096
Mvaa 252,36,156
L_ v6,28,s65,0
Mvaa 244,224,192
S_ s65,s59,2654208
Mvaa 244,36,152
S_ m0,s52,19456
Ta 32,%0,28680
Ta 34,%0,29192
Mvaa 236,224,188
Mvaa 236,36,148
L_ v6,28,s65,0
Mvaa 228,224,184
S_ s65,s60,512
Mvaa 228,36,144
S_ m0,s50,0
Mvaa 220,224,180
Mvaa 220,36,140
W_ 4
Mvaa 16,20,60
s_nop 0
Ta 36,v1,24576
Ta 38,v1,25088
Mvaa 16,24,100
L_ v4,24,s65,0
Mvaa 24,20,64
S_ s65,s60,10752
Mvaa 24,24,104
S_ m0,s50,1024
Ta 224,v1,28672
Ta 226,v1,29184
Mvaa 32,20,68
Mvaa 32,24,108
L_ v5,24,s65,0
Mvaa 40,20,72
S_ s65,s60,20992
Mvaa 40,24,112
S_ m0,s50,2048
Mvaa 48,20,76
Mvaa 48,24,116
W_ 4
Mvaa 52,32,116
s_nop 0
Ta 20,v1,24584
Ta 22,v1,25096
Mvaa 52,28,76
L_ v4,24,s65,0
Mvaa 44,32,112
S_ s65,s60,31232
Mvaa 44,28,72
S_ m0,s50,3072
Ta 24,v1,28680
Ta 26,v1,29192
Mvaa 36,32,108
Mvaa 36,28,68
L_ v5,24,s65,0
Mvaa 28,32,104
S_ s65,s60,41472
Mvaa 28,28,64
S_ m0,s50,4096
Mvaa 20,32,100
s_waitcnt vmcnt(14) lgkmcnt(0)
s_barrier
s_cmp_eq_u32 s11, 2
s_cbranch_scc1 .Lqsl6
.Lqsl6_back:
Mvaa 20,28,60
L_ v4,24,s65,0
Mvaa 56,36,60
S_ s65,s59,2359296
Ta 28,%0,0
Ta 30,%0,512
Mvaa 56,224,100
S_ m0,s52,24448
Mvaa 64,36,64
Mvaa 64,224,104
s_nop 0
Ta 32,%0,4096
Ta 34,%0,4608
Mvaa 72,36,68
L_ v6,28,s65,128
Mvaa 72,224,108
S_ s65,s59,2457600
Mvaa 80,36,72
S_ m0,s52,25472
Mvaa 80,224,112
Mvaa 88,36,76
L_ v6,28,s65,128
Mvaa 88,224,116
S_ s65,s59,2555904
Mvaa 92,24,116
S_ m0,s52,26496
Ta 36,%0,8
Ta 38,%0,520
Mvaa 92,20,76
S_ s55,s56,4718592
Mvaa 84,24,112
L_ v6,28,s65,128
Mvaa 84,20,72
S_ s65,s59,2654208
Ta 224,%0,4104
Ta 226,%0,4616
Mvaa 76,24,108
S_ m0,s52,27520
Mvaa 76,20,68
Mvaa 68,24,104
L_ v6,28,s65,128
Mvaa 68,20,64
Mvaa 60,24,100
Mvaa 60,20,60
W_ 4
Mvaa 96,28,60
s_nop 0
Ta 20,v1,0
Ta 22,v1,512
Mvaa 96,32,100
buffer_load_dword a254, v10, s[28:31], s55 offen
Mvaa 104,28,64
Mvaa 104,32,104
s_nop 0
Ta 24,v1,4096
Ta 26,v1,4608
Mvaa 112,28,68
Mvaa 112,32,108
buffer_load_dword a254, v12, s[24:27], s63 offen offset:768
Mvaa 120,28,72
Mvaa 120,32,112
Mvaa 128,28,76
Mvaa 128,32,116
W_ 4
Mvaa 132,224,116
s_nop 0
Ta 28,v1,8
Ta 30,v1,520
Mvaa 132,36,76
Mvaa 124,224,112
Mvaa 124,36,72
s_nop 0
Ta 32,v1,4104
Ta 34,v1,4616
Mvaa 116,224,108
Mvaa 116,36,68
Mvaa 108,224,104
Mvaa 108,36,64
Mvaa 100,224,100
s_waitcnt vmcnt(15) lgkmcnt(0)
s_barrier
s_cmp_eq_u32 s11, 1
s_cbranch_scc1 .Lpace7
.Lpace7_back:
Mvaa 100,36,60
S_ s65,s59,2359296
Da 140,v2,20480
Mvaa 136,20,60
S_ m0,s52,-256
Ta 36,%0,8192
Ta 38,%0,8704
Mvaa 136,24,100
L_ v6,28,s65,256
Mvaa 144,20,64
S_ s65,s59,2457600
Da 180,v3,20480
Mvaa 144,24,104
S_ m0,s52,768
Ta 224,%0,12288
Ta 226,%0,12800
Mvaa 152,20,68
Mvaa 152,24,108
Da 144,v2,22528
Mvaa 160,20,72
L_ v6,28,s65,256
Mvaa 160,24,112
S_ s65,s59,2555904
Mvaa 168,20,76
S_ m0,s52,1792
Da 184,v3,22528
Mvaa 168,24,116
Mvaa 172,32,116
s_nop 0
Ta 20,%0,8200
Ta 22,%0,8712
Mvaa 172,28,76
Da 148,v2,24576
Mvaa 164,32,112
L_ v6,28,s65,256
Mvaa 164,28,72
S_ s65,s59,2654208
Ta 24,%0,12296
Ta 26,%0,12808
Mvaa 156,32,108
S_ m0,s52,2816
Da 188,v3,24576
Mvaa 156,28,68
Mvaa 148,32,104
L_ v6,28,s65,256
Mvaa 148,28,64
Da 152,v2,26624
Mvaa 140,32,100
Mvaa 140,28,60
W_ 9
Mvaa 176,36,60
s_nop 0
Ta 28,v1,8192
Ta 30,v1,8704
Mvaa 176,224,100
Da 192,v3,26624
Mvaa 184,36,64
Mvaa 184,224,104
s_nop 0
Ta 32,v1,12288
Ta 34,v1,12800
Mvaa 192,36,68
Da 156,v2,28672
Mvaa 192,224,108
Mvaa 200,36,72
Da 196,v3,28672
Mvaa 200,224,112
Mvaa 208,36,76
Mvaa 208,224,116
W_ 9
Mvaa 212,24,116
s_nop 0
Ta 36,v1,8200
Ta 38,v1,8712
Mvaa 212,20,76
Mvaa 204,24,112
Mvaa 204,20,72
s_nop 0
Ta 224,v1,12296
Ta 226,v1,12808
Mvaa 196,24,108
Mvaa 196,20,68
Mvaa 188,24,104
Mvaa 188,20,64
Mvaa 180,24,100
s_waitcnt vmcnt(15) lgkmcnt(0)
s_barrier
s_cmp_eq_u32 s11, 2
s_cbranch_scc1 .Lqsl8
.Lqsl8_back:
Mvaa 180,20,60
S_ s65,s59,3145728
Mvaa 216,28,60
S_ m0,s52,8192
Ta 20,%0,16384
Ta 22,%0,16896
Mvaa 216,32,100
L_ v6,28,s65,0
Mvaa 224,28,64
S_ s65,s59,3244032
Mvaa 224,32,104
S_ m0,s52,9216
Ta 24,%0,20480
Ta 26,%0,20992
Mvaa 232,28,68
Mvaa 232,32,108
L_ v6,28,s65,0
Mvaa 240,28,72
S_ s65,s59,3342336
Mvaa 240,32,112
S_ m0,s52,10240
Mvaa 248,28,76
Mvaa 248,32,116
Mvaa 252,224,116
s_nop 0
Ta 28,%0,16392
Ta 30,%0,16904
Mvaa 252,36,76
L_ v6,28,s65,0
Mvaa 244,224,112
S_ s65,s59,3440640
Mvaa 244,36,72
S_ m0,s52,11264
Ta 32,%0,20488
Ta 34,%0,21000
Mvaa 236,224,108
Mvaa 236,36,68
L_ v6,28,s65,0
Mvaa 228,224,104
S_ s65,s60,640
Mvaa 228,36,64
S_ m0,s50,20480
Mvaa 220,224,100
Mvaa 220,36,60
W_ 4
Mvaa 16,20,140
s_nop 0
Ta 36,v1,16384
Ta 38,v1,16896
Mvaa 16,24,180
L_ v4,24,s65,0
Mvaa 24,20,144
S_ s65,s60,10880
Mvaa 24,24,184
S_ m0,s50,21504
Ta 224,v1,20480
Ta 226,v1,20992
Mvaa 32,20,148
Mvaa 32,24,188
L_ v5,24,s65,0
Mvaa 40,20,152
S_ s65,s60,21120
Mvaa 40,24,192
S_ m0,s50,22528
Mvaa 48,20,156
Mvaa 48,24,196
W_ 4
Mvaa 52,32,196
s_nop 0
Ta 20,v1,16392
Ta 22,v1,16904
Mvaa 52,28,156
L_ v4,24,s65,0
Mvaa 44,32,192
S_ s65,s60,31360
Mvaa 44,28,152
S_ m0,s50,23552
Ta 24,v1,20488
Ta 26,v1,21000
Mvaa 36,32,188
Mvaa 36,28,148
L_ v5,24,s65,0
Mvaa 28,32,184
S_ s65,s60,41600
Mvaa 28,28,144
S_ m0,s50,24576
Mvaa 20,32,180
s_waitcnt vmcnt(14) lgkmcnt(0)
s_barrier
s_cmp_eq_u32 s11, 1
s_cbranch_scc1 .Lpace9
.Lpace9_back:
Mvaa 20,28,140
L_ v4,24,s65,0
Mvaa 56,36,140
S_ s65,s59,3145728
Ta 28,%0,24576
Ta 30,%0,25088
Mvaa 56,224,180
S_ m0,s52,16256
Mvaa 64,36,144
Mvaa 64,224,184
s_nop 0
Ta 32,%0,28672
Ta 34,%0,29184
Mvaa 72,36,148
L_ v6,28,s65,128
Mvaa 72,224,188
S_ s65,s59,3244032
Mvaa 80,36,152
S_ m0,s52,17280
Mvaa 80,224,192
Mvaa 88,36,156
L_ v6,28,s65,128
Mvaa 88,224,196
S_ s65,s59,3342336
Mvaa 92,24,196
S_ m0,s52,18304
Ta 36,%0,24584
Ta 38,%0,25096
Mvaa 92,20,156
S_ s55,s56,5505024
Mvaa 84,24,192
L_ v6,28,s65,128
Mvaa 84,20,152
S_ s65,s59,3440640
Ta 224,%0,28680
Ta 226,%0,29192
Mvaa 76,24,188
S_ m0,s52,19328
Mvaa 76,20,148
Mvaa 68,24,184
L_ v6,28,s65,128
Mvaa 68,20,144
Mvaa 60,24,180
Mvaa 60,20,140
W_ 4
Mvaa 96,28,140
s_nop 0
Ta 20,v1,24576
Ta 22,v1,25088
Mvaa 96,32,180
buffer_load_dword a254, v10, s[28:31], s55 offen
Mvaa 104,28,144
Mvaa 104,32,184
s_nop 0
Ta 24,v1,28672
Ta 26,v1,29184
Mvaa 112,28,148
Mvaa 112,32,188
buffer_load_dword a254, v12, s[24:27], s63 offen offset:896
Mvaa 120,28,152
Mvaa 120,32,192
Mvaa 128,28,156
Mvaa 128,32,196
W_ 4
Mvaa 132,224,196
s_nop 0
Ta 28,v1,24584
Ta 30,v1,25096
Mvaa 132,36,156
Mvaa 124,224,192
Mvaa 124,36,152
s_nop 0
Ta 32,v1,28680
Ta 34,v1,29192
Mvaa 116,224,188
Mvaa 116,36,148
Mvaa 108,224,184
Mvaa 108,36,144
Mvaa 100,224,180
s_waitcnt vmcnt(15) lgkmcnt(0)
s_barrier
s_cmp_eq_u32 s11, 2
s_cbranch_scc1 .Lqsl10
.Lqsl10_back:
Mvaa 100,36,140
S_ s65,s59,3145728
Da 60,v2,0
Mvaa 136,20,140
S_ m0,s52,24320
Ta 36,%0,0
Ta 38,%0,512
Mvaa 136,24,180
L_ v6,28,s65,256
Mvaa 144,20,144
S_ s65,s59,3244032
Da 100,v3,0
Mvaa 144,24,184
S_ m0,s52,25344
Ta 224,%0,4096
Ta 226,%0,4608
Mvaa 152,20,148
Mvaa 152,24,188
Da 64,v2,2048
Mvaa 160,20,152
L_ v6,28,s65,256
Mvaa 160,24,192
S_ s65,s59,3342336
Mvaa 168,20,156
S_ m0,s52,26368
Da 104,v3,2048
Mvaa 168,24,196
Mvaa 172,32,196
s_nop 0
Ta 20,%0,8
Ta 22,%0,520
Mvaa 172,28,156
Da 68,v2,4096
Mvaa 164,32,192
L_ v6,28,s65,256
Mvaa 164,28,152
S_ s65,s59,3440640
Ta 24,%0,4104
Ta 26,%0,4616
Mvaa 156,32,188
S_ m0,s52,27392
Da 108,v3,4096
Mvaa 156,28,148
Mvaa 148,32,184
L_ v6,28,s65,256
Mvaa 148,28,144
Da 72,v2,6144
Mvaa 140,32,180
Mvaa 140,28,140
W_ 9
Mvaa 176,36,140
s_nop 0
Ta 28,v1,0
Ta 30,v1,512
Mvaa 176,224,180
Da 112,v3,6144
Mvaa 184,36,144
Mvaa 184,224,184
s_nop 0
Ta 32,v1,4096
Ta 34,v1,4608
Mvaa 192,36,148
Da 76,v2,8192
Mvaa 192,224,188
Mvaa 200,36,152
Da 116,v3,8192
Mvaa 200,224,192
Mvaa 208,36,156
Mvaa 208,224,196
W_ 9
Mvaa 212,24,196
s_nop 0
Ta 36,v1,8
Ta 38,v1,520
Mvaa 212,20,156
Mvaa 204,24,192
.Lpad_q2_3:
s_cbranch_execz .Lhop_q2_r
Mvaa 204,20,152
.Lpad_qx_1:
s_cbranch_execz .Lhop_qx_r
Ta 224,v1,4104
Ta 226,v1,4616
Mvaa 196,24,188
Mvaa 196,20,148
Mvaa 188,24,184
Mvaa 188,20,144
Mvaa 180,24,180
s_waitcnt vmcnt(15) lgkmcnt(0)
s_barrier
s_cmp_eq_u32 s11, 1
s_cbranch_scc1 .Lpace11
.Lpace11_back:
Mvaa 180,20,140
S_ s65,s59,3932160
Mvaa 216,28,140
S_ m0,s52,0
Ta 20,%0,8192
Ta 22,%0,8704
Mvaa 216,32,180
L_ v6,28,s65,0
Mvaa 224,28,144
S_ s65,s59,4030464
Mvaa 224,32,184
S_ m0,s52,1024
Ta 24,%0,12288
Ta 26,%0,12800
Mvaa 232,28,148
Mvaa 232,32,188
L_ v6,28,s65,0
Mvaa 240,28,152
S_ s65,s59,4128768
Mvaa 240,32,192
S_ m0,s52,2048
Mvaa 248,28,156
Mvaa 248,32,196
Mvaa 252,224,196
s_nop 0
Ta 28,%0,8200
Ta 30,%0,8712
Mvaa 252,36,156
L_ v6,28,s65,0
Mvaa 244,224,192
S_ s65,s59,4227072
Mvaa 244,36,152
S_ m0,s52,3072
Ta 32,%0,12296
Ta 34,%0,12808
Mvaa 236,224,188
Mvaa 236,36,148
L_ v6,28,s65,0
Mvaa 228,224,184
S_ s65,s60,768
Mvaa 228,36,144
S_ m0,s50,0
Mvaa 220,224,180
Mvaa 220,36,140
W_ 4
Mvaa 16,20,60
s_nop 0
Ta 36,v1,8192
Ta 38,v1,8704
Mvaa 16,24,100
L_ v4,24,s65,0
Mvaa 24,20,64
S_ s65,s60,11008
R_ v13,a248
Mvaa 24,24,104
S_ m0,s50,1024
Ta 224,v1,12288
Ta 226,v1,12800
Mvaa 32,20,68
v_min_u32 v13, 240, v13
Mvaa 32,24,108
L_ v5,24,s65,0
Mvaa 40,20,72
S_ s65,s60,21248
v_lshlrev_b32 v13, 3, v13
Mvaa 40,24,112
S_ m0,s50,2048
Mvaa 48,20,76
Mvaa 48,24,116
W_ 4
Mvaa 52,32,116
s_nop 0
Ta 20,v1,8200
Ta 22,v1,8712
Mvaa 52,28,76
L_ v4,24,s65,0
Mvaa 44,32,112
S_ s65,s60,31488
Mvaa 44,28,72
S_ m0,s50,3072
Ta 24,v1,12296
Ta 26,v1,12808
Mvaa 36,32,108
Mvaa 36,28,68
L_ v5,24,s65,0
Mvaa 28,32,104
S_ s65,s60,41728
Mvaa 28,28,64
S_ m0,s50,4096
Mvaa 20,32,100
s_waitcnt vmcnt(14) lgkmcnt(0)
s_barrier
s_cmp_eq_u32 s11, 2
s_cbranch_scc1 .Lqsl12
.Lqsl12_back:
Mvaa 20,28,60
L_ v4,24,s65,0
Mvaa 56,36,60
S_ s65,s59,3932160
Ta 28,%0,16384
Ta 30,%0,16896
Mvaa 56,224,100
S_ m0,s52,8064
Mvaa 64,36,64
Mvaa 64,224,104
s_nop 0
Ta 32,%0,20480
Ta 34,%0,20992
Mvaa 72,36,68
L_ v6,28,s65,128
Mvaa 72,224,108
S_ s65,s59,4030464
Mvaa 80,36,72
S_ m0,s52,9088
Mvaa 80,224,112
Mvaa 88,36,76
L_ v6,28,s65,128
Mvaa 88,224,116
S_ s65,s59,4128768
Mvaa 92,24,116
S_ m0,s52,10112
Ta 36,%0,16392
Ta 38,%0,16904
Mvaa 92,20,76
S_ s55,s56,6291456
Mvaa 84,24,112
L_ v6,28,s65,128
Mvaa 84,20,72
S_ s65,s59,4227072
Ta 224,%0,20488
Ta 226,%0,21000
Mvaa 76,24,108
S_ m0,s52,11136
Mvaa 76,20,68
Mvaa 68,24,104
L_ v6,28,s65,128
Mvaa 68,20,64
Mvaa 60,24,100
Mvaa 60,20,60
W_ 4
Mvaa 96,28,60
s_nop 0
Ta 20,v1,16384
Ta 22,v1,16896
Mvaa 96,32,100
buffer_load_dword a220, v13, s[44:47], s66 offen
Mvaa 104,28,64
Mvaa 104,32,104
s_nop 0
Ta 24,v1,20480
Ta 26,v1,20992
Mvaa 112,28,68
Mvaa 112,32,108
buffer_load_dword a254, v10, s[28:31], s55 offen
Mvaa 120,28,72
Mvaa 120,32,112
Mvaa 128,28,76
Mvaa 128,32,116
W_ 4
Mvaa 132,224,116
s_nop 0
Ta 28,v1,16392
Ta 30,v1,16904
Mvaa 132,36,76
buffer_load_dword a254, v12, s[24:27], s63 offen offset:1024
Mvaa 124,224,112
Mvaa 124,36,72
s_nop 0
Ta 32,v1,20488
Ta 34,v1,21000
Mvaa 116,224,108
Mvaa 116,36,68
Mvaa 108,224,104
Mvaa 108,36,64
Mvaa 100,224,100
s_waitcnt vmcnt(16) lgkmcnt(0)
s_barrier
s_cmp_eq_u32 s11, 1
s_cbranch_scc1 .Lpace13
.Lpace13_back:
Mvaa 100,36,60
S_ s65,s59,3932160
Da 140,v2,20480
Mvaa 136,20,60
S_ m0,s52,16128
Ta 36,%0,24576
Ta 38,%0,25088
Mvaa 136,24,100
L_ v6,28,s65,256
Mvaa 144,20,64
S_ s65,s59,4030464
Da 180,v3,20480
Mvaa 144,24,104
S_ m0,s52,17152
Ta 224,%0,28672
Ta 226,%0,29184
Mvaa 152,20,68
Mvaa 152,24,108
Da 144,v2,22528
Mvaa 160,20,72
L_ v6,28,s65,256
Mvaa 160,24,112
S_ s65,s59,4128768
Mvaa 168,20,76
S_ m0,s52,18176
Da 184,v3,22528
Mvaa 168,24,116
Mvaa 172,32,116
s_nop 0
Ta 20,%0,24584
Ta 22,%0,25096
Mvaa 172,28,76
Da 148,v2,24576
Mvaa 164,32,112
L_ v6,28,s65,256
Mvaa 164,28,72
S_ s65,s59,4227072
Ta 24,%0,28680
Ta 26,%0,29192
Mvaa 156,32,108
S_ m0,s52,19200
Da 188,v3,24576
Mvaa 156,28,68
Mvaa 148,32,104
L_ v6,28,s65,256
Mvaa 148,28,64
Da 152,v2,26624
Mvaa 140,32,100
Mvaa 140,28,60
W_ 9
Mvaa 176,36,60
s_nop 0
Ta 28,v1,24576
Ta 30,v1,25088
Mvaa 176,224,100
Da 192,v3,26624
Mvaa 184,36,64
Mvaa 184,224,104
s_nop 0
Ta 32,v1,28672
Ta 34,v1,29184
Mvaa 192,36,68
Da 156,v2,28672
Mvaa 192,224,108
Mvaa 200,36,72
Da 196,v3,28672
Mvaa 200,224,112
Mvaa 208,36,76
Mvaa 208,224,116
W_ 9
Mvaa 212,24,116
s_nop 0
Ta 36,v1,24584
Ta 38,v1,25096
Mvaa 212,20,76
Mvaa 204,24,112
Mvaa 204,20,72
s_nop 0
Ta 224,v1,28680
Ta 226,v1,29192
Mvaa 196,24,108
Mvaa 196,20,68
Mvaa 188,24,104
Mvaa 188,20,64
Mvaa 180,24,100
s_waitcnt vmcnt(16) lgkmcnt(0)
s_barrier
s_cmp_eq_u32 s11, 2
s_cbranch_scc1 .Lqsl14
.Lqsl14_back:
Mvaa 180,20,60
S_ s65,s59,4718592
Mvaa 216,28,60
S_ m0,s52,24576
Ta 20,%0,0
Ta 22,%0,512
Mvaa 216,32,100
L_ v6,28,s65,0
Mvaa 224,28,64
S_ s65,s59,4816896
Mvaa 224,32,104
S_ m0,s52,25600
Ta 24,%0,4096
Ta 26,%0,4608
Mvaa 232,28,68
Mvaa 232,32,108
L_ v6,28,s65,0
Mvaa 240,28,72
S_ s65,s59,4915200
Mvaa 240,32,112
S_ m0,s52,26624
Mvaa 248,28,76
Mvaa 248,32,116
Mvaa 252,224,116
s_nop 0
Ta 28,%0,8
Ta 30,%0,520
Mvaa 252,36,76
L_ v6,28,s65,0
Mvaa 244,224,112
S_ s65,s59,5013504
Mvaa 244,36,72
S_ m0,s52,27648
Ta 32,%0,4104
Ta 34,%0,4616
Mvaa 236,224,108
Mvaa 236,36,68
L_ v6,28,s65,0
Mvaa 228,224,104
S_ s65,s60,896
Mvaa 228,36,64
S_ m0,s50,20480
Mvaa 220,224,100
Mvaa 220,36,60
W_ 4
Mvaa 16,20,140
s_nop 0
Ta 36,v1,0
Ta 38,v1,512
Mvaa 16,24,180
L_ v4,24,s65,0
Mvaa 24,20,144
S_ s65,s60,11136
Mvaa 24,24,184
S_ m0,s50,21504
Ta 224,v1,4096
Ta 226,v1,4608
Mvaa 32,20,148
Mvaa 32,24,188
L_ v5,24,s65,0
Mvaa 40,20,152
S_ s65,s60,21376
Mvaa 40,24,192
S_ m0,s50,22528
Mvaa 48,20,156
Mvaa 48,24,196
W_ 4
Mvaa 52,32,196
s_nop 0
Ta 20,v1,8
Ta 22,v1,520
Mvaa 52,28,156
L_ v4,24,s65,0
Mvaa 44,32,192
S_ s65,s60,31616
Mvaa 44,28,152
S_ m0,s50,23552
Ta 24,v1,4104
Ta 26,v1,4616
Mvaa 36,32,188
Mvaa 36,28,148
L_ v5,24,s65,0
Mvaa 28,32,184
S_ s65,s60,41856
Mvaa 28,28,144
S_ m0,s50,24576
Mvaa 20,32,180
s_waitcnt vmcnt(15) lgkmcnt(0)
s_barrier
s_cmp_eq_u32 s11, 1
s_cbranch_scc1 .Lpace15
.Lpace15_back:
Mvaa 20,28,140
L_ v4,24,s65,0
Mvaa 56,36,140
S_ s65,s59,4718592
Ta 28,%0,8192
Ta 30,%0,8704
Mvaa 56,224,180
S_ m0,s52,-128
Mvaa 64,36,144
Mvaa 64,224,184
s_nop 0
Ta 32,%0,12288
Ta 34,%0,12800
Mvaa 72,36,148
L_ v6,28,s65,128
Mvaa 72,224,188
S_ s65,s59,4816896
Mvaa 80,36,152
S_ m0,s52,896
Mvaa 80,224,192
Mvaa 88,36,156
L_ v6,28,s65,128
Mvaa 88,224,196
S_ s65,s59,4915200
Mvaa 92,24,196
S_ m0,s52,1920
Ta 36,%0,8200
Ta 38,%0,8712
Mvaa 92,20,156
S_ s55,s56,7077888
Mvaa 84,24,192
L_ v6,28,s65,128
Mvaa 84,20,152
S_ s65,s59,5013504
Ta 224,%0,12296
Ta 226,%0,12808
Mvaa 76,24,188
S_ m0,s52,2944
Mvaa 76,20,148
Mvaa 68,24,184
L_ v6,28,s65,128
Mvaa 68,20,144
Mvaa 60,24,180
Mvaa 60,20,140
W_ 4
Mvaa 96,28,140
s_nop 0
Ta 20,v1,8192
Ta 22,v1,8704
Mvaa 96,32,180
buffer_load_dword a254, v10, s[28:31], s55 offen
Mvaa 104,28,144
Mvaa 104,32,184
s_nop 0
Ta 24,v1,12288
Ta 26,v1,12800
Mvaa 112,28,148
Mvaa 112,32,188
buffer_load_dword a254, v12, s[24:27], s63 offen offset:1152
Mvaa 120,28,152
Mvaa 120,32,192
Mvaa 128,28,156
Mvaa 128,32,196
W_ 4
Mvaa 132,224,196
s_nop 0
Ta 28,v1,8200
Ta 30,v1,8712
Mvaa 132,36,156
Mvaa 124,224,192
Mvaa 124,36,152
s_nop 0
Ta 32,v1,12296
Ta 34,v1,12808
Mvaa 116,224,188
Mvaa 116,36,148
Mvaa 108,224,184
Mvaa 108,36,144
Mvaa 100,224,180
s_waitcnt vmcnt(15) lgkmcnt(0)
s_barrier
Mvaa 100,36,140
S_ s65,s59,4718592
Da 60,v2,0
Mvaa 136,20,140
S_ m0,s52,7936
Ta 36,%0,16384
Ta 38,%0,16896
Mvaa 136,24,180
L_ v6,28,s65,256
Mvaa 144,20,144
S_ s65,s59,4816896
Da 100,v3,0
Mvaa 144,24,184
S_ m0,s52,8960
Ta 224,%0,20480
Ta 226,%0,20992
Mvaa 152,20,148
Mvaa 152,24,188
Da 64,v2,2048
Mvaa 160,20,152
L_ v6,28,s65,256
Mvaa 160,24,192
S_ s65,s59,4915200
Mvaa 168,20,156
S_ m0,s52,9984
Da 104,v3,2048
Mvaa 168,24,196
Mvaa 172,32,196
s_nop 0
Ta 20,%0,16392
Ta 22,%0,16904
Mvaa 172,28,156
Da 68,v2,4096
Mvaa 164,32,192
L_ v6,28,s65,256
Mvaa 164,28,152
S_ s65,s59,5013504
Ta 24,%0,20488
Ta 26,%0,21000
Mvaa 156,32,188
S_ m0,s52,11008
Da 108,v3,4096
Mvaa 156,28,148
Mvaa 148,32,184
L_ v6,28,s65,256
Mvaa 148,28,144
Da 72,v2,6144
Mvaa 140,32,180
Mvaa 140,28,140
W_ 9
Mvaa 176,36,140
s_nop 0
Ta 28,v1,16384
Ta 30,v1,16896
Mvaa 176,224,180
Da 112,v3,6144
Mvaa 184,36,144
Mvaa 184,224,184
s_nop 0
Ta 32,v1,20480
Ta 34,v1,20992
Mvaa 192,36,148
Da 76,v2,8192
Mvaa 192,224,188
Mvaa 200,36,152
Da 116,v3,8192
Mvaa 200,224,192
Mvaa 208,36,156
Mvaa 208,224,196
W_ 9
Mvaa 212,24,196
s_nop 0
Ta 36,v1,16392
Ta 38,v1,16904
Mvaa 212,20,156
Mvaa 204,24,192
Mvaa 204,20,152
s_nop 0
Ta 224,v1,20488
Ta 226,v1,21000
Mvaa 196,24,188
Mvaa 196,20,148
Mvaa 188,24,184
Mvaa 188,20,144
Mvaa 180,24,180
s_waitcnt vmcnt(15) lgkmcnt(0)
s_barrier
s_cmp_eq_u32 s11, 1
s_cbranch_scc1 .Lpace17
.Lpace17_back:
Mvaa 180,20,140
S_ s65,s59,5505024
Mvaa 216,28,140
S_ m0,s52,16384
Ta 20,%0,24576
Ta 22,%0,25088
Mvaa 216,32,180
L_ v6,28,s65,0
Mvaa 224,28,144
S_ s65,s59,5603328
Mvaa 224,32,184
.Lpad_q2_4:
s_cbranch_execz .Lhop_q2_r
.Lpad_qx_2:
s_cbranch_execz .Lhop_qx_r
S_ m0,s52,17408
Ta 24,%0,28672
Ta 26,%0,29184
Mvaa 232,28,148
Mvaa 232,32,188
L_ v6,28,s65,0
Mvaa 240,28,152
S_ s65,s59,5701632
Mvaa 240,32,192
S_ m0,s52,18432
Mvaa 248,28,156
Mvaa 248,32,196
Mvaa 252,224,196
s_nop 0
Ta 28,%0,24584
Ta 30,%0,25096
Mvaa 252,36,156
L_ v6,28,s65,0
Mvaa 244,224,192
S_ s65,s59,5799936
Mvaa 244,36,152
S_ m0,s52,19456
Ta 32,%0,28680
Ta 34,%0,29192
Mvaa 236,224,188
Mvaa 236,36,148
L_ v6,28,s65,0
Mvaa 228,224,184
S_ s65,s60,1024
Mvaa 228,36,144
S_ m0,s50,0
Mvaa 220,224,180
Mvaa 220,36,140
W_ 4
Mvaa 16,20,60
s_nop 0
Ta 36,v1,24576
Ta 38,v1,25088
Mvaa 16,24,100
L_ v4,24,s65,0
Mvaa 24,20,64
S_ s65,s60,11264
Mvaa 24,24,104
S_ m0,s50,1024
Ta 224,v1,28672
Ta 226,v1,29184
Mvaa 32,20,68
Mvaa 32,24,108
L_ v5,24,s65,0
Mvaa 40,20,72
S_ s65,s60,21504
Mvaa 40,24,112
S_ m0,s50,2048
Mvaa 48,20,76
Mvaa 48,24,116
W_ 4
Mvaa 52,32,116
s_nop 0
Ta 20,v1,24584
Ta 22,v1,25096
Mvaa 52,28,76
L_ v4,24,s65,0
Mvaa 44,32,112
S_ s65,s60,31744
Mvaa 44,28,72
S_ m0,s50,3072
Ta 24,v1,28680
Ta 26,v1,29192
Mvaa 36,32,108
Mvaa 36,28,68
L_ v5,24,s65,0
Mvaa 28,32,104
S_ s65,s60,41984
Mvaa 28,28,64
S_ m0,s50,4096
Mvaa 20,32,100
s_waitcnt vmcnt(14) lgkmcnt(0)
s_barrier
Mvaa 20,28,60
L_ v4,24,s65,0
Mvaa 56,36,60
S_ s65,s59,5505024
Ta 28,%0,0
Ta 30,%0,512
Mvaa 56,224,100
S_ m0,s52,24448
Mvaa 64,36,64
Mvaa 64,224,104
s_nop 0
Ta 32,%0,4096
Ta 34,%0,4608
Mvaa 72,36,68
L_ v6,28,s65,128
Mvaa 72,224,108
S_ s65,s59,5603328
Mvaa 80,36,72
S_ m0,s52,25472
Mvaa 80,224,112
Mvaa 88,36,76
L_ v6,28,s65,128
Mvaa 88,224,116
S_ s65,s59,5701632
Mvaa 92,24,116
S_ m0,s52,26496
Ta 36,%0,8
Ta 38,%0,520
Mvaa 92,20,76
Mvaa 84,24,112
L_ v6,28,s65,128
Mvaa 84,20,72
S_ s65,s59,5799936
Ta 224,%0,4104
Ta 226,%0,4616
Mvaa 76,24,108
S_ m0,s52,27520
Mvaa 76,20,68
Mvaa 68,24,104
L_ v6,28,s65,128
Mvaa 68,20,64
S_ s65,s61,0
Mvaa 60,24,100
S_ m0,s51,36864
Mvaa 60,20,60
W_ 4
Mvaa 96,28,60
s_nop 0
Ta 20,v1,0
Ta 22,v1,512
Mvaa 96,32,100
Ln v7,32,s65,0
Mvaa 104,28,64
Mvaa 104,32,104
s_nop 0
Ta 24,v1,4096
Ta 26,v1,4608
Mvaa 112,28,68
Mvaa 112,32,108
Ln v8,32,s65,3072
Mvaa 120,28,72
S_ m0,s51,37760
Mvaa 120,32,112
Mvaa 128,28,76
Mvaa 128,32,116
W_ 4
Mvaa 132,224,116
s_nop 0
Ta 28,v1,8
Ta 30,v1,520
Mvaa 132,36,76
Ln v7,32,s65,128
Mvaa 124,224,112
Mvaa 124,36,72
s_nop 0
Ta 32,v1,4104
Ta 34,v1,4616
Mvaa 116,224,108
Mvaa 116,36,68
Ln v8,32,s65,3200
Mvaa 108,224,104
S_ m0,s51,38656
Mvaa 108,36,64
Mvaa 100,224,100
s_waitcnt vmcnt(17) lgkmcnt(0)
s_barrier
s_cmp_eq_u32 s11, 1
s_cbranch_scc1 .Lpace19
.Lpace19_back:
Mvaa 100,36,60
Da 140,v2,20480
Mvaa 136,20,60
s_nop 0
Ta 36,%0,8192
Ta 38,%0,8704
Mvaa 136,24,100
Ln v7,32,s65,256
Mvaa 144,20,64
Da 180,v3,20480
Mvaa 144,24,104
s_nop 0
Ta 224,%0,12288
Ta 226,%0,12800
Mvaa 152,20,68
Mvaa 152,24,108
Da 144,v2,22528
Mvaa 160,20,72
Ln v8,32,s65,3328
Mvaa 160,24,112
S_ s65,s59,5505024
Mvaa 168,20,76
S_ m0,s52,-256
Da 184,v3,22528
Mvaa 168,24,116
Mvaa 172,32,116
s_nop 0
Ta 20,%0,8200
Ta 22,%0,8712
Mvaa 172,28,76
Da 148,v2,24576
Mvaa 164,32,112
L_ v6,28,s65,256
Mvaa 164,28,72
S_ s65,s59,5603328
Ta 24,%0,12296
Ta 26,%0,12808
Mvaa 156,32,108
S_ m0,s52,768
Da 188,v3,24576
Mvaa 156,28,68
Mvaa 148,32,104
L_ v6,28,s65,256
Mvaa 148,28,64
S_ s65,s59,5701632
Da 152,v2,26624
Mvaa 140,32,100
S_ m0,s52,1792
Mvaa 140,28,60
W_ 9
Mvaa 176,36,60
s_nop 0
Ta 28,v1,8192
Ta 30,v1,8704
Mvaa 176,224,100
Da 192,v3,26624
Mvaa 184,36,64
L_ v6,28,s65,256
Mvaa 184,224,104
S_ s65,s59,5799936
Ta 32,v1,12288
Ta 34,v1,12800
Mvaa 192,36,68
S_ m0,s52,2816
Da 156,v2,28672
Mvaa 192,224,108
Mvaa 200,36,72
Da 196,v3,28672
Mvaa 200,224,112
L_ v6,28,s65,256
Mvaa 208,36,76
Mvaa 208,224,116
W_ 9
Mvaa 212,24,116
s_nop 0
Ta 36,v1,8200
Ta 38,v1,8712
Mvaa 212,20,76
Mvaa 204,24,112
Mvaa 204,20,72
s_nop 0
Ta 224,v1,12296
Ta 226,v1,12808
Mvaa 196,24,108
Mvaa 196,20,68
Mvaa 188,24,104
Mvaa 188,20,64
Mvaa 180,24,100
s_waitcnt vmcnt(19) lgkmcnt(0)
s_barrier
Mvaa 180,20,60
S_ s65,s59,6291456
Mvaa 216,28,60
S_ m0,s52,7936
Ta 20,%0,16384
Ta 22,%0,16896
Mvaa 216,32,100
L_ v6,28,s65,256
Mvaa 224,28,64
S_ s65,s59,6389760
Mvaa 224,32,104
S_ m0,s52,8960
Ta 24,%0,20480
Ta 26,%0,20992
Mvaa 232,28,68
Mvaa 232,32,108
L_ v6,28,s65,256
Mvaa 240,28,72
S_ s65,s59,6488064
Mvaa 240,32,112
S_ m0,s52,9984
Mvaa 248,28,76
Mvaa 248,32,116
Mvaa 252,224,116
s_nop 0
Ta 28,%0,16392
Ta 30,%0,16904
Mvaa 252,36,76
L_ v6,28,s65,256
Mvaa 244,224,112
S_ s65,s59,6586368
Mvaa 244,36,72
S_ m0,s52,11008
Ta 32,%0,20488
Ta 34,%0,21000
Mvaa 236,224,108
Mvaa 236,36,68
L_ v6,28,s65,256
Mvaa 228,224,104
S_ s65,s60,1152
Mvaa 228,36,64
S_ m0,s50,20480
Mvaa 220,224,100
Mvaa 220,36,60
W_ 4
Mvaa 16,20,140
s_nop 0
Ta 36,v1,16384
Ta 38,v1,16896
Mvaa 16,24,180
L_ v4,24,s65,0
Mvaa 24,20,144
S_ s65,s60,11392
Mvaa 24,24,184
S_ m0,s50,21504
Ta 224,v1,20480
Ta 226,v1,20992
Mvaa 32,20,148
Mvaa 32,24,188
L_ v5,24,s65,0
Mvaa 40,20,152
S_ s65,s60,21632
Mvaa 40,24,192
S_ m0,s50,22528
Mvaa 48,20,156
Mvaa 48,24,196
W_ 4
Mvaa 52,32,196
s_nop 0
Ta 20,v1,16392
Ta 22,v1,16904
Mvaa 52,28,156
L_ v4,24,s65,0
Mvaa 44,32,192
S_ s65,s60,31872
Mvaa 44,28,152
S_ m0,s50,23552
Ta 24,v1,20488
Ta 26,v1,21000
Mvaa 36,32,188
Mvaa 36,28,148
L_ v5,24,s65,0
Mvaa 28,32,184
S_ s65,s60,42112
Mvaa 28,28,144
S_ m0,s50,24576
Mvaa 20,32,180
s_waitcnt vmcnt(18) lgkmcnt(0)
s_barrier
s_cmp_eq_u32 s11, 1
s_cbranch_scc1 .Lpace21
.Lpace21_back:
Mvaa 20,28,140
L_ v4,24,s65,0
Mvaa 56,36,140
S_ s65,s59,7077888
Ta 28,%0,24576
Ta 30,%0,25088
Mvaa 56,224,180
S_ m0,s52,16128
Mvaa 64,36,144
Mvaa 64,224,184
s_nop 0
Ta 32,%0,28672
Ta 34,%0,29184
Mvaa 72,36,148
L_ v6,28,s65,256
Mvaa 72,224,188
S_ s65,s59,7176192
Mvaa 80,36,152
S_ m0,s52,17152
Mvaa 80,224,192
Mvaa 88,36,156
L_ v6,28,s65,256
Mvaa 88,224,196
S_ s65,s59,7274496
Mvaa 92,24,196
S_ m0,s52,18176
Ta 36,%0,24584
Ta 38,%0,25096
Mvaa 92,20,156
Mvaa 84,24,192
L_ v6,28,s65,256
Mvaa 84,20,152
S_ s65,s59,7372800
Ta 224,%0,28680
Ta 226,%0,29192
Mvaa 76,24,188
S_ m0,s52,19200
Mvaa 76,20,148
Mvaa 68,24,184
L_ v6,28,s65,256
Mvaa 68,20,144
Mvaa 60,24,180
Mvaa 60,20,140
W_ 4
Mvaa 96,28,140
s_nop 0
Ta 20,v1,24576
Ta 22,v1,25088
Mvaa 96,32,180
Mvaa 104,28,144
Mvaa 104,32,184
s_nop 0
Ta 24,v1,28672
Ta 26,v1,29184
Mvaa 112,28,148
Mvaa 112,32,188
Mvaa 120,28,152
Mvaa 120,32,192
Mvaa 128,28,156
Mvaa 128,32,196
W_ 4
Mvaa 132,224,196
s_nop 0
Ta 28,v1,24584
Ta 30,v1,25096
Mvaa 132,36,156
Mvaa 124,224,192
Mvaa 124,36,152
s_nop 0
Ta 32,v1,28680
Ta 34,v1,29192
Mvaa 116,224,188
Mvaa 116,36,148
Mvaa 108,224,184
Mvaa 108,36,144
Mvaa 100,224,180
s_waitcnt vmcnt(13) lgkmcnt(0)
s_barrier
Mvaa 100,36,140
S_ s65,s59,6291456
Da 60,v2,0
Mvaa 136,20,140
S_ m0,s52,24448
Ta 36,%0,0
Ta 38,%0,512
Mvaa 136,24,180
L_ v6,28,s65,128
Mvaa 144,20,144
S_ s65,s59,6389760
Da 100,v3,0
Mvaa 144,24,184
S_ m0,s52,25472
Ta 224,%0,4096
Ta 226,%0,4608
Mvaa 152,20,148
Mvaa 152,24,188
Da 64,v2,2048
Mvaa 160,20,152
L_ v6,28,s65,128
Mvaa 160,24,192
S_ s65,s59,6488064
Mvaa 168,20,156
S_ m0,s52,26496
Da 104,v3,2048
Mvaa 168,24,196
Mvaa 172,32,196
s_nop 0
Ta 20,%0,8
Ta 22,%0,520
Mvaa 172,28,156
Da 68,v2,4096
Mvaa 164,32,192
L_ v6,28,s65,128
Mvaa 164,28,152
S_ s65,s59,6586368
Ta 24,%0,4104
Ta 26,%0,4616
Mvaa 156,32,188
S_ m0,s52,27520
Da 108,v3,4096
Mvaa 156,28,148
Mvaa 148,32,184
L_ v6,28,s65,128
Mvaa 148,28,144
S_ s65,s61,12288
Da 72,v2,6144
Mvaa 140,32,180
S_ m0,s51,49152
Mvaa 140,28,140
W_ 9
Mvaa 176,36,140
s_nop 0
Ta 28,v1,0
Ta 30,v1,512
Mvaa 176,224,180
Da 112,v3,6144
Mvaa 184,36,144
Ln v7,32,s65,0
Mvaa 184,224,184
s_nop 0
Ta 32,v1,4096
Ta 34,v1,4608
Mvaa 192,36,148
Da 76,v2,8192
Mvaa 192,224,188
Mvaa 200,36,152
Da 116,v3,8192
Mvaa 200,224,192
Ln v8,32,s65,3072
Mvaa 208,36,156
S_ m0,s51,50048
Mvaa 208,224,196
W_ 9
Mvaa 212,24,196
s_nop 0
Ta 36,v1,8
Ta 38,v1,520
Mvaa 212,20,156
Ln v7,32,s65,128
Mvaa 204,24,192
Mvaa 204,20,152
s_nop 0
Ta 224,v1,4104
Ta 226,v1,4616
Mvaa 196,24,188
Mvaa 196,20,148
Ln v8,32,s65,3200
Mvaa 188,24,184
S_ m0,s51,50944
Mvaa 188,20,144
Mvaa 180,24,180
s_waitcnt vmcnt(12) lgkmcnt(0)
s_barrier
s_cmp_eq_u32 s11, 1
s_cbranch_scc1 .Lpace23
.Lpace23_back:
Mvaa 180,20,140
Ln v7,32,s65,256
Mvaa 216,28,140
s_nop 0
Ta 20,v1,8200
Ta 22,v1,8712
Mvaa 216,32,180
Mvaa 224,28,144
Mvaa 224,32,184
s_nop 0
Ta 24,v1,12296
Ta 26,v1,12808
Mvaa 232,28,148
Ln v8,32,s65,3328
Mvaa 232,32,188
S_ s65,s59,7077888
Mvaa 240,28,152
S_ m0,s52,-128
Mvaa 240,32,192
.Lpad_q2_5:
s_cbranch_execz .Lhop_q2_r
Mvaa 248,28,156
.Lpad_qx_3:
s_cbranch_execz .Lhop_qx_r
L_ v6,28,s65,128
Mvaa 248,32,196
S_ s65,s59,7176192
Mvaa 252,224,196
S_ m0,s52,896
Ta 28,v1,8192
Ta 30,v1,8704
Mvaa 252,36,156
Mvaa 244,224,192
L_ v6,28,s65,128
Mvaa 244,36,152
S_ s65,s59,7274496
Ta 32,v1,12288
Ta 34,v1,12800
Mvaa 236,224,188
S_ m0,s52,1920
Mvaa 236,36,148
Mvaa 228,224,184
L_ v6,28,s65,128
Mvaa 228,36,144
S_ s65,s59,7372800
Mvaa 220,224,180
S_ m0,s52,2944
Mvaa 220,36,140
W_ 4
Mvaa 220,20,60
s_nop 0
Ta 36,%0,8200
Ta 38,%0,8712
Mvaa 220,24,100
Da 140,v2,20480
Mvaa 228,20,64
L_ v6,28,s65,128
Mvaa 228,24,104
s_nop 0
Ta 224,%0,12296
Ta 226,%0,12808
Mvaa 236,20,68
Da 180,v3,20480
Mvaa 236,24,108
Mvaa 244,20,72
Da 144,v2,22528
Mvaa 244,24,112
Mvaa 252,20,76
Mvaa 252,24,116
W_ 7
Da 184,v3,22528
Mvaa 248,32,116
s_nop 0
Ta 20,%0,8192
Ta 22,%0,8704
Mvaa 248,28,76
Mvaa 240,32,112
Da 148,v2,24576
Mvaa 240,28,72
s_nop 0
Ta 24,%0,12288
Ta 26,%0,12800
Mvaa 232,32,108
Mvaa 232,28,68
Da 188,v3,24576
Mvaa 224,32,104
Mvaa 224,28,64
Mvaa 216,32,100
s_waitcnt vmcnt(14) lgkmcnt(0)
s_barrier
Da 152,v2,26624
Mvaa 216,28,60
S_ s65,s59,6291456
Mvaa 180,36,60
S_ m0,s52,8192
Ta 28,v1,16392
Ta 30,v1,16904
Mvaa 180,224,100
Da 192,v3,26624
Mvaa 188,36,64
L_ v6,28,s65,0
Mvaa 188,224,104
S_ s65,s59,6389760
Ta 32,v1,20488
Ta 34,v1,21000
Mvaa 196,36,68
S_ m0,s52,9216
Da 156,v2,28672
Mvaa 196,224,108
Mvaa 204,36,72
L_ v6,28,s65,0
Mvaa 204,224,112
S_ s65,s59,6488064
Da 196,v3,28672
Mvaa 212,36,76
S_ m0,s52,10240
Mvaa 212,224,116
Mvaa 208,24,116
s_nop 0
Ta 36,v1,16384
Ta 38,v1,16896
Mvaa 208,20,76
L_ v6,28,s65,0
Mvaa 200,24,112
S_ s65,s59,6586368
Mvaa 200,20,72
S_ m0,s52,11264
Ta 224,v1,20480
Ta 226,v1,20992
Mvaa 192,24,108
Mvaa 192,20,68
L_ v6,28,s65,0
Mvaa 184,24,104
Mvaa 184,20,64
Mvaa 176,24,100
Mvaa 176,20,60
W_ 6
Mvaa 220,28,140
s_nop 0
Ta 20,%0,16392
Ta 22,%0,16904
Mvaa 220,32,180
Mvaa 228,28,144
Mvaa 228,32,184
s_nop 0
Ta 24,%0,20488
Ta 26,%0,21000
Mvaa 236,28,148
Mvaa 236,32,188
Mvaa 244,28,152
Mvaa 244,32,192
W_ 9
Mvaa 252,28,156
W_ 8
Mvaa 252,32,196
W_ 4
Mvaa 248,224,196
s_nop 0
Ta 28,%0,16384
Ta 30,%0,16896
Mvaa 248,36,156
Mvaa 240,224,192
Mvaa 240,36,152
s_nop 0
Ta 32,%0,20480
Ta 34,%0,20992
Mvaa 232,224,188
Mvaa 232,36,148
Mvaa 224,224,184
Mvaa 224,36,144
Mvaa 216,224,180
s_cmp_eq_u32 s58, 0
s_cbranch_scc0 .Lhq_0
s_mov_b64 exec, 0
s_bitcmp1_b32 s4, 1
s_cbranch_scc1 .Lhop_a0_b
s_bitcmp1_b32 s4, 0
s_cbranch_scc1 .Lpad_a0_1
s_branch .Lpad_a0_0
.Lhop_a0_b:
s_bitcmp1_b32 s4, 0
s_cbranch_scc1 .Lpad_a0_3
s_branch .Lpad_a0_2
.Lhop_a0_r:
s_mov_b64 exec, -1
s_nop 1
.Lhq_0:
s_cmp_eq_u32 s58, 1
s_cbranch_scc0 .Lhq_1
s_mov_b64 exec, 0
s_bitcmp1_b32 s4, 1
s_cbranch_scc1 .Lhop_a1_b
s_bitcmp1_b32 s4, 0
s_cbranch_scc1 .Lpad_a1_1
s_branch .Lpad_a1_0
.Lhop_a1_b:
s_bitcmp1_b32 s4, 0
s_cbranch_scc1 .Lpad_a1_3
s_branch .Lpad_a1_2
.Lhop_a1_r:
s_mov_b64 exec, -1
s_nop 1
.Lhq_1:
s_cmp_eq_u32 s58, 2
s_cbranch_scc0 .Lhq_2
s_mov_b64 exec, 0
s_bitcmp1_b32 s4, 1
s_cbranch_scc1 .Lhop_a2_b
s_bitcmp1_b32 s4, 0
s_cbranch_scc1 .Lpad_a2_1
s_branch .Lpad_a2_0
.Lhop_a2_b:
s_bitcmp1_b32 s4, 0
s_cbranch_scc1 .Lpad_a2_3
s_branch .Lpad_a2_2
.Lhop_a2_r:
s_mov_b64 exec, -1
s_nop 1
.Lhq_2:
s_waitcnt vmcnt(14) lgkmcnt(0)
s_barrier
s_cmp_eq_u32 s11, 1
s_cbranch_scc1 .Lpace25
.Lpace25_back:
Mvaa 216,36,140
S_ s65,s59,7077888
Mvaa 180,20,140
S_ m0,s52,16384
Ta 36,v1,24584
Ta 38,v1,25096
Mvaa 180,24,180
L_ v6,28,s65,0
Mvaa 188,20,144
S_ s65,s59,7176192
Mvaa 188,24,184
S_ m0,s52,17408
Ta 224,v1,28680
Ta 226,v1,29192
Mvaa 196,20,148
U_ v248,s48,v248
U_ v249,s48,v249
Mvaa 196,24,188
L_ v6,28,s65,0
Mvaa 204,20,152
S_ s65,s59,7274496
U_ v250,s48,v250
U_ v251,s48,v251
Mvaa 204,24,192
S_ m0,s52,18432
U_ v252,s48,v252
U_ v253,s48,v253
Mvaa 212,20,156
U_ v254,s48,v254
U_ v255,s48,v255
Mvaa 212,24,196
C_ 255,254,255
C_ 254,252,253
Mvaa 208,32,196
s_nop 0
Ta 20,v1,24576
Ta 22,v1,25088
Mvaa 208,28,156
L_ v6,28,s65,0
Mvaa 200,32,192
S_ s65,s59,7372800
C_ 253,250,251
C_ 252,248,249
Mvaa 200,28,152
S_ m0,s52,19456
Ta 24,v1,28672
Ta 26,v1,29184
Mvaa 192,32,188
U_ v240,s48,v240
U_ v241,s48,v241
Mvaa 192,28,148
L_ v6,28,s65,0
)A"
R"A(Mvaa 184,32,184
U_ v242,s48,v242
U_ v243,s48,v243
Mvaa 184,28,144
U_ v244,s48,v244
U_ v245,s48,v245
Mvaa 176,32,180
U_ v246,s48,v246
U_ v247,s48,v247
Mvaa 176,28,140
W_ 4
C_ 251,246,247
C_ 250,244,245
Mvaa 140,36,60
s_nop 0
Ta 28,%0,24584
Ta 30,%0,25096
Mvaa 140,224,100
C_ 249,242,243
C_ 248,240,241
Mvaa 148,36,64
U_ v232,s48,v232
U_ v233,s48,v233
Mvaa 148,224,104
s_nop 0
Ta 32,%0,28680
Ta 34,%0,29192
Mvaa 156,36,68
U_ v234,s48,v234
U_ v235,s48,v235
Mvaa 156,224,108
U_ v236,s48,v236
U_ v237,s48,v237
Mvaa 164,36,72
U_ v238,s48,v238
U_ v239,s48,v239
Mvaa 164,224,112
C_ 247,238,239
C_ 246,236,237
Mvaa 172,36,76
C_ 245,234,235
C_ 244,232,233
Mvaa 172,224,116
W_ 4
U_ v224,s48,v224
U_ v225,s48,v225
Mvaa 168,24,116
s_nop 0
Ta 36,%0,24576
Ta 38,%0,25088
Mvaa 168,20,76
U_ v226,s48,v226
U_ v227,s48,v227
Mvaa 160,24,112
U_ v228,s48,v228
U_ v229,s48,v229
Mvaa 160,20,72
s_nop 0
Ta 224,%0,28672
Ta 226,%0,29184
Mvaa 152,24,108
U_ v230,s48,v230
U_ v231,s48,v231
Mvaa 152,20,68
C_ 243,230,231
C_ 242,228,229
Mvaa 144,24,104
C_ 241,226,227
C_ 240,224,225
Mvaa 144,20,64
U_ v216,s48,v216
U_ v217,s48,v217
Mvaa 136,24,100
s_waitcnt vmcnt(8) lgkmcnt(0)
s_barrier
U_ v218,s48,v218
Mvaa 136,20,60
S_ s65,s61,0
U_ v219,s48,v219
Mvaa 100,28,60
S_ m0,s51,24576
Ta 20,v1,8
Ta 22,v1,520
Mvaa 100,32,100
Ln v9,36,s65,0
Mvaa 108,28,64
U_ v220,s48,v220
U_ v221,s48,v221
Mvaa 108,32,104
s_nop 0
Ta 24,v1,4104
Ta 26,v1,4616
Mvaa 116,28,68
U_ v222,s48,v222
U_ v223,s48,v223
Mvaa 116,32,108
Ln v9,36,s65,3072
Mvaa 124,28,72
S_ m0,s51,25472
C_ 239,222,223
C_ 238,220,221
Mvaa 124,32,112
C_ 237,218,219
C_ 236,216,217
Mvaa 132,28,76
U_ v208,s48,v208
U_ v209,s48,v209
Mvaa 132,32,116
U_ v210,s48,v210
U_ v211,s48,v211
Mvaa 128,224,116
s_nop 0
Ta 28,v1,0
Ta 30,v1,512
Mvaa 128,36,76
Ln v9,36,s65,128
Mvaa 120,224,112
U_ v212,s48,v212
U_ v213,s48,v213
Mvaa 120,36,72
s_nop 0
Ta 32,v1,4096
Ta 34,v1,4608
Mvaa 112,224,108
U_ v214,s48,v214
U_ v215,s48,v215
Mvaa 112,36,68
Ln v9,36,s65,3200
Mvaa 104,224,104
S_ m0,s51,26368
C_ 235,214,215
C_ 234,212,213
Mvaa 104,36,64
C_ 233,210,211
C_ 232,208,209
Mvaa 96,224,100
U_ v200,s48,v200
U_ v201,s48,v201
Mvaa 96,36,60
W_ 4
U_ v202,s48,v202
U_ v203,s48,v203
Mvaa 140,20,140
s_nop 0
Ta 36,%0,8
Ta 38,%0,520
Mvaa 140,24,180
Ln v9,36,s65,256
Mvaa 148,20,144
U_ v204,s48,v204
U_ v205,s48,v205
Mvaa 148,24,184
s_nop 0
Ta 224,%0,4104
Ta 226,%0,4616
Mvaa 156,20,148
U_ v206,s48,v206
U_ v207,s48,v207
Mvaa 156,24,188
Ln v9,36,s65,3328
Mvaa 164,20,152
C_ 231,206,207
C_ 230,204,205
Mvaa 164,24,192
C_ 229,202,203
C_ 228,200,201
Mvaa 172,20,156
U_ v192,s48,v192
U_ v193,s48,v193
Mvaa 172,24,196
W_ 4
U_ v194,s48,v194
U_ v195,s48,v195
Mvaa 168,32,196
s_nop 0
Ta 20,%0,0
Ta 22,%0,512
Mvaa 168,28,156
U_ v196,s48,v196
U_ v197,s48,v197
Mvaa 160,32,192
U_ v198,s48,v198
U_ v199,s48,v199
Mvaa 160,28,152
s_nop 0
Ta 24,%0,4096
Ta 26,%0,4608
Mvaa 152,32,188
C_ 227,198,199
C_ 226,196,197
Mvaa 152,28,148
C_ 225,194,195
C_ 224,192,193
Mvaa 144,32,184
U_ v184,s48,v184
U_ v185,s48,v185
Mvaa 144,28,144
U_ v186,s48,v186
U_ v187,s48,v187
Mvaa 136,32,180
s_waitcnt vmcnt(10) lgkmcnt(0)
s_barrier
s_cmp_eq_u32 s11, 1
s_cbranch_scc1 .Lpace27
.Lpace27_back:
U_ v188,s48,v188
Mvaa 136,28,140
U_ v189,s48,v189
Mvaa 100,36,140
s_nop 0
Ta 28,v1,8200
Ta 30,v1,8712
Mvaa 100,224,180
U_ v190,s48,v190
U_ v191,s48,v191
Mvaa 108,36,144
C_ 223,190,191
C_ 222,188,189
Mvaa 108,224,184
s_nop 0
Ta 32,v1,12296
Ta 34,v1,12808
Mvaa 116,36,148
C_ 221,186,187
C_ 220,184,185
Mvaa 116,224,188
U_ v176,s48,v176
U_ v177,s48,v177
Mvaa 124,36,152
U_ v178,s48,v178
U_ v179,s48,v179
Mvaa 124,224,192
U_ v180,s48,v180
U_ v181,s48,v181
Mvaa 132,36,156
U_ v182,s48,v182
U_ v183,s48,v183
Mvaa 132,224,196
C_ 219,182,183
C_ 218,180,181
Mvaa 128,24,196
s_nop 0
Ta 36,v1,8192
Ta 38,v1,8704
Mvaa 128,20,156
C_ 217,178,179
C_ 216,176,177
Mvaa 120,24,192
U_ v168,s48,v168
U_ v169,s48,v169
Mvaa 120,20,152
s_nop 0
Ta 224,v1,12288
Ta 226,v1,12800
Mvaa 112,24,188
U_ v170,s48,v170
U_ v171,s48,v171
Mvaa 112,20,148
U_ v172,s48,v172
U_ v173,s48,v173
Mvaa 104,24,184
U_ v174,s48,v174
U_ v175,s48,v175
Mvaa 104,20,144
C_ 215,174,175
C_ 214,172,173
Mvaa 96,24,180
C_ 213,170,171
C_ 212,168,169
Mvaa 96,20,140
W_ 4
U_ v160,s48,v160
U_ v161,s48,v161
Mvaa 60,28,60
s_nop 0
Ta 20,%0,8200
Ta 22,%0,8712
Mvaa 60,32,100
U_ v162,s48,v162
U_ v163,s48,v163
Mvaa 68,28,64
U_ v164,s48,v164
U_ v165,s48,v165
Mvaa 68,32,104
s_nop 0
Ta 24,%0,12296
Ta 26,%0,12808
Mvaa 76,28,68
U_ v166,s48,v166
U_ v167,s48,v167
Mvaa 76,32,108
C_ 211,166,167
C_ 210,164,165
Mvaa 84,28,72
C_ 209,162,163
C_ 208,160,161
Mvaa 84,32,112
U_ v152,s48,v152
U_ v153,s48,v153
Mvaa 92,28,76
U_ v154,s48,v154
U_ v155,s48,v155
Mvaa 92,32,116
W_ 4
U_ v156,s48,v156
U_ v157,s48,v157
Mvaa 88,224,116
s_nop 0
Ta 28,%0,8192
Ta 30,%0,8704
Mvaa 88,36,76
U_ v158,s48,v158
U_ v159,s48,v159
Mvaa 80,224,112
C_ 207,158,159
C_ 206,156,157
Mvaa 80,36,72
s_nop 0
Ta 32,%0,12288
Ta 34,%0,12800
Mvaa 72,224,108
C_ 205,154,155
C_ 204,152,153
Mvaa 72,36,68
U_ v144,s48,v144
U_ v145,s48,v145
Mvaa 64,224,104
U_ v146,s48,v146
U_ v147,s48,v147
Mvaa 64,36,64
U_ v148,s48,v148
U_ v149,s48,v149
Mvaa 56,224,100
s_waitcnt vmcnt(6) lgkmcnt(0)
s_barrier
U_ v150,s48,v150
Mvaa 56,36,60
S_ s65,s61,24576
U_ v151,s48,v151
Mvaa 20,20,60
S_ m0,s51,0
Ta 36,v1,16392
Ta 38,v1,16904
Mvaa 20,24,100
Ln v7,32,s65,0
Mvaa 28,20,64
C_ 203,150,151
C_ 202,148,149
Mvaa 28,24,104
s_nop 0
Ta 224,v1,20488
Ta 226,v1,21000
Mvaa 36,20,68
C_ 201,146,147
C_ 200,144,145
Mvaa 36,24,108
Ln v8,32,s65,3072
Mvaa 44,20,72
S_ m0,s51,896
U_ v136,s48,v136
U_ v137,s48,v137
Mvaa 44,24,112
U_ v138,s48,v138
U_ v139,s48,v139
Mvaa 52,20,76
U_ v140,s48,v140
U_ v141,s48,v141
Mvaa 52,24,116
U_ v142,s48,v142
U_ v143,s48,v143
Mvaa 48,32,116
s_nop 0
Ta 20,v1,16384
Ta 22,v1,16896
Mvaa 48,28,76
Ln v7,32,s65,128
Mvaa 40,32,112
.Lpad_q2_6:
s_cbranch_execz .Lhop_q2_r
C_ 199,142,143
C_ 198,140,141
.Lpad_qx_4:
s_cbranch_execz .Lhop_qx_r
Mvaa 40,28,72
s_nop 0
Ta 24,v1,20480
Ta 26,v1,20992
Mvaa 32,32,108
C_ 197,138,139
C_ 196,136,137
Mvaa 32,28,68
Ln v8,32,s65,3200
Mvaa 24,32,104
S_ m0,s51,1792
U_ v128,s48,v128
U_ v129,s48,v129
Mvaa 24,28,64
U_ v130,s48,v130
U_ v131,s48,v131
Mvaa 16,32,100
U_ v132,s48,v132
U_ v133,s48,v133
Mvaa 16,28,60
W_ 4
U_ v134,s48,v134
U_ v135,s48,v135
Mvaa 60,36,140
s_nop 0
Ta 28,%0,16392
Ta 30,%0,16904
Mvaa 60,224,180
Ln v7,32,s65,256
Mvaa 68,36,144
C_ 195,134,135
C_ 194,132,133
Mvaa 68,224,184
s_nop 0
Ta 32,%0,20488
Ta 34,%0,21000
Mvaa 76,36,148
C_ 193,130,131
C_ 192,128,129
Mvaa 76,224,188
Ln v8,32,s65,3328
Mvaa 84,36,152
U_ v120,s48,v120
U_ v121,s48,v121
Mvaa 84,224,192
U_ v122,s48,v122
U_ v123,s48,v123
Mvaa 92,36,156
U_ v124,s48,v124
U_ v125,s48,v125
Mvaa 92,224,196
W_ 4
U_ v126,s48,v126
U_ v127,s48,v127
Mvaa 88,24,196
s_nop 0
Ta 36,%0,16384
Ta 38,%0,16896
Mvaa 88,20,156
C_ 191,126,127
C_ 190,124,125
Mvaa 80,24,192
C_ 189,122,123
C_ 188,120,121
Mvaa 80,20,152
s_nop 0
Ta 224,%0,20480
Ta 226,%0,20992
Mvaa 72,24,188
U_ v112,s48,v112
U_ v113,s48,v113
Mvaa 72,20,148
U_ v114,s48,v114
U_ v115,s48,v115
Mvaa 64,24,184
U_ v116,s48,v116
U_ v117,s48,v117
Mvaa 64,20,144
U_ v118,s48,v118
U_ v119,s48,v119
Mvaa 56,24,180
s_waitcnt vmcnt(51) lgkmcnt(0)
s_barrier
s_cmp_eq_u32 s11, 1
s_cbranch_scc1 .Lpace29
.Lpace29_back:
C_ 187,118,119
Mvaa 56,20,140
S_ s65,s61,12288
C_ 186,116,117
Mvaa 20,28,140
S_ m0,s51,12288
C_ 185,114,115
C_ 184,112,113
Mvaa 20,32,180
Ln v9,36,s65,0
Mvaa 28,28,144
U_ v104,s48,v104
U_ v105,s48,v105
Mvaa 28,32,184
U_ v106,s48,v106
U_ v107,s48,v107
Mvaa 36,28,148
U_ v108,s48,v108
U_ v109,s48,v109
Mvaa 36,32,188
Ln v9,36,s65,3072
Mvaa 44,28,152
S_ m0,s51,13184
U_ v110,s48,v110
U_ v111,s48,v111
Mvaa 44,32,192
C_ 183,110,111
C_ 182,108,109
Mvaa 52,28,156
C_ 181,106,107
C_ 180,104,105
Mvaa 52,32,196
U_ v96,s48,v96
U_ v97,s48,v97
Mvaa 48,224,196
Ln v9,36,s65,128
Mvaa 48,36,156
U_ v98,s48,v98
U_ v99,s48,v99
Mvaa 40,224,192
U_ v100,s48,v100
U_ v101,s48,v101
Mvaa 40,36,152
U_ v102,s48,v102
U_ v103,s48,v103
Mvaa 32,224,188
Ln v9,36,s65,3200
Mvaa 32,36,148
S_ m0,s51,14080
C_ 179,102,103
C_ 178,100,101
Mvaa 24,224,184
C_ 177,98,99
C_ 176,96,97
Mvaa 24,36,144
v_pk_mul_f32 v[88:89], v[88:89], s[48:49]
v_pk_mul_f32 v[90:91], v[90:91], s[48:49]
Mvaa 16,224,180
Ln v9,36,s65,256
Mvaa 16,36,140
v_pk_mul_f32 v[92:93], v[92:93], s[48:49]
v_pk_mul_f32 v[94:95], v[94:95], s[48:49]
Ln v9,36,s65,3328
C_ 175,94,95
C_ 174,92,93
C_ 173,90,91
C_ 172,88,89
v_pk_mul_f32 v[80:81], v[80:81], s[48:49]
v_pk_mul_f32 v[82:83], v[82:83], s[48:49]
v_pk_mul_f32 v[84:85], v[84:85], s[48:49]
v_pk_mul_f32 v[86:87], v[86:87], s[48:49]
C_ 171,86,87
C_ 170,84,85
C_ 169,82,83
C_ 168,80,81
v_pk_mul_f32 v[72:73], v[72:73], s[48:49]
v_pk_mul_f32 v[74:75], v[74:75], s[48:49]
v_pk_mul_f32 v[76:77], v[76:77], s[48:49]
v_pk_mul_f32 v[78:79], v[78:79], s[48:49]
C_ 167,78,79
C_ 166,76,77
C_ 165,74,75
C_ 164,72,73
v_pk_mul_f32 v[40:41], v[40:41], s[48:49]
v_pk_mul_f32 v[42:43], v[42:43], s[48:49]
v_pk_mul_f32 v[44:45], v[44:45], s[48:49]
v_pk_mul_f32 v[46:47], v[46:47], s[48:49]
C_ 151,46,47
C_ 150,44,45
C_ 149,42,43
C_ 148,40,41
v_pk_mul_f32 v[32:33], v[32:33], s[48:49]
v_pk_mul_f32 v[34:35], v[34:35], s[48:49]
v_pk_mul_f32 v[36:37], v[36:37], s[48:49]
v_pk_mul_f32 v[38:39], v[38:39], s[48:49]
C_ 147,38,39
C_ 146,36,37
C_ 145,34,35
C_ 144,32,33
v_pk_mul_f32 v[24:25], v[24:25], s[48:49]
v_pk_mul_f32 v[26:27], v[26:27], s[48:49]
v_pk_mul_f32 v[28:29], v[28:29], s[48:49]
v_pk_mul_f32 v[30:31], v[30:31], s[48:49]
C_ 143,30,31
C_ 142,28,29
C_ 141,26,27
C_ 140,24,25
R_ %0,a244
v_xor_b32 v1, 64, %0
Dv 30,%0,43008
Dv 34,v1,43008
Dv 38,%0,44032
v_pk_mul_f32 v[64:65], v[64:65], s[48:49]
v_pk_mul_f32 v[66:67], v[66:67], s[48:49]
v_pk_mul_f32 v[68:69], v[68:69], s[48:49]
v_pk_mul_f32 v[70:71], v[70:71], s[48:49]
C_ 163,70,71
C_ 162,68,69
C_ 161,66,67
C_ 160,64,65
v_pk_mul_f32 v[56:57], v[56:57], s[48:49]
v_pk_mul_f32 v[58:59], v[58:59], s[48:49]
v_pk_mul_f32 v[60:61], v[60:61], s[48:49]
v_pk_mul_f32 v[62:63], v[62:63], s[48:49]
C_ 159,62,63
C_ 158,60,61
C_ 157,58,59
C_ 156,56,57
v_pk_mul_f32 v[48:49], v[48:49], s[48:49]
v_pk_mul_f32 v[50:51], v[50:51], s[48:49]
v_pk_mul_f32 v[52:53], v[52:53], s[48:49]
v_pk_mul_f32 v[54:55], v[54:55], s[48:49]
C_ 155,54,55
C_ 154,52,53
C_ 153,50,51
C_ 152,48,49
v_pk_mul_f32 v[16:17], v[16:17], s[48:49]
v_pk_mul_f32 v[18:19], v[18:19], s[48:49]
v_pk_mul_f32 v[20:21], v[20:21], s[48:49]
v_pk_mul_f32 v[22:23], v[22:23], s[48:49]
C_ 139,22,23
C_ 138,20,21
C_ 137,18,19
C_ 136,16,17
s_cmp_eq_u32 s58, 1
s_cbranch_scc1 .Latt1
s_cmp_eq_u32 s58, 2
s_cbranch_scc1 .Latt2
.Latt0:
s_cmp_eq_u32 s4, 0
s_cbranch_scc0 .Lhs_tok0
R_ v11,a241
buffer_load_dword a255, v11, s[92:95], 0 offen offset:0 sc1
.Lhs_tok0:
S_ s59,s59,768
W_ 2
Zvvv 96,30,136
Dv 42,v1,44032
R_ v2,a245
Zvvv 100,30,140
v_xor_b32 v3, 32, v2
v_xor_b32 v4, 64, v2
Zvvv 104,30,144
v_xor_b32 v5, 96, v2
Zvvv 108,30,148
s_cmp_eq_u32 s4, 0
s_cselect_b32 s67, 13, 0
Zvvv 112,30,152
W_ 2
s_or_b32 s67, s67, 8192
Mvvv 96,34,156
Dv 30,%0,45056
Mvvv 100,34,160
Mvvv 104,34,164
Mvvv 108,34,168
Mvvv 112,34,172
W_ 2
Mvvv 96,38,176
Dv 34,v1,45056
Mvvv 100,38,180
Mvvv 104,38,184
Mvvv 108,38,188
Mvvv 112,38,192
W_ 2
Mvvv 96,42,196
Dv 38,%0,36864
Mvvv 100,42,200
Mvvv 104,42,204
Mvvv 108,42,208
Mvvv 112,42,212
W_ 2
Mvvv 96,30,216
Dv 42,v1,36864
Mvvv 100,30,220
Mvvv 104,30,224
Mvvv 108,30,228
Mvvv 112,30,232
W_ 2
Mvvv 96,34,236
Dv 30,%0,37888
Mvvv 100,34,240
Mvvv 104,34,244
Mvvv 108,34,248
Mvvv 112,34,252
W_ 2
s_nop 1
v_max3_f32 v56, v96, v97, v98
v_max_f32 v56, v56, v99
Zvvv 76,38,136
s_memtime s[12:13]
Dv 34,v1,37888
v_max3_f32 v60, v100, v101, v102
Zvvv 80,38,140
s_memrealtime s[100:101]
v_max_f32 v60, v60, v103
Zvvv 84,38,144
v_max3_f32 v64, v104, v105, v106
v_max_f32 v64, v64, v107
Zvvv 88,38,148
v_max3_f32 v68, v108, v109, v110
v_max_f32 v68, v68, v111
Zvvv 92,38,152
W_ 2
v_max3_f32 v72, v112, v113, v114
v_max_f32 v72, v72, v115
Mvvv 76,42,156
Dv 38,%0,38912
Mvvv 80,42,160
Mvvv 84,42,164
Mvvv 88,42,168
Mvvv 92,42,172
W_ 2
Mvvv 76,30,176
Dv 42,v1,38912
Mvvv 80,30,180
Mvvv 84,30,184
Mvvv 88,30,188
Mvvv 92,30,192
s_waitcnt vmcnt(30) lgkmcnt(0)
s_barrier
Mvvv 76,34,196
S_ s65,s61,184320
Dv 30,%0,55296
Mvvv 76,38,216
S_ m0,s51,36864
s_sub_u32 s12, s12, s90
Mvvv 76,42,236
L_ v9,36,s65,0
Mvvv 80,34,200
s_sub_u32 s100, s100, s68
Mvvv 80,38,220
L_ v9,36,s65,3072
Mvvv 80,42,240
S_ m0,s51,37760
s_sub_u32 s101, s68, s98
Mvvv 84,34,204
L_ v9,36,s65,128
Mvvv 84,38,224
s_mul_i32 s12, s12, s101
v_max3_f32 v56, v56, v76, v77
v_max3_f32 v56, v56, v78, v79
Mvvv 84,42,244
L_ v9,36,s65,3200
Mvvv 88,34,208
S_ m0,s51,38656
v_max3_f32 v60, v60, v80, v81
s_sub_u32 s101, s90, s88
Mvvv 88,38,228
L_ v9,36,s65,256
Mvvv 88,42,248
s_mul_i32 s100, s100, s101
v_max3_f32 v60, v60, v82, v83
Mvvv 92,34,212
L_ v9,36,s65,3328
Mvvv 92,38,232
s_lshr_b32 s100, s100, 6
v_max3_f32 v64, v64, v84, v85
v_max3_f32 v64, v64, v86, v87
Mvvv 92,42,252
s_mul_i32 s100, s100, 47
s_cmp_gt_u32 s101, 4000
s_cselect_b32 s13, 2, 0
s_cmp_gt_u32 s12, s100
s_cselect_b32 s13, s13, 0
Dv 34,v1,55296
.p2align 3
v_mov_b32 v11, 0x3f803f80
v_permlane32_swap_b32 v56, v60
v_max3_f32 v68, v68, v88, v89
v_max3_f32 v68, v68, v90, v91
v_max_f32 v56, v56, v60
v_max3_f32 v72, v72, v92, v93
v_max3_f32 v72, v72, v94, v95
v_mov_b32 v73, v72
v_permlane32_swap_b32 v64, v68
v_max_f32 v64, v64, v68
v_permlane32_swap_b32 v72, v73
v_max_f32 v72, v72, v73
v_mov_b32 v73, v72
v_permlane16_swap_b32 v56, v64
v_max_f32 v56, v56, v64
v_permlane16_swap_b32 v72, v73
B_ v56,0xc0e00000,v56
v_max_f32 v72, v72, v73
v_mov_b32 v64, v56
B_ v72,0xc0e00000,v72
v_mov_b32 v73, v72
v_permlane16_swap_b32 v56, v64
v_mov_b32 v60, v56
v_mov_b32 v68, v64
v_mov_b64 v[74:75], v[72:73]
v_permlane32_swap_b32 v56, v60
v_permlane32_swap_b32 v64, v68
v_mov_b32 v57, v56
v_mov_b32 v61, v60
v_mov_b32 v65, v64
v_mov_b32 v69, v68
v_pk_add_f32 v[96:97], v[96:97], v[56:57]
v_pk_add_f32 v[98:99], v[98:99], v[56:57]
v_pk_add_f32 v[76:77], v[76:77], v[56:57]
v_pk_add_f32 v[78:79], v[78:79], v[56:57]
v_mov_b64 v[58:59], v[56:57]
v_pk_add_f32 v[100:101], v[100:101], v[60:61]
v_pk_add_f32 v[102:103], v[102:103], v[60:61]
v_pk_add_f32 v[80:81], v[80:81], v[60:61]
v_pk_add_f32 v[82:83], v[82:83], v[60:61]
v_mov_b64 v[62:63], v[60:61]
v_pk_add_f32 v[104:105], v[104:105], v[64:65]
v_pk_add_f32 v[106:107], v[106:107], v[64:65]
v_pk_add_f32 v[84:85], v[84:85], v[64:65]
v_pk_add_f32 v[86:87], v[86:87], v[64:65]
v_mov_b64 v[66:67], v[64:65]
v_pk_add_f32 v[108:109], v[108:109], v[68:69]
v_pk_add_f32 v[110:111], v[110:111], v[68:69]
v_pk_add_f32 v[88:89], v[88:89], v[68:69]
v_pk_add_f32 v[90:91], v[90:91], v[68:69]
v_mov_b64 v[70:71], v[68:69]
v_pk_add_f32 v[112:113], v[112:113], v[72:73]
v_pk_add_f32 v[114:115], v[114:115], v[72:73]
v_pk_add_f32 v[92:93], v[92:93], v[72:73]
v_pk_add_f32 v[94:95], v[94:95], v[72:73]
E_ 96
E_ 97
E_ 98
E_ 99
E_ 76
E_ 77
E_ 78
E_ 79
C_ 99,98,99
C_ 98,96,97
C_ 96,76,77
C_ 97,78,79
E_ 100
E_ 101
E_ 102
E_ 103
E_ 80
E_ 81
E_ 82
E_ 83
C_ 103,102,103
C_ 102,100,101
C_ 100,80,81
C_ 101,82,83
.Lstep0_1:
W_ 1
Nvvv 116,30,136,56
s_nop 0
Dv 38,%0,56320
Dv 42,v1,56320
W_ 2
Mvvv 116,34,156
E_ 104
Nvvv 120,30,140,60
E_ 105
Mvvv 120,34,160
E_ 106
Nvvv 124,30,144,64
E_ 107
Mvvv 124,34,164
E_ 84
Nvvv 128,30,148,68
E_ 85
Mvvv 128,34,168
E_ 86
Nvvv 132,30,152,72
E_ 87
Mvvv 132,34,172
W_ 0
C_ 107,106,107
C_ 106,104,105
Mvvv 132,38,192
s_nop 0
Dv 30,%0,57344
Dv 34,v1,57344
Mvvv 132,42,212
C_ 104,84,85
C_ 105,86,87
Mvvv 128,38,188
E_ 108
Mvvv 128,42,208
E_ 109
Mvvv 124,38,184
E_ 110
Mvvv 124,42,204
E_ 111
Mvvv 120,38,180
E_ 88
Mvvv 120,42,200
E_ 89
Mvvv 116,38,176
E_ 90
Mvvv 116,42,196
W_ 0
E_ 91
Mvvv 116,30,216
s_nop 0
Dv 38,%0,49152
Dv 42,v1,49152
Mvvv 116,34,236
C_ 111,110,111
C_ 110,108,109
Mvvv 120,30,220
C_ 108,88,89
C_ 109,90,91
Mvvv 120,34,240
E_ 112
Mvvv 124,30,224
E_ 113
Mvvv 124,34,244
E_ 114
Mvvv 128,30,228
E_ 115
Mvvv 128,34,248
E_ 92
Mvvv 132,30,232
E_ 93
Mvvv 132,34,252
W_ 0
E_ 94
Nvvv 76,38,136,56
s_nop 0
Dv 30,%0,50176
Dv 34,v1,50176
Mvvv 76,42,156
E_ 95
Nvvv 80,38,140,60
C_ 115,114,115
C_ 114,112,113
Mvvv 80,42,160
C_ 112,92,93
C_ 113,94,95
Nvvv 84,38,144,64
E_ 116
Mvvv 84,42,164
E_ 117
Nvvv 88,38,148,68
E_ 118
Mvvv 88,42,168
E_ 119
Nvvv 92,38,152,72
C_ 119,118,119
C_ 118,116,117
Mvvv 92,42,172
W_ 0
E_ 120
Mvvv 92,30,192
s_nop 0
Dv 38,%0,51200
Dv 42,v1,51200
Mvvv 92,34,212
X_ a180,v11
X_ a181,v11
Mvvv 88,30,188
X_ a182,v11
X_ a183,v11
Mvvv 88,34,208
E_ 121
Mvvv 84,30,184
E_ 122
Mvvv 84,34,204
E_ 123
Mvvv 80,30,180
C_ 123,122,123
C_ 122,120,121
Mvvv 80,34,200
E_ 124
Mvvv 76,30,176
E_ 125
Mvvv 76,34,196
S_ s65,s61,36864
E_ 126
W_ 1
Mvvv 76,38,216
S_ m0,s51,49152
E_ 127
W_ 0
Mvvv 76,42,236
s_waitcnt vmcnt(18) lgkmcnt(0)
s_barrier
C_ 127,126,127
Mvvv 80,38,220
Ln v7,32,s65,0
Mvvv 80,42,240
C_ 126,124,125
Mvvv 84,38,224
Ln v8,32,s65,3072
S_ m0,s51,50048
Mvvv 84,42,244
s_nop 0
Tv 30,v2,24576
Tv 32,v2,30720
Mvvv 88,38,228
Ln v7,32,s65,128
Mvvv 88,42,248
.Lpad_a0_0:
s_cbranch_execz .Lhop_a0_r
Tv 34,v3,24576
Tv 36,v3,30720
Mvvv 92,38,232
Ln v8,32,s65,3200
S_ m0,s51,50944
Mvvv 92,42,252
E_ 128
Zaav 220,180,96
Ln v7,32,s65,256
Zaav 224,180,100
E_ 129
Zaav 228,180,104
Ln v8,32,s65,3328
Zaav 232,180,108
E_ 130
Zaav 236,180,112
W_ 0
E_ 131
Zavv 0,30,96
s_nop 0
Tv 38,v4,24576
Tv 40,v4,30720
Zavv 20,34,96
C_ 131,130,131
C_ 130,128,129
Zavv 24,34,100
s_nop 0
Tv 42,v5,24576
Tv 44,v5,30720
Zavv 4,30,100
E_ 132
Zavv 8,30,104
E_ 133
Zavv 28,34,104
R_ v46,a220
R_ v47,a224
Zavv 32,34,108
R_ v48,a228
R_ v49,a232
Zavv 12,30,108
R_ v50,a236
Zavv 16,30,112
E_ 134
Zavv 36,34,112
W_ 0
E_ 135
Zavv 56,38,112
s_nop 0
Tv 30,v2,25600
Tv 32,v2,31744
Zavv 76,42,112
C_ 135,134,135
C_ 134,132,133
Zavv 72,42,108
s_nop 0
Tv 34,v3,25600
Tv 36,v3,31744
Zavv 52,38,108
E_ 76
Zavv 48,38,104
E_ 77
Zavv 68,42,104
E_ 78
Zavv 64,42,100
E_ 79
Zavv 44,38,100
C_ 116,76,77
C_ 117,78,79
Zavv 40,38,96
E_ 80
Zavv 60,42,96
W_ 0
E_ 81
Zavv 80,30,96
s_nop 0
Tv 38,v4,25600
Tv 40,v4,31744
Zavv 100,34,96
E_ 82
Zavv 104,34,100
s_nop 0
Tv 42,v5,25600
Tv 44,v5,31744
Zavv 84,30,100
E_ 83
Zavv 88,30,104
C_ 120,80,81
C_ 121,82,83
Zavv 108,34,104
E_ 84
Zavv 112,34,108
E_ 85
Zavv 92,30,108
E_ 86
Zavv 96,30,112
E_ 87
Zavv 116,34,112
W_ 0
C_ 124,84,85
C_ 125,86,87
Zavv 136,38,112
s_nop 0
Tv 30,v2,26624
Tv 32,v2,32768
Zavv 156,42,112
E_ 88
Zavv 152,42,108
s_nop 0
Tv 34,v3,26624
Tv 36,v3,32768
Zavv 132,38,108
E_ 89
Zavv 128,38,104
E_ 90
Zavv 148,42,104
E_ 91
Zavv 144,42,100
C_ 128,88,89
C_ 129,90,91
Zavv 124,38,100
E_ 92
Zavv 120,38,96
E_ 93
Zavv 140,42,96
W_ 0
E_ 94
Zavv 160,30,96
s_nop 0
Tv 38,v4,26624
Tv 40,v4,32768
Zavv 180,34,96
E_ 95
Zavv 184,34,100
s_nop 0
Tv 42,v5,26624
Tv 44,v5,32768
Zavv 164,30,100
C_ 132,92,93
C_ 133,94,95
Zavv 168,30,104
v_mov_b32 v92, 0x3f803f80
v_mov_b32 v93, 0x3f803f80
Zavv 188,34,104
S_ s65,s61,24576
v_mov_b32 v94, 0x3f803f80
v_mov_b32 v95, 0x3f803f80
Zavv 192,34,108
S_ m0,s51,24576
Zavv 172,30,108
s_waitcnt vmcnt(18) lgkmcnt(0)
s_barrier
Zavv 176,30,112
L_ v9,36,s65,0
Zavv 196,34,112
Zavv 216,38,112
Dv 30,%0,6144
Zavv 236,42,112
Dv 34,v1,6144
Zavv 232,42,108
L_ v9,36,s65,3072
S_ m0,s51,25472
Zavv 212,38,108
Zavv 208,38,104
L_ v9,36,s65,128
Zavv 228,42,104
Zavv 224,42,100
L_ v9,36,s65,3200
S_ m0,s51,26368
Zavv 204,38,100
Zavv 200,38,96
L_ v9,36,s65,256
Zavv 220,42,96
.Lstep0_2:
Zvvv 76,92,116
Dv 38,%0,7168
Zvvv 80,92,120
Dv 42,v1,7168
Zvvv 84,92,124
Zvvv 88,92,128
Zvvv 92,92,132
W_ 2
Nvvv 96,30,136,56
Mvvv 96,34,156
Nvvv 100,30,140,60
s_nop 1
v_max3_f32 v10, v76, v80, v84
s_nop 0
v_max3_f32 v10, v10, v88, v92
Mvvv 100,34,160
v_cmp_lt_f32 vcc, 0x42000000, v10
s_cbranch_vccnz .Ltramp0_1
Nvvv 104,30,144,64
A_ v46,v46,v76
A_ v47,v47,v80
Mvvv 104,34,164
A_ v48,v48,v84
A_ v49,v49,v88
Nvvv 108,30,148,68
A_ v50,v50,v92
Mvvv 108,34,168
L_ v9,36,s65,3328
Nvvv 112,30,152,72
R_ v11,a248
Mvvv 112,34,172
W_ 0
v_min_u32 v11, 240, v11
Mvvv 112,38,192
Dv 30,%0,8192
v_lshlrev_b32 v11, 3, v11
Mvvv 112,42,212
Dv 34,v1,8192
Mvvv 108,38,188
buffer_load_dword a246, v11, s[44:47], s66 offen
Mvvv 108,42,208
Mvvv 104,38,184
Mvvv 104,42,204
Mvvv 100,38,180
Mvvv 100,42,200
Mvvv 96,38,176
Mvvv 96,42,196
W_ 0
Mvvv 96,30,216
Dv 38,%0,0
Mvvv 96,34,236
Dv 42,v1,0
Mvvv 100,30,220
Mvvv 100,34,240
Mvvv 104,30,224
Mvvv 104,34,244
Mvvv 108,30,228
Mvvv 108,34,248
s_nop 0
E_ 96
Mvvv 112,30,232
E_ 97
Mvvv 112,34,252
W_ 0
E_ 98
Nvvv 76,38,136,56
Dv 30,%0,1024
Mvvv 76,42,156
Dv 34,v1,1024
Nvvv 80,38,140,60
E_ 99
Mvvv 80,42,160
C_ 99,98,99
C_ 98,96,97
Nvvv 84,38,144,64
E_ 100
Mvvv 84,42,164
E_ 101
Nvvv 88,38,148,68
E_ 102
Mvvv 88,42,168
E_ 103
Nvvv 92,38,152,72
C_ 103,102,103
C_ 102,100,101
Mvvv 92,42,172
W_ 0
E_ 104
Mvvv 92,30,192
Dv 38,%0,2048
Mvvv 92,34,212
Dv 42,v1,2048
Mvvv 88,30,188
E_ 105
Mvvv 88,34,208
E_ 106
Mvvv 84,30,184
S_ s65,s61,49152
E_ 107
Mvvv 84,34,204
S_ m0,s51,0
C_ 107,106,107
C_ 106,104,105
Mvvv 80,30,180
s_waitcnt vmcnt(13) lgkmcnt(0)
s_barrier
s_cmp_eq_u32 s11, 2
s_cbranch_scc1 .Lxs2m
.Lxs2m_back:
Mvvv 80,34,200
Ln v7,32,s65,0
Mvvv 76,30,176
E_ 108
Mvvv 76,34,196
Ln v8,32,s65,3072
S_ m0,s51,896
Mvvv 76,38,216
Tv 30,v2,12288
Tv 32,v2,18432
Mvvv 76,42,236
Ln v7,32,s65,128
Mvvv 80,38,220
s_nop 0
Tv 34,v3,12288
Tv 36,v3,18432
Mvvv 80,42,240
Ln v8,32,s65,3200
S_ m0,s51,1792
Mvvv 84,38,224
E_ 109
Mvvv 84,42,244
Ln v7,32,s65,256
Mvvv 88,38,228
E_ 110
Mvvv 88,42,248
Ln v8,32,s65,3328
Mvvv 92,38,232
E_ 111
Mvvv 92,42,252
W_ 0
C_ 111,110,111
C_ 110,108,109
Mavv 0,30,116
s_nop 0
Tv 38,v4,12288
Tv 40,v4,18432
Mavv 20,34,116
E_ 112
Mavv 24,34,120
s_nop 0
Tv 42,v5,12288
Tv 44,v5,18432
Mavv 4,30,120
E_ 113
Mavv 8,30,124
E_ 114
Mavv 28,34,124
E_ 115
Mavv 32,34,128
C_ 115,114,115
C_ 114,112,113
Mavv 12,30,128
E_ 76
Mavv 16,30,132
E_ 77
Mavv 36,34,132
W_ 0
E_ 78
Mavv 56,38,132
s_nop 0
Tv 30,v2,13312
Tv 32,v2,19456
Mavv 76,42,132
E_ 79
Mavv 72,42,128
s_nop 0
Tv 34,v3,13312
Tv 36,v3,19456
Mavv 52,38,128
C_ 96,76,77
C_ 97,78,79
Mavv 48,38,124
E_ 80
Mavv 68,42,124
E_ 81
Mavv 64,42,120
E_ 82
Mavv 44,38,120
E_ 83
Mavv 40,38,116
C_ 100,80,81
C_ 101,82,83
Mavv 60,42,116
W_ 0
E_ 84
Mavv 80,30,116
s_nop 0
Tv 38,v4,13312
Tv 40,v4,19456
Mavv 100,34,116
E_ 85
Mavv 104,34,120
s_nop 0
Tv 42,v5,13312
Tv 44,v5,19456
Mavv 84,30,120
E_ 86
Mavv 88,30,124
E_ 87
Mavv 108,34,124
C_ 104,84,85
C_ 105,86,87
Mavv 112,34,128
E_ 88
Mavv 92,30,128
E_ 89
Mavv 96,30,132
E_ 90
Mavv 116,34,132
W_ 0
E_ 91
Mavv 136,38,132
s_nop 0
Tv 30,v2,14336
Tv 32,v2,20480
Mavv 156,42,132
C_ 108,88,89
C_ 109,90,91
Mavv 152,42,128
s_nop 0
Tv 34,v3,14336
Tv 36,v3,20480
Mavv 132,38,128
E_ 92
Mavv 128,38,124
E_ 93
Mavv 148,42,124
E_ 94
Mavv 144,42,120
E_ 95
Mavv 124,38,120
C_ 112,92,93
C_ 113,94,95
Mavv 120,38,116
v_mov_b32 v92, 0x3f803f80
v_mov_b32 v93, 0x3f803f80
Mavv 140,42,116
W_ 0
v_mov_b32 v94, 0x3f803f80
v_mov_b32 v95, 0x3f803f80
Mavv 160,30,116
s_nop 0
Tv 38,v4,14336
Tv 40,v4,20480
Mavv 180,34,116
Mavv 184,34,120
s_nop 0
Tv 42,v5,14336
Tv 44,v5,20480
Mavv 164,30,120
Mavv 168,30,124
Mavv 188,34,124
S_ s65,s61,36864
Mavv 192,34,128
S_ m0,s51,12288
Mavv 172,30,128
s_waitcnt vmcnt(13) lgkmcnt(0)
s_barrier
s_cmp_eq_u32 s11, 2
s_cbranch_scc1 .Lxs2e
.Lxs2e_back:
Mavv 176,30,132
L_ v9,36,s65,0
Mavv 196,34,132
Mavv 216,38,132
Dv 30,%0,55296
Mavv 236,42,132
Dv 34,v1,55296
Mavv 232,42,128
L_ v9,36,s65,3072
S_ m0,s51,13184
Mavv 212,38,128
Mavv 208,38,124
L_ v9,36,s65,128
Mavv 228,42,124
Mavv 224,42,120
L_ v9,36,s65,3200
S_ m0,s51,14080
Mavv 204,38,120
Mavv 200,38,116
L_ v9,36,s65,256
Mavv 220,42,116
s_bitcmp1_b32 s67, 0
s_cbranch_scc1 .Lhsd0_0
.Lhsd0_0_back:
.Lstep0_3:
Zvvv 76,92,96
Dv 38,%0,56320
Zvvv 80,92,100
Dv 42,v1,56320
Zvvv 84,92,104
Zvvv 88,92,108
Zvvv 92,92,112
W_ 2
Nvvv 116,30,136,56
Mvvv 116,34,156
Nvvv 120,30,140,60
s_nop 1
v_max3_f32 v10, v76, v80, v84
s_nop 0
v_max3_f32 v10, v10, v88, v92
Mvvv 120,34,160
v_cmp_lt_f32 vcc, 0x42000000, v10
s_cbranch_vccnz .Ltramp0_2
Nvvv 124,30,144,64
A_ v46,v46,v76
A_ v47,v47,v80
Mvvv 124,34,164
A_ v48,v48,v84
A_ v49,v49,v88
Nvvv 128,30,148,68
A_ v50,v50,v92
Mvvv 128,34,168
L_ v9,36,s65,3328
Nvvv 132,30,152,72
Mvvv 132,34,172
W_ 0
Mvvv 132,38,192
Dv 30,%0,57344
Mvvv 132,42,212
Dv 34,v1,57344
Mvvv 128,38,188
Mvvv 128,42,208
Mvvv 124,38,184
Mvvv 124,42,204
Mvvv 120,38,180
Mvvv 120,42,200
Mvvv 116,38,176
Mvvv 116,42,196
W_ 0
Mvvv 116,30,216
Dv 38,%0,49152
Mvvv 116,34,236
Dv 42,v1,49152
Mvvv 120,30,220
Mvvv 120,34,240
Mvvv 124,30,224
Mvvv 124,34,244
Mvvv 128,30,228
Mvvv 128,34,248
s_nop 0
E_ 116
Mvvv 132,30,232
E_ 117
Mvvv 132,34,252
W_ 0
E_ 118
Nvvv 76,38,136,56
Dv 30,%0,50176
Mvvv 76,42,156
Dv 34,v1,50176
Nvvv 80,38,140,60
E_ 119
Mvvv 80,42,160
C_ 119,118,119
C_ 118,116,117
Nvvv 84,38,144,64
E_ 120
Mvvv 84,42,164
E_ 121
Nvvv 88,38,148,68
E_ 122
Mvvv 88,42,168
E_ 123
Nvvv 92,38,152,72
C_ 123,122,123
C_ 122,120,121
Mvvv 92,42,172
W_ 0
E_ 124
Mvvv 92,30,192
Dv 38,%0,51200
Mvvv 92,34,212
Dv 42,v1,51200
Mvvv 88,30,188
E_ 125
Mvvv 88,34,208
E_ 126
Mvvv 84,30,184
S_ s65,s61,61440
E_ 127
Mvvv 84,34,204
S_ m0,s51,49152
C_ 127,126,127
C_ 126,124,125
Mvvv 80,30,180
s_waitcnt vmcnt(13) lgkmcnt(0)
s_barrier
s_cmp_eq_u32 s11, 2
s_cbranch_scc1 .Lxs3m
.Lxs3m_back:
Mvvv 80,34,200
Ln v7,32,s65,0
Mvvv 76,30,176
E_ 128
Mvvv 76,34,196
Ln v8,32,s65,3072
S_ m0,s51,50048
Mvvv 76,38,216
Tv 30,v2,24576
Tv 32,v2,30720
Mvvv 76,42,236
Ln v7,32,s65,128
Mvvv 80,38,220
s_nop 0
Tv 34,v3,24576
Tv 36,v3,30720
Mvvv 80,42,240
Ln v8,32,s65,3200
S_ m0,s51,50944
Mvvv 84,38,224
E_ 129
Mvvv 84,42,244
Ln v7,32,s65,256
Mvvv 88,38,228
.Lpad_a0_1:
s_cbranch_execz .Lhop_a0_r
E_ 130
Mvvv 88,42,248
Ln v8,32,s65,3328
Mvvv 92,38,232
E_ 131
Mvvv 92,42,252
W_ 0
C_ 131,130,131
C_ 130,128,129
Mavv 0,30,96
s_nop 0
Tv 38,v4,24576
Tv 40,v4,30720
Mavv 20,34,96
E_ 132
Mavv 24,34,100
s_nop 0
Tv 42,v5,24576
Tv 44,v5,30720
Mavv 4,30,100
E_ 133
Mavv 8,30,104
E_ 134
Mavv 28,34,104
E_ 135
Mavv 32,34,108
C_ 135,134,135
C_ 134,132,133
Mavv 12,30,108
E_ 76
Mavv 16,30,112
E_ 77
Mavv 36,34,112
W_ 0
E_ 78
Mavv 56,38,112
s_nop 0
Tv 30,v2,25600
Tv 32,v2,31744
Mavv 76,42,112
E_ 79
Mavv 72,42,108
s_nop 0
Tv 34,v3,25600
Tv 36,v3,31744
Mavv 52,38,108
C_ 116,76,77
C_ 117,78,79
Mavv 48,38,104
E_ 80
Mavv 68,42,104
E_ 81
Mavv 64,42,100
E_ 82
Mavv 44,38,100
E_ 83
Mavv 40,38,96
C_ 120,80,81
C_ 121,82,83
Mavv 60,42,96
W_ 0
E_ 84
Mavv 80,30,96
s_nop 0
Tv 38,v4,25600
Tv 40,v4,31744
Mavv 100,34,96
E_ 85
Mavv 104,34,100
s_nop 0
Tv 42,v5,25600
Tv 44,v5,31744
Mavv 84,30,100
E_ 86
Mavv 88,30,104
E_ 87
Mavv 108,34,104
C_ 124,84,85
C_ 125,86,87
Mavv 112,34,108
E_ 88
Mavv 92,30,108
E_ 89
Mavv 96,30,112
E_ 90
Mavv 116,34,112
W_ 0
E_ 91
Mavv 136,38,112
s_nop 0
Tv 30,v2,26624
Tv 32,v2,32768
Mavv 156,42,112
C_ 128,88,89
C_ 129,90,91
Mavv 152,42,108
s_nop 0
Tv 34,v3,26624
Tv 36,v3,32768
Mavv 132,38,108
E_ 92
Mavv 128,38,104
E_ 93
Mavv 148,42,104
E_ 94
Mavv 144,42,100
E_ 95
Mavv 124,38,100
C_ 132,92,93
C_ 133,94,95
Mavv 120,38,96
v_mov_b32 v92, 0x3f803f80
v_mov_b32 v93, 0x3f803f80
Mavv 140,42,96
W_ 0
v_mov_b32 v94, 0x3f803f80
v_mov_b32 v95, 0x3f803f80
Mavv 160,30,96
s_nop 0
Tv 38,v4,26624
Tv 40,v4,32768
Mavv 180,34,96
Mavv 184,34,100
s_nop 0
Tv 42,v5,26624
Tv 44,v5,32768
Mavv 164,30,100
Mavv 168,30,104
Mavv 188,34,104
S_ s65,s61,49152
Mavv 192,34,108
S_ m0,s51,24576
Mavv 172,30,108
s_waitcnt vmcnt(12) lgkmcnt(0)
s_barrier
s_cmp_eq_u32 s11, 2
s_cbranch_scc1 .Lxs3e
.Lxs3e_back:
Mavv 176,30,112
L_ v9,36,s65,0
Mavv 196,34,112
S_ s61,s61,24576
Mavv 216,38,112
s_lshr_b32 s67, s67, 1
Dv 30,%0,6144
Mavv 236,42,112
s_bitcmp1_b32 s67, 9
s_cselect_b32 s11, s13, s11
Dv 34,v1,6144
Mavv 232,42,108
L_ v9,36,s65,3072
S_ m0,s51,25472
Mavv 212,38,108
Mavv 208,38,104
L_ v9,36,s65,128
Mavv 228,42,104
Mavv 224,42,100
L_ v9,36,s65,3200
S_ m0,s51,26368
Mavv 204,38,100
Mavv 200,38,96
L_ v9,36,s65,256
Mavv 220,42,96
s_cmp_ge_u32 s67, 256
s_cbranch_scc1 .Lstep0_2
s_sub_u32 s61, s61, 147456
.Lstep0_14:
Zvvv 76,92,116
Dv 38,%0,7168
Zvvv 80,92,120
Dv 42,v1,7168
Zvvv 84,92,124
Zvvv 88,92,128
Zvvv 92,92,132
W_ 2
Nvvv 96,30,136,56
Mvvv 96,34,156
Nvvv 100,30,140,60
s_nop 1
v_max3_f32 v10, v76, v80, v84
s_nop 0
v_max3_f32 v10, v10, v88, v92
Mvvv 100,34,160
v_cmp_lt_f32 vcc, 0x42000000, v10
s_cbranch_vccnz .Ltramp0_13
Nvvv 104,30,144,64
A_ v46,v46,v76
A_ v47,v47,v80
Mvvv 104,34,164
A_ v48,v48,v84
A_ v49,v49,v88
Nvvv 108,30,148,68
A_ v50,v50,v92
Mvvv 108,34,168
L_ v9,36,s65,3328
Nvvv 112,30,152,72
R_ v20,a240
Mvvv 112,34,172
W_ 0
v_xor_b32 v21, 64, v20
Mvvv 112,38,192
S_ s65,s60,0
Dv 30,%0,8192
Mvvv 112,42,212
S_ m0,s50,0
Dv 34,v1,8192
Mvvv 108,38,188
L_ v20,24,s65,0
Mvvv 108,42,208
S_ s65,s60,10240
Mvvv 104,38,184
S_ m0,s50,1024
Mvvv 104,42,204
L_ v21,24,s65,0
Mvvv 100,38,180
S_ s65,s60,20480
Mvvv 100,42,200
S_ m0,s50,2048
Mvvv 96,38,176
L_ v20,24,s65,0
Mvvv 96,42,196
S_ s65,s60,30720
W_ 0
Mvvv 96,30,216
S_ m0,s50,3072
Dv 38,%0,0
Mvvv 96,34,236
Dv 42,v1,0
Mvvv 100,30,220
L_ v21,24,s65,0
Mvvv 100,34,240
S_ s65,s60,40960
Mvvv 104,30,224
S_ m0,s50,4096
Mvvv 104,34,244
L_ v20,24,s65,0
Mvvv 108,30,228
Mvvv 108,34,248
E_ 96
Mvvv 112,30,232
E_ 97
Mvvv 112,34,252
W_ 0
E_ 98
Nvvv 76,38,136,56
Dv 30,%0,1024
Mvvv 76,42,156
Dv 34,v1,1024
Nvvv 80,38,140,60
E_ 99
Mvvv 80,42,160
C_ 99,98,99
C_ 98,96,97
Nvvv 84,38,144,64
E_ 100
Mvvv 84,42,164
E_ 101
Nvvv 88,38,148,68
E_ 102
Mvvv 88,42,168
E_ 103
Nvvv 92,38,152,72
C_ 103,102,103
C_ 102,100,101
Mvvv 92,42,172
W_ 0
E_ 104
Mvvv 92,30,192
Dv 38,%0,2048
Mvvv 92,34,212
Dv 42,v1,2048
Mvvv 88,30,188
E_ 105
Mvvv 88,34,208
E_ 106
Mvvv 84,30,184
E_ 107
Mvvv 84,34,204
C_ 107,106,107
C_ 106,104,105
Mvvv 80,30,180
s_waitcnt vmcnt(17) lgkmcnt(0)
s_barrier
s_cmp_eq_u32 s11, 2
s_cbranch_scc1 .Lxs14m
.Lxs14m_back:
Mvvv 80,34,200
E_ 108
Mvvv 76,30,176
E_ 109
Mvvv 76,34,196
E_ 110
Mvvv 76,38,216
s_nop 0
Tv 30,v2,12288
Tv 32,v2,18432
Mvvv 76,42,236
E_ 111
Mvvv 80,38,220
s_nop 0
Tv 34,v3,12288
Tv 36,v3,18432
Mvvv 80,42,240
C_ 111,110,111
C_ 110,108,109
Mvvv 84,38,224
E_ 112
Mvvv 84,42,244
E_ 113
Mvvv 88,38,228
E_ 114
Mvvv 88,42,248
E_ 115
Mvvv 92,38,232
C_ 115,114,115
C_ 114,112,113
Mvvv 92,42,252
W_ 0
E_ 76
Mavv 0,30,116
s_nop 0
Tv 38,v4,12288
Tv 40,v4,18432
Mavv 20,34,116
E_ 77
Mavv 24,34,120
s_nop 0
Tv 42,v5,12288
Tv 44,v5,18432
Mavv 4,30,120
E_ 78
Mavv 8,30,124
E_ 79
Mavv 28,34,124
C_ 96,76,77
C_ 97,78,79
Mavv 32,34,128
E_ 80
Mavv 12,30,128
E_ 81
Mavv 16,30,132
E_ 82
Mavv 36,34,132
W_ 0
E_ 83
Mavv 56,38,132
s_nop 0
Tv 30,v2,13312
Tv 32,v2,19456
Mavv 76,42,132
C_ 100,80,81
C_ 101,82,83
Mavv 72,42,128
s_nop 0
Tv 34,v3,13312
Tv 36,v3,19456
Mavv 52,38,128
E_ 84
Mavv 48,38,124
E_ 85
Mavv 68,42,124
E_ 86
Mavv 64,42,120
E_ 87
Mavv 44,38,120
C_ 104,84,85
C_ 105,86,87
Mavv 40,38,116
E_ 88
Mavv 60,42,116
W_ 0
E_ 89
Mavv 80,30,116
s_nop 0
Tv 38,v4,13312
Tv 40,v4,19456
Mavv 100,34,116
E_ 90
Mavv 104,34,120
s_nop 0
Tv 42,v5,13312
Tv 44,v5,19456
Mavv 84,30,120
E_ 91
Mavv 88,30,124
C_ 108,88,89
C_ 109,90,91
Mavv 108,34,124
E_ 92
Mavv 112,34,128
E_ 93
Mavv 92,30,128
E_ 94
Mavv 96,30,132
E_ 95
Mavv 116,34,132
W_ 0
C_ 112,92,93
C_ 113,94,95
Mavv 136,38,132
s_nop 0
Tv 30,v2,14336
Tv 32,v2,20480
Mavv 156,42,132
v_mov_b32 v92, 0x3f803f80
v_mov_b32 v93, 0x3f803f80
Mavv 152,42,128
s_nop 0
Tv 34,v3,14336
Tv 36,v3,20480
Mavv 132,38,128
v_mov_b32 v94, 0x3f803f80
v_mov_b32 v95, 0x3f803f80
)A"
R"A(Mavv 128,38,124
Mavv 148,42,124
Mavv 144,42,120
Mavv 124,38,120
Mavv 120,38,116
Mavv 140,42,116
W_ 0
Mavv 160,30,116
s_nop 0
Tv 38,v4,14336
Tv 40,v4,20480
Mavv 180,34,116
Mavv 184,34,120
s_nop 0
Tv 42,v5,14336
Tv 44,v5,20480
Mavv 164,30,120
Mavv 168,30,124
Mavv 188,34,124
Mavv 192,34,128
Mavv 172,30,128
s_waitcnt vmcnt(11) lgkmcnt(0)
s_barrier
s_cmp_eq_u32 s11, 2
s_cbranch_scc1 .Lxs14e
.Lxs14e_back:
Mavv 176,30,132
S_ s65,s59,0
Mavv 196,34,132
S_ m0,s52,8192
Mavv 216,38,132
Dv 30,%0,55296
Mavv 236,42,132
Dv 34,v1,55296
Mavv 232,42,128
L_ v6,28,s65,0
Mavv 212,38,128
S_ s65,s59,98304
Mavv 208,38,124
S_ m0,s52,9216
Mavv 228,42,124
L_ v6,28,s65,0
Mavv 224,42,120
S_ s65,s59,196608
Mavv 204,38,120
S_ m0,s52,10240
Mavv 200,38,116
L_ v6,28,s65,0
Mavv 220,42,116
S_ s65,s59,294912
.Lstep0_15:
Zvvv 76,92,96
Dv 38,%0,56320
Zvvv 80,92,100
Dv 42,v1,56320
Zvvv 84,92,104
Zvvv 88,92,108
Zvvv 92,92,112
W_ 2
Nvvv 116,30,136,56
Mvvv 116,34,156
Nvvv 120,30,140,60
s_nop 1
v_max3_f32 v10, v76, v80, v84
s_nop 0
v_max3_f32 v10, v10, v88, v92
Mvvv 120,34,160
v_cmp_lt_f32 vcc, 0x42000000, v10
s_cbranch_vccnz .Ltramp0_14
Nvvv 124,30,144,64
S_ m0,s52,11264
A_ v46,v46,v76
A_ v47,v47,v80
Mvvv 124,34,164
A_ v48,v48,v84
A_ v49,v49,v88
Nvvv 128,30,148,68
A_ v50,v50,v92
Mvvv 128,34,168
L_ v6,28,s65,0
Nvvv 132,30,152,72
S_ s65,s59,0
Mvvv 132,34,172
S_ m0,s52,16256
W_ 0
Mvvv 132,38,192
Dv 30,%0,57344
Mvvv 132,42,212
Dv 34,v1,57344
Mvvv 128,38,188
L_ v6,28,s65,128
Mvvv 128,42,208
S_ s65,s59,98304
Mvvv 124,38,184
S_ m0,s52,17280
Mvvv 124,42,204
L_ v6,28,s65,128
Mvvv 120,38,180
S_ s65,s59,196608
Mvvv 120,42,200
S_ m0,s52,18304
Mvvv 116,38,176
L_ v6,28,s65,128
Mvvv 116,42,196
S_ s65,s59,294912
W_ 0
Mvvv 116,30,216
S_ m0,s52,19328
Dv 38,%0,49152
Mvvv 116,34,236
Dv 42,v1,49152
Mvvv 120,30,220
L_ v6,28,s65,128
Mvvv 120,34,240
S_ s65,s59,786432
Mvvv 124,30,224
S_ m0,s52,0
Mvvv 124,34,244
L_ v6,28,s65,0
Mvvv 128,30,228
S_ s65,s59,884736
Mvvv 128,34,248
S_ m0,s52,1024
E_ 116
Mvvv 132,30,232
L_ v6,28,s65,0
Mvvv 132,34,252
S_ s65,s59,983040
E_ 117
W_ 1
Nvvv 76,38,136,56
S_ m0,s52,2048
Dv 30,%0,50176
W_ 1
Mvvv 76,42,156
Dv 34,v1,50176
Nvvv 80,38,140,60
L_ v6,28,s65,0
Mvvv 80,42,160
S_ s65,s59,1081344
E_ 118
Nvvv 84,38,144,64
S_ m0,s52,3072
E_ 119
Mvvv 84,42,164
L_ v6,28,s65,0
Nvvv 88,38,148,68
S_ s65,s60,128
C_ 119,118,119
C_ 118,116,117
Mvvv 88,42,168
S_ m0,s50,20480
E_ 120
Nvvv 92,38,152,72
L_ v20,24,s65,0
Mvvv 92,42,172
S_ s65,s60,10368
E_ 121
W_ 1
Mvvv 92,30,192
S_ m0,s50,21504
Dv 38,%0,51200
W_ 1
Mvvv 92,34,212
Dv 42,v1,51200
Mvvv 88,30,188
L_ v21,24,s65,0
Mvvv 88,34,208
S_ s65,s60,20608
E_ 122
Mvvv 84,30,184
S_ m0,s50,22528
E_ 123
Mvvv 84,34,204
L_ v20,24,s65,0
Mvvv 80,30,180
s_waitcnt vmcnt(20) lgkmcnt(0)
s_barrier
s_cmp_eq_u32 s11, 2
s_cbranch_scc1 .Lxs15m
.Lxs15m_back:
S_ s65,s60,30848
Mvvv 80,34,200
S_ m0,s50,23552
C_ 123,122,123
C_ 122,120,121
Mvvv 76,30,176
L_ v21,24,s65,0
Mvvv 76,34,196
S_ s65,s60,41088
E_ 124
Mvvv 76,38,216
.Lpad_a0_2:
s_cbranch_execz .Lhop_a0_r
S_ m0,s50,24576
Tv 30,v2,24576
Tv 32,v2,30720
Mvvv 76,42,236
L_ v20,24,s65,0
Mvvv 80,38,220
Tv 34,v3,24576
Tv 36,v3,30720
Mvvv 80,42,240
E_ 125
Mvvv 84,38,224
E_ 126
Mvvv 84,42,244
E_ 127
Mvvv 88,38,228
C_ 127,126,127
C_ 126,124,125
Mvvv 88,42,248
E_ 128
Mvvv 92,38,232
E_ 129
Mvvv 92,42,252
W_ 0
E_ 130
Mavv 0,30,96
s_nop 0
Tv 38,v4,24576
Tv 40,v4,30720
Mavv 20,34,96
E_ 131
Mavv 24,34,100
s_nop 0
Tv 42,v5,24576
Tv 44,v5,30720
Mavv 4,30,100
C_ 131,130,131
C_ 130,128,129
Mavv 8,30,104
E_ 132
Mavv 28,34,104
E_ 133
Mavv 32,34,108
E_ 134
Mavv 12,30,108
E_ 135
Mavv 16,30,112
C_ 135,134,135
C_ 134,132,133
Mavv 36,34,112
W_ 0
E_ 76
Mavv 56,38,112
s_nop 0
Tv 30,v2,25600
Tv 32,v2,31744
Mavv 76,42,112
E_ 77
Mavv 72,42,108
s_nop 0
Tv 34,v3,25600
Tv 36,v3,31744
Mavv 52,38,108
E_ 78
Mavv 48,38,104
E_ 79
Mavv 68,42,104
C_ 116,76,77
C_ 117,78,79
Mavv 64,42,100
E_ 80
Mavv 44,38,100
E_ 81
Mavv 40,38,96
E_ 82
Mavv 60,42,96
W_ 0
E_ 83
Mavv 80,30,96
s_nop 0
Tv 38,v4,25600
Tv 40,v4,31744
Mavv 100,34,96
C_ 120,80,81
C_ 121,82,83
Mavv 104,34,100
s_nop 0
Tv 42,v5,25600
Tv 44,v5,31744
Mavv 84,30,100
E_ 84
Mavv 88,30,104
E_ 85
Mavv 108,34,104
E_ 86
Mavv 112,34,108
E_ 87
Mavv 92,30,108
C_ 124,84,85
C_ 125,86,87
Mavv 96,30,112
E_ 88
Mavv 116,34,112
W_ 0
E_ 89
Mavv 136,38,112
s_nop 0
Tv 30,v2,26624
Tv 32,v2,32768
Mavv 156,42,112
E_ 90
Mavv 152,42,108
s_nop 0
Tv 34,v3,26624
Tv 36,v3,32768
Mavv 132,38,108
E_ 91
Mavv 128,38,104
C_ 128,88,89
C_ 129,90,91
Mavv 148,42,104
E_ 92
Mavv 144,42,100
E_ 93
Mavv 124,38,100
E_ 94
Mavv 120,38,96
E_ 95
Mavv 140,42,96
W_ 0
C_ 132,92,93
C_ 133,94,95
Mavv 160,30,96
s_nop 0
Tv 38,v4,26624
Tv 40,v4,32768
Mavv 180,34,96
v_mov_b32 v92, 0x3f803f80
v_mov_b32 v93, 0x3f803f80
Mavv 184,34,100
s_nop 0
Tv 42,v5,26624
Tv 44,v5,32768
Mavv 164,30,100
v_mov_b32 v94, 0x3f803f80
v_mov_b32 v95, 0x3f803f80
Mavv 168,30,104
Mavv 188,34,104
Mavv 192,34,108
Mavv 172,30,108
s_waitcnt vmcnt(63) lgkmcnt(0)
s_barrier
s_cmp_eq_u32 s11, 2
s_cbranch_scc1 .Lxs15e
.Lxs15e_back:
Mavv 176,30,112
S_ s65,s59,0
Mavv 196,34,112
S_ m0,s52,24320
Mavv 216,38,112
s_nop 0
Tv 30,v2,36864
Tv 32,v2,43008
Zvvv 76,92,116
L_ v6,28,s65,256
Zvvv 80,92,120
S_ s65,s59,98304
Tv 34,v3,36864
Tv 36,v3,43008
Zvvv 84,92,124
S_ m0,s52,25344
Zvvv 88,92,128
L_ v6,28,s65,256
Zvvv 92,92,132
S_ s65,s59,196608
Mavv 236,42,112
S_ m0,s52,26368
Mavv 232,42,108
L_ v6,28,s65,256
Mavv 212,38,108
S_ s65,s59,294912
Mavv 208,38,104
S_ m0,s52,27392
v_max3_f32 v10, v76, v80, v84
Mavv 228,42,104
v_max3_f32 v10, v10, v88, v92
v_cmp_lt_f32 vcc, 0x42000000, v10
Mavv 224,42,100
L_ v6,28,s65,256
Mavv 204,38,100
Mavv 200,38,96
Mavv 220,42,96
s_cbranch_vccnz .Ltramp0_15
.Ljoin0:
W_ 2
Mavv 0,30,116
s_mov_b32 s58, 1
Tv 38,v4,36864
Tv 40,v4,43008
W_ 2
Mavv 20,34,116
S_ s61,s61,393216
R_ v12,a248
s_mov_b32 s85, 4026531840
Mavv 24,34,120
s_nop 0
Tv 42,v5,36864
Tv 44,v5,43008
Mavv 4,30,120
A_ v46,v46,v76
A_ v47,v47,v80
Mavv 8,30,124
A_ v48,v48,v84
A_ v49,v49,v88
Mavv 28,34,124
A_ v50,v50,v92
Mavv 32,34,128
v_rcp_f32 v46, v46
Mavv 12,30,128
v_rcp_f32 v47, v47
Mavv 16,30,132
v_rcp_f32 v48, v48
Mavv 36,34,132
W_ 0
v_rcp_f32 v49, v49
Mavv 56,38,132
s_nop 0
Tv 30,v2,37888
Tv 32,v2,44032
Mavv 76,42,132
v_rcp_f32 v50, v50
Mavv 72,42,128
s_nop 0
Tv 34,v3,37888
Tv 36,v3,44032
Mavv 52,38,128
R_ v96,a0
R_ v97,a1
Mavv 48,38,124
R_ v98,a2
R_ v99,a3
Mavv 68,42,124
R_ v100,a20
R_ v101,a21
Mavv 64,42,120
R_ v102,a22
R_ v103,a23
Mavv 44,38,120
U_ v96,v96,v46
U_ v97,v97,v46
Mavv 40,38,116
U_ v98,v98,v46
U_ v99,v99,v46
Mavv 60,42,116
W_ 0
U_ v100,v100,v46
U_ v101,v101,v46
Mavv 80,30,116
s_nop 0
Tv 38,v4,37888
Tv 40,v4,44032
Mavv 100,34,116
U_ v102,v102,v46
U_ v103,v103,v46
Mavv 104,34,120
s_nop 0
Tv 42,v5,37888
Tv 44,v5,44032
Mavv 84,30,120
C_ 76,96,97
C_ 77,98,99
Mavv 88,30,124
C_ 78,100,101
C_ 79,102,103
Mavv 108,34,124
X_ a0,v76
X_ a1,v77
Mavv 112,34,128
X_ a2,v78
X_ a3,v79
Mavv 92,30,128
R_ v104,a4
R_ v105,a5
Mavv 96,30,132
R_ v106,a6
R_ v107,a7
Mavv 116,34,132
W_ 0
R_ v108,a24
R_ v109,a25
Mavv 136,38,132
s_nop 0
Tv 30,v2,38912
Tv 32,v2,45056
Mavv 156,42,132
R_ v110,a26
R_ v111,a27
Mavv 152,42,128
s_nop 0
Tv 34,v3,38912
Tv 36,v3,45056
Mavv 132,38,128
U_ v104,v104,v47
U_ v105,v105,v47
Mavv 128,38,124
U_ v106,v106,v47
U_ v107,v107,v47
Mavv 148,42,124
U_ v108,v108,v47
U_ v109,v109,v47
Mavv 144,42,120
U_ v110,v110,v47
U_ v111,v111,v47
Mavv 124,38,120
C_ 80,104,105
C_ 81,106,107
Mavv 120,38,116
C_ 82,108,109
C_ 83,110,111
Mavv 140,42,116
W_ 0
X_ a4,v80
X_ a5,v81
Mavv 160,30,116
s_nop 0
Tv 38,v4,38912
Tv 40,v4,45056
Mavv 180,34,116
X_ a6,v82
X_ a7,v83
Mavv 184,34,120
s_nop 0
Tv 42,v5,38912
Tv 44,v5,45056
Mavv 164,30,120
R_ v96,a8
R_ v97,a9
Mavv 168,30,124
R_ v98,a10
R_ v99,a11
Mavv 188,34,124
R_ v100,a28
R_ v101,a29
Mavv 192,34,128
R_ v102,a30
R_ v103,a31
Mavv 172,30,128
U_ v96,v96,v48
U_ v97,v97,v48
Mavv 176,30,132
U_ v98,v98,v48
U_ v99,v99,v48
Mavv 196,34,132
W_ 0
U_ v100,v100,v48
U_ v101,v101,v48
Mavv 216,38,132
U_ v102,v102,v48
U_ v103,v103,v48
Mavv 236,42,132
C_ 84,96,97
C_ 85,98,99
Mavv 232,42,128
C_ 86,100,101
C_ 87,102,103
Mavv 212,38,128
X_ a8,v84
X_ a9,v85
Mavv 208,38,124
X_ a10,v86
X_ a11,v87
Mavv 228,42,124
Mavv 224,42,120
Mavv 204,38,120
Mavv 200,38,116
Mavv 220,42,116
.p2align 3
R_ v104,a12
R_ v105,a13
R_ v106,a14
R_ v107,a15
R_ v108,a32
R_ v109,a33
R_ v110,a34
R_ v111,a35
v_pk_mul_f32 v[104:105], v[104:105], v[48:49] op_sel:[0,1]
v_pk_mul_f32 v[106:107], v[106:107], v[48:49] op_sel:[0,1]
v_pk_mul_f32 v[108:109], v[108:109], v[48:49] op_sel:[0,1]
v_pk_mul_f32 v[110:111], v[110:111], v[48:49] op_sel:[0,1]
C_ 88,104,105
C_ 89,106,107
C_ 90,108,109
C_ 91,110,111
X_ a12,v88
X_ a13,v89
X_ a14,v90
X_ a15,v91
.p2align 3
R_ v96,a16
R_ v97,a17
R_ v98,a18
R_ v99,a19
R_ v100,a36
R_ v101,a37
R_ v102,a38
R_ v103,a39
v_pk_mul_f32 v[96:97], v[96:97], v[50:51] op_sel_hi:[1,0]
v_pk_mul_f32 v[98:99], v[98:99], v[50:51] op_sel_hi:[1,0]
v_pk_mul_f32 v[100:101], v[100:101], v[50:51] op_sel_hi:[1,0]
v_pk_mul_f32 v[102:103], v[102:103], v[50:51] op_sel_hi:[1,0]
C_ 92,96,97
C_ 93,98,99
C_ 94,100,101
C_ 95,102,103
X_ a16,v92
X_ a17,v93
X_ a18,v94
X_ a19,v95
.p2align 3
R_ v104,a40
R_ v105,a41
R_ v106,a42
R_ v107,a43
R_ v108,a60
R_ v109,a61
R_ v110,a62
R_ v111,a63
v_pk_mul_f32 v[104:105], v[104:105], v[46:47] op_sel_hi:[1,0]
v_pk_mul_f32 v[106:107], v[106:107], v[46:47] op_sel_hi:[1,0]
v_pk_mul_f32 v[108:109], v[108:109], v[46:47] op_sel_hi:[1,0]
v_pk_mul_f32 v[110:111], v[110:111], v[46:47] op_sel_hi:[1,0]
C_ 76,104,105
C_ 77,106,107
C_ 78,108,109
C_ 79,110,111
X_ a40,v76
X_ a41,v77
X_ a42,v78
X_ a43,v79
.p2align 3
R_ v96,a44
R_ v97,a45
R_ v98,a46
R_ v99,a47
R_ v100,a64
R_ v101,a65
R_ v102,a66
R_ v103,a67
v_pk_mul_f32 v[96:97], v[96:97], v[46:47] op_sel:[0,1]
v_pk_mul_f32 v[98:99], v[98:99], v[46:47] op_sel:[0,1]
v_pk_mul_f32 v[100:101], v[100:101], v[46:47] op_sel:[0,1]
v_pk_mul_f32 v[102:103], v[102:103], v[46:47] op_sel:[0,1]
C_ 80,96,97
C_ 81,98,99
C_ 82,100,101
C_ 83,102,103
X_ a44,v80
X_ a45,v81
X_ a46,v82
X_ a47,v83
.p2align 3
R_ v104,a48
R_ v105,a49
R_ v106,a50
R_ v107,a51
R_ v108,a68
R_ v109,a69
R_ v110,a70
R_ v111,a71
v_pk_mul_f32 v[104:105], v[104:105], v[48:49] op_sel_hi:[1,0]
v_pk_mul_f32 v[106:107], v[106:107], v[48:49] op_sel_hi:[1,0]
v_pk_mul_f32 v[108:109], v[108:109], v[48:49] op_sel_hi:[1,0]
v_pk_mul_f32 v[110:111], v[110:111], v[48:49] op_sel_hi:[1,0]
C_ 84,104,105
C_ 85,106,107
C_ 86,108,109
C_ 87,110,111
X_ a48,v84
X_ a49,v85
X_ a50,v86
X_ a51,v87
.p2align 3
R_ v96,a52
R_ v97,a53
R_ v98,a54
R_ v99,a55
R_ v100,a72
R_ v101,a73
R_ v102,a74
R_ v103,a75
v_pk_mul_f32 v[96:97], v[96:97], v[48:49] op_sel:[0,1]
v_pk_mul_f32 v[98:99], v[98:99], v[48:49] op_sel:[0,1]
v_pk_mul_f32 v[100:101], v[100:101], v[48:49] op_sel:[0,1]
v_pk_mul_f32 v[102:103], v[102:103], v[48:49] op_sel:[0,1]
C_ 88,96,97
C_ 89,98,99
C_ 90,100,101
C_ 91,102,103
X_ a52,v88
X_ a53,v89
X_ a54,v90
X_ a55,v91
.p2align 3
R_ v104,a56
R_ v105,a57
R_ v106,a58
R_ v107,a59
R_ v108,a76
R_ v109,a77
R_ v110,a78
R_ v111,a79
v_pk_mul_f32 v[104:105], v[104:105], v[50:51] op_sel_hi:[1,0]
v_pk_mul_f32 v[106:107], v[106:107], v[50:51] op_sel_hi:[1,0]
v_pk_mul_f32 v[108:109], v[108:109], v[50:51] op_sel_hi:[1,0]
v_pk_mul_f32 v[110:111], v[110:111], v[50:51] op_sel_hi:[1,0]
C_ 92,104,105
C_ 93,106,107
C_ 94,108,109
C_ 95,110,111
X_ a56,v92
X_ a57,v93
X_ a58,v94
X_ a59,v95
.p2align 3
R_ v96,a80
R_ v97,a81
R_ v98,a82
R_ v99,a83
R_ v100,a100
R_ v101,a101
R_ v102,a102
R_ v103,a103
v_pk_mul_f32 v[96:97], v[96:97], v[46:47] op_sel_hi:[1,0]
v_pk_mul_f32 v[98:99], v[98:99], v[46:47] op_sel_hi:[1,0]
v_pk_mul_f32 v[100:101], v[100:101], v[46:47] op_sel_hi:[1,0]
v_pk_mul_f32 v[102:103], v[102:103], v[46:47] op_sel_hi:[1,0]
C_ 76,96,97
C_ 77,98,99
C_ 78,100,101
C_ 79,102,103
X_ a80,v76
X_ a81,v77
X_ a82,v78
X_ a83,v79
.p2align 3
R_ v104,a84
R_ v105,a85
R_ v106,a86
R_ v107,a87
R_ v108,a104
R_ v109,a105
R_ v110,a106
R_ v111,a107
v_pk_mul_f32 v[104:105], v[104:105], v[46:47] op_sel:[0,1]
v_pk_mul_f32 v[106:107], v[106:107], v[46:47] op_sel:[0,1]
v_pk_mul_f32 v[108:109], v[108:109], v[46:47] op_sel:[0,1]
v_pk_mul_f32 v[110:111], v[110:111], v[46:47] op_sel:[0,1]
C_ 80,104,105
C_ 81,106,107
C_ 82,108,109
C_ 83,110,111
X_ a84,v80
X_ a85,v81
X_ a86,v82
X_ a87,v83
.p2align 3
R_ v96,a88
R_ v97,a89
R_ v98,a90
R_ v99,a91
R_ v100,a108
R_ v101,a109
R_ v102,a110
R_ v103,a111
v_pk_mul_f32 v[96:97], v[96:97], v[48:49] op_sel_hi:[1,0]
v_pk_mul_f32 v[98:99], v[98:99], v[48:49] op_sel_hi:[1,0]
.Lpad_a0_3:
s_cbranch_execz .Lhop_a0_r
v_pk_mul_f32 v[100:101], v[100:101], v[48:49] op_sel_hi:[1,0]
v_pk_mul_f32 v[102:103], v[102:103], v[48:49] op_sel_hi:[1,0]
C_ 84,96,97
C_ 85,98,99
C_ 86,100,101
C_ 87,102,103
X_ a88,v84
X_ a89,v85
X_ a90,v86
X_ a91,v87
.p2align 3
R_ v104,a92
R_ v105,a93
R_ v106,a94
R_ v107,a95
R_ v108,a112
R_ v109,a113
R_ v110,a114
R_ v111,a115
v_pk_mul_f32 v[104:105], v[104:105], v[48:49] op_sel:[0,1]
v_pk_mul_f32 v[106:107], v[106:107], v[48:49] op_sel:[0,1]
v_pk_mul_f32 v[108:109], v[108:109], v[48:49] op_sel:[0,1]
v_pk_mul_f32 v[110:111], v[110:111], v[48:49] op_sel:[0,1]
C_ 88,104,105
C_ 89,106,107
C_ 90,108,109
C_ 91,110,111
X_ a92,v88
X_ a93,v89
X_ a94,v90
X_ a95,v91
.p2align 3
R_ v96,a96
R_ v97,a97
R_ v98,a98
R_ v99,a99
R_ v100,a116
R_ v101,a117
R_ v102,a118
R_ v103,a119
v_pk_mul_f32 v[96:97], v[96:97], v[50:51] op_sel_hi:[1,0]
v_pk_mul_f32 v[98:99], v[98:99], v[50:51] op_sel_hi:[1,0]
v_pk_mul_f32 v[100:101], v[100:101], v[50:51] op_sel_hi:[1,0]
v_pk_mul_f32 v[102:103], v[102:103], v[50:51] op_sel_hi:[1,0]
C_ 92,96,97
C_ 93,98,99
C_ 94,100,101
C_ 95,102,103
X_ a96,v92
X_ a97,v93
X_ a98,v94
X_ a99,v95
.p2align 3
R_ v104,a120
R_ v105,a121
R_ v106,a122
R_ v107,a123
R_ v108,a140
R_ v109,a141
R_ v110,a142
R_ v111,a143
v_pk_mul_f32 v[104:105], v[104:105], v[46:47] op_sel_hi:[1,0]
v_pk_mul_f32 v[106:107], v[106:107], v[46:47] op_sel_hi:[1,0]
v_pk_mul_f32 v[108:109], v[108:109], v[46:47] op_sel_hi:[1,0]
v_pk_mul_f32 v[110:111], v[110:111], v[46:47] op_sel_hi:[1,0]
C_ 76,104,105
C_ 77,106,107
C_ 78,108,109
C_ 79,110,111
X_ a120,v76
X_ a121,v77
X_ a122,v78
X_ a123,v79
.p2align 3
R_ v96,a124
R_ v97,a125
R_ v98,a126
R_ v99,a127
R_ v100,a144
R_ v101,a145
R_ v102,a146
R_ v103,a147
v_pk_mul_f32 v[96:97], v[96:97], v[46:47] op_sel:[0,1]
v_pk_mul_f32 v[98:99], v[98:99], v[46:47] op_sel:[0,1]
v_pk_mul_f32 v[100:101], v[100:101], v[46:47] op_sel:[0,1]
v_pk_mul_f32 v[102:103], v[102:103], v[46:47] op_sel:[0,1]
C_ 80,96,97
C_ 81,98,99
C_ 82,100,101
C_ 83,102,103
X_ a124,v80
X_ a125,v81
X_ a126,v82
X_ a127,v83
.p2align 3
R_ v104,a128
R_ v105,a129
R_ v106,a130
R_ v107,a131
R_ v108,a148
R_ v109,a149
R_ v110,a150
R_ v111,a151
v_pk_mul_f32 v[104:105], v[104:105], v[48:49] op_sel_hi:[1,0]
v_pk_mul_f32 v[106:107], v[106:107], v[48:49] op_sel_hi:[1,0]
v_pk_mul_f32 v[108:109], v[108:109], v[48:49] op_sel_hi:[1,0]
v_pk_mul_f32 v[110:111], v[110:111], v[48:49] op_sel_hi:[1,0]
C_ 84,104,105
C_ 85,106,107
C_ 86,108,109
C_ 87,110,111
X_ a128,v84
X_ a129,v85
X_ a130,v86
X_ a131,v87
.p2align 3
R_ v96,a132
R_ v97,a133
R_ v98,a134
R_ v99,a135
R_ v100,a152
R_ v101,a153
R_ v102,a154
R_ v103,a155
v_pk_mul_f32 v[96:97], v[96:97], v[48:49] op_sel:[0,1]
v_pk_mul_f32 v[98:99], v[98:99], v[48:49] op_sel:[0,1]
v_pk_mul_f32 v[100:101], v[100:101], v[48:49] op_sel:[0,1]
v_pk_mul_f32 v[102:103], v[102:103], v[48:49] op_sel:[0,1]
C_ 88,96,97
C_ 89,98,99
C_ 90,100,101
C_ 91,102,103
X_ a132,v88
X_ a133,v89
X_ a134,v90
X_ a135,v91
.p2align 3
R_ v104,a136
R_ v105,a137
R_ v106,a138
R_ v107,a139
R_ v108,a156
R_ v109,a157
R_ v110,a158
R_ v111,a159
v_pk_mul_f32 v[104:105], v[104:105], v[50:51] op_sel_hi:[1,0]
v_pk_mul_f32 v[106:107], v[106:107], v[50:51] op_sel_hi:[1,0]
v_pk_mul_f32 v[108:109], v[108:109], v[50:51] op_sel_hi:[1,0]
v_pk_mul_f32 v[110:111], v[110:111], v[50:51] op_sel_hi:[1,0]
C_ 92,104,105
C_ 93,106,107
C_ 94,108,109
C_ 95,110,111
X_ a136,v92
X_ a137,v93
X_ a138,v94
X_ a139,v95
.p2align 3
R_ v96,a160
R_ v97,a161
R_ v98,a162
R_ v99,a163
R_ v100,a180
R_ v101,a181
R_ v102,a182
R_ v103,a183
v_pk_mul_f32 v[96:97], v[96:97], v[46:47] op_sel_hi:[1,0]
v_pk_mul_f32 v[98:99], v[98:99], v[46:47] op_sel_hi:[1,0]
v_pk_mul_f32 v[100:101], v[100:101], v[46:47] op_sel_hi:[1,0]
v_pk_mul_f32 v[102:103], v[102:103], v[46:47] op_sel_hi:[1,0]
C_ 76,96,97
C_ 77,98,99
C_ 78,100,101
C_ 79,102,103
X_ a160,v76
X_ a161,v77
X_ a162,v78
X_ a163,v79
.p2align 3
R_ v104,a164
R_ v105,a165
R_ v106,a166
R_ v107,a167
R_ v108,a184
R_ v109,a185
R_ v110,a186
R_ v111,a187
v_pk_mul_f32 v[104:105], v[104:105], v[46:47] op_sel:[0,1]
v_pk_mul_f32 v[106:107], v[106:107], v[46:47] op_sel:[0,1]
v_pk_mul_f32 v[108:109], v[108:109], v[46:47] op_sel:[0,1]
v_pk_mul_f32 v[110:111], v[110:111], v[46:47] op_sel:[0,1]
C_ 80,104,105
C_ 81,106,107
C_ 82,108,109
C_ 83,110,111
X_ a164,v80
X_ a165,v81
X_ a166,v82
X_ a167,v83
.p2align 3
R_ v96,a168
R_ v97,a169
R_ v98,a170
R_ v99,a171
R_ v100,a188
R_ v101,a189
R_ v102,a190
R_ v103,a191
v_pk_mul_f32 v[96:97], v[96:97], v[48:49] op_sel_hi:[1,0]
v_pk_mul_f32 v[98:99], v[98:99], v[48:49] op_sel_hi:[1,0]
v_pk_mul_f32 v[100:101], v[100:101], v[48:49] op_sel_hi:[1,0]
v_pk_mul_f32 v[102:103], v[102:103], v[48:49] op_sel_hi:[1,0]
C_ 84,96,97
C_ 85,98,99
C_ 86,100,101
C_ 87,102,103
X_ a168,v84
X_ a169,v85
X_ a170,v86
X_ a171,v87
.p2align 3
R_ v104,a172
R_ v105,a173
R_ v106,a174
R_ v107,a175
R_ v108,a192
R_ v109,a193
R_ v110,a194
R_ v111,a195
v_pk_mul_f32 v[104:105], v[104:105], v[48:49] op_sel:[0,1]
v_pk_mul_f32 v[106:107], v[106:107], v[48:49] op_sel:[0,1]
v_pk_mul_f32 v[108:109], v[108:109], v[48:49] op_sel:[0,1]
v_pk_mul_f32 v[110:111], v[110:111], v[48:49] op_sel:[0,1]
C_ 88,104,105
C_ 89,106,107
C_ 90,108,109
C_ 91,110,111
X_ a172,v88
X_ a173,v89
X_ a174,v90
X_ a175,v91
.p2align 3
R_ v96,a176
R_ v97,a177
R_ v98,a178
R_ v99,a179
R_ v100,a196
R_ v101,a197
R_ v102,a198
R_ v103,a199
v_pk_mul_f32 v[96:97], v[96:97], v[50:51] op_sel_hi:[1,0]
v_pk_mul_f32 v[98:99], v[98:99], v[50:51] op_sel_hi:[1,0]
v_pk_mul_f32 v[100:101], v[100:101], v[50:51] op_sel_hi:[1,0]
v_pk_mul_f32 v[102:103], v[102:103], v[50:51] op_sel_hi:[1,0]
C_ 92,96,97
C_ 93,98,99
C_ 94,100,101
C_ 95,102,103
X_ a176,v92
X_ a177,v93
X_ a178,v94
X_ a179,v95
.p2align 3
R_ v104,a200
R_ v105,a201
R_ v106,a202
R_ v107,a203
R_ v108,a220
R_ v109,a221
R_ v110,a222
R_ v111,a223
v_pk_mul_f32 v[104:105], v[104:105], v[46:47] op_sel_hi:[1,0]
v_pk_mul_f32 v[106:107], v[106:107], v[46:47] op_sel_hi:[1,0]
v_pk_mul_f32 v[108:109], v[108:109], v[46:47] op_sel_hi:[1,0]
v_pk_mul_f32 v[110:111], v[110:111], v[46:47] op_sel_hi:[1,0]
C_ 76,104,105
C_ 77,106,107
C_ 78,108,109
C_ 79,110,111
X_ a200,v76
X_ a201,v77
X_ a202,v78
X_ a203,v79
.p2align 3
R_ v96,a204
R_ v97,a205
R_ v98,a206
R_ v99,a207
R_ v100,a224
R_ v101,a225
R_ v102,a226
R_ v103,a227
v_pk_mul_f32 v[96:97], v[96:97], v[46:47] op_sel:[0,1]
v_pk_mul_f32 v[98:99], v[98:99], v[46:47] op_sel:[0,1]
v_pk_mul_f32 v[100:101], v[100:101], v[46:47] op_sel:[0,1]
v_pk_mul_f32 v[102:103], v[102:103], v[46:47] op_sel:[0,1]
C_ 80,96,97
C_ 81,98,99
C_ 82,100,101
C_ 83,102,103
X_ a204,v80
X_ a205,v81
X_ a206,v82
X_ a207,v83
v_mov_b32 v4, v20
v_mov_b32 v5, v21
R_ %0,a242
R_ v2,a243
v_xor_b32 v1, 64, %0
v_xor_b32 v3, 64, v2
s_waitcnt vmcnt(0) lgkmcnt(0)
s_barrier
Ta 20,%0,8192
Ta 22,%0,8704
Ta 24,%0,12288
Ta 26,%0,12800
Da 60,v2,0
Da 100,v3,0
Da 64,v2,2048
Da 104,v3,2048
Da 68,v2,4096
Da 108,v3,4096
Da 72,v2,6144
Da 112,v3,6144
Da 76,v2,8192
Da 116,v3,8192
Ta 28,%0,8200
Ta 30,%0,8712
Ta 32,%0,12296
Ta 34,%0,12808
.p2align 3
R_ v104,a208
R_ v105,a209
R_ v106,a210
R_ v107,a211
R_ v108,a228
R_ v109,a229
R_ v110,a230
R_ v111,a231
v_pk_mul_f32 v[104:105], v[104:105], v[48:49] op_sel_hi:[1,0]
v_pk_mul_f32 v[106:107], v[106:107], v[48:49] op_sel_hi:[1,0]
v_pk_mul_f32 v[108:109], v[108:109], v[48:49] op_sel_hi:[1,0]
v_pk_mul_f32 v[110:111], v[110:111], v[48:49] op_sel_hi:[1,0]
C_ 84,104,105
C_ 85,106,107
C_ 86,108,109
C_ 87,110,111
X_ a208,v84
X_ a209,v85
X_ a210,v86
X_ a211,v87
.p2align 3
R_ v96,a212
R_ v97,a213
R_ v98,a214
R_ v99,a215
R_ v100,a232
R_ v101,a233
R_ v102,a234
R_ v103,a235
v_pk_mul_f32 v[96:97], v[96:97], v[48:49] op_sel:[0,1]
v_pk_mul_f32 v[98:99], v[98:99], v[48:49] op_sel:[0,1]
v_pk_mul_f32 v[100:101], v[100:101], v[48:49] op_sel:[0,1]
v_pk_mul_f32 v[102:103], v[102:103], v[48:49] op_sel:[0,1]
C_ 88,96,97
C_ 89,98,99
C_ 90,100,101
C_ 91,102,103
X_ a212,v88
X_ a213,v89
X_ a214,v90
X_ a215,v91
.p2align 3
R_ v104,a216
R_ v105,a217
R_ v106,a218
R_ v107,a219
R_ v108,a236
R_ v109,a237
R_ v110,a238
R_ v111,a239
v_pk_mul_f32 v[104:105], v[104:105], v[50:51] op_sel_hi:[1,0]
v_pk_mul_f32 v[106:107], v[106:107], v[50:51] op_sel_hi:[1,0]
v_pk_mul_f32 v[108:109], v[108:109], v[50:51] op_sel_hi:[1,0]
v_pk_mul_f32 v[110:111], v[110:111], v[50:51] op_sel_hi:[1,0]
C_ 92,104,105
C_ 93,106,107
C_ 94,108,109
C_ 95,110,111
X_ a216,v92
X_ a217,v93
X_ a218,v94
X_ a219,v95
s_branch .Lq
.Ltramp0_1:
s_mov_b32 s63, 1
s_branch .Lslow1
.Ltramp0_2:
s_mov_b32 s63, 2
s_branch .Lslow0
.Ltramp0_13:
s_mov_b32 s63, 13
s_branch .Lslow1
.Ltramp0_14:
s_mov_b32 s63, 14
s_branch .Lslow0
.Ltramp0_15:
s_mov_b32 s63, 15
s_branch .Lslow1
.Lstep0_16:
Zvvv 76,92,116
Zvvv 80,92,120
Zvvv 84,92,124
Zvvv 88,92,128
Zvvv 92,92,132
s_nop 5
v_max3_f32 v10, v76, v80, v84
s_nop 0
v_max3_f32 v10, v10, v88, v92
v_cmp_lt_f32 vcc, 0x42000000, v10
s_cbranch_vccnz .Ltramp0_15
s_branch .Ljoin0
.Ltramp1_1:
s_mov_b32 s63, 1
s_branch .Lslow1
.Ltramp1_2:
s_mov_b32 s63, 2
s_branch .Lslow0
.Ltramp1_3:
s_mov_b32 s63, 3
s_branch .Lslow1
.Ltramp1_4:
s_mov_b32 s63, 4
s_branch .Lslow0
.Ltramp1_5:
s_mov_b32 s63, 5
s_branch .Lslow1
.Ltramp1_6:
s_mov_b32 s63, 6
s_branch .Lslow0
.Ltramp1_7:
s_mov_b32 s63, 7
s_branch .Lslow1
.Ltramp1_8:
s_mov_b32 s63, 8
s_branch .Lslow0
.Ltramp1_9:
s_mov_b32 s63, 9
s_branch .Lslow1
.Ltramp1_10:
s_mov_b32 s63, 10
s_branch .Lslow0
.Ltramp1_11:
s_mov_b32 s63, 11
s_branch .Lslow1
.Ltramp1_12:
s_mov_b32 s63, 12
s_branch .Lslow0
.Ltramp1_13:
s_mov_b32 s63, 13
s_branch .Lslow1
.Ltramp1_14:
s_mov_b32 s63, 14
s_branch .Lslow0
.Ltramp1_15:
s_mov_b32 s63, 15
s_branch .Lslow1
.Lstep1_16:
Zvvv 76,92,116
Zvvv 80,92,120
Zvvv 84,92,124
Zvvv 88,92,128
Zvvv 92,92,132
s_nop 5
v_max3_f32 v10, v76, v80, v84
s_nop 0
v_max3_f32 v10, v10, v88, v92
v_cmp_lt_f32 vcc, 0x42000000, v10
s_cbranch_vccnz .Ltramp1_15
s_branch .Ljoin1
.Ltramp2_1:
s_mov_b32 s63, 1
s_branch .Lslow1
.Ltramp2_2:
s_mov_b32 s63, 2
s_branch .Lslow0
.Ltramp2_3:
s_mov_b32 s63, 3
s_branch .Lslow1
.Ltramp2_4:
s_mov_b32 s63, 4
s_branch .Lslow0
.Ltramp2_5:
s_mov_b32 s63, 5
s_branch .Lslow1
.Ltramp2_6:
s_mov_b32 s63, 6
s_branch .Lslow0
.Ltramp2_7:
s_mov_b32 s63, 7
s_branch .Lslow1
.Ltramp2_8:
s_mov_b32 s63, 8
s_branch .Lslow0
.Ltramp2_9:
s_mov_b32 s63, 9
s_branch .Lslow1
.Ltramp2_10:
s_mov_b32 s63, 10
s_branch .Lslow0
.Ltramp2_11:
s_mov_b32 s63, 11
s_branch .Lslow1
.Ltramp2_12:
s_mov_b32 s63, 12
s_branch .Lslow0
.Ltramp2_13:
s_mov_b32 s63, 13
s_branch .Lslow1
.Ltramp2_14:
s_mov_b32 s63, 14
s_branch .Lslow0
.Ltramp2_15:
s_mov_b32 s63, 15
s_branch .Lslow1
.Lstep2_16:
Zvvv 76,92,116
Zvvv 80,92,120
Zvvv 84,92,124
Zvvv 88,92,128
Zvvv 92,92,132
s_nop 5
v_max3_f32 v10, v76, v80, v84
s_nop 0
v_max3_f32 v10, v10, v88, v92
v_cmp_lt_f32 vcc, 0x42000000, v10
s_cbranch_vccnz .Ltramp2_15
s_branch .Ljoin2
.Lslow0:
W_ 0
s_nop 7
s_nop 7
s_and_b32 s64, s61, 4294959104
s_mul_i32 s53, s63, 12288
S_ s64,s64,s53
R_ v11,a248
v_and_b32 v10, 0xf0, v11
v_mul_u32_u24 v10, 24, v10
v_lshrrev_b32 v11, 8, v11
v_lshl_add_u32 v10, v11, 4, v10
S_ s53,s64,6144
buffer_load_dwordx4 v[116:119], v10, s[32:35], s53 offen offset:0
S_ s53,s64,6144
buffer_load_dwordx4 v[120:123], v10, s[32:35], s53 offen offset:64
S_ s53,s64,6144
buffer_load_dwordx4 v[124:127], v10, s[32:35], s53 offen offset:128
S_ s53,s64,6144
buffer_load_dwordx4 v[128:131], v10, s[32:35], s53 offen offset:192
s_waitcnt vmcnt(0)
Nvvv 96,116,136,56
Nvvv 100,116,140,60
Nvvv 104,116,144,64
Nvvv 108,116,148,68
Nvvv 112,116,152,72
Mvvv 96,120,156
Mvvv 100,120,160
Mvvv 104,120,164
Mvvv 108,120,168
Mvvv 112,120,172
Mvvv 96,124,176
Mvvv 100,124,180
Mvvv 104,124,184
Mvvv 108,124,188
Mvvv 112,124,192
Mvvv 96,128,196
Mvvv 100,128,200
Mvvv 104,128,204
Mvvv 108,128,208
Mvvv 112,128,212
s_nop 7
S_ s53,s64,6144
buffer_load_dwordx4 v[116:119], v10, s[32:35], s53 offen offset:256
S_ s53,s64,6144
buffer_load_dwordx4 v[120:123], v10, s[32:35], s53 offen offset:320
S_ s53,s64,0
buffer_load_dwordx4 v[124:127], v10, s[32:35], s53 offen offset:0
S_ s53,s64,0
buffer_load_dwordx4 v[128:131], v10, s[32:35], s53 offen offset:64
s_waitcnt vmcnt(0)
Mvvv 96,116,216
Mvvv 100,116,220
Mvvv 104,116,224
Mvvv 108,116,228
Mvvv 112,116,232
Mvvv 96,120,236
Mvvv 100,120,240
Mvvv 104,120,244
Mvvv 108,120,248
Mvvv 112,120,252
Nvvv 76,124,136,56
Nvvv 80,124,140,60
Nvvv 84,124,144,64
Nvvv 88,124,148,68
Nvvv 92,124,152,72
Mvvv 76,128,156
Mvvv 80,128,160
Mvvv 84,128,164
Mvvv 88,128,168
Mvvv 92,128,172
s_nop 7
S_ s53,s64,0
buffer_load_dwordx4 v[116:119], v10, s[32:35], s53 offen offset:128
S_ s53,s64,0
buffer_load_dwordx4 v[120:123], v10, s[32:35], s53 offen offset:192
S_ s53,s64,0
buffer_load_dwordx4 v[124:127], v10, s[32:35], s53 offen offset:256
S_ s53,s64,0
buffer_load_dwordx4 v[128:131], v10, s[32:35], s53 offen offset:320
s_waitcnt vmcnt(0)
Mvvv 76,116,176
Mvvv 80,116,180
Mvvv 84,116,184
Mvvv 88,116,188
Mvvv 92,116,192
Mvvv 76,120,196
Mvvv 80,120,200
Mvvv 84,120,204
Mvvv 88,120,208
Mvvv 92,120,212
Mvvv 76,124,216
Mvvv 80,124,220
Mvvv 84,124,224
Mvvv 88,124,228
Mvvv 92,124,232
Mvvv 76,128,236
Mvvv 80,128,240
Mvvv 84,128,244
Mvvv 88,128,248
Mvvv 92,128,252
s_nop 7
s_nop 2
v_max3_f32 v10, v76, v77, v78
v_max3_f32 v10, v10, v79, v96
v_max3_f32 v10, v10, v97, v98
v_max_f32 v10, v10, v99
v_mov_b32 v11, v10
s_nop 1
v_permlane16_swap_b32 v10, v11
v_max_f32 v10, v10, v11
v_mov_b32 v11, v10
s_nop 1
v_permlane32_swap_b32 v10, v11
v_max_f32 v10, v10, v11
A_ v10,0x40e00000,v10
v_max_f32 v10, 0, v10
v_exp_f32 v11, -v10
s_nop 0
U_ v46,v46,v11
R_ v12,a0
U_ v12,v12,v11
X_ a0,v12
R_ v13,a1
U_ v13,v13,v11
X_ a1,v13
R_ v12,a2
U_ v12,v12,v11
X_ a2,v12
R_ v13,a3
U_ v13,v13,v11
X_ a3,v13
R_ v12,a20
U_ v12,v12,v11
X_ a20,v12
R_ v13,a21
U_ v13,v13,v11
X_ a21,v13
R_ v12,a22
U_ v12,v12,v11
X_ a22,v12
R_ v13,a23
U_ v13,v13,v11
X_ a23,v13
R_ v12,a40
U_ v12,v12,v11
X_ a40,v12
R_ v13,a41
U_ v13,v13,v11
X_ a41,v13
R_ v12,a42
U_ v12,v12,v11
X_ a42,v12
R_ v13,a43
U_ v13,v13,v11
X_ a43,v13
R_ v12,a60
U_ v12,v12,v11
X_ a60,v12
R_ v13,a61
U_ v13,v13,v11
X_ a61,v13
R_ v12,a62
U_ v12,v12,v11
X_ a62,v12
R_ v13,a63
U_ v13,v13,v11
X_ a63,v13
R_ v12,a80
U_ v12,v12,v11
X_ a80,v12
R_ v13,a81
U_ v13,v13,v11
X_ a81,v13
R_ v12,a82
U_ v12,v12,v11
X_ a82,v12
R_ v13,a83
U_ v13,v13,v11
X_ a83,v13
R_ v12,a100
U_ v12,v12,v11
X_ a100,v12
R_ v13,a101
U_ v13,v13,v11
X_ a101,v13
R_ v12,a102
U_ v12,v12,v11
X_ a102,v12
R_ v13,a103
U_ v13,v13,v11
X_ a103,v13
R_ v12,a120
U_ v12,v12,v11
X_ a120,v12
R_ v13,a121
U_ v13,v13,v11
X_ a121,v13
R_ v12,a122
U_ v12,v12,v11
X_ a122,v12
R_ v13,a123
U_ v13,v13,v11
X_ a123,v13
R_ v12,a140
U_ v12,v12,v11
X_ a140,v12
R_ v13,a141
U_ v13,v13,v11
X_ a141,v13
R_ v12,a142
U_ v12,v12,v11
X_ a142,v12
R_ v13,a143
U_ v13,v13,v11
X_ a143,v13
R_ v12,a160
U_ v12,v12,v11
X_ a160,v12
R_ v13,a161
U_ v13,v13,v11
X_ a161,v13
R_ v12,a162
U_ v12,v12,v11
X_ a162,v12
R_ v13,a163
U_ v13,v13,v11
X_ a163,v13
R_ v12,a180
U_ v12,v12,v11
X_ a180,v12
R_ v13,a181
U_ v13,v13,v11
X_ a181,v13
R_ v12,a182
U_ v12,v12,v11
X_ a182,v12
R_ v13,a183
U_ v13,v13,v11
X_ a183,v13
R_ v12,a200
U_ v12,v12,v11
X_ a200,v12
R_ v13,a201
U_ v13,v13,v11
X_ a201,v13
R_ v12,a202
U_ v12,v12,v11
X_ a202,v12
R_ v13,a203
U_ v13,v13,v11
X_ a203,v13
R_ v12,a220
U_ v12,v12,v11
X_ a220,v12
R_ v13,a221
U_ v13,v13,v11
X_ a221,v13
R_ v12,a222
U_ v12,v12,v11
X_ a222,v12
R_ v13,a223
U_ v13,v13,v11
X_ a223,v13
B_ v56,v56,v10
B_ v57,v57,v10
B_ v58,v58,v10
B_ v59,v59,v10
B_ v76,v76,v10
B_ v77,v77,v10
B_ v78,v78,v10
B_ v79,v79,v10
B_ v96,v96,v10
B_ v97,v97,v10
B_ v98,v98,v10
B_ v99,v99,v10
E_ 76
E_ 77
E_ 78
E_ 79
E_ 96
E_ 97
E_ 98
E_ 99
s_nop 0
C_ 99,98,99
C_ 98,96,97
C_ 96,76,77
C_ 97,78,79
v_max3_f32 v10, v80, v81, v82
v_max3_f32 v10, v10, v83, v100
v_max3_f32 v10, v10, v101, v102
v_max_f32 v10, v10, v103
v_mov_b32 v11, v10
s_nop 1
v_permlane16_swap_b32 v10, v11
v_max_f32 v10, v10, v11
v_mov_b32 v11, v10
s_nop 1
v_permlane32_swap_b32 v10, v11
v_max_f32 v10, v10, v11
A_ v10,0x40e00000,v10
v_max_f32 v10, 0, v10
v_exp_f32 v11, -v10
s_nop 0
U_ v47,v47,v11
R_ v12,a4
U_ v12,v12,v11
X_ a4,v12
R_ v13,a5
U_ v13,v13,v11
X_ a5,v13
R_ v12,a6
U_ v12,v12,v11
X_ a6,v12
R_ v13,a7
U_ v13,v13,v11
X_ a7,v13
R_ v12,a24
U_ v12,v12,v11
X_ a24,v12
R_ v13,a25
U_ v13,v13,v11
X_ a25,v13
R_ v12,a26
U_ v12,v12,v11
X_ a26,v12
R_ v13,a27
U_ v13,v13,v11
X_ a27,v13
R_ v12,a44
U_ v12,v12,v11
X_ a44,v12
R_ v13,a45
U_ v13,v13,v11
X_ a45,v13
R_ v12,a46
U_ v12,v12,v11
X_ a46,v12
R_ v13,a47
U_ v13,v13,v11
X_ a47,v13
R_ v12,a64
U_ v12,v12,v11
X_ a64,v12
R_ v13,a65
U_ v13,v13,v11
X_ a65,v13
R_ v12,a66
U_ v12,v12,v11
X_ a66,v12
R_ v13,a67
U_ v13,v13,v11
X_ a67,v13
R_ v12,a84
U_ v12,v12,v11
X_ a84,v12
R_ v13,a85
U_ v13,v13,v11
X_ a85,v13
R_ v12,a86
U_ v12,v12,v11
X_ a86,v12
R_ v13,a87
U_ v13,v13,v11
X_ a87,v13
R_ v12,a104
U_ v12,v12,v11
X_ a104,v12
R_ v13,a105
U_ v13,v13,v11
X_ a105,v13
R_ v12,a106
U_ v12,v12,v11
X_ a106,v12
R_ v13,a107
U_ v13,v13,v11
X_ a107,v13
R_ v12,a124
U_ v12,v12,v11
X_ a124,v12
R_ v13,a125
U_ v13,v13,v11
X_ a125,v13
R_ v12,a126
U_ v12,v12,v11
X_ a126,v12
R_ v13,a127
U_ v13,v13,v11
X_ a127,v13
R_ v12,a144
U_ v12,v12,v11
X_ a144,v12
R_ v13,a145
U_ v13,v13,v11
X_ a145,v13
R_ v12,a146
U_ v12,v12,v11
X_ a146,v12
R_ v13,a147
U_ v13,v13,v11
X_ a147,v13
R_ v12,a164
U_ v12,v12,v11
X_ a164,v12
R_ v13,a165
U_ v13,v13,v11
X_ a165,v13
R_ v12,a166
U_ v12,v12,v11
X_ a166,v12
R_ v13,a167
U_ v13,v13,v11
X_ a167,v13
R_ v12,a184
U_ v12,v12,v11
X_ a184,v12
R_ v13,a185
U_ v13,v13,v11
X_ a185,v13
R_ v12,a186
U_ v12,v12,v11
X_ a186,v12
R_ v13,a187
U_ v13,v13,v11
X_ a187,v13
R_ v12,a204
U_ v12,v12,v11
X_ a204,v12
R_ v13,a205
U_ v13,v13,v11
X_ a205,v13
R_ v12,a206
U_ v12,v12,v11
X_ a206,v12
R_ v13,a207
U_ v13,v13,v11
X_ a207,v13
R_ v12,a224
U_ v12,v12,v11
X_ a224,v12
R_ v13,a225
U_ v13,v13,v11
X_ a225,v13
R_ v12,a226
U_ v12,v12,v11
X_ a226,v12
R_ v13,a227
U_ v13,v13,v11
X_ a227,v13
B_ v60,v60,v10
B_ v61,v61,v10
B_ v62,v62,v10
B_ v63,v63,v10
B_ v80,v80,v10
B_ v81,v81,v10
B_ v82,v82,v10
B_ v83,v83,v10
B_ v100,v100,v10
B_ v101,v101,v10
B_ v102,v102,v10
B_ v103,v103,v10
E_ 80
E_ 81
E_ 82
E_ 83
E_ 100
E_ 101
E_ 102
E_ 103
s_nop 0
C_ 103,102,103
C_ 102,100,101
C_ 100,80,81
C_ 101,82,83
v_max3_f32 v10, v84, v85, v86
v_max3_f32 v10, v10, v87, v104
v_max3_f32 v10, v10, v105, v106
v_max_f32 v10, v10, v107
v_mov_b32 v11, v10
s_nop 1
v_permlane16_swap_b32 v10, v11
v_max_f32 v10, v10, v11
v_mov_b32 v11, v10
s_nop 1
v_permlane32_swap_b32 v10, v11
v_max_f32 v10, v10, v11
A_ v10,0x40e00000,v10
v_max_f32 v10, 0, v10
v_exp_f32 v11, -v10
s_nop 0
U_ v48,v48,v11
R_ v12,a8
U_ v12,v12,v11
X_ a8,v12
R_ v13,a9
U_ v13,v13,v11
X_ a9,v13
R_ v12,a10
U_ v12,v12,v11
X_ a10,v12
R_ v13,a11
U_ v13,v13,v11
X_ a11,v13
R_ v12,a28
U_ v12,v12,v11
X_ a28,v12
R_ v13,a29
U_ v13,v13,v11
X_ a29,v13
R_ v12,a30
U_ v12,v12,v11
X_ a30,v12
R_ v13,a31
U_ v13,v13,v11
X_ a31,v13
R_ v12,a48
U_ v12,v12,v11
X_ a48,v12
R_ v13,a49
U_ v13,v13,v11
X_ a49,v13
R_ v12,a50
U_ v12,v12,v11
X_ a50,v12
R_ v13,a51
U_ v13,v13,v11
X_ a51,v13
R_ v12,a68
U_ v12,v12,v11
X_ a68,v12
R_ v13,a69
U_ v13,v13,v11
X_ a69,v13
R_ v12,a70
U_ v12,v12,v11
X_ a70,v12
R_ v13,a71
U_ v13,v13,v11
X_ a71,v13
R_ v12,a88
U_ v12,v12,v11
X_ a88,v12
R_ v13,a89
U_ v13,v13,v11
X_ a89,v13
R_ v12,a90
U_ v12,v12,v11
X_ a90,v12
R_ v13,a91
U_ v13,v13,v11
X_ a91,v13
R_ v12,a108
U_ v12,v12,v11
X_ a108,v12
R_ v13,a109
U_ v13,v13,v11
X_ a109,v13
R_ v12,a110
U_ v12,v12,v11
X_ a110,v12
R_ v13,a111
U_ v13,v13,v11
X_ a111,v13
R_ v12,a128
U_ v12,v12,v11
X_ a128,v12
R_ v13,a129
U_ v13,v13,v11
X_ a129,v13
R_ v12,a130
U_ v12,v12,v11
X_ a130,v12
R_ v13,a131
U_ v13,v13,v11
X_ a131,v13
R_ v12,a148
U_ v12,v12,v11
X_ a148,v12
R_ v13,a149
U_ v13,v13,v11
X_ a149,v13
R_ v12,a150
U_ v12,v12,v11
X_ a150,v12
R_ v13,a151
U_ v13,v13,v11
X_ a151,v13
R_ v12,a168
U_ v12,v12,v11
X_ a168,v12
R_ v13,a169
U_ v13,v13,v11
X_ a169,v13
R_ v12,a170
U_ v12,v12,v11
X_ a170,v12
R_ v13,a171
U_ v13,v13,v11
X_ a171,v13
R_ v12,a188
U_ v12,v12,v11
X_ a188,v12
R_ v13,a189
U_ v13,v13,v11
X_ a189,v13
R_ v12,a190
U_ v12,v12,v11
X_ a190,v12
R_ v13,a191
U_ v13,v13,v11
X_ a191,v13
R_ v12,a208
U_ v12,v12,v11
X_ a208,v12
R_ v13,a209
U_ v13,v13,v11
X_ a209,v13
R_ v12,a210
U_ v12,v12,v11
X_ a210,v12
R_ v13,a211
U_ v13,v13,v11
X_ a211,v13
R_ v12,a228
U_ v12,v12,v11
X_ a228,v12
R_ v13,a229
U_ v13,v13,v11
X_ a229,v13
R_ v12,a230
U_ v12,v12,v11
X_ a230,v12
R_ v13,a231
U_ v13,v13,v11
X_ a231,v13
B_ v64,v64,v10
B_ v65,v65,v10
B_ v66,v66,v10
B_ v67,v67,v10
B_ v84,v84,v10
B_ v85,v85,v10
B_ v86,v86,v10
B_ v87,v87,v10
B_ v104,v104,v10
B_ v105,v105,v10
B_ v106,v106,v10
B_ v107,v107,v10
E_ 84
E_ 85
E_ 86
E_ 87
E_ 104
E_ 105
E_ 106
E_ 107
s_nop 0
C_ 107,106,107
C_ 106,104,105
C_ 104,84,85
C_ 105,86,87
v_max3_f32 v10, v88, v89, v90
v_max3_f32 v10, v10, v91, v108
v_max3_f32 v10, v10, v109, v110
v_max_f32 v10, v10, v111
v_mov_b32 v11, v10
s_nop 1
v_permlane16_swap_b32 v10, v11
v_max_f32 v10, v10, v11
v_mov_b32 v11, v10
s_nop 1
v_permlane32_swap_b32 v10, v11
v_max_f32 v10, v10, v11
A_ v10,0x40e00000,v10
v_max_f32 v10, 0, v10
v_exp_f32 v11, -v10
s_nop 0
U_ v49,v49,v11
R_ v12,a12
U_ v12,v12,v11
X_ a12,v12
R_ v13,a13
U_ v13,v13,v11
X_ a13,v13
R_ v12,a14
U_ v12,v12,v11
X_ a14,v12
R_ v13,a15
U_ v13,v13,v11
X_ a15,v13
R_ v12,a32
)A"
R"A(U_ v12,v12,v11
X_ a32,v12
R_ v13,a33
U_ v13,v13,v11
X_ a33,v13
R_ v12,a34
U_ v12,v12,v11
X_ a34,v12
R_ v13,a35
U_ v13,v13,v11
X_ a35,v13
R_ v12,a52
U_ v12,v12,v11
X_ a52,v12
R_ v13,a53
U_ v13,v13,v11
X_ a53,v13
R_ v12,a54
U_ v12,v12,v11
X_ a54,v12
R_ v13,a55
U_ v13,v13,v11
X_ a55,v13
R_ v12,a72
U_ v12,v12,v11
X_ a72,v12
R_ v13,a73
U_ v13,v13,v11
X_ a73,v13
R_ v12,a74
U_ v12,v12,v11
X_ a74,v12
R_ v13,a75
U_ v13,v13,v11
X_ a75,v13
R_ v12,a92
U_ v12,v12,v11
X_ a92,v12
R_ v13,a93
U_ v13,v13,v11
X_ a93,v13
R_ v12,a94
U_ v12,v12,v11
X_ a94,v12
R_ v13,a95
U_ v13,v13,v11
X_ a95,v13
R_ v12,a112
U_ v12,v12,v11
X_ a112,v12
R_ v13,a113
U_ v13,v13,v11
X_ a113,v13
R_ v12,a114
U_ v12,v12,v11
X_ a114,v12
R_ v13,a115
U_ v13,v13,v11
X_ a115,v13
R_ v12,a132
U_ v12,v12,v11
X_ a132,v12
R_ v13,a133
U_ v13,v13,v11
X_ a133,v13
R_ v12,a134
U_ v12,v12,v11
X_ a134,v12
R_ v13,a135
U_ v13,v13,v11
X_ a135,v13
R_ v12,a152
U_ v12,v12,v11
X_ a152,v12
R_ v13,a153
U_ v13,v13,v11
X_ a153,v13
R_ v12,a154
U_ v12,v12,v11
X_ a154,v12
R_ v13,a155
U_ v13,v13,v11
X_ a155,v13
R_ v12,a172
U_ v12,v12,v11
X_ a172,v12
R_ v13,a173
U_ v13,v13,v11
X_ a173,v13
R_ v12,a174
U_ v12,v12,v11
X_ a174,v12
R_ v13,a175
U_ v13,v13,v11
X_ a175,v13
R_ v12,a192
U_ v12,v12,v11
X_ a192,v12
R_ v13,a193
U_ v13,v13,v11
X_ a193,v13
R_ v12,a194
U_ v12,v12,v11
X_ a194,v12
R_ v13,a195
U_ v13,v13,v11
X_ a195,v13
R_ v12,a212
U_ v12,v12,v11
X_ a212,v12
R_ v13,a213
U_ v13,v13,v11
X_ a213,v13
R_ v12,a214
U_ v12,v12,v11
X_ a214,v12
R_ v13,a215
U_ v13,v13,v11
X_ a215,v13
R_ v12,a232
U_ v12,v12,v11
X_ a232,v12
R_ v13,a233
U_ v13,v13,v11
X_ a233,v13
R_ v12,a234
U_ v12,v12,v11
X_ a234,v12
R_ v13,a235
U_ v13,v13,v11
X_ a235,v13
B_ v68,v68,v10
B_ v69,v69,v10
B_ v70,v70,v10
B_ v71,v71,v10
B_ v88,v88,v10
B_ v89,v89,v10
B_ v90,v90,v10
B_ v91,v91,v10
B_ v108,v108,v10
B_ v109,v109,v10
B_ v110,v110,v10
B_ v111,v111,v10
E_ 88
E_ 89
E_ 90
E_ 91
E_ 108
E_ 109
E_ 110
E_ 111
s_nop 0
C_ 111,110,111
C_ 110,108,109
C_ 108,88,89
C_ 109,90,91
v_max3_f32 v10, v92, v93, v94
v_max3_f32 v10, v10, v95, v112
v_max3_f32 v10, v10, v113, v114
v_max_f32 v10, v10, v115
v_mov_b32 v11, v10
s_nop 1
v_permlane16_swap_b32 v10, v11
v_max_f32 v10, v10, v11
v_mov_b32 v11, v10
s_nop 1
v_permlane32_swap_b32 v10, v11
v_max_f32 v10, v10, v11
A_ v10,0x40e00000,v10
v_max_f32 v10, 0, v10
v_exp_f32 v11, -v10
s_nop 0
U_ v50,v50,v11
R_ v12,a16
U_ v12,v12,v11
X_ a16,v12
R_ v13,a17
U_ v13,v13,v11
X_ a17,v13
R_ v12,a18
U_ v12,v12,v11
X_ a18,v12
R_ v13,a19
U_ v13,v13,v11
X_ a19,v13
R_ v12,a36
U_ v12,v12,v11
X_ a36,v12
R_ v13,a37
U_ v13,v13,v11
X_ a37,v13
R_ v12,a38
U_ v12,v12,v11
X_ a38,v12
R_ v13,a39
U_ v13,v13,v11
X_ a39,v13
R_ v12,a56
U_ v12,v12,v11
X_ a56,v12
R_ v13,a57
U_ v13,v13,v11
X_ a57,v13
R_ v12,a58
U_ v12,v12,v11
X_ a58,v12
R_ v13,a59
U_ v13,v13,v11
X_ a59,v13
R_ v12,a76
U_ v12,v12,v11
X_ a76,v12
R_ v13,a77
U_ v13,v13,v11
X_ a77,v13
R_ v12,a78
U_ v12,v12,v11
X_ a78,v12
R_ v13,a79
U_ v13,v13,v11
X_ a79,v13
R_ v12,a96
U_ v12,v12,v11
X_ a96,v12
R_ v13,a97
U_ v13,v13,v11
X_ a97,v13
R_ v12,a98
U_ v12,v12,v11
X_ a98,v12
R_ v13,a99
U_ v13,v13,v11
X_ a99,v13
R_ v12,a116
U_ v12,v12,v11
X_ a116,v12
R_ v13,a117
U_ v13,v13,v11
X_ a117,v13
R_ v12,a118
U_ v12,v12,v11
X_ a118,v12
R_ v13,a119
U_ v13,v13,v11
X_ a119,v13
R_ v12,a136
U_ v12,v12,v11
X_ a136,v12
R_ v13,a137
U_ v13,v13,v11
X_ a137,v13
R_ v12,a138
U_ v12,v12,v11
X_ a138,v12
R_ v13,a139
U_ v13,v13,v11
X_ a139,v13
R_ v12,a156
U_ v12,v12,v11
X_ a156,v12
R_ v13,a157
U_ v13,v13,v11
X_ a157,v13
R_ v12,a158
U_ v12,v12,v11
X_ a158,v12
R_ v13,a159
U_ v13,v13,v11
X_ a159,v13
R_ v12,a176
U_ v12,v12,v11
X_ a176,v12
R_ v13,a177
U_ v13,v13,v11
X_ a177,v13
R_ v12,a178
U_ v12,v12,v11
X_ a178,v12
R_ v13,a179
U_ v13,v13,v11
X_ a179,v13
R_ v12,a196
U_ v12,v12,v11
X_ a196,v12
R_ v13,a197
U_ v13,v13,v11
X_ a197,v13
R_ v12,a198
U_ v12,v12,v11
X_ a198,v12
R_ v13,a199
U_ v13,v13,v11
X_ a199,v13
R_ v12,a216
U_ v12,v12,v11
X_ a216,v12
R_ v13,a217
U_ v13,v13,v11
X_ a217,v13
R_ v12,a218
U_ v12,v12,v11
X_ a218,v12
R_ v13,a219
U_ v13,v13,v11
X_ a219,v13
R_ v12,a236
U_ v12,v12,v11
X_ a236,v12
R_ v13,a237
U_ v13,v13,v11
X_ a237,v13
R_ v12,a238
U_ v12,v12,v11
X_ a238,v12
R_ v13,a239
U_ v13,v13,v11
X_ a239,v13
B_ v72,v72,v10
B_ v73,v73,v10
B_ v74,v74,v10
B_ v75,v75,v10
B_ v92,v92,v10
B_ v93,v93,v10
B_ v94,v94,v10
B_ v95,v95,v10
B_ v112,v112,v10
B_ v113,v113,v10
B_ v114,v114,v10
B_ v115,v115,v10
E_ 92
E_ 93
E_ 94
E_ 95
E_ 112
E_ 113
E_ 114
E_ 115
s_nop 0
C_ 115,114,115
C_ 114,112,113
C_ 112,92,93
C_ 113,94,95
v_mov_b32 v92, 0x3f803f80
v_mov_b32 v93, 0x3f803f80
v_mov_b32 v94, 0x3f803f80
v_mov_b32 v95, 0x3f803f80
s_nop 7
s_lshl_b32 s53, s58, 4
S_ s53,s53,s63
s_cmp_eq_u32 s53, 2
s_cbranch_scc1 .Lstep0_3
s_cmp_eq_u32 s53, 14
s_cbranch_scc1 .Lstep0_15
s_cmp_eq_u32 s53, 18
s_cbranch_scc1 .Lstep1_3
s_cmp_eq_u32 s53, 20
s_cbranch_scc1 .Lstep1_5
s_cmp_eq_u32 s53, 22
s_cbranch_scc1 .Lstep1_7
s_cmp_eq_u32 s53, 24
s_cbranch_scc1 .Lstep1_9
s_cmp_eq_u32 s53, 26
s_cbranch_scc1 .Lstep1_11
s_cmp_eq_u32 s53, 28
s_cbranch_scc1 .Lstep1_13
s_cmp_eq_u32 s53, 30
s_cbranch_scc1 .Lstep1_15
s_cmp_eq_u32 s53, 34
s_cbranch_scc1 .Lstep2_3
s_cmp_eq_u32 s53, 36
s_cbranch_scc1 .Lstep2_5
s_cmp_eq_u32 s53, 38
s_cbranch_scc1 .Lstep2_7
s_cmp_eq_u32 s53, 40
s_cbranch_scc1 .Lstep2_9
s_cmp_eq_u32 s53, 42
s_cbranch_scc1 .Lstep2_11
s_cmp_eq_u32 s53, 44
s_cbranch_scc1 .Lstep2_13
s_cmp_eq_u32 s53, 46
s_cbranch_scc1 .Lstep2_15
s_endpgm
.Lslow1:
W_ 0
s_nop 7
s_nop 7
s_and_b32 s64, s61, 4294959104
s_mul_i32 s53, s63, 12288
S_ s64,s64,s53
R_ v11,a248
v_and_b32 v10, 0xf0, v11
v_mul_u32_u24 v10, 24, v10
v_lshrrev_b32 v11, 8, v11
v_lshl_add_u32 v10, v11, 4, v10
S_ s53,s64,6144
buffer_load_dwordx4 v[96:99], v10, s[32:35], s53 offen offset:0
S_ s53,s64,6144
buffer_load_dwordx4 v[100:103], v10, s[32:35], s53 offen offset:64
S_ s53,s64,6144
buffer_load_dwordx4 v[104:107], v10, s[32:35], s53 offen offset:128
S_ s53,s64,6144
buffer_load_dwordx4 v[108:111], v10, s[32:35], s53 offen offset:192
s_waitcnt vmcnt(0)
Nvvv 116,96,136,56
Nvvv 120,96,140,60
Nvvv 124,96,144,64
Nvvv 128,96,148,68
Nvvv 132,96,152,72
Mvvv 116,100,156
Mvvv 120,100,160
Mvvv 124,100,164
Mvvv 128,100,168
Mvvv 132,100,172
Mvvv 116,104,176
Mvvv 120,104,180
Mvvv 124,104,184
Mvvv 128,104,188
Mvvv 132,104,192
Mvvv 116,108,196
Mvvv 120,108,200
Mvvv 124,108,204
Mvvv 128,108,208
Mvvv 132,108,212
s_nop 7
S_ s53,s64,6144
buffer_load_dwordx4 v[96:99], v10, s[32:35], s53 offen offset:256
S_ s53,s64,6144
buffer_load_dwordx4 v[100:103], v10, s[32:35], s53 offen offset:320
S_ s53,s64,0
buffer_load_dwordx4 v[104:107], v10, s[32:35], s53 offen offset:0
S_ s53,s64,0
buffer_load_dwordx4 v[108:111], v10, s[32:35], s53 offen offset:64
s_waitcnt vmcnt(0)
Mvvv 116,96,216
Mvvv 120,96,220
Mvvv 124,96,224
Mvvv 128,96,228
Mvvv 132,96,232
Mvvv 116,100,236
Mvvv 120,100,240
Mvvv 124,100,244
Mvvv 128,100,248
Mvvv 132,100,252
Nvvv 76,104,136,56
Nvvv 80,104,140,60
Nvvv 84,104,144,64
Nvvv 88,104,148,68
Nvvv 92,104,152,72
Mvvv 76,108,156
Mvvv 80,108,160
Mvvv 84,108,164
Mvvv 88,108,168
Mvvv 92,108,172
s_nop 7
S_ s53,s64,0
buffer_load_dwordx4 v[96:99], v10, s[32:35], s53 offen offset:128
S_ s53,s64,0
buffer_load_dwordx4 v[100:103], v10, s[32:35], s53 offen offset:192
S_ s53,s64,0
buffer_load_dwordx4 v[104:107], v10, s[32:35], s53 offen offset:256
S_ s53,s64,0
buffer_load_dwordx4 v[108:111], v10, s[32:35], s53 offen offset:320
s_waitcnt vmcnt(0)
Mvvv 76,96,176
Mvvv 80,96,180
Mvvv 84,96,184
Mvvv 88,96,188
Mvvv 92,96,192
Mvvv 76,100,196
Mvvv 80,100,200
Mvvv 84,100,204
Mvvv 88,100,208
Mvvv 92,100,212
Mvvv 76,104,216
Mvvv 80,104,220
Mvvv 84,104,224
Mvvv 88,104,228
Mvvv 92,104,232
Mvvv 76,108,236
Mvvv 80,108,240
Mvvv 84,108,244
Mvvv 88,108,248
Mvvv 92,108,252
s_nop 7
s_nop 2
v_max3_f32 v10, v76, v77, v78
v_max3_f32 v10, v10, v79, v116
v_max3_f32 v10, v10, v117, v118
v_max_f32 v10, v10, v119
v_mov_b32 v11, v10
s_nop 1
v_permlane16_swap_b32 v10, v11
v_max_f32 v10, v10, v11
v_mov_b32 v11, v10
s_nop 1
v_permlane32_swap_b32 v10, v11
v_max_f32 v10, v10, v11
A_ v10,0x40e00000,v10
v_max_f32 v10, 0, v10
v_exp_f32 v11, -v10
s_nop 0
U_ v46,v46,v11
R_ v12,a0
U_ v12,v12,v11
X_ a0,v12
R_ v13,a1
U_ v13,v13,v11
X_ a1,v13
R_ v12,a2
U_ v12,v12,v11
X_ a2,v12
R_ v13,a3
U_ v13,v13,v11
X_ a3,v13
R_ v12,a20
U_ v12,v12,v11
X_ a20,v12
R_ v13,a21
U_ v13,v13,v11
X_ a21,v13
R_ v12,a22
U_ v12,v12,v11
X_ a22,v12
R_ v13,a23
U_ v13,v13,v11
X_ a23,v13
R_ v12,a40
U_ v12,v12,v11
X_ a40,v12
R_ v13,a41
U_ v13,v13,v11
X_ a41,v13
R_ v12,a42
U_ v12,v12,v11
X_ a42,v12
R_ v13,a43
U_ v13,v13,v11
X_ a43,v13
R_ v12,a60
U_ v12,v12,v11
X_ a60,v12
R_ v13,a61
U_ v13,v13,v11
X_ a61,v13
R_ v12,a62
U_ v12,v12,v11
X_ a62,v12
R_ v13,a63
U_ v13,v13,v11
X_ a63,v13
R_ v12,a80
U_ v12,v12,v11
X_ a80,v12
R_ v13,a81
U_ v13,v13,v11
X_ a81,v13
R_ v12,a82
U_ v12,v12,v11
X_ a82,v12
R_ v13,a83
U_ v13,v13,v11
X_ a83,v13
R_ v12,a100
U_ v12,v12,v11
X_ a100,v12
R_ v13,a101
U_ v13,v13,v11
X_ a101,v13
R_ v12,a102
U_ v12,v12,v11
X_ a102,v12
R_ v13,a103
U_ v13,v13,v11
X_ a103,v13
R_ v12,a120
U_ v12,v12,v11
X_ a120,v12
R_ v13,a121
U_ v13,v13,v11
X_ a121,v13
R_ v12,a122
U_ v12,v12,v11
X_ a122,v12
R_ v13,a123
U_ v13,v13,v11
X_ a123,v13
R_ v12,a140
U_ v12,v12,v11
X_ a140,v12
R_ v13,a141
U_ v13,v13,v11
X_ a141,v13
R_ v12,a142
U_ v12,v12,v11
X_ a142,v12
R_ v13,a143
U_ v13,v13,v11
X_ a143,v13
R_ v12,a160
U_ v12,v12,v11
X_ a160,v12
R_ v13,a161
U_ v13,v13,v11
X_ a161,v13
R_ v12,a162
U_ v12,v12,v11
X_ a162,v12
R_ v13,a163
U_ v13,v13,v11
X_ a163,v13
R_ v12,a180
U_ v12,v12,v11
X_ a180,v12
R_ v13,a181
U_ v13,v13,v11
X_ a181,v13
R_ v12,a182
U_ v12,v12,v11
X_ a182,v12
R_ v13,a183
U_ v13,v13,v11
X_ a183,v13
R_ v12,a200
U_ v12,v12,v11
X_ a200,v12
R_ v13,a201
U_ v13,v13,v11
X_ a201,v13
R_ v12,a202
U_ v12,v12,v11
X_ a202,v12
R_ v13,a203
U_ v13,v13,v11
X_ a203,v13
R_ v12,a220
U_ v12,v12,v11
X_ a220,v12
R_ v13,a221
U_ v13,v13,v11
X_ a221,v13
R_ v12,a222
U_ v12,v12,v11
X_ a222,v12
R_ v13,a223
U_ v13,v13,v11
X_ a223,v13
B_ v56,v56,v10
B_ v57,v57,v10
B_ v58,v58,v10
B_ v59,v59,v10
B_ v76,v76,v10
B_ v77,v77,v10
B_ v78,v78,v10
B_ v79,v79,v10
B_ v116,v116,v10
B_ v117,v117,v10
B_ v118,v118,v10
B_ v119,v119,v10
E_ 76
E_ 77
E_ 78
E_ 79
E_ 116
E_ 117
E_ 118
E_ 119
s_nop 0
C_ 119,118,119
C_ 118,116,117
C_ 116,76,77
C_ 117,78,79
v_max3_f32 v10, v80, v81, v82
v_max3_f32 v10, v10, v83, v120
v_max3_f32 v10, v10, v121, v122
v_max_f32 v10, v10, v123
v_mov_b32 v11, v10
s_nop 1
v_permlane16_swap_b32 v10, v11
v_max_f32 v10, v10, v11
v_mov_b32 v11, v10
s_nop 1
v_permlane32_swap_b32 v10, v11
v_max_f32 v10, v10, v11
A_ v10,0x40e00000,v10
v_max_f32 v10, 0, v10
v_exp_f32 v11, -v10
s_nop 0
U_ v47,v47,v11
R_ v12,a4
U_ v12,v12,v11
X_ a4,v12
R_ v13,a5
U_ v13,v13,v11
X_ a5,v13
R_ v12,a6
U_ v12,v12,v11
X_ a6,v12
R_ v13,a7
U_ v13,v13,v11
X_ a7,v13
R_ v12,a24
U_ v12,v12,v11
X_ a24,v12
R_ v13,a25
U_ v13,v13,v11
X_ a25,v13
R_ v12,a26
U_ v12,v12,v11
X_ a26,v12
R_ v13,a27
U_ v13,v13,v11
X_ a27,v13
R_ v12,a44
U_ v12,v12,v11
X_ a44,v12
R_ v13,a45
U_ v13,v13,v11
X_ a45,v13
R_ v12,a46
U_ v12,v12,v11
X_ a46,v12
R_ v13,a47
U_ v13,v13,v11
X_ a47,v13
R_ v12,a64
U_ v12,v12,v11
X_ a64,v12
R_ v13,a65
U_ v13,v13,v11
X_ a65,v13
R_ v12,a66
U_ v12,v12,v11
X_ a66,v12
R_ v13,a67
U_ v13,v13,v11
X_ a67,v13
R_ v12,a84
U_ v12,v12,v11
X_ a84,v12
R_ v13,a85
U_ v13,v13,v11
X_ a85,v13
R_ v12,a86
U_ v12,v12,v11
X_ a86,v12
R_ v13,a87
U_ v13,v13,v11
X_ a87,v13
R_ v12,a104
U_ v12,v12,v11
X_ a104,v12
R_ v13,a105
U_ v13,v13,v11
X_ a105,v13
R_ v12,a106
U_ v12,v12,v11
X_ a106,v12
R_ v13,a107
U_ v13,v13,v11
X_ a107,v13
R_ v12,a124
U_ v12,v12,v11
X_ a124,v12
R_ v13,a125
U_ v13,v13,v11
X_ a125,v13
R_ v12,a126
U_ v12,v12,v11
X_ a126,v12
R_ v13,a127
U_ v13,v13,v11
X_ a127,v13
R_ v12,a144
U_ v12,v12,v11
X_ a144,v12
R_ v13,a145
U_ v13,v13,v11
X_ a145,v13
R_ v12,a146
U_ v12,v12,v11
X_ a146,v12
R_ v13,a147
U_ v13,v13,v11
X_ a147,v13
R_ v12,a164
U_ v12,v12,v11
X_ a164,v12
R_ v13,a165
U_ v13,v13,v11
X_ a165,v13
R_ v12,a166
U_ v12,v12,v11
X_ a166,v12
R_ v13,a167
U_ v13,v13,v11
X_ a167,v13
R_ v12,a184
U_ v12,v12,v11
X_ a184,v12
R_ v13,a185
U_ v13,v13,v11
X_ a185,v13
R_ v12,a186
U_ v12,v12,v11
X_ a186,v12
R_ v13,a187
U_ v13,v13,v11
X_ a187,v13
R_ v12,a204
U_ v12,v12,v11
X_ a204,v12
R_ v13,a205
U_ v13,v13,v11
X_ a205,v13
R_ v12,a206
U_ v12,v12,v11
X_ a206,v12
R_ v13,a207
U_ v13,v13,v11
X_ a207,v13
R_ v12,a224
U_ v12,v12,v11
X_ a224,v12
R_ v13,a225
U_ v13,v13,v11
X_ a225,v13
R_ v12,a226
U_ v12,v12,v11
X_ a226,v12
R_ v13,a227
U_ v13,v13,v11
X_ a227,v13
B_ v60,v60,v10
B_ v61,v61,v10
B_ v62,v62,v10
B_ v63,v63,v10
B_ v80,v80,v10
B_ v81,v81,v10
B_ v82,v82,v10
B_ v83,v83,v10
B_ v120,v120,v10
B_ v121,v121,v10
B_ v122,v122,v10
B_ v123,v123,v10
E_ 80
E_ 81
E_ 82
E_ 83
E_ 120
E_ 121
E_ 122
E_ 123
s_nop 0
C_ 123,122,123
C_ 122,120,121
C_ 120,80,81
C_ 121,82,83
v_max3_f32 v10, v84, v85, v86
v_max3_f32 v10, v10, v87, v124
v_max3_f32 v10, v10, v125, v126
v_max_f32 v10, v10, v127
v_mov_b32 v11, v10
s_nop 1
v_permlane16_swap_b32 v10, v11
v_max_f32 v10, v10, v11
v_mov_b32 v11, v10
s_nop 1
v_permlane32_swap_b32 v10, v11
v_max_f32 v10, v10, v11
A_ v10,0x40e00000,v10
v_max_f32 v10, 0, v10
v_exp_f32 v11, -v10
s_nop 0
U_ v48,v48,v11
R_ v12,a8
U_ v12,v12,v11
X_ a8,v12
R_ v13,a9
U_ v13,v13,v11
X_ a9,v13
R_ v12,a10
U_ v12,v12,v11
X_ a10,v12
R_ v13,a11
U_ v13,v13,v11
X_ a11,v13
R_ v12,a28
U_ v12,v12,v11
X_ a28,v12
R_ v13,a29
U_ v13,v13,v11
X_ a29,v13
R_ v12,a30
U_ v12,v12,v11
X_ a30,v12
R_ v13,a31
U_ v13,v13,v11
X_ a31,v13
R_ v12,a48
U_ v12,v12,v11
X_ a48,v12
R_ v13,a49
U_ v13,v13,v11
X_ a49,v13
R_ v12,a50
U_ v12,v12,v11
X_ a50,v12
R_ v13,a51
U_ v13,v13,v11
X_ a51,v13
R_ v12,a68
U_ v12,v12,v11
X_ a68,v12
R_ v13,a69
U_ v13,v13,v11
X_ a69,v13
R_ v12,a70
U_ v12,v12,v11
X_ a70,v12
R_ v13,a71
U_ v13,v13,v11
X_ a71,v13
R_ v12,a88
U_ v12,v12,v11
X_ a88,v12
R_ v13,a89
U_ v13,v13,v11
X_ a89,v13
R_ v12,a90
U_ v12,v12,v11
X_ a90,v12
R_ v13,a91
U_ v13,v13,v11
X_ a91,v13
R_ v12,a108
U_ v12,v12,v11
X_ a108,v12
R_ v13,a109
U_ v13,v13,v11
X_ a109,v13
R_ v12,a110
U_ v12,v12,v11
X_ a110,v12
R_ v13,a111
U_ v13,v13,v11
X_ a111,v13
R_ v12,a128
U_ v12,v12,v11
X_ a128,v12
R_ v13,a129
U_ v13,v13,v11
X_ a129,v13
R_ v12,a130
U_ v12,v12,v11
X_ a130,v12
R_ v13,a131
U_ v13,v13,v11
X_ a131,v13
R_ v12,a148
U_ v12,v12,v11
X_ a148,v12
R_ v13,a149
U_ v13,v13,v11
X_ a149,v13
R_ v12,a150
U_ v12,v12,v11
X_ a150,v12
R_ v13,a151
U_ v13,v13,v11
X_ a151,v13
R_ v12,a168
U_ v12,v12,v11
X_ a168,v12
R_ v13,a169
U_ v13,v13,v11
X_ a169,v13
R_ v12,a170
U_ v12,v12,v11
X_ a170,v12
R_ v13,a171
U_ v13,v13,v11
X_ a171,v13
R_ v12,a188
U_ v12,v12,v11
X_ a188,v12
R_ v13,a189
U_ v13,v13,v11
X_ a189,v13
R_ v12,a190
U_ v12,v12,v11
X_ a190,v12
R_ v13,a191
U_ v13,v13,v11
X_ a191,v13
R_ v12,a208
U_ v12,v12,v11
X_ a208,v12
R_ v13,a209
U_ v13,v13,v11
X_ a209,v13
R_ v12,a210
U_ v12,v12,v11
X_ a210,v12
R_ v13,a211
U_ v13,v13,v11
X_ a211,v13
R_ v12,a228
U_ v12,v12,v11
X_ a228,v12
R_ v13,a229
U_ v13,v13,v11
X_ a229,v13
R_ v12,a230
U_ v12,v12,v11
X_ a230,v12
R_ v13,a231
U_ v13,v13,v11
X_ a231,v13
B_ v64,v64,v10
B_ v65,v65,v10
B_ v66,v66,v10
B_ v67,v67,v10
B_ v84,v84,v10
B_ v85,v85,v10
B_ v86,v86,v10
B_ v87,v87,v10
B_ v124,v124,v10
B_ v125,v125,v10
B_ v126,v126,v10
B_ v127,v127,v10
E_ 84
E_ 85
E_ 86
E_ 87
E_ 124
E_ 125
E_ 126
E_ 127
s_nop 0
C_ 127,126,127
C_ 126,124,125
C_ 124,84,85
C_ 125,86,87
v_max3_f32 v10, v88, v89, v90
v_max3_f32 v10, v10, v91, v128
v_max3_f32 v10, v10, v129, v130
v_max_f32 v10, v10, v131
v_mov_b32 v11, v10
s_nop 1
v_permlane16_swap_b32 v10, v11
v_max_f32 v10, v10, v11
v_mov_b32 v11, v10
s_nop 1
v_permlane32_swap_b32 v10, v11
v_max_f32 v10, v10, v11
A_ v10,0x40e00000,v10
v_max_f32 v10, 0, v10
v_exp_f32 v11, -v10
s_nop 0
U_ v49,v49,v11
R_ v12,a12
U_ v12,v12,v11
X_ a12,v12
R_ v13,a13
U_ v13,v13,v11
X_ a13,v13
R_ v12,a14
U_ v12,v12,v11
X_ a14,v12
R_ v13,a15
U_ v13,v13,v11
X_ a15,v13
R_ v12,a32
U_ v12,v12,v11
X_ a32,v12
R_ v13,a33
U_ v13,v13,v11
X_ a33,v13
R_ v12,a34
U_ v12,v12,v11
X_ a34,v12
R_ v13,a35
U_ v13,v13,v11
X_ a35,v13
R_ v12,a52
U_ v12,v12,v11
X_ a52,v12
R_ v13,a53
U_ v13,v13,v11
X_ a53,v13
R_ v12,a54
U_ v12,v12,v11
X_ a54,v12
R_ v13,a55
U_ v13,v13,v11
X_ a55,v13
R_ v12,a72
U_ v12,v12,v11
X_ a72,v12
R_ v13,a73
U_ v13,v13,v11
X_ a73,v13
R_ v12,a74
U_ v12,v12,v11
X_ a74,v12
R_ v13,a75
U_ v13,v13,v11
X_ a75,v13
R_ v12,a92
U_ v12,v12,v11
X_ a92,v12
R_ v13,a93
U_ v13,v13,v11
X_ a93,v13
R_ v12,a94
U_ v12,v12,v11
X_ a94,v12
R_ v13,a95
U_ v13,v13,v11
X_ a95,v13
R_ v12,a112
U_ v12,v12,v11
X_ a112,v12
R_ v13,a113
U_ v13,v13,v11
X_ a113,v13
R_ v12,a114
U_ v12,v12,v11
X_ a114,v12
R_ v13,a115
U_ v13,v13,v11
X_ a115,v13
R_ v12,a132
U_ v12,v12,v11
X_ a132,v12
R_ v13,a133
U_ v13,v13,v11
X_ a133,v13
R_ v12,a134
U_ v12,v12,v11
X_ a134,v12
R_ v13,a135
U_ v13,v13,v11
X_ a135,v13
R_ v12,a152
U_ v12,v12,v11
X_ a152,v12
R_ v13,a153
U_ v13,v13,v11
X_ a153,v13
R_ v12,a154
U_ v12,v12,v11
X_ a154,v12
R_ v13,a155
U_ v13,v13,v11
X_ a155,v13
R_ v12,a172
U_ v12,v12,v11
X_ a172,v12
R_ v13,a173
U_ v13,v13,v11
X_ a173,v13
R_ v12,a174
U_ v12,v12,v11
X_ a174,v12
R_ v13,a175
U_ v13,v13,v11
X_ a175,v13
R_ v12,a192
U_ v12,v12,v11
X_ a192,v12
R_ v13,a193
U_ v13,v13,v11
X_ a193,v13
R_ v12,a194
U_ v12,v12,v11
X_ a194,v12
R_ v13,a195
U_ v13,v13,v11
X_ a195,v13
R_ v12,a212
U_ v12,v12,v11
X_ a212,v12
R_ v13,a213
U_ v13,v13,v11
X_ a213,v13
R_ v12,a214
U_ v12,v12,v11
X_ a214,v12
R_ v13,a215
U_ v13,v13,v11
X_ a215,v13
R_ v12,a232
U_ v12,v12,v11
X_ a232,v12
R_ v13,a233
U_ v13,v13,v11
X_ a233,v13
R_ v12,a234
U_ v12,v12,v11
X_ a234,v12
R_ v13,a235
U_ v13,v13,v11
X_ a235,v13
B_ v68,v68,v10
B_ v69,v69,v10
B_ v70,v70,v10
B_ v71,v71,v10
B_ v88,v88,v10
B_ v89,v89,v10
B_ v90,v90,v10
B_ v91,v91,v10
B_ v128,v128,v10
B_ v129,v129,v10
B_ v130,v130,v10
B_ v131,v131,v10
E_ 88
E_ 89
E_ 90
E_ 91
E_ 128
E_ 129
E_ 130
E_ 131
s_nop 0
C_ 131,130,131
C_ 130,128,129
C_ 128,88,89
C_ 129,90,91
v_max3_f32 v10, v92, v93, v94
v_max3_f32 v10, v10, v95, v132
v_max3_f32 v10, v10, v133, v134
v_max_f32 v10, v10, v135
v_mov_b32 v11, v10
s_nop 1
v_permlane16_swap_b32 v10, v11
v_max_f32 v10, v10, v11
v_mov_b32 v11, v10
s_nop 1
v_permlane32_swap_b32 v10, v11
v_max_f32 v10, v10, v11
A_ v10,0x40e00000,v10
v_max_f32 v10, 0, v10
v_exp_f32 v11, -v10
s_nop 0
U_ v50,v50,v11
R_ v12,a16
U_ v12,v12,v11
X_ a16,v12
R_ v13,a17
U_ v13,v13,v11
X_ a17,v13
R_ v12,a18
U_ v12,v12,v11
X_ a18,v12
R_ v13,a19
U_ v13,v13,v11
X_ a19,v13
R_ v12,a36
U_ v12,v12,v11
X_ a36,v12
R_ v13,a37
U_ v13,v13,v11
X_ a37,v13
R_ v12,a38
U_ v12,v12,v11
X_ a38,v12
R_ v13,a39
U_ v13,v13,v11
X_ a39,v13
R_ v12,a56
U_ v12,v12,v11
X_ a56,v12
R_ v13,a57
U_ v13,v13,v11
X_ a57,v13
R_ v12,a58
U_ v12,v12,v11
X_ a58,v12
R_ v13,a59
U_ v13,v13,v11
X_ a59,v13
R_ v12,a76
U_ v12,v12,v11
X_ a76,v12
R_ v13,a77
U_ v13,v13,v11
X_ a77,v13
R_ v12,a78
U_ v12,v12,v11
X_ a78,v12
R_ v13,a79
U_ v13,v13,v11
X_ a79,v13
R_ v12,a96
U_ v12,v12,v11
X_ a96,v12
R_ v13,a97
U_ v13,v13,v11
X_ a97,v13
R_ v12,a98
U_ v12,v12,v11
X_ a98,v12
R_ v13,a99
U_ v13,v13,v11
X_ a99,v13
R_ v12,a116
U_ v12,v12,v11
X_ a116,v12
R_ v13,a117
U_ v13,v13,v11
X_ a117,v13
R_ v12,a118
U_ v12,v12,v11
X_ a118,v12
R_ v13,a119
U_ v13,v13,v11
X_ a119,v13
R_ v12,a136
U_ v12,v12,v11
X_ a136,v12
R_ v13,a137
U_ v13,v13,v11
X_ a137,v13
R_ v12,a138
U_ v12,v12,v11
X_ a138,v12
R_ v13,a139
U_ v13,v13,v11
X_ a139,v13
R_ v12,a156
U_ v12,v12,v11
X_ a156,v12
R_ v13,a157
U_ v13,v13,v11
X_ a157,v13
R_ v12,a158
U_ v12,v12,v11
X_ a158,v12
R_ v13,a159
U_ v13,v13,v11
X_ a159,v13
R_ v12,a176
U_ v12,v12,v11
X_ a176,v12
R_ v13,a177
U_ v13,v13,v11
X_ a177,v13
R_ v12,a178
U_ v12,v12,v11
X_ a178,v12
R_ v13,a179
U_ v13,v13,v11
X_ a179,v13
R_ v12,a196
U_ v12,v12,v11
X_ a196,v12
R_ v13,a197
U_ v13,v13,v11
X_ a197,v13
R_ v12,a198
U_ v12,v12,v11
X_ a198,v12
R_ v13,a199
U_ v13,v13,v11
X_ a199,v13
R_ v12,a216
U_ v12,v12,v11
X_ a216,v12
R_ v13,a217
U_ v13,v13,v11
X_ a217,v13
R_ v12,a218
U_ v12,v12,v11
X_ a218,v12
R_ v13,a219
U_ v13,v13,v11
X_ a219,v13
R_ v12,a236
U_ v12,v12,v11
X_ a236,v12
R_ v13,a237
U_ v13,v13,v11
X_ a237,v13
R_ v12,a238
U_ v12,v12,v11
X_ a238,v12
R_ v13,a239
U_ v13,v13,v11
X_ a239,v13
B_ v72,v72,v10
B_ v73,v73,v10
B_ v74,v74,v10
B_ v75,v75,v10
B_ v92,v92,v10
B_ v93,v93,v10
B_ v94,v94,v10
B_ v95,v95,v10
B_ v132,v132,v10
B_ v133,v133,v10
B_ v134,v134,v10
B_ v135,v135,v10
E_ 92
E_ 93
E_ 94
E_ 95
E_ 132
E_ 133
E_ 134
E_ 135
s_nop 0
C_ 135,134,135
C_ 134,132,133
C_ 132,92,93
C_ 133,94,95
v_mov_b32 v92, 0x3f803f80
v_mov_b32 v93, 0x3f803f80
v_mov_b32 v94, 0x3f803f80
v_mov_b32 v95, 0x3f803f80
s_nop 7
s_lshl_b32 s53, s58, 4
S_ s53,s53,s63
s_cmp_eq_u32 s53, 1
s_cbranch_scc1 .Lstep0_2
s_cmp_eq_u32 s53, 13
s_cbranch_scc1 .Lstep0_14
s_cmp_eq_u32 s53, 15
s_cbranch_scc1 .Lstep0_16
s_cmp_eq_u32 s53, 17
s_cbranch_scc1 .Lstep1_2
s_cmp_eq_u32 s53, 19
s_cbranch_scc1 .Lstep1_4
s_cmp_eq_u32 s53, 21
s_cbranch_scc1 .Lstep1_6
s_cmp_eq_u32 s53, 23
s_cbranch_scc1 .Lstep1_8
s_cmp_eq_u32 s53, 25
s_cbranch_scc1 .Lstep1_10
s_cmp_eq_u32 s53, 27
s_cbranch_scc1 .Lstep1_12
s_cmp_eq_u32 s53, 29
s_cbranch_scc1 .Lstep1_14
s_cmp_eq_u32 s53, 31
s_cbranch_scc1 .Lstep1_16
s_cmp_eq_u32 s53, 33
s_cbranch_scc1 .Lstep2_2
s_cmp_eq_u32 s53, 35
s_cbranch_scc1 .Lstep2_4
s_cmp_eq_u32 s53, 37
s_cbranch_scc1 .Lstep2_6
s_cmp_eq_u32 s53, 39
s_cbranch_scc1 .Lstep2_8
s_cmp_eq_u32 s53, 41
s_cbranch_scc1 .Lstep2_10
s_cmp_eq_u32 s53, 43
s_cbranch_scc1 .Lstep2_12
s_cmp_eq_u32 s53, 45
s_cbranch_scc1 .Lstep2_14
s_cmp_eq_u32 s53, 47
s_cbranch_scc1 .Lstep2_16
s_endpgm
.Lxs2m:
s_sleep 5
s_branch .Lxs2m_back
.Lxs2e:
s_sleep 5
s_branch .Lxs2e_back
.Lxs3m:
s_sleep 5
s_branch .Lxs3m_back
.Lxs3e:
s_sleep 5
s_branch .Lxs3e_back
.Lxs14m:
s_sleep 7
s_branch .Lxs14m_back
.Lxs14e:
s_sleep 7
s_branch .Lxs14e_back
.Lxs15m:
s_sleep 7
s_branch .Lxs15m_back
.Lxs15e:
s_sleep 7
s_branch .Lxs15e_back
.Lqsl2:
s_sleep 2
s_branch .Lqsl2_back
.Lqsl4:
s_sleep 2
s_branch .Lqsl4_back
.Lqsl6:
s_sleep 2
s_branch .Lqsl6_back
.Lqsl8:
s_sleep 2
s_branch .Lqsl8_back
.Lqsl10:
s_sleep 2
s_branch .Lqsl10_back
.Lqsl12:
s_sleep 2
s_branch .Lqsl12_back
.Lqsl14:
s_sleep 2
s_branch .Lqsl14_back
.Lpace1:
S_ s12,s12,s13
.Lpace1_l:
s_memrealtime s[100:101]
W_ 0
s_sub_u32 s100, s100, s98
s_cmp_ge_u32 s100, s12
s_cbranch_scc1 .Lpace1_back
s_sleep 2
s_branch .Lpace1_l
.Lpace3:
S_ s12,s12,s13
.Lpace3_l:
s_memrealtime s[100:101]
W_ 0
s_sub_u32 s100, s100, s98
s_cmp_ge_u32 s100, s12
s_cbranch_scc1 .Lpace3_back
s_sleep 2
s_branch .Lpace3_l
.Lpace5:
S_ s12,s12,s13
.Lpace5_l:
s_memrealtime s[100:101]
W_ 0
s_sub_u32 s100, s100, s98
s_cmp_ge_u32 s100, s12
s_cbranch_scc1 .Lpace5_back
s_sleep 2
s_branch .Lpace5_l
.Lpace7:
S_ s12,s12,s13
.Lpace7_l:
s_memrealtime s[100:101]
W_ 0
s_sub_u32 s100, s100, s98
s_cmp_ge_u32 s100, s12
s_cbranch_scc1 .Lpace7_back
s_sleep 2
s_branch .Lpace7_l
.Lpace9:
S_ s12,s12,s13
.Lpace9_l:
s_memrealtime s[100:101]
W_ 0
s_sub_u32 s100, s100, s98
s_cmp_ge_u32 s100, s12
s_cbranch_scc1 .Lpace9_back
s_sleep 2
s_branch .Lpace9_l
.Lpace11:
S_ s12,s12,s13
.Lpace11_l:
s_memrealtime s[100:101]
W_ 0
s_sub_u32 s100, s100, s98
s_cmp_ge_u32 s100, s12
s_cbranch_scc1 .Lpace11_back
s_sleep 2
s_branch .Lpace11_l
.Lpace13:
S_ s12,s12,s13
.Lpace13_l:
s_memrealtime s[100:101]
W_ 0
s_sub_u32 s100, s100, s98
s_cmp_ge_u32 s100, s12
s_cbranch_scc1 .Lpace13_back
s_sleep 2
s_branch .Lpace13_l
.Lpace15:
S_ s12,s12,s13
.Lpace15_l:
s_memrealtime s[100:101]
W_ 0
s_sub_u32 s100, s100, s98
s_cmp_ge_u32 s100, s12
s_cbranch_scc1 .Lpace15_back
s_sleep 2
s_branch .Lpace15_l
.Lpace17:
S_ s12,s12,s13
.Lpace17_l:
s_memrealtime s[100:101]
W_ 0
s_sub_u32 s100, s100, s98
s_cmp_ge_u32 s100, s12
s_cbranch_scc1 .Lpace17_back
s_sleep 2
s_branch .Lpace17_l
.Lpace19:
S_ s12,s12,s13
.Lpace19_l:
s_memrealtime s[100:101]
W_ 0
s_sub_u32 s100, s100, s98
s_cmp_ge_u32 s100, s12
s_cbranch_scc1 .Lpace19_back
s_sleep 2
s_branch .Lpace19_l
.Lpace21:
S_ s12,s12,s13
.Lpace21_l:
s_memrealtime s[100:101]
W_ 0
s_sub_u32 s100, s100, s98
s_cmp_ge_u32 s100, s12
s_cbranch_scc1 .Lpace21_back
s_sleep 2
s_branch .Lpace21_l
.Lpace23:
S_ s12,s12,s13
.Lpace23_l:
s_memrealtime s[100:101]
W_ 0
s_sub_u32 s100, s100, s98
s_cmp_ge_u32 s100, s12
s_cbranch_scc1 .Lpace23_back
s_sleep 2
s_branch .Lpace23_l
.Lpace25:
S_ s12,s12,s13
.Lpace25_l:
s_memrealtime s[100:101]
W_ 0
s_sub_u32 s100, s100, s98
s_cmp_ge_u32 s100, s12
s_cbranch_scc1 .Lpace25_back
s_sleep 2
s_branch .Lpace25_l
.Lpace27:
S_ s12,s12,s13
.Lpace27_l:
s_memrealtime s[100:101]
W_ 0
s_sub_u32 s100, s100, s98
s_cmp_ge_u32 s100, s12
s_cbranch_scc1 .Lpace27_back
s_sleep 2
s_branch .Lpace27_l
.Lpace29:
S_ s12,s12,s13
.Lpace29_l:
s_memrealtime s[100:101]
W_ 0
s_sub_u32 s100, s100, s98
s_cmp_ge_u32 s100, s12
s_cbranch_scc1 .Lpace29_back
s_sleep 2
s_branch .Lpace29_l
.Lhsd0_0:
s_bitcmp1_b32 s67, 13
s_cbranch_scc1 .Lhs_a0
s_bitcmp1_b32 s67, 11
s_cbranch_scc1 .Lhs_c0
s_branch .Lhs_d0
.Lhs_a0:
s_nop 4
s_waitcnt vmcnt(12)
R_ v10,a251
buffer_store_dword a255, v10, s[92:95], 0 offen offset:128 sc1
s_branch .Lhsd0_0_back
.Lhs_a1:
s_nop 4
R_ v11,a248
v_lshrrev_b32 v11, 2, v11
buffer_load_dword a254, v11, s[92:95], s96 offen offset:128 sc1
s_branch .Lhs_a1_back
.Lhs_a2:
s_nop 4
s_waitcnt vmcnt(12)
R_ v10,a251
buffer_store_dword a255, v10, s[92:95], 0 offen offset:128 sc1
s_branch .Lhs_a2_back
.Lhs_b1:
s_nop 4
s_waitcnt vmcnt(12)
R_ v11,a254
v_cmp_eq_u32 vcc, s62, v11
R_ v10,a251
v_mov_b32 v11, 0x7fff0000
v_cndmask_b32 v10, v11, v10, vcc
R_ v11,a253
v_add_u32 v11, 1, v11
buffer_store_dword v11, v10, s[92:95], 0 offen offset:256 sc1
s_and_b32 s53, vcc_lo, 255
s_cmp_eq_u32 s53, 0xFF
s_cselect_b32 s97, 1, 0
s_branch .Lhs_b1_back
.Lhs_c0:
s_nop 4
R_ v11,a248
v_lshrrev_b32 v11, 2, v11
buffer_load_dword a254, v11, s[92:95], s96 offen offset:128 sc1
s_branch .Lhsd0_0_back
.Lhs_c1:
s_nop 4
R_ v11,a248
v_lshrrev_b32 v11, 2, v11
buffer_load_dword a254, v11, s[92:95], s96 offen offset:256 sc1
s_branch .Lhs_c1_back
.Lhs_c2:
s_nop 4
R_ v11,a248
v_lshrrev_b32 v11, 2, v11
buffer_load_dword a254, v11, s[92:95], s96 offen offset:128 sc1
s_branch .Lhs_c2_back
.Lhs_d0:
s_nop 4
s_waitcnt vmcnt(12)
R_ v11,a254
v_cmp_eq_u32 vcc, s62, v11
R_ v10,a251
v_mov_b32 v11, 0x7fff0000
v_cndmask_b32 v10, v11, v10, vcc
R_ v11,a253
v_add_u32 v11, 1, v11
buffer_store_dword v11, v10, s[92:95], 0 offen offset:256 sc1
s_and_b32 s53, vcc_lo, 255
s_cmp_eq_u32 s53, 0xFF
s_cselect_b32 s97, 1, 0
s_branch .Lhsd0_0_back
.Lhs_d2:
s_nop 4
s_waitcnt vmcnt(12)
R_ v11,a254
v_cmp_eq_u32 vcc, s62, v11
R_ v10,a251
v_mov_b32 v11, 0x7fff0000
v_cndmask_b32 v10, v11, v10, vcc
R_ v11,a253
v_add_u32 v11, 1, v11
buffer_store_dword v11, v10, s[92:95], 0 offen offset:256 sc1
s_and_b32 s53, vcc_lo, 255
s_cmp_eq_u32 s53, 0xFF
s_cselect_b32 s97, 1, 0
s_branch .Lhs_d2_back
.Lhs_d1:
s_nop 4
s_waitcnt vmcnt(12)
R_ v11,a254
R_ v10,a252
v_cmp_ne_u32 vcc, v11, v10
s_nop 1
s_and_b32 s53, vcc_lo, 255
s_cmp_eq_u32 s53, 0xFF
s_cselect_b32 s53, s97, 0
s_cmp_eq_u32 s53, 1
s_cbranch_scc1 .Lhs_ok
.Lhs_spin:
s_cmp_eq_u32 s57, 0
s_cbranch_scc1 .Lhs_ok
s_sub_u32 s57, s57, 1
s_sleep 8
R_ v10,a251
s_lshl_b32 s53, s6, 2
v_subrev_u32 v10, s53, v10
buffer_load_dword v11, v10, s[92:95], 0 offen offset:0 sc1
R_ v10,a248
v_lshrrev_b32 v10, 2, v10
buffer_load_dword v12, v10, s[92:95], s96 offen offset:128 sc1
buffer_load_dword v13, v10, s[92:95], s96 offen offset:256 sc1
s_waitcnt vmcnt(0)
X_ a255,v11
R_ v10,a251
buffer_store_dword v11, v10, s[92:95], 0 offen offset:128 sc1
s_nop 1
v_cmp_eq_u32 vcc, s62, v12
R_ v10,a251
v_mov_b32 v12, 0x7fff0000
v_cndmask_b32 v10, v12, v10, vcc
R_ v12,a253
v_add_u32 v12, 1, v12
buffer_store_dword v12, v10, s[92:95], 0 offen offset:256 sc1
s_and_b32 s53, vcc_lo, 255
s_cmp_eq_u32 s53, 0xFF
s_cselect_b32 s97, 1, 0
s_nop 1
R_ v10,a252
v_cmp_ne_u32 vcc, v13, v10
s_nop 1
s_and_b32 s53, vcc_lo, 255
s_cmp_eq_u32 s53, 0xFF
s_cselect_b32 s53, s97, 0
s_cmp_eq_u32 s53, 1
s_cbranch_scc0 .Lhs_spin
.Lhs_ok:
s_branch .Lhs_d1_back
.Latt2:
R_ v13,a248
S_ s53,s51,36864
v_add_u32 v12, s53, v13
s_mul_i32 s53, s4, 10240
S_ s53,s53,0
v_add_u32 v13, s53, v13
s_cmp_eq_u32 s4, 0
s_cbranch_scc0 .Lhs_tok2
.Lpad_a2_0:
s_cbranch_execz .Lpad_a2_4
R_ v11,a241
buffer_load_dword a255, v11, s[92:95], 0 offen offset:0 sc1
.Lhs_tok2:
W_ 2
Zvvv 96,30,136
Dv 42,v1,44032
R_ v2,a245
Zvvv 100,30,140
ds_write_b128 v13, a[0:3] offset:0
Zvvv 104,30,144
v_xor_b32 v3, 32, v2
v_xor_b32 v4, 64, v2
Zvvv 108,30,148
ds_write_b128 v13, a[4:7] offset:1024
Zvvv 112,30,152
W_ 4
v_xor_b32 v5, 96, v2
s_mov_b64 s[24:25], s[16:17]
Mvvv 96,34,156
s_mov_b32 s26, 7864320
Dv 30,%0,45056
s_mov_b64 s[28:29], s[22:23]
Mvvv 100,34,160
ds_write_b128 v13, a[8:11] offset:2048
Mvvv 104,34,164
s_mov_b32 s30, 1638400
R_ v20,a80
s_mul_i32 s52, s4, 10240
Mvvv 108,34,168
ds_write_b128 v13, a[12:15] offset:3072
Mvvv 112,34,172
W_ 6
R_ v21,a81
R_ v22,a82
Mvvv 96,38,176
Dv 34,v1,45056
R_ v23,a83
Mvvv 100,38,180
ds_write_b128 v13, a[16:19] offset:4096
Mvvv 104,38,184
R_ v24,a84
R_ v25,a85
Mvvv 108,38,188
ds_write_b128 v13, a[40:43] offset:5120
Mvvv 112,38,192
W_ 8
R_ v26,a86
R_ v27,a87
Mvvv 96,42,196
Dv 38,%0,36864
R_ v52,a88
Mvvv 100,42,200
ds_write_b128 v13, a[44:47] offset:6144
Mvvv 104,42,204
R_ v53,a89
R_ v54,a90
Mvvv 108,42,208
ds_write_b128 v13, a[48:51] offset:7168
Mvvv 112,42,212
W_ 8
R_ v55,a91
v_accvgpr_mov_b32 a240, a92
Mvvv 96,30,216
Dv 42,v1,36864
v_accvgpr_mov_b32 a241, a93
Mvvv 100,30,220
ds_write_b128 v13, a[52:55] offset:8192
Mvvv 104,30,224
v_accvgpr_mov_b32 a242, a94
v_accvgpr_mov_b32 a243, a95
Mvvv 108,30,228
ds_write_b128 v13, a[56:59] offset:9216
Mvvv 112,30,232
W_ 8
v_accvgpr_mov_b32 a244, a96
v_accvgpr_mov_b32 a245, a97
Mvvv 96,34,236
Dv 30,%0,37888
v_accvgpr_mov_b32 a246, a98
Mvvv 100,34,240
v_accvgpr_mov_b32 a247, a99
R_ v16,a120
Mvvv 104,34,244
R_ v17,a121
R_ v18,a122
Mvvv 108,34,248
R_ v19,a123
R_ v28,a124
Mvvv 112,34,252
W_ 6
R_ v29,a125
R_ v51,a126
Zvvv 76,38,136
Dv 34,v1,37888
R_ v6,a127
Zvvv 80,38,140
R_ v14,a128
R_ v15,a129
Zvvv 84,38,144
v_accvgpr_mov_b32 a249, a130
v_accvgpr_mov_b32 a250, a131
Zvvv 88,38,148
v_max3_f32 v56, v96, v97, v98
v_max_f32 v56, v56, v99
Zvvv 92,38,152
W_ 4
v_max3_f32 v60, v100, v101, v102
v_max_f32 v60, v60, v103
Mvvv 76,42,156
Dv 38,%0,38912
v_max3_f32 v64, v104, v105, v106
Mvvv 80,42,160
v_max_f32 v64, v64, v107
Mvvv 84,42,164
v_max3_f32 v68, v108, v109, v110
v_max_f32 v68, v68, v111
Mvvv 88,42,168
v_max3_f32 v72, v112, v113, v114
v_max_f32 v72, v72, v115
Mvvv 92,42,172
W_ 2
Mvvv 76,30,176
Dv 42,v1,38912
Mvvv 80,30,180
Mvvv 84,30,184
Mvvv 88,30,188
Mvvv 92,30,192
s_waitcnt vmcnt(30) lgkmcnt(0)
s_barrier
Mvvv 76,34,196
Dv 30,%0,55296
Mvvv 76,38,216
Mvvv 76,42,236
Mvvv 80,34,200
Mvvv 80,38,220
Mvvv 80,42,240
Mvvv 84,34,204
s_nop 3
v_max3_f32 v56, v56, v76, v77
v_max3_f32 v56, v56, v78, v79
Mvvv 84,38,224
Mvvv 84,42,244
Mvvv 88,34,208
v_max3_f32 v60, v60, v80, v81
v_max3_f32 v60, v60, v82, v83
Mvvv 88,38,228
Mvvv 88,42,248
Mvvv 92,34,212
s_nop 1
v_max3_f32 v64, v64, v84, v85
v_max3_f32 v64, v64, v86, v87
Mvvv 92,38,232
Mvvv 92,42,252
Dv 34,v1,55296
.p2align 3
v_mov_b32 v11, 0x3f803f80
v_permlane32_swap_b32 v56, v60
v_max3_f32 v68, v68, v88, v89
v_max3_f32 v68, v68, v90, v91
v_max_f32 v56, v56, v60
s_nop 1
v_max3_f32 v72, v72, v92, v93
v_max3_f32 v72, v72, v94, v95
v_mov_b32 v73, v72
v_permlane32_swap_b32 v64, v68
v_max_f32 v64, v64, v68
v_permlane32_swap_b32 v72, v73
v_max_f32 v72, v72, v73
v_mov_b32 v73, v72
v_permlane16_swap_b32 v56, v64
v_max_f32 v56, v56, v64
v_permlane16_swap_b32 v72, v73
B_ v56,0xc0e00000,v56
v_max_f32 v72, v72, v73
v_mov_b32 v64, v56
B_ v72,0xc0e00000,v72
v_mov_b32 v73, v72
v_permlane16_swap_b32 v56, v64
v_mov_b32 v60, v56
v_mov_b32 v68, v64
v_mov_b64 v[74:75], v[72:73]
v_permlane32_swap_b32 v56, v60
v_permlane32_swap_b32 v64, v68
v_mov_b32 v57, v56
v_mov_b32 v61, v60
v_mov_b32 v65, v64
v_mov_b32 v69, v68
v_pk_add_f32 v[96:97], v[96:97], v[56:57]
v_pk_add_f32 v[98:99], v[98:99], v[56:57]
v_pk_add_f32 v[76:77], v[76:77], v[56:57]
v_pk_add_f32 v[78:79], v[78:79], v[56:57]
v_mov_b64 v[58:59], v[56:57]
v_pk_add_f32 v[100:101], v[100:101], v[60:61]
v_pk_add_f32 v[102:103], v[102:103], v[60:61]
v_pk_add_f32 v[80:81], v[80:81], v[60:61]
)A"
R"A(v_pk_add_f32 v[82:83], v[82:83], v[60:61]
v_mov_b64 v[62:63], v[60:61]
v_pk_add_f32 v[104:105], v[104:105], v[64:65]
v_pk_add_f32 v[106:107], v[106:107], v[64:65]
v_pk_add_f32 v[84:85], v[84:85], v[64:65]
v_pk_add_f32 v[86:87], v[86:87], v[64:65]
v_mov_b64 v[66:67], v[64:65]
v_pk_add_f32 v[108:109], v[108:109], v[68:69]
v_pk_add_f32 v[110:111], v[110:111], v[68:69]
v_pk_add_f32 v[88:89], v[88:89], v[68:69]
v_pk_add_f32 v[90:91], v[90:91], v[68:69]
v_mov_b64 v[70:71], v[68:69]
v_pk_add_f32 v[112:113], v[112:113], v[72:73]
v_pk_add_f32 v[114:115], v[114:115], v[72:73]
v_pk_add_f32 v[92:93], v[92:93], v[72:73]
v_pk_add_f32 v[94:95], v[94:95], v[72:73]
E_ 96
E_ 97
E_ 98
E_ 99
E_ 76
E_ 77
E_ 78
E_ 79
C_ 99,98,99
C_ 98,96,97
C_ 96,76,77
C_ 97,78,79
E_ 100
E_ 101
E_ 102
E_ 103
E_ 80
E_ 81
E_ 82
E_ 83
C_ 103,102,103
C_ 102,100,101
C_ 100,80,81
C_ 101,82,83
.Lstep2_1:
W_ 1
Nvvv 116,30,136,56
s_nop 0
Dv 38,%0,56320
Dv 42,v1,56320
W_ 2
Mvvv 116,34,156
E_ 104
Nvvv 120,30,140,60
E_ 105
Mvvv 120,34,160
E_ 106
Nvvv 124,30,144,64
E_ 107
Mvvv 124,34,164
E_ 84
Nvvv 128,30,148,68
E_ 85
Mvvv 128,34,168
E_ 86
Nvvv 132,30,152,72
E_ 87
Mvvv 132,34,172
W_ 0
C_ 107,106,107
C_ 106,104,105
Mvvv 132,38,192
s_nop 0
Dv 30,%0,57344
Dv 34,v1,57344
Mvvv 132,42,212
C_ 104,84,85
C_ 105,86,87
Mvvv 128,38,188
E_ 108
Mvvv 128,42,208
E_ 109
Mvvv 124,38,184
E_ 110
Mvvv 124,42,204
E_ 111
Mvvv 120,38,180
E_ 88
Mvvv 120,42,200
E_ 89
Mvvv 116,38,176
E_ 90
Mvvv 116,42,196
W_ 0
E_ 91
Mvvv 116,30,216
s_nop 0
Dv 38,%0,49152
Dv 42,v1,49152
Mvvv 116,34,236
C_ 111,110,111
C_ 110,108,109
Mvvv 120,30,220
C_ 108,88,89
C_ 109,90,91
Mvvv 120,34,240
E_ 112
Mvvv 124,30,224
E_ 113
Mvvv 124,34,244
E_ 114
Mvvv 128,30,228
.Lpad_a2_1:
s_cbranch_execz .Lpad_a2_5
E_ 115
Mvvv 128,34,248
E_ 92
Mvvv 132,30,232
E_ 93
Mvvv 132,34,252
W_ 0
E_ 94
Nvvv 76,38,136,56
s_nop 0
Dv 30,%0,50176
Dv 34,v1,50176
Mvvv 76,42,156
E_ 95
Nvvv 80,38,140,60
C_ 115,114,115
C_ 114,112,113
Mvvv 80,42,160
C_ 112,92,93
C_ 113,94,95
Nvvv 84,38,144,64
E_ 116
Mvvv 84,42,164
E_ 117
Nvvv 88,38,148,68
E_ 118
Mvvv 88,42,168
E_ 119
Nvvv 92,38,152,72
C_ 119,118,119
C_ 118,116,117
Mvvv 92,42,172
W_ 0
E_ 120
Mvvv 92,30,192
s_nop 0
Dv 38,%0,51200
Dv 42,v1,51200
Mvvv 92,34,212
X_ a180,v11
X_ a181,v11
Mvvv 88,30,188
X_ a182,v11
X_ a183,v11
Mvvv 88,34,208
ds_write_b128 v12, a[132:135] offset:0
Mvvv 84,30,184
E_ 121
Mvvv 84,34,204
E_ 122
Mvvv 80,30,180
ds_write_b128 v12, a[136:139] offset:1024
Mvvv 80,34,200
E_ 123
Mvvv 76,30,176
C_ 123,122,123
C_ 122,120,121
Mvvv 76,34,196
ds_write_b128 v12, a[160:163] offset:2048
W_ 4
Mvvv 76,38,216
W_ 3
E_ 124
Mvvv 76,42,236
s_waitcnt vmcnt(12) lgkmcnt(0)
s_barrier
Mvvv 80,38,220
ds_write_b128 v12, a[164:167] offset:3072
Mvvv 80,42,240
E_ 125
Mvvv 84,38,224
E_ 126
Mvvv 84,42,244
s_nop 0
Tv 30,v2,24576
Tv 32,v2,30720
Mvvv 88,38,228
ds_write_b128 v12, a[168:171] offset:4096
Mvvv 88,42,248
s_nop 0
Tv 34,v3,24576
Tv 36,v3,30720
Mvvv 92,38,232
E_ 127
Mvvv 92,42,252
ds_write_b128 v12, a[172:175] offset:5120
Zaav 220,180,96
C_ 127,126,127
C_ 126,124,125
Zaav 224,180,100
E_ 128
Zaav 228,180,104
ds_write_b128 v12, a[176:179] offset:12288
Zaav 232,180,108
E_ 129
Zaav 236,180,112
W_ 2
E_ 130
Zavv 0,30,96
s_nop 0
Tv 38,v4,24576
Tv 40,v4,30720
Zavv 20,34,96
ds_write_b128 v12, a[200:203] offset:13312
Zavv 24,34,100
s_nop 0
Tv 42,v5,24576
Tv 44,v5,30720
Zavv 4,30,100
E_ 131
Zavv 8,30,104
ds_write_b128 v12, a[204:207] offset:14336
Zavv 28,34,104
R_ v46,a220
R_ v47,a224
Zavv 32,34,108
R_ v48,a228
R_ v49,a232
Zavv 12,30,108
R_ v50,a236
C_ 131,130,131
Zavv 16,30,112
ds_write_b128 v12, a[208:211] offset:15360
Zavv 36,34,112
W_ 2
C_ 130,128,129
Zavv 56,38,112
s_nop 0
Tv 30,v2,25600
Tv 32,v2,31744
Zavv 76,42,112
ds_write_b128 v12, a[212:215] offset:16384
Zavv 72,42,108
s_nop 0
Tv 34,v3,25600
Tv 36,v3,31744
Zavv 52,38,108
E_ 132
Zavv 48,38,104
ds_write_b128 v12, a[216:219] offset:17408
Zavv 68,42,104
E_ 133
Zavv 64,42,100
E_ 134
Zavv 44,38,100
E_ 135
Zavv 40,38,96
C_ 135,134,135
C_ 134,132,133
Zavv 60,42,96
W_ 1
E_ 76
Zavv 80,30,96
s_nop 0
Tv 38,v4,25600
Tv 40,v4,31744
Zavv 100,34,96
E_ 77
Zavv 104,34,100
s_nop 0
Tv 42,v5,25600
Tv 44,v5,31744
Zavv 84,30,100
E_ 78
Zavv 88,30,104
E_ 79
Zavv 108,34,104
C_ 116,76,77
C_ 117,78,79
Zavv 112,34,108
E_ 80
Zavv 92,30,108
E_ 81
Zavv 96,30,112
E_ 82
Zavv 116,34,112
W_ 0
E_ 83
Zavv 136,38,112
s_nop 0
Tv 30,v2,26624
Tv 32,v2,32768
Zavv 156,42,112
C_ 120,80,81
C_ 121,82,83
Zavv 152,42,108
s_nop 0
Tv 34,v3,26624
Tv 36,v3,32768
Zavv 132,38,108
E_ 84
Zavv 128,38,104
E_ 85
Zavv 148,42,104
E_ 86
Zavv 144,42,100
E_ 87
Zavv 124,38,100
C_ 124,84,85
C_ 125,86,87
Zavv 120,38,96
E_ 88
Zavv 140,42,96
W_ 0
E_ 89
Zavv 160,30,96
s_nop 0
Tv 38,v4,26624
Tv 40,v4,32768
Zavv 180,34,96
E_ 90
Zavv 184,34,100
s_nop 0
Tv 42,v5,26624
Tv 44,v5,32768
Zavv 164,30,100
E_ 91
Zavv 168,30,104
C_ 128,88,89
C_ 129,90,91
Zavv 188,34,104
S_ s65,s61,36864
E_ 92
Zavv 192,34,108
S_ m0,s51,24576
E_ 93
Zavv 172,30,108
s_waitcnt vmcnt(6) lgkmcnt(0)
s_barrier
Zavv 176,30,112
Ln v7,32,s65,0
Zavv 196,34,112
E_ 94
Zavv 216,38,112
Dv 30,%0,6144
Zavv 236,42,112
Dv 34,v1,6144
Zavv 232,42,108
Ln v8,32,s65,3072
S_ m0,s51,25472
Zavv 212,38,108
E_ 95
Zavv 208,38,104
Ln v7,32,s65,128
Zavv 228,42,104
C_ 132,92,93
C_ 133,94,95
Zavv 224,42,100
Ln v8,32,s65,3200
S_ m0,s51,26368
Zavv 204,38,100
v_mov_b32 v92, 0x3f803f80
v_mov_b32 v93, 0x3f803f80
Zavv 200,38,96
Ln v7,32,s65,256
Zavv 220,42,96
v_mov_b32 v94, 0x3f803f80
v_mov_b32 v95, 0x3f803f80
Ln v8,32,s65,3328
.Lstep2_2:
s_nop 0
Zvvv 76,92,116
Dv 38,%0,7168
Zvvv 80,92,120
Dv 42,v1,7168
Zvvv 84,92,124
Zvvv 88,92,128
Zvvv 92,92,132
W_ 2
Nvvv 96,30,136,56
Mvvv 96,34,156
Nvvv 100,30,140,60
s_nop 1
v_max3_f32 v10, v76, v80, v84
s_nop 0
v_max3_f32 v10, v10, v88, v92
Mvvv 100,34,160
v_cmp_lt_f32 vcc, 0x42000000, v10
s_cbranch_vccnz .Ltramp2_1
Nvvv 104,30,144,64
A_ v46,v46,v76
A_ v47,v47,v80
Mvvv 104,34,164
A_ v48,v48,v84
A_ v49,v49,v88
Nvvv 108,30,148,68
A_ v50,v50,v92
Mvvv 108,34,168
Nvvv 112,30,152,72
Mvvv 112,34,172
W_ 0
Mvvv 112,38,192
Dv 30,%0,8192
Mvvv 112,42,212
Dv 34,v1,8192
Mvvv 108,38,188
Mvvv 108,42,208
Mvvv 104,38,184
Mvvv 104,42,204
Mvvv 100,38,180
Mvvv 100,42,200
Mvvv 96,38,176
Mvvv 96,42,196
W_ 0
Mvvv 96,30,216
Dv 38,%0,0
Mvvv 96,34,236
Dv 42,v1,0
Mvvv 100,30,220
Mvvv 100,34,240
Mvvv 104,30,224
Mvvv 104,34,244
Mvvv 108,30,228
Mvvv 108,34,248
s_nop 0
E_ 96
Mvvv 112,30,232
E_ 97
Mvvv 112,34,252
W_ 0
E_ 98
Nvvv 76,38,136,56
Dv 30,%0,1024
Mvvv 76,42,156
Dv 34,v1,1024
Nvvv 80,38,140,60
E_ 99
Mvvv 80,42,160
C_ 99,98,99
C_ 98,96,97
Nvvv 84,38,144,64
E_ 100
Mvvv 84,42,164
E_ 101
Nvvv 88,38,148,68
E_ 102
Mvvv 88,42,168
E_ 103
Nvvv 92,38,152,72
C_ 103,102,103
C_ 102,100,101
Mvvv 92,42,172
W_ 0
E_ 104
Mvvv 92,30,192
Dv 38,%0,2048
Mvvv 92,34,212
Dv 42,v1,2048
Mvvv 88,30,188
E_ 105
Mvvv 88,34,208
E_ 106
Mvvv 84,30,184
S_ s65,s61,24576
E_ 107
Mvvv 84,34,204
S_ m0,s51,0
C_ 107,106,107
C_ 106,104,105
Mvvv 80,30,180
s_waitcnt vmcnt(6) lgkmcnt(0)
s_barrier
Mvvv 80,34,200
L_ v9,36,s65,0
Mvvv 76,30,176
E_ 108
Mvvv 76,34,196
L_ v9,36,s65,3072
S_ m0,s51,896
Mvvv 76,38,216
Tv 30,v2,12288
Tv 32,v2,18432
Mvvv 76,42,236
L_ v9,36,s65,128
Mvvv 80,38,220
s_nop 0
Tv 34,v3,12288
Tv 36,v3,18432
Mvvv 80,42,240
L_ v9,36,s65,3200
S_ m0,s51,1792
Mvvv 84,38,224
E_ 109
Mvvv 84,42,244
L_ v9,36,s65,256
Mvvv 88,38,228
E_ 110
Mvvv 88,42,248
L_ v9,36,s65,3328
Mvvv 92,38,232
E_ 111
Mvvv 92,42,252
W_ 0
C_ 111,110,111
C_ 110,108,109
Mavv 0,30,116
s_nop 0
Tv 38,v4,12288
Tv 40,v4,18432
Mavv 20,34,116
E_ 112
Mavv 24,34,120
s_nop 0
Tv 42,v5,12288
Tv 44,v5,18432
Mavv 4,30,120
E_ 113
Mavv 8,30,124
E_ 114
Mavv 28,34,124
E_ 115
Mavv 32,34,128
C_ 115,114,115
C_ 114,112,113
Mavv 12,30,128
E_ 76
Mavv 16,30,132
E_ 77
Mavv 36,34,132
W_ 0
E_ 78
Mavv 56,38,132
s_nop 0
Tv 30,v2,13312
Tv 32,v2,19456
Mavv 76,42,132
E_ 79
Mavv 72,42,128
s_nop 0
Tv 34,v3,13312
Tv 36,v3,19456
Mavv 52,38,128
C_ 96,76,77
C_ 97,78,79
Mavv 48,38,124
E_ 80
Mavv 68,42,124
E_ 81
Mavv 64,42,120
E_ 82
Mavv 44,38,120
E_ 83
Mavv 40,38,116
C_ 100,80,81
C_ 101,82,83
Mavv 60,42,116
W_ 0
E_ 84
Mavv 80,30,116
s_nop 0
Tv 38,v4,13312
Tv 40,v4,19456
Mavv 100,34,116
E_ 85
Mavv 104,34,120
s_nop 0
Tv 42,v5,13312
Tv 44,v5,19456
Mavv 84,30,120
E_ 86
Mavv 88,30,124
E_ 87
Mavv 108,34,124
C_ 104,84,85
C_ 105,86,87
Mavv 112,34,128
E_ 88
Mavv 92,30,128
E_ 89
Mavv 96,30,132
E_ 90
Mavv 116,34,132
W_ 0
E_ 91
Mavv 136,38,132
s_nop 0
Tv 30,v2,14336
Tv 32,v2,20480
Mavv 156,42,132
C_ 108,88,89
C_ 109,90,91
Mavv 152,42,128
s_nop 0
Tv 34,v3,14336
Tv 36,v3,20480
Mavv 132,38,128
E_ 92
Mavv 128,38,124
E_ 93
Mavv 148,42,124
E_ 94
Mavv 144,42,120
E_ 95
Mavv 124,38,120
C_ 112,92,93
C_ 113,94,95
Mavv 120,38,116
v_mov_b32 v92, 0x3f803f80
v_mov_b32 v93, 0x3f803f80
Mavv 140,42,116
W_ 0
v_mov_b32 v94, 0x3f803f80
v_mov_b32 v95, 0x3f803f80
Mavv 160,30,116
s_nop 0
Tv 38,v4,14336
Tv 40,v4,20480
Mavv 180,34,116
Mavv 184,34,120
s_nop 0
Tv 42,v5,14336
Tv 44,v5,20480
Mavv 164,30,120
Mavv 168,30,124
Mavv 188,34,124
S_ s65,s61,49152
Mavv 192,34,128
S_ m0,s51,12288
Mavv 172,30,128
s_waitcnt vmcnt(6) lgkmcnt(0)
s_barrier
Mavv 176,30,132
Ln v7,32,s65,0
Mavv 196,34,132
Mavv 216,38,132
Dv 30,%0,30720
Mavv 236,42,132
Dv 34,v1,30720
Mavv 232,42,128
Ln v8,32,s65,3072
S_ m0,s51,13184
Mavv 212,38,128
Mavv 208,38,124
Ln v7,32,s65,128
Mavv 228,42,124
Mavv 224,42,120
Ln v8,32,s65,3200
S_ m0,s51,14080
Mavv 204,38,120
Mavv 200,38,116
Ln v7,32,s65,256
Mavv 220,42,116
s_cmp_eq_u32 s4, 0
s_cbranch_scc1 .Lhs_a2
.Lhs_a2_back:
.Lstep2_3:
Zvvv 76,92,96
Dv 38,%0,31744
Zvvv 80,92,100
Dv 42,v1,31744
Zvvv 84,92,104
Zvvv 88,92,108
Zvvv 92,92,112
W_ 2
Nvvv 116,30,136,56
Mvvv 116,34,156
Nvvv 120,30,140,60
s_nop 1
v_max3_f32 v10, v76, v80, v84
s_nop 0
v_max3_f32 v10, v10, v88, v92
Mvvv 120,34,160
v_cmp_lt_f32 vcc, 0x42000000, v10
s_cbranch_vccnz .Ltramp2_2
Nvvv 124,30,144,64
A_ v46,v46,v76
A_ v47,v47,v80
Mvvv 124,34,164
A_ v48,v48,v84
A_ v49,v49,v88
Nvvv 128,30,148,68
A_ v50,v50,v92
Mvvv 128,34,168
Ln v8,32,s65,3328
Nvvv 132,30,152,72
Mvvv 132,34,172
W_ 0
Mvvv 132,38,192
Dv 30,%0,32768
Mvvv 132,42,212
Dv 34,v1,32768
Mvvv 128,38,188
Mvvv 128,42,208
Mvvv 124,38,184
Mvvv 124,42,204
Mvvv 120,38,180
Mvvv 120,42,200
Mvvv 116,38,176
Mvvv 116,42,196
W_ 0
Mvvv 116,30,216
Dv 38,%0,24576
Mvvv 116,34,236
Dv 42,v1,24576
Mvvv 120,30,220
.Lpad_a2_2:
s_cbranch_execz .Lpad_a2_6
Mvvv 120,34,240
Mvvv 124,30,224
Mvvv 124,34,244
Mvvv 128,30,228
Mvvv 128,34,248
s_nop 0
E_ 116
Mvvv 132,30,232
E_ 117
Mvvv 132,34,252
W_ 0
E_ 118
Nvvv 76,38,136,56
Dv 30,%0,25600
Mvvv 76,42,156
Dv 34,v1,25600
Nvvv 80,38,140,60
E_ 119
Mvvv 80,42,160
C_ 119,118,119
C_ 118,116,117
Nvvv 84,38,144,64
E_ 120
Mvvv 84,42,164
E_ 121
Nvvv 88,38,148,68
E_ 122
Mvvv 88,42,168
E_ 123
Nvvv 92,38,152,72
C_ 123,122,123
C_ 122,120,121
Mvvv 92,42,172
W_ 0
E_ 124
Mvvv 92,30,192
Dv 38,%0,26624
Mvvv 92,34,212
Dv 42,v1,26624
Mvvv 88,30,188
E_ 125
Mvvv 88,34,208
E_ 126
Mvvv 84,30,184
S_ s65,s61,36864
E_ 127
Mvvv 84,34,204
S_ m0,s51,24576
C_ 127,126,127
C_ 126,124,125
Mvvv 80,30,180
s_waitcnt vmcnt(6) lgkmcnt(0)
s_barrier
Mvvv 80,34,200
L_ v9,36,s65,0
Mvvv 76,30,176
E_ 128
Mvvv 76,34,196
L_ v9,36,s65,3072
S_ m0,s51,25472
Mvvv 76,38,216
Tv 30,v2,0
Tv 32,v2,6144
Mvvv 76,42,236
L_ v9,36,s65,128
Mvvv 80,38,220
s_nop 0
Tv 34,v3,0
Tv 36,v3,6144
Mvvv 80,42,240
L_ v9,36,s65,3200
S_ m0,s51,26368
Mvvv 84,38,224
E_ 129
Mvvv 84,42,244
L_ v9,36,s65,256
Mvvv 88,38,228
E_ 130
Mvvv 88,42,248
L_ v9,36,s65,3328
Mvvv 92,38,232
E_ 131
Mvvv 92,42,252
W_ 0
C_ 131,130,131
C_ 130,128,129
Mavv 0,30,96
s_nop 0
Tv 38,v4,0
Tv 40,v4,6144
Mavv 20,34,96
E_ 132
Mavv 24,34,100
s_nop 0
Tv 42,v5,0
Tv 44,v5,6144
Mavv 4,30,100
E_ 133
Mavv 8,30,104
E_ 134
Mavv 28,34,104
E_ 135
Mavv 32,34,108
C_ 135,134,135
C_ 134,132,133
Mavv 12,30,108
E_ 76
Mavv 16,30,112
E_ 77
Mavv 36,34,112
W_ 0
E_ 78
Mavv 56,38,112
s_nop 0
Tv 30,v2,1024
Tv 32,v2,7168
Mavv 76,42,112
E_ 79
Mavv 72,42,108
s_nop 0
Tv 34,v3,1024
Tv 36,v3,7168
Mavv 52,38,108
C_ 116,76,77
C_ 117,78,79
Mavv 48,38,104
E_ 80
Mavv 68,42,104
E_ 81
Mavv 64,42,100
E_ 82
Mavv 44,38,100
E_ 83
Mavv 40,38,96
C_ 120,80,81
C_ 121,82,83
Mavv 60,42,96
W_ 0
E_ 84
Mavv 80,30,96
s_nop 0
Tv 38,v4,1024
Tv 40,v4,7168
Mavv 100,34,96
E_ 85
Mavv 104,34,100
s_nop 0
Tv 42,v5,1024
Tv 44,v5,7168
Mavv 84,30,100
E_ 86
Mavv 88,30,104
E_ 87
Mavv 108,34,104
C_ 124,84,85
C_ 125,86,87
Mavv 112,34,108
E_ 88
Mavv 92,30,108
E_ 89
Mavv 96,30,112
E_ 90
Mavv 116,34,112
W_ 0
E_ 91
Mavv 136,38,112
s_nop 0
Tv 30,v2,2048
Tv 32,v2,8192
Mavv 156,42,112
C_ 128,88,89
C_ 129,90,91
Mavv 152,42,108
s_nop 0
Tv 34,v3,2048
Tv 36,v3,8192
Mavv 132,38,108
E_ 92
Mavv 128,38,104
E_ 93
Mavv 148,42,104
E_ 94
Mavv 144,42,100
E_ 95
Mavv 124,38,100
C_ 132,92,93
C_ 133,94,95
Mavv 120,38,96
v_mov_b32 v92, 0x3f803f80
v_mov_b32 v93, 0x3f803f80
Mavv 140,42,96
W_ 0
v_mov_b32 v94, 0x3f803f80
v_mov_b32 v95, 0x3f803f80
Mavv 160,30,96
s_nop 0
Tv 38,v4,2048
Tv 40,v4,8192
Mavv 180,34,96
Mavv 184,34,100
s_nop 0
Tv 42,v5,2048
Tv 44,v5,8192
Mavv 164,30,100
Mavv 168,30,104
Mavv 188,34,104
S_ s65,s61,61440
Mavv 192,34,108
S_ m0,s51,0
Mavv 172,30,108
s_waitcnt vmcnt(6) lgkmcnt(0)
s_barrier
Mavv 176,30,112
Ln v7,32,s65,0
Mavv 196,34,112
Mavv 216,38,112
Dv 30,%0,18432
Mavv 236,42,112
Dv 34,v1,18432
Mavv 232,42,108
Ln v8,32,s65,3072
S_ m0,s51,896
Mavv 212,38,108
Mavv 208,38,104
Ln v7,32,s65,128
Mavv 228,42,104
Mavv 224,42,100
Ln v8,32,s65,3200
S_ m0,s51,1792
Mavv 204,38,100
Mavv 200,38,96
Ln v7,32,s65,256
Mavv 220,42,96
.Lstep2_4:
Zvvv 76,92,116
Dv 38,%0,19456
Zvvv 80,92,120
Dv 42,v1,19456
Zvvv 84,92,124
Zvvv 88,92,128
Zvvv 92,92,132
W_ 2
Nvvv 96,30,136,56
Mvvv 96,34,156
Nvvv 100,30,140,60
s_nop 1
v_max3_f32 v10, v76, v80, v84
s_nop 0
v_max3_f32 v10, v10, v88, v92
Mvvv 100,34,160
v_cmp_lt_f32 vcc, 0x42000000, v10
s_cbranch_vccnz .Ltramp2_3
Nvvv 104,30,144,64
A_ v46,v46,v76
A_ v47,v47,v80
Mvvv 104,34,164
A_ v48,v48,v84
A_ v49,v49,v88
Nvvv 108,30,148,68
A_ v50,v50,v92
Mvvv 108,34,168
Ln v8,32,s65,3328
Nvvv 112,30,152,72
Mvvv 112,34,172
W_ 0
Mvvv 112,38,192
Dv 30,%0,20480
Mvvv 112,42,212
Dv 34,v1,20480
Mvvv 108,38,188
Mvvv 108,42,208
Mvvv 104,38,184
Mvvv 104,42,204
Mvvv 100,38,180
Mvvv 100,42,200
Mvvv 96,38,176
Mvvv 96,42,196
W_ 0
Mvvv 96,30,216
Dv 38,%0,12288
Mvvv 96,34,236
Dv 42,v1,12288
Mvvv 100,30,220
Mvvv 100,34,240
Mvvv 104,30,224
Mvvv 104,34,244
Mvvv 108,30,228
Mvvv 108,34,248
s_nop 0
E_ 96
Mvvv 112,30,232
E_ 97
Mvvv 112,34,252
W_ 0
E_ 98
Nvvv 76,38,136,56
Dv 30,%0,13312
Mvvv 76,42,156
Dv 34,v1,13312
Nvvv 80,38,140,60
E_ 99
Mvvv 80,42,160
C_ 99,98,99
C_ 98,96,97
Nvvv 84,38,144,64
E_ 100
Mvvv 84,42,164
E_ 101
Nvvv 88,38,148,68
E_ 102
Mvvv 88,42,168
E_ 103
Nvvv 92,38,152,72
C_ 103,102,103
C_ 102,100,101
Mvvv 92,42,172
W_ 0
E_ 104
Mvvv 92,30,192
Dv 38,%0,14336
Mvvv 92,34,212
Dv 42,v1,14336
Mvvv 88,30,188
E_ 105
Mvvv 88,34,208
E_ 106
Mvvv 84,30,184
S_ s65,s61,49152
E_ 107
Mvvv 84,34,204
S_ m0,s51,12288
C_ 107,106,107
C_ 106,104,105
Mvvv 80,30,180
s_waitcnt vmcnt(6) lgkmcnt(0)
s_barrier
Mvvv 80,34,200
L_ v9,36,s65,0
Mvvv 76,30,176
E_ 108
Mvvv 76,34,196
L_ v9,36,s65,3072
S_ m0,s51,13184
Mvvv 76,38,216
Tv 30,v2,24576
Tv 32,v2,30720
Mvvv 76,42,236
L_ v9,36,s65,128
Mvvv 80,38,220
s_nop 0
Tv 34,v3,24576
Tv 36,v3,30720
Mvvv 80,42,240
L_ v9,36,s65,3200
S_ m0,s51,14080
Mvvv 84,38,224
E_ 109
Mvvv 84,42,244
L_ v9,36,s65,256
Mvvv 88,38,228
E_ 110
Mvvv 88,42,248
L_ v9,36,s65,3328
Mvvv 92,38,232
E_ 111
Mvvv 92,42,252
W_ 0
C_ 111,110,111
C_ 110,108,109
Mavv 0,30,116
s_nop 0
Tv 38,v4,24576
Tv 40,v4,30720
Mavv 20,34,116
E_ 112
Mavv 24,34,120
s_nop 0
Tv 42,v5,24576
Tv 44,v5,30720
Mavv 4,30,120
E_ 113
Mavv 8,30,124
E_ 114
Mavv 28,34,124
E_ 115
Mavv 32,34,128
C_ 115,114,115
C_ 114,112,113
Mavv 12,30,128
E_ 76
Mavv 16,30,132
E_ 77
Mavv 36,34,132
W_ 0
E_ 78
Mavv 56,38,132
s_nop 0
Tv 30,v2,25600
Tv 32,v2,31744
Mavv 76,42,132
E_ 79
Mavv 72,42,128
s_nop 0
Tv 34,v3,25600
Tv 36,v3,31744
Mavv 52,38,128
C_ 96,76,77
C_ 97,78,79
Mavv 48,38,124
E_ 80
Mavv 68,42,124
E_ 81
Mavv 64,42,120
E_ 82
Mavv 44,38,120
E_ 83
Mavv 40,38,116
C_ 100,80,81
C_ 101,82,83
Mavv 60,42,116
W_ 0
E_ 84
Mavv 80,30,116
s_nop 0
Tv 38,v4,25600
Tv 40,v4,31744
Mavv 100,34,116
E_ 85
Mavv 104,34,120
s_nop 0
Tv 42,v5,25600
Tv 44,v5,31744
Mavv 84,30,120
E_ 86
Mavv 88,30,124
E_ 87
Mavv 108,34,124
C_ 104,84,85
C_ 105,86,87
Mavv 112,34,128
E_ 88
Mavv 92,30,128
E_ 89
Mavv 96,30,132
E_ 90
Mavv 116,34,132
W_ 0
E_ 91
Mavv 136,38,132
s_nop 0
Tv 30,v2,26624
Tv 32,v2,32768
Mavv 156,42,132
C_ 108,88,89
C_ 109,90,91
Mavv 152,42,128
s_nop 0
Tv 34,v3,26624
Tv 36,v3,32768
Mavv 132,38,128
E_ 92
Mavv 128,38,124
E_ 93
Mavv 148,42,124
E_ 94
Mavv 144,42,120
E_ 95
Mavv 124,38,120
C_ 112,92,93
C_ 113,94,95
Mavv 120,38,116
v_mov_b32 v92, 0x3f803f80
v_mov_b32 v93, 0x3f803f80
Mavv 140,42,116
W_ 0
v_mov_b32 v94, 0x3f803f80
v_mov_b32 v95, 0x3f803f80
Mavv 160,30,116
s_nop 0
Tv 38,v4,26624
Tv 40,v4,32768
Mavv 180,34,116
Mavv 184,34,120
s_nop 0
Tv 42,v5,26624
Tv 44,v5,32768
Mavv 164,30,120
Mavv 168,30,124
Mavv 188,34,124
S_ s65,s61,73728
Mavv 192,34,128
S_ m0,s51,24576
Mavv 172,30,128
s_waitcnt vmcnt(6) lgkmcnt(0)
s_barrier
Mavv 176,30,132
Ln v7,32,s65,0
Mavv 196,34,132
Mavv 216,38,132
Dv 30,%0,6144
Mavv 236,42,132
Dv 34,v1,6144
Mavv 232,42,128
Ln v8,32,s65,3072
S_ m0,s51,25472
Mavv 212,38,128
Mavv 208,38,124
Ln v7,32,s65,128
Mavv 228,42,124
Mavv 224,42,120
Ln v8,32,s65,3200
S_ m0,s51,26368
Mavv 204,38,120
Mavv 200,38,116
Ln v7,32,s65,256
Mavv 220,42,116
.Lstep2_5:
Zvvv 76,92,96
Dv 38,%0,7168
Zvvv 80,92,100
Dv 42,v1,7168
Zvvv 84,92,104
Zvvv 88,92,108
Zvvv 92,92,112
W_ 2
Nvvv 116,30,136,56
Mvvv 116,34,156
Nvvv 120,30,140,60
s_nop 1
v_max3_f32 v10, v76, v80, v84
s_nop 0
v_max3_f32 v10, v10, v88, v92
Mvvv 120,34,160
v_cmp_lt_f32 vcc, 0x42000000, v10
s_cbranch_vccnz .Ltramp2_4
Nvvv 124,30,144,64
A_ v46,v46,v76
A_ v47,v47,v80
Mvvv 124,34,164
A_ v48,v48,v84
A_ v49,v49,v88
Nvvv 128,30,148,68
A_ v50,v50,v92
Mvvv 128,34,168
Ln v8,32,s65,3328
Nvvv 132,30,152,72
Mvvv 132,34,172
W_ 0
Mvvv 132,38,192
Dv 30,%0,8192
Mvvv 132,42,212
Dv 34,v1,8192
Mvvv 128,38,188
Mvvv 128,42,208
Mvvv 124,38,184
Mvvv 124,42,204
Mvvv 120,38,180
Mvvv 120,42,200
Mvvv 116,38,176
Mvvv 116,42,196
W_ 0
Mvvv 116,30,216
Dv 38,%0,0
Mvvv 116,34,236
Dv 42,v1,0
Mvvv 120,30,220
Mvvv 120,34,240
Mvvv 124,30,224
Mvvv 124,34,244
Mvvv 128,30,228
Mvvv 128,34,248
s_nop 0
E_ 116
Mvvv 132,30,232
E_ 117
Mvvv 132,34,252
W_ 0
E_ 118
Nvvv 76,38,136,56
Dv 30,%0,1024
Mvvv 76,42,156
Dv 34,v1,1024
Nvvv 80,38,140,60
E_ 119
Mvvv 80,42,160
C_ 119,118,119
C_ 118,116,117
Nvvv 84,38,144,64
E_ 120
Mvvv 84,42,164
E_ 121
Nvvv 88,38,148,68
.Lpad_a2_3:
s_cbranch_execz .Lpad_a2_7
E_ 122
Mvvv 88,42,168
E_ 123
Nvvv 92,38,152,72
C_ 123,122,123
C_ 122,120,121
Mvvv 92,42,172
W_ 0
E_ 124
Mvvv 92,30,192
Dv 38,%0,2048
Mvvv 92,34,212
Dv 42,v1,2048
Mvvv 88,30,188
E_ 125
Mvvv 88,34,208
E_ 126
Mvvv 84,30,184
S_ s65,s61,61440
E_ 127
Mvvv 84,34,204
S_ m0,s51,0
C_ 127,126,127
C_ 126,124,125
Mvvv 80,30,180
s_waitcnt vmcnt(6) lgkmcnt(0)
s_barrier
Mvvv 80,34,200
L_ v9,36,s65,0
Mvvv 76,30,176
E_ 128
Mvvv 76,34,196
L_ v9,36,s65,3072
S_ m0,s51,896
Mvvv 76,38,216
Tv 30,v2,12288
Tv 32,v2,18432
Mvvv 76,42,236
L_ v9,36,s65,128
Mvvv 80,38,220
s_nop 0
Tv 34,v3,12288
Tv 36,v3,18432
Mvvv 80,42,240
L_ v9,36,s65,3200
S_ m0,s51,1792
Mvvv 84,38,224
E_ 129
Mvvv 84,42,244
L_ v9,36,s65,256
Mvvv 88,38,228
E_ 130
Mvvv 88,42,248
L_ v9,36,s65,3328
Mvvv 92,38,232
E_ 131
Mvvv 92,42,252
W_ 0
C_ 131,130,131
C_ 130,128,129
Mavv 0,30,96
s_nop 0
Tv 38,v4,12288
Tv 40,v4,18432
Mavv 20,34,96
E_ 132
Mavv 24,34,100
s_nop 0
Tv 42,v5,12288
Tv 44,v5,18432
Mavv 4,30,100
E_ 133
Mavv 8,30,104
E_ 134
Mavv 28,34,104
E_ 135
Mavv 32,34,108
C_ 135,134,135
C_ 134,132,133
Mavv 12,30,108
E_ 76
Mavv 16,30,112
E_ 77
Mavv 36,34,112
W_ 0
E_ 78
Mavv 56,38,112
s_nop 0
Tv 30,v2,13312
Tv 32,v2,19456
Mavv 76,42,112
E_ 79
Mavv 72,42,108
s_nop 0
Tv 34,v3,13312
Tv 36,v3,19456
Mavv 52,38,108
C_ 116,76,77
C_ 117,78,79
Mavv 48,38,104
E_ 80
Mavv 68,42,104
E_ 81
Mavv 64,42,100
E_ 82
Mavv 44,38,100
E_ 83
Mavv 40,38,96
C_ 120,80,81
C_ 121,82,83
Mavv 60,42,96
W_ 0
E_ 84
Mavv 80,30,96
s_nop 0
Tv 38,v4,13312
Tv 40,v4,19456
Mavv 100,34,96
E_ 85
Mavv 104,34,100
s_nop 0
Tv 42,v5,13312
Tv 44,v5,19456
Mavv 84,30,100
E_ 86
Mavv 88,30,104
E_ 87
Mavv 108,34,104
C_ 124,84,85
C_ 125,86,87
Mavv 112,34,108
E_ 88
Mavv 92,30,108
E_ 89
Mavv 96,30,112
E_ 90
Mavv 116,34,112
W_ 0
E_ 91
Mavv 136,38,112
s_nop 0
Tv 30,v2,14336
Tv 32,v2,20480
Mavv 156,42,112
C_ 128,88,89
C_ 129,90,91
Mavv 152,42,108
s_nop 0
Tv 34,v3,14336
Tv 36,v3,20480
Mavv 132,38,108
E_ 92
Mavv 128,38,104
E_ 93
Mavv 148,42,104
E_ 94
Mavv 144,42,100
E_ 95
Mavv 124,38,100
C_ 132,92,93
C_ 133,94,95
Mavv 120,38,96
v_mov_b32 v92, 0x3f803f80
v_mov_b32 v93, 0x3f803f80
Mavv 140,42,96
W_ 0
v_mov_b32 v94, 0x3f803f80
v_mov_b32 v95, 0x3f803f80
Mavv 160,30,96
s_nop 0
Tv 38,v4,14336
Tv 40,v4,20480
Mavv 180,34,96
Mavv 184,34,100
s_nop 0
Tv 42,v5,14336
Tv 44,v5,20480
Mavv 164,30,100
Mavv 168,30,104
Mavv 188,34,104
S_ s65,s61,86016
Mavv 192,34,108
S_ m0,s51,12288
Mavv 172,30,108
s_waitcnt vmcnt(6) lgkmcnt(0)
s_barrier
Mavv 176,30,112
Ln v7,32,s65,0
Mavv 196,34,112
Mavv 216,38,112
Dv 30,%0,30720
Mavv 236,42,112
Dv 34,v1,30720
Mavv 232,42,108
Ln v8,32,s65,3072
S_ m0,s51,13184
Mavv 212,38,108
Mavv 208,38,104
Ln v7,32,s65,128
Mavv 228,42,104
Mavv 224,42,100
Ln v8,32,s65,3200
S_ m0,s51,14080
Mavv 204,38,100
Mavv 200,38,96
Ln v7,32,s65,256
Mavv 220,42,96
.Lstep2_6:
Zvvv 76,92,116
Dv 38,%0,31744
Zvvv 80,92,120
Dv 42,v1,31744
Zvvv 84,92,124
Zvvv 88,92,128
Zvvv 92,92,132
W_ 2
Nvvv 96,30,136,56
Mvvv 96,34,156
Nvvv 100,30,140,60
s_nop 1
v_max3_f32 v10, v76, v80, v84
s_nop 0
v_max3_f32 v10, v10, v88, v92
Mvvv 100,34,160
v_cmp_lt_f32 vcc, 0x42000000, v10
s_cbranch_vccnz .Ltramp2_5
Nvvv 104,30,144,64
A_ v46,v46,v76
A_ v47,v47,v80
Mvvv 104,34,164
A_ v48,v48,v84
A_ v49,v49,v88
Nvvv 108,30,148,68
A_ v50,v50,v92
Mvvv 108,34,168
Ln v8,32,s65,3328
Nvvv 112,30,152,72
R_ v11,a248
Mvvv 112,34,172
W_ 0
v_min_u32 v11, 240, v11
Mvvv 112,38,192
Dv 30,%0,32768
v_lshlrev_b32 v11, 3, v11
Mvvv 112,42,212
Dv 34,v1,32768
Mvvv 108,38,188
buffer_load_dword a254, v11, s[44:47], s66 offen
Mvvv 108,42,208
Mvvv 104,38,184
Mvvv 104,42,204
Mvvv 100,38,180
Mvvv 100,42,200
Mvvv 96,38,176
Mvvv 96,42,196
W_ 0
Mvvv 96,30,216
Dv 38,%0,24576
Mvvv 96,34,236
Dv 42,v1,24576
Mvvv 100,30,220
Mvvv 100,34,240
Mvvv 104,30,224
Mvvv 104,34,244
Mvvv 108,30,228
Mvvv 108,34,248
s_nop 0
E_ 96
Mvvv 112,30,232
E_ 97
Mvvv 112,34,252
W_ 0
E_ 98
Nvvv 76,38,136,56
Dv 30,%0,25600
Mvvv 76,42,156
Dv 34,v1,25600
Nvvv 80,38,140,60
E_ 99
Mvvv 80,42,160
C_ 99,98,99
C_ 98,96,97
Nvvv 84,38,144,64
E_ 100
Mvvv 84,42,164
E_ 101
Nvvv 88,38,148,68
E_ 102
Mvvv 88,42,168
E_ 103
Nvvv 92,38,152,72
C_ 103,102,103
C_ 102,100,101
Mvvv 92,42,172
W_ 0
E_ 104
Mvvv 92,30,192
Dv 38,%0,26624
Mvvv 92,34,212
Dv 42,v1,26624
Mvvv 88,30,188
E_ 105
Mvvv 88,34,208
E_ 106
Mvvv 84,30,184
S_ s65,s61,73728
E_ 107
Mvvv 84,34,204
S_ m0,s51,24576
C_ 107,106,107
C_ 106,104,105
Mvvv 80,30,180
s_waitcnt vmcnt(7) lgkmcnt(0)
s_barrier
Mvvv 80,34,200
L_ v9,36,s65,0
Mvvv 76,30,176
E_ 108
Mvvv 76,34,196
L_ v9,36,s65,3072
S_ m0,s51,25472
Mvvv 76,38,216
Tv 30,v2,0
Tv 32,v2,6144
Mvvv 76,42,236
L_ v9,36,s65,128
Mvvv 80,38,220
s_nop 0
Tv 34,v3,0
Tv 36,v3,6144
Mvvv 80,42,240
L_ v9,36,s65,3200
S_ m0,s51,26368
Mvvv 84,38,224
E_ 109
Mvvv 84,42,244
L_ v9,36,s65,256
Mvvv 88,38,228
E_ 110
Mvvv 88,42,248
L_ v9,36,s65,3328
Mvvv 92,38,232
E_ 111
Mvvv 92,42,252
W_ 0
C_ 111,110,111
C_ 110,108,109
Mavv 0,30,116
s_nop 0
Tv 38,v4,0
Tv 40,v4,6144
Mavv 20,34,116
E_ 112
Mavv 24,34,120
s_nop 0
Tv 42,v5,0
Tv 44,v5,6144
Mavv 4,30,120
E_ 113
Mavv 8,30,124
E_ 114
Mavv 28,34,124
E_ 115
Mavv 32,34,128
C_ 115,114,115
C_ 114,112,113
Mavv 12,30,128
E_ 76
Mavv 16,30,132
E_ 77
Mavv 36,34,132
W_ 0
E_ 78
Mavv 56,38,132
s_nop 0
Tv 30,v2,1024
Tv 32,v2,7168
Mavv 76,42,132
E_ 79
Mavv 72,42,128
s_nop 0
Tv 34,v3,1024
Tv 36,v3,7168
Mavv 52,38,128
C_ 96,76,77
C_ 97,78,79
Mavv 48,38,124
E_ 80
Mavv 68,42,124
E_ 81
Mavv 64,42,120
E_ 82
Mavv 44,38,120
E_ 83
Mavv 40,38,116
C_ 100,80,81
C_ 101,82,83
Mavv 60,42,116
W_ 0
E_ 84
Mavv 80,30,116
s_nop 0
Tv 38,v4,1024
Tv 40,v4,7168
Mavv 100,34,116
E_ 85
Mavv 104,34,120
s_nop 0
Tv 42,v5,1024
Tv 44,v5,7168
Mavv 84,30,120
E_ 86
Mavv 88,30,124
E_ 87
Mavv 108,34,124
C_ 104,84,85
C_ 105,86,87
Mavv 112,34,128
E_ 88
Mavv 92,30,128
E_ 89
Mavv 96,30,132
E_ 90
Mavv 116,34,132
W_ 0
E_ 91
Mavv 136,38,132
s_nop 0
Tv 30,v2,2048
Tv 32,v2,8192
Mavv 156,42,132
C_ 108,88,89
C_ 109,90,91
Mavv 152,42,128
s_nop 0
Tv 34,v3,2048
Tv 36,v3,8192
Mavv 132,38,128
E_ 92
Mavv 128,38,124
E_ 93
Mavv 148,42,124
E_ 94
Mavv 144,42,120
E_ 95
Mavv 124,38,120
C_ 112,92,93
C_ 113,94,95
Mavv 120,38,116
v_mov_b32 v92, 0x3f803f80
v_mov_b32 v93, 0x3f803f80
Mavv 140,42,116
W_ 0
v_mov_b32 v94, 0x3f803f80
v_mov_b32 v95, 0x3f803f80
Mavv 160,30,116
s_nop 0
Tv 38,v4,2048
Tv 40,v4,8192
Mavv 180,34,116
Mavv 184,34,120
s_nop 0
Tv 42,v5,2048
Tv 44,v5,8192
Mavv 164,30,120
Mavv 168,30,124
Mavv 188,34,124
S_ s65,s61,98304
Mavv 192,34,128
S_ m0,s51,0
Mavv 172,30,128
s_waitcnt vmcnt(7) lgkmcnt(0)
s_barrier
Mavv 176,30,132
Ln v7,32,s65,0
Mavv 196,34,132
Mavv 216,38,132
Dv 30,%0,18432
Mavv 236,42,132
Dv 34,v1,18432
Mavv 232,42,128
Ln v8,32,s65,3072
S_ m0,s51,896
Mavv 212,38,128
Mavv 208,38,124
Ln v7,32,s65,128
Mavv 228,42,124
Mavv 224,42,120
Ln v8,32,s65,3200
S_ m0,s51,1792
Mavv 204,38,120
Mavv 200,38,116
Ln v7,32,s65,256
Mavv 220,42,116
s_cmp_eq_u32 s4, 0
s_cbranch_scc1 .Lhs_c2
.Lhs_c2_back:
.Lstep2_7:
Zvvv 76,92,96
Dv 38,%0,19456
Zvvv 80,92,100
Dv 42,v1,19456
Zvvv 84,92,104
Zvvv 88,92,108
Zvvv 92,92,112
W_ 2
Nvvv 116,30,136,56
Mvvv 116,34,156
Nvvv 120,30,140,60
s_nop 1
v_max3_f32 v10, v76, v80, v84
s_nop 0
v_max3_f32 v10, v10, v88, v92
Mvvv 120,34,160
v_cmp_lt_f32 vcc, 0x42000000, v10
s_cbranch_vccnz .Ltramp2_6
Nvvv 124,30,144,64
A_ v46,v46,v76
A_ v47,v47,v80
Mvvv 124,34,164
A_ v48,v48,v84
A_ v49,v49,v88
Nvvv 128,30,148,68
A_ v50,v50,v92
Mvvv 128,34,168
Ln v8,32,s65,3328
Nvvv 132,30,152,72
Mvvv 132,34,172
W_ 0
Mvvv 132,38,192
Dv 30,%0,20480
Mvvv 132,42,212
Dv 34,v1,20480
Mvvv 128,38,188
Mvvv 128,42,208
Mvvv 124,38,184
Mvvv 124,42,204
Mvvv 120,38,180
Mvvv 120,42,200
Mvvv 116,38,176
Mvvv 116,42,196
W_ 0
Mvvv 116,30,216
Dv 38,%0,12288
Mvvv 116,34,236
Dv 42,v1,12288
Mvvv 120,30,220
Mvvv 120,34,240
Mvvv 124,30,224
Mvvv 124,34,244
Mvvv 128,30,228
Mvvv 128,34,248
s_nop 0
E_ 116
Mvvv 132,30,232
E_ 117
Mvvv 132,34,252
W_ 0
E_ 118
Nvvv 76,38,136,56
Dv 30,%0,13312
Mvvv 76,42,156
Dv 34,v1,13312
Nvvv 80,38,140,60
E_ 119
Mvvv 80,42,160
C_ 119,118,119
C_ 118,116,117
Nvvv 84,38,144,64
E_ 120
Mvvv 84,42,164
E_ 121
Nvvv 88,38,148,68
E_ 122
Mvvv 88,42,168
E_ 123
Nvvv 92,38,152,72
C_ 123,122,123
C_ 122,120,121
Mvvv 92,42,172
W_ 0
E_ 124
Mvvv 92,30,192
Dv 38,%0,14336
Mvvv 92,34,212
Dv 42,v1,14336
Mvvv 88,30,188
E_ 125
Mvvv 88,34,208
E_ 126
Mvvv 84,30,184
S_ s65,s61,86016
E_ 127
Mvvv 84,34,204
S_ m0,s51,12288
C_ 127,126,127
C_ 126,124,125
Mvvv 80,30,180
s_waitcnt vmcnt(6) lgkmcnt(0)
s_barrier
Mvvv 80,34,200
L_ v9,36,s65,0
.Lpad_a2_4:
s_cbranch_execz .Lhop_a2_r
Mvvv 76,30,176
E_ 128
Mvvv 76,34,196
L_ v9,36,s65,3072
S_ m0,s51,13184
Mvvv 76,38,216
Tv 30,v2,24576
Tv 32,v2,30720
Mvvv 76,42,236
L_ v9,36,s65,128
Mvvv 80,38,220
s_nop 0
Tv 34,v3,24576
Tv 36,v3,30720
Mvvv 80,42,240
L_ v9,36,s65,3200
S_ m0,s51,14080
Mvvv 84,38,224
E_ 129
Mvvv 84,42,244
L_ v9,36,s65,256
Mvvv 88,38,228
E_ 130
Mvvv 88,42,248
L_ v9,36,s65,3328
Mvvv 92,38,232
E_ 131
Mvvv 92,42,252
W_ 0
C_ 131,130,131
C_ 130,128,129
Mavv 0,30,96
s_nop 0
Tv 38,v4,24576
Tv 40,v4,30720
Mavv 20,34,96
E_ 132
Mavv 24,34,100
s_nop 0
Tv 42,v5,24576
Tv 44,v5,30720
Mavv 4,30,100
E_ 133
Mavv 8,30,104
E_ 134
Mavv 28,34,104
E_ 135
Mavv 32,34,108
C_ 135,134,135
C_ 134,132,133
Mavv 12,30,108
E_ 76
Mavv 16,30,112
E_ 77
Mavv 36,34,112
W_ 0
E_ 78
Mavv 56,38,112
s_nop 0
Tv 30,v2,25600
Tv 32,v2,31744
Mavv 76,42,112
E_ 79
Mavv 72,42,108
s_nop 0
Tv 34,v3,25600
Tv 36,v3,31744
Mavv 52,38,108
C_ 116,76,77
C_ 117,78,79
Mavv 48,38,104
E_ 80
Mavv 68,42,104
E_ 81
Mavv 64,42,100
E_ 82
Mavv 44,38,100
E_ 83
Mavv 40,38,96
C_ 120,80,81
C_ 121,82,83
Mavv 60,42,96
W_ 0
E_ 84
Mavv 80,30,96
s_nop 0
Tv 38,v4,25600
Tv 40,v4,31744
Mavv 100,34,96
E_ 85
Mavv 104,34,100
s_nop 0
Tv 42,v5,25600
Tv 44,v5,31744
Mavv 84,30,100
)A"
R"A(E_ 86
Mavv 88,30,104
E_ 87
Mavv 108,34,104
C_ 124,84,85
C_ 125,86,87
Mavv 112,34,108
E_ 88
Mavv 92,30,108
E_ 89
Mavv 96,30,112
E_ 90
Mavv 116,34,112
W_ 0
E_ 91
Mavv 136,38,112
s_nop 0
Tv 30,v2,26624
Tv 32,v2,32768
Mavv 156,42,112
C_ 128,88,89
C_ 129,90,91
Mavv 152,42,108
s_nop 0
Tv 34,v3,26624
Tv 36,v3,32768
Mavv 132,38,108
E_ 92
Mavv 128,38,104
E_ 93
Mavv 148,42,104
E_ 94
Mavv 144,42,100
E_ 95
Mavv 124,38,100
C_ 132,92,93
C_ 133,94,95
Mavv 120,38,96
v_mov_b32 v92, 0x3f803f80
v_mov_b32 v93, 0x3f803f80
Mavv 140,42,96
W_ 0
v_mov_b32 v94, 0x3f803f80
v_mov_b32 v95, 0x3f803f80
Mavv 160,30,96
s_nop 0
Tv 38,v4,26624
Tv 40,v4,32768
Mavv 180,34,96
Mavv 184,34,100
s_nop 0
Tv 42,v5,26624
Tv 44,v5,32768
Mavv 164,30,100
Mavv 168,30,104
Mavv 188,34,104
S_ s65,s61,110592
Mavv 192,34,108
S_ m0,s51,24576
Mavv 172,30,108
s_waitcnt vmcnt(6) lgkmcnt(0)
s_barrier
Mavv 176,30,112
Ln v7,32,s65,0
Mavv 196,34,112
Mavv 216,38,112
Dv 30,%0,6144
Mavv 236,42,112
Dv 34,v1,6144
Mavv 232,42,108
Ln v8,32,s65,3072
S_ m0,s51,25472
Mavv 212,38,108
Mavv 208,38,104
Ln v7,32,s65,128
Mavv 228,42,104
Mavv 224,42,100
Ln v8,32,s65,3200
S_ m0,s51,26368
Mavv 204,38,100
Mavv 200,38,96
Ln v7,32,s65,256
Mavv 220,42,96
.Lstep2_8:
Zvvv 76,92,116
Dv 38,%0,7168
Zvvv 80,92,120
Dv 42,v1,7168
Zvvv 84,92,124
Zvvv 88,92,128
Zvvv 92,92,132
W_ 2
Nvvv 96,30,136,56
Mvvv 96,34,156
Nvvv 100,30,140,60
s_nop 1
v_max3_f32 v10, v76, v80, v84
s_nop 0
v_max3_f32 v10, v10, v88, v92
Mvvv 100,34,160
v_cmp_lt_f32 vcc, 0x42000000, v10
s_cbranch_vccnz .Ltramp2_7
Nvvv 104,30,144,64
A_ v46,v46,v76
A_ v47,v47,v80
Mvvv 104,34,164
A_ v48,v48,v84
A_ v49,v49,v88
Nvvv 108,30,148,68
A_ v50,v50,v92
Mvvv 108,34,168
Ln v8,32,s65,3328
Nvvv 112,30,152,72
Mvvv 112,34,172
W_ 0
Mvvv 112,38,192
Dv 30,%0,8192
Mvvv 112,42,212
Dv 34,v1,8192
Mvvv 108,38,188
Mvvv 108,42,208
Mvvv 104,38,184
Mvvv 104,42,204
Mvvv 100,38,180
Mvvv 100,42,200
Mvvv 96,38,176
Mvvv 96,42,196
W_ 0
Mvvv 96,30,216
Dv 38,%0,0
Mvvv 96,34,236
Dv 42,v1,0
Mvvv 100,30,220
Mvvv 100,34,240
Mvvv 104,30,224
Mvvv 104,34,244
Mvvv 108,30,228
Mvvv 108,34,248
s_nop 0
E_ 96
Mvvv 112,30,232
E_ 97
Mvvv 112,34,252
W_ 0
E_ 98
Nvvv 76,38,136,56
Dv 30,%0,1024
Mvvv 76,42,156
Dv 34,v1,1024
Nvvv 80,38,140,60
E_ 99
Mvvv 80,42,160
C_ 99,98,99
C_ 98,96,97
Nvvv 84,38,144,64
E_ 100
Mvvv 84,42,164
E_ 101
Nvvv 88,38,148,68
E_ 102
Mvvv 88,42,168
E_ 103
Nvvv 92,38,152,72
C_ 103,102,103
C_ 102,100,101
Mvvv 92,42,172
W_ 0
E_ 104
Mvvv 92,30,192
Dv 38,%0,2048
Mvvv 92,34,212
Dv 42,v1,2048
Mvvv 88,30,188
E_ 105
Mvvv 88,34,208
E_ 106
Mvvv 84,30,184
S_ s65,s61,98304
E_ 107
Mvvv 84,34,204
S_ m0,s51,0
C_ 107,106,107
C_ 106,104,105
Mvvv 80,30,180
s_waitcnt vmcnt(6) lgkmcnt(0)
s_barrier
Mvvv 80,34,200
L_ v9,36,s65,0
Mvvv 76,30,176
E_ 108
Mvvv 76,34,196
L_ v9,36,s65,3072
S_ m0,s51,896
Mvvv 76,38,216
Tv 30,v2,12288
Tv 32,v2,18432
Mvvv 76,42,236
L_ v9,36,s65,128
Mvvv 80,38,220
s_nop 0
Tv 34,v3,12288
Tv 36,v3,18432
Mvvv 80,42,240
L_ v9,36,s65,3200
S_ m0,s51,1792
Mvvv 84,38,224
E_ 109
Mvvv 84,42,244
L_ v9,36,s65,256
Mvvv 88,38,228
E_ 110
Mvvv 88,42,248
L_ v9,36,s65,3328
Mvvv 92,38,232
E_ 111
Mvvv 92,42,252
W_ 0
C_ 111,110,111
C_ 110,108,109
Mavv 0,30,116
s_nop 0
Tv 38,v4,12288
Tv 40,v4,18432
Mavv 20,34,116
E_ 112
Mavv 24,34,120
s_nop 0
Tv 42,v5,12288
Tv 44,v5,18432
Mavv 4,30,120
E_ 113
Mavv 8,30,124
E_ 114
Mavv 28,34,124
E_ 115
Mavv 32,34,128
C_ 115,114,115
C_ 114,112,113
Mavv 12,30,128
E_ 76
Mavv 16,30,132
E_ 77
Mavv 36,34,132
W_ 0
E_ 78
Mavv 56,38,132
s_nop 0
Tv 30,v2,13312
Tv 32,v2,19456
Mavv 76,42,132
E_ 79
Mavv 72,42,128
s_nop 0
Tv 34,v3,13312
Tv 36,v3,19456
Mavv 52,38,128
C_ 96,76,77
C_ 97,78,79
Mavv 48,38,124
E_ 80
Mavv 68,42,124
E_ 81
Mavv 64,42,120
E_ 82
Mavv 44,38,120
E_ 83
Mavv 40,38,116
C_ 100,80,81
C_ 101,82,83
Mavv 60,42,116
W_ 0
E_ 84
Mavv 80,30,116
s_nop 0
Tv 38,v4,13312
Tv 40,v4,19456
Mavv 100,34,116
E_ 85
Mavv 104,34,120
s_nop 0
Tv 42,v5,13312
Tv 44,v5,19456
Mavv 84,30,120
E_ 86
Mavv 88,30,124
E_ 87
Mavv 108,34,124
C_ 104,84,85
C_ 105,86,87
Mavv 112,34,128
E_ 88
Mavv 92,30,128
E_ 89
Mavv 96,30,132
E_ 90
Mavv 116,34,132
W_ 0
E_ 91
Mavv 136,38,132
s_nop 0
Tv 30,v2,14336
Tv 32,v2,20480
Mavv 156,42,132
C_ 108,88,89
C_ 109,90,91
Mavv 152,42,128
s_nop 0
Tv 34,v3,14336
Tv 36,v3,20480
Mavv 132,38,128
E_ 92
Mavv 128,38,124
E_ 93
Mavv 148,42,124
E_ 94
Mavv 144,42,120
E_ 95
Mavv 124,38,120
C_ 112,92,93
C_ 113,94,95
Mavv 120,38,116
v_mov_b32 v92, 0x3f803f80
v_mov_b32 v93, 0x3f803f80
Mavv 140,42,116
W_ 0
v_mov_b32 v94, 0x3f803f80
v_mov_b32 v95, 0x3f803f80
Mavv 160,30,116
s_nop 0
Tv 38,v4,14336
Tv 40,v4,20480
Mavv 180,34,116
Mavv 184,34,120
s_nop 0
Tv 42,v5,14336
Tv 44,v5,20480
Mavv 164,30,120
Mavv 168,30,124
Mavv 188,34,124
S_ s65,s61,122880
Mavv 192,34,128
S_ m0,s51,12288
Mavv 172,30,128
s_waitcnt vmcnt(6) lgkmcnt(0)
s_barrier
Mavv 176,30,132
Ln v7,32,s65,0
Mavv 196,34,132
Mavv 216,38,132
Dv 30,%0,30720
Mavv 236,42,132
Dv 34,v1,30720
Mavv 232,42,128
Ln v8,32,s65,3072
S_ m0,s51,13184
Mavv 212,38,128
Mavv 208,38,124
Ln v7,32,s65,128
Mavv 228,42,124
Mavv 224,42,120
Ln v8,32,s65,3200
S_ m0,s51,14080
Mavv 204,38,120
Mavv 200,38,116
Ln v7,32,s65,256
Mavv 220,42,116
s_cmp_eq_u32 s4, 0
s_cbranch_scc1 .Lhs_d2
.Lhs_d2_back:
.Lstep2_9:
Zvvv 76,92,96
Dv 38,%0,31744
Zvvv 80,92,100
Dv 42,v1,31744
Zvvv 84,92,104
Zvvv 88,92,108
Zvvv 92,92,112
W_ 2
Nvvv 116,30,136,56
Mvvv 116,34,156
Nvvv 120,30,140,60
s_nop 1
v_max3_f32 v10, v76, v80, v84
s_nop 0
v_max3_f32 v10, v10, v88, v92
Mvvv 120,34,160
v_cmp_lt_f32 vcc, 0x42000000, v10
s_cbranch_vccnz .Ltramp2_8
Nvvv 124,30,144,64
A_ v46,v46,v76
A_ v47,v47,v80
Mvvv 124,34,164
A_ v48,v48,v84
A_ v49,v49,v88
Nvvv 128,30,148,68
A_ v50,v50,v92
Mvvv 128,34,168
Ln v8,32,s65,3328
Nvvv 132,30,152,72
Mvvv 132,34,172
W_ 0
Mvvv 132,38,192
Dv 30,%0,32768
Mvvv 132,42,212
Dv 34,v1,32768
Mvvv 128,38,188
Mvvv 128,42,208
Mvvv 124,38,184
Mvvv 124,42,204
Mvvv 120,38,180
Mvvv 120,42,200
Mvvv 116,38,176
Mvvv 116,42,196
W_ 0
Mvvv 116,30,216
Dv 38,%0,24576
Mvvv 116,34,236
Dv 42,v1,24576
Mvvv 120,30,220
Mvvv 120,34,240
Mvvv 124,30,224
Mvvv 124,34,244
Mvvv 128,30,228
Mvvv 128,34,248
s_nop 0
E_ 116
Mvvv 132,30,232
E_ 117
Mvvv 132,34,252
W_ 0
E_ 118
Nvvv 76,38,136,56
Dv 30,%0,25600
Mvvv 76,42,156
Dv 34,v1,25600
Nvvv 80,38,140,60
E_ 119
Mvvv 80,42,160
C_ 119,118,119
C_ 118,116,117
Nvvv 84,38,144,64
E_ 120
Mvvv 84,42,164
E_ 121
Nvvv 88,38,148,68
E_ 122
Mvvv 88,42,168
E_ 123
Nvvv 92,38,152,72
C_ 123,122,123
C_ 122,120,121
Mvvv 92,42,172
W_ 0
E_ 124
Mvvv 92,30,192
Dv 38,%0,26624
Mvvv 92,34,212
Dv 42,v1,26624
Mvvv 88,30,188
E_ 125
Mvvv 88,34,208
E_ 126
Mvvv 84,30,184
S_ s65,s61,110592
E_ 127
Mvvv 84,34,204
S_ m0,s51,24576
C_ 127,126,127
C_ 126,124,125
Mvvv 80,30,180
s_waitcnt vmcnt(6) lgkmcnt(0)
s_barrier
Mvvv 80,34,200
L_ v9,36,s65,0
Mvvv 76,30,176
E_ 128
Mvvv 76,34,196
L_ v9,36,s65,3072
S_ m0,s51,25472
Mvvv 76,38,216
Tv 30,v2,0
Tv 32,v2,6144
Mvvv 76,42,236
L_ v9,36,s65,128
Mvvv 80,38,220
s_nop 0
Tv 34,v3,0
Tv 36,v3,6144
Mvvv 80,42,240
L_ v9,36,s65,3200
S_ m0,s51,26368
Mvvv 84,38,224
.Lpad_a2_5:
s_cbranch_execz .Lhop_a2_r
E_ 129
Mvvv 84,42,244
L_ v9,36,s65,256
Mvvv 88,38,228
E_ 130
Mvvv 88,42,248
L_ v9,36,s65,3328
Mvvv 92,38,232
E_ 131
Mvvv 92,42,252
W_ 0
C_ 131,130,131
C_ 130,128,129
Mavv 0,30,96
s_nop 0
Tv 38,v4,0
Tv 40,v4,6144
Mavv 20,34,96
E_ 132
Mavv 24,34,100
s_nop 0
Tv 42,v5,0
Tv 44,v5,6144
Mavv 4,30,100
E_ 133
Mavv 8,30,104
E_ 134
Mavv 28,34,104
E_ 135
Mavv 32,34,108
C_ 135,134,135
C_ 134,132,133
Mavv 12,30,108
E_ 76
Mavv 16,30,112
E_ 77
Mavv 36,34,112
W_ 0
E_ 78
Mavv 56,38,112
s_nop 0
Tv 30,v2,1024
Tv 32,v2,7168
Mavv 76,42,112
E_ 79
Mavv 72,42,108
s_nop 0
Tv 34,v3,1024
Tv 36,v3,7168
Mavv 52,38,108
C_ 116,76,77
C_ 117,78,79
Mavv 48,38,104
E_ 80
Mavv 68,42,104
E_ 81
Mavv 64,42,100
E_ 82
Mavv 44,38,100
E_ 83
Mavv 40,38,96
C_ 120,80,81
C_ 121,82,83
Mavv 60,42,96
W_ 0
E_ 84
Mavv 80,30,96
s_nop 0
Tv 38,v4,1024
Tv 40,v4,7168
Mavv 100,34,96
E_ 85
Mavv 104,34,100
s_nop 0
Tv 42,v5,1024
Tv 44,v5,7168
Mavv 84,30,100
E_ 86
Mavv 88,30,104
E_ 87
Mavv 108,34,104
C_ 124,84,85
C_ 125,86,87
Mavv 112,34,108
E_ 88
Mavv 92,30,108
E_ 89
Mavv 96,30,112
E_ 90
Mavv 116,34,112
W_ 0
E_ 91
Mavv 136,38,112
s_nop 0
Tv 30,v2,2048
Tv 32,v2,8192
Mavv 156,42,112
C_ 128,88,89
C_ 129,90,91
Mavv 152,42,108
s_nop 0
Tv 34,v3,2048
Tv 36,v3,8192
Mavv 132,38,108
E_ 92
Mavv 128,38,104
E_ 93
Mavv 148,42,104
E_ 94
Mavv 144,42,100
E_ 95
Mavv 124,38,100
C_ 132,92,93
C_ 133,94,95
Mavv 120,38,96
v_mov_b32 v92, 0x3f803f80
v_mov_b32 v93, 0x3f803f80
Mavv 140,42,96
W_ 0
v_mov_b32 v94, 0x3f803f80
v_mov_b32 v95, 0x3f803f80
Mavv 160,30,96
s_nop 0
Tv 38,v4,2048
Tv 40,v4,8192
Mavv 180,34,96
Mavv 184,34,100
s_nop 0
Tv 42,v5,2048
Tv 44,v5,8192
Mavv 164,30,100
Mavv 168,30,104
Mavv 188,34,104
S_ s65,s61,135168
Mavv 192,34,108
S_ m0,s51,0
Mavv 172,30,108
s_waitcnt vmcnt(6) lgkmcnt(0)
s_barrier
Mavv 176,30,112
Ln v7,32,s65,0
Mavv 196,34,112
Mavv 216,38,112
Dv 30,%0,18432
Mavv 236,42,112
Dv 34,v1,18432
Mavv 232,42,108
Ln v8,32,s65,3072
S_ m0,s51,896
Mavv 212,38,108
Mavv 208,38,104
Ln v7,32,s65,128
Mavv 228,42,104
Mavv 224,42,100
Ln v8,32,s65,3200
S_ m0,s51,1792
Mavv 204,38,100
Mavv 200,38,96
Ln v7,32,s65,256
Mavv 220,42,96
.Lstep2_10:
Zvvv 76,92,116
Dv 38,%0,19456
Zvvv 80,92,120
Dv 42,v1,19456
Zvvv 84,92,124
Zvvv 88,92,128
Zvvv 92,92,132
W_ 2
Nvvv 96,30,136,56
Mvvv 96,34,156
Nvvv 100,30,140,60
s_nop 1
v_max3_f32 v10, v76, v80, v84
s_nop 0
v_max3_f32 v10, v10, v88, v92
Mvvv 100,34,160
v_cmp_lt_f32 vcc, 0x42000000, v10
s_cbranch_vccnz .Ltramp2_9
Nvvv 104,30,144,64
A_ v46,v46,v76
A_ v47,v47,v80
Mvvv 104,34,164
A_ v48,v48,v84
A_ v49,v49,v88
Nvvv 108,30,148,68
A_ v50,v50,v92
Mvvv 108,34,168
Ln v8,32,s65,3328
Nvvv 112,30,152,72
Mvvv 112,34,172
W_ 0
Mvvv 112,38,192
Dv 30,%0,20480
Mvvv 112,42,212
Dv 34,v1,20480
Mvvv 108,38,188
Mvvv 108,42,208
Mvvv 104,38,184
Mvvv 104,42,204
Mvvv 100,38,180
Mvvv 100,42,200
Mvvv 96,38,176
Mvvv 96,42,196
W_ 0
Mvvv 96,30,216
Dv 38,%0,12288
Mvvv 96,34,236
Dv 42,v1,12288
Mvvv 100,30,220
Mvvv 100,34,240
Mvvv 104,30,224
Mvvv 104,34,244
Mvvv 108,30,228
Mvvv 108,34,248
s_nop 0
E_ 96
Mvvv 112,30,232
E_ 97
Mvvv 112,34,252
W_ 0
E_ 98
Nvvv 76,38,136,56
Dv 30,%0,13312
Mvvv 76,42,156
Dv 34,v1,13312
Nvvv 80,38,140,60
E_ 99
Mvvv 80,42,160
C_ 99,98,99
C_ 98,96,97
Nvvv 84,38,144,64
E_ 100
Mvvv 84,42,164
E_ 101
Nvvv 88,38,148,68
E_ 102
Mvvv 88,42,168
E_ 103
Nvvv 92,38,152,72
C_ 103,102,103
C_ 102,100,101
Mvvv 92,42,172
W_ 0
E_ 104
Mvvv 92,30,192
Dv 38,%0,14336
Mvvv 92,34,212
Dv 42,v1,14336
Mvvv 88,30,188
E_ 105
Mvvv 88,34,208
E_ 106
Mvvv 84,30,184
S_ s65,s61,122880
E_ 107
Mvvv 84,34,204
S_ m0,s51,12288
C_ 107,106,107
C_ 106,104,105
Mvvv 80,30,180
s_waitcnt vmcnt(6) lgkmcnt(0)
s_barrier
Mvvv 80,34,200
L_ v9,36,s65,0
Mvvv 76,30,176
E_ 108
Mvvv 76,34,196
L_ v9,36,s65,3072
S_ m0,s51,13184
Mvvv 76,38,216
Tv 30,v2,24576
Tv 32,v2,30720
Mvvv 76,42,236
L_ v9,36,s65,128
Mvvv 80,38,220
s_nop 0
Tv 34,v3,24576
Tv 36,v3,30720
Mvvv 80,42,240
L_ v9,36,s65,3200
S_ m0,s51,14080
Mvvv 84,38,224
E_ 109
Mvvv 84,42,244
L_ v9,36,s65,256
Mvvv 88,38,228
E_ 110
Mvvv 88,42,248
L_ v9,36,s65,3328
Mvvv 92,38,232
E_ 111
Mvvv 92,42,252
W_ 0
C_ 111,110,111
C_ 110,108,109
Mavv 0,30,116
s_nop 0
Tv 38,v4,24576
Tv 40,v4,30720
Mavv 20,34,116
E_ 112
Mavv 24,34,120
s_nop 0
Tv 42,v5,24576
Tv 44,v5,30720
Mavv 4,30,120
E_ 113
Mavv 8,30,124
E_ 114
Mavv 28,34,124
E_ 115
Mavv 32,34,128
C_ 115,114,115
C_ 114,112,113
Mavv 12,30,128
E_ 76
Mavv 16,30,132
E_ 77
Mavv 36,34,132
W_ 0
E_ 78
Mavv 56,38,132
s_nop 0
Tv 30,v2,25600
Tv 32,v2,31744
Mavv 76,42,132
E_ 79
Mavv 72,42,128
s_nop 0
Tv 34,v3,25600
Tv 36,v3,31744
Mavv 52,38,128
C_ 96,76,77
C_ 97,78,79
Mavv 48,38,124
E_ 80
Mavv 68,42,124
E_ 81
Mavv 64,42,120
E_ 82
Mavv 44,38,120
E_ 83
Mavv 40,38,116
C_ 100,80,81
C_ 101,82,83
Mavv 60,42,116
W_ 0
E_ 84
Mavv 80,30,116
s_nop 0
Tv 38,v4,25600
Tv 40,v4,31744
Mavv 100,34,116
E_ 85
Mavv 104,34,120
s_nop 0
Tv 42,v5,25600
Tv 44,v5,31744
Mavv 84,30,120
E_ 86
Mavv 88,30,124
E_ 87
Mavv 108,34,124
C_ 104,84,85
C_ 105,86,87
Mavv 112,34,128
E_ 88
Mavv 92,30,128
E_ 89
Mavv 96,30,132
E_ 90
Mavv 116,34,132
W_ 0
E_ 91
Mavv 136,38,132
s_nop 0
Tv 30,v2,26624
Tv 32,v2,32768
Mavv 156,42,132
C_ 108,88,89
C_ 109,90,91
Mavv 152,42,128
s_nop 0
Tv 34,v3,26624
Tv 36,v3,32768
Mavv 132,38,128
E_ 92
Mavv 128,38,124
E_ 93
Mavv 148,42,124
E_ 94
Mavv 144,42,120
E_ 95
Mavv 124,38,120
C_ 112,92,93
C_ 113,94,95
Mavv 120,38,116
v_mov_b32 v92, 0x3f803f80
v_mov_b32 v93, 0x3f803f80
Mavv 140,42,116
W_ 0
v_mov_b32 v94, 0x3f803f80
v_mov_b32 v95, 0x3f803f80
Mavv 160,30,116
s_nop 0
Tv 38,v4,26624
Tv 40,v4,32768
Mavv 180,34,116
Mavv 184,34,120
s_nop 0
Tv 42,v5,26624
Tv 44,v5,32768
Mavv 164,30,120
Mavv 168,30,124
Mavv 188,34,124
S_ s65,s61,147456
Mavv 192,34,128
S_ m0,s51,24576
Mavv 172,30,128
s_waitcnt vmcnt(6) lgkmcnt(0)
s_barrier
Mavv 176,30,132
Ln v7,32,s65,0
Mavv 196,34,132
Mavv 216,38,132
Dv 30,%0,6144
Mavv 236,42,132
Dv 34,v1,6144
Mavv 232,42,128
Ln v8,32,s65,3072
S_ m0,s51,25472
Mavv 212,38,128
Mavv 208,38,124
Ln v7,32,s65,128
Mavv 228,42,124
Mavv 224,42,120
Ln v8,32,s65,3200
S_ m0,s51,26368
Mavv 204,38,120
Mavv 200,38,116
Ln v7,32,s65,256
Mavv 220,42,116
.Lstep2_11:
Zvvv 76,92,96
Dv 38,%0,7168
Zvvv 80,92,100
Dv 42,v1,7168
Zvvv 84,92,104
Zvvv 88,92,108
Zvvv 92,92,112
W_ 2
Nvvv 116,30,136,56
Mvvv 116,34,156
Nvvv 120,30,140,60
s_nop 1
v_max3_f32 v10, v76, v80, v84
s_nop 0
v_max3_f32 v10, v10, v88, v92
Mvvv 120,34,160
v_cmp_lt_f32 vcc, 0x42000000, v10
s_cbranch_vccnz .Ltramp2_10
Nvvv 124,30,144,64
A_ v46,v46,v76
A_ v47,v47,v80
Mvvv 124,34,164
A_ v48,v48,v84
A_ v49,v49,v88
Nvvv 128,30,148,68
A_ v50,v50,v92
Mvvv 128,34,168
Ln v8,32,s65,3328
Nvvv 132,30,152,72
Mvvv 132,34,172
W_ 0
Mvvv 132,38,192
Dv 30,%0,8192
Mvvv 132,42,212
Dv 34,v1,8192
Mvvv 128,38,188
Mvvv 128,42,208
Mvvv 124,38,184
Mvvv 124,42,204
Mvvv 120,38,180
Mvvv 120,42,200
Mvvv 116,38,176
Mvvv 116,42,196
W_ 0
Mvvv 116,30,216
Dv 38,%0,0
Mvvv 116,34,236
Dv 42,v1,0
Mvvv 120,30,220
Mvvv 120,34,240
Mvvv 124,30,224
Mvvv 124,34,244
Mvvv 128,30,228
Mvvv 128,34,248
s_nop 0
E_ 116
Mvvv 132,30,232
E_ 117
Mvvv 132,34,252
W_ 0
E_ 118
Nvvv 76,38,136,56
Dv 30,%0,1024
Mvvv 76,42,156
Dv 34,v1,1024
Nvvv 80,38,140,60
E_ 119
Mvvv 80,42,160
C_ 119,118,119
C_ 118,116,117
Nvvv 84,38,144,64
E_ 120
Mvvv 84,42,164
E_ 121
Nvvv 88,38,148,68
E_ 122
Mvvv 88,42,168
E_ 123
Nvvv 92,38,152,72
C_ 123,122,123
C_ 122,120,121
Mvvv 92,42,172
W_ 0
E_ 124
Mvvv 92,30,192
Dv 38,%0,2048
Mvvv 92,34,212
Dv 42,v1,2048
Mvvv 88,30,188
E_ 125
Mvvv 88,34,208
E_ 126
Mvvv 84,30,184
S_ s65,s61,135168
E_ 127
Mvvv 84,34,204
S_ m0,s51,0
C_ 127,126,127
C_ 126,124,125
Mvvv 80,30,180
s_waitcnt vmcnt(6) lgkmcnt(0)
s_barrier
Mvvv 80,34,200
L_ v9,36,s65,0
Mvvv 76,30,176
E_ 128
Mvvv 76,34,196
L_ v9,36,s65,3072
S_ m0,s51,896
Mvvv 76,38,216
Tv 30,v2,12288
Tv 32,v2,18432
Mvvv 76,42,236
L_ v9,36,s65,128
Mvvv 80,38,220
s_nop 0
Tv 34,v3,12288
Tv 36,v3,18432
Mvvv 80,42,240
L_ v9,36,s65,3200
S_ m0,s51,1792
Mvvv 84,38,224
E_ 129
Mvvv 84,42,244
L_ v9,36,s65,256
Mvvv 88,38,228
E_ 130
Mvvv 88,42,248
L_ v9,36,s65,3328
Mvvv 92,38,232
E_ 131
Mvvv 92,42,252
W_ 0
C_ 131,130,131
C_ 130,128,129
Mavv 0,30,96
s_nop 0
Tv 38,v4,12288
Tv 40,v4,18432
Mavv 20,34,96
E_ 132
Mavv 24,34,100
s_nop 0
Tv 42,v5,12288
Tv 44,v5,18432
Mavv 4,30,100
.Lpad_a2_6:
s_cbranch_execz .Lhop_a2_r
E_ 133
Mavv 8,30,104
E_ 134
Mavv 28,34,104
E_ 135
Mavv 32,34,108
C_ 135,134,135
C_ 134,132,133
Mavv 12,30,108
E_ 76
Mavv 16,30,112
E_ 77
Mavv 36,34,112
W_ 0
E_ 78
Mavv 56,38,112
s_nop 0
Tv 30,v2,13312
Tv 32,v2,19456
Mavv 76,42,112
E_ 79
Mavv 72,42,108
s_nop 0
Tv 34,v3,13312
Tv 36,v3,19456
Mavv 52,38,108
C_ 116,76,77
C_ 117,78,79
Mavv 48,38,104
E_ 80
Mavv 68,42,104
E_ 81
Mavv 64,42,100
E_ 82
Mavv 44,38,100
E_ 83
Mavv 40,38,96
C_ 120,80,81
C_ 121,82,83
Mavv 60,42,96
W_ 0
E_ 84
Mavv 80,30,96
s_nop 0
Tv 38,v4,13312
Tv 40,v4,19456
Mavv 100,34,96
E_ 85
Mavv 104,34,100
s_nop 0
Tv 42,v5,13312
Tv 44,v5,19456
Mavv 84,30,100
E_ 86
Mavv 88,30,104
E_ 87
Mavv 108,34,104
C_ 124,84,85
C_ 125,86,87
Mavv 112,34,108
E_ 88
Mavv 92,30,108
E_ 89
Mavv 96,30,112
E_ 90
Mavv 116,34,112
W_ 0
E_ 91
Mavv 136,38,112
s_nop 0
Tv 30,v2,14336
Tv 32,v2,20480
Mavv 156,42,112
C_ 128,88,89
C_ 129,90,91
Mavv 152,42,108
s_nop 0
Tv 34,v3,14336
Tv 36,v3,20480
Mavv 132,38,108
E_ 92
Mavv 128,38,104
E_ 93
Mavv 148,42,104
E_ 94
Mavv 144,42,100
E_ 95
Mavv 124,38,100
C_ 132,92,93
C_ 133,94,95
Mavv 120,38,96
v_mov_b32 v92, 0x3f803f80
v_mov_b32 v93, 0x3f803f80
Mavv 140,42,96
W_ 0
v_mov_b32 v94, 0x3f803f80
v_mov_b32 v95, 0x3f803f80
Mavv 160,30,96
s_nop 0
Tv 38,v4,14336
Tv 40,v4,20480
Mavv 180,34,96
Mavv 184,34,100
s_nop 0
Tv 42,v5,14336
Tv 44,v5,20480
Mavv 164,30,100
Mavv 168,30,104
Mavv 188,34,104
S_ s65,s61,159744
Mavv 192,34,108
S_ m0,s51,12288
Mavv 172,30,108
s_waitcnt vmcnt(6) lgkmcnt(0)
s_barrier
Mavv 176,30,112
Ln v7,32,s65,0
Mavv 196,34,112
Mavv 216,38,112
Dv 30,%0,30720
Mavv 236,42,112
Dv 34,v1,30720
Mavv 232,42,108
Ln v8,32,s65,3072
S_ m0,s51,13184
Mavv 212,38,108
Mavv 208,38,104
Ln v7,32,s65,128
Mavv 228,42,104
Mavv 224,42,100
Ln v8,32,s65,3200
S_ m0,s51,14080
Mavv 204,38,100
Mavv 200,38,96
Ln v7,32,s65,256
Mavv 220,42,96
.Lstep2_12:
Zvvv 76,92,116
Dv 38,%0,31744
Zvvv 80,92,120
Dv 42,v1,31744
Zvvv 84,92,124
Zvvv 88,92,128
Zvvv 92,92,132
W_ 2
Nvvv 96,30,136,56
Mvvv 96,34,156
Nvvv 100,30,140,60
s_nop 1
v_max3_f32 v10, v76, v80, v84
s_nop 0
v_max3_f32 v10, v10, v88, v92
Mvvv 100,34,160
v_cmp_lt_f32 vcc, 0x42000000, v10
s_cbranch_vccnz .Ltramp2_11
Nvvv 104,30,144,64
A_ v46,v46,v76
A_ v47,v47,v80
Mvvv 104,34,164
A_ v48,v48,v84
A_ v49,v49,v88
Nvvv 108,30,148,68
A_ v50,v50,v92
Mvvv 108,34,168
Ln v8,32,s65,3328
Nvvv 112,30,152,72
R_ v11,a248
Mvvv 112,34,172
W_ 0
v_min_u32 v11, 240, v11
Mvvv 112,38,192
Dv 30,%0,32768
v_lshlrev_b32 v11, 3, v11
Mvvv 112,42,212
Dv 34,v1,32768
Mvvv 108,38,188
buffer_load_dword a254, v11, s[44:47], s66 offen
Mvvv 108,42,208
Mvvv 104,38,184
Mvvv 104,42,204
Mvvv 100,38,180
Mvvv 100,42,200
Mvvv 96,38,176
Mvvv 96,42,196
W_ 0
Mvvv 96,30,216
Dv 38,%0,24576
Mvvv 96,34,236
Dv 42,v1,24576
Mvvv 100,30,220
Mvvv 100,34,240
Mvvv 104,30,224
Mvvv 104,34,244
Mvvv 108,30,228
Mvvv 108,34,248
s_nop 0
E_ 96
Mvvv 112,30,232
E_ 97
Mvvv 112,34,252
W_ 0
E_ 98
Nvvv 76,38,136,56
Dv 30,%0,25600
Mvvv 76,42,156
Dv 34,v1,25600
Nvvv 80,38,140,60
E_ 99
Mvvv 80,42,160
C_ 99,98,99
C_ 98,96,97
Nvvv 84,38,144,64
E_ 100
Mvvv 84,42,164
E_ 101
Nvvv 88,38,148,68
E_ 102
Mvvv 88,42,168
E_ 103
Nvvv 92,38,152,72
C_ 103,102,103
C_ 102,100,101
Mvvv 92,42,172
W_ 0
E_ 104
Mvvv 92,30,192
Dv 38,%0,26624
Mvvv 92,34,212
Dv 42,v1,26624
Mvvv 88,30,188
E_ 105
Mvvv 88,34,208
E_ 106
Mvvv 84,30,184
S_ s65,s61,147456
E_ 107
Mvvv 84,34,204
S_ m0,s51,24576
C_ 107,106,107
C_ 106,104,105
Mvvv 80,30,180
s_waitcnt vmcnt(7) lgkmcnt(0)
s_barrier
Mvvv 80,34,200
Ln v9,36,s65,0
Mvvv 76,30,176
E_ 108
Mvvv 76,34,196
Ln v9,36,s65,3072
S_ m0,s51,25472
Mvvv 76,38,216
Tv 30,v2,0
Tv 32,v2,6144
Mvvv 76,42,236
Ln v9,36,s65,128
Mvvv 80,38,220
s_nop 0
Tv 34,v3,0
Tv 36,v3,6144
Mvvv 80,42,240
Ln v9,36,s65,3200
S_ m0,s51,26368
Mvvv 84,38,224
E_ 109
Mvvv 84,42,244
Ln v9,36,s65,256
Mvvv 88,38,228
E_ 110
Mvvv 88,42,248
Ln v9,36,s65,3328
Mvvv 92,38,232
E_ 111
Mvvv 92,42,252
W_ 0
C_ 111,110,111
C_ 110,108,109
Mavv 0,30,116
s_nop 0
Tv 38,v4,0
Tv 40,v4,6144
Mavv 20,34,116
E_ 112
Mavv 24,34,120
s_nop 0
Tv 42,v5,0
Tv 44,v5,6144
Mavv 4,30,120
E_ 113
Mavv 8,30,124
E_ 114
Mavv 28,34,124
E_ 115
Mavv 32,34,128
C_ 115,114,115
C_ 114,112,113
Mavv 12,30,128
E_ 76
Mavv 16,30,132
E_ 77
Mavv 36,34,132
W_ 0
E_ 78
Mavv 56,38,132
s_nop 0
Tv 30,v2,1024
Tv 32,v2,7168
Mavv 76,42,132
E_ 79
Mavv 72,42,128
s_nop 0
Tv 34,v3,1024
Tv 36,v3,7168
Mavv 52,38,128
C_ 96,76,77
C_ 97,78,79
Mavv 48,38,124
E_ 80
Mavv 68,42,124
E_ 81
Mavv 64,42,120
E_ 82
Mavv 44,38,120
E_ 83
Mavv 40,38,116
C_ 100,80,81
C_ 101,82,83
Mavv 60,42,116
W_ 0
E_ 84
Mavv 80,30,116
s_nop 0
Tv 38,v4,1024
Tv 40,v4,7168
Mavv 100,34,116
E_ 85
Mavv 104,34,120
s_nop 0
Tv 42,v5,1024
Tv 44,v5,7168
Mavv 84,30,120
E_ 86
Mavv 88,30,124
E_ 87
Mavv 108,34,124
C_ 104,84,85
C_ 105,86,87
Mavv 112,34,128
E_ 88
Mavv 92,30,128
E_ 89
Mavv 96,30,132
E_ 90
Mavv 116,34,132
W_ 0
E_ 91
Mavv 136,38,132
s_nop 0
Tv 30,v2,2048
Tv 32,v2,8192
Mavv 156,42,132
C_ 108,88,89
C_ 109,90,91
Mavv 152,42,128
s_nop 0
Tv 34,v3,2048
Tv 36,v3,8192
Mavv 132,38,128
E_ 92
Mavv 128,38,124
E_ 93
Mavv 148,42,124
E_ 94
Mavv 144,42,120
E_ 95
Mavv 124,38,120
C_ 112,92,93
C_ 113,94,95
Mavv 120,38,116
v_mov_b32 v92, 0x3f803f80
v_mov_b32 v93, 0x3f803f80
Mavv 140,42,116
W_ 0
v_mov_b32 v94, 0x3f803f80
v_mov_b32 v95, 0x3f803f80
Mavv 160,30,116
s_nop 0
Tv 38,v4,2048
Tv 40,v4,8192
Mavv 180,34,116
Mavv 184,34,120
s_nop 0
Tv 42,v5,2048
Tv 44,v5,8192
Mavv 164,30,120
Mavv 168,30,124
Mavv 188,34,124
S_ s65,s61,172032
Mavv 192,34,128
S_ m0,s51,0
Mavv 172,30,128
s_waitcnt vmcnt(7) lgkmcnt(0)
s_barrier
Mavv 176,30,132
Ln v7,32,s65,0
Mavv 196,34,132
Mavv 216,38,132
Dv 30,%0,18432
Mavv 236,42,132
Dv 34,v1,18432
Mavv 232,42,128
Ln v8,32,s65,3072
S_ m0,s51,896
Mavv 212,38,128
Mavv 208,38,124
Ln v7,32,s65,128
Mavv 228,42,124
Mavv 224,42,120
Ln v8,32,s65,3200
S_ m0,s51,1792
Mavv 204,38,120
Mavv 200,38,116
Ln v7,32,s65,256
Mavv 220,42,116
.Lstep2_13:
Zvvv 76,92,96
Dv 38,%0,19456
Zvvv 80,92,100
Dv 42,v1,19456
Zvvv 84,92,104
Zvvv 88,92,108
Zvvv 92,92,112
W_ 2
Nvvv 116,30,136,56
Mvvv 116,34,156
Nvvv 120,30,140,60
s_nop 1
v_max3_f32 v10, v76, v80, v84
s_nop 0
v_max3_f32 v10, v10, v88, v92
Mvvv 120,34,160
v_cmp_lt_f32 vcc, 0x42000000, v10
s_cbranch_vccnz .Ltramp2_12
Nvvv 124,30,144,64
A_ v46,v46,v76
A_ v47,v47,v80
Mvvv 124,34,164
A_ v48,v48,v84
A_ v49,v49,v88
Nvvv 128,30,148,68
A_ v50,v50,v92
Mvvv 128,34,168
Ln v8,32,s65,3328
Nvvv 132,30,152,72
Mvvv 132,34,172
W_ 0
Mvvv 132,38,192
Dv 30,%0,20480
Mvvv 132,42,212
Dv 34,v1,20480
Mvvv 128,38,188
Mvvv 128,42,208
Mvvv 124,38,184
Mvvv 124,42,204
Mvvv 120,38,180
Mvvv 120,42,200
Mvvv 116,38,176
Mvvv 116,42,196
W_ 0
Mvvv 116,30,216
Dv 38,%0,12288
Mvvv 116,34,236
Dv 42,v1,12288
Mvvv 120,30,220
Mvvv 120,34,240
Mvvv 124,30,224
Mvvv 124,34,244
Mvvv 128,30,228
Mvvv 128,34,248
s_nop 0
E_ 116
Mvvv 132,30,232
E_ 117
Mvvv 132,34,252
W_ 0
E_ 118
Nvvv 76,38,136,56
Dv 30,%0,13312
Mvvv 76,42,156
Dv 34,v1,13312
Nvvv 80,38,140,60
E_ 119
Mvvv 80,42,160
C_ 119,118,119
C_ 118,116,117
Nvvv 84,38,144,64
E_ 120
Mvvv 84,42,164
E_ 121
Nvvv 88,38,148,68
E_ 122
Mvvv 88,42,168
E_ 123
Nvvv 92,38,152,72
C_ 123,122,123
C_ 122,120,121
Mvvv 92,42,172
W_ 0
E_ 124
Mvvv 92,30,192
Dv 38,%0,14336
Mvvv 92,34,212
Dv 42,v1,14336
Mvvv 88,30,188
E_ 125
Mvvv 88,34,208
E_ 126
Mvvv 84,30,184
S_ s65,s61,159744
E_ 127
Mvvv 84,34,204
S_ m0,s51,12288
C_ 127,126,127
C_ 126,124,125
Mvvv 80,30,180
s_waitcnt vmcnt(6) lgkmcnt(0)
s_barrier
Mvvv 80,34,200
Ln v9,36,s65,0
Mvvv 76,30,176
E_ 128
Mvvv 76,34,196
Ln v9,36,s65,3072
S_ m0,s51,13184
Mvvv 76,38,216
Tv 30,v2,24576
Tv 32,v2,30720
Mvvv 76,42,236
Ln v9,36,s65,128
Mvvv 80,38,220
s_nop 0
Tv 34,v3,24576
Tv 36,v3,30720
Mvvv 80,42,240
Ln v9,36,s65,3200
S_ m0,s51,14080
Mvvv 84,38,224
E_ 129
Mvvv 84,42,244
Ln v9,36,s65,256
Mvvv 88,38,228
E_ 130
Mvvv 88,42,248
Ln v9,36,s65,3328
Mvvv 92,38,232
E_ 131
Mvvv 92,42,252
W_ 0
C_ 131,130,131
C_ 130,128,129
Mavv 0,30,96
s_nop 0
Tv 38,v4,24576
Tv 40,v4,30720
Mavv 20,34,96
E_ 132
Mavv 24,34,100
s_nop 0
Tv 42,v5,24576
Tv 44,v5,30720
Mavv 4,30,100
E_ 133
Mavv 8,30,104
E_ 134
Mavv 28,34,104
E_ 135
Mavv 32,34,108
C_ 135,134,135
C_ 134,132,133
Mavv 12,30,108
E_ 76
Mavv 16,30,112
E_ 77
Mavv 36,34,112
W_ 0
E_ 78
Mavv 56,38,112
s_nop 0
Tv 30,v2,25600
Tv 32,v2,31744
Mavv 76,42,112
E_ 79
Mavv 72,42,108
.Lpad_a2_7:
s_cbranch_execz .Lhop_a2_r
Tv 34,v3,25600
Tv 36,v3,31744
Mavv 52,38,108
C_ 116,76,77
C_ 117,78,79
Mavv 48,38,104
E_ 80
Mavv 68,42,104
E_ 81
Mavv 64,42,100
E_ 82
Mavv 44,38,100
E_ 83
Mavv 40,38,96
C_ 120,80,81
C_ 121,82,83
Mavv 60,42,96
W_ 0
E_ 84
Mavv 80,30,96
s_nop 0
Tv 38,v4,25600
Tv 40,v4,31744
Mavv 100,34,96
E_ 85
Mavv 104,34,100
s_nop 0
Tv 42,v5,25600
Tv 44,v5,31744
Mavv 84,30,100
E_ 86
Mavv 88,30,104
E_ 87
Mavv 108,34,104
C_ 124,84,85
C_ 125,86,87
Mavv 112,34,108
E_ 88
Mavv 92,30,108
E_ 89
Mavv 96,30,112
E_ 90
Mavv 116,34,112
W_ 0
E_ 91
Mavv 136,38,112
s_nop 0
Tv 30,v2,26624
Tv 32,v2,32768
Mavv 156,42,112
C_ 128,88,89
C_ 129,90,91
Mavv 152,42,108
s_nop 0
Tv 34,v3,26624
Tv 36,v3,32768
Mavv 132,38,108
E_ 92
Mavv 128,38,104
E_ 93
Mavv 148,42,104
E_ 94
Mavv 144,42,100
E_ 95
Mavv 124,38,100
C_ 132,92,93
C_ 133,94,95
Mavv 120,38,96
v_mov_b32 v92, 0x3f803f80
v_mov_b32 v93, 0x3f803f80
Mavv 140,42,96
W_ 0
v_mov_b32 v94, 0x3f803f80
v_mov_b32 v95, 0x3f803f80
Mavv 160,30,96
s_nop 0
Tv 38,v4,26624
Tv 40,v4,32768
Mavv 180,34,96
Mavv 184,34,100
s_nop 0
Tv 42,v5,26624
Tv 44,v5,32768
Mavv 164,30,100
Mavv 168,30,104
Mavv 188,34,104
S_ s65,s61,184320
Mavv 192,34,108
S_ m0,s51,24576
Mavv 172,30,108
s_waitcnt vmcnt(6) lgkmcnt(0)
s_barrier
Mavv 176,30,112
Ln v7,32,s65,0
Mavv 196,34,112
Mavv 216,38,112
Dv 30,%0,6144
Mavv 236,42,112
Dv 34,v1,6144
Mavv 232,42,108
Ln v8,32,s65,3072
S_ m0,s51,25472
Mavv 212,38,108
Mavv 208,38,104
Ln v7,32,s65,128
Mavv 228,42,104
Mavv 224,42,100
Ln v8,32,s65,3200
S_ m0,s51,26368
Mavv 204,38,100
Mavv 200,38,96
Ln v7,32,s65,256
Mavv 220,42,96
.Lstep2_14:
Zvvv 76,92,116
Dv 38,%0,7168
Zvvv 80,92,120
Dv 42,v1,7168
Zvvv 84,92,124
Zvvv 88,92,128
Zvvv 92,92,132
W_ 2
Nvvv 96,30,136,56
Mvvv 96,34,156
Nvvv 100,30,140,60
s_nop 1
v_max3_f32 v10, v76, v80, v84
s_nop 0
v_max3_f32 v10, v10, v88, v92
Mvvv 100,34,160
v_cmp_lt_f32 vcc, 0x42000000, v10
s_cbranch_vccnz .Ltramp2_13
Nvvv 104,30,144,64
A_ v46,v46,v76
A_ v47,v47,v80
Mvvv 104,34,164
A_ v48,v48,v84
A_ v49,v49,v88
Nvvv 108,30,148,68
A_ v50,v50,v92
Mvvv 108,34,168
Ln v8,32,s65,3328
Nvvv 112,30,152,72
Mvvv 112,34,172
W_ 0
Mvvv 112,38,192
Dv 30,%0,8192
Mvvv 112,42,212
Dv 34,v1,8192
Mvvv 108,38,188
Mvvv 108,42,208
Mvvv 104,38,184
Mvvv 104,42,204
Mvvv 100,38,180
Mvvv 100,42,200
Mvvv 96,38,176
Mvvv 96,42,196
W_ 0
Mvvv 96,30,216
Dv 38,%0,0
Mvvv 96,34,236
Dv 42,v1,0
Mvvv 100,30,220
Mvvv 100,34,240
Mvvv 104,30,224
Mvvv 104,34,244
Mvvv 108,30,228
Mvvv 108,34,248
s_nop 0
E_ 96
Mvvv 112,30,232
E_ 97
Mvvv 112,34,252
W_ 0
E_ 98
Nvvv 76,38,136,56
Dv 30,%0,1024
Mvvv 76,42,156
Dv 34,v1,1024
Nvvv 80,38,140,60
E_ 99
Mvvv 80,42,160
C_ 99,98,99
C_ 98,96,97
Nvvv 84,38,144,64
E_ 100
Mvvv 84,42,164
E_ 101
Nvvv 88,38,148,68
E_ 102
Mvvv 88,42,168
E_ 103
Nvvv 92,38,152,72
C_ 103,102,103
C_ 102,100,101
Mvvv 92,42,172
W_ 0
E_ 104
Mvvv 92,30,192
Dv 38,%0,2048
Mvvv 92,34,212
Dv 42,v1,2048
Mvvv 88,30,188
E_ 105
Mvvv 88,34,208
E_ 106
Mvvv 84,30,184
S_ s65,s61,172032
E_ 107
Mvvv 84,34,204
S_ m0,s51,0
C_ 107,106,107
C_ 106,104,105
Mvvv 80,30,180
s_waitcnt vmcnt(6) lgkmcnt(0)
s_barrier
Mvvv 80,34,200
Ln v9,36,s65,0
Mvvv 76,30,176
E_ 108
Mvvv 76,34,196
Ln v9,36,s65,3072
S_ m0,s51,896
Mvvv 76,38,216
Tv 30,v2,12288
Tv 32,v2,18432
Mvvv 76,42,236
Ln v9,36,s65,128
Mvvv 80,38,220
s_nop 0
Tv 34,v3,12288
Tv 36,v3,18432
Mvvv 80,42,240
Ln v9,36,s65,3200
S_ m0,s51,1792
Mvvv 84,38,224
E_ 109
Mvvv 84,42,244
Ln v9,36,s65,256
Mvvv 88,38,228
E_ 110
Mvvv 88,42,248
Ln v9,36,s65,3328
Mvvv 92,38,232
E_ 111
Mvvv 92,42,252
W_ 0
C_ 111,110,111
C_ 110,108,109
Mavv 0,30,116
s_nop 0
Tv 38,v4,12288
Tv 40,v4,18432
Mavv 20,34,116
E_ 112
Mavv 24,34,120
s_nop 0
Tv 42,v5,12288
Tv 44,v5,18432
Mavv 4,30,120
E_ 113
Mavv 8,30,124
E_ 114
Mavv 28,34,124
E_ 115
Mavv 32,34,128
C_ 115,114,115
C_ 114,112,113
Mavv 12,30,128
E_ 76
Mavv 16,30,132
E_ 77
Mavv 36,34,132
W_ 0
E_ 78
Mavv 56,38,132
s_nop 0
Tv 30,v2,13312
Tv 32,v2,19456
Mavv 76,42,132
E_ 79
Mavv 72,42,128
s_nop 0
Tv 34,v3,13312
Tv 36,v3,19456
Mavv 52,38,128
C_ 96,76,77
C_ 97,78,79
Mavv 48,38,124
E_ 80
Mavv 68,42,124
E_ 81
Mavv 64,42,120
E_ 82
Mavv 44,38,120
E_ 83
Mavv 40,38,116
C_ 100,80,81
C_ 101,82,83
Mavv 60,42,116
W_ 0
E_ 84
Mavv 80,30,116
s_nop 0
Tv 38,v4,13312
Tv 40,v4,19456
Mavv 100,34,116
E_ 85
Mavv 104,34,120
s_nop 0
Tv 42,v5,13312
Tv 44,v5,19456
Mavv 84,30,120
E_ 86
Mavv 88,30,124
E_ 87
Mavv 108,34,124
C_ 104,84,85
C_ 105,86,87
Mavv 112,34,128
)A"
R"A(E_ 88
Mavv 92,30,128
E_ 89
Mavv 96,30,132
E_ 90
Mavv 116,34,132
W_ 0
E_ 91
Mavv 136,38,132
s_nop 0
Tv 30,v2,14336
Tv 32,v2,20480
Mavv 156,42,132
C_ 108,88,89
C_ 109,90,91
Mavv 152,42,128
s_nop 0
Tv 34,v3,14336
Tv 36,v3,20480
Mavv 132,38,128
E_ 92
Mavv 128,38,124
E_ 93
Mavv 148,42,124
E_ 94
Mavv 144,42,120
E_ 95
Mavv 124,38,120
C_ 112,92,93
C_ 113,94,95
Mavv 120,38,116
v_mov_b32 v92, 0x3f803f80
v_mov_b32 v93, 0x3f803f80
Mavv 140,42,116
W_ 0
v_mov_b32 v94, 0x3f803f80
v_mov_b32 v95, 0x3f803f80
Mavv 160,30,116
s_nop 0
Tv 38,v4,14336
Tv 40,v4,20480
Mavv 180,34,116
Mavv 184,34,120
s_nop 0
Tv 42,v5,14336
Tv 44,v5,20480
Mavv 164,30,120
Mavv 168,30,124
Mavv 188,34,124
S_ s65,s61,184320
Mavv 192,34,128
S_ m0,s51,12288
Mavv 172,30,128
s_waitcnt vmcnt(6) lgkmcnt(0)
s_barrier
Mavv 176,30,132
Ln v9,36,s65,0
Mavv 196,34,132
Mavv 216,38,132
Dv 30,%0,30720
Mavv 236,42,132
Dv 34,v1,30720
Mavv 232,42,128
Ln v9,36,s65,3072
S_ m0,s51,13184
Mavv 212,38,128
Mavv 208,38,124
Ln v9,36,s65,128
Mavv 228,42,124
Mavv 224,42,120
Ln v9,36,s65,3200
S_ m0,s51,14080
Mavv 204,38,120
Mavv 200,38,116
Ln v9,36,s65,256
Mavv 220,42,116
.Lstep2_15:
Zvvv 76,92,96
Dv 38,%0,31744
Zvvv 80,92,100
Dv 42,v1,31744
Zvvv 84,92,104
Zvvv 88,92,108
Zvvv 92,92,112
W_ 2
Nvvv 116,30,136,56
Mvvv 116,34,156
Nvvv 120,30,140,60
s_nop 1
v_max3_f32 v10, v76, v80, v84
s_nop 0
v_max3_f32 v10, v10, v88, v92
Mvvv 120,34,160
v_cmp_lt_f32 vcc, 0x42000000, v10
s_cbranch_vccnz .Ltramp2_14
Nvvv 124,30,144,64
A_ v46,v46,v76
A_ v47,v47,v80
Mvvv 124,34,164
A_ v48,v48,v84
A_ v49,v49,v88
Nvvv 128,30,148,68
A_ v50,v50,v92
Mvvv 128,34,168
Ln v9,36,s65,3328
Nvvv 132,30,152,72
Mvvv 132,34,172
W_ 0
Mvvv 132,38,192
Dv 30,%0,32768
Mvvv 132,42,212
Dv 34,v1,32768
Mvvv 128,38,188
Mvvv 128,42,208
Mvvv 124,38,184
Mvvv 124,42,204
Mvvv 120,38,180
Mvvv 120,42,200
Mvvv 116,38,176
Mvvv 116,42,196
W_ 0
Mvvv 116,30,216
Dv 38,%0,24576
Mvvv 116,34,236
Dv 42,v1,24576
Mvvv 120,30,220
Mvvv 120,34,240
Mvvv 124,30,224
Mvvv 124,34,244
Mvvv 128,30,228
Mvvv 128,34,248
s_nop 0
E_ 116
Mvvv 132,30,232
E_ 117
Mvvv 132,34,252
W_ 0
E_ 118
Nvvv 76,38,136,56
Dv 30,%0,25600
Mvvv 76,42,156
Dv 34,v1,25600
Nvvv 80,38,140,60
E_ 119
Mvvv 80,42,160
C_ 119,118,119
C_ 118,116,117
Nvvv 84,38,144,64
E_ 120
Mvvv 84,42,164
E_ 121
Nvvv 88,38,148,68
E_ 122
Mvvv 88,42,168
E_ 123
Nvvv 92,38,152,72
C_ 123,122,123
C_ 122,120,121
Mvvv 92,42,172
W_ 0
E_ 124
Mvvv 92,30,192
Dv 38,%0,26624
Mvvv 92,34,212
Dv 42,v1,26624
Mvvv 88,30,188
E_ 125
Mvvv 88,34,208
E_ 126
Mvvv 84,30,184
S_ s65,s61,393216
E_ 127
Mvvv 84,34,204
S_ m0,s51,24576
C_ 127,126,127
C_ 126,124,125
Mvvv 80,30,180
s_waitcnt vmcnt(6) lgkmcnt(0)
s_barrier
Mvvv 80,34,200
Ln v9,36,s65,0
Mvvv 76,30,176
E_ 128
Mvvv 76,34,196
Ln v9,36,s65,3072
S_ m0,s51,25472
Mvvv 76,38,216
Tv 30,v2,0
Tv 32,v2,6144
Mvvv 76,42,236
Ln v9,36,s65,128
Mvvv 80,38,220
s_nop 0
Tv 34,v3,0
Tv 36,v3,6144
Mvvv 80,42,240
Ln v9,36,s65,3200
S_ m0,s51,26368
Mvvv 84,38,224
E_ 129
Mvvv 84,42,244
Ln v9,36,s65,256
Mvvv 88,38,228
E_ 130
Mvvv 88,42,248
Ln v9,36,s65,3328
Mvvv 92,38,232
E_ 131
Mvvv 92,42,252
W_ 0
C_ 131,130,131
C_ 130,128,129
Mavv 0,30,96
s_nop 0
Tv 38,v4,0
Tv 40,v4,6144
Mavv 20,34,96
E_ 132
Mavv 24,34,100
s_nop 0
Tv 42,v5,0
Tv 44,v5,6144
Mavv 4,30,100
E_ 133
Mavv 8,30,104
E_ 134
Mavv 28,34,104
E_ 135
Mavv 32,34,108
C_ 135,134,135
C_ 134,132,133
Mavv 12,30,108
E_ 76
Mavv 16,30,112
E_ 77
Mavv 36,34,112
W_ 0
E_ 78
Mavv 56,38,112
s_nop 0
Tv 30,v2,1024
Tv 32,v2,7168
Mavv 76,42,112
E_ 79
Mavv 72,42,108
s_nop 0
Tv 34,v3,1024
Tv 36,v3,7168
Mavv 52,38,108
C_ 116,76,77
C_ 117,78,79
Mavv 48,38,104
E_ 80
Mavv 68,42,104
E_ 81
Mavv 64,42,100
E_ 82
Mavv 44,38,100
E_ 83
Mavv 40,38,96
C_ 120,80,81
C_ 121,82,83
Mavv 60,42,96
W_ 0
E_ 84
Mavv 80,30,96
s_nop 0
Tv 38,v4,1024
Tv 40,v4,7168
Mavv 100,34,96
E_ 85
Mavv 104,34,100
s_nop 0
Tv 42,v5,1024
Tv 44,v5,7168
Mavv 84,30,100
E_ 86
Mavv 88,30,104
E_ 87
Mavv 108,34,104
C_ 124,84,85
C_ 125,86,87
Mavv 112,34,108
E_ 88
Mavv 92,30,108
E_ 89
Mavv 96,30,112
E_ 90
Mavv 116,34,112
W_ 0
E_ 91
Mavv 136,38,112
s_nop 0
Tv 30,v2,2048
Tv 32,v2,8192
Mavv 156,42,112
C_ 128,88,89
C_ 129,90,91
Mavv 152,42,108
s_nop 0
Tv 34,v3,2048
Tv 36,v3,8192
Mavv 132,38,108
E_ 92
Mavv 128,38,104
E_ 93
Mavv 148,42,104
E_ 94
Mavv 144,42,100
E_ 95
Mavv 124,38,100
C_ 132,92,93
C_ 133,94,95
Mavv 120,38,96
v_mov_b32 v92, 0x3f803f80
v_mov_b32 v93, 0x3f803f80
Mavv 140,42,96
W_ 0
v_mov_b32 v94, 0x3f803f80
v_mov_b32 v95, 0x3f803f80
Mavv 160,30,96
s_nop 0
Tv 38,v4,2048
Tv 40,v4,8192
Mavv 180,34,96
Mavv 184,34,100
s_nop 0
Tv 42,v5,2048
Tv 44,v5,8192
Mavv 164,30,100
Mavv 168,30,104
Mavv 188,34,104
S_ s65,s61,393216
Mavv 192,34,108
S_ m0,s51,0
Mavv 172,30,108
s_waitcnt vmcnt(6) lgkmcnt(0)
s_barrier
Mavv 176,30,112
Ln v7,32,s65,0
Mavv 196,34,112
Mavv 216,38,112
s_nop 0
Tv 30,v2,12288
Tv 32,v2,18432
Zvvv 76,92,116
Ln v8,32,s65,3072
S_ m0,s51,896
Zvvv 80,92,120
s_nop 0
Tv 34,v3,12288
Tv 36,v3,18432
Zvvv 84,92,124
Ln v7,32,s65,128
Zvvv 88,92,128
Zvvv 92,92,132
Ln v8,32,s65,3200
S_ m0,s51,1792
Mavv 236,42,112
Mavv 232,42,108
Ln v7,32,s65,256
Mavv 212,38,108
Mavv 208,38,104
v_max3_f32 v10, v76, v80, v84
Mavv 228,42,104
v_max3_f32 v10, v10, v88, v92
v_cmp_lt_f32 vcc, 0x42000000, v10
Mavv 224,42,100
Ln v8,32,s65,3328
Mavv 204,38,100
Mavv 200,38,96
Mavv 220,42,96
s_cbranch_vccnz .Ltramp2_15
.Ljoin2:
W_ 2
Mavv 0,30,116
s_mov_b32 s58, 1
Tv 38,v4,12288
Tv 40,v4,18432
W_ 2
Mavv 20,34,116
R_ v11,a248
R_ v13,a248
Mavv 24,34,120
S_ s53,s51,36864
Tv 42,v5,12288
Tv 44,v5,18432
Mavv 4,30,120
v_add_u32 v12, s53, v13
s_mul_i32 s53, s4, 10240
Mavv 8,30,124
v_add_u32 v13, s53, v13
A_ v46,v46,v76
Mavv 28,34,124
A_ v47,v47,v80
Mavv 32,34,128
A_ v48,v48,v84
A_ v49,v49,v88
Mavv 12,30,128
A_ v50,v50,v92
Mavv 16,30,132
v_rcp_f32 v46, v46
Mavv 36,34,132
W_ 0
v_rcp_f32 v47, v47
Mavv 56,38,132
s_nop 0
Tv 30,v2,13312
Tv 32,v2,19456
Mavv 76,42,132
v_rcp_f32 v48, v48
Mavv 72,42,128
s_nop 0
Tv 34,v3,13312
Tv 36,v3,19456
Mavv 52,38,128
v_rcp_f32 v49, v49
Mavv 48,38,124
v_rcp_f32 v50, v50
Mavv 68,42,124
s_mov_b32 s56, 12288
v_mov_b32 v176, v20
v_mov_b32 v177, v21
Mavv 64,42,120
Dv 136,v13,0
v_mov_b32 v178, v22
Mavv 44,38,120
Dv 140,v13,1024
v_mov_b32 v179, v23
Mavv 40,38,116
Dv 144,v13,2048
v_mov_b32 v180, v24
Mavv 60,42,116
W_ 3
Dv 148,v13,3072
v_mov_b32 v181, v25
Mavv 80,30,116
s_nop 0
Tv 38,v4,13312
Tv 40,v4,19456
Mavv 100,34,116
Dv 152,v13,4096
v_mov_b32 v182, v26
Mavv 104,34,120
s_nop 0
Tv 42,v5,13312
Tv 44,v5,19456
Mavv 84,30,120
Dv 156,v13,5120
v_mov_b32 v183, v27
Mavv 88,30,124
Dv 160,v13,6144
v_mov_b32 v184, v52
Mavv 108,34,124
Dv 164,v13,7168
v_mov_b32 v185, v53
Mavv 112,34,128
Dv 168,v13,8192
v_mov_b32 v186, v54
Mavv 92,30,128
Dv 172,v13,9216
v_mov_b32 v187, v55
Mavv 96,30,132
Dv 208,v12,0
R_ v188,a240
Mavv 116,34,132
W_ 6
Dv 212,v12,1024
R_ v189,a241
Mavv 136,38,132
s_nop 0
Tv 30,v2,14336
Tv 32,v2,20480
Mavv 156,42,132
Dv 216,v12,2048
R_ v190,a242
Mavv 152,42,128
s_nop 0
Tv 34,v3,14336
Tv 36,v3,20480
Mavv 132,38,128
Dv 220,v12,3072
R_ v191,a243
Mavv 128,38,124
Dv 224,v12,4096
R_ v192,a244
Mavv 148,42,124
Dv 228,v12,5120
R_ v193,a245
Mavv 144,42,120
Dv 232,v12,12288
R_ v194,a246
Mavv 124,38,120
Dv 236,v12,13312
R_ v195,a247
Mavv 120,38,116
Dv 240,v12,14336
v_mov_b32 v196, v16
Mavv 140,42,116
W_ 6
Dv 244,v12,15360
v_mov_b32 v197, v17
Mavv 160,30,116
s_nop 0
Tv 38,v4,14336
Tv 40,v4,20480
Mavv 180,34,116
Dv 248,v12,16384
v_mov_b32 v198, v18
Mavv 184,34,120
s_nop 0
Tv 42,v5,14336
Tv 44,v5,20480
Mavv 164,30,120
Dv 252,v12,17408
v_mov_b32 v199, v19
Mavv 168,30,124
v_mov_b32 v200, v28
v_mov_b32 v201, v29
Mavv 188,34,124
v_mov_b32 v202, v51
v_mov_b32 v203, v6
Mavv 192,34,128
v_mov_b32 v204, v14
v_mov_b32 v205, v15
Mavv 172,30,128
S_ s65,s61,405504
R_ v206,a249
R_ v207,a250
Mavv 176,30,132
S_ m0,s51,12288
R_ v96,a0
R_ v97,a1
Mavv 196,34,132
s_waitcnt vmcnt(63) lgkmcnt(0)
s_barrier
R_ v98,a2
Mavv 216,38,132
Ln v7,32,s65,0
Mavv 236,42,132
R_ v99,a3
Mavv 232,42,128
Ln v8,32,s65,3072
S_ m0,s51,13184
Mavv 212,38,128
R_ v100,a20
R_ v101,a21
Mavv 208,38,124
Ln v7,32,s65,128
Mavv 228,42,124
R_ v102,a22
R_ v103,a23
Mavv 224,42,120
Ln v8,32,s65,3200
S_ m0,s51,14080
Mavv 204,38,120
U_ v96,v96,v46
U_ v97,v97,v46
Mavv 200,38,116
Ln v7,32,s65,256
Mavv 220,42,116
U_ v98,v98,v46
U_ v99,v99,v46
U_ v100,v100,v46
U_ v101,v101,v46
U_ v102,v102,v46
U_ v103,v103,v46
C_ 76,96,97
C_ 77,98,99
C_ 78,100,101
C_ 79,102,103
ds_write_b128 v13, v[76:79] offset:0
Ln v8,32,s65,3328
S_ s61,s61,393216
.p2align 3
R_ v104,a4
R_ v105,a5
R_ v106,a6
R_ v107,a7
R_ v108,a24
R_ v109,a25
R_ v110,a26
R_ v111,a27
v_pk_mul_f32 v[104:105], v[104:105], v[46:47] op_sel:[0,1]
v_pk_mul_f32 v[106:107], v[106:107], v[46:47] op_sel:[0,1]
v_pk_mul_f32 v[108:109], v[108:109], v[46:47] op_sel:[0,1]
v_pk_mul_f32 v[110:111], v[110:111], v[46:47] op_sel:[0,1]
C_ 80,104,105
C_ 81,106,107
C_ 82,108,109
C_ 83,110,111
ds_write_b128 v13, v[80:83] offset:1024
.p2align 3
R_ v96,a8
R_ v97,a9
R_ v98,a10
R_ v99,a11
R_ v100,a28
R_ v101,a29
R_ v102,a30
R_ v103,a31
v_pk_mul_f32 v[96:97], v[96:97], v[48:49] op_sel_hi:[1,0]
v_pk_mul_f32 v[98:99], v[98:99], v[48:49] op_sel_hi:[1,0]
v_pk_mul_f32 v[100:101], v[100:101], v[48:49] op_sel_hi:[1,0]
v_pk_mul_f32 v[102:103], v[102:103], v[48:49] op_sel_hi:[1,0]
C_ 84,96,97
C_ 85,98,99
C_ 86,100,101
C_ 87,102,103
ds_write_b128 v13, v[84:87] offset:2048
.p2align 3
R_ v104,a12
R_ v105,a13
R_ v106,a14
R_ v107,a15
R_ v108,a32
R_ v109,a33
R_ v110,a34
R_ v111,a35
v_pk_mul_f32 v[104:105], v[104:105], v[48:49] op_sel:[0,1]
v_pk_mul_f32 v[106:107], v[106:107], v[48:49] op_sel:[0,1]
v_pk_mul_f32 v[108:109], v[108:109], v[48:49] op_sel:[0,1]
v_pk_mul_f32 v[110:111], v[110:111], v[48:49] op_sel:[0,1]
C_ 88,104,105
C_ 89,106,107
C_ 90,108,109
C_ 91,110,111
ds_write_b128 v13, v[88:91] offset:3072
.p2align 3
R_ v96,a16
R_ v97,a17
R_ v98,a18
R_ v99,a19
R_ v100,a36
R_ v101,a37
R_ v102,a38
R_ v103,a39
v_pk_mul_f32 v[96:97], v[96:97], v[50:51] op_sel_hi:[1,0]
v_pk_mul_f32 v[98:99], v[98:99], v[50:51] op_sel_hi:[1,0]
v_pk_mul_f32 v[100:101], v[100:101], v[50:51] op_sel_hi:[1,0]
v_pk_mul_f32 v[102:103], v[102:103], v[50:51] op_sel_hi:[1,0]
C_ 92,96,97
C_ 93,98,99
C_ 94,100,101
C_ 95,102,103
ds_write_b128 v13, v[92:95] offset:4096
.p2align 3
R_ v104,a40
R_ v105,a41
R_ v106,a42
R_ v107,a43
R_ v108,a60
R_ v109,a61
R_ v110,a62
R_ v111,a63
v_pk_mul_f32 v[104:105], v[104:105], v[46:47] op_sel_hi:[1,0]
v_pk_mul_f32 v[106:107], v[106:107], v[46:47] op_sel_hi:[1,0]
v_pk_mul_f32 v[108:109], v[108:109], v[46:47] op_sel_hi:[1,0]
v_pk_mul_f32 v[110:111], v[110:111], v[46:47] op_sel_hi:[1,0]
C_ 76,104,105
C_ 77,106,107
C_ 78,108,109
C_ 79,110,111
ds_write_b128 v13, v[76:79] offset:5120
.p2align 3
R_ v96,a44
R_ v97,a45
R_ v98,a46
R_ v99,a47
R_ v100,a64
R_ v101,a65
R_ v102,a66
R_ v103,a67
v_pk_mul_f32 v[96:97], v[96:97], v[46:47] op_sel:[0,1]
v_pk_mul_f32 v[98:99], v[98:99], v[46:47] op_sel:[0,1]
v_pk_mul_f32 v[100:101], v[100:101], v[46:47] op_sel:[0,1]
v_pk_mul_f32 v[102:103], v[102:103], v[46:47] op_sel:[0,1]
C_ 80,96,97
C_ 81,98,99
C_ 82,100,101
C_ 83,102,103
ds_write_b128 v13, v[80:83] offset:6144
.p2align 3
R_ v104,a48
R_ v105,a49
R_ v106,a50
R_ v107,a51
R_ v108,a68
R_ v109,a69
R_ v110,a70
R_ v111,a71
v_pk_mul_f32 v[104:105], v[104:105], v[48:49] op_sel_hi:[1,0]
v_pk_mul_f32 v[106:107], v[106:107], v[48:49] op_sel_hi:[1,0]
v_pk_mul_f32 v[108:109], v[108:109], v[48:49] op_sel_hi:[1,0]
v_pk_mul_f32 v[110:111], v[110:111], v[48:49] op_sel_hi:[1,0]
C_ 84,104,105
C_ 85,106,107
C_ 86,108,109
C_ 87,110,111
ds_write_b128 v13, v[84:87] offset:7168
.p2align 3
R_ v96,a52
R_ v97,a53
R_ v98,a54
R_ v99,a55
R_ v100,a72
R_ v101,a73
R_ v102,a74
R_ v103,a75
v_pk_mul_f32 v[96:97], v[96:97], v[48:49] op_sel:[0,1]
v_pk_mul_f32 v[98:99], v[98:99], v[48:49] op_sel:[0,1]
v_pk_mul_f32 v[100:101], v[100:101], v[48:49] op_sel:[0,1]
v_pk_mul_f32 v[102:103], v[102:103], v[48:49] op_sel:[0,1]
C_ 88,96,97
C_ 89,98,99
C_ 90,100,101
C_ 91,102,103
ds_write_b128 v13, v[88:91] offset:8192
.p2align 3
R_ v104,a56
R_ v105,a57
R_ v106,a58
R_ v107,a59
R_ v108,a76
R_ v109,a77
R_ v110,a78
R_ v111,a79
v_pk_mul_f32 v[104:105], v[104:105], v[50:51] op_sel_hi:[1,0]
v_pk_mul_f32 v[106:107], v[106:107], v[50:51] op_sel_hi:[1,0]
v_pk_mul_f32 v[108:109], v[108:109], v[50:51] op_sel_hi:[1,0]
v_pk_mul_f32 v[110:111], v[110:111], v[50:51] op_sel_hi:[1,0]
C_ 92,104,105
C_ 93,106,107
C_ 94,108,109
C_ 95,110,111
ds_write_b128 v13, v[92:95] offset:9216
.p2align 3
R_ v104,a92
R_ v105,a93
R_ v106,a94
R_ v107,a95
R_ v108,a112
R_ v109,a113
R_ v110,a114
R_ v111,a115
v_pk_mul_f32 v[104:105], v[104:105], v[48:49] op_sel:[0,1]
v_pk_mul_f32 v[106:107], v[106:107], v[48:49] op_sel:[0,1]
v_pk_mul_f32 v[108:109], v[108:109], v[48:49] op_sel:[0,1]
v_pk_mul_f32 v[110:111], v[110:111], v[48:49] op_sel:[0,1]
C_ 88,104,105
C_ 89,106,107
C_ 90,108,109
C_ 91,110,111
X_ a240,v88
X_ a241,v89
X_ a242,v90
X_ a243,v91
.p2align 3
R_ v96,a96
R_ v97,a97
R_ v98,a98
R_ v99,a99
R_ v100,a116
R_ v101,a117
R_ v102,a118
R_ v103,a119
v_pk_mul_f32 v[96:97], v[96:97], v[50:51] op_sel_hi:[1,0]
v_pk_mul_f32 v[98:99], v[98:99], v[50:51] op_sel_hi:[1,0]
v_pk_mul_f32 v[100:101], v[100:101], v[50:51] op_sel_hi:[1,0]
v_pk_mul_f32 v[102:103], v[102:103], v[50:51] op_sel_hi:[1,0]
C_ 92,96,97
C_ 93,98,99
C_ 94,100,101
C_ 95,102,103
X_ a244,v92
X_ a245,v93
X_ a246,v94
X_ a247,v95
.p2align 3
R_ v96,a124
R_ v97,a125
R_ v98,a126
R_ v99,a127
R_ v100,a144
R_ v101,a145
R_ v102,a146
R_ v103,a147
v_pk_mul_f32 v[96:97], v[96:97], v[46:47] op_sel:[0,1]
v_pk_mul_f32 v[98:99], v[98:99], v[46:47] op_sel:[0,1]
v_pk_mul_f32 v[100:101], v[100:101], v[46:47] op_sel:[0,1]
v_pk_mul_f32 v[102:103], v[102:103], v[46:47] op_sel:[0,1]
C_ 80,96,97
C_ 81,98,99
C_ 82,100,101
C_ 83,102,103
v_mov_b32 v28, v80
v_mov_b32 v29, v81
v_mov_b32 v51, v82
v_mov_b32 v6, v83
.p2align 3
R_ v104,a128
R_ v105,a129
R_ v106,a130
R_ v107,a131
R_ v108,a148
R_ v109,a149
R_ v110,a150
R_ v111,a151
v_pk_mul_f32 v[104:105], v[104:105], v[48:49] op_sel_hi:[1,0]
v_pk_mul_f32 v[106:107], v[106:107], v[48:49] op_sel_hi:[1,0]
v_pk_mul_f32 v[108:109], v[108:109], v[48:49] op_sel_hi:[1,0]
v_pk_mul_f32 v[110:111], v[110:111], v[48:49] op_sel_hi:[1,0]
C_ 84,104,105
C_ 85,106,107
C_ 86,108,109
C_ 87,110,111
v_mov_b32 v14, v84
v_mov_b32 v15, v85
X_ a249,v86
X_ a250,v87
.p2align 3
R_ v96,a132
R_ v97,a133
R_ v98,a134
R_ v99,a135
R_ v100,a152
R_ v101,a153
R_ v102,a154
R_ v103,a155
v_pk_mul_f32 v[96:97], v[96:97], v[48:49] op_sel:[0,1]
v_pk_mul_f32 v[98:99], v[98:99], v[48:49] op_sel:[0,1]
v_pk_mul_f32 v[100:101], v[100:101], v[48:49] op_sel:[0,1]
v_pk_mul_f32 v[102:103], v[102:103], v[48:49] op_sel:[0,1]
C_ 88,96,97
C_ 89,98,99
C_ 90,100,101
C_ 91,102,103
ds_write_b128 v12, v[88:91] offset:0
.p2align 3
R_ v104,a136
R_ v105,a137
R_ v106,a138
R_ v107,a139
R_ v108,a156
R_ v109,a157
R_ v110,a158
R_ v111,a159
v_pk_mul_f32 v[104:105], v[104:105], v[50:51] op_sel_hi:[1,0]
v_pk_mul_f32 v[106:107], v[106:107], v[50:51] op_sel_hi:[1,0]
v_pk_mul_f32 v[108:109], v[108:109], v[50:51] op_sel_hi:[1,0]
v_pk_mul_f32 v[110:111], v[110:111], v[50:51] op_sel_hi:[1,0]
C_ 92,104,105
C_ 93,106,107
C_ 94,108,109
C_ 95,110,111
ds_write_b128 v12, v[92:95] offset:1024
.p2align 3
R_ v96,a160
R_ v97,a161
R_ v98,a162
R_ v99,a163
R_ v100,a180
R_ v101,a181
R_ v102,a182
R_ v103,a183
v_pk_mul_f32 v[96:97], v[96:97], v[46:47] op_sel_hi:[1,0]
v_pk_mul_f32 v[98:99], v[98:99], v[46:47] op_sel_hi:[1,0]
v_pk_mul_f32 v[100:101], v[100:101], v[46:47] op_sel_hi:[1,0]
v_pk_mul_f32 v[102:103], v[102:103], v[46:47] op_sel_hi:[1,0]
C_ 76,96,97
C_ 77,98,99
C_ 78,100,101
C_ 79,102,103
ds_write_b128 v12, v[76:79] offset:2048
.p2align 3
R_ v104,a164
R_ v105,a165
R_ v106,a166
R_ v107,a167
R_ v108,a184
R_ v109,a185
R_ v110,a186
R_ v111,a187
v_pk_mul_f32 v[104:105], v[104:105], v[46:47] op_sel:[0,1]
v_pk_mul_f32 v[106:107], v[106:107], v[46:47] op_sel:[0,1]
v_pk_mul_f32 v[108:109], v[108:109], v[46:47] op_sel:[0,1]
v_pk_mul_f32 v[110:111], v[110:111], v[46:47] op_sel:[0,1]
C_ 80,104,105
C_ 81,106,107
C_ 82,108,109
C_ 83,110,111
ds_write_b128 v12, v[80:83] offset:3072
.p2align 3
R_ v96,a168
R_ v97,a169
R_ v98,a170
R_ v99,a171
R_ v100,a188
R_ v101,a189
R_ v102,a190
R_ v103,a191
v_pk_mul_f32 v[96:97], v[96:97], v[48:49] op_sel_hi:[1,0]
v_pk_mul_f32 v[98:99], v[98:99], v[48:49] op_sel_hi:[1,0]
v_pk_mul_f32 v[100:101], v[100:101], v[48:49] op_sel_hi:[1,0]
v_pk_mul_f32 v[102:103], v[102:103], v[48:49] op_sel_hi:[1,0]
C_ 84,96,97
C_ 85,98,99
C_ 86,100,101
C_ 87,102,103
ds_write_b128 v12, v[84:87] offset:4096
.p2align 3
R_ v104,a172
R_ v105,a173
R_ v106,a174
R_ v107,a175
R_ v108,a192
R_ v109,a193
R_ v110,a194
R_ v111,a195
v_pk_mul_f32 v[104:105], v[104:105], v[48:49] op_sel:[0,1]
v_pk_mul_f32 v[106:107], v[106:107], v[48:49] op_sel:[0,1]
v_pk_mul_f32 v[108:109], v[108:109], v[48:49] op_sel:[0,1]
v_pk_mul_f32 v[110:111], v[110:111], v[48:49] op_sel:[0,1]
C_ 88,104,105
C_ 89,106,107
C_ 90,108,109
C_ 91,110,111
ds_write_b128 v12, v[88:91] offset:5120
.p2align 3
R_ v96,a176
R_ v97,a177
R_ v98,a178
R_ v99,a179
R_ v100,a196
R_ v101,a197
R_ v102,a198
R_ v103,a199
v_pk_mul_f32 v[96:97], v[96:97], v[50:51] op_sel_hi:[1,0]
v_pk_mul_f32 v[98:99], v[98:99], v[50:51] op_sel_hi:[1,0]
v_pk_mul_f32 v[100:101], v[100:101], v[50:51] op_sel_hi:[1,0]
v_pk_mul_f32 v[102:103], v[102:103], v[50:51] op_sel_hi:[1,0]
C_ 92,96,97
C_ 93,98,99
C_ 94,100,101
C_ 95,102,103
ds_write_b128 v12, v[92:95] offset:12288
.p2align 3
R_ v104,a200
R_ v105,a201
R_ v106,a202
R_ v107,a203
R_ v108,a220
R_ v109,a221
R_ v110,a222
R_ v111,a223
v_pk_mul_f32 v[104:105], v[104:105], v[46:47] op_sel_hi:[1,0]
v_pk_mul_f32 v[106:107], v[106:107], v[46:47] op_sel_hi:[1,0]
v_pk_mul_f32 v[108:109], v[108:109], v[46:47] op_sel_hi:[1,0]
v_pk_mul_f32 v[110:111], v[110:111], v[46:47] op_sel_hi:[1,0]
C_ 76,104,105
C_ 77,106,107
C_ 78,108,109
C_ 79,110,111
ds_write_b128 v12, v[76:79] offset:13312
.p2align 3
R_ v96,a204
R_ v97,a205
R_ v98,a206
R_ v99,a207
R_ v100,a224
R_ v101,a225
R_ v102,a226
R_ v103,a227
v_pk_mul_f32 v[96:97], v[96:97], v[46:47] op_sel:[0,1]
v_pk_mul_f32 v[98:99], v[98:99], v[46:47] op_sel:[0,1]
v_pk_mul_f32 v[100:101], v[100:101], v[46:47] op_sel:[0,1]
v_pk_mul_f32 v[102:103], v[102:103], v[46:47] op_sel:[0,1]
C_ 80,96,97
C_ 81,98,99
C_ 82,100,101
C_ 83,102,103
ds_write_b128 v12, v[80:83] offset:14336
.p2align 3
R_ v104,a208
R_ v105,a209
R_ v106,a210
R_ v107,a211
R_ v108,a228
R_ v109,a229
R_ v110,a230
R_ v111,a231
v_pk_mul_f32 v[104:105], v[104:105], v[48:49] op_sel_hi:[1,0]
v_pk_mul_f32 v[106:107], v[106:107], v[48:49] op_sel_hi:[1,0]
v_pk_mul_f32 v[108:109], v[108:109], v[48:49] op_sel_hi:[1,0]
v_pk_mul_f32 v[110:111], v[110:111], v[48:49] op_sel_hi:[1,0]
C_ 84,104,105
C_ 85,106,107
C_ 86,108,109
C_ 87,110,111
ds_write_b128 v12, v[84:87] offset:15360
s_waitcnt vmcnt(6) lgkmcnt(0)
s_barrier
Dv 30,%0,6144
Dv 34,v1,6144
Dv 38,%0,7168
.p2align 3
R_ v96,a212
R_ v97,a213
R_ v98,a214
R_ v99,a215
R_ v100,a232
R_ v101,a233
R_ v102,a234
R_ v103,a235
v_pk_mul_f32 v[96:97], v[96:97], v[48:49] op_sel:[0,1]
v_pk_mul_f32 v[98:99], v[98:99], v[48:49] op_sel:[0,1]
v_pk_mul_f32 v[100:101], v[100:101], v[48:49] op_sel:[0,1]
v_pk_mul_f32 v[102:103], v[102:103], v[48:49] op_sel:[0,1]
C_ 88,96,97
C_ 89,98,99
C_ 90,100,101
C_ 91,102,103
ds_write_b128 v12, v[88:91] offset:16384
.p2align 3
R_ v104,a216
R_ v105,a217
R_ v106,a218
R_ v107,a219
R_ v108,a236
R_ v109,a237
R_ v110,a238
R_ v111,a239
v_pk_mul_f32 v[104:105], v[104:105], v[50:51] op_sel_hi:[1,0]
v_pk_mul_f32 v[106:107], v[106:107], v[50:51] op_sel_hi:[1,0]
v_pk_mul_f32 v[108:109], v[108:109], v[50:51] op_sel_hi:[1,0]
v_pk_mul_f32 v[110:111], v[110:111], v[50:51] op_sel_hi:[1,0]
C_ 92,104,105
C_ 93,106,107
C_ 94,108,109
C_ 95,110,111
ds_write_b128 v12, v[92:95] offset:17408
.Latt3:
W_ 4
Zvvv 96,30,136
Dv 42,v1,7168
R_ v116,a80
Zvvv 100,30,140
R_ v117,a81
R_ v118,a82
Zvvv 104,30,144
R_ v119,a83
R_ v120,a100
Zvvv 108,30,148
R_ v121,a101
R_ v122,a102
Zvvv 112,30,152
W_ 4
R_ v123,a103
U_ v116,v116,v46
Mvvv 96,34,156
Dv 30,%0,8192
U_ v117,v117,v46
Mvvv 100,34,160
U_ v118,v118,v46
U_ v119,v119,v46
Mvvv 104,34,164
U_ v120,v120,v46
U_ v121,v121,v46
Mvvv 108,34,168
U_ v122,v122,v46
U_ v123,v123,v46
Mvvv 112,34,172
W_ 4
C_ 20,116,117
C_ 21,118,119
Mvvv 96,38,176
Dv 34,v1,8192
C_ 22,120,121
Mvvv 100,38,180
C_ 23,122,123
R_ v124,a84
Mvvv 104,38,184
R_ v125,a85
R_ v126,a86
Mvvv 108,38,188
R_ v127,a87
R_ v128,a104
Mvvv 112,38,192
W_ 2
R_ v129,a105
R_ v130,a106
Mvvv 96,42,196
Dv 38,%0,0
R_ v131,a107
Mvvv 100,42,200
U_ v124,v124,v47
U_ v125,v125,v47
Mvvv 104,42,204
U_ v126,v126,v47
U_ v127,v127,v47
Mvvv 108,42,208
U_ v128,v128,v47
U_ v129,v129,v47
Mvvv 112,42,212
W_ 2
U_ v130,v130,v47
U_ v131,v131,v47
Mvvv 96,30,216
Dv 42,v1,0
C_ 24,124,125
Mvvv 100,30,220
C_ 25,126,127
C_ 26,128,129
Mvvv 104,30,224
C_ 27,130,131
R_ v116,a88
Mvvv 108,30,228
R_ v117,a89
R_ v118,a90
Mvvv 112,30,232
W_ 2
R_ v119,a91
R_ v120,a108
Mvvv 96,34,236
Dv 30,%0,1024
R_ v121,a109
Mvvv 100,34,240
R_ v122,a110
R_ v123,a111
Mvvv 104,34,244
U_ v116,v116,v48
U_ v117,v117,v48
Mvvv 108,34,248
U_ v118,v118,v48
U_ v119,v119,v48
Mvvv 112,34,252
W_ 2
U_ v120,v120,v48
U_ v121,v121,v48
Zvvv 76,38,136
Dv 34,v1,1024
U_ v122,v122,v48
Zvvv 80,38,140
U_ v123,v123,v48
C_ 52,116,117
Zvvv 84,38,144
C_ 53,118,119
C_ 54,120,121
Zvvv 88,38,148
C_ 55,122,123
R_ v124,a120
Zvvv 92,38,152
W_ 2
R_ v125,a121
R_ v126,a122
Mvvv 76,42,156
Dv 38,%0,2048
R_ v127,a123
Mvvv 80,42,160
R_ v128,a140
R_ v129,a141
Mvvv 84,42,164
R_ v130,a142
R_ v131,a143
Mvvv 88,42,168
U_ v124,v124,v46
U_ v125,v125,v46
Mvvv 92,42,172
W_ 2
U_ v126,v126,v46
U_ v127,v127,v46
Mvvv 76,30,176
Dv 42,v1,2048
U_ v128,v128,v46
Mvvv 80,30,180
U_ v129,v129,v46
U_ v130,v130,v46
Mvvv 84,30,184
U_ v131,v131,v46
C_ 16,124,125
Mvvv 88,30,188
C_ 17,126,127
C_ 18,128,129
Mvvv 92,30,192
s_waitcnt vmcnt(0) lgkmcnt(0)
s_barrier
Mvvv 76,34,196
S_ s65,s61,24576
Dv 30,%0,18432
C_ 19,130,131
Mvvv 76,38,216
S_ m0,s51,0
v_max3_f32 v56, v96, v97, v98
v_max_f32 v56, v56, v99
Mvvv 76,42,236
Ln v7,32,s65,0
Mvvv 80,34,200
v_max3_f32 v60, v100, v101, v102
v_max_f32 v60, v60, v103
Mvvv 80,38,220
Ln v8,32,s65,3072
Mvvv 80,42,240
S_ m0,s51,896
v_max3_f32 v64, v104, v105, v106
v_max_f32 v64, v64, v107
Mvvv 84,34,204
Ln v7,32,s65,128
Mvvv 84,38,224
v_max3_f32 v68, v108, v109, v110
v_max_f32 v68, v68, v111
Mvvv 84,42,244
Ln v8,32,s65,3200
Mvvv 88,34,208
S_ m0,s51,1792
v_max3_f32 v72, v112, v113, v114
v_max_f32 v72, v72, v115
Mvvv 88,38,228
Ln v7,32,s65,256
Mvvv 88,42,248
v_max3_f32 v56, v56, v76, v77
v_max3_f32 v56, v56, v78, v79
Mvvv 92,34,212
Ln v8,32,s65,3328
Mvvv 92,38,232
v_max3_f32 v60, v60, v80, v81
v_max3_f32 v60, v60, v82, v83
Mvvv 92,42,252
v_max3_f32 v64, v64, v84, v85
v_max3_f32 v64, v64, v86, v87
Dv 34,v1,18432
.p2align 3
v_mov_b32 v11, 0x3f803f80
v_permlane32_swap_b32 v56, v60
v_max3_f32 v68, v68, v88, v89
v_max3_f32 v68, v68, v90, v91
v_max_f32 v56, v56, v60
v_max3_f32 v72, v72, v92, v93
v_max3_f32 v72, v72, v94, v95
v_mov_b32 v73, v72
v_permlane32_swap_b32 v64, v68
v_max_f32 v64, v64, v68
v_permlane32_swap_b32 v72, v73
v_max_f32 v72, v72, v73
v_mov_b32 v73, v72
v_permlane16_swap_b32 v56, v64
v_max_f32 v56, v56, v64
v_permlane16_swap_b32 v72, v73
B_ v56,0xc0e00000,v56
v_max_f32 v72, v72, v73
v_mov_b32 v64, v56
B_ v72,0xc0e00000,v72
v_mov_b32 v73, v72
v_permlane16_swap_b32 v56, v64
v_mov_b32 v60, v56
v_mov_b32 v68, v64
v_mov_b64 v[74:75], v[72:73]
v_permlane32_swap_b32 v56, v60
v_permlane32_swap_b32 v64, v68
v_mov_b32 v57, v56
v_mov_b32 v61, v60
v_mov_b32 v65, v64
v_mov_b32 v69, v68
v_pk_add_f32 v[96:97], v[96:97], v[56:57]
v_pk_add_f32 v[98:99], v[98:99], v[56:57]
v_pk_add_f32 v[76:77], v[76:77], v[56:57]
v_pk_add_f32 v[78:79], v[78:79], v[56:57]
v_mov_b64 v[58:59], v[56:57]
v_pk_add_f32 v[100:101], v[100:101], v[60:61]
v_pk_add_f32 v[102:103], v[102:103], v[60:61]
v_pk_add_f32 v[80:81], v[80:81], v[60:61]
v_pk_add_f32 v[82:83], v[82:83], v[60:61]
v_mov_b64 v[62:63], v[60:61]
v_pk_add_f32 v[104:105], v[104:105], v[64:65]
v_pk_add_f32 v[106:107], v[106:107], v[64:65]
v_pk_add_f32 v[84:85], v[84:85], v[64:65]
v_pk_add_f32 v[86:87], v[86:87], v[64:65]
v_mov_b64 v[66:67], v[64:65]
v_pk_add_f32 v[108:109], v[108:109], v[68:69]
v_pk_add_f32 v[110:111], v[110:111], v[68:69]
v_pk_add_f32 v[88:89], v[88:89], v[68:69]
v_pk_add_f32 v[90:91], v[90:91], v[68:69]
v_mov_b64 v[70:71], v[68:69]
v_pk_add_f32 v[112:113], v[112:113], v[72:73]
v_pk_add_f32 v[114:115], v[114:115], v[72:73]
v_pk_add_f32 v[92:93], v[92:93], v[72:73]
v_pk_add_f32 v[94:95], v[94:95], v[72:73]
E_ 96
E_ 97
E_ 98
E_ 99
E_ 76
E_ 77
E_ 78
E_ 79
C_ 99,98,99
C_ 98,96,97
C_ 96,76,77
C_ 97,78,79
E_ 100
E_ 101
E_ 102
E_ 103
E_ 80
E_ 81
E_ 82
E_ 83
C_ 103,102,103
C_ 102,100,101
C_ 100,80,81
C_ 101,82,83
.Lstep3_1:
W_ 1
Nvvv 116,30,136,56
s_nop 0
Dv 38,%0,19456
Dv 42,v1,19456
W_ 2
Mvvv 116,34,156
E_ 104
Nvvv 120,30,140,60
E_ 105
Mvvv 120,34,160
E_ 106
Nvvv 124,30,144,64
E_ 107
Mvvv 124,34,164
E_ 84
Nvvv 128,30,148,68
E_ 85
Mvvv 128,34,168
E_ 86
Nvvv 132,30,152,72
E_ 87
Mvvv 132,34,172
W_ 0
C_ 107,106,107
C_ 106,104,105
Mvvv 132,38,192
s_nop 0
Dv 30,%0,20480
Dv 34,v1,20480
Mvvv 132,42,212
C_ 104,84,85
C_ 105,86,87
Mvvv 128,38,188
E_ 108
Mvvv 128,42,208
E_ 109
Mvvv 124,38,184
E_ 110
Mvvv 124,42,204
E_ 111
Mvvv 120,38,180
E_ 88
Mvvv 120,42,200
E_ 89
Mvvv 116,38,176
E_ 90
Mvvv 116,42,196
W_ 0
E_ 91
Mvvv 116,30,216
s_nop 0
Dv 38,%0,12288
Dv 42,v1,12288
Mvvv 116,34,236
C_ 111,110,111
C_ 110,108,109
Mvvv 120,30,220
C_ 108,88,89
C_ 109,90,91
Mvvv 120,34,240
E_ 112
Mvvv 124,30,224
E_ 113
Mvvv 124,34,244
E_ 114
Mvvv 128,30,228
E_ 115
Mvvv 128,34,248
E_ 92
Mvvv 132,30,232
E_ 93
Mvvv 132,34,252
W_ 0
E_ 94
Nvvv 76,38,136,56
s_nop 0
Dv 30,%0,13312
Dv 34,v1,13312
Mvvv 76,42,156
E_ 95
Nvvv 80,38,140,60
C_ 115,114,115
C_ 114,112,113
Mvvv 80,42,160
C_ 112,92,93
C_ 113,94,95
Nvvv 84,38,144,64
E_ 116
Mvvv 84,42,164
E_ 117
Nvvv 88,38,148,68
E_ 118
Mvvv 88,42,168
E_ 119
Nvvv 92,38,152,72
C_ 119,118,119
C_ 118,116,117
Mvvv 92,42,172
W_ 0
E_ 120
Mvvv 92,30,192
s_nop 0
Dv 38,%0,14336
Dv 42,v1,14336
Mvvv 92,34,212
X_ a180,v11
X_ a181,v11
Mvvv 88,30,188
X_ a182,v11
X_ a183,v11
Mvvv 88,34,208
E_ 121
Mvvv 84,30,184
E_ 122
Mvvv 84,34,204
E_ 123
Mvvv 80,30,180
C_ 123,122,123
C_ 122,120,121
Mvvv 80,34,200
E_ 124
Mvvv 76,30,176
E_ 125
Mvvv 76,34,196
S_ s65,s61,12288
E_ 126
W_ 1
Mvvv 76,38,216
S_ m0,s51,12288
E_ 127
W_ 0
Mvvv 76,42,236
s_waitcnt vmcnt(18) lgkmcnt(0)
s_barrier
C_ 127,126,127
Mvvv 80,38,220
Ln v9,36,s65,0
Mvvv 80,42,240
C_ 126,124,125
Mvvv 84,38,224
Ln v9,36,s65,3072
S_ m0,s51,13184
Mvvv 84,42,244
s_nop 0
Tv 30,v2,24576
Tv 32,v2,30720
Mvvv 88,38,228
Ln v9,36,s65,128
Mvvv 88,42,248
s_nop 0
Tv 34,v3,24576
Tv 36,v3,30720
Mvvv 92,38,232
Ln v9,36,s65,3200
S_ m0,s51,14080
Mvvv 92,42,252
E_ 128
Zaav 220,180,96
Ln v9,36,s65,256
Zaav 224,180,100
E_ 129
Zaav 228,180,104
Ln v9,36,s65,3328
Zaav 232,180,108
E_ 130
Zaav 236,180,112
W_ 0
E_ 131
Zavv 0,30,96
s_nop 0
Tv 38,v4,24576
Tv 40,v4,30720
Zavv 20,34,96
C_ 131,130,131
C_ 130,128,129
Zavv 24,34,100
s_nop 0
Tv 42,v5,24576
Tv 44,v5,30720
Zavv 4,30,100
E_ 132
Zavv 8,30,104
E_ 133
Zavv 28,34,104
R_ v46,a220
R_ v47,a224
Zavv 32,34,108
R_ v48,a228
R_ v49,a232
Zavv 12,30,108
R_ v50,a236
Zavv 16,30,112
E_ 134
Zavv 36,34,112
W_ 0
E_ 135
Zavv 56,38,112
s_nop 0
Tv 30,v2,25600
Tv 32,v2,31744
Zavv 76,42,112
C_ 135,134,135
C_ 134,132,133
Zavv 72,42,108
s_nop 0
Tv 34,v3,25600
Tv 36,v3,31744
Zavv 52,38,108
E_ 76
Zavv 48,38,104
E_ 77
Zavv 68,42,104
E_ 78
Zavv 64,42,100
E_ 79
Zavv 44,38,100
C_ 116,76,77
C_ 117,78,79
Zavv 40,38,96
E_ 80
Zavv 60,42,96
W_ 0
E_ 81
Zavv 80,30,96
s_nop 0
Tv 38,v4,25600
Tv 40,v4,31744
Zavv 100,34,96
E_ 82
Zavv 104,34,100
s_nop 0
Tv 42,v5,25600
Tv 44,v5,31744
Zavv 84,30,100
E_ 83
Zavv 88,30,104
C_ 120,80,81
C_ 121,82,83
Zavv 108,34,104
E_ 84
Zavv 112,34,108
E_ 85
Zavv 92,30,108
E_ 86
Zavv 96,30,112
E_ 87
Zavv 116,34,112
W_ 0
C_ 124,84,85
C_ 125,86,87
Zavv 136,38,112
s_nop 0
Tv 30,v2,26624
Tv 32,v2,32768
Zavv 156,42,112
E_ 88
Zavv 152,42,108
s_nop 0
Tv 34,v3,26624
Tv 36,v3,32768
Zavv 132,38,108
E_ 89
Zavv 128,38,104
E_ 90
Zavv 148,42,104
E_ 91
Zavv 144,42,100
C_ 128,88,89
C_ 129,90,91
Zavv 124,38,100
E_ 92
Zavv 120,38,96
E_ 93
Zavv 140,42,96
W_ 0
E_ 94
Zavv 160,30,96
s_nop 0
Tv 38,v4,26624
Tv 40,v4,32768
Zavv 180,34,96
E_ 95
Zavv 184,34,100
s_nop 0
Tv 42,v5,26624
Tv 44,v5,32768
Zavv 164,30,100
C_ 132,92,93
C_ 133,94,95
Zavv 168,30,104
v_mov_b32 v92, 0x3f803f80
v_mov_b32 v93, 0x3f803f80
Zavv 188,34,104
S_ s65,s61,36864
v_mov_b32 v94, 0x3f803f80
v_mov_b32 v95, 0x3f803f80
Zavv 192,34,108
S_ m0,s51,24576
Zavv 172,30,108
s_waitcnt vmcnt(6) lgkmcnt(0)
s_barrier
Zavv 176,30,112
Ln v7,32,s65,0
Zavv 196,34,112
Zavv 216,38,112
Dv 30,%0,6144
Zavv 236,42,112
Dv 34,v1,6144
Zavv 232,42,108
Ln v8,32,s65,3072
S_ m0,s51,25472
Zavv 212,38,108
Zavv 208,38,104
Ln v7,32,s65,128
Zavv 228,42,104
Zavv 224,42,100
Ln v8,32,s65,3200
S_ m0,s51,26368
Zavv 204,38,100
Zavv 200,38,96
Ln v7,32,s65,256
Zavv 220,42,96
Ln v8,32,s65,3328
W_ 0
s_nop 7
s_branch .Lstep1_2
.Latt1:
R_ v13,a248
S_ s53,s51,36864
v_add_u32 v12, s53, v13
s_mul_i32 s53, s4, 10240
S_ s53,s53,0
.Lpad_a1_0:
s_cbranch_execz .Lhop_a1_r
v_add_u32 v13, s53, v13
W_ 2
Zvvv 96,30,136
Dv 42,v1,44032
R_ v2,a245
Zvvv 100,30,140
ds_write_b128 v13, a[0:3] offset:0
Zvvv 104,30,144
v_xor_b32 v3, 32, v2
v_xor_b32 v4, 64, v2
Zvvv 108,30,148
ds_write_b128 v13, a[4:7] offset:1024
Zvvv 112,30,152
W_ 4
v_xor_b32 v5, 96, v2
s_mov_b64 s[24:25], s[16:17]
Mvvv 96,34,156
s_mov_b32 s26, 7864320
Dv 30,%0,45056
s_mov_b64 s[28:29], s[22:23]
Mvvv 100,34,160
ds_write_b128 v13, a[8:11] offset:2048
Mvvv 104,34,164
s_mov_b32 s30, 1638400
R_ v20,a80
s_mul_i32 s52, s4, 10240
Mvvv 108,34,168
ds_write_b128 v13, a[12:15] offset:3072
Mvvv 112,34,172
W_ 6
R_ v21,a81
R_ v22,a82
Mvvv 96,38,176
Dv 34,v1,45056
R_ v23,a83
Mvvv 100,38,180
ds_write_b128 v13, a[16:19] offset:4096
Mvvv 104,38,184
R_ v24,a84
R_ v25,a85
Mvvv 108,38,188
ds_write_b128 v13, a[40:43] offset:5120
Mvvv 112,38,192
W_ 8
R_ v26,a86
R_ v27,a87
Mvvv 96,42,196
Dv 38,%0,36864
R_ v52,a88
Mvvv 100,42,200
ds_write_b128 v13, a[44:47] offset:6144
Mvvv 104,42,204
R_ v53,a89
R_ v54,a90
Mvvv 108,42,208
ds_write_b128 v13, a[48:51] offset:7168
Mvvv 112,42,212
W_ 8
R_ v55,a91
v_accvgpr_mov_b32 a240, a92
Mvvv 96,30,216
Dv 42,v1,36864
v_accvgpr_mov_b32 a241, a93
Mvvv 100,30,220
ds_write_b128 v13, a[52:55] offset:8192
Mvvv 104,30,224
v_accvgpr_mov_b32 a242, a94
v_accvgpr_mov_b32 a243, a95
Mvvv 108,30,228
ds_write_b128 v13, a[56:59] offset:9216
Mvvv 112,30,232
W_ 8
v_accvgpr_mov_b32 a244, a96
v_accvgpr_mov_b32 a245, a97
Mvvv 96,34,236
Dv 30,%0,37888
v_accvgpr_mov_b32 a246, a98
Mvvv 100,34,240
v_accvgpr_mov_b32 a247, a99
R_ v16,a120
Mvvv 104,34,244
R_ v17,a121
R_ v18,a122
Mvvv 108,34,248
R_ v19,a123
R_ v28,a124
Mvvv 112,34,252
W_ 6
R_ v29,a125
R_ v51,a126
Zvvv 76,38,136
Dv 34,v1,37888
R_ v6,a127
Zvvv 80,38,140
R_ v14,a128
R_ v15,a129
Zvvv 84,38,144
v_accvgpr_mov_b32 a249, a130
v_accvgpr_mov_b32 a250, a131
Zvvv 88,38,148
v_max3_f32 v56, v96, v97, v98
v_max_f32 v56, v56, v99
Zvvv 92,38,152
W_ 4
v_max3_f32 v60, v100, v101, v102
v_max_f32 v60, v60, v103
Mvvv 76,42,156
Dv 38,%0,38912
v_max3_f32 v64, v104, v105, v106
Mvvv 80,42,160
v_max_f32 v64, v64, v107
Mvvv 84,42,164
v_max3_f32 v68, v108, v109, v110
v_max_f32 v68, v68, v111
Mvvv 88,42,168
v_max3_f32 v72, v112, v113, v114
v_max_f32 v72, v72, v115
Mvvv 92,42,172
W_ 2
Mvvv 76,30,176
Dv 42,v1,38912
Mvvv 80,30,180
Mvvv 84,30,184
Mvvv 88,30,188
Mvvv 92,30,192
s_waitcnt vmcnt(30) lgkmcnt(0)
s_barrier
Mvvv 76,34,196
Dv 30,%0,55296
Mvvv 76,38,216
Mvvv 76,42,236
Mvvv 80,34,200
Mvvv 80,38,220
Mvvv 80,42,240
Mvvv 84,34,204
s_nop 3
v_max3_f32 v56, v56, v76, v77
v_max3_f32 v56, v56, v78, v79
Mvvv 84,38,224
Mvvv 84,42,244
Mvvv 88,34,208
v_max3_f32 v60, v60, v80, v81
v_max3_f32 v60, v60, v82, v83
Mvvv 88,38,228
Mvvv 88,42,248
Mvvv 92,34,212
s_nop 1
v_max3_f32 v64, v64, v84, v85
v_max3_f32 v64, v64, v86, v87
Mvvv 92,38,232
Mvvv 92,42,252
Dv 34,v1,55296
.p2align 3
v_mov_b32 v11, 0x3f803f80
v_permlane32_swap_b32 v56, v60
v_max3_f32 v68, v68, v88, v89
v_max3_f32 v68, v68, v90, v91
v_max_f32 v56, v56, v60
s_nop 1
v_max3_f32 v72, v72, v92, v93
v_max3_f32 v72, v72, v94, v95
v_mov_b32 v73, v72
v_permlane32_swap_b32 v64, v68
v_max_f32 v64, v64, v68
v_permlane32_swap_b32 v72, v73
v_max_f32 v72, v72, v73
v_mov_b32 v73, v72
v_permlane16_swap_b32 v56, v64
v_max_f32 v56, v56, v64
v_permlane16_swap_b32 v72, v73
B_ v56,0xc0e00000,v56
v_max_f32 v72, v72, v73
v_mov_b32 v64, v56
B_ v72,0xc0e00000,v72
v_mov_b32 v73, v72
v_permlane16_swap_b32 v56, v64
v_mov_b32 v60, v56
v_mov_b32 v68, v64
v_mov_b64 v[74:75], v[72:73]
v_permlane32_swap_b32 v56, v60
v_permlane32_swap_b32 v64, v68
v_mov_b32 v57, v56
v_mov_b32 v61, v60
v_mov_b32 v65, v64
v_mov_b32 v69, v68
v_pk_add_f32 v[96:97], v[96:97], v[56:57]
v_pk_add_f32 v[98:99], v[98:99], v[56:57]
v_pk_add_f32 v[76:77], v[76:77], v[56:57]
v_pk_add_f32 v[78:79], v[78:79], v[56:57]
v_mov_b64 v[58:59], v[56:57]
v_pk_add_f32 v[100:101], v[100:101], v[60:61]
v_pk_add_f32 v[102:103], v[102:103], v[60:61]
v_pk_add_f32 v[80:81], v[80:81], v[60:61]
v_pk_add_f32 v[82:83], v[82:83], v[60:61]
v_mov_b64 v[62:63], v[60:61]
v_pk_add_f32 v[104:105], v[104:105], v[64:65]
v_pk_add_f32 v[106:107], v[106:107], v[64:65]
v_pk_add_f32 v[84:85], v[84:85], v[64:65]
v_pk_add_f32 v[86:87], v[86:87], v[64:65]
v_mov_b64 v[66:67], v[64:65]
v_pk_add_f32 v[108:109], v[108:109], v[68:69]
v_pk_add_f32 v[110:111], v[110:111], v[68:69]
v_pk_add_f32 v[88:89], v[88:89], v[68:69]
v_pk_add_f32 v[90:91], v[90:91], v[68:69]
v_mov_b64 v[70:71], v[68:69]
v_pk_add_f32 v[112:113], v[112:113], v[72:73]
v_pk_add_f32 v[114:115], v[114:115], v[72:73]
v_pk_add_f32 v[92:93], v[92:93], v[72:73]
v_pk_add_f32 v[94:95], v[94:95], v[72:73]
E_ 96
E_ 97
E_ 98
E_ 99
E_ 76
E_ 77
E_ 78
E_ 79
C_ 99,98,99
C_ 98,96,97
C_ 96,76,77
C_ 97,78,79
E_ 100
E_ 101
E_ 102
E_ 103
E_ 80
E_ 81
E_ 82
E_ 83
C_ 103,102,103
C_ 102,100,101
C_ 100,80,81
C_ 101,82,83
.Lstep1_1:
W_ 1
Nvvv 116,30,136,56
s_nop 0
Dv 38,%0,56320
Dv 42,v1,56320
W_ 2
Mvvv 116,34,156
E_ 104
Nvvv 120,30,140,60
E_ 105
Mvvv 120,34,160
E_ 106
Nvvv 124,30,144,64
E_ 107
Mvvv 124,34,164
E_ 84
Nvvv 128,30,148,68
E_ 85
Mvvv 128,34,168
E_ 86
Nvvv 132,30,152,72
E_ 87
Mvvv 132,34,172
W_ 0
C_ 107,106,107
C_ 106,104,105
Mvvv 132,38,192
s_nop 0
Dv 30,%0,57344
Dv 34,v1,57344
Mvvv 132,42,212
C_ 104,84,85
C_ 105,86,87
Mvvv 128,38,188
E_ 108
Mvvv 128,42,208
E_ 109
Mvvv 124,38,184
E_ 110
Mvvv 124,42,204
E_ 111
Mvvv 120,38,180
E_ 88
Mvvv 120,42,200
E_ 89
Mvvv 116,38,176
E_ 90
Mvvv 116,42,196
W_ 0
E_ 91
Mvvv 116,30,216
s_nop 0
Dv 38,%0,49152
Dv 42,v1,49152
Mvvv 116,34,236
C_ 111,110,111
C_ 110,108,109
Mvvv 120,30,220
C_ 108,88,89
C_ 109,90,91
Mvvv 120,34,240
E_ 112
Mvvv 124,30,224
E_ 113
Mvvv 124,34,244
E_ 114
Mvvv 128,30,228
E_ 115
Mvvv 128,34,248
E_ 92
Mvvv 132,30,232
E_ 93
Mvvv 132,34,252
W_ 0
E_ 94
Nvvv 76,38,136,56
s_nop 0
Dv 30,%0,50176
Dv 34,v1,50176
Mvvv 76,42,156
E_ 95
Nvvv 80,38,140,60
C_ 115,114,115
C_ 114,112,113
Mvvv 80,42,160
C_ 112,92,93
C_ 113,94,95
Nvvv 84,38,144,64
E_ 116
Mvvv 84,42,164
E_ 117
Nvvv 88,38,148,68
E_ 118
Mvvv 88,42,168
E_ 119
Nvvv 92,38,152,72
C_ 119,118,119
C_ 118,116,117
Mvvv 92,42,172
W_ 0
E_ 120
Mvvv 92,30,192
s_nop 0
Dv 38,%0,51200
Dv 42,v1,51200
Mvvv 92,34,212
X_ a180,v11
X_ a181,v11
Mvvv 88,30,188
X_ a182,v11
X_ a183,v11
Mvvv 88,34,208
ds_write_b128 v12, a[132:135] offset:0
Mvvv 84,30,184
E_ 121
Mvvv 84,34,204
E_ 122
Mvvv 80,30,180
ds_write_b128 v12, a[136:139] offset:1024
Mvvv 80,34,200
)A"
R"A(E_ 123
Mvvv 76,30,176
C_ 123,122,123
C_ 122,120,121
Mvvv 76,34,196
ds_write_b128 v12, a[160:163] offset:2048
W_ 4
Mvvv 76,38,216
W_ 3
E_ 124
Mvvv 76,42,236
s_waitcnt vmcnt(12) lgkmcnt(0)
s_barrier
Mvvv 80,38,220
ds_write_b128 v12, a[164:167] offset:3072
Mvvv 80,42,240
E_ 125
Mvvv 84,38,224
E_ 126
Mvvv 84,42,244
s_nop 0
Tv 30,v2,24576
Tv 32,v2,30720
Mvvv 88,38,228
ds_write_b128 v12, a[168:171] offset:4096
Mvvv 88,42,248
s_nop 0
Tv 34,v3,24576
Tv 36,v3,30720
Mvvv 92,38,232
E_ 127
Mvvv 92,42,252
ds_write_b128 v12, a[172:175] offset:5120
Zaav 220,180,96
C_ 127,126,127
C_ 126,124,125
Zaav 224,180,100
E_ 128
Zaav 228,180,104
ds_write_b128 v12, a[176:179] offset:12288
Zaav 232,180,108
E_ 129
Zaav 236,180,112
W_ 2
E_ 130
Zavv 0,30,96
s_nop 0
Tv 38,v4,24576
Tv 40,v4,30720
Zavv 20,34,96
ds_write_b128 v12, a[200:203] offset:13312
Zavv 24,34,100
.Lpad_a1_1:
s_cbranch_execz .Lhop_a1_r
Tv 42,v5,24576
Tv 44,v5,30720
Zavv 4,30,100
E_ 131
Zavv 8,30,104
ds_write_b128 v12, a[204:207] offset:14336
Zavv 28,34,104
R_ v46,a220
R_ v47,a224
Zavv 32,34,108
R_ v48,a228
R_ v49,a232
Zavv 12,30,108
R_ v50,a236
C_ 131,130,131
Zavv 16,30,112
ds_write_b128 v12, a[208:211] offset:15360
Zavv 36,34,112
W_ 2
C_ 130,128,129
Zavv 56,38,112
s_nop 0
Tv 30,v2,25600
Tv 32,v2,31744
Zavv 76,42,112
ds_write_b128 v12, a[212:215] offset:16384
Zavv 72,42,108
s_nop 0
Tv 34,v3,25600
Tv 36,v3,31744
Zavv 52,38,108
E_ 132
Zavv 48,38,104
ds_write_b128 v12, a[216:219] offset:17408
Zavv 68,42,104
E_ 133
Zavv 64,42,100
E_ 134
Zavv 44,38,100
E_ 135
Zavv 40,38,96
C_ 135,134,135
C_ 134,132,133
Zavv 60,42,96
W_ 1
E_ 76
Zavv 80,30,96
s_nop 0
Tv 38,v4,25600
Tv 40,v4,31744
Zavv 100,34,96
E_ 77
Zavv 104,34,100
s_nop 0
Tv 42,v5,25600
Tv 44,v5,31744
Zavv 84,30,100
E_ 78
Zavv 88,30,104
E_ 79
Zavv 108,34,104
C_ 116,76,77
C_ 117,78,79
Zavv 112,34,108
E_ 80
Zavv 92,30,108
E_ 81
Zavv 96,30,112
E_ 82
Zavv 116,34,112
W_ 0
E_ 83
Zavv 136,38,112
s_nop 0
Tv 30,v2,26624
Tv 32,v2,32768
Zavv 156,42,112
C_ 120,80,81
C_ 121,82,83
Zavv 152,42,108
s_nop 0
Tv 34,v3,26624
Tv 36,v3,32768
Zavv 132,38,108
E_ 84
Zavv 128,38,104
E_ 85
Zavv 148,42,104
E_ 86
Zavv 144,42,100
E_ 87
Zavv 124,38,100
C_ 124,84,85
C_ 125,86,87
Zavv 120,38,96
E_ 88
Zavv 140,42,96
W_ 0
E_ 89
Zavv 160,30,96
s_nop 0
Tv 38,v4,26624
Tv 40,v4,32768
Zavv 180,34,96
E_ 90
Zavv 184,34,100
s_nop 0
Tv 42,v5,26624
Tv 44,v5,32768
Zavv 164,30,100
E_ 91
Zavv 168,30,104
C_ 128,88,89
C_ 129,90,91
Zavv 188,34,104
S_ s65,s61,36864
E_ 92
Zavv 192,34,108
S_ m0,s51,24576
E_ 93
Zavv 172,30,108
s_waitcnt vmcnt(6) lgkmcnt(0)
s_barrier
Zavv 176,30,112
Ln v7,32,s65,0
Zavv 196,34,112
E_ 94
Zavv 216,38,112
Dv 30,%0,6144
Zavv 236,42,112
Dv 34,v1,6144
Zavv 232,42,108
Ln v8,32,s65,3072
S_ m0,s51,25472
Zavv 212,38,108
E_ 95
Zavv 208,38,104
Ln v7,32,s65,128
Zavv 228,42,104
C_ 132,92,93
C_ 133,94,95
Zavv 224,42,100
Ln v8,32,s65,3200
S_ m0,s51,26368
Zavv 204,38,100
v_mov_b32 v92, 0x3f803f80
v_mov_b32 v93, 0x3f803f80
Zavv 200,38,96
Ln v7,32,s65,256
Zavv 220,42,96
v_mov_b32 v94, 0x3f803f80
v_mov_b32 v95, 0x3f803f80
Ln v8,32,s65,3328
.Lstep1_2:
s_nop 0
Zvvv 76,92,116
Dv 38,%0,7168
Zvvv 80,92,120
Dv 42,v1,7168
Zvvv 84,92,124
Zvvv 88,92,128
Zvvv 92,92,132
W_ 2
Nvvv 96,30,136,56
Mvvv 96,34,156
Nvvv 100,30,140,60
s_nop 1
v_max3_f32 v10, v76, v80, v84
s_nop 0
v_max3_f32 v10, v10, v88, v92
Mvvv 100,34,160
v_cmp_lt_f32 vcc, 0x42000000, v10
s_cbranch_vccnz .Ltramp1_1
Nvvv 104,30,144,64
A_ v46,v46,v76
A_ v47,v47,v80
Mvvv 104,34,164
A_ v48,v48,v84
A_ v49,v49,v88
Nvvv 108,30,148,68
A_ v50,v50,v92
Mvvv 108,34,168
Nvvv 112,30,152,72
Mvvv 112,34,172
W_ 0
Mvvv 112,38,192
Dv 30,%0,8192
Mvvv 112,42,212
Dv 34,v1,8192
Mvvv 108,38,188
Mvvv 108,42,208
Mvvv 104,38,184
Mvvv 104,42,204
Mvvv 100,38,180
Mvvv 100,42,200
Mvvv 96,38,176
Mvvv 96,42,196
W_ 0
Mvvv 96,30,216
Dv 38,%0,0
Mvvv 96,34,236
Dv 42,v1,0
Mvvv 100,30,220
Mvvv 100,34,240
Mvvv 104,30,224
Mvvv 104,34,244
Mvvv 108,30,228
Mvvv 108,34,248
s_nop 0
E_ 96
Mvvv 112,30,232
E_ 97
Mvvv 112,34,252
W_ 0
E_ 98
Nvvv 76,38,136,56
Dv 30,%0,1024
Mvvv 76,42,156
Dv 34,v1,1024
Nvvv 80,38,140,60
E_ 99
Mvvv 80,42,160
C_ 99,98,99
C_ 98,96,97
Nvvv 84,38,144,64
E_ 100
Mvvv 84,42,164
E_ 101
Nvvv 88,38,148,68
E_ 102
Mvvv 88,42,168
E_ 103
Nvvv 92,38,152,72
C_ 103,102,103
C_ 102,100,101
Mvvv 92,42,172
W_ 0
E_ 104
Mvvv 92,30,192
Dv 38,%0,2048
Mvvv 92,34,212
Dv 42,v1,2048
Mvvv 88,30,188
E_ 105
Mvvv 88,34,208
E_ 106
Mvvv 84,30,184
S_ s65,s61,24576
E_ 107
Mvvv 84,34,204
S_ m0,s51,0
C_ 107,106,107
C_ 106,104,105
Mvvv 80,30,180
s_waitcnt vmcnt(6) lgkmcnt(0)
s_barrier
Mvvv 80,34,200
Ln v9,36,s65,0
Mvvv 76,30,176
E_ 108
Mvvv 76,34,196
Ln v9,36,s65,3072
S_ m0,s51,896
Mvvv 76,38,216
Tv 30,v2,12288
Tv 32,v2,18432
Mvvv 76,42,236
Ln v9,36,s65,128
Mvvv 80,38,220
s_nop 0
Tv 34,v3,12288
Tv 36,v3,18432
Mvvv 80,42,240
Ln v9,36,s65,3200
S_ m0,s51,1792
Mvvv 84,38,224
E_ 109
Mvvv 84,42,244
Ln v9,36,s65,256
Mvvv 88,38,228
E_ 110
Mvvv 88,42,248
Ln v9,36,s65,3328
Mvvv 92,38,232
E_ 111
Mvvv 92,42,252
W_ 0
C_ 111,110,111
C_ 110,108,109
Mavv 0,30,116
s_nop 0
Tv 38,v4,12288
Tv 40,v4,18432
Mavv 20,34,116
E_ 112
Mavv 24,34,120
s_nop 0
Tv 42,v5,12288
Tv 44,v5,18432
Mavv 4,30,120
E_ 113
Mavv 8,30,124
E_ 114
Mavv 28,34,124
E_ 115
Mavv 32,34,128
C_ 115,114,115
C_ 114,112,113
Mavv 12,30,128
E_ 76
Mavv 16,30,132
E_ 77
Mavv 36,34,132
W_ 0
E_ 78
Mavv 56,38,132
s_nop 0
Tv 30,v2,13312
Tv 32,v2,19456
Mavv 76,42,132
E_ 79
Mavv 72,42,128
s_nop 0
Tv 34,v3,13312
Tv 36,v3,19456
Mavv 52,38,128
C_ 96,76,77
C_ 97,78,79
Mavv 48,38,124
E_ 80
Mavv 68,42,124
E_ 81
Mavv 64,42,120
E_ 82
Mavv 44,38,120
E_ 83
Mavv 40,38,116
C_ 100,80,81
C_ 101,82,83
Mavv 60,42,116
W_ 0
E_ 84
Mavv 80,30,116
s_nop 0
Tv 38,v4,13312
Tv 40,v4,19456
Mavv 100,34,116
E_ 85
Mavv 104,34,120
s_nop 0
Tv 42,v5,13312
Tv 44,v5,19456
Mavv 84,30,120
E_ 86
Mavv 88,30,124
E_ 87
Mavv 108,34,124
C_ 104,84,85
C_ 105,86,87
Mavv 112,34,128
E_ 88
Mavv 92,30,128
E_ 89
Mavv 96,30,132
E_ 90
Mavv 116,34,132
W_ 0
E_ 91
Mavv 136,38,132
s_nop 0
Tv 30,v2,14336
Tv 32,v2,20480
Mavv 156,42,132
C_ 108,88,89
C_ 109,90,91
Mavv 152,42,128
s_nop 0
Tv 34,v3,14336
Tv 36,v3,20480
Mavv 132,38,128
E_ 92
Mavv 128,38,124
E_ 93
Mavv 148,42,124
E_ 94
Mavv 144,42,120
E_ 95
Mavv 124,38,120
C_ 112,92,93
C_ 113,94,95
Mavv 120,38,116
v_mov_b32 v92, 0x3f803f80
v_mov_b32 v93, 0x3f803f80
Mavv 140,42,116
W_ 0
v_mov_b32 v94, 0x3f803f80
v_mov_b32 v95, 0x3f803f80
Mavv 160,30,116
s_nop 0
Tv 38,v4,14336
Tv 40,v4,20480
Mavv 180,34,116
Mavv 184,34,120
s_nop 0
Tv 42,v5,14336
Tv 44,v5,20480
Mavv 164,30,120
Mavv 168,30,124
Mavv 188,34,124
S_ s65,s61,49152
Mavv 192,34,128
S_ m0,s51,12288
Mavv 172,30,128
s_waitcnt vmcnt(6) lgkmcnt(0)
s_barrier
Mavv 176,30,132
Ln v7,32,s65,0
Mavv 196,34,132
Mavv 216,38,132
Dv 30,%0,30720
Mavv 236,42,132
Dv 34,v1,30720
Mavv 232,42,128
Ln v8,32,s65,3072
S_ m0,s51,13184
Mavv 212,38,128
Mavv 208,38,124
Ln v7,32,s65,128
Mavv 228,42,124
Mavv 224,42,120
Ln v8,32,s65,3200
S_ m0,s51,14080
Mavv 204,38,120
Mavv 200,38,116
Ln v7,32,s65,256
Mavv 220,42,116
s_cmp_eq_u32 s4, 0
s_cbranch_scc1 .Lhs_a1
.Lhs_a1_back:
.Lstep1_3:
Zvvv 76,92,96
Dv 38,%0,31744
Zvvv 80,92,100
Dv 42,v1,31744
Zvvv 84,92,104
Zvvv 88,92,108
Zvvv 92,92,112
W_ 2
Nvvv 116,30,136,56
Mvvv 116,34,156
Nvvv 120,30,140,60
s_nop 1
v_max3_f32 v10, v76, v80, v84
s_nop 0
v_max3_f32 v10, v10, v88, v92
Mvvv 120,34,160
v_cmp_lt_f32 vcc, 0x42000000, v10
s_cbranch_vccnz .Ltramp1_2
Nvvv 124,30,144,64
A_ v46,v46,v76
A_ v47,v47,v80
Mvvv 124,34,164
A_ v48,v48,v84
A_ v49,v49,v88
Nvvv 128,30,148,68
A_ v50,v50,v92
Mvvv 128,34,168
Ln v8,32,s65,3328
Nvvv 132,30,152,72
Mvvv 132,34,172
W_ 0
Mvvv 132,38,192
Dv 30,%0,32768
Mvvv 132,42,212
Dv 34,v1,32768
Mvvv 128,38,188
Mvvv 128,42,208
Mvvv 124,38,184
Mvvv 124,42,204
Mvvv 120,38,180
Mvvv 120,42,200
Mvvv 116,38,176
Mvvv 116,42,196
W_ 0
Mvvv 116,30,216
Dv 38,%0,24576
Mvvv 116,34,236
Dv 42,v1,24576
Mvvv 120,30,220
Mvvv 120,34,240
Mvvv 124,30,224
Mvvv 124,34,244
Mvvv 128,30,228
Mvvv 128,34,248
s_nop 0
E_ 116
Mvvv 132,30,232
E_ 117
Mvvv 132,34,252
W_ 0
E_ 118
Nvvv 76,38,136,56
Dv 30,%0,25600
Mvvv 76,42,156
Dv 34,v1,25600
Nvvv 80,38,140,60
E_ 119
Mvvv 80,42,160
C_ 119,118,119
C_ 118,116,117
Nvvv 84,38,144,64
E_ 120
Mvvv 84,42,164
E_ 121
Nvvv 88,38,148,68
E_ 122
Mvvv 88,42,168
E_ 123
Nvvv 92,38,152,72
C_ 123,122,123
C_ 122,120,121
Mvvv 92,42,172
W_ 0
E_ 124
Mvvv 92,30,192
Dv 38,%0,26624
Mvvv 92,34,212
Dv 42,v1,26624
Mvvv 88,30,188
E_ 125
Mvvv 88,34,208
E_ 126
Mvvv 84,30,184
S_ s65,s61,36864
E_ 127
Mvvv 84,34,204
S_ m0,s51,24576
C_ 127,126,127
C_ 126,124,125
Mvvv 80,30,180
s_waitcnt vmcnt(6) lgkmcnt(0)
s_barrier
Mvvv 80,34,200
Ln v9,36,s65,0
Mvvv 76,30,176
E_ 128
Mvvv 76,34,196
Ln v9,36,s65,3072
S_ m0,s51,25472
Mvvv 76,38,216
Tv 30,v2,0
Tv 32,v2,6144
Mvvv 76,42,236
Ln v9,36,s65,128
Mvvv 80,38,220
s_nop 0
Tv 34,v3,0
Tv 36,v3,6144
Mvvv 80,42,240
Ln v9,36,s65,3200
S_ m0,s51,26368
Mvvv 84,38,224
E_ 129
Mvvv 84,42,244
Ln v9,36,s65,256
Mvvv 88,38,228
E_ 130
Mvvv 88,42,248
Ln v9,36,s65,3328
Mvvv 92,38,232
E_ 131
Mvvv 92,42,252
W_ 0
C_ 131,130,131
C_ 130,128,129
Mavv 0,30,96
s_nop 0
Tv 38,v4,0
Tv 40,v4,6144
Mavv 20,34,96
E_ 132
Mavv 24,34,100
s_nop 0
Tv 42,v5,0
Tv 44,v5,6144
Mavv 4,30,100
E_ 133
Mavv 8,30,104
E_ 134
Mavv 28,34,104
E_ 135
Mavv 32,34,108
C_ 135,134,135
C_ 134,132,133
Mavv 12,30,108
.Lpad_a1_2:
s_cbranch_execz .Lhop_a1_r
E_ 76
Mavv 16,30,112
E_ 77
Mavv 36,34,112
W_ 0
E_ 78
Mavv 56,38,112
s_nop 0
Tv 30,v2,1024
Tv 32,v2,7168
Mavv 76,42,112
E_ 79
Mavv 72,42,108
s_nop 0
Tv 34,v3,1024
Tv 36,v3,7168
Mavv 52,38,108
C_ 116,76,77
C_ 117,78,79
Mavv 48,38,104
E_ 80
Mavv 68,42,104
E_ 81
Mavv 64,42,100
E_ 82
Mavv 44,38,100
E_ 83
Mavv 40,38,96
C_ 120,80,81
C_ 121,82,83
Mavv 60,42,96
W_ 0
E_ 84
Mavv 80,30,96
s_nop 0
Tv 38,v4,1024
Tv 40,v4,7168
Mavv 100,34,96
E_ 85
Mavv 104,34,100
s_nop 0
Tv 42,v5,1024
Tv 44,v5,7168
Mavv 84,30,100
E_ 86
Mavv 88,30,104
E_ 87
Mavv 108,34,104
C_ 124,84,85
C_ 125,86,87
Mavv 112,34,108
E_ 88
Mavv 92,30,108
E_ 89
Mavv 96,30,112
E_ 90
Mavv 116,34,112
W_ 0
E_ 91
Mavv 136,38,112
s_nop 0
Tv 30,v2,2048
Tv 32,v2,8192
Mavv 156,42,112
C_ 128,88,89
C_ 129,90,91
Mavv 152,42,108
s_nop 0
Tv 34,v3,2048
Tv 36,v3,8192
Mavv 132,38,108
E_ 92
Mavv 128,38,104
E_ 93
Mavv 148,42,104
E_ 94
Mavv 144,42,100
E_ 95
Mavv 124,38,100
C_ 132,92,93
C_ 133,94,95
Mavv 120,38,96
v_mov_b32 v92, 0x3f803f80
v_mov_b32 v93, 0x3f803f80
Mavv 140,42,96
W_ 0
v_mov_b32 v94, 0x3f803f80
v_mov_b32 v95, 0x3f803f80
Mavv 160,30,96
s_nop 0
Tv 38,v4,2048
Tv 40,v4,8192
Mavv 180,34,96
Mavv 184,34,100
s_nop 0
Tv 42,v5,2048
Tv 44,v5,8192
Mavv 164,30,100
Mavv 168,30,104
Mavv 188,34,104
S_ s65,s61,61440
Mavv 192,34,108
S_ m0,s51,0
Mavv 172,30,108
s_waitcnt vmcnt(6) lgkmcnt(0)
s_barrier
Mavv 176,30,112
Ln v7,32,s65,0
Mavv 196,34,112
Mavv 216,38,112
Dv 30,%0,18432
Mavv 236,42,112
Dv 34,v1,18432
Mavv 232,42,108
Ln v8,32,s65,3072
S_ m0,s51,896
Mavv 212,38,108
Mavv 208,38,104
Ln v7,32,s65,128
Mavv 228,42,104
Mavv 224,42,100
Ln v8,32,s65,3200
S_ m0,s51,1792
Mavv 204,38,100
Mavv 200,38,96
Ln v7,32,s65,256
Mavv 220,42,96
.Lstep1_4:
Zvvv 76,92,116
Dv 38,%0,19456
Zvvv 80,92,120
Dv 42,v1,19456
Zvvv 84,92,124
Zvvv 88,92,128
Zvvv 92,92,132
W_ 2
Nvvv 96,30,136,56
Mvvv 96,34,156
Nvvv 100,30,140,60
s_nop 1
v_max3_f32 v10, v76, v80, v84
s_nop 0
v_max3_f32 v10, v10, v88, v92
Mvvv 100,34,160
v_cmp_lt_f32 vcc, 0x42000000, v10
s_cbranch_vccnz .Ltramp1_3
Nvvv 104,30,144,64
A_ v46,v46,v76
A_ v47,v47,v80
Mvvv 104,34,164
A_ v48,v48,v84
A_ v49,v49,v88
Nvvv 108,30,148,68
A_ v50,v50,v92
Mvvv 108,34,168
Ln v8,32,s65,3328
Nvvv 112,30,152,72
Mvvv 112,34,172
W_ 0
Mvvv 112,38,192
Dv 30,%0,20480
Mvvv 112,42,212
Dv 34,v1,20480
Mvvv 108,38,188
Mvvv 108,42,208
Mvvv 104,38,184
Mvvv 104,42,204
Mvvv 100,38,180
Mvvv 100,42,200
Mvvv 96,38,176
Mvvv 96,42,196
W_ 0
Mvvv 96,30,216
Dv 38,%0,12288
Mvvv 96,34,236
Dv 42,v1,12288
Mvvv 100,30,220
Mvvv 100,34,240
Mvvv 104,30,224
Mvvv 104,34,244
Mvvv 108,30,228
Mvvv 108,34,248
s_nop 0
E_ 96
Mvvv 112,30,232
E_ 97
Mvvv 112,34,252
W_ 0
E_ 98
Nvvv 76,38,136,56
Dv 30,%0,13312
Mvvv 76,42,156
Dv 34,v1,13312
Nvvv 80,38,140,60
E_ 99
Mvvv 80,42,160
C_ 99,98,99
C_ 98,96,97
Nvvv 84,38,144,64
E_ 100
Mvvv 84,42,164
E_ 101
Nvvv 88,38,148,68
E_ 102
Mvvv 88,42,168
E_ 103
Nvvv 92,38,152,72
C_ 103,102,103
C_ 102,100,101
Mvvv 92,42,172
W_ 0
E_ 104
Mvvv 92,30,192
Dv 38,%0,14336
Mvvv 92,34,212
Dv 42,v1,14336
Mvvv 88,30,188
E_ 105
Mvvv 88,34,208
E_ 106
Mvvv 84,30,184
S_ s65,s61,49152
E_ 107
Mvvv 84,34,204
S_ m0,s51,12288
C_ 107,106,107
C_ 106,104,105
Mvvv 80,30,180
s_waitcnt vmcnt(6) lgkmcnt(0)
s_barrier
Mvvv 80,34,200
L_ v9,36,s65,0
Mvvv 76,30,176
E_ 108
Mvvv 76,34,196
L_ v9,36,s65,3072
S_ m0,s51,13184
Mvvv 76,38,216
Tv 30,v2,24576
Tv 32,v2,30720
Mvvv 76,42,236
L_ v9,36,s65,128
Mvvv 80,38,220
s_nop 0
Tv 34,v3,24576
Tv 36,v3,30720
Mvvv 80,42,240
L_ v9,36,s65,3200
S_ m0,s51,14080
Mvvv 84,38,224
E_ 109
Mvvv 84,42,244
L_ v9,36,s65,256
Mvvv 88,38,228
E_ 110
Mvvv 88,42,248
L_ v9,36,s65,3328
Mvvv 92,38,232
E_ 111
Mvvv 92,42,252
W_ 0
C_ 111,110,111
C_ 110,108,109
Mavv 0,30,116
s_nop 0
Tv 38,v4,24576
Tv 40,v4,30720
Mavv 20,34,116
E_ 112
Mavv 24,34,120
s_nop 0
Tv 42,v5,24576
Tv 44,v5,30720
Mavv 4,30,120
E_ 113
Mavv 8,30,124
E_ 114
Mavv 28,34,124
E_ 115
Mavv 32,34,128
C_ 115,114,115
C_ 114,112,113
Mavv 12,30,128
E_ 76
Mavv 16,30,132
E_ 77
Mavv 36,34,132
W_ 0
E_ 78
Mavv 56,38,132
s_nop 0
Tv 30,v2,25600
Tv 32,v2,31744
Mavv 76,42,132
E_ 79
Mavv 72,42,128
s_nop 0
Tv 34,v3,25600
Tv 36,v3,31744
Mavv 52,38,128
C_ 96,76,77
C_ 97,78,79
Mavv 48,38,124
E_ 80
Mavv 68,42,124
E_ 81
Mavv 64,42,120
E_ 82
Mavv 44,38,120
E_ 83
Mavv 40,38,116
C_ 100,80,81
C_ 101,82,83
Mavv 60,42,116
W_ 0
E_ 84
Mavv 80,30,116
s_nop 0
Tv 38,v4,25600
Tv 40,v4,31744
Mavv 100,34,116
E_ 85
Mavv 104,34,120
s_nop 0
Tv 42,v5,25600
Tv 44,v5,31744
Mavv 84,30,120
E_ 86
Mavv 88,30,124
E_ 87
Mavv 108,34,124
C_ 104,84,85
C_ 105,86,87
Mavv 112,34,128
E_ 88
Mavv 92,30,128
E_ 89
Mavv 96,30,132
E_ 90
Mavv 116,34,132
W_ 0
E_ 91
Mavv 136,38,132
s_nop 0
Tv 30,v2,26624
Tv 32,v2,32768
Mavv 156,42,132
C_ 108,88,89
C_ 109,90,91
Mavv 152,42,128
s_nop 0
Tv 34,v3,26624
Tv 36,v3,32768
Mavv 132,38,128
E_ 92
Mavv 128,38,124
E_ 93
Mavv 148,42,124
E_ 94
Mavv 144,42,120
E_ 95
Mavv 124,38,120
C_ 112,92,93
C_ 113,94,95
Mavv 120,38,116
v_mov_b32 v92, 0x3f803f80
v_mov_b32 v93, 0x3f803f80
Mavv 140,42,116
W_ 0
v_mov_b32 v94, 0x3f803f80
v_mov_b32 v95, 0x3f803f80
Mavv 160,30,116
s_nop 0
Tv 38,v4,26624
Tv 40,v4,32768
Mavv 180,34,116
Mavv 184,34,120
s_nop 0
Tv 42,v5,26624
Tv 44,v5,32768
Mavv 164,30,120
Mavv 168,30,124
Mavv 188,34,124
S_ s65,s61,73728
Mavv 192,34,128
S_ m0,s51,24576
Mavv 172,30,128
s_waitcnt vmcnt(6) lgkmcnt(0)
s_barrier
Mavv 176,30,132
Ln v7,32,s65,0
Mavv 196,34,132
Mavv 216,38,132
Dv 30,%0,6144
Mavv 236,42,132
Dv 34,v1,6144
Mavv 232,42,128
Ln v8,32,s65,3072
S_ m0,s51,25472
Mavv 212,38,128
Mavv 208,38,124
Ln v7,32,s65,128
Mavv 228,42,124
Mavv 224,42,120
Ln v8,32,s65,3200
S_ m0,s51,26368
Mavv 204,38,120
Mavv 200,38,116
Ln v7,32,s65,256
Mavv 220,42,116
s_cmp_eq_u32 s4, 0
s_cbranch_scc1 .Lhs_b1
.Lhs_b1_back:
.Lstep1_5:
Zvvv 76,92,96
Dv 38,%0,7168
Zvvv 80,92,100
Dv 42,v1,7168
Zvvv 84,92,104
Zvvv 88,92,108
Zvvv 92,92,112
W_ 2
Nvvv 116,30,136,56
Mvvv 116,34,156
Nvvv 120,30,140,60
s_nop 1
v_max3_f32 v10, v76, v80, v84
s_nop 0
v_max3_f32 v10, v10, v88, v92
Mvvv 120,34,160
v_cmp_lt_f32 vcc, 0x42000000, v10
s_cbranch_vccnz .Ltramp1_4
Nvvv 124,30,144,64
A_ v46,v46,v76
A_ v47,v47,v80
Mvvv 124,34,164
A_ v48,v48,v84
A_ v49,v49,v88
Nvvv 128,30,148,68
A_ v50,v50,v92
Mvvv 128,34,168
Ln v8,32,s65,3328
Nvvv 132,30,152,72
Mvvv 132,34,172
W_ 0
Mvvv 132,38,192
Dv 30,%0,8192
Mvvv 132,42,212
Dv 34,v1,8192
Mvvv 128,38,188
Mvvv 128,42,208
Mvvv 124,38,184
Mvvv 124,42,204
Mvvv 120,38,180
Mvvv 120,42,200
Mvvv 116,38,176
Mvvv 116,42,196
W_ 0
Mvvv 116,30,216
Dv 38,%0,0
Mvvv 116,34,236
Dv 42,v1,0
Mvvv 120,30,220
Mvvv 120,34,240
Mvvv 124,30,224
Mvvv 124,34,244
Mvvv 128,30,228
Mvvv 128,34,248
s_nop 0
E_ 116
Mvvv 132,30,232
E_ 117
Mvvv 132,34,252
W_ 0
E_ 118
Nvvv 76,38,136,56
Dv 30,%0,1024
Mvvv 76,42,156
Dv 34,v1,1024
Nvvv 80,38,140,60
E_ 119
Mvvv 80,42,160
C_ 119,118,119
C_ 118,116,117
Nvvv 84,38,144,64
E_ 120
Mvvv 84,42,164
E_ 121
Nvvv 88,38,148,68
E_ 122
Mvvv 88,42,168
E_ 123
Nvvv 92,38,152,72
C_ 123,122,123
C_ 122,120,121
Mvvv 92,42,172
W_ 0
E_ 124
Mvvv 92,30,192
Dv 38,%0,2048
Mvvv 92,34,212
Dv 42,v1,2048
Mvvv 88,30,188
E_ 125
Mvvv 88,34,208
E_ 126
Mvvv 84,30,184
S_ s65,s61,61440
E_ 127
Mvvv 84,34,204
S_ m0,s51,0
C_ 127,126,127
C_ 126,124,125
Mvvv 80,30,180
s_waitcnt vmcnt(6) lgkmcnt(0)
s_barrier
Mvvv 80,34,200
L_ v9,36,s65,0
Mvvv 76,30,176
E_ 128
Mvvv 76,34,196
L_ v9,36,s65,3072
S_ m0,s51,896
Mvvv 76,38,216
Tv 30,v2,12288
Tv 32,v2,18432
Mvvv 76,42,236
L_ v9,36,s65,128
Mvvv 80,38,220
s_nop 0
Tv 34,v3,12288
Tv 36,v3,18432
Mvvv 80,42,240
L_ v9,36,s65,3200
S_ m0,s51,1792
Mvvv 84,38,224
E_ 129
Mvvv 84,42,244
L_ v9,36,s65,256
Mvvv 88,38,228
E_ 130
Mvvv 88,42,248
L_ v9,36,s65,3328
Mvvv 92,38,232
E_ 131
Mvvv 92,42,252
W_ 0
C_ 131,130,131
C_ 130,128,129
Mavv 0,30,96
s_nop 0
Tv 38,v4,12288
Tv 40,v4,18432
Mavv 20,34,96
E_ 132
Mavv 24,34,100
s_nop 0
Tv 42,v5,12288
Tv 44,v5,18432
Mavv 4,30,100
E_ 133
Mavv 8,30,104
E_ 134
Mavv 28,34,104
E_ 135
Mavv 32,34,108
C_ 135,134,135
C_ 134,132,133
Mavv 12,30,108
E_ 76
Mavv 16,30,112
E_ 77
Mavv 36,34,112
W_ 0
E_ 78
Mavv 56,38,112
s_nop 0
Tv 30,v2,13312
Tv 32,v2,19456
Mavv 76,42,112
E_ 79
Mavv 72,42,108
s_nop 0
Tv 34,v3,13312
Tv 36,v3,19456
Mavv 52,38,108
C_ 116,76,77
C_ 117,78,79
Mavv 48,38,104
E_ 80
Mavv 68,42,104
E_ 81
Mavv 64,42,100
.Lpad_a1_3:
s_cbranch_execz .Lhop_a1_r
E_ 82
Mavv 44,38,100
E_ 83
Mavv 40,38,96
C_ 120,80,81
C_ 121,82,83
Mavv 60,42,96
W_ 0
E_ 84
Mavv 80,30,96
s_nop 0
Tv 38,v4,13312
Tv 40,v4,19456
Mavv 100,34,96
E_ 85
Mavv 104,34,100
s_nop 0
Tv 42,v5,13312
Tv 44,v5,19456
Mavv 84,30,100
E_ 86
Mavv 88,30,104
E_ 87
Mavv 108,34,104
C_ 124,84,85
C_ 125,86,87
Mavv 112,34,108
E_ 88
Mavv 92,30,108
E_ 89
Mavv 96,30,112
E_ 90
Mavv 116,34,112
W_ 0
E_ 91
Mavv 136,38,112
s_nop 0
Tv 30,v2,14336
Tv 32,v2,20480
Mavv 156,42,112
C_ 128,88,89
C_ 129,90,91
Mavv 152,42,108
s_nop 0
Tv 34,v3,14336
Tv 36,v3,20480
Mavv 132,38,108
E_ 92
Mavv 128,38,104
E_ 93
Mavv 148,42,104
E_ 94
Mavv 144,42,100
E_ 95
Mavv 124,38,100
C_ 132,92,93
C_ 133,94,95
Mavv 120,38,96
v_mov_b32 v92, 0x3f803f80
v_mov_b32 v93, 0x3f803f80
Mavv 140,42,96
W_ 0
v_mov_b32 v94, 0x3f803f80
v_mov_b32 v95, 0x3f803f80
Mavv 160,30,96
s_nop 0
Tv 38,v4,14336
Tv 40,v4,20480
Mavv 180,34,96
Mavv 184,34,100
s_nop 0
Tv 42,v5,14336
Tv 44,v5,20480
Mavv 164,30,100
Mavv 168,30,104
Mavv 188,34,104
S_ s65,s61,86016
Mavv 192,34,108
S_ m0,s51,12288
Mavv 172,30,108
s_waitcnt vmcnt(6) lgkmcnt(0)
s_barrier
Mavv 176,30,112
Ln v7,32,s65,0
Mavv 196,34,112
Mavv 216,38,112
Dv 30,%0,30720
Mavv 236,42,112
Dv 34,v1,30720
Mavv 232,42,108
Ln v8,32,s65,3072
S_ m0,s51,13184
Mavv 212,38,108
Mavv 208,38,104
Ln v7,32,s65,128
Mavv 228,42,104
Mavv 224,42,100
Ln v8,32,s65,3200
S_ m0,s51,14080
Mavv 204,38,100
Mavv 200,38,96
Ln v7,32,s65,256
Mavv 220,42,96
.Lstep1_6:
Zvvv 76,92,116
Dv 38,%0,31744
Zvvv 80,92,120
Dv 42,v1,31744
Zvvv 84,92,124
Zvvv 88,92,128
Zvvv 92,92,132
W_ 2
Nvvv 96,30,136,56
Mvvv 96,34,156
Nvvv 100,30,140,60
s_nop 1
v_max3_f32 v10, v76, v80, v84
s_nop 0
v_max3_f32 v10, v10, v88, v92
Mvvv 100,34,160
v_cmp_lt_f32 vcc, 0x42000000, v10
s_cbranch_vccnz .Ltramp1_5
Nvvv 104,30,144,64
A_ v46,v46,v76
A_ v47,v47,v80
Mvvv 104,34,164
A_ v48,v48,v84
A_ v49,v49,v88
Nvvv 108,30,148,68
A_ v50,v50,v92
Mvvv 108,34,168
Ln v8,32,s65,3328
Nvvv 112,30,152,72
R_ v11,a248
Mvvv 112,34,172
W_ 0
v_min_u32 v11, 240, v11
Mvvv 112,38,192
Dv 30,%0,32768
v_lshlrev_b32 v11, 3, v11
Mvvv 112,42,212
Dv 34,v1,32768
Mvvv 108,38,188
buffer_load_dword a254, v11, s[44:47], s66 offen
Mvvv 108,42,208
Mvvv 104,38,184
Mvvv 104,42,204
Mvvv 100,38,180
Mvvv 100,42,200
Mvvv 96,38,176
Mvvv 96,42,196
W_ 0
Mvvv 96,30,216
Dv 38,%0,24576
Mvvv 96,34,236
Dv 42,v1,24576
Mvvv 100,30,220
Mvvv 100,34,240
Mvvv 104,30,224
Mvvv 104,34,244
Mvvv 108,30,228
Mvvv 108,34,248
s_nop 0
E_ 96
Mvvv 112,30,232
E_ 97
Mvvv 112,34,252
W_ 0
E_ 98
Nvvv 76,38,136,56
Dv 30,%0,25600
Mvvv 76,42,156
Dv 34,v1,25600
Nvvv 80,38,140,60
E_ 99
Mvvv 80,42,160
C_ 99,98,99
C_ 98,96,97
Nvvv 84,38,144,64
E_ 100
Mvvv 84,42,164
E_ 101
Nvvv 88,38,148,68
E_ 102
Mvvv 88,42,168
E_ 103
Nvvv 92,38,152,72
C_ 103,102,103
C_ 102,100,101
Mvvv 92,42,172
W_ 0
E_ 104
Mvvv 92,30,192
Dv 38,%0,26624
Mvvv 92,34,212
Dv 42,v1,26624
Mvvv 88,30,188
E_ 105
Mvvv 88,34,208
E_ 106
Mvvv 84,30,184
S_ s65,s61,73728
E_ 107
Mvvv 84,34,204
S_ m0,s51,24576
C_ 107,106,107
C_ 106,104,105
Mvvv 80,30,180
s_waitcnt vmcnt(7) lgkmcnt(0)
s_barrier
Mvvv 80,34,200
L_ v9,36,s65,0
Mvvv 76,30,176
E_ 108
Mvvv 76,34,196
L_ v9,36,s65,3072
S_ m0,s51,25472
Mvvv 76,38,216
Tv 30,v2,0
Tv 32,v2,6144
Mvvv 76,42,236
L_ v9,36,s65,128
Mvvv 80,38,220
s_nop 0
Tv 34,v3,0
Tv 36,v3,6144
Mvvv 80,42,240
L_ v9,36,s65,3200
S_ m0,s51,26368
Mvvv 84,38,224
E_ 109
Mvvv 84,42,244
L_ v9,36,s65,256
Mvvv 88,38,228
E_ 110
Mvvv 88,42,248
L_ v9,36,s65,3328
Mvvv 92,38,232
E_ 111
Mvvv 92,42,252
W_ 0
C_ 111,110,111
C_ 110,108,109
Mavv 0,30,116
s_nop 0
Tv 38,v4,0
Tv 40,v4,6144
Mavv 20,34,116
E_ 112
Mavv 24,34,120
s_nop 0
Tv 42,v5,0
Tv 44,v5,6144
Mavv 4,30,120
E_ 113
Mavv 8,30,124
E_ 114
Mavv 28,34,124
E_ 115
Mavv 32,34,128
C_ 115,114,115
C_ 114,112,113
Mavv 12,30,128
E_ 76
Mavv 16,30,132
E_ 77
Mavv 36,34,132
W_ 0
E_ 78
Mavv 56,38,132
s_nop 0
Tv 30,v2,1024
Tv 32,v2,7168
Mavv 76,42,132
E_ 79
Mavv 72,42,128
s_nop 0
Tv 34,v3,1024
Tv 36,v3,7168
Mavv 52,38,128
C_ 96,76,77
C_ 97,78,79
Mavv 48,38,124
E_ 80
Mavv 68,42,124
E_ 81
Mavv 64,42,120
E_ 82
Mavv 44,38,120
E_ 83
Mavv 40,38,116
C_ 100,80,81
C_ 101,82,83
Mavv 60,42,116
W_ 0
E_ 84
Mavv 80,30,116
s_nop 0
Tv 38,v4,1024
Tv 40,v4,7168
Mavv 100,34,116
E_ 85
Mavv 104,34,120
s_nop 0
Tv 42,v5,1024
Tv 44,v5,7168
Mavv 84,30,120
E_ 86
Mavv 88,30,124
E_ 87
Mavv 108,34,124
C_ 104,84,85
C_ 105,86,87
Mavv 112,34,128
E_ 88
Mavv 92,30,128
E_ 89
Mavv 96,30,132
E_ 90
Mavv 116,34,132
W_ 0
E_ 91
Mavv 136,38,132
s_nop 0
Tv 30,v2,2048
Tv 32,v2,8192
Mavv 156,42,132
C_ 108,88,89
C_ 109,90,91
Mavv 152,42,128
s_nop 0
Tv 34,v3,2048
Tv 36,v3,8192
Mavv 132,38,128
E_ 92
Mavv 128,38,124
E_ 93
Mavv 148,42,124
E_ 94
Mavv 144,42,120
E_ 95
Mavv 124,38,120
C_ 112,92,93
C_ 113,94,95
Mavv 120,38,116
v_mov_b32 v92, 0x3f803f80
v_mov_b32 v93, 0x3f803f80
Mavv 140,42,116
W_ 0
v_mov_b32 v94, 0x3f803f80
v_mov_b32 v95, 0x3f803f80
Mavv 160,30,116
s_nop 0
Tv 38,v4,2048
Tv 40,v4,8192
Mavv 180,34,116
Mavv 184,34,120
s_nop 0
Tv 42,v5,2048
Tv 44,v5,8192
Mavv 164,30,120
Mavv 168,30,124
Mavv 188,34,124
S_ s65,s61,98304
Mavv 192,34,128
S_ m0,s51,0
Mavv 172,30,128
s_waitcnt vmcnt(7) lgkmcnt(0)
s_barrier
Mavv 176,30,132
Ln v7,32,s65,0
Mavv 196,34,132
Mavv 216,38,132
Dv 30,%0,18432
Mavv 236,42,132
Dv 34,v1,18432
Mavv 232,42,128
Ln v8,32,s65,3072
S_ m0,s51,896
Mavv 212,38,128
Mavv 208,38,124
Ln v7,32,s65,128
Mavv 228,42,124
Mavv 224,42,120
Ln v8,32,s65,3200
S_ m0,s51,1792
Mavv 204,38,120
Mavv 200,38,116
Ln v7,32,s65,256
Mavv 220,42,116
s_cmp_eq_u32 s4, 0
s_cbranch_scc1 .Lhs_c1
.Lhs_c1_back:
.Lstep1_7:
Zvvv 76,92,96
Dv 38,%0,19456
Zvvv 80,92,100
Dv 42,v1,19456
Zvvv 84,92,104
Zvvv 88,92,108
Zvvv 92,92,112
W_ 2
Nvvv 116,30,136,56
Mvvv 116,34,156
Nvvv 120,30,140,60
s_nop 1
v_max3_f32 v10, v76, v80, v84
s_nop 0
v_max3_f32 v10, v10, v88, v92
Mvvv 120,34,160
v_cmp_lt_f32 vcc, 0x42000000, v10
s_cbranch_vccnz .Ltramp1_6
Nvvv 124,30,144,64
A_ v46,v46,v76
A_ v47,v47,v80
Mvvv 124,34,164
A_ v48,v48,v84
A_ v49,v49,v88
Nvvv 128,30,148,68
A_ v50,v50,v92
Mvvv 128,34,168
Ln v8,32,s65,3328
Nvvv 132,30,152,72
Mvvv 132,34,172
W_ 0
Mvvv 132,38,192
Dv 30,%0,20480
Mvvv 132,42,212
Dv 34,v1,20480
Mvvv 128,38,188
Mvvv 128,42,208
Mvvv 124,38,184
Mvvv 124,42,204
Mvvv 120,38,180
Mvvv 120,42,200
Mvvv 116,38,176
Mvvv 116,42,196
W_ 0
Mvvv 116,30,216
Dv 38,%0,12288
Mvvv 116,34,236
Dv 42,v1,12288
Mvvv 120,30,220
Mvvv 120,34,240
Mvvv 124,30,224
Mvvv 124,34,244
Mvvv 128,30,228
Mvvv 128,34,248
s_nop 0
E_ 116
Mvvv 132,30,232
E_ 117
Mvvv 132,34,252
W_ 0
E_ 118
Nvvv 76,38,136,56
Dv 30,%0,13312
Mvvv 76,42,156
Dv 34,v1,13312
Nvvv 80,38,140,60
E_ 119
Mvvv 80,42,160
C_ 119,118,119
C_ 118,116,117
Nvvv 84,38,144,64
E_ 120
Mvvv 84,42,164
E_ 121
Nvvv 88,38,148,68
E_ 122
Mvvv 88,42,168
E_ 123
Nvvv 92,38,152,72
C_ 123,122,123
C_ 122,120,121
Mvvv 92,42,172
W_ 0
E_ 124
Mvvv 92,30,192
Dv 38,%0,14336
Mvvv 92,34,212
Dv 42,v1,14336
Mvvv 88,30,188
E_ 125
Mvvv 88,34,208
E_ 126
Mvvv 84,30,184
S_ s65,s61,86016
E_ 127
Mvvv 84,34,204
S_ m0,s51,12288
C_ 127,126,127
C_ 126,124,125
Mvvv 80,30,180
s_waitcnt vmcnt(6) lgkmcnt(0)
s_barrier
Mvvv 80,34,200
L_ v9,36,s65,0
Mvvv 76,30,176
E_ 128
Mvvv 76,34,196
L_ v9,36,s65,3072
S_ m0,s51,13184
Mvvv 76,38,216
Tv 30,v2,24576
Tv 32,v2,30720
Mvvv 76,42,236
L_ v9,36,s65,128
Mvvv 80,38,220
s_nop 0
Tv 34,v3,24576
Tv 36,v3,30720
Mvvv 80,42,240
L_ v9,36,s65,3200
S_ m0,s51,14080
Mvvv 84,38,224
E_ 129
Mvvv 84,42,244
L_ v9,36,s65,256
Mvvv 88,38,228
E_ 130
Mvvv 88,42,248
L_ v9,36,s65,3328
Mvvv 92,38,232
E_ 131
Mvvv 92,42,252
W_ 0
C_ 131,130,131
C_ 130,128,129
Mavv 0,30,96
s_nop 0
Tv 38,v4,24576
Tv 40,v4,30720
Mavv 20,34,96
E_ 132
Mavv 24,34,100
s_nop 0
Tv 42,v5,24576
Tv 44,v5,30720
Mavv 4,30,100
E_ 133
Mavv 8,30,104
E_ 134
Mavv 28,34,104
E_ 135
Mavv 32,34,108
C_ 135,134,135
C_ 134,132,133
Mavv 12,30,108
E_ 76
Mavv 16,30,112
E_ 77
Mavv 36,34,112
W_ 0
E_ 78
Mavv 56,38,112
s_nop 0
Tv 30,v2,25600
Tv 32,v2,31744
Mavv 76,42,112
E_ 79
Mavv 72,42,108
s_nop 0
Tv 34,v3,25600
Tv 36,v3,31744
Mavv 52,38,108
C_ 116,76,77
C_ 117,78,79
Mavv 48,38,104
E_ 80
Mavv 68,42,104
E_ 81
Mavv 64,42,100
E_ 82
Mavv 44,38,100
E_ 83
Mavv 40,38,96
C_ 120,80,81
C_ 121,82,83
Mavv 60,42,96
W_ 0
E_ 84
Mavv 80,30,96
s_nop 0
Tv 38,v4,25600
Tv 40,v4,31744
Mavv 100,34,96
E_ 85
Mavv 104,34,100
s_nop 0
Tv 42,v5,25600
Tv 44,v5,31744
Mavv 84,30,100
E_ 86
Mavv 88,30,104
E_ 87
Mavv 108,34,104
C_ 124,84,85
C_ 125,86,87
Mavv 112,34,108
E_ 88
Mavv 92,30,108
E_ 89
Mavv 96,30,112
E_ 90
Mavv 116,34,112
W_ 0
E_ 91
Mavv 136,38,112
s_nop 0
Tv 30,v2,26624
Tv 32,v2,32768
Mavv 156,42,112
C_ 128,88,89
C_ 129,90,91
Mavv 152,42,108
s_nop 0
Tv 34,v3,26624
Tv 36,v3,32768
Mavv 132,38,108
E_ 92
Mavv 128,38,104
E_ 93
Mavv 148,42,104
E_ 94
Mavv 144,42,100
E_ 95
Mavv 124,38,100
C_ 132,92,93
C_ 133,94,95
Mavv 120,38,96
v_mov_b32 v92, 0x3f803f80
v_mov_b32 v93, 0x3f803f80
Mavv 140,42,96
W_ 0
v_mov_b32 v94, 0x3f803f80
v_mov_b32 v95, 0x3f803f80
Mavv 160,30,96
s_nop 0
Tv 38,v4,26624
Tv 40,v4,32768
Mavv 180,34,96
Mavv 184,34,100
s_nop 0
Tv 42,v5,26624
Tv 44,v5,32768
Mavv 164,30,100
Mavv 168,30,104
Mavv 188,34,104
S_ s65,s61,110592
Mavv 192,34,108
S_ m0,s51,24576
Mavv 172,30,108
s_waitcnt vmcnt(6) lgkmcnt(0)
s_barrier
Mavv 176,30,112
Ln v7,32,s65,0
Mavv 196,34,112
Mavv 216,38,112
Dv 30,%0,6144
Mavv 236,42,112
Dv 34,v1,6144
Mavv 232,42,108
Ln v8,32,s65,3072
S_ m0,s51,25472
Mavv 212,38,108
Mavv 208,38,104
Ln v7,32,s65,128
Mavv 228,42,104
Mavv 224,42,100
Ln v8,32,s65,3200
S_ m0,s51,26368
Mavv 204,38,100
Mavv 200,38,96
Ln v7,32,s65,256
Mavv 220,42,96
.Lstep1_8:
Zvvv 76,92,116
Dv 38,%0,7168
Zvvv 80,92,120
Dv 42,v1,7168
Zvvv 84,92,124
Zvvv 88,92,128
Zvvv 92,92,132
W_ 2
Nvvv 96,30,136,56
Mvvv 96,34,156
Nvvv 100,30,140,60
s_nop 1
v_max3_f32 v10, v76, v80, v84
s_nop 0
v_max3_f32 v10, v10, v88, v92
Mvvv 100,34,160
v_cmp_lt_f32 vcc, 0x42000000, v10
s_cbranch_vccnz .Ltramp1_7
Nvvv 104,30,144,64
A_ v46,v46,v76
A_ v47,v47,v80
Mvvv 104,34,164
A_ v48,v48,v84
A_ v49,v49,v88
Nvvv 108,30,148,68
A_ v50,v50,v92
Mvvv 108,34,168
Ln v8,32,s65,3328
Nvvv 112,30,152,72
Mvvv 112,34,172
W_ 0
Mvvv 112,38,192
Dv 30,%0,8192
Mvvv 112,42,212
Dv 34,v1,8192
Mvvv 108,38,188
Mvvv 108,42,208
Mvvv 104,38,184
Mvvv 104,42,204
Mvvv 100,38,180
Mvvv 100,42,200
Mvvv 96,38,176
Mvvv 96,42,196
W_ 0
Mvvv 96,30,216
Dv 38,%0,0
Mvvv 96,34,236
Dv 42,v1,0
Mvvv 100,30,220
Mvvv 100,34,240
Mvvv 104,30,224
Mvvv 104,34,244
Mvvv 108,30,228
Mvvv 108,34,248
s_nop 0
E_ 96
Mvvv 112,30,232
E_ 97
Mvvv 112,34,252
W_ 0
E_ 98
Nvvv 76,38,136,56
Dv 30,%0,1024
Mvvv 76,42,156
Dv 34,v1,1024
Nvvv 80,38,140,60
E_ 99
Mvvv 80,42,160
C_ 99,98,99
C_ 98,96,97
Nvvv 84,38,144,64
E_ 100
Mvvv 84,42,164
E_ 101
Nvvv 88,38,148,68
E_ 102
Mvvv 88,42,168
)A"
R"A(E_ 103
Nvvv 92,38,152,72
C_ 103,102,103
C_ 102,100,101
Mvvv 92,42,172
W_ 0
E_ 104
Mvvv 92,30,192
Dv 38,%0,2048
Mvvv 92,34,212
Dv 42,v1,2048
Mvvv 88,30,188
E_ 105
Mvvv 88,34,208
E_ 106
Mvvv 84,30,184
S_ s65,s61,98304
E_ 107
Mvvv 84,34,204
S_ m0,s51,0
C_ 107,106,107
C_ 106,104,105
Mvvv 80,30,180
s_waitcnt vmcnt(6) lgkmcnt(0)
s_barrier
Mvvv 80,34,200
L_ v9,36,s65,0
Mvvv 76,30,176
E_ 108
Mvvv 76,34,196
L_ v9,36,s65,3072
S_ m0,s51,896
Mvvv 76,38,216
Tv 30,v2,12288
Tv 32,v2,18432
Mvvv 76,42,236
L_ v9,36,s65,128
Mvvv 80,38,220
s_nop 0
Tv 34,v3,12288
Tv 36,v3,18432
Mvvv 80,42,240
L_ v9,36,s65,3200
S_ m0,s51,1792
Mvvv 84,38,224
E_ 109
Mvvv 84,42,244
L_ v9,36,s65,256
Mvvv 88,38,228
E_ 110
Mvvv 88,42,248
L_ v9,36,s65,3328
Mvvv 92,38,232
E_ 111
Mvvv 92,42,252
W_ 0
C_ 111,110,111
C_ 110,108,109
Mavv 0,30,116
s_nop 0
Tv 38,v4,12288
Tv 40,v4,18432
Mavv 20,34,116
E_ 112
Mavv 24,34,120
s_nop 0
Tv 42,v5,12288
Tv 44,v5,18432
Mavv 4,30,120
E_ 113
Mavv 8,30,124
E_ 114
Mavv 28,34,124
E_ 115
Mavv 32,34,128
C_ 115,114,115
C_ 114,112,113
Mavv 12,30,128
E_ 76
Mavv 16,30,132
E_ 77
Mavv 36,34,132
W_ 0
E_ 78
Mavv 56,38,132
s_nop 0
Tv 30,v2,13312
Tv 32,v2,19456
Mavv 76,42,132
E_ 79
Mavv 72,42,128
s_nop 0
Tv 34,v3,13312
Tv 36,v3,19456
Mavv 52,38,128
C_ 96,76,77
C_ 97,78,79
Mavv 48,38,124
E_ 80
Mavv 68,42,124
E_ 81
Mavv 64,42,120
E_ 82
Mavv 44,38,120
E_ 83
Mavv 40,38,116
C_ 100,80,81
C_ 101,82,83
Mavv 60,42,116
W_ 0
E_ 84
Mavv 80,30,116
s_nop 0
Tv 38,v4,13312
Tv 40,v4,19456
Mavv 100,34,116
E_ 85
Mavv 104,34,120
s_nop 0
Tv 42,v5,13312
Tv 44,v5,19456
Mavv 84,30,120
E_ 86
Mavv 88,30,124
E_ 87
Mavv 108,34,124
C_ 104,84,85
C_ 105,86,87
Mavv 112,34,128
E_ 88
Mavv 92,30,128
E_ 89
Mavv 96,30,132
E_ 90
Mavv 116,34,132
W_ 0
E_ 91
Mavv 136,38,132
s_nop 0
Tv 30,v2,14336
Tv 32,v2,20480
Mavv 156,42,132
C_ 108,88,89
C_ 109,90,91
Mavv 152,42,128
s_nop 0
Tv 34,v3,14336
Tv 36,v3,20480
Mavv 132,38,128
E_ 92
Mavv 128,38,124
E_ 93
Mavv 148,42,124
E_ 94
Mavv 144,42,120
E_ 95
Mavv 124,38,120
C_ 112,92,93
C_ 113,94,95
Mavv 120,38,116
v_mov_b32 v92, 0x3f803f80
v_mov_b32 v93, 0x3f803f80
Mavv 140,42,116
W_ 0
v_mov_b32 v94, 0x3f803f80
v_mov_b32 v95, 0x3f803f80
Mavv 160,30,116
s_nop 0
Tv 38,v4,14336
Tv 40,v4,20480
Mavv 180,34,116
Mavv 184,34,120
s_nop 0
Tv 42,v5,14336
Tv 44,v5,20480
Mavv 164,30,120
Mavv 168,30,124
Mavv 188,34,124
S_ s65,s61,122880
Mavv 192,34,128
S_ m0,s51,12288
Mavv 172,30,128
s_waitcnt vmcnt(6) lgkmcnt(0)
s_barrier
Mavv 176,30,132
Ln v7,32,s65,0
Mavv 196,34,132
Mavv 216,38,132
Dv 30,%0,30720
Mavv 236,42,132
Dv 34,v1,30720
Mavv 232,42,128
Ln v8,32,s65,3072
S_ m0,s51,13184
Mavv 212,38,128
Mavv 208,38,124
Ln v7,32,s65,128
Mavv 228,42,124
Mavv 224,42,120
Ln v8,32,s65,3200
S_ m0,s51,14080
Mavv 204,38,120
Mavv 200,38,116
Ln v7,32,s65,256
Mavv 220,42,116
s_cmp_eq_u32 s4, 0
s_cbranch_scc1 .Lhs_d1
.Lhs_d1_back:
.Lstep1_9:
Zvvv 76,92,96
Dv 38,%0,31744
Zvvv 80,92,100
Dv 42,v1,31744
Zvvv 84,92,104
Zvvv 88,92,108
Zvvv 92,92,112
W_ 2
Nvvv 116,30,136,56
Mvvv 116,34,156
Nvvv 120,30,140,60
s_nop 1
v_max3_f32 v10, v76, v80, v84
s_nop 0
v_max3_f32 v10, v10, v88, v92
Mvvv 120,34,160
v_cmp_lt_f32 vcc, 0x42000000, v10
s_cbranch_vccnz .Ltramp1_8
Nvvv 124,30,144,64
A_ v46,v46,v76
A_ v47,v47,v80
Mvvv 124,34,164
A_ v48,v48,v84
A_ v49,v49,v88
Nvvv 128,30,148,68
A_ v50,v50,v92
Mvvv 128,34,168
Ln v8,32,s65,3328
Nvvv 132,30,152,72
Mvvv 132,34,172
W_ 0
Mvvv 132,38,192
Dv 30,%0,32768
Mvvv 132,42,212
Dv 34,v1,32768
Mvvv 128,38,188
Mvvv 128,42,208
Mvvv 124,38,184
Mvvv 124,42,204
Mvvv 120,38,180
Mvvv 120,42,200
Mvvv 116,38,176
Mvvv 116,42,196
W_ 0
Mvvv 116,30,216
Dv 38,%0,24576
Mvvv 116,34,236
Dv 42,v1,24576
Mvvv 120,30,220
Mvvv 120,34,240
Mvvv 124,30,224
Mvvv 124,34,244
Mvvv 128,30,228
Mvvv 128,34,248
s_nop 0
E_ 116
Mvvv 132,30,232
E_ 117
Mvvv 132,34,252
W_ 0
E_ 118
Nvvv 76,38,136,56
Dv 30,%0,25600
Mvvv 76,42,156
Dv 34,v1,25600
Nvvv 80,38,140,60
E_ 119
Mvvv 80,42,160
C_ 119,118,119
C_ 118,116,117
Nvvv 84,38,144,64
E_ 120
Mvvv 84,42,164
E_ 121
Nvvv 88,38,148,68
E_ 122
Mvvv 88,42,168
E_ 123
Nvvv 92,38,152,72
C_ 123,122,123
C_ 122,120,121
Mvvv 92,42,172
W_ 0
E_ 124
Mvvv 92,30,192
Dv 38,%0,26624
Mvvv 92,34,212
Dv 42,v1,26624
Mvvv 88,30,188
E_ 125
Mvvv 88,34,208
E_ 126
Mvvv 84,30,184
S_ s65,s61,110592
E_ 127
Mvvv 84,34,204
S_ m0,s51,24576
C_ 127,126,127
C_ 126,124,125
Mvvv 80,30,180
s_waitcnt vmcnt(6) lgkmcnt(0)
s_barrier
Mvvv 80,34,200
L_ v9,36,s65,0
Mvvv 76,30,176
E_ 128
Mvvv 76,34,196
L_ v9,36,s65,3072
S_ m0,s51,25472
Mvvv 76,38,216
Tv 30,v2,0
Tv 32,v2,6144
Mvvv 76,42,236
L_ v9,36,s65,128
Mvvv 80,38,220
s_nop 0
Tv 34,v3,0
Tv 36,v3,6144
Mvvv 80,42,240
L_ v9,36,s65,3200
S_ m0,s51,26368
Mvvv 84,38,224
E_ 129
Mvvv 84,42,244
L_ v9,36,s65,256
Mvvv 88,38,228
E_ 130
Mvvv 88,42,248
L_ v9,36,s65,3328
Mvvv 92,38,232
E_ 131
Mvvv 92,42,252
W_ 0
C_ 131,130,131
C_ 130,128,129
Mavv 0,30,96
s_nop 0
Tv 38,v4,0
Tv 40,v4,6144
Mavv 20,34,96
E_ 132
Mavv 24,34,100
s_nop 0
Tv 42,v5,0
Tv 44,v5,6144
Mavv 4,30,100
E_ 133
Mavv 8,30,104
E_ 134
Mavv 28,34,104
E_ 135
Mavv 32,34,108
C_ 135,134,135
C_ 134,132,133
Mavv 12,30,108
E_ 76
Mavv 16,30,112
E_ 77
Mavv 36,34,112
W_ 0
E_ 78
Mavv 56,38,112
s_nop 0
Tv 30,v2,1024
Tv 32,v2,7168
Mavv 76,42,112
E_ 79
Mavv 72,42,108
s_nop 0
Tv 34,v3,1024
Tv 36,v3,7168
Mavv 52,38,108
C_ 116,76,77
C_ 117,78,79
Mavv 48,38,104
E_ 80
Mavv 68,42,104
E_ 81
Mavv 64,42,100
E_ 82
Mavv 44,38,100
E_ 83
Mavv 40,38,96
C_ 120,80,81
C_ 121,82,83
Mavv 60,42,96
W_ 0
E_ 84
Mavv 80,30,96
s_nop 0
Tv 38,v4,1024
Tv 40,v4,7168
Mavv 100,34,96
E_ 85
Mavv 104,34,100
s_nop 0
Tv 42,v5,1024
Tv 44,v5,7168
Mavv 84,30,100
E_ 86
Mavv 88,30,104
E_ 87
Mavv 108,34,104
C_ 124,84,85
C_ 125,86,87
Mavv 112,34,108
E_ 88
Mavv 92,30,108
E_ 89
Mavv 96,30,112
E_ 90
Mavv 116,34,112
W_ 0
E_ 91
Mavv 136,38,112
s_nop 0
Tv 30,v2,2048
Tv 32,v2,8192
Mavv 156,42,112
C_ 128,88,89
C_ 129,90,91
Mavv 152,42,108
s_nop 0
Tv 34,v3,2048
Tv 36,v3,8192
Mavv 132,38,108
E_ 92
Mavv 128,38,104
E_ 93
Mavv 148,42,104
E_ 94
Mavv 144,42,100
E_ 95
Mavv 124,38,100
C_ 132,92,93
C_ 133,94,95
Mavv 120,38,96
v_mov_b32 v92, 0x3f803f80
v_mov_b32 v93, 0x3f803f80
Mavv 140,42,96
W_ 0
v_mov_b32 v94, 0x3f803f80
v_mov_b32 v95, 0x3f803f80
Mavv 160,30,96
s_nop 0
Tv 38,v4,2048
Tv 40,v4,8192
Mavv 180,34,96
Mavv 184,34,100
s_nop 0
Tv 42,v5,2048
Tv 44,v5,8192
Mavv 164,30,100
Mavv 168,30,104
Mavv 188,34,104
S_ s65,s61,135168
Mavv 192,34,108
S_ m0,s51,0
Mavv 172,30,108
s_waitcnt vmcnt(6) lgkmcnt(0)
s_barrier
Mavv 176,30,112
Ln v7,32,s65,0
Mavv 196,34,112
Mavv 216,38,112
Dv 30,%0,18432
Mavv 236,42,112
Dv 34,v1,18432
Mavv 232,42,108
Ln v8,32,s65,3072
S_ m0,s51,896
Mavv 212,38,108
Mavv 208,38,104
Ln v7,32,s65,128
Mavv 228,42,104
Mavv 224,42,100
Ln v8,32,s65,3200
S_ m0,s51,1792
Mavv 204,38,100
Mavv 200,38,96
Ln v7,32,s65,256
Mavv 220,42,96
.Lstep1_10:
Zvvv 76,92,116
Dv 38,%0,19456
Zvvv 80,92,120
Dv 42,v1,19456
Zvvv 84,92,124
Zvvv 88,92,128
Zvvv 92,92,132
W_ 2
Nvvv 96,30,136,56
Mvvv 96,34,156
Nvvv 100,30,140,60
s_nop 1
v_max3_f32 v10, v76, v80, v84
s_nop 0
v_max3_f32 v10, v10, v88, v92
Mvvv 100,34,160
v_cmp_lt_f32 vcc, 0x42000000, v10
s_cbranch_vccnz .Ltramp1_9
Nvvv 104,30,144,64
A_ v46,v46,v76
A_ v47,v47,v80
Mvvv 104,34,164
A_ v48,v48,v84
A_ v49,v49,v88
Nvvv 108,30,148,68
A_ v50,v50,v92
Mvvv 108,34,168
Ln v8,32,s65,3328
Nvvv 112,30,152,72
Mvvv 112,34,172
W_ 0
Mvvv 112,38,192
Dv 30,%0,20480
Mvvv 112,42,212
Dv 34,v1,20480
Mvvv 108,38,188
Mvvv 108,42,208
Mvvv 104,38,184
Mvvv 104,42,204
Mvvv 100,38,180
Mvvv 100,42,200
Mvvv 96,38,176
Mvvv 96,42,196
W_ 0
Mvvv 96,30,216
Dv 38,%0,12288
Mvvv 96,34,236
Dv 42,v1,12288
Mvvv 100,30,220
Mvvv 100,34,240
Mvvv 104,30,224
Mvvv 104,34,244
Mvvv 108,30,228
Mvvv 108,34,248
s_nop 0
E_ 96
Mvvv 112,30,232
E_ 97
Mvvv 112,34,252
W_ 0
E_ 98
Nvvv 76,38,136,56
Dv 30,%0,13312
Mvvv 76,42,156
Dv 34,v1,13312
Nvvv 80,38,140,60
E_ 99
Mvvv 80,42,160
C_ 99,98,99
C_ 98,96,97
Nvvv 84,38,144,64
E_ 100
Mvvv 84,42,164
E_ 101
Nvvv 88,38,148,68
E_ 102
Mvvv 88,42,168
E_ 103
Nvvv 92,38,152,72
C_ 103,102,103
C_ 102,100,101
Mvvv 92,42,172
W_ 0
E_ 104
Mvvv 92,30,192
Dv 38,%0,14336
Mvvv 92,34,212
Dv 42,v1,14336
Mvvv 88,30,188
E_ 105
Mvvv 88,34,208
E_ 106
Mvvv 84,30,184
S_ s65,s61,122880
E_ 107
Mvvv 84,34,204
S_ m0,s51,12288
C_ 107,106,107
C_ 106,104,105
Mvvv 80,30,180
s_waitcnt vmcnt(6) lgkmcnt(0)
s_barrier
Mvvv 80,34,200
L_ v9,36,s65,0
Mvvv 76,30,176
E_ 108
Mvvv 76,34,196
L_ v9,36,s65,3072
S_ m0,s51,13184
Mvvv 76,38,216
Tv 30,v2,24576
Tv 32,v2,30720
Mvvv 76,42,236
L_ v9,36,s65,128
Mvvv 80,38,220
s_nop 0
Tv 34,v3,24576
Tv 36,v3,30720
Mvvv 80,42,240
L_ v9,36,s65,3200
S_ m0,s51,14080
Mvvv 84,38,224
E_ 109
Mvvv 84,42,244
L_ v9,36,s65,256
Mvvv 88,38,228
E_ 110
Mvvv 88,42,248
L_ v9,36,s65,3328
Mvvv 92,38,232
E_ 111
Mvvv 92,42,252
W_ 0
C_ 111,110,111
C_ 110,108,109
Mavv 0,30,116
s_nop 0
Tv 38,v4,24576
Tv 40,v4,30720
Mavv 20,34,116
E_ 112
Mavv 24,34,120
s_nop 0
Tv 42,v5,24576
Tv 44,v5,30720
Mavv 4,30,120
E_ 113
Mavv 8,30,124
E_ 114
Mavv 28,34,124
E_ 115
Mavv 32,34,128
C_ 115,114,115
C_ 114,112,113
Mavv 12,30,128
E_ 76
Mavv 16,30,132
E_ 77
Mavv 36,34,132
W_ 0
E_ 78
Mavv 56,38,132
s_nop 0
Tv 30,v2,25600
Tv 32,v2,31744
Mavv 76,42,132
E_ 79
Mavv 72,42,128
s_nop 0
Tv 34,v3,25600
Tv 36,v3,31744
Mavv 52,38,128
C_ 96,76,77
C_ 97,78,79
Mavv 48,38,124
E_ 80
Mavv 68,42,124
E_ 81
Mavv 64,42,120
E_ 82
Mavv 44,38,120
E_ 83
Mavv 40,38,116
C_ 100,80,81
C_ 101,82,83
Mavv 60,42,116
W_ 0
E_ 84
Mavv 80,30,116
s_nop 0
Tv 38,v4,25600
Tv 40,v4,31744
Mavv 100,34,116
E_ 85
Mavv 104,34,120
s_nop 0
Tv 42,v5,25600
Tv 44,v5,31744
Mavv 84,30,120
E_ 86
Mavv 88,30,124
E_ 87
Mavv 108,34,124
C_ 104,84,85
C_ 105,86,87
Mavv 112,34,128
E_ 88
Mavv 92,30,128
E_ 89
Mavv 96,30,132
E_ 90
Mavv 116,34,132
W_ 0
E_ 91
Mavv 136,38,132
s_nop 0
Tv 30,v2,26624
Tv 32,v2,32768
Mavv 156,42,132
C_ 108,88,89
C_ 109,90,91
Mavv 152,42,128
s_nop 0
Tv 34,v3,26624
Tv 36,v3,32768
Mavv 132,38,128
E_ 92
Mavv 128,38,124
E_ 93
Mavv 148,42,124
E_ 94
Mavv 144,42,120
E_ 95
Mavv 124,38,120
C_ 112,92,93
C_ 113,94,95
Mavv 120,38,116
v_mov_b32 v92, 0x3f803f80
v_mov_b32 v93, 0x3f803f80
Mavv 140,42,116
W_ 0
v_mov_b32 v94, 0x3f803f80
v_mov_b32 v95, 0x3f803f80
Mavv 160,30,116
s_nop 0
Tv 38,v4,26624
Tv 40,v4,32768
Mavv 180,34,116
Mavv 184,34,120
s_nop 0
Tv 42,v5,26624
Tv 44,v5,32768
Mavv 164,30,120
Mavv 168,30,124
Mavv 188,34,124
S_ s65,s61,147456
Mavv 192,34,128
S_ m0,s51,24576
Mavv 172,30,128
s_waitcnt vmcnt(6) lgkmcnt(0)
s_barrier
Mavv 176,30,132
Ln v7,32,s65,0
Mavv 196,34,132
Mavv 216,38,132
Dv 30,%0,6144
Mavv 236,42,132
Dv 34,v1,6144
Mavv 232,42,128
Ln v8,32,s65,3072
S_ m0,s51,25472
Mavv 212,38,128
Mavv 208,38,124
Ln v7,32,s65,128
Mavv 228,42,124
Mavv 224,42,120
Ln v8,32,s65,3200
S_ m0,s51,26368
Mavv 204,38,120
Mavv 200,38,116
Ln v7,32,s65,256
Mavv 220,42,116
.Lstep1_11:
Zvvv 76,92,96
Dv 38,%0,7168
Zvvv 80,92,100
Dv 42,v1,7168
Zvvv 84,92,104
Zvvv 88,92,108
Zvvv 92,92,112
W_ 2
Nvvv 116,30,136,56
Mvvv 116,34,156
Nvvv 120,30,140,60
s_nop 1
v_max3_f32 v10, v76, v80, v84
s_nop 0
v_max3_f32 v10, v10, v88, v92
Mvvv 120,34,160
v_cmp_lt_f32 vcc, 0x42000000, v10
s_cbranch_vccnz .Ltramp1_10
Nvvv 124,30,144,64
A_ v46,v46,v76
A_ v47,v47,v80
Mvvv 124,34,164
A_ v48,v48,v84
A_ v49,v49,v88
Nvvv 128,30,148,68
A_ v50,v50,v92
Mvvv 128,34,168
Ln v8,32,s65,3328
Nvvv 132,30,152,72
Mvvv 132,34,172
W_ 0
Mvvv 132,38,192
Dv 30,%0,8192
Mvvv 132,42,212
Dv 34,v1,8192
Mvvv 128,38,188
Mvvv 128,42,208
Mvvv 124,38,184
Mvvv 124,42,204
Mvvv 120,38,180
Mvvv 120,42,200
Mvvv 116,38,176
Mvvv 116,42,196
W_ 0
Mvvv 116,30,216
Dv 38,%0,0
Mvvv 116,34,236
Dv 42,v1,0
Mvvv 120,30,220
Mvvv 120,34,240
Mvvv 124,30,224
Mvvv 124,34,244
Mvvv 128,30,228
Mvvv 128,34,248
s_nop 0
E_ 116
Mvvv 132,30,232
E_ 117
Mvvv 132,34,252
W_ 0
E_ 118
Nvvv 76,38,136,56
Dv 30,%0,1024
Mvvv 76,42,156
Dv 34,v1,1024
Nvvv 80,38,140,60
E_ 119
Mvvv 80,42,160
C_ 119,118,119
C_ 118,116,117
Nvvv 84,38,144,64
E_ 120
Mvvv 84,42,164
E_ 121
Nvvv 88,38,148,68
E_ 122
Mvvv 88,42,168
E_ 123
Nvvv 92,38,152,72
C_ 123,122,123
C_ 122,120,121
Mvvv 92,42,172
W_ 0
E_ 124
Mvvv 92,30,192
Dv 38,%0,2048
Mvvv 92,34,212
Dv 42,v1,2048
Mvvv 88,30,188
E_ 125
Mvvv 88,34,208
E_ 126
Mvvv 84,30,184
S_ s65,s61,135168
E_ 127
Mvvv 84,34,204
S_ m0,s51,0
C_ 127,126,127
C_ 126,124,125
Mvvv 80,30,180
s_waitcnt vmcnt(6) lgkmcnt(0)
s_barrier
Mvvv 80,34,200
L_ v9,36,s65,0
Mvvv 76,30,176
E_ 128
Mvvv 76,34,196
L_ v9,36,s65,3072
S_ m0,s51,896
Mvvv 76,38,216
Tv 30,v2,12288
Tv 32,v2,18432
Mvvv 76,42,236
L_ v9,36,s65,128
Mvvv 80,38,220
s_nop 0
Tv 34,v3,12288
Tv 36,v3,18432
Mvvv 80,42,240
L_ v9,36,s65,3200
S_ m0,s51,1792
Mvvv 84,38,224
E_ 129
Mvvv 84,42,244
L_ v9,36,s65,256
Mvvv 88,38,228
E_ 130
Mvvv 88,42,248
L_ v9,36,s65,3328
Mvvv 92,38,232
E_ 131
Mvvv 92,42,252
W_ 0
C_ 131,130,131
C_ 130,128,129
Mavv 0,30,96
s_nop 0
Tv 38,v4,12288
Tv 40,v4,18432
Mavv 20,34,96
E_ 132
Mavv 24,34,100
s_nop 0
Tv 42,v5,12288
Tv 44,v5,18432
Mavv 4,30,100
E_ 133
Mavv 8,30,104
E_ 134
Mavv 28,34,104
E_ 135
Mavv 32,34,108
C_ 135,134,135
C_ 134,132,133
Mavv 12,30,108
E_ 76
Mavv 16,30,112
E_ 77
Mavv 36,34,112
W_ 0
E_ 78
Mavv 56,38,112
s_nop 0
Tv 30,v2,13312
Tv 32,v2,19456
Mavv 76,42,112
E_ 79
Mavv 72,42,108
s_nop 0
Tv 34,v3,13312
Tv 36,v3,19456
Mavv 52,38,108
C_ 116,76,77
C_ 117,78,79
Mavv 48,38,104
E_ 80
Mavv 68,42,104
E_ 81
Mavv 64,42,100
E_ 82
Mavv 44,38,100
E_ 83
Mavv 40,38,96
C_ 120,80,81
C_ 121,82,83
Mavv 60,42,96
W_ 0
E_ 84
Mavv 80,30,96
s_nop 0
Tv 38,v4,13312
Tv 40,v4,19456
Mavv 100,34,96
E_ 85
Mavv 104,34,100
s_nop 0
Tv 42,v5,13312
Tv 44,v5,19456
Mavv 84,30,100
E_ 86
Mavv 88,30,104
E_ 87
Mavv 108,34,104
C_ 124,84,85
C_ 125,86,87
Mavv 112,34,108
E_ 88
Mavv 92,30,108
E_ 89
Mavv 96,30,112
E_ 90
Mavv 116,34,112
W_ 0
E_ 91
Mavv 136,38,112
s_nop 0
Tv 30,v2,14336
Tv 32,v2,20480
Mavv 156,42,112
C_ 128,88,89
C_ 129,90,91
Mavv 152,42,108
s_nop 0
Tv 34,v3,14336
Tv 36,v3,20480
Mavv 132,38,108
E_ 92
Mavv 128,38,104
E_ 93
Mavv 148,42,104
E_ 94
Mavv 144,42,100
E_ 95
Mavv 124,38,100
C_ 132,92,93
C_ 133,94,95
Mavv 120,38,96
v_mov_b32 v92, 0x3f803f80
v_mov_b32 v93, 0x3f803f80
Mavv 140,42,96
W_ 0
v_mov_b32 v94, 0x3f803f80
v_mov_b32 v95, 0x3f803f80
Mavv 160,30,96
s_nop 0
Tv 38,v4,14336
Tv 40,v4,20480
Mavv 180,34,96
Mavv 184,34,100
s_nop 0
Tv 42,v5,14336
Tv 44,v5,20480
Mavv 164,30,100
Mavv 168,30,104
Mavv 188,34,104
S_ s65,s61,159744
Mavv 192,34,108
S_ m0,s51,12288
Mavv 172,30,108
s_waitcnt vmcnt(6) lgkmcnt(0)
s_barrier
Mavv 176,30,112
Ln v7,32,s65,0
Mavv 196,34,112
Mavv 216,38,112
Dv 30,%0,30720
Mavv 236,42,112
Dv 34,v1,30720
Mavv 232,42,108
Ln v8,32,s65,3072
S_ m0,s51,13184
Mavv 212,38,108
Mavv 208,38,104
Ln v7,32,s65,128
Mavv 228,42,104
Mavv 224,42,100
Ln v8,32,s65,3200
S_ m0,s51,14080
Mavv 204,38,100
Mavv 200,38,96
Ln v7,32,s65,256
Mavv 220,42,96
.Lstep1_12:
Zvvv 76,92,116
Dv 38,%0,31744
Zvvv 80,92,120
Dv 42,v1,31744
Zvvv 84,92,124
Zvvv 88,92,128
Zvvv 92,92,132
W_ 2
Nvvv 96,30,136,56
Mvvv 96,34,156
Nvvv 100,30,140,60
s_nop 1
v_max3_f32 v10, v76, v80, v84
s_nop 0
v_max3_f32 v10, v10, v88, v92
Mvvv 100,34,160
v_cmp_lt_f32 vcc, 0x42000000, v10
s_cbranch_vccnz .Ltramp1_11
Nvvv 104,30,144,64
A_ v46,v46,v76
A_ v47,v47,v80
Mvvv 104,34,164
A_ v48,v48,v84
A_ v49,v49,v88
Nvvv 108,30,148,68
A_ v50,v50,v92
Mvvv 108,34,168
Ln v8,32,s65,3328
Nvvv 112,30,152,72
R_ v11,a248
Mvvv 112,34,172
W_ 0
v_min_u32 v11, 240, v11
Mvvv 112,38,192
Dv 30,%0,32768
v_lshlrev_b32 v11, 3, v11
Mvvv 112,42,212
Dv 34,v1,32768
Mvvv 108,38,188
buffer_load_dword a254, v11, s[44:47], s66 offen
Mvvv 108,42,208
Mvvv 104,38,184
Mvvv 104,42,204
Mvvv 100,38,180
Mvvv 100,42,200
Mvvv 96,38,176
Mvvv 96,42,196
W_ 0
Mvvv 96,30,216
Dv 38,%0,24576
Mvvv 96,34,236
Dv 42,v1,24576
Mvvv 100,30,220
Mvvv 100,34,240
Mvvv 104,30,224
Mvvv 104,34,244
Mvvv 108,30,228
Mvvv 108,34,248
s_nop 0
E_ 96
Mvvv 112,30,232
E_ 97
Mvvv 112,34,252
W_ 0
E_ 98
Nvvv 76,38,136,56
Dv 30,%0,25600
Mvvv 76,42,156
Dv 34,v1,25600
Nvvv 80,38,140,60
E_ 99
Mvvv 80,42,160
C_ 99,98,99
C_ 98,96,97
Nvvv 84,38,144,64
E_ 100
Mvvv 84,42,164
E_ 101
Nvvv 88,38,148,68
E_ 102
Mvvv 88,42,168
E_ 103
Nvvv 92,38,152,72
C_ 103,102,103
C_ 102,100,101
Mvvv 92,42,172
W_ 0
E_ 104
Mvvv 92,30,192
Dv 38,%0,26624
Mvvv 92,34,212
Dv 42,v1,26624
Mvvv 88,30,188
E_ 105
Mvvv 88,34,208
E_ 106
Mvvv 84,30,184
S_ s65,s61,147456
E_ 107
Mvvv 84,34,204
S_ m0,s51,24576
C_ 107,106,107
C_ 106,104,105
Mvvv 80,30,180
s_waitcnt vmcnt(7) lgkmcnt(0)
s_barrier
Mvvv 80,34,200
L_ v9,36,s65,0
Mvvv 76,30,176
E_ 108
Mvvv 76,34,196
L_ v9,36,s65,3072
S_ m0,s51,25472
Mvvv 76,38,216
Tv 30,v2,0
Tv 32,v2,6144
Mvvv 76,42,236
L_ v9,36,s65,128
Mvvv 80,38,220
s_nop 0
Tv 34,v3,0
Tv 36,v3,6144
Mvvv 80,42,240
L_ v9,36,s65,3200
S_ m0,s51,26368
Mvvv 84,38,224
E_ 109
Mvvv 84,42,244
L_ v9,36,s65,256
Mvvv 88,38,228
E_ 110
Mvvv 88,42,248
L_ v9,36,s65,3328
Mvvv 92,38,232
E_ 111
Mvvv 92,42,252
W_ 0
C_ 111,110,111
C_ 110,108,109
Mavv 0,30,116
s_nop 0
Tv 38,v4,0
Tv 40,v4,6144
Mavv 20,34,116
E_ 112
Mavv 24,34,120
s_nop 0
Tv 42,v5,0
Tv 44,v5,6144
Mavv 4,30,120
E_ 113
Mavv 8,30,124
E_ 114
Mavv 28,34,124
E_ 115
Mavv 32,34,128
C_ 115,114,115
C_ 114,112,113
Mavv 12,30,128
E_ 76
Mavv 16,30,132
E_ 77
Mavv 36,34,132
W_ 0
E_ 78
Mavv 56,38,132
s_nop 0
Tv 30,v2,1024
Tv 32,v2,7168
Mavv 76,42,132
E_ 79
Mavv 72,42,128
s_nop 0
Tv 34,v3,1024
Tv 36,v3,7168
Mavv 52,38,128
C_ 96,76,77
C_ 97,78,79
Mavv 48,38,124
E_ 80
Mavv 68,42,124
E_ 81
Mavv 64,42,120
E_ 82
Mavv 44,38,120
E_ 83
Mavv 40,38,116
C_ 100,80,81
C_ 101,82,83
Mavv 60,42,116
W_ 0
E_ 84
Mavv 80,30,116
s_nop 0
Tv 38,v4,1024
Tv 40,v4,7168
Mavv 100,34,116
E_ 85
Mavv 104,34,120
s_nop 0
Tv 42,v5,1024
Tv 44,v5,7168
Mavv 84,30,120
E_ 86
Mavv 88,30,124
E_ 87
Mavv 108,34,124
C_ 104,84,85
C_ 105,86,87
Mavv 112,34,128
E_ 88
Mavv 92,30,128
E_ 89
Mavv 96,30,132
E_ 90
Mavv 116,34,132
W_ 0
E_ 91
Mavv 136,38,132
s_nop 0
Tv 30,v2,2048
Tv 32,v2,8192
Mavv 156,42,132
C_ 108,88,89
C_ 109,90,91
Mavv 152,42,128
s_nop 0
Tv 34,v3,2048
Tv 36,v3,8192
Mavv 132,38,128
E_ 92
Mavv 128,38,124
E_ 93
Mavv 148,42,124
E_ 94
Mavv 144,42,120
E_ 95
Mavv 124,38,120
C_ 112,92,93
C_ 113,94,95
Mavv 120,38,116
v_mov_b32 v92, 0x3f803f80
v_mov_b32 v93, 0x3f803f80
Mavv 140,42,116
W_ 0
v_mov_b32 v94, 0x3f803f80
v_mov_b32 v95, 0x3f803f80
Mavv 160,30,116
s_nop 0
Tv 38,v4,2048
Tv 40,v4,8192
Mavv 180,34,116
Mavv 184,34,120
s_nop 0
Tv 42,v5,2048
Tv 44,v5,8192
Mavv 164,30,120
Mavv 168,30,124
Mavv 188,34,124
S_ s65,s61,172032
Mavv 192,34,128
S_ m0,s51,0
Mavv 172,30,128
s_waitcnt vmcnt(7) lgkmcnt(0)
s_barrier
Mavv 176,30,132
Ln v7,32,s65,0
Mavv 196,34,132
Mavv 216,38,132
Dv 30,%0,18432
Mavv 236,42,132
Dv 34,v1,18432
Mavv 232,42,128
Ln v8,32,s65,3072
S_ m0,s51,896
Mavv 212,38,128
Mavv 208,38,124
Ln v7,32,s65,128
Mavv 228,42,124
Mavv 224,42,120
Ln v8,32,s65,3200
S_ m0,s51,1792
Mavv 204,38,120
Mavv 200,38,116
Ln v7,32,s65,256
Mavv 220,42,116
.Lstep1_13:
Zvvv 76,92,96
Dv 38,%0,19456
Zvvv 80,92,100
Dv 42,v1,19456
Zvvv 84,92,104
Zvvv 88,92,108
Zvvv 92,92,112
W_ 2
Nvvv 116,30,136,56
Mvvv 116,34,156
Nvvv 120,30,140,60
s_nop 1
v_max3_f32 v10, v76, v80, v84
s_nop 0
v_max3_f32 v10, v10, v88, v92
Mvvv 120,34,160
v_cmp_lt_f32 vcc, 0x42000000, v10
s_cbranch_vccnz .Ltramp1_12
Nvvv 124,30,144,64
A_ v46,v46,v76
A_ v47,v47,v80
Mvvv 124,34,164
A_ v48,v48,v84
A_ v49,v49,v88
Nvvv 128,30,148,68
A_ v50,v50,v92
Mvvv 128,34,168
Ln v8,32,s65,3328
Nvvv 132,30,152,72
Mvvv 132,34,172
W_ 0
Mvvv 132,38,192
Dv 30,%0,20480
Mvvv 132,42,212
Dv 34,v1,20480
Mvvv 128,38,188
Mvvv 128,42,208
Mvvv 124,38,184
Mvvv 124,42,204
Mvvv 120,38,180
Mvvv 120,42,200
Mvvv 116,38,176
Mvvv 116,42,196
W_ 0
Mvvv 116,30,216
Dv 38,%0,12288
Mvvv 116,34,236
Dv 42,v1,12288
Mvvv 120,30,220
Mvvv 120,34,240
Mvvv 124,30,224
Mvvv 124,34,244
Mvvv 128,30,228
Mvvv 128,34,248
s_nop 0
E_ 116
Mvvv 132,30,232
E_ 117
Mvvv 132,34,252
W_ 0
E_ 118
Nvvv 76,38,136,56
Dv 30,%0,13312
Mvvv 76,42,156
Dv 34,v1,13312
Nvvv 80,38,140,60
E_ 119
Mvvv 80,42,160
C_ 119,118,119
C_ 118,116,117
Nvvv 84,38,144,64
E_ 120
Mvvv 84,42,164
E_ 121
Nvvv 88,38,148,68
E_ 122
Mvvv 88,42,168
E_ 123
Nvvv 92,38,152,72
C_ 123,122,123
C_ 122,120,121
Mvvv 92,42,172
W_ 0
E_ 124
Mvvv 92,30,192
Dv 38,%0,14336
Mvvv 92,34,212
Dv 42,v1,14336
Mvvv 88,30,188
E_ 125
Mvvv 88,34,208
E_ 126
Mvvv 84,30,184
S_ s65,s61,159744
E_ 127
Mvvv 84,34,204
S_ m0,s51,12288
C_ 127,126,127
C_ 126,124,125
Mvvv 80,30,180
s_waitcnt vmcnt(6) lgkmcnt(0)
s_barrier
Mvvv 80,34,200
L_ v9,36,s65,0
Mvvv 76,30,176
E_ 128
Mvvv 76,34,196
L_ v9,36,s65,3072
S_ m0,s51,13184
Mvvv 76,38,216
Tv 30,v2,24576
Tv 32,v2,30720
Mvvv 76,42,236
L_ v9,36,s65,128
Mvvv 80,38,220
s_nop 0
Tv 34,v3,24576
Tv 36,v3,30720
Mvvv 80,42,240
L_ v9,36,s65,3200
S_ m0,s51,14080
Mvvv 84,38,224
E_ 129
Mvvv 84,42,244
L_ v9,36,s65,256
Mvvv 88,38,228
E_ 130
Mvvv 88,42,248
L_ v9,36,s65,3328
Mvvv 92,38,232
E_ 131
Mvvv 92,42,252
W_ 0
C_ 131,130,131
C_ 130,128,129
Mavv 0,30,96
s_nop 0
Tv 38,v4,24576
Tv 40,v4,30720
Mavv 20,34,96
E_ 132
Mavv 24,34,100
s_nop 0
Tv 42,v5,24576
Tv 44,v5,30720
Mavv 4,30,100
E_ 133
Mavv 8,30,104
E_ 134
Mavv 28,34,104
E_ 135
Mavv 32,34,108
C_ 135,134,135
C_ 134,132,133
Mavv 12,30,108
E_ 76
Mavv 16,30,112
E_ 77
Mavv 36,34,112
W_ 0
E_ 78
Mavv 56,38,112
s_nop 0
Tv 30,v2,25600
Tv 32,v2,31744
Mavv 76,42,112
E_ 79
Mavv 72,42,108
s_nop 0
Tv 34,v3,25600
Tv 36,v3,31744
Mavv 52,38,108
C_ 116,76,77
C_ 117,78,79
Mavv 48,38,104
E_ 80
Mavv 68,42,104
E_ 81
Mavv 64,42,100
E_ 82
Mavv 44,38,100
E_ 83
Mavv 40,38,96
C_ 120,80,81
C_ 121,82,83
Mavv 60,42,96
W_ 0
E_ 84
Mavv 80,30,96
s_nop 0
Tv 38,v4,25600
Tv 40,v4,31744
Mavv 100,34,96
E_ 85
Mavv 104,34,100
s_nop 0
Tv 42,v5,25600
Tv 44,v5,31744
Mavv 84,30,100
E_ 86
Mavv 88,30,104
E_ 87
Mavv 108,34,104
C_ 124,84,85
C_ 125,86,87
Mavv 112,34,108
E_ 88
Mavv 92,30,108
E_ 89
Mavv 96,30,112
E_ 90
Mavv 116,34,112
W_ 0
E_ 91
Mavv 136,38,112
s_nop 0
Tv 30,v2,26624
Tv 32,v2,32768
Mavv 156,42,112
C_ 128,88,89
C_ 129,90,91
Mavv 152,42,108
s_nop 0
Tv 34,v3,26624
Tv 36,v3,32768
Mavv 132,38,108
E_ 92
Mavv 128,38,104
E_ 93
Mavv 148,42,104
E_ 94
Mavv 144,42,100
E_ 95
Mavv 124,38,100
C_ 132,92,93
C_ 133,94,95
Mavv 120,38,96
v_mov_b32 v92, 0x3f803f80
v_mov_b32 v93, 0x3f803f80
Mavv 140,42,96
W_ 0
v_mov_b32 v94, 0x3f803f80
v_mov_b32 v95, 0x3f803f80
Mavv 160,30,96
s_nop 0
Tv 38,v4,26624
Tv 40,v4,32768
Mavv 180,34,96
Mavv 184,34,100
s_nop 0
Tv 42,v5,26624
Tv 44,v5,32768
Mavv 164,30,100
Mavv 168,30,104
Mavv 188,34,104
S_ s65,s61,184320
Mavv 192,34,108
S_ m0,s51,24576
Mavv 172,30,108
s_waitcnt vmcnt(6) lgkmcnt(0)
s_barrier
Mavv 176,30,112
Ln v7,32,s65,0
Mavv 196,34,112
Mavv 216,38,112
Dv 30,%0,6144
Mavv 236,42,112
Dv 34,v1,6144
Mavv 232,42,108
Ln v8,32,s65,3072
S_ m0,s51,25472
Mavv 212,38,108
Mavv 208,38,104
Ln v7,32,s65,128
Mavv 228,42,104
Mavv 224,42,100
Ln v8,32,s65,3200
S_ m0,s51,26368
Mavv 204,38,100
Mavv 200,38,96
Ln v7,32,s65,256
Mavv 220,42,96
.Lstep1_14:
Zvvv 76,92,116
Dv 38,%0,7168
Zvvv 80,92,120
Dv 42,v1,7168
Zvvv 84,92,124
Zvvv 88,92,128
Zvvv 92,92,132
W_ 2
Nvvv 96,30,136,56
Mvvv 96,34,156
Nvvv 100,30,140,60
s_nop 1
v_max3_f32 v10, v76, v80, v84
s_nop 0
v_max3_f32 v10, v10, v88, v92
Mvvv 100,34,160
v_cmp_lt_f32 vcc, 0x42000000, v10
s_cbranch_vccnz .Ltramp1_13
Nvvv 104,30,144,64
A_ v46,v46,v76
A_ v47,v47,v80
Mvvv 104,34,164
A_ v48,v48,v84
A_ v49,v49,v88
Nvvv 108,30,148,68
A_ v50,v50,v92
Mvvv 108,34,168
Ln v8,32,s65,3328
Nvvv 112,30,152,72
Mvvv 112,34,172
W_ 0
Mvvv 112,38,192
Dv 30,%0,8192
Mvvv 112,42,212
Dv 34,v1,8192
Mvvv 108,38,188
Mvvv 108,42,208
Mvvv 104,38,184
Mvvv 104,42,204
Mvvv 100,38,180
Mvvv 100,42,200
Mvvv 96,38,176
Mvvv 96,42,196
W_ 0
Mvvv 96,30,216
Dv 38,%0,0
Mvvv 96,34,236
Dv 42,v1,0
Mvvv 100,30,220
Mvvv 100,34,240
Mvvv 104,30,224
Mvvv 104,34,244
Mvvv 108,30,228
Mvvv 108,34,248
s_nop 0
E_ 96
Mvvv 112,30,232
E_ 97
Mvvv 112,34,252
W_ 0
E_ 98
Nvvv 76,38,136,56
Dv 30,%0,1024
Mvvv 76,42,156
Dv 34,v1,1024
Nvvv 80,38,140,60
E_ 99
Mvvv 80,42,160
C_ 99,98,99
C_ 98,96,97
Nvvv 84,38,144,64
E_ 100
Mvvv 84,42,164
E_ 101
Nvvv 88,38,148,68
E_ 102
Mvvv 88,42,168
E_ 103
Nvvv 92,38,152,72
C_ 103,102,103
C_ 102,100,101
Mvvv 92,42,172
W_ 0
E_ 104
Mvvv 92,30,192
Dv 38,%0,2048
Mvvv 92,34,212
Dv 42,v1,2048
Mvvv 88,30,188
E_ 105
Mvvv 88,34,208
E_ 106
Mvvv 84,30,184
S_ s65,s61,172032
E_ 107
Mvvv 84,34,204
S_ m0,s51,0
C_ 107,106,107
C_ 106,104,105
Mvvv 80,30,180
s_mov_b64 exec, 0
s_bitcmp1_b32 s4, 1
s_cbranch_scc1 .Lhop_o_b
s_bitcmp1_b32 s4, 0
s_cbranch_scc1 .Lpad_o_1
s_branch .Lpad_o_0
.Lhop_o_b:
s_bitcmp1_b32 s4, 0
s_cbranch_scc1 .Lpad_o_3
s_branch .Lpad_o_2
.Lhop_o_r:
s_mov_b64 exec, -1
s_nop 1
s_waitcnt vmcnt(6) lgkmcnt(0)
s_barrier
Mvvv 80,34,200
L_ v9,36,s65,0
Mvvv 76,30,176
E_ 108
Mvvv 76,34,196
L_ v9,36,s65,3072
S_ m0,s51,896
Mvvv 76,38,216
Tv 30,v2,12288
Tv 32,v2,18432
Mvvv 76,42,236
L_ v9,36,s65,128
Mvvv 80,38,220
s_nop 0
Tv 34,v3,12288
Tv 36,v3,18432
Mvvv 80,42,240
L_ v9,36,s65,3200
S_ m0,s51,1792
Mvvv 84,38,224
E_ 109
Mvvv 84,42,244
L_ v9,36,s65,256
Mvvv 88,38,228
E_ 110
Mvvv 88,42,248
L_ v9,36,s65,3328
Mvvv 92,38,232
E_ 111
Mvvv 92,42,252
W_ 0
C_ 111,110,111
C_ 110,108,109
Mavv 0,30,116
s_nop 0
Tv 38,v4,12288
Tv 40,v4,18432
Mavv 20,34,116
E_ 112
Mavv 24,34,120
s_nop 0
Tv 42,v5,12288
Tv 44,v5,18432
Mavv 4,30,120
E_ 113
Mavv 8,30,124
E_ 114
Mavv 28,34,124
E_ 115
Mavv 32,34,128
C_ 115,114,115
C_ 114,112,113
Mavv 12,30,128
E_ 76
Mavv 16,30,132
E_ 77
Mavv 36,34,132
W_ 0
E_ 78
Mavv 56,38,132
s_nop 0
Tv 30,v2,13312
Tv 32,v2,19456
Mavv 76,42,132
E_ 79
Mavv 72,42,128
s_nop 0
Tv 34,v3,13312
Tv 36,v3,19456
Mavv 52,38,128
C_ 96,76,77
C_ 97,78,79
Mavv 48,38,124
E_ 80
Mavv 68,42,124
E_ 81
Mavv 64,42,120
E_ 82
Mavv 44,38,120
E_ 83
Mavv 40,38,116
C_ 100,80,81
C_ 101,82,83
Mavv 60,42,116
W_ 0
E_ 84
Mavv 80,30,116
s_nop 0
Tv 38,v4,13312
Tv 40,v4,19456
Mavv 100,34,116
E_ 85
Mavv 104,34,120
s_nop 0
Tv 42,v5,13312
Tv 44,v5,19456
Mavv 84,30,120
E_ 86
Mavv 88,30,124
E_ 87
Mavv 108,34,124
C_ 104,84,85
C_ 105,86,87
Mavv 112,34,128
E_ 88
Mavv 92,30,128
E_ 89
Mavv 96,30,132
E_ 90
Mavv 116,34,132
W_ 0
E_ 91
Mavv 136,38,132
s_nop 0
Tv 30,v2,14336
Tv 32,v2,20480
Mavv 156,42,132
C_ 108,88,89
C_ 109,90,91
Mavv 152,42,128
s_nop 0
Tv 34,v3,14336
Tv 36,v3,20480
Mavv 132,38,128
E_ 92
Mavv 128,38,124
E_ 93
Mavv 148,42,124
E_ 94
Mavv 144,42,120
E_ 95
Mavv 124,38,120
C_ 112,92,93
C_ 113,94,95
Mavv 120,38,116
v_mov_b32 v92, 0x3f803f80
v_mov_b32 v93, 0x3f803f80
Mavv 140,42,116
W_ 0
v_mov_b32 v94, 0x3f803f80
v_mov_b32 v95, 0x3f803f80
Mavv 160,30,116
s_nop 0
Tv 38,v4,14336
Tv 40,v4,20480
Mavv 180,34,116
Mavv 184,34,120
s_nop 0
Tv 42,v5,14336
Tv 44,v5,20480
Mavv 164,30,120
Mavv 168,30,124
Mavv 188,34,124
S_ s65,s61,184320
Mavv 192,34,128
S_ m0,s51,12288
Mavv 172,30,128
s_waitcnt vmcnt(6) lgkmcnt(0)
s_barrier
Mavv 176,30,132
L_ v9,36,s65,0
Mavv 196,34,132
Mavv 216,38,132
Dv 30,%0,30720
Mavv 236,42,132
Dv 34,v1,30720
Mavv 232,42,128
L_ v9,36,s65,3072
S_ m0,s51,13184
Mavv 212,38,128
Mavv 208,38,124
L_ v9,36,s65,128
Mavv 228,42,124
Mavv 224,42,120
L_ v9,36,s65,3200
S_ m0,s51,14080
Mavv 204,38,120
Mavv 200,38,116
L_ v9,36,s65,256
Mavv 220,42,116
.Lstep1_15:
Zvvv 76,92,96
Dv 38,%0,31744
Zvvv 80,92,100
Dv 42,v1,31744
Zvvv 84,92,104
Zvvv 88,92,108
Zvvv 92,92,112
W_ 2
Nvvv 116,30,136,56
Mvvv 116,34,156
Nvvv 120,30,140,60
s_nop 1
v_max3_f32 v10, v76, v80, v84
s_nop 0
v_max3_f32 v10, v10, v88, v92
Mvvv 120,34,160
v_cmp_lt_f32 vcc, 0x42000000, v10
s_cbranch_vccnz .Ltramp1_14
Nvvv 124,30,144,64
A_ v46,v46,v76
A_ v47,v47,v80
Mvvv 124,34,164
A_ v48,v48,v84
A_ v49,v49,v88
Nvvv 128,30,148,68
A_ v50,v50,v92
Mvvv 128,34,168
L_ v9,36,s65,3328
Nvvv 132,30,152,72
Mvvv 132,34,172
W_ 0
Mvvv 132,38,192
Dv 30,%0,32768
Mvvv 132,42,212
Dv 34,v1,32768
Mvvv 128,38,188
Mvvv 128,42,208
Mvvv 124,38,184
Mvvv 124,42,204
Mvvv 120,38,180
Mvvv 120,42,200
Mvvv 116,38,176
Mvvv 116,42,196
W_ 0
Mvvv 116,30,216
Dv 38,%0,24576
Mvvv 116,34,236
Dv 42,v1,24576
Mvvv 120,30,220
Mvvv 120,34,240
Mvvv 124,30,224
Mvvv 124,34,244
Mvvv 128,30,228
Mvvv 128,34,248
s_nop 0
E_ 116
Mvvv 132,30,232
E_ 117
Mvvv 132,34,252
W_ 0
E_ 118
Nvvv 76,38,136,56
Dv 30,%0,25600
Mvvv 76,42,156
Dv 34,v1,25600
Nvvv 80,38,140,60
E_ 119
Mvvv 80,42,160
C_ 119,118,119
C_ 118,116,117
Nvvv 84,38,144,64
E_ 120
Mvvv 84,42,164
E_ 121
Nvvv 88,38,148,68
E_ 122
)A"
R"A(Mvvv 88,42,168
E_ 123
Nvvv 92,38,152,72
C_ 123,122,123
C_ 122,120,121
Mvvv 92,42,172
W_ 0
E_ 124
Mvvv 92,30,192
Dv 38,%0,26624
Mvvv 92,34,212
Dv 42,v1,26624
Mvvv 88,30,188
E_ 125
Mvvv 88,34,208
E_ 126
Mvvv 84,30,184
E_ 127
Mvvv 84,34,204
C_ 127,126,127
C_ 126,124,125
Mvvv 80,30,180
s_waitcnt vmcnt(6) lgkmcnt(0)
s_barrier
Mvvv 80,34,200
R_ v7,a248
Mvvv 76,30,176
v_lshrrev_b32 v7, 7, v7
Mvvv 76,34,196
v_mul_u32_u24 v7, 896, v7
Mvvv 76,38,216
s_nop 0
Tv 30,v2,0
Tv 32,v2,6144
Mvvv 76,42,236
v_add_u32 v7, v7, v9
Mvvv 80,38,220
S_ s71,s72,1024
Tv 34,v3,0
Tv 36,v3,6144
Mvvv 80,42,240
S_ s55,s71,491520
E_ 128
Mvvv 84,38,224
S_ m0,s51,24576
E_ 129
Mvvv 84,42,244
L_ v7,24,s55,0
Mvvv 88,38,228
S_ s55,s71,501760
E_ 130
Mvvv 88,42,248
S_ m0,s51,25600
E_ 131
Mvvv 92,38,232
L_ v7,24,s55,0
Mvvv 92,42,252
S_ s55,s71,512000
C_ 131,130,131
C_ 130,128,129
W_ 2
Mavv 0,30,96
S_ m0,s51,26624
Tv 38,v4,0
Tv 40,v4,6144
W_ 2
Mavv 20,34,96
L_ v7,24,s55,0
Mavv 24,34,100
S_ s55,s71,522240
Tv 42,v5,0
Tv 44,v5,6144
Mavv 4,30,100
S_ m0,s51,27648
E_ 132
Mavv 8,30,104
L_ v7,24,s55,0
Mavv 28,34,104
S_ s55,s71,532480
E_ 133
Mavv 32,34,108
S_ m0,s51,28672
E_ 134
Mavv 12,30,108
L_ v7,24,s55,0
Mavv 16,30,112
S_ s55,s71,542720
E_ 135
Mavv 36,34,112
S_ m0,s51,29696
C_ 135,134,135
C_ 134,132,133
W_ 2
Mavv 56,38,112
Tv 30,v2,1024
Tv 32,v2,7168
W_ 2
Mavv 76,42,112
L_ v7,24,s55,0
Mavv 72,42,108
Tv 34,v3,1024
Tv 36,v3,7168
Mavv 52,38,108
E_ 76
Mavv 48,38,104
E_ 77
Mavv 68,42,104
E_ 78
Mavv 64,42,100
E_ 79
Mavv 44,38,100
C_ 116,76,77
C_ 117,78,79
Mavv 40,38,96
E_ 80
Mavv 60,42,96
W_ 0
E_ 81
Mavv 80,30,96
s_nop 0
Tv 38,v4,1024
Tv 40,v4,7168
Mavv 100,34,96
E_ 82
Mavv 104,34,100
s_nop 0
Tv 42,v5,1024
Tv 44,v5,7168
Mavv 84,30,100
E_ 83
Mavv 88,30,104
C_ 120,80,81
C_ 121,82,83
Mavv 108,34,104
E_ 84
Mavv 112,34,108
E_ 85
Mavv 92,30,108
E_ 86
Mavv 96,30,112
E_ 87
Mavv 116,34,112
W_ 0
C_ 124,84,85
C_ 125,86,87
Mavv 136,38,112
s_nop 0
Tv 30,v2,2048
Tv 32,v2,8192
Mavv 156,42,112
E_ 88
Mavv 152,42,108
s_nop 0
Tv 34,v3,2048
Tv 36,v3,8192
Mavv 132,38,108
E_ 89
Mavv 128,38,104
E_ 90
Mavv 148,42,104
E_ 91
Mavv 144,42,100
C_ 128,88,89
C_ 129,90,91
Mavv 124,38,100
E_ 92
Mavv 120,38,96
E_ 93
Mavv 140,42,96
W_ 0
E_ 94
Mavv 160,30,96
s_nop 0
Tv 38,v4,2048
Tv 40,v4,8192
Mavv 180,34,96
E_ 95
Mavv 184,34,100
s_nop 0
Tv 42,v5,2048
Tv 44,v5,8192
Mavv 164,30,100
C_ 132,92,93
C_ 133,94,95
Mavv 168,30,104
v_mov_b32 v92, 0x3f803f80
v_mov_b32 v93, 0x3f803f80
Mavv 188,34,104
v_mov_b32 v94, 0x3f803f80
v_mov_b32 v95, 0x3f803f80
Mavv 192,34,108
Mavv 172,30,108
s_waitcnt vmcnt(6) lgkmcnt(0)
s_barrier
Mavv 176,30,112
S_ s55,s71,614400
Mavv 196,34,112
S_ m0,s51,0
Mavv 216,38,112
s_nop 0
Tv 30,v2,12288
Tv 32,v2,18432
Zvvv 76,92,116
L_ v7,24,s55,0
Zvvv 80,92,120
S_ s55,s71,624640
Tv 34,v3,12288
Tv 36,v3,18432
Zvvv 84,92,124
S_ m0,s51,1024
R_ v11,a248
Zvvv 88,92,128
L_ v7,24,s55,0
Zvvv 92,92,132
S_ s55,s71,634880
v_mul_u32_u24 v11, 80, v11
Mavv 236,42,112
S_ m0,s51,2048
v_min_u32 v11, 29440, v11
Mavv 232,42,108
L_ v7,24,s55,0
Mavv 212,38,108
S_ s55,s71,645120
S_ s85,s87,1024
Mavv 208,38,104
S_ m0,s51,3072
v_max3_f32 v10, v76, v80, v84
Mavv 228,42,104
v_max3_f32 v10, v10, v88, v92
v_cmp_lt_f32 vcc, 0x42000000, v10
Mavv 224,42,100
L_ v7,24,s55,0
Mavv 204,38,100
S_ s55,s71,655360
Mavv 200,38,96
buffer_load_dword a254, v11, s[24:27], s85 offen
Mavv 220,42,96
S_ m0,s51,4096
S_ s85,s87,1152
buffer_load_dword a254, v11, s[24:27], s85 offen
s_cbranch_vccnz .Ltramp1_15
.Ljoin1:
W_ 2
Mavv 0,30,116
s_nop 0
Tv 38,v4,12288
Tv 40,v4,18432
W_ 2
Mavv 20,34,116
L_ v7,24,s55,0
Mavv 24,34,120
S_ s55,s71,665600
Tv 42,v5,12288
Tv 44,v5,18432
Mavv 4,30,120
S_ m0,s51,5120
R_ v13,a248
S_ s53,s51,36864
Mavv 8,30,124
L_ v7,24,s55,0
Mavv 28,34,124
v_add_u32 v12, s53, v13
s_mul_i32 s53, s4, 10240
Mavv 32,34,128
v_add_u32 v13, s53, v13
R_ v10,a248
Mavv 12,30,128
buffer_load_dword v11, off, s[92:95], s96 offset:0 sc1
Mavv 16,30,132
A_ v46,v46,v76
A_ v47,v47,v80
Mavv 36,34,132
W_ 0
A_ v48,v48,v84
A_ v49,v49,v88
Mavv 56,38,132
s_nop 0
Tv 30,v2,13312
Tv 32,v2,19456
Mavv 76,42,132
A_ v50,v50,v92
Mavv 72,42,128
s_nop 0
Tv 34,v3,13312
Tv 36,v3,19456
Mavv 52,38,128
v_rcp_f32 v46, v46
Mavv 48,38,124
v_rcp_f32 v47, v47
Mavv 68,42,124
v_rcp_f32 v48, v48
Mavv 64,42,120
v_rcp_f32 v49, v49
Mavv 44,38,120
v_rcp_f32 v50, v50
Mavv 40,38,116
R_ v96,a0
R_ v97,a1
Mavv 60,42,116
W_ 0
R_ v98,a2
R_ v99,a3
Mavv 80,30,116
s_nop 0
Tv 38,v4,13312
Tv 40,v4,19456
Mavv 100,34,116
R_ v100,a20
R_ v101,a21
Mavv 104,34,120
s_nop 0
Tv 42,v5,13312
Tv 44,v5,19456
Mavv 84,30,120
R_ v102,a22
R_ v103,a23
Mavv 88,30,124
U_ v96,v96,v46
U_ v97,v97,v46
Mavv 108,34,124
U_ v98,v98,v46
U_ v99,v99,v46
Mavv 112,34,128
U_ v100,v100,v46
U_ v101,v101,v46
Mavv 92,30,128
U_ v102,v102,v46
U_ v103,v103,v46
Mavv 96,30,132
C_ 136,96,97
C_ 137,98,99
Mavv 116,34,132
W_ 0
C_ 138,100,101
C_ 139,102,103
Mavv 136,38,132
s_nop 0
Tv 30,v2,14336
Tv 32,v2,20480
Mavv 156,42,132
Da 0,v13,0
Mavv 152,42,128
s_nop 0
Tv 34,v3,14336
Tv 36,v3,20480
Mavv 132,38,128
R_ v104,a4
R_ v105,a5
Mavv 128,38,124
R_ v106,a6
R_ v107,a7
Mavv 148,42,124
R_ v108,a24
R_ v109,a25
Mavv 144,42,120
R_ v110,a26
R_ v111,a27
Mavv 124,38,120
U_ v104,v104,v47
U_ v105,v105,v47
Mavv 120,38,116
U_ v106,v106,v47
U_ v107,v107,v47
Mavv 140,42,116
W_ 0
U_ v108,v108,v47
U_ v109,v109,v47
Mavv 160,30,116
s_nop 0
Tv 38,v4,14336
Tv 40,v4,20480
Mavv 180,34,116
U_ v110,v110,v47
U_ v111,v111,v47
Mavv 184,34,120
s_nop 0
Tv 42,v5,14336
Tv 44,v5,20480
Mavv 164,30,120
C_ 140,104,105
C_ 141,106,107
Mavv 168,30,124
C_ 142,108,109
C_ 143,110,111
Mavv 188,34,124
Da 4,v13,1024
Mavv 192,34,128
R_ v96,a8
R_ v97,a9
Mavv 172,30,128
S_ s55,s71,0
R_ v98,a10
R_ v99,a11
Mavv 176,30,132
S_ m0,s51,12288
R_ v100,a28
R_ v101,a29
Mavv 196,34,132
s_waitcnt vmcnt(63) lgkmcnt(0)
s_barrier
R_ v102,a30
Mavv 216,38,132
L_ v7,24,s55,0
S_ s55,s71,10240
S_ m0,s51,13312
Mavv 236,42,132
R_ v103,a31
Mavv 232,42,128
L_ v7,24,s55,0
S_ s55,s71,20480
S_ m0,s51,14336
Mavv 212,38,128
U_ v96,v96,v48
U_ v97,v97,v48
Mavv 208,38,124
L_ v7,24,s55,0
S_ s55,s71,30720
S_ m0,s51,15360
Mavv 228,42,124
U_ v98,v98,v48
U_ v99,v99,v48
Mavv 224,42,120
L_ v7,24,s55,0
S_ s55,s71,40960
S_ m0,s51,16384
Mavv 204,38,120
U_ v100,v100,v48
U_ v101,v101,v48
Mavv 200,38,116
L_ v7,24,s55,0
S_ s55,s71,51200
S_ m0,s51,17408
Mavv 220,42,116
U_ v102,v102,v48
U_ v103,v103,v48
C_ 144,96,97
C_ 145,98,99
C_ 146,100,101
C_ 147,102,103
Da 8,v13,2048
L_ v7,24,s55,0
.p2align 3
R_ v104,a12
R_ v105,a13
R_ v106,a14
R_ v107,a15
R_ v108,a32
R_ v109,a33
R_ v110,a34
R_ v111,a35
v_pk_mul_f32 v[104:105], v[104:105], v[48:49] op_sel:[0,1]
v_pk_mul_f32 v[106:107], v[106:107], v[48:49] op_sel:[0,1]
v_pk_mul_f32 v[108:109], v[108:109], v[48:49] op_sel:[0,1]
v_pk_mul_f32 v[110:111], v[110:111], v[48:49] op_sel:[0,1]
C_ 148,104,105
C_ 149,106,107
C_ 150,108,109
C_ 151,110,111
Da 12,v13,3072
.p2align 3
R_ v96,a16
R_ v97,a17
R_ v98,a18
R_ v99,a19
R_ v100,a36
R_ v101,a37
R_ v102,a38
R_ v103,a39
v_pk_mul_f32 v[96:97], v[96:97], v[50:51] op_sel_hi:[1,0]
v_pk_mul_f32 v[98:99], v[98:99], v[50:51] op_sel_hi:[1,0]
v_pk_mul_f32 v[100:101], v[100:101], v[50:51] op_sel_hi:[1,0]
v_pk_mul_f32 v[102:103], v[102:103], v[50:51] op_sel_hi:[1,0]
C_ 152,96,97
C_ 153,98,99
C_ 154,100,101
C_ 155,102,103
Da 16,v13,4096
.p2align 3
R_ v104,a40
R_ v105,a41
R_ v106,a42
R_ v107,a43
R_ v108,a60
R_ v109,a61
R_ v110,a62
R_ v111,a63
v_pk_mul_f32 v[104:105], v[104:105], v[46:47] op_sel_hi:[1,0]
v_pk_mul_f32 v[106:107], v[106:107], v[46:47] op_sel_hi:[1,0]
v_pk_mul_f32 v[108:109], v[108:109], v[46:47] op_sel_hi:[1,0]
v_pk_mul_f32 v[110:111], v[110:111], v[46:47] op_sel_hi:[1,0]
C_ 156,104,105
C_ 157,106,107
C_ 158,108,109
C_ 159,110,111
Da 40,v13,5120
.p2align 3
R_ v96,a44
R_ v97,a45
R_ v98,a46
R_ v99,a47
R_ v100,a64
R_ v101,a65
R_ v102,a66
R_ v103,a67
v_pk_mul_f32 v[96:97], v[96:97], v[46:47] op_sel:[0,1]
v_pk_mul_f32 v[98:99], v[98:99], v[46:47] op_sel:[0,1]
v_pk_mul_f32 v[100:101], v[100:101], v[46:47] op_sel:[0,1]
v_pk_mul_f32 v[102:103], v[102:103], v[46:47] op_sel:[0,1]
C_ 160,96,97
C_ 161,98,99
C_ 162,100,101
C_ 163,102,103
Da 44,v13,6144
.p2align 3
R_ v104,a48
R_ v105,a49
R_ v106,a50
R_ v107,a51
R_ v108,a68
R_ v109,a69
R_ v110,a70
R_ v111,a71
v_pk_mul_f32 v[104:105], v[104:105], v[48:49] op_sel_hi:[1,0]
v_pk_mul_f32 v[106:107], v[106:107], v[48:49] op_sel_hi:[1,0]
v_pk_mul_f32 v[108:109], v[108:109], v[48:49] op_sel_hi:[1,0]
v_pk_mul_f32 v[110:111], v[110:111], v[48:49] op_sel_hi:[1,0]
C_ 164,104,105
C_ 165,106,107
C_ 166,108,109
C_ 167,110,111
Da 48,v13,7168
.p2align 3
R_ v96,a52
R_ v97,a53
R_ v98,a54
R_ v99,a55
R_ v100,a72
R_ v101,a73
R_ v102,a74
R_ v103,a75
v_pk_mul_f32 v[96:97], v[96:97], v[48:49] op_sel:[0,1]
v_pk_mul_f32 v[98:99], v[98:99], v[48:49] op_sel:[0,1]
v_pk_mul_f32 v[100:101], v[100:101], v[48:49] op_sel:[0,1]
v_pk_mul_f32 v[102:103], v[102:103], v[48:49] op_sel:[0,1]
C_ 168,96,97
C_ 169,98,99
C_ 170,100,101
C_ 171,102,103
Da 52,v13,8192
.p2align 3
R_ v104,a56
R_ v105,a57
R_ v106,a58
R_ v107,a59
R_ v108,a76
R_ v109,a77
R_ v110,a78
R_ v111,a79
v_pk_mul_f32 v[104:105], v[104:105], v[50:51] op_sel_hi:[1,0]
v_pk_mul_f32 v[106:107], v[106:107], v[50:51] op_sel_hi:[1,0]
v_pk_mul_f32 v[108:109], v[108:109], v[50:51] op_sel_hi:[1,0]
v_pk_mul_f32 v[110:111], v[110:111], v[50:51] op_sel_hi:[1,0]
C_ 172,104,105
C_ 173,106,107
C_ 174,108,109
C_ 175,110,111
Da 56,v13,9216
.p2align 3
R_ v96,a80
R_ v97,a81
R_ v98,a82
R_ v99,a83
R_ v100,a100
R_ v101,a101
R_ v102,a102
R_ v103,a103
v_pk_mul_f32 v[96:97], v[96:97], v[46:47] op_sel_hi:[1,0]
v_pk_mul_f32 v[98:99], v[98:99], v[46:47] op_sel_hi:[1,0]
v_pk_mul_f32 v[100:101], v[100:101], v[46:47] op_sel_hi:[1,0]
v_pk_mul_f32 v[102:103], v[102:103], v[46:47] op_sel_hi:[1,0]
C_ 176,96,97
C_ 177,98,99
C_ 178,100,101
C_ 179,102,103
X_ a80,v20
X_ a81,v21
X_ a82,v22
X_ a83,v23
.p2align 3
R_ v104,a84
R_ v105,a85
R_ v106,a86
R_ v107,a87
R_ v108,a104
R_ v109,a105
R_ v110,a106
R_ v111,a107
v_pk_mul_f32 v[104:105], v[104:105], v[46:47] op_sel:[0,1]
v_pk_mul_f32 v[106:107], v[106:107], v[46:47] op_sel:[0,1]
v_pk_mul_f32 v[108:109], v[108:109], v[46:47] op_sel:[0,1]
v_pk_mul_f32 v[110:111], v[110:111], v[46:47] op_sel:[0,1]
C_ 180,104,105
C_ 181,106,107
C_ 182,108,109
C_ 183,110,111
X_ a84,v24
X_ a85,v25
X_ a86,v26
X_ a87,v27
.p2align 3
R_ v96,a88
R_ v97,a89
R_ v98,a90
R_ v99,a91
R_ v100,a108
R_ v101,a109
R_ v102,a110
R_ v103,a111
v_pk_mul_f32 v[96:97], v[96:97], v[48:49] op_sel_hi:[1,0]
v_pk_mul_f32 v[98:99], v[98:99], v[48:49] op_sel_hi:[1,0]
v_pk_mul_f32 v[100:101], v[100:101], v[48:49] op_sel_hi:[1,0]
v_pk_mul_f32 v[102:103], v[102:103], v[48:49] op_sel_hi:[1,0]
C_ 184,96,97
C_ 185,98,99
C_ 186,100,101
C_ 187,102,103
X_ a88,v52
X_ a89,v53
X_ a90,v54
X_ a91,v55
.p2align 3
R_ v104,a92
R_ v105,a93
R_ v106,a94
R_ v107,a95
R_ v108,a112
R_ v109,a113
R_ v110,a114
R_ v111,a115
v_pk_mul_f32 v[104:105], v[104:105], v[48:49] op_sel:[0,1]
v_pk_mul_f32 v[106:107], v[106:107], v[48:49] op_sel:[0,1]
v_pk_mul_f32 v[108:109], v[108:109], v[48:49] op_sel:[0,1]
v_pk_mul_f32 v[110:111], v[110:111], v[48:49] op_sel:[0,1]
C_ 188,104,105
C_ 189,106,107
C_ 190,108,109
C_ 191,110,111
.p2align 3
R_ v96,a96
R_ v97,a97
R_ v98,a98
R_ v99,a99
R_ v100,a116
R_ v101,a117
R_ v102,a118
R_ v103,a119
v_pk_mul_f32 v[96:97], v[96:97], v[50:51] op_sel_hi:[1,0]
v_pk_mul_f32 v[98:99], v[98:99], v[50:51] op_sel_hi:[1,0]
v_pk_mul_f32 v[100:101], v[100:101], v[50:51] op_sel_hi:[1,0]
v_pk_mul_f32 v[102:103], v[102:103], v[50:51] op_sel_hi:[1,0]
C_ 192,96,97
C_ 193,98,99
C_ 194,100,101
C_ 195,102,103
.p2align 3
R_ v104,a120
R_ v105,a121
R_ v106,a122
R_ v107,a123
R_ v108,a140
R_ v109,a141
R_ v110,a142
R_ v111,a143
v_pk_mul_f32 v[104:105], v[104:105], v[46:47] op_sel_hi:[1,0]
v_pk_mul_f32 v[106:107], v[106:107], v[46:47] op_sel_hi:[1,0]
v_pk_mul_f32 v[108:109], v[108:109], v[46:47] op_sel_hi:[1,0]
v_pk_mul_f32 v[110:111], v[110:111], v[46:47] op_sel_hi:[1,0]
C_ 196,104,105
C_ 197,106,107
C_ 198,108,109
C_ 199,110,111
X_ a120,v16
X_ a121,v17
X_ a122,v18
X_ a123,v19
.p2align 3
R_ v96,a124
R_ v97,a125
R_ v98,a126
R_ v99,a127
R_ v100,a144
R_ v101,a145
R_ v102,a146
R_ v103,a147
v_pk_mul_f32 v[96:97], v[96:97], v[46:47] op_sel:[0,1]
v_pk_mul_f32 v[98:99], v[98:99], v[46:47] op_sel:[0,1]
v_pk_mul_f32 v[100:101], v[100:101], v[46:47] op_sel:[0,1]
v_pk_mul_f32 v[102:103], v[102:103], v[46:47] op_sel:[0,1]
C_ 200,96,97
C_ 201,98,99
C_ 202,100,101
C_ 203,102,103
X_ a124,v28
X_ a125,v29
X_ a126,v51
X_ a127,v6
.p2align 3
R_ v104,a128
R_ v105,a129
R_ v106,a130
R_ v107,a131
R_ v108,a148
R_ v109,a149
R_ v110,a150
R_ v111,a151
v_pk_mul_f32 v[104:105], v[104:105], v[48:49] op_sel_hi:[1,0]
v_pk_mul_f32 v[106:107], v[106:107], v[48:49] op_sel_hi:[1,0]
v_pk_mul_f32 v[108:109], v[108:109], v[48:49] op_sel_hi:[1,0]
v_pk_mul_f32 v[110:111], v[110:111], v[48:49] op_sel_hi:[1,0]
C_ 204,104,105
C_ 205,106,107
C_ 206,108,109
C_ 207,110,111
X_ a128,v14
X_ a129,v15
v_accvgpr_mov_b32 a130, a249
v_accvgpr_mov_b32 a131, a250
.p2align 3
R_ v96,a132
R_ v97,a133
R_ v98,a134
R_ v99,a135
R_ v100,a152
R_ v101,a153
R_ v102,a154
R_ v103,a155
v_pk_mul_f32 v[96:97], v[96:97], v[48:49] op_sel:[0,1]
v_pk_mul_f32 v[98:99], v[98:99], v[48:49] op_sel:[0,1]
v_pk_mul_f32 v[100:101], v[100:101], v[48:49] op_sel:[0,1]
v_pk_mul_f32 v[102:103], v[102:103], v[48:49] op_sel:[0,1]
C_ 208,96,97
C_ 209,98,99
C_ 210,100,101
C_ 211,102,103
Da 132,v12,0
.p2align 3
R_ v104,a136
R_ v105,a137
R_ v106,a138
R_ v107,a139
R_ v108,a156
R_ v109,a157
R_ v110,a158
R_ v111,a159
v_pk_mul_f32 v[104:105], v[104:105], v[50:51] op_sel_hi:[1,0]
v_pk_mul_f32 v[106:107], v[106:107], v[50:51] op_sel_hi:[1,0]
v_pk_mul_f32 v[108:109], v[108:109], v[50:51] op_sel_hi:[1,0]
v_pk_mul_f32 v[110:111], v[110:111], v[50:51] op_sel_hi:[1,0]
C_ 212,104,105
C_ 213,106,107
C_ 214,108,109
C_ 215,110,111
Da 136,v12,1024
.p2align 3
R_ v96,a160
R_ v97,a161
R_ v98,a162
R_ v99,a163
R_ v100,a180
R_ v101,a181
R_ v102,a182
R_ v103,a183
v_pk_mul_f32 v[96:97], v[96:97], v[46:47] op_sel_hi:[1,0]
v_pk_mul_f32 v[98:99], v[98:99], v[46:47] op_sel_hi:[1,0]
v_pk_mul_f32 v[100:101], v[100:101], v[46:47] op_sel_hi:[1,0]
v_pk_mul_f32 v[102:103], v[102:103], v[46:47] op_sel_hi:[1,0]
C_ 216,96,97
C_ 217,98,99
C_ 218,100,101
C_ 219,102,103
.Lpad_o_0:
s_cbranch_execz .Lpad_o_4
Da 160,v12,2048
.p2align 3
R_ v104,a164
R_ v105,a165
R_ v106,a166
R_ v107,a167
R_ v108,a184
R_ v109,a185
R_ v110,a186
R_ v111,a187
v_pk_mul_f32 v[104:105], v[104:105], v[46:47] op_sel:[0,1]
v_pk_mul_f32 v[106:107], v[106:107], v[46:47] op_sel:[0,1]
v_pk_mul_f32 v[108:109], v[108:109], v[46:47] op_sel:[0,1]
v_pk_mul_f32 v[110:111], v[110:111], v[46:47] op_sel:[0,1]
C_ 220,104,105
C_ 221,106,107
C_ 222,108,109
C_ 223,110,111
Da 164,v12,3072
.p2align 3
R_ v96,a168
R_ v97,a169
R_ v98,a170
R_ v99,a171
R_ v100,a188
R_ v101,a189
R_ v102,a190
R_ v103,a191
v_pk_mul_f32 v[96:97], v[96:97], v[48:49] op_sel_hi:[1,0]
v_pk_mul_f32 v[98:99], v[98:99], v[48:49] op_sel_hi:[1,0]
v_pk_mul_f32 v[100:101], v[100:101], v[48:49] op_sel_hi:[1,0]
v_pk_mul_f32 v[102:103], v[102:103], v[48:49] op_sel_hi:[1,0]
C_ 224,96,97
C_ 225,98,99
C_ 226,100,101
C_ 227,102,103
Da 168,v12,4096
.p2align 3
R_ v104,a172
R_ v105,a173
R_ v106,a174
R_ v107,a175
R_ v108,a192
R_ v109,a193
R_ v110,a194
R_ v111,a195
v_pk_mul_f32 v[104:105], v[104:105], v[48:49] op_sel:[0,1]
v_pk_mul_f32 v[106:107], v[106:107], v[48:49] op_sel:[0,1]
v_pk_mul_f32 v[108:109], v[108:109], v[48:49] op_sel:[0,1]
v_pk_mul_f32 v[110:111], v[110:111], v[48:49] op_sel:[0,1]
C_ 228,104,105
C_ 229,106,107
C_ 230,108,109
C_ 231,110,111
Da 172,v12,5120
.p2align 3
R_ v96,a176
R_ v97,a177
R_ v98,a178
R_ v99,a179
R_ v100,a196
R_ v101,a197
R_ v102,a198
R_ v103,a199
v_pk_mul_f32 v[96:97], v[96:97], v[50:51] op_sel_hi:[1,0]
v_pk_mul_f32 v[98:99], v[98:99], v[50:51] op_sel_hi:[1,0]
v_pk_mul_f32 v[100:101], v[100:101], v[50:51] op_sel_hi:[1,0]
v_pk_mul_f32 v[102:103], v[102:103], v[50:51] op_sel_hi:[1,0]
C_ 232,96,97
C_ 233,98,99
C_ 234,100,101
C_ 235,102,103
Da 176,v12,12288
.p2align 3
R_ v104,a200
R_ v105,a201
R_ v106,a202
R_ v107,a203
R_ v108,a220
R_ v109,a221
R_ v110,a222
R_ v111,a223
v_pk_mul_f32 v[104:105], v[104:105], v[46:47] op_sel_hi:[1,0]
v_pk_mul_f32 v[106:107], v[106:107], v[46:47] op_sel_hi:[1,0]
v_pk_mul_f32 v[108:109], v[108:109], v[46:47] op_sel_hi:[1,0]
v_pk_mul_f32 v[110:111], v[110:111], v[46:47] op_sel_hi:[1,0]
C_ 236,104,105
C_ 237,106,107
C_ 238,108,109
C_ 239,110,111
Da 200,v12,13312
.p2align 3
R_ v96,a204
R_ v97,a205
R_ v98,a206
R_ v99,a207
R_ v100,a224
R_ v101,a225
R_ v102,a226
R_ v103,a227
v_pk_mul_f32 v[96:97], v[96:97], v[46:47] op_sel:[0,1]
v_pk_mul_f32 v[98:99], v[98:99], v[46:47] op_sel:[0,1]
v_pk_mul_f32 v[100:101], v[100:101], v[46:47] op_sel:[0,1]
v_pk_mul_f32 v[102:103], v[102:103], v[46:47] op_sel:[0,1]
C_ 240,96,97
C_ 241,98,99
C_ 242,100,101
C_ 243,102,103
Da 204,v12,14336
v_mov_b32 v6, v10
v_lshrrev_b32 v10, 9, v6
v_lshlrev_b32 v10, 11, v10
v_sub_u32 %0, v2, v10
v_xor_b32 %0, s83, %0
s_waitcnt vmcnt(15) lgkmcnt(0)
s_barrier
Ta 180,%0,24576
Ta 182,%0,26624
Ta 184,%0,28672
Ta 186,%0,30720
Ta 188,%0,32768
Ta 190,%0,34816
.p2align 3
R_ v104,a208
R_ v105,a209
R_ v106,a210
R_ v107,a211
R_ v108,a228
R_ v109,a229
R_ v110,a230
R_ v111,a231
v_pk_mul_f32 v[104:105], v[104:105], v[48:49] op_sel_hi:[1,0]
v_pk_mul_f32 v[106:107], v[106:107], v[48:49] op_sel_hi:[1,0]
v_pk_mul_f32 v[108:109], v[108:109], v[48:49] op_sel_hi:[1,0]
v_pk_mul_f32 v[110:111], v[110:111], v[48:49] op_sel_hi:[1,0]
C_ 244,104,105
C_ 245,106,107
C_ 246,108,109
C_ 247,110,111
Da 208,v12,15360
.p2align 3
R_ v96,a212
R_ v97,a213
R_ v98,a214
R_ v99,a215
R_ v100,a232
R_ v101,a233
R_ v102,a234
R_ v103,a235
v_pk_mul_f32 v[96:97], v[96:97], v[48:49] op_sel:[0,1]
v_pk_mul_f32 v[98:99], v[98:99], v[48:49] op_sel:[0,1]
v_pk_mul_f32 v[100:101], v[100:101], v[48:49] op_sel:[0,1]
v_pk_mul_f32 v[102:103], v[102:103], v[48:49] op_sel:[0,1]
C_ 248,96,97
C_ 249,98,99
C_ 250,100,101
C_ 251,102,103
Da 212,v12,16384
.p2align 3
R_ v104,a216
R_ v105,a217
R_ v106,a218
R_ v107,a219
R_ v108,a236
R_ v109,a237
R_ v110,a238
R_ v111,a239
v_pk_mul_f32 v[104:105], v[104:105], v[50:51] op_sel_hi:[1,0]
v_pk_mul_f32 v[106:107], v[106:107], v[50:51] op_sel_hi:[1,0]
v_pk_mul_f32 v[108:109], v[108:109], v[50:51] op_sel_hi:[1,0]
v_pk_mul_f32 v[110:111], v[110:111], v[50:51] op_sel_hi:[1,0]
C_ 252,104,105
C_ 253,106,107
C_ 254,108,109
C_ 255,110,111
Da 216,v12,17408
v_xor_b32 v1, 32, %0
W_ 7
Zvav 56,180,136
s_mul_i32 s76, s6, 204800
Ta 192,v1,24576
Ta 194,v1,26624
W_ 7
Mvav 56,184,156
v_xor_b32 v2, 64, %0
v_xor_b32 v3, 96, %0
W_ 5
Mvav 56,188,176
s_nop 0
Ta 196,v1,28672
Ta 198,v1,30720
Zvav 60,180,140
W_ 4
R_ v4,a248
v_mul_u32_u24 v4, 80, v4
Mvav 60,184,160
S_ s55,s71,122880
Ta 220,v1,32768
Ta 222,v1,34816
Mvav 60,188,180
S_ m0,s51,49152
v_min_u32 v4, 29440, v4
s_waitcnt vmcnt(6)
Zvav 64,180,144
v_readfirstlane_b32 s62, v11
v_lshrrev_b32 v8, 2, v6
Mvav 64,184,164
v_add_u32 v5, s75, v8
R_ v9,a251
Mvav 64,188,184
v_or_b32 v9, s73, v9
v_lshrrev_b32 v10, 8, v6
Zvav 68,180,148
v_bfe_u32 v12, v6, 6, 2
v_cmp_eq_u32 vcc, v10, v12
Mvav 68,184,168
v_and_b32 v10, 16, v6
v_mov_b32 v12, 0x3f80
Mvav 68,188,188
v_lshlrev_b32 v12, v10, v12
v_cndmask_b32 v12, 0, v12, vcc
Zvav 72,180,152
v_and_b32 v10, 32, v6
v_cmp_eq_u32 vcc, 0, v10
Mvav 72,184,172
X_ a68,0
X_ a69,0
Mvav 72,188,192
W_ 0
X_ a72,0
X_ a73,0
Zvav 92,220,192
s_nop 0
Ta 180,v2,24576
Ta 182,v2,26624
Mvav 92,196,172
v_cndmask_b32 v10, 0, v12, vcc
v_sub_u32 v12, v12, v10
Mvav 92,192,152
s_nop 0
Ta 184,v2,28672
Ta 186,v2,30720
Zvav 88,220,188
X_ a70,v10
X_ a71,v12
Mvav 88,196,168
s_mov_b64 s[32:33], s[20:21]
Ta 188,v2,32768
Ta 190,v2,34816
Mvav 88,192,148
s_mov_b32 s34, 409600
S_ s85,s6,1
Zvav 84,220,184
L_ v7,24,s55,0
Mvav 84,196,164
S_ s55,s71,133120
s_and_b32 s85, s85, 7
Mvav 84,192,144
S_ m0,s51,50176
s_lshl_b32 s85, s85, 7
Zvav 80,220,180
S_ s85,s85,s87
Mvav 80,196,160
L_ v7,24,s55,0
Mvav 80,192,140
S_ s55,s71,143360
Zvav 76,220,176
S_ m0,s51,51200
Mvav 76,196,156
Mvav 76,192,136
W_ 0
Zvav 16,180,136
s_nop 0
Ta 192,v3,24576
Ta 194,v3,26624
Mvav 16,184,156
L_ v7,24,s55,0
Mvav 16,188,176
S_ s55,s71,153600
Ta 196,v3,28672
Ta 198,v3,30720
Zvav 20,180,140
S_ m0,s51,52224
Mvav 20,184,160
s_nop 0
Ta 220,v3,32768
Ta 222,v3,34816
Mvav 20,188,180
L_ v7,24,s55,0
Zvav 24,180,144
S_ s55,s71,163840
Mvav 24,184,164
S_ m0,s51,53248
Mvav 24,188,184
Zvav 28,180,148
L_ v7,24,s55,0
Mvav 28,184,168
S_ s55,s71,174080
Mvav 28,188,188
S_ m0,s51,54272
Zvav 32,180,152
Mvav 32,184,172
L_ v7,24,s55,0
Mvav 32,188,192
s_waitcnt vmcnt(13) lgkmcnt(0)
s_barrier
S_ s71,s72,1152
Zvav 52,220,192
S_ s55,s71,491520
Ta 180,%0,0
Ta 182,%0,2048
Mvav 52,196,172
S_ m0,s51,36864
Mvav 52,192,152
s_nop 0
Ta 184,%0,4096
Ta 186,%0,6144
Zvav 48,220,188
L_ v7,24,s55,0
Mvav 48,196,168
S_ s55,s71,501760
Ta 188,%0,8192
Ta 190,%0,10240
Mvav 48,192,148
S_ m0,s51,37888
Zvav 44,220,184
Mvav 44,196,164
L_ v7,24,s55,0
Mvav 44,192,144
S_ s55,s71,512000
Zvav 40,220,180
S_ m0,s51,38912
Mvav 40,196,160
Mvav 40,192,140
L_ v7,24,s55,0
Zvav 36,220,176
S_ s55,s71,522240
Mvav 36,196,156
S_ m0,s51,39936
Mvav 36,192,136
W_ 0
Mvav 56,180,196
s_nop 0
Ta 192,v1,0
Ta 194,v1,2048
Mvav 56,184,216
L_ v7,24,s55,0
Mvav 56,188,236
S_ s55,s71,532480
Ta 196,v1,4096
Ta 198,v1,6144
Mvav 60,180,200
S_ m0,s51,40960
Mvav 60,184,220
s_nop 0
Ta 220,v1,8192
Ta 222,v1,10240
Mvav 60,188,240
L_ v7,24,s55,0
Mvav 64,180,204
S_ s55,s71,542720
Mvav 64,184,224
S_ m0,s51,41984
Mvav 64,188,244
Mvav 68,180,208
L_ v7,24,s55,0
Mvav 68,184,228
S_ s55,s71,614400
Mvav 68,188,248
S_ m0,s51,24576
Mvav 72,180,212
Mvav 72,184,232
L_ v7,24,s55,0
Mvav 72,188,252
W_ 0
S_ s55,s71,624640
Mvav 92,220,252
S_ m0,s51,25600
Ta 180,v2,0
Ta 182,v2,2048
Mvav 92,196,232
Mvav 92,192,212
s_nop 0
Ta 184,v2,4096
Ta 186,v2,6144
Mvav 88,220,248
L_ v7,24,s55,0
Mvav 88,196,228
S_ s55,s71,634880
Ta 188,v2,8192
Ta 190,v2,10240
Mvav 88,192,208
S_ m0,s51,26624
Mvav 84,220,244
Mvav 84,196,224
L_ v7,24,s55,0
Mvav 84,192,204
S_ s55,s71,645120
Mvav 80,220,240
S_ m0,s51,27648
Mvav 80,196,220
Mvav 80,192,200
L_ v7,24,s55,0
Mvav 76,220,236
S_ s55,s71,655360
Mvav 76,196,216
S_ m0,s51,28672
Mvav 76,192,196
W_ 0
Mvav 16,180,196
s_nop 0
Ta 192,v3,0
Ta 194,v3,2048
Mvav 16,184,216
L_ v7,24,s55,0
Mvav 16,188,236
S_ s55,s71,665600
Ta 196,v3,4096
Ta 198,v3,6144
Mvav 20,180,200
S_ m0,s51,29696
Mvav 20,184,220
s_nop 0
Ta 220,v3,8192
Ta 222,v3,10240
Mvav 20,188,240
L_ v7,24,s55,0
Mvav 24,180,204
Mvav 24,184,224
Mvav 24,188,244
Mvav 28,180,208
buffer_load_dword a74, v4, s[24:27], s85 offen
Mvav 28,184,228
S_ s85,s6,2
Mvav 28,188,248
s_and_b32 s85, s85, 7
Mvav 32,180,212
s_lshl_b32 s85, s85, 7
Mvav 32,184,232
S_ s85,s85,s87
Mvav 32,188,252
s_waitcnt vmcnt(19) lgkmcnt(0)
s_barrier
Mvav 52,220,252
S_ s55,s71,0
Ta 180,%0,12288
Ta 182,%0,14336
Mvav 52,196,232
buffer_load_dword a74, v4, s[24:27], s85 offen
Mvav 52,192,212
S_ m0,s51,0
Ta 184,%0,16384
Ta 186,%0,18432
Mvav 48,220,248
Mvav 48,196,228
s_nop 0
Ta 188,%0,20480
Ta 190,%0,22528
Mvav 48,192,208
L_ v7,24,s55,0
Mvav 44,220,244
S_ s55,s71,10240
Mvav 44,196,224
S_ m0,s51,1024
Mvav 44,192,204
Mvav 40,220,240
L_ v7,24,s55,0
Mvav 40,196,220
S_ s55,s71,20480
Mvav 40,192,200
S_ m0,s51,2048
Mvav 36,220,236
Mvav 36,196,216
L_ v7,24,s55,0
Mvav 36,192,196
W_ 0
S_ s55,s71,30720
Mvaa 56,180,0
S_ m0,s51,3072
Ta 192,v1,12288
Ta 194,v1,14336
Mvaa 56,184,40
Mvaa 56,188,80
s_nop 0
Ta 196,v1,16384
Ta 198,v1,18432
Mvaa 60,180,4
L_ v7,24,s55,0
Mvaa 60,184,44
S_ s55,s71,40960
Ta 220,v1,20480
Ta 222,v1,22528
Mvaa 60,188,84
S_ m0,s51,4096
Mvaa 64,180,8
Mvaa 64,184,48
L_ v7,24,s55,0
Mvaa 64,188,88
S_ s55,s71,51200
Mvaa 68,180,12
S_ m0,s51,5120
Mvaa 68,184,52
Mvaa 68,188,240
L_ v7,24,s55,0
Mvaa 72,180,16
Mvaa 72,184,56
Mvaa 72,188,244
W_ 0
Mvaa 92,220,244
s_nop 0
Ta 180,v2,12288
Ta 182,v2,14336
Mvaa 92,196,56
Mvaa 92,192,16
s_nop 0
Ta 184,v2,16384
Ta 186,v2,18432
Mvaa 88,220,240
Mvaa 88,196,52
s_nop 0
Ta 188,v2,20480
Ta 190,v2,22528
Mvaa 88,192,12
Mvaa 84,220,88
Mvaa 84,196,48
Mvaa 84,192,8
Mvaa 80,220,84
Mvaa 80,196,44
Mvaa 80,192,4
Mvaa 76,220,80
Mvaa 76,196,40
Mvaa 76,192,0
W_ 0
Mvaa 16,180,0
s_nop 0
Ta 192,v3,12288
Ta 194,v3,14336
Mvaa 16,184,40
Mvaa 16,188,80
s_nop 0
Ta 196,v3,16384
Ta 198,v3,18432
Mvaa 20,180,4
Mvaa 20,184,44
s_nop 0
Ta 220,v3,20480
Ta 222,v3,22528
Mvaa 20,188,84
.Lpad_o_1:
s_cbranch_execz .Lpad_o_5
Mvaa 24,180,8
Mvaa 24,184,48
Mvaa 24,188,88
Mvaa 28,180,12
Mvaa 28,184,52
Mvaa 28,188,240
Mvaa 32,180,16
Mvaa 32,184,56
Mvaa 32,188,244
s_waitcnt vmcnt(20) lgkmcnt(0)
s_barrier
Mvaa 52,220,244
s_nop 0
Ta 180,%0,49152
Ta 182,%0,51200
Mvaa 52,196,56
Mvaa 52,192,16
s_nop 0
Ta 184,%0,53248
Ta 186,%0,55296
Mvaa 48,220,240
Mvaa 48,196,52
s_nop 0
Ta 188,%0,57344
Ta 190,%0,59392
Mvaa 48,192,12
Mvaa 44,220,88
Mvaa 44,196,48
Mvaa 44,192,8
Mvaa 40,220,84
Mvaa 40,196,44
Mvaa 40,192,4
Mvaa 36,220,80
Mvaa 36,196,40
Mvaa 36,192,0
W_ 0
Mvaa 56,180,120
S_ s73,s78,163840
Ta 192,v1,49152
Ta 194,v1,51200
Mvaa 56,184,160
S_ s73,s73,4096
Mvaa 56,188,200
s_nop 0
Ta 196,v1,53248
Ta 198,v1,55296
Mvaa 60,180,124
s_mov_b32 m0, s52
Mvaa 60,184,164
s_nop 0
Ta 220,v1,57344
Ta 222,v1,59392
Mvaa 60,188,204
s_nop 0
Y_ 56,0
Y_ 57,256
Mvaa 64,180,128
s_nop 0
Y_ 58,512
Y_ 59,768
Mvaa 64,184,168
Mvaa 64,188,208
s_nop 0
Y_ 60,1024
Y_ 61,1280
Mvaa 68,180,132
s_nop 0
Y_ 62,1536
Y_ 63,1792
Mvaa 68,184,172
Mvaa 68,188,212
s_nop 0
Y_ 64,2048
Y_ 65,2304
Mvaa 72,180,136
s_nop 0
Y_ 66,2560
Y_ 67,2816
Mvaa 72,184,176
Mvaa 72,188,216
W_ 12
Y_ 68,3072
Y_ 69,3328
Mvaa 92,220,216
s_nop 0
Ta 180,v2,49152
Ta 182,v2,51200
Mvaa 92,196,176
s_nop 0
Y_ 70,3584
Y_ 71,3840
Mvaa 92,192,136
s_nop 0
Ta 184,v2,53248
Ta 186,v2,55296
Mvaa 88,220,212
s_nop 0
Y_ 72,4096
Y_ 73,4352
Mvaa 88,196,172
s_nop 0
Ta 188,v2,57344
Ta 190,v2,59392
Mvaa 88,192,132
s_nop 0
Y_ 74,4608
Y_ 75,4864
Mvaa 84,220,208
s_nop 0
Y_ 92,9216
Y_ 93,9472
Mvaa 84,196,168
s_nop 0
Y_ 94,9728
Y_ 95,9984
Mvaa 84,192,128
s_nop 0
Y_ 88,8192
Y_ 89,8448
Mvaa 80,220,204
s_nop 0
Y_ 90,8704
Y_ 91,8960
Mvaa 80,196,164
Mvaa 80,192,124
s_nop 0
Y_ 84,7168
Y_ 85,7424
Mvaa 76,220,200
s_nop 0
Y_ 86,7680
Y_ 87,7936
Mvaa 76,196,160
Mvaa 76,192,120
W_ 14
Y_ 80,6144
Y_ 81,6400
Mvaa 16,180,120
s_nop 0
Ta 192,v3,49152
Ta 194,v3,51200
Mvaa 16,184,160
s_nop 0
Y_ 82,6656
Y_ 83,6912
Mvaa 16,188,200
s_nop 0
Ta 196,v3,53248
Ta 198,v3,55296
Mvaa 20,180,124
s_nop 0
Y_ 76,5120
Y_ 77,5376
Mvaa 20,184,164
s_nop 0
Ta 220,v3,57344
Ta 222,v3,59392
Mvaa 20,188,204
s_nop 0
Y_ 78,5632
Y_ 79,5888
Mvaa 24,180,128
Mvaa 24,184,168
Mvaa 24,188,208
s_waitcnt vmcnt(14) lgkmcnt(0)
s_barrier
Mvaa 28,180,132
S_ s55,s71,122880
ds_read2st64_b32 v[96:97], v5 offset0:0 offset1:1
Mvaa 28,184,172
S_ m0,s51,12288
ds_read2st64_b32 v[98:99], v5 offset0:2 offset1:3
Mvaa 28,188,212
L_ v7,24,s55,0
Mvaa 32,180,136
S_ s55,s71,133120
ds_read2st64_b32 v[104:105], v5 offset0:4 offset1:5
Mvaa 32,184,176
S_ m0,s51,13312
ds_read2st64_b32 v[106:107], v5 offset0:6 offset1:7
Mvaa 32,188,216
ds_read2st64_b32 v[112:113], v5 offset0:8 offset1:9
Mvaa 52,220,216
s_nop 0
Ta 180,%0,36864
Ta 182,%0,38912
Mvaa 52,196,176
L_ v7,24,s55,0
Mvaa 52,192,136
W_ 5
Ta 184,%0,40960
Ta 186,%0,43008
Mvaa 48,220,212
S_ s55,s71,143360
ds_read2st64_b32 v[114:115], v5 offset0:10 offset1:11
A_ v96,v16,v96
Mvaa 48,196,172
S_ m0,s51,14336
Ta 188,%0,45056
Ta 190,%0,47104
Mvaa 48,192,132
L_ v7,24,s55,0
Mvaa 44,220,208
S_ s55,s71,153600
ds_read2st64_b32 v[120:121], v5 offset0:12 offset1:13
A_ v97,v17,v97
Mvaa 44,196,168
S_ m0,s51,15360
ds_read2st64_b32 v[122:123], v5 offset0:14 offset1:15
A_ v98,v18,v98
Mvaa 44,192,128
ds_read2st64_b32 v[128:129], v5 offset0:16 offset1:17
A_ v99,v19,v99
Mvaa 40,220,204
L_ v7,24,s55,0
Mvaa 40,196,164
W_ 11
ds_read2st64_b32 v[130:131], v5 offset0:18 offset1:19
A_ v104,v20,v104
Mvaa 40,192,124
S_ s55,s71,163840
ds_read2st64_b32 v[132:133], v5 offset0:36 offset1:37
A_ v105,v21,v105
Mvaa 36,220,200
S_ m0,s51,16384
ds_read2st64_b32 v[134:135], v5 offset0:38 offset1:39
A_ v106,v22,v106
Mvaa 36,196,160
L_ v7,24,s55,0
Mvaa 36,192,120
W_ 6
ds_read2st64_b32 v[124:125], v5 offset0:32 offset1:33
A_ v107,v23,v107
Zvav 56,180,136
Ta 192,v1,36864
Ta 194,v1,38912
Mvav 56,184,156
S_ s55,s71,174080
ds_read2st64_b32 v[126:127], v5 offset0:34 offset1:35
A_ v112,v24,v112
Mvav 56,188,176
S_ m0,s51,17408
Ta 196,v1,40960
Ta 198,v1,43008
Zvav 60,180,140
L_ v7,24,s55,0
Mvav 60,184,160
S_ s79,s6,1
Ta 220,v1,45056
Ta 222,v1,47104
Mvav 60,188,180
s_and_b32 s79, s79, 7
ds_read2st64_b32 v[116:117], v5 offset0:28 offset1:29
A_ v113,v25,v113
Zvav 64,180,144
s_lshl_b32 s79, s79, 7
ds_read2st64_b32 v[118:119], v5 offset0:30 offset1:31
A_ v114,v26,v114
Mvav 64,184,164
S_ s71,s72,s79
ds_read2st64_b32 v[108:109], v5 offset0:24 offset1:25
A_ v115,v27,v115
Mvav 64,188,184
W_ 14
ds_read2st64_b32 v[110:111], v5 offset0:26 offset1:27
A_ v120,v28,v120
Zvav 68,180,148
S_ s55,s71,491520
ds_read2st64_b32 v[100:101], v5 offset0:20 offset1:21
A_ v121,v29,v121
Mvav 68,184,168
S_ m0,s51,49152
ds_read2st64_b32 v[102:103], v5 offset0:22 offset1:23
A_ v122,v30,v122
Mvav 68,188,188
L_ v7,24,s55,0
Zvav 72,180,152
S_ s55,s71,501760
A_ v123,v31,v123
Mvav 72,184,172
W_ 14
A_ v128,v32,v128
A_ v129,v33,v129
Mvav 72,188,192
W_ 6
A_ v130,v34,v130
A_ v131,v35,v131
Zvav 92,220,192
Ta 180,v2,36864
Ta 182,v2,38912
Mvav 92,196,172
S_ m0,s51,50176
A_ v132,v52,v132
A_ v133,v53,v133
Mvav 92,192,152
s_nop 0
Ta 184,v2,40960
Ta 186,v2,43008
Zvav 88,220,188
L_ v7,24,s55,0
Mvav 88,196,168
S_ s55,s71,512000
Ta 188,v2,45056
Ta 190,v2,47104
Mvav 88,192,148
S_ m0,s51,51200
A_ v134,v54,v134
A_ v135,v55,v135
Zvav 84,220,184
A_ v124,v48,v124
A_ v125,v49,v125
Mvav 84,196,164
L_ v7,24,s55,0
Mvav 84,192,144
S_ s55,s71,522240
A_ v126,v50,v126
A_ v127,v51,v127
Zvav 80,220,180
W_ 10
A_ v116,v44,v116
A_ v117,v45,v117
Mvav 80,196,160
S_ m0,s51,52224
A_ v118,v46,v118
A_ v119,v47,v119
Mvav 80,192,140
L_ v7,24,s55,0
Zvav 76,220,176
W_ 8
A_ v108,v40,v108
A_ v109,v41,v109
Mvav 76,196,156
S_ s55,s71,532480
A_ v110,v42,v110
A_ v111,v43,v111
Mvav 76,192,136
W_ 0
A_ v100,v36,v100
Zvav 16,180,136
S_ m0,s51,53248
Ta 192,v3,36864
Ta 194,v3,38912
Mvav 16,184,156
L_ v7,24,s55,0
Mvav 16,188,176
S_ s55,s71,542720
Ta 196,v3,40960
Ta 198,v3,43008
Zvav 20,180,140
S_ m0,s51,54272
A_ v101,v37,v101
A_ v102,v38,v102
Mvav 20,184,160
S_ s85,s6,3
Ta 220,v3,45056
Ta 222,v3,47104
Mvav 20,188,180
L_ v7,24,s55,0
Zvav 24,180,144
s_and_b32 s85, s85, 7
A_ v103,v39,v103
C_ 131,130,131
Mvav 24,184,164
s_lshl_b32 s85, s85, 7
C_ 130,128,129
C_ 132,132,133
Mvav 24,188,184
S_ s85,s85,s87
C_ 133,134,135
Zvav 28,180,148
buffer_store_dwordx4 v[130:133], v6, s[28:31], s73 offen offset:0 sc1
Mvav 28,184,168
s_sub_u32 s73, s73, 4096
Mvav 28,188,188
C_ 123,122,123
C_ 122,120,121
Zvav 32,180,152
C_ 124,124,125
C_ 125,126,127
Mvav 32,184,172
Mvav 32,188,192
s_waitcnt vmcnt(21) lgkmcnt(0)
s_barrier
Zvav 52,220,192
S_ s55,s71,614400
Ta 180,%0,24576
Ta 182,%0,26624
Mvav 52,196,172
S_ m0,s51,36864
Mvav 52,192,152
s_nop 0
Ta 184,%0,28672
Ta 186,%0,30720
Zvav 48,220,188
buffer_store_dwordx4 v[122:125], v6, s[28:31], s73 offen offset:3072 sc1
Mvav 48,196,168
s_nop 0
Ta 188,%0,32768
Ta 190,%0,34816
Mvav 48,192,148
C_ 115,114,115
C_ 114,112,113
Zvav 44,220,184
C_ 116,116,117
C_ 117,118,119
Mvav 44,196,164
Mvav 44,192,144
Zvav 40,220,180
Mvav 40,196,160
Mvav 40,192,140
buffer_store_dwordx4 v[114:117], v6, s[28:31], s73 offen offset:2048 sc1
Zvav 36,220,176
C_ 107,106,107
C_ 106,104,105
Mvav 36,196,156
C_ 108,108,109
C_ 109,110,111
Mvav 36,192,136
W_ 0
Mvav 56,180,196
s_nop 0
Ta 192,v1,24576
Ta 194,v1,26624
Mvav 56,184,216
Mvav 56,188,236
s_nop 0
Ta 196,v1,28672
Ta 198,v1,30720
Mvav 60,180,200
Mvav 60,184,220
s_nop 0
Ta 220,v1,32768
Ta 222,v1,34816
Mvav 60,188,240
buffer_store_dwordx4 v[106:109], v6, s[28:31], s73 offen offset:1024 sc1
Mvav 64,180,204
C_ 99,98,99
C_ 98,96,97
Mvav 64,184,224
C_ 100,100,101
C_ 101,102,103
Mvav 64,188,244
Mvav 68,180,208
Mvav 68,184,228
Mvav 68,188,248
Mvav 72,180,212
Mvav 72,184,232
buffer_store_dwordx4 v[98:101], v6, s[28:31], s73 offen offset:0 sc1
Mvav 72,188,252
W_ 0
Mvav 92,220,252
s_nop 0
Ta 180,v2,24576
Ta 182,v2,26624
Mvav 92,196,232
Mvav 92,192,212
s_nop 0
Ta 184,v2,28672
Ta 186,v2,30720
Mvav 88,220,248
Mvav 88,196,228
s_nop 0
Ta 188,v2,32768
Ta 190,v2,34816
Mvav 88,192,208
Mvav 84,220,244
L_ v7,24,s55,0
Mvav 84,196,224
S_ s55,s71,624640
Mvav 84,192,204
S_ m0,s51,37888
Mvav 80,220,240
Mvav 80,196,220
L_ v7,24,s55,0
Mvav 80,192,200
S_ s55,s71,634880
Mvav 76,220,236
S_ m0,s51,38912
Mvav 76,196,216
Mvav 76,192,196
W_ 0
Mvav 16,180,196
s_nop 0
Ta 192,v3,24576
Ta 194,v3,26624
Mvav 16,184,216
L_ v7,24,s55,0
Mvav 16,188,236
S_ s55,s71,645120
Ta 196,v3,28672
Ta 198,v3,30720
Mvav 20,180,200
S_ m0,s51,39936
Mvav 20,184,220
s_nop 0
Ta 220,v3,32768
Ta 222,v3,34816
Mvav 20,188,240
L_ v7,24,s55,0
Mvav 24,180,204
S_ s55,s71,655360
Mvav 24,184,224
S_ m0,s51,40960
Mvav 24,188,244
Mvav 28,180,208
L_ v7,24,s55,0
Mvav 28,184,228
S_ s55,s71,665600
Mvav 28,188,248
S_ m0,s51,41984
Mvav 32,180,212
Mvav 32,184,232
L_ v7,24,s55,0
Mvav 32,188,252
s_waitcnt vmcnt(23) lgkmcnt(0)
s_barrier
Mvav 52,220,252
S_ s55,s71,0
Ta 180,%0,0
Ta 182,%0,2048
Mvav 52,196,232
S_ m0,s51,24576
Mvav 52,192,212
s_nop 0
Ta 184,%0,4096
Ta 186,%0,6144
Mvav 48,220,248
L_ v7,24,s55,0
Mvav 48,196,228
S_ s55,s71,10240
Ta 188,%0,8192
Ta 190,%0,10240
Mvav 48,192,208
S_ m0,s51,25600
Mvav 44,220,244
Mvav 44,196,224
L_ v7,24,s55,0
Mvav 44,192,204
S_ s55,s71,20480
Mvav 40,220,240
S_ m0,s51,26624
Mvav 40,196,220
Mvav 40,192,200
L_ v7,24,s55,0
Mvav 36,220,236
S_ s55,s71,30720
Mvav 36,196,216
S_ m0,s51,27648
Mvav 36,192,196
W_ 0
Mvaa 56,180,0
s_nop 0
Ta 192,v1,0
Ta 194,v1,2048
Mvaa 56,184,40
L_ v7,24,s55,0
Mvaa 56,188,80
S_ s55,s71,40960
Ta 196,v1,4096
Ta 198,v1,6144
Mvaa 60,180,4
S_ m0,s51,28672
Mvaa 60,184,44
s_nop 0
Ta 220,v1,8192
Ta 222,v1,10240
Mvaa 60,188,84
L_ v7,24,s55,0
Mvaa 64,180,8
S_ s55,s71,51200
Mvaa 64,184,48
S_ m0,s51,29696
Mvaa 64,188,88
.Lpad_o_2:
s_cbranch_execz .Lpad_o_6
Mvaa 68,180,12
L_ v7,24,s55,0
Mvaa 68,184,52
Mvaa 68,188,240
Mvaa 72,180,16
Mvaa 72,184,56
buffer_load_dword a74, v4, s[24:27], s85 offen
Mvaa 72,188,244
W_ 0
Mvaa 92,220,244
s_nop 0
Ta 180,v2,0
Ta 182,v2,2048
Mvaa 92,196,56
Mvaa 92,192,16
s_nop 0
Ta 184,v2,4096
Ta 186,v2,6144
Mvaa 88,220,240
Mvaa 88,196,52
s_nop 0
Ta 188,v2,8192
Ta 190,v2,10240
Mvaa 88,192,12
Mvaa 84,220,88
Mvaa 84,196,48
Mvaa 84,192,8
Mvaa 80,220,84
Mvaa 80,196,44
Mvaa 80,192,4
Mvaa 76,220,80
Mvaa 76,196,40
X_ a20,-1
X_ a21,-1
Mvaa 76,192,0
W_ 0
X_ a22,-1
X_ a23,-1
Mvaa 16,180,0
S_ s81,s6,7
Ta 192,v3,0
Ta 194,v3,2048
Mvaa 16,184,40
s_and_b32 s81, s81, 7
Mvaa 16,188,80
s_mul_i32 s81, s81, 20480
Ta 196,v3,4096
Ta 198,v3,6144
Mvaa 20,180,4
S_ s81,s81,s78
Mvaa 20,184,44
s_nop 0
Ta 220,v3,8192
Ta 222,v3,10240
Mvaa 20,188,84
buffer_store_dwordx4 a[20:23], v6, s[28:31], s81 offen offset:0 sc1
Mvaa 24,180,8
Mvaa 24,184,48
Mvaa 24,188,88
Mvaa 28,180,12
buffer_store_dwordx4 a[20:23], v6, s[28:31], s81 offen offset:1024 sc1
Mvaa 28,184,52
Mvaa 28,188,240
Mvaa 32,180,16
Mvaa 32,184,56
Mvaa 32,188,244
s_waitcnt vmcnt(26) lgkmcnt(0)
s_barrier
Mvaa 52,220,244
s_nop 0
Ta 180,%0,12288
Ta 182,%0,14336
Mvaa 52,196,56
buffer_store_dwordx4 a[20:23], v6, s[28:31], s81 offen offset:2048 sc1
Mvaa 52,192,16
s_nop 0
Ta 184,%0,16384
Ta 186,%0,18432
Mvaa 48,220,240
Mvaa 48,196,52
s_nop 0
Ta 188,%0,20480
Ta 190,%0,22528
Mvaa 48,192,12
Mvaa 44,220,88
Mvaa 44,196,48
buffer_store_dwordx4 a[20:23], v6, s[28:31], s81 offen offset:3072 sc1
Mvaa 44,192,8
S_ s81,s81,4096
Mvaa 40,220,84
Mvaa 40,196,44
Mvaa 40,192,4
Mvaa 36,220,80
)A"
R"A(Mvaa 36,196,40
Mvaa 36,192,0
W_ 0
Mvaa 56,180,120
s_nop 0
Ta 192,v1,12288
Ta 194,v1,14336
Mvaa 56,184,160
buffer_store_dwordx4 a[20:23], v6, s[28:31], s81 offen offset:0 sc1
Mvaa 56,188,200
S_ s73,s78,184320
Ta 196,v1,16384
Ta 198,v1,18432
Mvaa 60,180,124
s_mov_b32 m0, s52
S_ s73,s73,4096
Mvaa 60,184,164
s_nop 0
Ta 220,v1,20480
Ta 222,v1,22528
Mvaa 60,188,204
s_nop 0
Y_ 56,0
Y_ 57,256
Mvaa 64,180,128
s_nop 0
Y_ 58,512
Y_ 59,768
Mvaa 64,184,168
Mvaa 64,188,208
s_nop 0
Y_ 60,1024
Y_ 61,1280
Mvaa 68,180,132
s_nop 0
Y_ 62,1536
Y_ 63,1792
Mvaa 68,184,172
Mvaa 68,188,212
s_nop 0
Y_ 64,2048
Y_ 65,2304
Mvaa 72,180,136
s_nop 0
Y_ 66,2560
Y_ 67,2816
Mvaa 72,184,176
Mvaa 72,188,216
W_ 12
Y_ 68,3072
Y_ 69,3328
Mvaa 92,220,216
s_nop 0
Ta 180,v2,12288
Ta 182,v2,14336
Mvaa 92,196,176
s_nop 0
Y_ 70,3584
Y_ 71,3840
Mvaa 92,192,136
s_nop 0
Ta 184,v2,16384
Ta 186,v2,18432
Mvaa 88,220,212
s_nop 0
Y_ 72,4096
Y_ 73,4352
Mvaa 88,196,172
s_nop 0
Ta 188,v2,20480
Ta 190,v2,22528
Mvaa 88,192,132
s_nop 0
Y_ 74,4608
Y_ 75,4864
Mvaa 84,220,208
s_nop 0
Y_ 92,9216
Y_ 93,9472
Mvaa 84,196,168
s_nop 0
Y_ 94,9728
Y_ 95,9984
Mvaa 84,192,128
s_nop 0
Y_ 88,8192
Y_ 89,8448
Mvaa 80,220,204
s_nop 0
Y_ 90,8704
Y_ 91,8960
Mvaa 80,196,164
Mvaa 80,192,124
s_nop 0
Y_ 84,7168
Y_ 85,7424
Mvaa 76,220,200
s_nop 0
Y_ 86,7680
Y_ 87,7936
Mvaa 76,196,160
Mvaa 76,192,120
W_ 14
Y_ 80,6144
Y_ 81,6400
Mvaa 16,180,120
s_nop 0
Ta 192,v3,12288
Ta 194,v3,14336
Mvaa 16,184,160
s_nop 0
Y_ 82,6656
Y_ 83,6912
Mvaa 16,188,200
s_nop 0
Ta 196,v3,16384
Ta 198,v3,18432
Mvaa 20,180,124
s_nop 0
Y_ 76,5120
Y_ 77,5376
Mvaa 20,184,164
s_nop 0
Ta 220,v3,20480
Ta 222,v3,22528
Mvaa 20,188,204
s_nop 0
Y_ 78,5632
Y_ 79,5888
Mvaa 24,180,128
Mvaa 24,184,168
Mvaa 24,188,208
s_waitcnt vmcnt(23) lgkmcnt(0)
s_barrier
Mvaa 28,180,132
S_ s55,s71,122880
ds_read2st64_b32 v[96:97], v5 offset0:0 offset1:1
Mvaa 28,184,172
S_ m0,s51,0
ds_read2st64_b32 v[98:99], v5 offset0:2 offset1:3
Mvaa 28,188,212
L_ v7,24,s55,0
Mvaa 32,180,136
S_ s55,s71,133120
ds_read2st64_b32 v[104:105], v5 offset0:4 offset1:5
Mvaa 32,184,176
S_ m0,s51,1024
ds_read2st64_b32 v[106:107], v5 offset0:6 offset1:7
Mvaa 32,188,216
ds_read2st64_b32 v[112:113], v5 offset0:8 offset1:9
Mvaa 52,220,216
s_nop 0
Ta 180,%0,49152
Ta 182,%0,51200
Mvaa 52,196,176
L_ v7,24,s55,0
Mvaa 52,192,136
W_ 5
Ta 184,%0,53248
Ta 186,%0,55296
Mvaa 48,220,212
S_ s55,s71,143360
ds_read2st64_b32 v[114:115], v5 offset0:10 offset1:11
A_ v96,v16,v96
Mvaa 48,196,172
S_ m0,s51,2048
Ta 188,%0,57344
Ta 190,%0,59392
Mvaa 48,192,132
L_ v7,24,s55,0
Mvaa 44,220,208
S_ s55,s71,153600
ds_read2st64_b32 v[120:121], v5 offset0:12 offset1:13
A_ v97,v17,v97
Mvaa 44,196,168
S_ m0,s51,3072
ds_read2st64_b32 v[122:123], v5 offset0:14 offset1:15
A_ v98,v18,v98
Mvaa 44,192,128
ds_read2st64_b32 v[128:129], v5 offset0:16 offset1:17
A_ v99,v19,v99
Mvaa 40,220,204
L_ v7,24,s55,0
Mvaa 40,196,164
W_ 11
ds_read2st64_b32 v[130:131], v5 offset0:18 offset1:19
A_ v104,v20,v104
Mvaa 40,192,124
S_ s55,s71,163840
ds_read2st64_b32 v[132:133], v5 offset0:36 offset1:37
A_ v105,v21,v105
Mvaa 36,220,200
S_ m0,s51,4096
ds_read2st64_b32 v[134:135], v5 offset0:38 offset1:39
A_ v106,v22,v106
Mvaa 36,196,160
L_ v7,24,s55,0
Mvaa 36,192,120
W_ 6
ds_read2st64_b32 v[124:125], v5 offset0:32 offset1:33
A_ v107,v23,v107
Zvav 56,180,136
Ta 192,v1,49152
Ta 194,v1,51200
Mvav 56,184,156
S_ s55,s71,174080
ds_read2st64_b32 v[126:127], v5 offset0:34 offset1:35
A_ v112,v24,v112
Mvav 56,188,176
S_ m0,s51,5120
Ta 196,v1,53248
Ta 198,v1,55296
Zvav 60,180,140
L_ v7,24,s55,0
Mvav 60,184,160
S_ s79,s6,2
Ta 220,v1,57344
Ta 222,v1,59392
Mvav 60,188,180
s_and_b32 s79, s79, 7
ds_read2st64_b32 v[116:117], v5 offset0:28 offset1:29
A_ v113,v25,v113
Zvav 64,180,144
s_lshl_b32 s79, s79, 7
ds_read2st64_b32 v[118:119], v5 offset0:30 offset1:31
A_ v114,v26,v114
Mvav 64,184,164
S_ s71,s72,s79
ds_read2st64_b32 v[108:109], v5 offset0:24 offset1:25
A_ v115,v27,v115
Mvav 64,188,184
W_ 14
ds_read2st64_b32 v[110:111], v5 offset0:26 offset1:27
A_ v120,v28,v120
Zvav 68,180,148
S_ s55,s71,491520
ds_read2st64_b32 v[100:101], v5 offset0:20 offset1:21
A_ v121,v29,v121
Mvav 68,184,168
S_ m0,s51,12288
ds_read2st64_b32 v[102:103], v5 offset0:22 offset1:23
A_ v122,v30,v122
Mvav 68,188,188
L_ v7,24,s55,0
Zvav 72,180,152
S_ s55,s71,501760
A_ v123,v31,v123
R_ v12,a248
Mvav 72,184,172
W_ 14
A_ v128,v32,v128
A_ v129,v33,v129
Mvav 72,188,192
W_ 6
A_ v130,v34,v130
A_ v131,v35,v131
Zvav 92,220,192
Ta 180,v2,49152
Ta 182,v2,51200
Mvav 92,196,172
S_ m0,s51,13312
A_ v132,v52,v132
A_ v133,v53,v133
Mvav 92,192,152
s_nop 0
Ta 184,v2,53248
Ta 186,v2,55296
Zvav 88,220,188
L_ v7,24,s55,0
Mvav 88,196,168
S_ s55,s71,512000
Ta 188,v2,57344
Ta 190,v2,59392
Mvav 88,192,148
S_ m0,s51,14336
A_ v134,v54,v134
A_ v135,v55,v135
Zvav 84,220,184
A_ v124,v48,v124
A_ v125,v49,v125
Mvav 84,196,164
L_ v7,24,s55,0
Mvav 84,192,144
S_ s55,s71,522240
A_ v126,v50,v126
A_ v127,v51,v127
Zvav 80,220,180
W_ 10
A_ v116,v44,v116
A_ v117,v45,v117
Mvav 80,196,160
S_ m0,s51,15360
A_ v118,v46,v118
A_ v119,v47,v119
Mvav 80,192,140
L_ v7,24,s55,0
Zvav 76,220,176
W_ 8
A_ v108,v40,v108
A_ v109,v41,v109
Mvav 76,196,156
S_ s55,s71,532480
A_ v110,v42,v110
A_ v111,v43,v111
Mvav 76,192,136
W_ 0
A_ v100,v36,v100
Zvav 16,180,136
S_ m0,s51,16384
Ta 192,v3,49152
Ta 194,v3,51200
Mvav 16,184,156
L_ v7,24,s55,0
Mvav 16,188,176
S_ s55,s71,542720
Ta 196,v3,53248
Ta 198,v3,55296
Zvav 20,180,140
S_ m0,s51,17408
A_ v101,v37,v101
A_ v102,v38,v102
Mvav 20,184,160
S_ s85,s6,4
Ta 220,v3,57344
Ta 222,v3,59392
Mvav 20,188,180
L_ v7,24,s55,0
Zvav 24,180,144
s_and_b32 s85, s85, 7
A_ v103,v39,v103
C_ 131,130,131
Mvav 24,184,164
s_lshl_b32 s85, s85, 7
C_ 130,128,129
C_ 132,132,133
Mvav 24,188,184
S_ s85,s85,s87
C_ 133,134,135
v_min_u32 v12, 240, v12
Zvav 28,180,148
buffer_store_dwordx4 v[130:133], v6, s[28:31], s73 offen offset:0 sc1
Mvav 28,184,168
s_sub_u32 s73, s73, 4096
v_lshlrev_b32 v12, 3, v12
Mvav 28,188,188
C_ 123,122,123
C_ 122,120,121
Zvav 32,180,152
C_ 124,124,125
C_ 125,126,127
Mvav 32,184,172
Mvav 32,188,192
s_waitcnt vmcnt(25) lgkmcnt(0)
s_barrier
Zvav 52,220,192
S_ s55,s71,614400
Ta 180,%0,36864
Ta 182,%0,38912
Mvav 52,196,172
S_ m0,s51,49152
Mvav 52,192,152
s_nop 0
Ta 184,%0,40960
Ta 186,%0,43008
Zvav 48,220,188
buffer_store_dwordx4 v[122:125], v6, s[28:31], s73 offen offset:3072 sc1
Mvav 48,196,168
s_nop 0
Ta 188,%0,45056
Ta 190,%0,47104
Mvav 48,192,148
C_ 115,114,115
C_ 114,112,113
Zvav 44,220,184
C_ 116,116,117
C_ 117,118,119
Mvav 44,196,164
Mvav 44,192,144
Zvav 40,220,180
Mvav 40,196,160
Mvav 40,192,140
buffer_store_dwordx4 v[114:117], v6, s[28:31], s73 offen offset:2048 sc1
Zvav 36,220,176
C_ 107,106,107
C_ 106,104,105
Mvav 36,196,156
C_ 108,108,109
C_ 109,110,111
Mvav 36,192,136
W_ 0
Mvav 56,180,196
s_nop 0
Ta 192,v1,36864
Ta 194,v1,38912
Mvav 56,184,216
Mvav 56,188,236
s_nop 0
Ta 196,v1,40960
Ta 198,v1,43008
Mvav 60,180,200
Mvav 60,184,220
s_nop 0
Ta 220,v1,45056
Ta 222,v1,47104
Mvav 60,188,240
buffer_store_dwordx4 v[106:109], v6, s[28:31], s73 offen offset:1024 sc1
Mvav 64,180,204
C_ 99,98,99
C_ 98,96,97
Mvav 64,184,224
C_ 100,100,101
C_ 101,102,103
Mvav 64,188,244
Mvav 68,180,208
Mvav 68,184,228
Mvav 68,188,248
Mvav 72,180,212
Mvav 72,184,232
buffer_store_dwordx4 v[98:101], v6, s[28:31], s73 offen offset:0 sc1
Mvav 72,188,252
W_ 0
Mvav 92,220,252
s_nop 0
Ta 180,v2,36864
Ta 182,v2,38912
Mvav 92,196,232
Mvav 92,192,212
s_nop 0
Ta 184,v2,40960
Ta 186,v2,43008
Mvav 88,220,248
Mvav 88,196,228
s_nop 0
Ta 188,v2,45056
Ta 190,v2,47104
Mvav 88,192,208
Mvav 84,220,244
L_ v7,24,s55,0
Mvav 84,196,224
S_ s55,s71,624640
Mvav 84,192,204
S_ m0,s51,50176
Mvav 80,220,240
Mvav 80,196,220
L_ v7,24,s55,0
Mvav 80,192,200
S_ s55,s71,634880
Mvav 76,220,236
S_ m0,s51,51200
Mvav 76,196,216
Mvav 76,192,196
W_ 0
Mvav 16,180,196
s_nop 0
Ta 192,v3,36864
Ta 194,v3,38912
Mvav 16,184,216
L_ v7,24,s55,0
Mvav 16,188,236
S_ s55,s71,645120
Ta 196,v3,40960
Ta 198,v3,43008
Mvav 20,180,200
S_ m0,s51,52224
Mvav 20,184,220
s_nop 0
Ta 220,v3,45056
Ta 222,v3,47104
Mvav 20,188,240
L_ v7,24,s55,0
Mvav 24,180,204
S_ s55,s71,655360
Mvav 24,184,224
S_ m0,s51,53248
Mvav 24,188,244
Mvav 28,180,208
L_ v7,24,s55,0
Mvav 28,184,228
S_ s55,s71,665600
Mvav 28,188,248
S_ m0,s51,54272
Mvav 32,180,212
.Lpad_o_3:
s_cbranch_execz .Lpad_o_7
Mvav 32,184,232
L_ v7,24,s55,0
Mvav 32,188,252
s_waitcnt vmcnt(29) lgkmcnt(0)
s_barrier
Mvav 52,220,252
S_ s55,s71,0
Ta 180,%0,24576
Ta 182,%0,26624
Mvav 52,196,232
S_ m0,s51,36864
Mvav 52,192,212
s_nop 0
Ta 184,%0,28672
Ta 186,%0,30720
Mvav 48,220,248
L_ v7,24,s55,0
Mvav 48,196,228
S_ s55,s71,10240
Ta 188,%0,32768
Ta 190,%0,34816
Mvav 48,192,208
S_ m0,s51,37888
Mvav 44,220,244
Mvav 44,196,224
L_ v7,24,s55,0
Mvav 44,192,204
S_ s55,s71,20480
Mvav 40,220,240
S_ m0,s51,38912
Mvav 40,196,220
Mvav 40,192,200
L_ v7,24,s55,0
Mvav 36,220,236
S_ s55,s71,30720
Mvav 36,196,216
S_ m0,s51,39936
Mvav 36,192,196
W_ 0
Mvaa 56,180,0
s_nop 0
Ta 192,v1,24576
Ta 194,v1,26624
Mvaa 56,184,40
L_ v7,24,s55,0
Mvaa 56,188,80
S_ s55,s71,40960
Ta 196,v1,28672
Ta 198,v1,30720
Mvaa 60,180,4
S_ m0,s51,40960
Mvaa 60,184,44
s_nop 0
Ta 220,v1,32768
Ta 222,v1,34816
Mvaa 60,188,84
L_ v7,24,s55,0
Mvaa 64,180,8
S_ s55,s71,51200
Mvaa 64,184,48
S_ m0,s51,41984
Mvaa 64,188,88
Mvaa 68,180,12
L_ v7,24,s55,0
Mvaa 68,184,52
Mvaa 68,188,240
Mvaa 72,180,16
Mvaa 72,184,56
buffer_load_dword a74, v12, s[44:47], s66 offen
Mvaa 72,188,244
W_ 0
Mvaa 92,220,244
s_nop 0
Ta 180,v2,24576
Ta 182,v2,26624
Mvaa 92,196,56
Mvaa 92,192,16
s_nop 0
Ta 184,v2,28672
Ta 186,v2,30720
Mvaa 88,220,240
buffer_load_dword a74, v4, s[24:27], s85 offen
Mvaa 88,196,52
s_nop 0
Ta 188,v2,32768
Ta 190,v2,34816
Mvaa 88,192,12
Mvaa 84,220,88
Mvaa 84,196,48
Mvaa 84,192,8
Mvaa 80,220,84
Mvaa 80,196,44
Mvaa 80,192,4
Mvaa 76,220,80
Mvaa 76,196,40
Mvaa 76,192,0
W_ 0
Mvaa 16,180,0
s_nop 0
Ta 192,v3,24576
Ta 194,v3,26624
Mvaa 16,184,40
Mvaa 16,188,80
s_nop 0
Ta 196,v3,28672
Ta 198,v3,30720
Mvaa 20,180,4
Mvaa 20,184,44
s_nop 0
Ta 220,v3,32768
Ta 222,v3,34816
Mvaa 20,188,84
Mvaa 24,180,8
Mvaa 24,184,48
Mvaa 24,188,88
Mvaa 28,180,12
Mvaa 28,184,52
Mvaa 28,188,240
Mvaa 32,180,16
Mvaa 32,184,56
Mvaa 32,188,244
s_waitcnt vmcnt(25) lgkmcnt(0)
s_barrier
Mvaa 52,220,244
s_nop 0
Ta 180,%0,0
Ta 182,%0,2048
Mvaa 52,196,56
Mvaa 52,192,16
s_nop 0
Ta 184,%0,4096
Ta 186,%0,6144
Mvaa 48,220,240
Mvaa 48,196,52
s_nop 0
Ta 188,%0,8192
Ta 190,%0,10240
Mvaa 48,192,12
Mvaa 44,220,88
Mvaa 44,196,48
Mvaa 44,192,8
Mvaa 40,220,84
Mvaa 40,196,44
Mvaa 40,192,4
Mvaa 36,220,80
Mvaa 36,196,40
Mvaa 36,192,0
W_ 0
Mvaa 56,180,120
S_ s81,s6,1
Ta 192,v1,0
Ta 194,v1,2048
Mvaa 56,184,160
s_and_b32 s81, s81, 7
Mvaa 56,188,200
s_mul_i32 s81, s81, 20480
Ta 196,v1,4096
Ta 198,v1,6144
Mvaa 60,180,124
s_mov_b32 m0, s52
S_ s73,s81,s78
Mvaa 60,184,164
S_ s73,s73,4096
Ta 220,v1,8192
Ta 222,v1,10240
Mvaa 60,188,204
s_nop 0
Y_ 56,0
Y_ 57,256
Mvaa 64,180,128
s_nop 0
Y_ 58,512
Y_ 59,768
Mvaa 64,184,168
Mvaa 64,188,208
s_nop 0
Y_ 60,1024
Y_ 61,1280
Mvaa 68,180,132
s_nop 0
Y_ 62,1536
Y_ 63,1792
Mvaa 68,184,172
Mvaa 68,188,212
s_nop 0
Y_ 64,2048
Y_ 65,2304
Mvaa 72,180,136
s_nop 0
Y_ 66,2560
Y_ 67,2816
Mvaa 72,184,176
Mvaa 72,188,216
W_ 12
Y_ 68,3072
Y_ 69,3328
Mvaa 92,220,216
s_nop 0
Ta 180,v2,0
Ta 182,v2,2048
Mvaa 92,196,176
s_nop 0
Y_ 70,3584
Y_ 71,3840
Mvaa 92,192,136
s_nop 0
Ta 184,v2,4096
Ta 186,v2,6144
Mvaa 88,220,212
s_nop 0
Y_ 72,4096
Y_ 73,4352
Mvaa 88,196,172
s_nop 0
Ta 188,v2,8192
Ta 190,v2,10240
Mvaa 88,192,132
s_nop 0
Y_ 74,4608
Y_ 75,4864
Mvaa 84,220,208
s_nop 0
Y_ 92,9216
Y_ 93,9472
Mvaa 84,196,168
s_nop 0
Y_ 94,9728
Y_ 95,9984
Mvaa 84,192,128
s_nop 0
Y_ 88,8192
Y_ 89,8448
Mvaa 80,220,204
s_nop 0
Y_ 90,8704
Y_ 91,8960
Mvaa 80,196,164
Mvaa 80,192,124
s_nop 0
Y_ 84,7168
Y_ 85,7424
Mvaa 76,220,200
s_nop 0
Y_ 86,7680
Y_ 87,7936
Mvaa 76,196,160
Mvaa 76,192,120
W_ 14
Y_ 80,6144
Y_ 81,6400
Mvaa 16,180,120
s_nop 0
Ta 192,v3,0
Ta 194,v3,2048
Mvaa 16,184,160
s_nop 0
Y_ 82,6656
Y_ 83,6912
Mvaa 16,188,200
s_nop 0
Ta 196,v3,4096
Ta 198,v3,6144
Mvaa 20,180,124
s_nop 0
Y_ 76,5120
Y_ 77,5376
Mvaa 20,184,164
s_nop 0
Ta 220,v3,8192
Ta 222,v3,10240
Mvaa 20,188,204
s_nop 0
Y_ 78,5632
Y_ 79,5888
Mvaa 24,180,128
Mvaa 24,184,168
Mvaa 24,188,208
s_waitcnt vmcnt(19) lgkmcnt(0)
s_barrier
Mvaa 28,180,132
S_ s55,s71,122880
ds_read2st64_b32 v[96:97], v5 offset0:0 offset1:1
Mvaa 28,184,172
S_ m0,s51,24576
ds_read2st64_b32 v[98:99], v5 offset0:2 offset1:3
Mvaa 28,188,212
L_ v7,24,s55,0
Mvaa 32,180,136
S_ s55,s71,133120
ds_read2st64_b32 v[104:105], v5 offset0:4 offset1:5
Mvaa 32,184,176
S_ m0,s51,25600
ds_read2st64_b32 v[106:107], v5 offset0:6 offset1:7
Mvaa 32,188,216
ds_read2st64_b32 v[112:113], v5 offset0:8 offset1:9
Mvaa 52,220,216
s_nop 0
Ta 180,%0,12288
Ta 182,%0,14336
Mvaa 52,196,176
L_ v7,24,s55,0
Mvaa 52,192,136
W_ 5
Ta 184,%0,16384
Ta 186,%0,18432
Mvaa 48,220,212
S_ s55,s71,143360
ds_read2st64_b32 v[114:115], v5 offset0:10 offset1:11
A_ v96,v16,v96
Mvaa 48,196,172
S_ m0,s51,26624
Ta 188,%0,20480
Ta 190,%0,22528
Mvaa 48,192,132
L_ v7,24,s55,0
Mvaa 44,220,208
S_ s55,s71,153600
ds_read2st64_b32 v[120:121], v5 offset0:12 offset1:13
A_ v97,v17,v97
Mvaa 44,196,168
S_ m0,s51,27648
ds_read2st64_b32 v[122:123], v5 offset0:14 offset1:15
A_ v98,v18,v98
Mvaa 44,192,128
ds_read2st64_b32 v[128:129], v5 offset0:16 offset1:17
A_ v99,v19,v99
Mvaa 40,220,204
L_ v7,24,s55,0
Mvaa 40,196,164
W_ 11
ds_read2st64_b32 v[130:131], v5 offset0:18 offset1:19
A_ v104,v20,v104
Mvaa 40,192,124
S_ s55,s71,163840
ds_read2st64_b32 v[132:133], v5 offset0:36 offset1:37
A_ v105,v21,v105
Mvaa 36,220,200
S_ m0,s51,28672
ds_read2st64_b32 v[134:135], v5 offset0:38 offset1:39
A_ v106,v22,v106
Mvaa 36,196,160
L_ v7,24,s55,0
Mvaa 36,192,120
W_ 6
ds_read2st64_b32 v[124:125], v5 offset0:32 offset1:33
A_ v107,v23,v107
Zvav 56,180,136
Ta 192,v1,12288
Ta 194,v1,14336
Mvav 56,184,156
S_ s55,s71,174080
ds_read2st64_b32 v[126:127], v5 offset0:34 offset1:35
A_ v112,v24,v112
Mvav 56,188,176
S_ m0,s51,29696
Ta 196,v1,16384
Ta 198,v1,18432
Zvav 60,180,140
L_ v7,24,s55,0
Mvav 60,184,160
S_ s79,s6,3
Ta 220,v1,20480
Ta 222,v1,22528
Mvav 60,188,180
s_and_b32 s79, s79, 7
ds_read2st64_b32 v[116:117], v5 offset0:28 offset1:29
A_ v113,v25,v113
Zvav 64,180,144
s_lshl_b32 s79, s79, 7
ds_read2st64_b32 v[118:119], v5 offset0:30 offset1:31
A_ v114,v26,v114
Mvav 64,184,164
S_ s71,s72,s79
ds_read2st64_b32 v[108:109], v5 offset0:24 offset1:25
A_ v115,v27,v115
Mvav 64,188,184
W_ 14
ds_read2st64_b32 v[110:111], v5 offset0:26 offset1:27
A_ v120,v28,v120
Zvav 68,180,148
S_ s55,s71,491520
ds_read2st64_b32 v[100:101], v5 offset0:20 offset1:21
A_ v121,v29,v121
Mvav 68,184,168
S_ m0,s51,0
ds_read2st64_b32 v[102:103], v5 offset0:22 offset1:23
A_ v122,v30,v122
Mvav 68,188,188
L_ v7,24,s55,0
Zvav 72,180,152
S_ s55,s71,501760
A_ v123,v31,v123
Mvav 72,184,172
W_ 14
A_ v128,v32,v128
A_ v129,v33,v129
Mvav 72,188,192
W_ 6
A_ v130,v34,v130
A_ v131,v35,v131
Zvav 92,220,192
Ta 180,v2,12288
Ta 182,v2,14336
Mvav 92,196,172
S_ m0,s51,1024
A_ v132,v52,v132
A_ v133,v53,v133
Mvav 92,192,152
s_nop 0
Ta 184,v2,16384
Ta 186,v2,18432
Zvav 88,220,188
L_ v7,24,s55,0
Mvav 88,196,168
S_ s55,s71,512000
Ta 188,v2,20480
Ta 190,v2,22528
Mvav 88,192,148
S_ m0,s51,2048
A_ v134,v54,v134
A_ v135,v55,v135
Zvav 84,220,184
A_ v124,v48,v124
A_ v125,v49,v125
Mvav 84,196,164
L_ v7,24,s55,0
Mvav 84,192,144
S_ s55,s71,522240
A_ v126,v50,v126
A_ v127,v51,v127
Zvav 80,220,180
W_ 10
A_ v116,v44,v116
A_ v117,v45,v117
Mvav 80,196,160
S_ m0,s51,3072
A_ v118,v46,v118
A_ v119,v47,v119
Mvav 80,192,140
L_ v7,24,s55,0
Zvav 76,220,176
W_ 8
A_ v108,v40,v108
A_ v109,v41,v109
Mvav 76,196,156
S_ s55,s71,532480
A_ v110,v42,v110
A_ v111,v43,v111
Mvav 76,192,136
W_ 0
A_ v100,v36,v100
Zvav 16,180,136
S_ m0,s51,4096
Ta 192,v3,12288
Ta 194,v3,14336
Mvav 16,184,156
L_ v7,24,s55,0
Mvav 16,188,176
S_ s55,s71,542720
Ta 196,v3,16384
Ta 198,v3,18432
Zvav 20,180,140
S_ m0,s51,5120
A_ v101,v37,v101
A_ v102,v38,v102
Mvav 20,184,160
S_ s85,s6,5
Ta 220,v3,20480
Ta 222,v3,22528
Mvav 20,188,180
L_ v7,24,s55,0
Zvav 24,180,144
s_and_b32 s85, s85, 7
A_ v103,v39,v103
C_ 131,130,131
Mvav 24,184,164
s_lshl_b32 s85, s85, 7
C_ 130,128,129
C_ 132,132,133
Mvav 24,188,184
S_ s85,s85,s87
C_ 133,134,135
Zvav 28,180,148
buffer_store_dwordx4 v[130:133], v6, s[28:31], s73 offen offset:0 sc1
Mvav 28,184,168
s_sub_u32 s73, s73, 4096
Mvav 28,188,188
C_ 123,122,123
C_ 122,120,121
Zvav 32,180,152
C_ 124,124,125
C_ 125,126,127
Mvav 32,184,172
Mvav 32,188,192
s_waitcnt vmcnt(21) lgkmcnt(0)
s_barrier
Zvav 52,220,192
S_ s56,s76,s74
Ta 180,%0,49152
Ta 182,%0,51200
Mvav 52,196,172
S_ s55,s71,614400
Mvav 52,192,152
S_ m0,s51,12288
Ta 184,%0,53248
Ta 186,%0,55296
Zvav 48,220,188
buffer_load_dwordx4 a[60:63], v6, s[28:31], s56 offen offset:0 sc1
Mvav 48,196,168
S_ s56,s76,s82
Ta 188,%0,57344
Ta 190,%0,59392
Mvav 48,192,148
Zvav 44,220,184
Mvav 44,196,164
buffer_load_dwordx4 a[64:67], v6, s[28:31], s56 offen offset:0 sc1
Mvav 44,192,144
Zvav 40,220,180
Mvav 40,196,160
Mvav 40,192,140
buffer_store_dwordx4 v[122:125], v6, s[28:31], s73 offen offset:3072 sc1
Zvav 36,220,176
C_ 115,114,115
C_ 114,112,113
Mvav 36,196,156
C_ 116,116,117
C_ 117,118,119
Mvav 36,192,136
W_ 0
Mvav 56,180,196
s_nop 0
Ta 192,v1,49152
Ta 194,v1,51200
Mvav 56,184,216
Mvav 56,188,236
s_nop 0
Ta 196,v1,53248
Ta 198,v1,55296
Mvav 60,180,200
Mvav 60,184,220
s_nop 0
Ta 220,v1,57344
Ta 222,v1,59392
Mvav 60,188,240
buffer_store_dwordx4 v[114:117], v6, s[28:31], s73 offen offset:2048 sc1
Mvav 64,180,204
C_ 107,106,107
C_ 106,104,105
Mvav 64,184,224
C_ 108,108,109
C_ 109,110,111
Mvav 64,188,244
Mvav 68,180,208
Mvav 68,184,228
Mvav 68,188,248
Mvav 72,180,212
Mvav 72,184,232
buffer_store_dwordx4 v[106:109], v6, s[28:31], s73 offen offset:1024 sc1
Mvav 72,188,252
W_ 0
C_ 99,98,99
C_ 98,96,97
Mvav 92,220,252
s_nop 0
Ta 180,v2,49152
Ta 182,v2,51200
Mvav 92,196,232
.Lpad_o_4:
s_cbranch_execz .Lpad_o_8
C_ 100,100,101
C_ 101,102,103
Mvav 92,192,212
s_nop 0
Ta 184,v2,53248
Ta 186,v2,55296
Mvav 88,220,248
Mvav 88,196,228
s_nop 0
Ta 188,v2,57344
Ta 190,v2,59392
Mvav 88,192,208
Mvav 84,220,244
buffer_store_dwordx4 v[98:101], v6, s[28:31], s73 offen offset:0 sc1
Mvav 84,196,224
Mvav 84,192,204
Mvav 80,220,240
Mvav 80,196,220
S_ s83,s6,7
Mvav 80,192,200
s_and_b32 s83, s83, 7
Mvav 76,220,236
s_lshl_b32 s83, s83, 2
Mvav 76,196,216
v_cmp_eq_u32_e64 s[36:37], s83, v8
Mvav 76,192,196
W_ 0
v_mov_b32 v10, 0x7fff0000
Mvav 16,180,196
s_nop 0
Ta 192,v3,49152
Ta 194,v3,51200
Mvav 16,184,216
L_ v7,24,s55,0
Mvav 16,188,236
S_ s55,s71,624640
Ta 196,v3,53248
Ta 198,v3,55296
Mvav 20,180,200
S_ m0,s51,13312
v_cndmask_b32_e64 v10, v10, v9, s[36:37]
Mvav 20,184,220
s_nop 0
Ta 220,v3,57344
Ta 222,v3,59392
Mvav 20,188,240
L_ v7,24,s55,0
Mvav 24,180,204
S_ s55,s71,634880
Mvav 24,184,224
S_ m0,s51,14336
Mvav 24,188,244
Mvav 28,180,208
L_ v7,24,s55,0
Mvav 28,184,228
S_ s55,s71,645120
Mvav 28,188,248
S_ m0,s51,15360
Mvav 32,180,212
Mvav 32,184,232
L_ v7,24,s55,0
Mvav 32,188,252
s_waitcnt vmcnt(25) lgkmcnt(0)
s_barrier
S_ s55,s71,655360
Mvav 52,220,252
S_ m0,s51,16384
Ta 180,%0,36864
Ta 182,%0,38912
Mvav 52,196,232
Mvav 52,192,212
s_nop 0
Ta 184,%0,40960
Ta 186,%0,43008
Mvav 48,220,248
buffer_store_dword a255, v10, s[92:95], 0 offen offset:384 sc1
Mvav 48,196,228
s_nop 0
Ta 188,%0,45056
Ta 190,%0,47104
Mvav 48,192,208
Mvav 44,220,244
Mvav 44,196,224
L_ v7,24,s55,0
Mvav 44,192,204
S_ s55,s71,665600
Mvav 40,220,240
S_ m0,s51,17408
Mvav 40,196,220
Mvav 40,192,200
L_ v7,24,s55,0
Mvav 36,220,236
S_ s55,s71,0
Mvav 36,196,216
S_ m0,s51,49152
Mvav 36,192,196
W_ 0
Mvaa 56,180,0
s_nop 0
Ta 192,v1,36864
Ta 194,v1,38912
Mvaa 56,184,40
L_ v7,24,s55,0
Mvaa 56,188,80
S_ s55,s71,10240
Ta 196,v1,40960
Ta 198,v1,43008
Mvaa 60,180,4
S_ m0,s51,50176
Mvaa 60,184,44
s_nop 0
Ta 220,v1,45056
Ta 222,v1,47104
Mvaa 60,188,84
L_ v7,24,s55,0
Mvaa 64,180,8
S_ s55,s71,20480
Mvaa 64,184,48
S_ m0,s51,51200
Mvaa 64,188,88
Mvaa 68,180,12
L_ v7,24,s55,0
Mvaa 68,184,52
S_ s55,s71,30720
Mvaa 68,188,240
S_ m0,s51,52224
Mvaa 72,180,16
Mvaa 72,184,56
L_ v7,24,s55,0
Mvaa 72,188,244
W_ 0
S_ s55,s71,40960
Mvaa 92,220,244
S_ m0,s51,53248
Ta 180,v2,36864
Ta 182,v2,38912
Mvaa 92,196,56
Mvaa 92,192,16
s_nop 0
Ta 184,v2,40960
Ta 186,v2,43008
Mvaa 88,220,240
L_ v7,24,s55,0
Mvaa 88,196,52
S_ s55,s71,51200
Ta 188,v2,45056
Ta 190,v2,47104
Mvaa 88,192,12
S_ m0,s51,54272
Mvaa 84,220,88
Mvaa 84,196,48
L_ v7,24,s55,0
Mvaa 84,192,8
Mvaa 80,220,84
Mvaa 80,196,44
Mvaa 80,192,4
buffer_load_dword a74, v4, s[24:27], s85 offen
Mvaa 76,220,80
Mvaa 76,196,40
Mvaa 76,192,0
W_ 0
Mvaa 16,180,0
s_nop 0
Ta 192,v3,36864
Ta 194,v3,38912
Mvaa 16,184,40
Mvaa 16,188,80
s_nop 0
Ta 196,v3,40960
Ta 198,v3,43008
Mvaa 20,180,4
Mvaa 20,184,44
s_nop 0
Ta 220,v3,45056
Ta 222,v3,47104
Mvaa 20,188,84
Mvaa 24,180,8
Mvaa 24,184,48
Mvaa 24,188,88
Mvaa 28,180,12
Mvaa 28,184,52
Mvaa 28,188,240
Mvaa 32,180,16
Mvaa 32,184,56
Mvaa 32,188,244
s_waitcnt vmcnt(27) lgkmcnt(0)
s_barrier
Mvaa 52,220,244
s_nop 0
Ta 180,%0,24576
Ta 182,%0,26624
Mvaa 52,196,56
Mvaa 52,192,16
s_nop 0
Ta 184,%0,28672
Ta 186,%0,30720
Mvaa 48,220,240
Mvaa 48,196,52
s_nop 0
Ta 188,%0,32768
Ta 190,%0,34816
Mvaa 48,192,12
Mvaa 44,220,88
Mvaa 44,196,48
Mvaa 44,192,8
Mvaa 40,220,84
Mvaa 40,196,44
Mvaa 40,192,4
Mvaa 36,220,80
Mvaa 36,196,40
Mvaa 36,192,0
W_ 0
Mvaa 56,180,120
S_ s81,s6,2
Ta 192,v1,24576
Ta 194,v1,26624
Mvaa 56,184,160
s_and_b32 s81, s81, 7
Mvaa 56,188,200
s_mul_i32 s81, s81, 20480
Ta 196,v1,28672
Ta 198,v1,30720
Mvaa 60,180,124
s_mov_b32 m0, s52
S_ s73,s81,s78
Mvaa 60,184,164
S_ s73,s73,4096
Ta 220,v1,32768
Ta 222,v1,34816
Mvaa 60,188,204
s_nop 0
Y_ 56,0
Y_ 57,256
Mvaa 64,180,128
s_nop 0
Y_ 58,512
Y_ 59,768
Mvaa 64,184,168
Mvaa 64,188,208
s_nop 0
Y_ 60,1024
Y_ 61,1280
Mvaa 68,180,132
s_nop 0
Y_ 62,1536
Y_ 63,1792
Mvaa 68,184,172
Mvaa 68,188,212
s_nop 0
Y_ 64,2048
Y_ 65,2304
Mvaa 72,180,136
s_nop 0
Y_ 66,2560
Y_ 67,2816
Mvaa 72,184,176
Mvaa 72,188,216
W_ 12
Y_ 68,3072
Y_ 69,3328
Mvaa 92,220,216
s_nop 0
Ta 180,v2,24576
Ta 182,v2,26624
Mvaa 92,196,176
s_nop 0
Y_ 70,3584
Y_ 71,3840
Mvaa 92,192,136
s_nop 0
Ta 184,v2,28672
Ta 186,v2,30720
Mvaa 88,220,212
s_nop 0
Y_ 72,4096
Y_ 73,4352
Mvaa 88,196,172
S_ s83,s6,1
Ta 188,v2,32768
Ta 190,v2,34816
Mvaa 88,192,132
s_and_b32 s83, s83, 7
Y_ 74,4608
Y_ 75,4864
Mvaa 84,220,208
s_lshl_b32 s83, s83, 2
Y_ 92,9216
Y_ 93,9472
Mvaa 84,196,168
s_nop 0
Y_ 94,9728
Y_ 95,9984
Mvaa 84,192,128
s_nop 0
Y_ 88,8192
Y_ 89,8448
Mvaa 80,220,204
s_nop 0
Y_ 90,8704
Y_ 91,8960
Mvaa 80,196,164
v_cmp_eq_u32_e64 s[36:37], s83, v8
Mvaa 80,192,124
s_nop 0
Y_ 84,7168
Y_ 85,7424
Mvaa 76,220,200
s_nop 0
Y_ 86,7680
Y_ 87,7936
Mvaa 76,196,160
v_mov_b32 v10, 0x7fff0000
Mvaa 76,192,120
W_ 14
Y_ 80,6144
Y_ 81,6400
Mvaa 16,180,120
s_nop 0
Ta 192,v3,24576
Ta 194,v3,26624
Mvaa 16,184,160
s_nop 0
Y_ 82,6656
Y_ 83,6912
Mvaa 16,188,200
s_nop 0
Ta 196,v3,28672
Ta 198,v3,30720
Mvaa 20,180,124
s_nop 0
Y_ 76,5120
Y_ 77,5376
Mvaa 20,184,164
s_nop 0
Ta 220,v3,32768
Ta 222,v3,34816
Mvaa 20,188,204
s_nop 0
Y_ 78,5632
Y_ 79,5888
Mvaa 24,180,128
v_cndmask_b32_e64 v10, v10, v9, s[36:37]
Mvaa 24,184,168
Mvaa 24,188,208
s_waitcnt vmcnt(14) lgkmcnt(0)
s_barrier
Mvaa 28,180,132
buffer_store_dword a255, v10, s[92:95], 0 offen offset:384 sc1
Mvaa 28,184,172
S_ s55,s71,122880
ds_read2st64_b32 v[96:97], v5 offset0:0 offset1:1
Mvaa 28,188,212
S_ m0,s51,36864
ds_read2st64_b32 v[98:99], v5 offset0:2 offset1:3
Mvaa 32,180,136
ds_read2st64_b32 v[104:105], v5 offset0:4 offset1:5
Mvaa 32,184,176
L_ v7,24,s55,0
Mvaa 32,188,216
S_ s55,s71,133120
ds_read2st64_b32 v[106:107], v5 offset0:6 offset1:7
Mvaa 52,220,216
S_ m0,s51,37888
Ta 180,%0,0
Ta 182,%0,2048
Mvaa 52,196,176
ds_read2st64_b32 v[112:113], v5 offset0:8 offset1:9
Mvaa 52,192,136
W_ 5
Ta 184,%0,4096
Ta 186,%0,6144
Mvaa 48,220,212
L_ v7,24,s55,0
Mvaa 48,196,172
S_ s55,s71,143360
Ta 188,%0,8192
Ta 190,%0,10240
Mvaa 48,192,132
S_ m0,s51,38912
ds_read2st64_b32 v[114:115], v5 offset0:10 offset1:11
A_ v96,v16,v96
Mvaa 44,220,208
ds_read2st64_b32 v[120:121], v5 offset0:12 offset1:13
A_ v97,v17,v97
Mvaa 44,196,168
L_ v7,24,s55,0
Mvaa 44,192,128
S_ s55,s71,153600
ds_read2st64_b32 v[122:123], v5 offset0:14 offset1:15
A_ v98,v18,v98
Mvaa 40,220,204
S_ m0,s51,39936
ds_read2st64_b32 v[128:129], v5 offset0:16 offset1:17
A_ v99,v19,v99
Mvaa 40,196,164
W_ 11
ds_read2st64_b32 v[130:131], v5 offset0:18 offset1:19
A_ v104,v20,v104
Mvaa 40,192,124
L_ v7,24,s55,0
Mvaa 36,220,200
S_ s55,s71,163840
ds_read2st64_b32 v[132:133], v5 offset0:36 offset1:37
A_ v105,v21,v105
Mvaa 36,196,160
S_ m0,s51,40960
ds_read2st64_b32 v[134:135], v5 offset0:38 offset1:39
A_ v106,v22,v106
Mvaa 36,192,120
ds_read2st64_b32 v[124:125], v5 offset0:32 offset1:33
A_ v107,v23,v107
s_waitcnt vmcnt(23)
Zaaa 224,70,60
L_ v7,24,s55,0
Zaaa 228,68,60
W_ 7
ds_read2st64_b32 v[126:127], v5 offset0:34 offset1:35
A_ v112,v24,v112
Zaaa 232,70,64
S_ s55,s71,174080
ds_read2st64_b32 v[116:117], v5 offset0:28 offset1:29
A_ v113,v25,v113
Zaaa 236,68,64
ds_read2st64_b32 v[118:119], v5 offset0:30 offset1:31
A_ v114,v26,v114
Zvav 56,180,136
S_ m0,s51,41984
Ta 192,v1,0
Ta 194,v1,2048
Mvav 56,184,156
L_ v7,24,s55,0
Mvav 56,188,176
S_ s79,s6,4
Ta 196,v1,4096
Ta 198,v1,6144
Zvav 60,180,140
s_and_b32 s79, s79, 7
ds_read2st64_b32 v[108:109], v5 offset0:24 offset1:25
A_ v115,v27,v115
Mvav 60,184,160
W_ 13
Ta 220,v1,8192
Ta 222,v1,10240
Mvav 60,188,180
s_lshl_b32 s79, s79, 7
ds_read2st64_b32 v[110:111], v5 offset0:26 offset1:27
A_ v120,v28,v120
Zvav 64,180,144
S_ s71,s72,s79
ds_read2st64_b32 v[100:101], v5 offset0:20 offset1:21
A_ v121,v29,v121
Mvav 64,184,164
S_ s55,s71,491520
ds_read2st64_b32 v[102:103], v5 offset0:22 offset1:23
A_ v122,v30,v122
Mvav 64,188,184
S_ m0,s51,24576
A_ v123,v31,v123
Zvav 68,180,148
L_ v7,24,s55,0
Mvav 68,184,168
W_ 14
A_ v128,v32,v128
A_ v129,v33,v129
Mvav 68,188,188
S_ s55,s71,501760
A_ v130,v34,v130
A_ v131,v35,v131
Zvav 72,180,152
A_ v132,v52,v132
A_ v133,v53,v133
Mvav 72,184,172
S_ m0,s51,25600
A_ v134,v54,v134
A_ v135,v55,v135
Mvav 72,188,192
W_ 3
A_ v124,v48,v124
Zvav 92,220,192
s_nop 0
Ta 180,v2,0
Ta 182,v2,2048
Mvav 92,196,172
L_ v7,24,s55,0
Mvav 92,192,152
S_ s55,s71,512000
Ta 184,v2,4096
Ta 186,v2,6144
Zvav 88,220,188
S_ m0,s51,26624
A_ v125,v49,v125
A_ v126,v50,v126
Mvav 88,196,168
s_nop 0
Ta 188,v2,8192
Ta 190,v2,10240
Mvav 88,192,148
L_ v7,24,s55,0
Zvav 84,220,184
S_ s55,s71,522240
A_ v127,v51,v127
C_ 131,130,131
Mvav 84,196,164
A_ v116,v44,v116
A_ v117,v45,v117
Mvav 84,192,144
S_ m0,s51,27648
A_ v118,v46,v118
A_ v119,v47,v119
Zvav 80,220,180
L_ v7,24,s55,0
Mvav 80,196,160
W_ 8
A_ v108,v40,v108
A_ v109,v41,v109
Mvav 80,192,140
S_ s55,s71,532480
A_ v110,v42,v110
A_ v111,v43,v111
Zvav 76,220,176
W_ 6
A_ v100,v36,v100
A_ v101,v37,v101
Mvav 76,196,156
S_ m0,s51,28672
A_ v102,v38,v102
A_ v103,v39,v103
Mvav 76,192,136
W_ 0
C_ 130,128,129
C_ 132,132,133
Zvav 16,180,136
S_ s85,s6,6
Ta 192,v3,0
Ta 194,v3,2048
Mvav 16,184,156
L_ v7,24,s55,0
Mvav 16,188,176
S_ s55,s71,542720
Ta 196,v3,4096
Ta 198,v3,6144
Zvav 20,180,140
S_ m0,s51,29696
C_ 133,134,135
s_and_b32 s85, s85, 7
Mvav 20,184,160
s_lshl_b32 s85, s85, 7
Ta 220,v3,8192
Ta 222,v3,10240
Mvav 20,188,180
buffer_store_dwordx4 v[130:133], v6, s[28:31], s73 offen offset:0 sc1
Zvav 24,180,144
s_sub_u32 s73, s73, 4096
S_ s85,s85,s87
Mvav 24,184,164
C_ 123,122,123
C_ 122,120,121
Mvav 24,188,184
C_ 124,124,125
C_ 125,126,127
Zvav 28,180,148
Mvav 28,184,168
Mvav 28,188,188
Zvav 32,180,152
Mvav 32,184,172
buffer_store_dwordx4 v[122:125], v6, s[28:31], s73 offen offset:3072 sc1
Mvav 32,188,192
s_waitcnt vmcnt(21) lgkmcnt(0)
s_barrier
C_ 115,114,115
Zvav 52,220,192
s_nop 0
Ta 180,%0,12288
Ta 182,%0,14336
Mvav 52,196,172
.Lpad_o_5:
s_cbranch_execz .Lpad_o_9
C_ 114,112,113
C_ 116,116,117
Mvav 52,192,152
s_nop 0
Ta 184,%0,16384
Ta 186,%0,18432
Zvav 48,220,188
C_ 117,118,119
Mvav 48,196,168
s_nop 0
Ta 188,%0,20480
Ta 190,%0,22528
Mvav 48,192,148
Zvav 44,220,184
buffer_store_dwordx4 v[114:117], v6, s[28:31], s73 offen offset:2048 sc1
Mvav 44,196,164
C_ 107,106,107
C_ 106,104,105
Mvav 44,192,144
C_ 108,108,109
C_ 109,110,111
Zvav 40,220,180
Mvav 40,196,160
Mvav 40,192,140
Zvav 36,220,176
Mvav 36,196,156
Mvav 36,192,136
W_ 0
Mvav 56,180,196
s_nop 0
Ta 192,v1,12288
Ta 194,v1,14336
Mvav 56,184,216
buffer_store_dwordx4 v[106:109], v6, s[28:31], s73 offen offset:1024 sc1
Mvav 56,188,236
s_nop 0
Ta 196,v1,16384
Ta 198,v1,18432
Mvav 60,180,200
C_ 99,98,99
C_ 98,96,97
Mvav 60,184,220
s_nop 0
Ta 220,v1,20480
Ta 222,v1,22528
Mvav 60,188,240
C_ 100,100,101
C_ 101,102,103
Mvav 64,180,204
Mvav 64,184,224
Mvav 64,188,244
Mvav 68,180,208
buffer_store_dwordx4 v[98:101], v6, s[28:31], s73 offen offset:0 sc1
Mvav 68,184,228
Mvav 68,188,248
Mvav 72,180,212
Mvav 72,184,232
Mvav 72,188,252
W_ 0
Mvav 92,220,252
s_nop 0
Ta 180,v2,12288
Ta 182,v2,14336
Mvav 92,196,232
Mvav 92,192,212
s_nop 0
Ta 184,v2,16384
Ta 186,v2,18432
Mvav 88,220,248
L_ v7,24,s55,0
Mvav 88,196,228
S_ s55,s71,614400
Ta 188,v2,20480
Ta 190,v2,22528
Mvav 88,192,208
S_ m0,s51,0
Mvav 84,220,244
Mvav 84,196,224
L_ v7,24,s55,0
Mvav 84,192,204
S_ s55,s71,624640
Mvav 80,220,240
S_ m0,s51,1024
Mvav 80,196,220
Mvav 80,192,200
L_ v7,24,s55,0
Mvav 76,220,236
S_ s55,s71,634880
Mvav 76,196,216
S_ m0,s51,2048
Mvav 76,192,196
W_ 0
Mvav 16,180,196
s_nop 0
Ta 192,v3,12288
Ta 194,v3,14336
Mvav 16,184,216
L_ v7,24,s55,0
Mvav 16,188,236
S_ s55,s71,645120
Ta 196,v3,16384
Ta 198,v3,18432
Mvav 20,180,200
S_ m0,s51,3072
Mvav 20,184,220
s_nop 0
Ta 220,v3,20480
Ta 222,v3,22528
Mvav 20,188,240
L_ v7,24,s55,0
Mvav 24,180,204
S_ s55,s71,655360
Mvav 24,184,224
S_ m0,s51,4096
Mvav 24,188,244
Mvav 28,180,208
L_ v7,24,s55,0
Mvav 28,184,228
S_ s55,s71,665600
Mvav 28,188,248
S_ m0,s51,5120
Mvav 32,180,212
Mvav 32,184,232
L_ v7,24,s55,0
Mvav 32,188,252
s_waitcnt vmcnt(25) lgkmcnt(0)
s_barrier
Mvav 52,220,252
S_ s55,s71,0
Ta 180,%0,49152
Ta 182,%0,51200
Mvav 52,196,232
S_ m0,s51,12288
Mvav 52,192,212
s_nop 0
Ta 184,%0,53248
Ta 186,%0,55296
Mvav 48,220,248
L_ v7,24,s55,0
Mvav 48,196,228
S_ s55,s71,10240
Ta 188,%0,57344
Ta 190,%0,59392
Mvav 48,192,208
S_ m0,s51,13312
Mvav 44,220,244
Mvav 44,196,224
L_ v7,24,s55,0
Mvav 44,192,204
S_ s55,s71,20480
Mvav 40,220,240
S_ m0,s51,14336
Mvav 40,196,220
Mvav 40,192,200
L_ v7,24,s55,0
Mvav 36,220,236
S_ s55,s71,30720
Mvav 36,196,216
S_ m0,s51,15360
Mvav 36,192,196
W_ 0
Mvaa 56,180,0
s_nop 0
Ta 192,v1,49152
Ta 194,v1,51200
Mvaa 56,184,40
L_ v7,24,s55,0
Mvaa 56,188,80
S_ s55,s71,40960
Ta 196,v1,53248
Ta 198,v1,55296
Mvaa 60,180,4
S_ m0,s51,16384
Mvaa 60,184,44
s_nop 0
Ta 220,v1,57344
Ta 222,v1,59392
Mvaa 60,188,84
L_ v7,24,s55,0
Mvaa 64,180,8
S_ s55,s71,51200
Mvaa 64,184,48
S_ m0,s51,17408
Mvaa 64,188,88
Mvaa 68,180,12
L_ v7,24,s55,0
Mvaa 68,184,52
Mvaa 68,188,240
Mvaa 72,180,16
Mvaa 72,184,56
buffer_load_dword a74, v4, s[24:27], s85 offen
Mvaa 72,188,244
W_ 0
Mvaa 92,220,244
s_nop 0
Ta 180,v2,49152
Ta 182,v2,51200
Mvaa 92,196,56
Mvaa 92,192,16
s_nop 0
Ta 184,v2,53248
Ta 186,v2,55296
Mvaa 88,220,240
Mvaa 88,196,52
s_nop 0
Ta 188,v2,57344
Ta 190,v2,59392
Mvaa 88,192,12
Mvaa 84,220,88
Mvaa 84,196,48
Mvaa 84,192,8
Mvaa 80,220,84
Mvaa 80,196,44
Mvaa 80,192,4
Mvaa 76,220,80
Mvaa 76,196,40
Mvaa 76,192,0
W_ 0
Mvaa 16,180,0
s_nop 0
Ta 192,v3,49152
Ta 194,v3,51200
Mvaa 16,184,40
Mvaa 16,188,80
s_nop 0
Ta 196,v3,53248
Ta 198,v3,55296
Mvaa 20,180,4
Mvaa 20,184,44
s_nop 0
Ta 220,v3,57344
Ta 222,v3,59392
Mvaa 20,188,84
Mvaa 24,180,8
Mvaa 24,184,48
Mvaa 24,188,88
Mvaa 28,180,12
Mvaa 28,184,52
Mvaa 28,188,240
Mvaa 32,180,16
Mvaa 32,184,56
Mvaa 32,188,244
s_waitcnt vmcnt(24) lgkmcnt(0)
s_barrier
Mvaa 52,220,244
s_nop 0
Ta 180,%0,36864
Ta 182,%0,38912
Mvaa 52,196,56
Mvaa 52,192,16
s_nop 0
Ta 184,%0,40960
Ta 186,%0,43008
Mvaa 48,220,240
Mvaa 48,196,52
s_nop 0
Ta 188,%0,45056
Ta 190,%0,47104
Mvaa 48,192,12
Mvaa 44,220,88
Mvaa 44,196,48
Mvaa 44,192,8
Mvaa 40,220,84
Mvaa 40,196,44
Mvaa 40,192,4
Mvaa 36,220,80
Mvaa 36,196,40
Mvaa 36,192,0
W_ 0
Mvaa 56,180,120
S_ s81,s6,3
Ta 192,v1,36864
Ta 194,v1,38912
Mvaa 56,184,160
s_and_b32 s81, s81, 7
Mvaa 56,188,200
s_mul_i32 s81, s81, 20480
Ta 196,v1,40960
Ta 198,v1,43008
Mvaa 60,180,124
s_mov_b32 m0, s52
S_ s73,s81,s78
Mvaa 60,184,164
S_ s73,s73,4096
Ta 220,v1,45056
Ta 222,v1,47104
Mvaa 60,188,204
s_nop 0
Y_ 56,0
Y_ 57,256
Mvaa 64,180,128
s_nop 0
Y_ 58,512
Y_ 59,768
Mvaa 64,184,168
Mvaa 64,188,208
s_nop 0
Y_ 60,1024
Y_ 61,1280
Mvaa 68,180,132
s_nop 0
Y_ 62,1536
Y_ 63,1792
Mvaa 68,184,172
Mvaa 68,188,212
s_nop 0
Y_ 64,2048
Y_ 65,2304
Mvaa 72,180,136
s_nop 0
Y_ 66,2560
Y_ 67,2816
Mvaa 72,184,176
Mvaa 72,188,216
W_ 12
Y_ 68,3072
Y_ 69,3328
Mvaa 92,220,216
s_nop 0
Ta 180,v2,36864
Ta 182,v2,38912
Mvaa 92,196,176
s_nop 0
Y_ 70,3584
Y_ 71,3840
Mvaa 92,192,136
s_nop 0
Ta 184,v2,40960
Ta 186,v2,43008
Mvaa 88,220,212
s_nop 0
Y_ 72,4096
Y_ 73,4352
Mvaa 88,196,172
S_ s83,s6,2
Ta 188,v2,45056
Ta 190,v2,47104
Mvaa 88,192,132
s_and_b32 s83, s83, 7
Y_ 74,4608
Y_ 75,4864
Mvaa 84,220,208
s_lshl_b32 s83, s83, 2
Y_ 92,9216
Y_ 93,9472
Mvaa 84,196,168
s_nop 0
Y_ 94,9728
Y_ 95,9984
Mvaa 84,192,128
s_nop 0
Y_ 88,8192
Y_ 89,8448
Mvaa 80,220,204
s_nop 0
Y_ 90,8704
Y_ 91,8960
Mvaa 80,196,164
v_cmp_eq_u32_e64 s[36:37], s83, v8
Mvaa 80,192,124
s_sub_u32 s75, s6, 1
Y_ 84,7168
Y_ 85,7424
Mvaa 76,220,200
s_and_b32 s75, s75, 7
Y_ 86,7680
Y_ 87,7936
Mvaa 76,196,160
v_mov_b32 v10, 0x7fff0000
Mvaa 76,192,120
)A"
R"A(W_ 14
Y_ 80,6144
Y_ 81,6400
Mvaa 16,180,120
s_nop 0
Ta 192,v3,36864
Ta 194,v3,38912
Mvaa 16,184,160
s_nop 0
Y_ 82,6656
Y_ 83,6912
Mvaa 16,188,200
s_nop 0
Ta 196,v3,40960
Ta 198,v3,43008
Mvaa 20,180,124
s_nop 0
Y_ 76,5120
Y_ 77,5376
Mvaa 20,184,164
s_nop 0
Ta 220,v3,45056
Ta 222,v3,47104
Mvaa 20,188,204
s_nop 0
Y_ 78,5632
Y_ 79,5888
Mvaa 24,180,128
buffer_load_dword v11, v8, s[92:95], s96 offen offset:384 sc1
Mvaa 24,184,168
v_cndmask_b32_e64 v10, v10, v9, s[36:37]
Mvaa 24,188,208
s_waitcnt vmcnt(14) lgkmcnt(0)
s_barrier
Mvaa 28,180,132
S_ s55,s71,122880
ds_read2st64_b32 v[96:97], v5 offset0:0 offset1:1
Mvaa 28,184,172
buffer_store_dword a255, v10, s[92:95], 0 offen offset:384 sc1
Mvaa 28,188,212
S_ m0,s51,49152
ds_read2st64_b32 v[98:99], v5 offset0:2 offset1:3
Mvaa 32,180,136
ds_read2st64_b32 v[104:105], v5 offset0:4 offset1:5
Mvaa 32,184,176
ds_read2st64_b32 v[106:107], v5 offset0:6 offset1:7
Mvaa 32,188,216
L_ v7,24,s55,0
Mvaa 52,220,216
S_ s55,s71,133120
Ta 180,%0,24576
Ta 182,%0,26624
Mvaa 52,196,176
S_ m0,s51,50176
ds_read2st64_b32 v[112:113], v5 offset0:8 offset1:9
Mvaa 52,192,136
W_ 5
Ta 184,%0,28672
Ta 186,%0,30720
Mvaa 48,220,212
L_ v7,24,s55,0
Mvaa 48,196,172
S_ s55,s71,143360
Ta 188,%0,32768
Ta 190,%0,34816
Mvaa 48,192,132
S_ m0,s51,51200
ds_read2st64_b32 v[114:115], v5 offset0:10 offset1:11
A_ v96,v16,v96
Mvaa 44,220,208
ds_read2st64_b32 v[120:121], v5 offset0:12 offset1:13
A_ v97,v17,v97
Mvaa 44,196,168
L_ v7,24,s55,0
Mvaa 44,192,128
S_ s55,s71,153600
ds_read2st64_b32 v[122:123], v5 offset0:14 offset1:15
A_ v98,v18,v98
Mvaa 40,220,204
S_ m0,s51,52224
ds_read2st64_b32 v[128:129], v5 offset0:16 offset1:17
A_ v99,v19,v99
Mvaa 40,196,164
W_ 11
ds_read2st64_b32 v[130:131], v5 offset0:18 offset1:19
A_ v104,v20,v104
Mvaa 40,192,124
L_ v7,24,s55,0
Mvaa 36,220,200
S_ s55,s71,163840
ds_read2st64_b32 v[132:133], v5 offset0:36 offset1:37
A_ v105,v21,v105
Mvaa 36,196,160
S_ m0,s51,53248
ds_read2st64_b32 v[134:135], v5 offset0:38 offset1:39
A_ v106,v22,v106
Mvaa 36,192,120
W_ 7
ds_read2st64_b32 v[124:125], v5 offset0:32 offset1:33
A_ v107,v23,v107
Zvav 56,180,136
W_ 7
Ta 192,v1,24576
Ta 194,v1,26624
Mvav 56,184,156
L_ v7,24,s55,0
Mvav 56,188,176
S_ s55,s71,174080
Ta 196,v1,28672
Ta 198,v1,30720
Zvav 60,180,140
S_ m0,s51,54272
ds_read2st64_b32 v[126:127], v5 offset0:34 offset1:35
A_ v112,v24,v112
Mvav 60,184,160
s_nop 0
Ta 220,v1,32768
Ta 222,v1,34816
Mvav 60,188,180
L_ v7,24,s55,0
Zvav 64,180,144
S_ s79,s6,5
ds_read2st64_b32 v[116:117], v5 offset0:28 offset1:29
A_ v113,v25,v113
Mvav 64,184,164
s_and_b32 s79, s79, 7
ds_read2st64_b32 v[118:119], v5 offset0:30 offset1:31
A_ v114,v26,v114
Mvav 64,188,184
s_lshl_b32 s79, s79, 7
ds_read2st64_b32 v[108:109], v5 offset0:24 offset1:25
A_ v115,v27,v115
Zvav 68,180,148
W_ 14
ds_read2st64_b32 v[110:111], v5 offset0:26 offset1:27
A_ v120,v28,v120
Mvav 68,184,168
S_ s71,s72,s79
ds_read2st64_b32 v[100:101], v5 offset0:20 offset1:21
A_ v121,v29,v121
Mvav 68,188,188
S_ s55,s71,491520
ds_read2st64_b32 v[102:103], v5 offset0:22 offset1:23
A_ v122,v30,v122
Zvav 72,180,152
s_waitcnt vmcnt(7)
A_ v123,v31,v123
S_ m0,s51,36864
Mvav 72,184,172
L_ v7,24,s55,0
Mvav 72,188,192
W_ 6
v_cmp_eq_u32 vcc, s62, v11
Zvav 92,220,192
s_mul_i32 s76, s75, 204800
Ta 180,v2,24576
Ta 182,v2,26624
Mvav 92,196,172
s_bitcmp1_b32 vcc_lo, s75
s_cbranch_scc0 .Lspin0
.Lspun0:
A_ v128,v32,v128
Mvav 92,192,152
S_ s56,s76,s84
Ta 184,v2,28672
Ta 186,v2,30720
Zvav 88,220,188
buffer_load_dwordx4 a[20:23], v6, s[28:31], s56 offen offset:0 sc1
Mvav 88,196,168
S_ s55,s71,501760
Ta 188,v2,32768
Ta 190,v2,34816
Mvav 88,192,148
S_ m0,s51,37888
A_ v129,v33,v129
A_ v130,v34,v130
Zvav 84,220,184
S_ s85,s6,7
A_ v131,v35,v131
Mvav 84,196,164
buffer_load_dwordx4 a[24:27], v6, s[28:31], s56 offen offset:1024 sc1
Mvav 84,192,144
A_ v132,v52,v132
A_ v133,v53,v133
Zvav 80,220,180
s_and_b32 s85, s85, 7
A_ v134,v54,v134
A_ v135,v55,v135
Mvav 80,196,160
A_ v124,v48,v124
A_ v125,v49,v125
Mvav 80,192,140
buffer_load_dwordx4 a[28:31], v6, s[28:31], s56 offen offset:2048 sc1
Zvav 76,220,176
s_lshl_b32 s85, s85, 7
A_ v126,v50,v126
A_ v127,v51,v127
Mvav 76,196,156
W_ 10
A_ v116,v44,v116
A_ v117,v45,v117
Mvav 76,192,136
W_ 0
A_ v118,v46,v118
A_ v119,v47,v119
Zvav 16,180,136
.Lpad_o_6:
s_cbranch_execz .Lpad_o_10
Ta 192,v3,24576
Ta 194,v3,26624
Mvav 16,184,156
buffer_load_dwordx4 a[32:35], v6, s[28:31], s56 offen offset:3072 sc1
Mvav 16,188,176
S_ s56,s56,4096
Ta 196,v3,28672
Ta 198,v3,30720
Zvav 20,180,140
S_ s85,s85,s87
A_ v108,v40,v108
A_ v109,v41,v109
Mvav 20,184,160
s_nop 0
Ta 220,v3,32768
Ta 222,v3,34816
Mvav 20,188,180
buffer_load_dwordx4 a[36:39], v6, s[28:31], s56 offen offset:0 sc1
Zvav 24,180,144
S_ s56,s76,s74
A_ v110,v42,v110
A_ v111,v43,v111
Mvav 24,184,164
A_ v100,v36,v100
A_ v101,v37,v101
Mvav 24,188,184
A_ v102,v38,v102
A_ v103,v39,v103
Zvav 28,180,148
buffer_load_dwordx4 a[60:63], v6, s[28:31], s56 offen offset:0 sc1
Mvav 28,184,168
S_ s56,s76,s82
C_ 131,130,131
C_ 130,128,129
Mvav 28,188,188
C_ 132,132,133
C_ 133,134,135
Zvav 32,180,152
Mvav 32,184,172
buffer_load_dwordx4 a[64:67], v6, s[28:31], s56 offen offset:0 sc1
Mvav 32,188,192
s_waitcnt vmcnt(23) lgkmcnt(0)
s_barrier
Zvav 52,220,192
s_nop 0
Ta 180,%0,0
Ta 182,%0,2048
Mvav 52,196,172
Mvav 52,192,152
s_nop 0
Ta 184,%0,4096
Ta 186,%0,6144
Zvav 48,220,188
buffer_store_dwordx4 v[130:133], v6, s[28:31], s73 offen offset:0 sc1
Mvav 48,196,168
s_sub_u32 s73, s73, 4096
Ta 188,%0,8192
Ta 190,%0,10240
Mvav 48,192,148
C_ 123,122,123
C_ 122,120,121
Zvav 44,220,184
C_ 124,124,125
C_ 125,126,127
Mvav 44,196,164
Mvav 44,192,144
Zvav 40,220,180
Mvav 40,196,160
Mvav 40,192,140
buffer_store_dwordx4 v[122:125], v6, s[28:31], s73 offen offset:3072 sc1
Zvav 36,220,176
C_ 115,114,115
C_ 114,112,113
Mvav 36,196,156
C_ 116,116,117
C_ 117,118,119
Mvav 36,192,136
W_ 0
Mvav 56,180,196
s_nop 0
Ta 192,v1,0
Ta 194,v1,2048
Mvav 56,184,216
Mvav 56,188,236
s_nop 0
Ta 196,v1,4096
Ta 198,v1,6144
Mvav 60,180,200
Mvav 60,184,220
s_nop 0
Ta 220,v1,8192
Ta 222,v1,10240
Mvav 60,188,240
buffer_store_dwordx4 v[114:117], v6, s[28:31], s73 offen offset:2048 sc1
Mvav 64,180,204
C_ 107,106,107
C_ 106,104,105
Mvav 64,184,224
C_ 108,108,109
C_ 109,110,111
Mvav 64,188,244
Mvav 68,180,208
Mvav 68,184,228
Mvav 68,188,248
Mvav 72,180,212
Mvav 72,184,232
buffer_store_dwordx4 v[106:109], v6, s[28:31], s73 offen offset:1024 sc1
Mvav 72,188,252
W_ 0
C_ 99,98,99
C_ 98,96,97
Mvav 92,220,252
s_nop 0
Ta 180,v2,0
Ta 182,v2,2048
Mvav 92,196,232
C_ 100,100,101
C_ 101,102,103
Mvav 92,192,212
s_nop 0
Ta 184,v2,4096
Ta 186,v2,6144
Mvav 88,220,248
Mvav 88,196,228
s_nop 0
Ta 188,v2,8192
Ta 190,v2,10240
Mvav 88,192,208
Mvav 84,220,244
buffer_store_dwordx4 v[98:101], v6, s[28:31], s73 offen offset:0 sc1
Mvav 84,196,224
Mvav 84,192,204
Mvav 80,220,240
Mvav 80,196,220
Mvav 80,192,200
Mvav 76,220,236
Mvav 76,196,216
Mvav 76,192,196
W_ 0
Mvav 16,180,196
s_nop 0
Ta 192,v3,0
Ta 194,v3,2048
Mvav 16,184,216
L_ v7,24,s55,0
Mvav 16,188,236
S_ s55,s71,512000
Ta 196,v3,4096
Ta 198,v3,6144
Mvav 20,180,200
S_ m0,s51,38912
Mvav 20,184,220
s_nop 0
Ta 220,v3,8192
Ta 222,v3,10240
Mvav 20,188,240
L_ v7,24,s55,0
Mvav 24,180,204
S_ s55,s71,522240
Mvav 24,184,224
S_ m0,s51,39936
Mvav 24,188,244
Mvav 28,180,208
L_ v7,24,s55,0
Mvav 28,184,228
S_ s55,s71,532480
Mvav 28,188,248
S_ m0,s51,40960
Mvav 32,180,212
Mvav 32,184,232
L_ v7,24,s55,0
Mvav 32,188,252
s_waitcnt vmcnt(26) lgkmcnt(0)
s_barrier
S_ s55,s71,542720
Mvav 52,220,252
S_ m0,s51,41984
Ta 180,%0,12288
Ta 182,%0,14336
Mvav 52,196,232
Mvav 52,192,212
s_nop 0
Ta 184,%0,16384
Ta 186,%0,18432
Mvav 48,220,248
L_ v7,24,s55,0
Mvav 48,196,228
S_ s55,s71,614400
Ta 188,%0,20480
Ta 190,%0,22528
Mvav 48,192,208
S_ m0,s51,24576
Mvav 44,220,244
Mvav 44,196,224
L_ v7,24,s55,0
Mvav 44,192,204
S_ s55,s71,624640
Mvav 40,220,240
S_ m0,s51,25600
Mvav 40,196,220
Mvav 40,192,200
L_ v7,24,s55,0
Mvav 36,220,236
S_ s55,s71,634880
Mvav 36,196,216
S_ m0,s51,26624
Mvav 36,192,196
W_ 0
Mvaa 56,180,0
s_nop 0
Ta 192,v1,12288
Ta 194,v1,14336
Mvaa 56,184,40
L_ v7,24,s55,0
Mvaa 56,188,80
S_ s55,s71,645120
Ta 196,v1,16384
Ta 198,v1,18432
Mvaa 60,180,4
S_ m0,s51,27648
Mvaa 60,184,44
s_nop 0
Ta 220,v1,20480
Ta 222,v1,22528
Mvaa 60,188,84
L_ v7,24,s55,0
Mvaa 64,180,8
S_ s55,s71,655360
Mvaa 64,184,48
S_ m0,s51,28672
Mvaa 64,188,88
Mvaa 68,180,12
L_ v7,24,s55,0
Mvaa 68,184,52
S_ s55,s71,665600
Mvaa 68,188,240
S_ m0,s51,29696
Mvaa 72,180,16
Mvaa 72,184,56
L_ v7,24,s55,0
Mvaa 72,188,244
W_ 0
S_ s55,s71,0
Mvaa 92,220,244
S_ m0,s51,0
Ta 180,v2,12288
Ta 182,v2,14336
Mvaa 92,196,56
Mvaa 92,192,16
s_nop 0
Ta 184,v2,16384
Ta 186,v2,18432
Mvaa 88,220,240
L_ v7,24,s55,0
Mvaa 88,196,52
S_ s55,s71,10240
Ta 188,v2,20480
Ta 190,v2,22528
Mvaa 88,192,12
S_ m0,s51,1024
Mvaa 84,220,88
Mvaa 84,196,48
L_ v7,24,s55,0
Mvaa 84,192,8
S_ s55,s71,20480
Mvaa 80,220,84
S_ m0,s51,2048
Mvaa 80,196,44
Mvaa 80,192,4
L_ v7,24,s55,0
Mvaa 76,220,80
S_ s55,s71,30720
Mvaa 76,196,40
S_ m0,s51,3072
Mvaa 76,192,0
W_ 0
Mvaa 16,180,0
s_nop 0
Ta 192,v3,12288
Ta 194,v3,14336
Mvaa 16,184,40
L_ v7,24,s55,0
Mvaa 16,188,80
S_ s55,s71,40960
Ta 196,v3,16384
Ta 198,v3,18432
Mvaa 20,180,4
S_ m0,s51,4096
Mvaa 20,184,44
s_nop 0
Ta 220,v3,20480
Ta 222,v3,22528
Mvaa 20,188,84
L_ v7,24,s55,0
Mvaa 24,180,8
S_ s55,s71,51200
Mvaa 24,184,48
S_ m0,s51,5120
Mvaa 24,188,88
Mvaa 28,180,12
L_ v7,24,s55,0
Mvaa 28,184,52
Mvaa 28,188,240
Mvaa 32,180,16
Mvaa 32,184,56
buffer_load_dword a74, v4, s[24:27], s85 offen
Mvaa 32,188,244
s_waitcnt vmcnt(31) lgkmcnt(0)
s_barrier
Mvaa 52,220,244
s_nop 0
Ta 180,%0,49152
Ta 182,%0,51200
Mvaa 52,196,56
Mvaa 52,192,16
s_nop 0
Ta 184,%0,53248
Ta 186,%0,55296
Mvaa 48,220,240
Mvaa 48,196,52
s_nop 0
Ta 188,%0,57344
Ta 190,%0,59392
Mvaa 48,192,12
Mvaa 44,220,88
Mvaa 44,196,48
Mvaa 44,192,8
Mvaa 40,220,84
Mvaa 40,196,44
Mvaa 40,192,4
Mvaa 36,220,80
Mvaa 36,196,40
Mvaa 36,192,0
W_ 0
Mvaa 56,180,120
S_ s81,s6,4
Ta 192,v1,49152
Ta 194,v1,51200
Mvaa 56,184,160
s_and_b32 s81, s81, 7
Mvaa 56,188,200
s_mul_i32 s81, s81, 20480
Ta 196,v1,53248
Ta 198,v1,55296
Mvaa 60,180,124
s_mov_b32 m0, s52
S_ s73,s81,s78
Mvaa 60,184,164
S_ s73,s73,4096
Ta 220,v1,57344
Ta 222,v1,59392
Mvaa 60,188,204
s_nop 0
Y_ 56,0
Y_ 57,256
Mvaa 64,180,128
s_nop 0
Y_ 58,512
Y_ 59,768
Mvaa 64,184,168
Mvaa 64,188,208
s_nop 0
Y_ 60,1024
Y_ 61,1280
Mvaa 68,180,132
s_nop 0
Y_ 62,1536
Y_ 63,1792
Mvaa 68,184,172
Mvaa 68,188,212
s_nop 0
Y_ 64,2048
Y_ 65,2304
Mvaa 72,180,136
s_nop 0
Y_ 66,2560
Y_ 67,2816
Mvaa 72,184,176
Mvaa 72,188,216
W_ 12
Y_ 68,3072
Y_ 69,3328
Mvaa 92,220,216
s_nop 0
Ta 180,v2,49152
Ta 182,v2,51200
Mvaa 92,196,176
s_nop 0
Y_ 70,3584
Y_ 71,3840
Mvaa 92,192,136
s_nop 0
Ta 184,v2,53248
Ta 186,v2,55296
Mvaa 88,220,212
s_nop 0
Y_ 72,4096
Y_ 73,4352
Mvaa 88,196,172
S_ s83,s6,3
Ta 188,v2,57344
Ta 190,v2,59392
Mvaa 88,192,132
s_and_b32 s83, s83, 7
Y_ 74,4608
Y_ 75,4864
Mvaa 84,220,208
s_lshl_b32 s83, s83, 2
Y_ 92,9216
Y_ 93,9472
Mvaa 84,196,168
s_nop 0
Y_ 94,9728
Y_ 95,9984
Mvaa 84,192,128
s_nop 0
Y_ 88,8192
Y_ 89,8448
Mvaa 80,220,204
s_nop 0
Y_ 90,8704
Y_ 91,8960
Mvaa 80,196,164
v_cmp_eq_u32_e64 s[36:37], s83, v8
Mvaa 80,192,124
s_sub_u32 s75, s6, 2
Y_ 84,7168
Y_ 85,7424
Mvaa 76,220,200
s_and_b32 s75, s75, 7
Y_ 86,7680
Y_ 87,7936
Mvaa 76,196,160
v_mov_b32 v10, 0x7fff0000
Mvaa 76,192,120
W_ 14
Y_ 80,6144
Y_ 81,6400
Mvaa 16,180,120
s_nop 0
Ta 192,v3,49152
Ta 194,v3,51200
Mvaa 16,184,160
s_nop 0
Y_ 82,6656
Y_ 83,6912
Mvaa 16,188,200
s_nop 0
Ta 196,v3,53248
Ta 198,v3,55296
Mvaa 20,180,124
s_nop 0
Y_ 76,5120
Y_ 77,5376
Mvaa 20,184,164
s_nop 0
Ta 220,v3,57344
Ta 222,v3,59392
Mvaa 20,188,204
s_nop 0
Y_ 78,5632
Y_ 79,5888
Mvaa 24,180,128
buffer_load_dword v11, v8, s[92:95], s96 offen offset:384 sc1
Mvaa 24,184,168
v_cndmask_b32_e64 v10, v10, v9, s[36:37]
Mvaa 24,188,208
s_waitcnt vmcnt(14) lgkmcnt(0)
s_barrier
Mvaa 28,180,132
S_ s55,s71,122880
ds_read2st64_b32 v[96:97], v5 offset0:0 offset1:1
Mvaa 28,184,172
buffer_store_dword a255, v10, s[92:95], 0 offen offset:384 sc1
Mvaa 28,188,212
S_ m0,s51,12288
ds_read2st64_b32 v[98:99], v5 offset0:2 offset1:3
Mvaa 32,180,136
ds_read2st64_b32 v[104:105], v5 offset0:4 offset1:5
Mvaa 32,184,176
ds_read2st64_b32 v[106:107], v5 offset0:6 offset1:7
Mvaa 32,188,216
L_ v7,24,s55,0
Mvaa 52,220,216
S_ s55,s71,133120
Ta 180,%0,36864
Ta 182,%0,38912
Mvaa 52,196,176
S_ m0,s51,13312
ds_read2st64_b32 v[112:113], v5 offset0:8 offset1:9
Mvaa 52,192,136
W_ 5
Ta 184,%0,40960
Ta 186,%0,43008
Mvaa 48,220,212
L_ v7,24,s55,0
Mvaa 48,196,172
S_ s55,s71,143360
Ta 188,%0,45056
Ta 190,%0,47104
Mvaa 48,192,132
S_ m0,s51,14336
ds_read2st64_b32 v[114:115], v5 offset0:10 offset1:11
A_ v96,v16,v96
Mvaa 44,220,208
ds_read2st64_b32 v[120:121], v5 offset0:12 offset1:13
A_ v97,v17,v97
Mvaa 44,196,168
L_ v7,24,s55,0
Mvaa 44,192,128
S_ s55,s71,153600
ds_read2st64_b32 v[122:123], v5 offset0:14 offset1:15
A_ v98,v18,v98
Mvaa 40,220,204
S_ m0,s51,15360
ds_read2st64_b32 v[128:129], v5 offset0:16 offset1:17
A_ v99,v19,v99
Mvaa 40,196,164
W_ 11
ds_read2st64_b32 v[130:131], v5 offset0:18 offset1:19
A_ v104,v20,v104
Mvaa 40,192,124
L_ v7,24,s55,0
Mvaa 36,220,200
S_ s55,s71,163840
ds_read2st64_b32 v[132:133], v5 offset0:36 offset1:37
A_ v105,v21,v105
Mvaa 36,196,160
S_ m0,s51,16384
ds_read2st64_b32 v[134:135], v5 offset0:38 offset1:39
A_ v106,v22,v106
Mvaa 36,192,120
ds_read2st64_b32 v[124:125], v5 offset0:32 offset1:33
A_ v107,v23,v107
s_waitcnt vmcnt(29)
Zaaa 100,70,20
L_ v7,24,s55,0
Zaaa 140,68,20
W_ 7
ds_read2st64_b32 v[126:127], v5 offset0:34 offset1:35
A_ v112,v24,v112
Zaaa 104,70,24
S_ s55,s71,174080
ds_read2st64_b32 v[116:117], v5 offset0:28 offset1:29
A_ v113,v25,v113
Zaaa 144,68,24
S_ m0,s51,17408
ds_read2st64_b32 v[118:119], v5 offset0:30 offset1:31
A_ v114,v26,v114
Zaaa 108,70,28
L_ v7,24,s55,0
Zaaa 148,68,28
S_ s79,s6,6
ds_read2st64_b32 v[108:109], v5 offset0:24 offset1:25
A_ v115,v27,v115
Zaaa 112,70,32
W_ 9
ds_read2st64_b32 v[110:111], v5 offset0:26 offset1:27
A_ v120,v28,v120
Zaaa 152,68,32
s_and_b32 s79, s79, 7
ds_read2st64_b32 v[100:101], v5 offset0:20 offset1:21
.Lpad_o_7:
s_cbranch_execz .Lpad_o_11
A_ v121,v29,v121
Zaaa 116,70,36
s_lshl_b32 s79, s79, 7
ds_read2st64_b32 v[102:103], v5 offset0:22 offset1:23
A_ v122,v30,v122
Zaaa 156,68,36
S_ s71,s72,s79
A_ v123,v31,v123
Maaa 224,70,60
W_ 10
A_ v128,v32,v128
A_ v129,v33,v129
Maaa 228,68,60
S_ s55,s71,491520
A_ v130,v34,v130
A_ v131,v35,v131
Maaa 232,70,64
W_ 8
A_ v132,v52,v132
A_ v133,v53,v133
Maaa 236,68,64
A_ v134,v54,v134
A_ v135,v55,v135
Zvav 56,180,136
W_ 6
Ta 192,v1,36864
Ta 194,v1,38912
Mvav 56,184,156
S_ m0,s51,49152
A_ v124,v48,v124
A_ v125,v49,v125
Mvav 56,188,176
s_nop 0
Ta 196,v1,40960
Ta 198,v1,43008
Zvav 60,180,140
L_ v7,24,s55,0
Mvav 60,184,160
S_ s55,s71,501760
Ta 220,v1,45056
Ta 222,v1,47104
Mvav 60,188,180
S_ m0,s51,50176
A_ v126,v50,v126
A_ v127,v51,v127
Zvav 64,180,144
W_ 10
A_ v116,v44,v116
A_ v117,v45,v117
Mvav 64,184,164
L_ v7,24,s55,0
Mvav 64,188,184
S_ s55,s71,512000
A_ v118,v46,v118
A_ v119,v47,v119
Zvav 68,180,148
W_ 8
A_ v108,v40,v108
A_ v109,v41,v109
Mvav 68,184,168
S_ m0,s51,51200
A_ v110,v42,v110
A_ v111,v43,v111
Mvav 68,188,188
L_ v7,24,s55,0
Zvav 72,180,152
s_waitcnt vmcnt(10)
W_ 7
A_ v100,v36,v100
W_ 6
Mvav 72,184,172
S_ s55,s71,522240
v_cmp_eq_u32 vcc, s62, v11
A_ v101,v37,v101
Mvav 72,188,192
W_ 0
A_ v102,v38,v102
s_mul_i32 s76, s75, 204800
Zvav 92,220,192
S_ m0,s51,52224
Ta 180,v2,36864
Ta 182,v2,38912
Mvav 92,196,172
L_ v7,24,s55,0
Mvav 92,192,152
S_ s55,s71,532480
Ta 184,v2,40960
Ta 186,v2,43008
Zvav 88,220,188
s_bitcmp1_b32 vcc_lo, s75
s_cbranch_scc0 .Lspin1
.Lspun1:
A_ v103,v39,v103
Mvav 88,196,168
S_ s56,s76,s84
Ta 188,v2,45056
Ta 190,v2,47104
Mvav 88,192,148
buffer_load_dwordx4 a[20:23], v6, s[28:31], s56 offen offset:0 sc1
Zvav 84,220,184
S_ m0,s51,53248
C_ 131,130,131
C_ 130,128,129
Mvav 84,196,164
S_ s85,s6,8
C_ 132,132,133
C_ 133,134,135
Mvav 84,192,144
s_and_b32 s85, s85, 7
Zvav 80,220,180
buffer_load_dwordx4 a[24:27], v6, s[28:31], s56 offen offset:1024 sc1
Mvav 80,196,160
s_lshl_b32 s85, s85, 7
Mvav 80,192,140
S_ s85,s85,s87
Zvav 76,220,176
Mvav 76,196,156
buffer_load_dwordx4 a[28:31], v6, s[28:31], s56 offen offset:2048 sc1
Mvav 76,192,136
W_ 0
Zvav 16,180,136
s_nop 0
Ta 192,v3,36864
Ta 194,v3,38912
Mvav 16,184,156
Mvav 16,188,176
s_nop 0
Ta 196,v3,40960
Ta 198,v3,43008
Zvav 20,180,140
buffer_load_dwordx4 a[32:35], v6, s[28:31], s56 offen offset:3072 sc1
Mvav 20,184,160
S_ s56,s56,4096
Ta 220,v3,45056
Ta 222,v3,47104
Mvav 20,188,180
Zvav 24,180,144
Mvav 24,184,164
buffer_load_dwordx4 a[36:39], v6, s[28:31], s56 offen offset:0 sc1
Mvav 24,188,184
S_ s56,s76,s74
Zvav 28,180,148
Mvav 28,184,168
Mvav 28,188,188
buffer_load_dwordx4 a[60:63], v6, s[28:31], s56 offen offset:0 sc1
Zvav 32,180,152
S_ s56,s76,s82
Mvav 32,184,172
Mvav 32,188,192
s_waitcnt vmcnt(25) lgkmcnt(0)
s_barrier
Zvav 52,220,192
s_nop 0
Ta 180,%0,24576
Ta 182,%0,26624
Mvav 52,196,172
buffer_load_dwordx4 a[64:67], v6, s[28:31], s56 offen offset:0 sc1
Mvav 52,192,152
s_nop 0
Ta 184,%0,28672
Ta 186,%0,30720
Zvav 48,220,188
Mvav 48,196,168
s_nop 0
Ta 188,%0,32768
Ta 190,%0,34816
Mvav 48,192,148
buffer_store_dwordx4 v[130:133], v6, s[28:31], s73 offen offset:0 sc1
Zvav 44,220,184
s_sub_u32 s73, s73, 4096
Mvav 44,196,164
C_ 123,122,123
C_ 122,120,121
Mvav 44,192,144
C_ 124,124,125
C_ 125,126,127
Zvav 40,220,180
Mvav 40,196,160
Mvav 40,192,140
Zvav 36,220,176
Mvav 36,196,156
buffer_store_dwordx4 v[122:125], v6, s[28:31], s73 offen offset:3072 sc1
Mvav 36,192,136
W_ 0
C_ 115,114,115
C_ 114,112,113
Mvav 56,180,196
s_nop 0
Ta 192,v1,24576
Ta 194,v1,26624
Mvav 56,184,216
C_ 116,116,117
C_ 117,118,119
Mvav 56,188,236
s_nop 0
Ta 196,v1,28672
Ta 198,v1,30720
Mvav 60,180,200
Mvav 60,184,220
s_nop 0
Ta 220,v1,32768
Ta 222,v1,34816
Mvav 60,188,240
Mvav 64,180,204
buffer_store_dwordx4 v[114:117], v6, s[28:31], s73 offen offset:2048 sc1
Mvav 64,184,224
C_ 107,106,107
C_ 106,104,105
Mvav 64,188,244
C_ 108,108,109
C_ 109,110,111
Mvav 68,180,208
Mvav 68,184,228
Mvav 68,188,248
Mvav 72,180,212
Mvav 72,184,232
Mvav 72,188,252
W_ 0
Mvav 92,220,252
s_nop 0
Ta 180,v2,24576
Ta 182,v2,26624
Mvav 92,196,232
buffer_store_dwordx4 v[106:109], v6, s[28:31], s73 offen offset:1024 sc1
Mvav 92,192,212
s_nop 0
Ta 184,v2,28672
Ta 186,v2,30720
Mvav 88,220,248
C_ 99,98,99
C_ 98,96,97
Mvav 88,196,228
s_nop 0
Ta 188,v2,32768
Ta 190,v2,34816
Mvav 88,192,208
C_ 100,100,101
C_ 101,102,103
Mvav 84,220,244
Mvav 84,196,224
Mvav 84,192,204
Mvav 80,220,240
buffer_store_dwordx4 v[98:101], v6, s[28:31], s73 offen offset:0 sc1
Mvav 80,196,220
Mvav 80,192,200
Mvav 76,220,236
Mvav 76,196,216
Mvav 76,192,196
W_ 0
Mvav 16,180,196
s_nop 0
Ta 192,v3,24576
Ta 194,v3,26624
Mvav 16,184,216
Mvav 16,188,236
s_nop 0
Ta 196,v3,28672
Ta 198,v3,30720
Mvav 20,180,200
L_ v7,24,s55,0
Mvav 20,184,220
S_ s55,s71,542720
Ta 220,v3,32768
Ta 222,v3,34816
Mvav 20,188,240
S_ m0,s51,54272
Mvav 24,180,204
Mvav 24,184,224
L_ v7,24,s55,0
Mvav 24,188,244
S_ s55,s71,614400
Mvav 28,180,208
S_ m0,s51,36864
Mvav 28,184,228
Mvav 28,188,248
L_ v7,24,s55,0
Mvav 32,180,212
S_ s55,s71,624640
Mvav 32,184,232
S_ m0,s51,37888
Mvav 32,188,252
s_waitcnt vmcnt(28) lgkmcnt(0)
s_barrier
Mvav 52,220,252
s_nop 0
Ta 180,%0,0
Ta 182,%0,2048
Mvav 52,196,232
L_ v7,24,s55,0
Mvav 52,192,212
S_ s55,s71,634880
Ta 184,%0,4096
Ta 186,%0,6144
Mvav 48,220,248
S_ m0,s51,38912
Mvav 48,196,228
s_nop 0
Ta 188,%0,8192
Ta 190,%0,10240
Mvav 48,192,208
L_ v7,24,s55,0
Mvav 44,220,244
S_ s55,s71,645120
Mvav 44,196,224
S_ m0,s51,39936
Mvav 44,192,204
Mvav 40,220,240
L_ v7,24,s55,0
Mvav 40,196,220
S_ s55,s71,655360
Mvav 40,192,200
S_ m0,s51,40960
Mvav 36,220,236
Mvav 36,196,216
L_ v7,24,s55,0
Mvav 36,192,196
W_ 0
S_ s55,s71,665600
Mvaa 56,180,0
S_ m0,s51,41984
Ta 192,v1,0
Ta 194,v1,2048
Mvaa 56,184,40
Mvaa 56,188,80
s_nop 0
Ta 196,v1,4096
Ta 198,v1,6144
Mvaa 60,180,4
L_ v7,24,s55,0
Mvaa 60,184,44
S_ s55,s71,0
Ta 220,v1,8192
Ta 222,v1,10240
Mvaa 60,188,84
S_ m0,s51,24576
Mvaa 64,180,8
Mvaa 64,184,48
L_ v7,24,s55,0
Mvaa 64,188,88
S_ s55,s71,10240
Mvaa 68,180,12
S_ m0,s51,25600
Mvaa 68,184,52
Mvaa 68,188,240
L_ v7,24,s55,0
Mvaa 72,180,16
S_ s55,s71,20480
Mvaa 72,184,56
S_ m0,s51,26624
Mvaa 72,188,244
W_ 0
Mvaa 92,220,244
s_nop 0
Ta 180,v2,0
Ta 182,v2,2048
Mvaa 92,196,56
L_ v7,24,s55,0
Mvaa 92,192,16
S_ s55,s71,30720
Ta 184,v2,4096
Ta 186,v2,6144
Mvaa 88,220,240
S_ m0,s51,27648
Mvaa 88,196,52
s_nop 0
Ta 188,v2,8192
Ta 190,v2,10240
Mvaa 88,192,12
L_ v7,24,s55,0
Mvaa 84,220,88
S_ s55,s71,40960
Mvaa 84,196,48
S_ m0,s51,28672
Mvaa 84,192,8
Mvaa 80,220,84
L_ v7,24,s55,0
Mvaa 80,196,44
S_ s55,s71,51200
Mvaa 80,192,4
S_ m0,s51,29696
Mvaa 76,220,80
Mvaa 76,196,40
L_ v7,24,s55,0
Mvaa 76,192,0
W_ 0
Mvaa 16,180,0
s_nop 0
Ta 192,v3,0
Ta 194,v3,2048
Mvaa 16,184,40
Mvaa 16,188,80
s_nop 0
Ta 196,v3,4096
Ta 198,v3,6144
Mvaa 20,180,4
buffer_load_dword a74, v4, s[24:27], s85 offen
Mvaa 20,184,44
s_nop 0
Ta 220,v3,8192
Ta 222,v3,10240
Mvaa 20,188,84
Mvaa 24,180,8
Mvaa 24,184,48
Mvaa 24,188,88
Mvaa 28,180,12
Mvaa 28,184,52
Mvaa 28,188,240
Mvaa 32,180,16
Mvaa 32,184,56
Mvaa 32,188,244
s_waitcnt vmcnt(31) lgkmcnt(0)
s_barrier
Mvaa 52,220,244
s_nop 0
Ta 180,%0,12288
Ta 182,%0,14336
Mvaa 52,196,56
Mvaa 52,192,16
s_nop 0
Ta 184,%0,16384
Ta 186,%0,18432
Mvaa 48,220,240
Mvaa 48,196,52
s_nop 0
Ta 188,%0,20480
Ta 190,%0,22528
Mvaa 48,192,12
Mvaa 44,220,88
Mvaa 44,196,48
Mvaa 44,192,8
Mvaa 40,220,84
Mvaa 40,196,44
Mvaa 40,192,4
Mvaa 36,220,80
Mvaa 36,196,40
Mvaa 36,192,0
W_ 0
Mvaa 56,180,120
S_ s81,s6,5
Ta 192,v1,12288
Ta 194,v1,14336
Mvaa 56,184,160
s_and_b32 s81, s81, 7
Mvaa 56,188,200
s_mul_i32 s81, s81, 20480
Ta 196,v1,16384
Ta 198,v1,18432
Mvaa 60,180,124
s_mov_b32 m0, s52
S_ s73,s81,s78
Mvaa 60,184,164
S_ s73,s73,4096
Ta 220,v1,20480
Ta 222,v1,22528
Mvaa 60,188,204
s_nop 0
Y_ 56,0
Y_ 57,256
Mvaa 64,180,128
s_nop 0
Y_ 58,512
Y_ 59,768
Mvaa 64,184,168
Mvaa 64,188,208
s_nop 0
Y_ 60,1024
Y_ 61,1280
Mvaa 68,180,132
s_nop 0
Y_ 62,1536
Y_ 63,1792
Mvaa 68,184,172
Mvaa 68,188,212
s_nop 0
Y_ 64,2048
Y_ 65,2304
Mvaa 72,180,136
s_nop 0
Y_ 66,2560
Y_ 67,2816
Mvaa 72,184,176
Mvaa 72,188,216
W_ 12
Y_ 68,3072
Y_ 69,3328
Mvaa 92,220,216
s_nop 0
Ta 180,v2,12288
Ta 182,v2,14336
Mvaa 92,196,176
s_nop 0
Y_ 70,3584
Y_ 71,3840
Mvaa 92,192,136
s_nop 0
Ta 184,v2,16384
Ta 186,v2,18432
Mvaa 88,220,212
s_nop 0
Y_ 72,4096
Y_ 73,4352
Mvaa 88,196,172
S_ s83,s6,4
Ta 188,v2,20480
Ta 190,v2,22528
Mvaa 88,192,132
s_and_b32 s83, s83, 7
Y_ 74,4608
Y_ 75,4864
Mvaa 84,220,208
s_lshl_b32 s83, s83, 2
Y_ 92,9216
Y_ 93,9472
Mvaa 84,196,168
s_nop 0
Y_ 94,9728
Y_ 95,9984
Mvaa 84,192,128
s_nop 0
Y_ 88,8192
Y_ 89,8448
Mvaa 80,220,204
s_nop 0
Y_ 90,8704
Y_ 91,8960
Mvaa 80,196,164
v_cmp_eq_u32_e64 s[36:37], s83, v8
Mvaa 80,192,124
s_sub_u32 s75, s6, 3
Y_ 84,7168
Y_ 85,7424
Mvaa 76,220,200
s_and_b32 s75, s75, 7
Y_ 86,7680
Y_ 87,7936
Mvaa 76,196,160
v_mov_b32 v10, 0x7fff0000
Mvaa 76,192,120
W_ 14
Y_ 80,6144
Y_ 81,6400
Mvaa 16,180,120
s_nop 0
Ta 192,v3,12288
Ta 194,v3,14336
Mvaa 16,184,160
s_nop 0
Y_ 82,6656
Y_ 83,6912
Mvaa 16,188,200
s_nop 0
Ta 196,v3,16384
Ta 198,v3,18432
Mvaa 20,180,124
s_nop 0
Y_ 76,5120
Y_ 77,5376
Mvaa 20,184,164
s_nop 0
Ta 220,v3,20480
Ta 222,v3,22528
Mvaa 20,188,204
s_nop 0
Y_ 78,5632
Y_ 79,5888
Mvaa 24,180,128
buffer_load_dword v11, v8, s[92:95], s96 offen offset:384 sc1
Mvaa 24,184,168
.Lpad_o_8:
s_cbranch_execz .Lhop_o_r
v_cndmask_b32_e64 v10, v10, v9, s[36:37]
Mvaa 24,188,208
s_waitcnt vmcnt(14) lgkmcnt(0)
s_barrier
Mvaa 28,180,132
S_ s55,s71,122880
ds_read2st64_b32 v[96:97], v5 offset0:0 offset1:1
Mvaa 28,184,172
buffer_store_dword a255, v10, s[92:95], 0 offen offset:384 sc1
Mvaa 28,188,212
S_ m0,s51,0
ds_read2st64_b32 v[98:99], v5 offset0:2 offset1:3
Mvaa 32,180,136
ds_read2st64_b32 v[104:105], v5 offset0:4 offset1:5
Mvaa 32,184,176
ds_read2st64_b32 v[106:107], v5 offset0:6 offset1:7
Mvaa 32,188,216
L_ v7,24,s55,0
Mvaa 52,220,216
S_ s55,s71,133120
Ta 180,%0,49152
Ta 182,%0,51200
Mvaa 52,196,176
S_ m0,s51,1024
ds_read2st64_b32 v[112:113], v5 offset0:8 offset1:9
Mvaa 52,192,136
W_ 5
Ta 184,%0,53248
Ta 186,%0,55296
Mvaa 48,220,212
L_ v7,24,s55,0
Mvaa 48,196,172
S_ s55,s71,143360
Ta 188,%0,57344
Ta 190,%0,59392
Mvaa 48,192,132
S_ m0,s51,2048
ds_read2st64_b32 v[114:115], v5 offset0:10 offset1:11
A_ v96,v16,v96
Mvaa 44,220,208
ds_read2st64_b32 v[120:121], v5 offset0:12 offset1:13
A_ v97,v17,v97
Mvaa 44,196,168
L_ v7,24,s55,0
Mvaa 44,192,128
S_ s55,s71,153600
ds_read2st64_b32 v[122:123], v5 offset0:14 offset1:15
A_ v98,v18,v98
Mvaa 40,220,204
S_ m0,s51,3072
ds_read2st64_b32 v[128:129], v5 offset0:16 offset1:17
A_ v99,v19,v99
Mvaa 40,196,164
W_ 11
ds_read2st64_b32 v[130:131], v5 offset0:18 offset1:19
A_ v104,v20,v104
Mvaa 40,192,124
L_ v7,24,s55,0
Mvaa 36,220,200
S_ s55,s71,163840
ds_read2st64_b32 v[132:133], v5 offset0:36 offset1:37
A_ v105,v21,v105
Mvaa 36,196,160
S_ m0,s51,4096
ds_read2st64_b32 v[134:135], v5 offset0:38 offset1:39
A_ v106,v22,v106
Mvaa 36,192,120
ds_read2st64_b32 v[124:125], v5 offset0:32 offset1:33
A_ v107,v23,v107
s_waitcnt vmcnt(26)
Maaa 100,70,20
L_ v7,24,s55,0
Maaa 140,68,20
W_ 7
ds_read2st64_b32 v[126:127], v5 offset0:34 offset1:35
A_ v112,v24,v112
Maaa 104,70,24
S_ s55,s71,174080
ds_read2st64_b32 v[116:117], v5 offset0:28 offset1:29
A_ v113,v25,v113
Maaa 144,68,24
S_ m0,s51,5120
ds_read2st64_b32 v[118:119], v5 offset0:30 offset1:31
A_ v114,v26,v114
Maaa 108,70,28
L_ v7,24,s55,0
Maaa 148,68,28
S_ s79,s6,7
ds_read2st64_b32 v[108:109], v5 offset0:24 offset1:25
A_ v115,v27,v115
Maaa 112,70,32
W_ 9
ds_read2st64_b32 v[110:111], v5 offset0:26 offset1:27
A_ v120,v28,v120
Maaa 152,68,32
s_and_b32 s79, s79, 7
ds_read2st64_b32 v[100:101], v5 offset0:20 offset1:21
A_ v121,v29,v121
Maaa 116,70,36
s_lshl_b32 s79, s79, 7
ds_read2st64_b32 v[102:103], v5 offset0:22 offset1:23
A_ v122,v30,v122
Maaa 156,68,36
S_ s71,s72,s79
A_ v123,v31,v123
Maaa 224,70,60
W_ 10
A_ v128,v32,v128
A_ v129,v33,v129
Maaa 228,68,60
S_ s55,s71,491520
A_ v130,v34,v130
A_ v131,v35,v131
Maaa 232,70,64
W_ 8
A_ v132,v52,v132
A_ v133,v53,v133
Maaa 236,68,64
A_ v134,v54,v134
A_ v135,v55,v135
Zvav 56,180,136
W_ 6
Ta 192,v1,49152
Ta 194,v1,51200
Mvav 56,184,156
S_ m0,s51,12288
A_ v124,v48,v124
A_ v125,v49,v125
Mvav 56,188,176
s_nop 0
Ta 196,v1,53248
Ta 198,v1,55296
Zvav 60,180,140
L_ v7,24,s55,0
Mvav 60,184,160
S_ s55,s71,501760
Ta 220,v1,57344
Ta 222,v1,59392
Mvav 60,188,180
S_ m0,s51,13312
A_ v126,v50,v126
A_ v127,v51,v127
Zvav 64,180,144
W_ 10
A_ v116,v44,v116
A_ v117,v45,v117
Mvav 64,184,164
L_ v7,24,s55,0
Mvav 64,188,184
S_ s55,s71,512000
A_ v118,v46,v118
A_ v119,v47,v119
Zvav 68,180,148
W_ 8
A_ v108,v40,v108
A_ v109,v41,v109
Mvav 68,184,168
S_ m0,s51,14336
A_ v110,v42,v110
A_ v111,v43,v111
Mvav 68,188,188
L_ v7,24,s55,0
Zvav 72,180,152
s_waitcnt vmcnt(10)
W_ 7
A_ v100,v36,v100
W_ 6
Mvav 72,184,172
S_ s55,s71,522240
v_cmp_eq_u32 vcc, s62, v11
A_ v101,v37,v101
Mvav 72,188,192
W_ 0
A_ v102,v38,v102
s_mul_i32 s76, s75, 204800
Zvav 92,220,192
S_ m0,s51,15360
Ta 180,v2,49152
Ta 182,v2,51200
Mvav 92,196,172
L_ v7,24,s55,0
Mvav 92,192,152
S_ s55,s71,532480
Ta 184,v2,53248
Ta 186,v2,55296
Zvav 88,220,188
s_bitcmp1_b32 vcc_lo, s75
s_cbranch_scc0 .Lspin2
.Lspun2:
A_ v103,v39,v103
Mvav 88,196,168
S_ s56,s76,s84
Ta 188,v2,57344
Ta 190,v2,59392
Mvav 88,192,148
buffer_load_dwordx4 a[20:23], v6, s[28:31], s56 offen offset:0 sc1
Zvav 84,220,184
S_ m0,s51,16384
C_ 131,130,131
C_ 130,128,129
Mvav 84,196,164
C_ 132,132,133
C_ 133,134,135
Mvav 84,192,144
Zvav 80,220,180
buffer_load_dwordx4 a[24:27], v6, s[28:31], s56 offen offset:1024 sc1
Mvav 80,196,160
Mvav 80,192,140
Zvav 76,220,176
Mvav 76,196,156
buffer_load_dwordx4 a[28:31], v6, s[28:31], s56 offen offset:2048 sc1
Mvav 76,192,136
W_ 0
Zvav 16,180,136
s_nop 0
Ta 192,v3,49152
Ta 194,v3,51200
Mvav 16,184,156
Mvav 16,188,176
s_nop 0
Ta 196,v3,53248
Ta 198,v3,55296
Zvav 20,180,140
buffer_load_dwordx4 a[32:35], v6, s[28:31], s56 offen offset:3072 sc1
Mvav 20,184,160
S_ s56,s56,4096
Ta 220,v3,57344
Ta 222,v3,59392
Mvav 20,188,180
Zvav 24,180,144
Mvav 24,184,164
buffer_load_dwordx4 a[36:39], v6, s[28:31], s56 offen offset:0 sc1
Mvav 24,188,184
S_ s56,s76,s74
Zvav 28,180,148
Mvav 28,184,168
Mvav 28,188,188
buffer_load_dwordx4 a[60:63], v6, s[28:31], s56 offen offset:0 sc1
Zvav 32,180,152
S_ s56,s76,s82
Mvav 32,184,172
Mvav 32,188,192
s_waitcnt vmcnt(25) lgkmcnt(0)
s_barrier
Zvav 52,220,192
s_nop 0
Ta 180,%0,36864
Ta 182,%0,38912
Mvav 52,196,172
buffer_load_dwordx4 a[64:67], v6, s[28:31], s56 offen offset:0 sc1
Mvav 52,192,152
s_nop 0
Ta 184,%0,40960
Ta 186,%0,43008
Zvav 48,220,188
Mvav 48,196,168
s_nop 0
Ta 188,%0,45056
Ta 190,%0,47104
Mvav 48,192,148
buffer_store_dwordx4 v[130:133], v6, s[28:31], s73 offen offset:0 sc1
Zvav 44,220,184
s_sub_u32 s73, s73, 4096
Mvav 44,196,164
C_ 123,122,123
C_ 122,120,121
Mvav 44,192,144
C_ 124,124,125
C_ 125,126,127
Zvav 40,220,180
Mvav 40,196,160
Mvav 40,192,140
Zvav 36,220,176
Mvav 36,196,156
buffer_store_dwordx4 v[122:125], v6, s[28:31], s73 offen offset:3072 sc1
Mvav 36,192,136
W_ 0
C_ 115,114,115
C_ 114,112,113
Mvav 56,180,196
s_nop 0
Ta 192,v1,36864
Ta 194,v1,38912
Mvav 56,184,216
C_ 116,116,117
C_ 117,118,119
Mvav 56,188,236
s_nop 0
Ta 196,v1,40960
Ta 198,v1,43008
Mvav 60,180,200
Mvav 60,184,220
s_nop 0
Ta 220,v1,45056
Ta 222,v1,47104
Mvav 60,188,240
Mvav 64,180,204
buffer_store_dwordx4 v[114:117], v6, s[28:31], s73 offen offset:2048 sc1
Mvav 64,184,224
C_ 107,106,107
C_ 106,104,105
Mvav 64,188,244
C_ 108,108,109
C_ 109,110,111
Mvav 68,180,208
Mvav 68,184,228
Mvav 68,188,248
Mvav 72,180,212
Mvav 72,184,232
Mvav 72,188,252
W_ 0
Mvav 92,220,252
s_nop 0
Ta 180,v2,36864
Ta 182,v2,38912
Mvav 92,196,232
buffer_store_dwordx4 v[106:109], v6, s[28:31], s73 offen offset:1024 sc1
Mvav 92,192,212
s_nop 0
Ta 184,v2,40960
Ta 186,v2,43008
Mvav 88,220,248
C_ 99,98,99
C_ 98,96,97
Mvav 88,196,228
s_nop 0
Ta 188,v2,45056
Ta 190,v2,47104
Mvav 88,192,208
C_ 100,100,101
C_ 101,102,103
Mvav 84,220,244
Mvav 84,196,224
Mvav 84,192,204
Mvav 80,220,240
buffer_store_dwordx4 v[98:101], v6, s[28:31], s73 offen offset:0 sc1
Mvav 80,196,220
Mvav 80,192,200
Mvav 76,220,236
Mvav 76,196,216
Mvav 76,192,196
W_ 0
Mvav 16,180,196
s_nop 0
Ta 192,v3,36864
Ta 194,v3,38912
Mvav 16,184,216
Mvav 16,188,236
s_nop 0
Ta 196,v3,40960
Ta 198,v3,43008
Mvav 20,180,200
L_ v7,24,s55,0
Mvav 20,184,220
S_ s55,s71,542720
Ta 220,v3,45056
Ta 222,v3,47104
Mvav 20,188,240
S_ m0,s51,17408
Mvav 24,180,204
Mvav 24,184,224
L_ v7,24,s55,0
Mvav 24,188,244
S_ s55,s71,614400
Mvav 28,180,208
S_ m0,s51,49152
Mvav 28,184,228
Mvav 28,188,248
L_ v7,24,s55,0
Mvav 32,180,212
S_ s55,s71,624640
Mvav 32,184,232
S_ m0,s51,50176
Mvav 32,188,252
s_waitcnt vmcnt(28) lgkmcnt(0)
s_barrier
Mvav 52,220,252
s_nop 0
Ta 180,%0,24576
Ta 182,%0,26624
Mvav 52,196,232
L_ v7,24,s55,0
Mvav 52,192,212
S_ s55,s71,634880
Ta 184,%0,28672
Ta 186,%0,30720
Mvav 48,220,248
S_ m0,s51,51200
Mvav 48,196,228
s_nop 0
Ta 188,%0,32768
Ta 190,%0,34816
Mvav 48,192,208
L_ v7,24,s55,0
Mvav 44,220,244
S_ s55,s71,645120
Mvav 44,196,224
S_ m0,s51,52224
Mvav 44,192,204
Mvav 40,220,240
L_ v7,24,s55,0
Mvav 40,196,220
S_ s55,s71,655360
Mvav 40,192,200
S_ m0,s51,53248
Mvav 36,220,236
Mvav 36,196,216
L_ v7,24,s55,0
Mvav 36,192,196
W_ 0
S_ s55,s71,665600
Mvaa 56,180,0
S_ m0,s51,54272
Ta 192,v1,24576
Ta 194,v1,26624
Mvaa 56,184,40
Mvaa 56,188,80
s_nop 0
Ta 196,v1,28672
Ta 198,v1,30720
Mvaa 60,180,4
L_ v7,24,s55,0
Mvaa 60,184,44
S_ s55,s71,0
Ta 220,v1,32768
Ta 222,v1,34816
Mvaa 60,188,84
S_ m0,s51,36864
Mvaa 64,180,8
Mvaa 64,184,48
L_ v7,24,s55,0
Mvaa 64,188,88
S_ s55,s71,10240
Mvaa 68,180,12
S_ m0,s51,37888
Mvaa 68,184,52
Mvaa 68,188,240
L_ v7,24,s55,0
Mvaa 72,180,16
S_ s55,s71,20480
Mvaa 72,184,56
S_ m0,s51,38912
Mvaa 72,188,244
W_ 0
Mvaa 92,220,244
s_nop 0
Ta 180,v2,24576
Ta 182,v2,26624
Mvaa 92,196,56
L_ v7,24,s55,0
Mvaa 92,192,16
S_ s55,s71,30720
Ta 184,v2,28672
Ta 186,v2,30720
Mvaa 88,220,240
S_ m0,s51,39936
Mvaa 88,196,52
s_nop 0
Ta 188,v2,32768
Ta 190,v2,34816
Mvaa 88,192,12
L_ v7,24,s55,0
Mvaa 84,220,88
S_ s55,s71,40960
Mvaa 84,196,48
S_ m0,s51,40960
Mvaa 84,192,8
Mvaa 80,220,84
L_ v7,24,s55,0
Mvaa 80,196,44
S_ s55,s71,51200
Mvaa 80,192,4
S_ m0,s51,41984
Mvaa 76,220,80
Mvaa 76,196,40
L_ v7,24,s55,0
Mvaa 76,192,0
W_ 0
Mvaa 16,180,0
s_nop 0
Ta 192,v3,24576
Ta 194,v3,26624
Mvaa 16,184,40
Mvaa 16,188,80
s_nop 0
Ta 196,v3,28672
Ta 198,v3,30720
Mvaa 20,180,4
Mvaa 20,184,44
s_nop 0
Ta 220,v3,32768
Ta 222,v3,34816
Mvaa 20,188,84
Mvaa 24,180,8
Mvaa 24,184,48
Mvaa 24,188,88
Mvaa 28,180,12
Mvaa 28,184,52
Mvaa 28,188,240
Mvaa 32,180,16
Mvaa 32,184,56
Mvaa 32,188,244
s_waitcnt vmcnt(30) lgkmcnt(0)
s_barrier
Mvaa 52,220,244
s_nop 0
Ta 180,%0,0
Ta 182,%0,2048
Mvaa 52,196,56
Mvaa 52,192,16
s_nop 0
Ta 184,%0,4096
Ta 186,%0,6144
Mvaa 48,220,240
Mvaa 48,196,52
s_nop 0
Ta 188,%0,8192
Ta 190,%0,10240
Mvaa 48,192,12
Mvaa 44,220,88
Mvaa 44,196,48
Mvaa 44,192,8
Mvaa 40,220,84
Mvaa 40,196,44
Mvaa 40,192,4
Mvaa 36,220,80
Mvaa 36,196,40
Mvaa 36,192,0
W_ 0
Mvaa 56,180,120
S_ s81,s6,6
Ta 192,v1,0
Ta 194,v1,2048
Mvaa 56,184,160
s_and_b32 s81, s81, 7
Mvaa 56,188,200
s_mul_i32 s81, s81, 20480
Ta 196,v1,4096
Ta 198,v1,6144
Mvaa 60,180,124
s_mov_b32 m0, s52
S_ s73,s81,s78
Mvaa 60,184,164
S_ s73,s73,4096
Ta 220,v1,8192
Ta 222,v1,10240
Mvaa 60,188,204
s_nop 0
Y_ 56,0
Y_ 57,256
Mvaa 64,180,128
s_nop 0
Y_ 58,512
Y_ 59,768
Mvaa 64,184,168
Mvaa 64,188,208
s_nop 0
Y_ 60,1024
Y_ 61,1280
Mvaa 68,180,132
s_nop 0
Y_ 62,1536
Y_ 63,1792
Mvaa 68,184,172
Mvaa 68,188,212
s_nop 0
Y_ 64,2048
Y_ 65,2304
Mvaa 72,180,136
.Lpad_o_9:
s_cbranch_execz .Lhop_o_r
Y_ 66,2560
Y_ 67,2816
Mvaa 72,184,176
Mvaa 72,188,216
W_ 12
Y_ 68,3072
Y_ 69,3328
Mvaa 92,220,216
s_nop 0
Ta 180,v2,0
Ta 182,v2,2048
Mvaa 92,196,176
s_nop 0
Y_ 70,3584
Y_ 71,3840
Mvaa 92,192,136
s_nop 0
Ta 184,v2,4096
Ta 186,v2,6144
Mvaa 88,220,212
s_nop 0
Y_ 72,4096
Y_ 73,4352
Mvaa 88,196,172
S_ s83,s6,5
Ta 188,v2,8192
Ta 190,v2,10240
Mvaa 88,192,132
s_and_b32 s83, s83, 7
Y_ 74,4608
Y_ 75,4864
Mvaa 84,220,208
s_lshl_b32 s83, s83, 2
Y_ 92,9216
Y_ 93,9472
Mvaa 84,196,168
s_nop 0
Y_ 94,9728
Y_ 95,9984
Mvaa 84,192,128
s_nop 0
Y_ 88,8192
Y_ 89,8448
Mvaa 80,220,204
s_nop 0
Y_ 90,8704
Y_ 91,8960
Mvaa 80,196,164
v_cmp_eq_u32_e64 s[36:37], s83, v8
Mvaa 80,192,124
s_sub_u32 s75, s6, 4
Y_ 84,7168
Y_ 85,7424
Mvaa 76,220,200
s_and_b32 s75, s75, 7
Y_ 86,7680
Y_ 87,7936
Mvaa 76,196,160
v_mov_b32 v10, 0x7fff0000
Mvaa 76,192,120
W_ 14
Y_ 80,6144
Y_ 81,6400
Mvaa 16,180,120
s_nop 0
Ta 192,v3,0
Ta 194,v3,2048
Mvaa 16,184,160
s_nop 0
Y_ 82,6656
Y_ 83,6912
Mvaa 16,188,200
s_nop 0
Ta 196,v3,4096
Ta 198,v3,6144
Mvaa 20,180,124
s_nop 0
Y_ 76,5120
Y_ 77,5376
Mvaa 20,184,164
s_nop 0
Ta 220,v3,8192
Ta 222,v3,10240
Mvaa 20,188,204
s_nop 0
Y_ 78,5632
Y_ 79,5888
)A"
R"A(Mvaa 24,180,128
buffer_load_dword v11, v8, s[92:95], s96 offen offset:384 sc1
Mvaa 24,184,168
v_cndmask_b32_e64 v10, v10, v9, s[36:37]
Mvaa 24,188,208
s_waitcnt vmcnt(13) lgkmcnt(0)
s_barrier
Mvaa 28,180,132
S_ s55,s71,122880
ds_read2st64_b32 v[96:97], v5 offset0:0 offset1:1
Mvaa 28,184,172
buffer_store_dword a255, v10, s[92:95], 0 offen offset:384 sc1
Mvaa 28,188,212
S_ m0,s51,24576
ds_read2st64_b32 v[98:99], v5 offset0:2 offset1:3
Mvaa 32,180,136
ds_read2st64_b32 v[104:105], v5 offset0:4 offset1:5
Mvaa 32,184,176
ds_read2st64_b32 v[106:107], v5 offset0:6 offset1:7
Mvaa 32,188,216
L_ v7,24,s55,0
Mvaa 52,220,216
S_ s55,s71,133120
Ta 180,%0,12288
Ta 182,%0,14336
Mvaa 52,196,176
S_ m0,s51,25600
ds_read2st64_b32 v[112:113], v5 offset0:8 offset1:9
Mvaa 52,192,136
W_ 5
Ta 184,%0,16384
Ta 186,%0,18432
Mvaa 48,220,212
L_ v7,24,s55,0
Mvaa 48,196,172
S_ s55,s71,143360
Ta 188,%0,20480
Ta 190,%0,22528
Mvaa 48,192,132
S_ m0,s51,26624
ds_read2st64_b32 v[114:115], v5 offset0:10 offset1:11
A_ v96,v16,v96
Mvaa 44,220,208
ds_read2st64_b32 v[120:121], v5 offset0:12 offset1:13
A_ v97,v17,v97
Mvaa 44,196,168
L_ v7,24,s55,0
Mvaa 44,192,128
S_ s55,s71,153600
ds_read2st64_b32 v[122:123], v5 offset0:14 offset1:15
A_ v98,v18,v98
Mvaa 40,220,204
S_ m0,s51,27648
ds_read2st64_b32 v[128:129], v5 offset0:16 offset1:17
A_ v99,v19,v99
Mvaa 40,196,164
W_ 11
ds_read2st64_b32 v[130:131], v5 offset0:18 offset1:19
A_ v104,v20,v104
Mvaa 40,192,124
L_ v7,24,s55,0
Mvaa 36,220,200
S_ s55,s71,163840
ds_read2st64_b32 v[132:133], v5 offset0:36 offset1:37
A_ v105,v21,v105
Mvaa 36,196,160
S_ m0,s51,28672
ds_read2st64_b32 v[134:135], v5 offset0:38 offset1:39
A_ v106,v22,v106
Mvaa 36,192,120
ds_read2st64_b32 v[124:125], v5 offset0:32 offset1:33
A_ v107,v23,v107
s_waitcnt vmcnt(25)
Maaa 100,70,20
L_ v7,24,s55,0
Maaa 140,68,20
W_ 7
ds_read2st64_b32 v[126:127], v5 offset0:34 offset1:35
A_ v112,v24,v112
Maaa 104,70,24
S_ s55,s71,174080
ds_read2st64_b32 v[116:117], v5 offset0:28 offset1:29
A_ v113,v25,v113
Maaa 144,68,24
S_ m0,s51,29696
ds_read2st64_b32 v[118:119], v5 offset0:30 offset1:31
A_ v114,v26,v114
Maaa 108,70,28
L_ v7,24,s55,0
Maaa 148,68,28
S_ s79,s6,8
ds_read2st64_b32 v[108:109], v5 offset0:24 offset1:25
A_ v115,v27,v115
Maaa 112,70,32
W_ 9
ds_read2st64_b32 v[110:111], v5 offset0:26 offset1:27
A_ v120,v28,v120
Maaa 152,68,32
s_and_b32 s79, s79, 7
ds_read2st64_b32 v[100:101], v5 offset0:20 offset1:21
A_ v121,v29,v121
Maaa 116,70,36
s_lshl_b32 s79, s79, 7
ds_read2st64_b32 v[102:103], v5 offset0:22 offset1:23
A_ v122,v30,v122
Maaa 156,68,36
S_ s71,s72,s79
A_ v123,v31,v123
Maaa 224,70,60
W_ 10
A_ v128,v32,v128
A_ v129,v33,v129
Maaa 228,68,60
S_ s55,s71,491520
A_ v130,v34,v130
A_ v131,v35,v131
Maaa 232,70,64
W_ 8
A_ v132,v52,v132
A_ v133,v53,v133
Maaa 236,68,64
A_ v134,v54,v134
A_ v135,v55,v135
Zvav 56,180,136
W_ 6
Ta 192,v1,12288
Ta 194,v1,14336
Mvav 56,184,156
S_ m0,s51,0
A_ v124,v48,v124
A_ v125,v49,v125
Mvav 56,188,176
s_nop 0
Ta 196,v1,16384
Ta 198,v1,18432
Zvav 60,180,140
L_ v7,24,s55,0
Mvav 60,184,160
S_ s55,s71,501760
Ta 220,v1,20480
Ta 222,v1,22528
Mvav 60,188,180
S_ m0,s51,1024
A_ v126,v50,v126
A_ v127,v51,v127
Zvav 64,180,144
W_ 10
A_ v116,v44,v116
A_ v117,v45,v117
Mvav 64,184,164
L_ v7,24,s55,0
Mvav 64,188,184
S_ s55,s71,512000
A_ v118,v46,v118
A_ v119,v47,v119
Zvav 68,180,148
W_ 8
A_ v108,v40,v108
A_ v109,v41,v109
Mvav 68,184,168
S_ m0,s51,2048
A_ v110,v42,v110
A_ v111,v43,v111
Mvav 68,188,188
L_ v7,24,s55,0
Zvav 72,180,152
s_waitcnt vmcnt(10)
W_ 7
A_ v100,v36,v100
W_ 6
Mvav 72,184,172
S_ s55,s71,522240
v_cmp_eq_u32 vcc, s62, v11
A_ v101,v37,v101
Mvav 72,188,192
W_ 0
A_ v102,v38,v102
s_mul_i32 s76, s75, 204800
Zvav 92,220,192
S_ m0,s51,3072
Ta 180,v2,12288
Ta 182,v2,14336
Mvav 92,196,172
L_ v7,24,s55,0
Mvav 92,192,152
S_ s55,s71,532480
Ta 184,v2,16384
Ta 186,v2,18432
Zvav 88,220,188
s_bitcmp1_b32 vcc_lo, s75
s_cbranch_scc0 .Lspin3
.Lspun3:
A_ v103,v39,v103
Mvav 88,196,168
S_ s56,s76,s84
Ta 188,v2,20480
Ta 190,v2,22528
Mvav 88,192,148
buffer_load_dwordx4 a[20:23], v6, s[28:31], s56 offen offset:0 sc1
Zvav 84,220,184
S_ m0,s51,4096
C_ 131,130,131
C_ 130,128,129
Mvav 84,196,164
C_ 132,132,133
C_ 133,134,135
Mvav 84,192,144
Zvav 80,220,180
buffer_load_dwordx4 a[24:27], v6, s[28:31], s56 offen offset:1024 sc1
Mvav 80,196,160
Mvav 80,192,140
Zvav 76,220,176
Mvav 76,196,156
buffer_load_dwordx4 a[28:31], v6, s[28:31], s56 offen offset:2048 sc1
Mvav 76,192,136
W_ 0
Zvav 16,180,136
s_nop 0
Ta 192,v3,12288
Ta 194,v3,14336
Mvav 16,184,156
Mvav 16,188,176
s_nop 0
Ta 196,v3,16384
Ta 198,v3,18432
Zvav 20,180,140
buffer_load_dwordx4 a[32:35], v6, s[28:31], s56 offen offset:3072 sc1
Mvav 20,184,160
S_ s56,s56,4096
Ta 220,v3,20480
Ta 222,v3,22528
Mvav 20,188,180
Zvav 24,180,144
Mvav 24,184,164
buffer_load_dwordx4 a[36:39], v6, s[28:31], s56 offen offset:0 sc1
Mvav 24,188,184
S_ s56,s76,s74
Zvav 28,180,148
Mvav 28,184,168
Mvav 28,188,188
buffer_load_dwordx4 a[60:63], v6, s[28:31], s56 offen offset:0 sc1
Zvav 32,180,152
S_ s56,s76,s82
Mvav 32,184,172
Mvav 32,188,192
s_waitcnt vmcnt(24) lgkmcnt(0)
s_barrier
Zvav 52,220,192
s_nop 0
Ta 180,%0,49152
Ta 182,%0,51200
Mvav 52,196,172
buffer_load_dwordx4 a[64:67], v6, s[28:31], s56 offen offset:0 sc1
Mvav 52,192,152
s_nop 0
Ta 184,%0,53248
Ta 186,%0,55296
Zvav 48,220,188
Mvav 48,196,168
s_nop 0
Ta 188,%0,57344
Ta 190,%0,59392
Mvav 48,192,148
buffer_store_dwordx4 v[130:133], v6, s[28:31], s73 offen offset:0 sc1
Zvav 44,220,184
s_sub_u32 s73, s73, 4096
Mvav 44,196,164
C_ 123,122,123
C_ 122,120,121
Mvav 44,192,144
C_ 124,124,125
C_ 125,126,127
Zvav 40,220,180
Mvav 40,196,160
Mvav 40,192,140
Zvav 36,220,176
Mvav 36,196,156
buffer_store_dwordx4 v[122:125], v6, s[28:31], s73 offen offset:3072 sc1
Mvav 36,192,136
W_ 0
C_ 115,114,115
C_ 114,112,113
Mvav 56,180,196
s_nop 0
Ta 192,v1,49152
Ta 194,v1,51200
Mvav 56,184,216
C_ 116,116,117
C_ 117,118,119
Mvav 56,188,236
s_nop 0
Ta 196,v1,53248
Ta 198,v1,55296
Mvav 60,180,200
Mvav 60,184,220
s_nop 0
Ta 220,v1,57344
Ta 222,v1,59392
Mvav 60,188,240
Mvav 64,180,204
buffer_store_dwordx4 v[114:117], v6, s[28:31], s73 offen offset:2048 sc1
Mvav 64,184,224
C_ 107,106,107
C_ 106,104,105
Mvav 64,188,244
C_ 108,108,109
C_ 109,110,111
Mvav 68,180,208
Mvav 68,184,228
Mvav 68,188,248
Mvav 72,180,212
Mvav 72,184,232
Mvav 72,188,252
W_ 0
Mvav 92,220,252
s_nop 0
Ta 180,v2,49152
Ta 182,v2,51200
Mvav 92,196,232
buffer_store_dwordx4 v[106:109], v6, s[28:31], s73 offen offset:1024 sc1
Mvav 92,192,212
s_nop 0
Ta 184,v2,53248
Ta 186,v2,55296
Mvav 88,220,248
C_ 99,98,99
C_ 98,96,97
Mvav 88,196,228
s_nop 0
Ta 188,v2,57344
Ta 190,v2,59392
Mvav 88,192,208
C_ 100,100,101
C_ 101,102,103
Mvav 84,220,244
Mvav 84,196,224
Mvav 84,192,204
Mvav 80,220,240
buffer_store_dwordx4 v[98:101], v6, s[28:31], s73 offen offset:0 sc1
Mvav 80,196,220
Mvav 80,192,200
Mvav 76,220,236
Mvav 76,196,216
Mvav 76,192,196
W_ 0
Mvav 16,180,196
s_nop 0
Ta 192,v3,49152
Ta 194,v3,51200
Mvav 16,184,216
Mvav 16,188,236
s_nop 0
Ta 196,v3,53248
Ta 198,v3,55296
Mvav 20,180,200
L_ v7,24,s55,0
Mvav 20,184,220
S_ s55,s71,542720
Ta 220,v3,57344
Ta 222,v3,59392
Mvav 20,188,240
S_ m0,s51,5120
Mvav 24,180,204
Mvav 24,184,224
L_ v7,24,s55,0
Mvav 24,188,244
S_ s55,s71,614400
Mvav 28,180,208
S_ m0,s51,12288
Mvav 28,184,228
Mvav 28,188,248
L_ v7,24,s55,0
Mvav 32,180,212
S_ s55,s71,624640
Mvav 32,184,232
S_ m0,s51,13312
Mvav 32,188,252
s_waitcnt vmcnt(27) lgkmcnt(0)
s_barrier
Mvav 52,220,252
s_nop 0
Ta 180,%0,36864
Ta 182,%0,38912
Mvav 52,196,232
L_ v7,24,s55,0
Mvav 52,192,212
S_ s55,s71,634880
Ta 184,%0,40960
Ta 186,%0,43008
Mvav 48,220,248
S_ m0,s51,14336
Mvav 48,196,228
s_nop 0
Ta 188,%0,45056
Ta 190,%0,47104
Mvav 48,192,208
L_ v7,24,s55,0
Mvav 44,220,244
s_sub_u32 s75, s6, 5
S_ s55,s71,645120
Mvav 44,196,224
s_and_b32 s75, s75, 7
S_ m0,s51,15360
Mvav 44,192,204
Mvav 40,220,240
buffer_load_dword v11, v8, s[92:95], s96 offen offset:384 sc1
Mvav 40,196,220
Mvav 40,192,200
Mvav 36,220,236
Mvav 36,196,216
L_ v7,24,s55,0
Mvav 36,192,196
W_ 0
S_ s55,s71,655360
Mvaa 56,180,0
S_ m0,s51,16384
Ta 192,v1,36864
Ta 194,v1,38912
Mvaa 56,184,40
Mvaa 56,188,80
s_nop 0
Ta 196,v1,40960
Ta 198,v1,43008
Mvaa 60,180,4
L_ v7,24,s55,0
Mvaa 60,184,44
S_ s55,s71,665600
Ta 220,v1,45056
Ta 222,v1,47104
Mvaa 60,188,84
S_ m0,s51,17408
Mvaa 64,180,8
Mvaa 64,184,48
L_ v7,24,s55,0
Mvaa 64,188,88
S_ s55,s71,0
Mvaa 68,180,12
S_ m0,s51,49152
Mvaa 68,184,52
Mvaa 68,188,240
L_ v7,24,s55,0
Mvaa 72,180,16
S_ s55,s71,10240
Mvaa 72,184,56
S_ m0,s51,50176
Mvaa 72,188,244
W_ 0
Mvaa 92,220,244
s_nop 0
Ta 180,v2,36864
Ta 182,v2,38912
Mvaa 92,196,56
L_ v7,24,s55,0
Mvaa 92,192,16
S_ s55,s71,20480
Ta 184,v2,40960
Ta 186,v2,43008
Mvaa 88,220,240
S_ m0,s51,51200
Mvaa 88,196,52
s_nop 0
Ta 188,v2,45056
Ta 190,v2,47104
Mvaa 88,192,12
L_ v7,24,s55,0
Mvaa 84,220,88
S_ s55,s71,30720
Mvaa 84,196,48
S_ m0,s51,52224
Mvaa 84,192,8
Mvaa 80,220,84
L_ v7,24,s55,0
Mvaa 80,196,44
S_ s83,s6,6
S_ s55,s71,40960
Mvaa 80,192,4
s_and_b32 s83, s83, 7
S_ m0,s51,53248
Mvaa 76,220,80
s_lshl_b32 s83, s83, 2
Mvaa 76,196,40
L_ v7,24,s55,0
Mvaa 76,192,0
W_ 0
v_cmp_eq_u32_e64 s[36:37], s83, v8
S_ s55,s71,51200
Mvaa 16,180,0
S_ m0,s51,54272
Ta 192,v3,36864
Ta 194,v3,38912
Mvaa 16,184,40
.Lpad_o_10:
s_cbranch_execz .Lhop_o_r
v_mov_b32 v10, 0x7fff0000
Mvaa 16,188,80
s_nop 0
Ta 196,v3,40960
Ta 198,v3,43008
Mvaa 20,180,4
L_ v7,24,s55,0
Mvaa 20,184,44
s_nop 0
Ta 220,v3,45056
Ta 222,v3,47104
Mvaa 20,188,84
v_cndmask_b32_e64 v10, v10, v9, s[36:37]
Mvaa 24,180,8
Mvaa 24,184,48
Mvaa 24,188,88
Mvaa 28,180,12
Mvaa 28,184,52
Mvaa 28,188,240
Mvaa 32,180,16
Mvaa 32,184,56
Mvaa 32,188,244
s_waitcnt vmcnt(15) lgkmcnt(0)
s_barrier
Mvaa 52,220,244
s_nop 0
Ta 180,%0,24576
Ta 182,%0,26624
Mvaa 52,196,56
buffer_store_dword a255, v10, s[92:95], 0 offen offset:384 sc1
Mvaa 52,192,16
s_nop 0
Ta 184,%0,28672
Ta 186,%0,30720
Mvaa 48,220,240
Mvaa 48,196,52
s_nop 0
Ta 188,%0,32768
Ta 190,%0,34816
Mvaa 48,192,12
Mvaa 44,220,88
Mvaa 44,196,48
Mvaa 44,192,8
Mvaa 40,220,84
Mvaa 40,196,44
Mvaa 40,192,4
Mvaa 36,220,80
Mvaa 36,196,40
Mvaa 36,192,0
W_ 0
Mvaa 56,180,120
S_ s81,s6,7
Ta 192,v1,24576
Ta 194,v1,26624
Mvaa 56,184,160
s_and_b32 s81, s81, 7
Mvaa 56,188,200
s_mul_i32 s81, s81, 20480
Ta 196,v1,28672
Ta 198,v1,30720
Mvaa 60,180,124
s_mov_b32 m0, s52
S_ s73,s81,s78
Mvaa 60,184,164
S_ s73,s73,4096
Ta 220,v1,32768
Ta 222,v1,34816
Mvaa 60,188,204
s_nop 0
Y_ 56,0
Y_ 57,256
Mvaa 64,180,128
s_nop 0
Y_ 58,512
Y_ 59,768
Mvaa 64,184,168
Mvaa 64,188,208
s_nop 0
Y_ 60,1024
Y_ 61,1280
Mvaa 68,180,132
s_nop 0
Y_ 62,1536
Y_ 63,1792
Mvaa 68,184,172
Mvaa 68,188,212
s_nop 0
Y_ 64,2048
Y_ 65,2304
Mvaa 72,180,136
s_nop 0
Y_ 66,2560
Y_ 67,2816
Mvaa 72,184,176
Mvaa 72,188,216
W_ 12
Y_ 68,3072
Y_ 69,3328
Mvaa 92,220,216
s_nop 0
Ta 180,v2,24576
Ta 182,v2,26624
Mvaa 92,196,176
s_nop 0
Y_ 70,3584
Y_ 71,3840
Mvaa 92,192,136
s_nop 0
Ta 184,v2,28672
Ta 186,v2,30720
Mvaa 88,220,212
s_nop 0
Y_ 72,4096
Y_ 73,4352
Mvaa 88,196,172
s_nop 0
Ta 188,v2,32768
Ta 190,v2,34816
Mvaa 88,192,132
s_nop 0
Y_ 74,4608
Y_ 75,4864
Mvaa 84,220,208
s_nop 0
Y_ 92,9216
Y_ 93,9472
Mvaa 84,196,168
s_nop 0
Y_ 94,9728
Y_ 95,9984
Mvaa 84,192,128
s_nop 0
Y_ 88,8192
Y_ 89,8448
Mvaa 80,220,204
s_nop 0
Y_ 90,8704
Y_ 91,8960
Mvaa 80,196,164
Mvaa 80,192,124
s_nop 0
Y_ 84,7168
Y_ 85,7424
Mvaa 76,220,200
s_nop 0
Y_ 86,7680
Y_ 87,7936
Mvaa 76,196,160
Mvaa 76,192,120
W_ 14
Y_ 80,6144
Y_ 81,6400
Mvaa 16,180,120
s_nop 0
Ta 192,v3,24576
Ta 194,v3,26624
Mvaa 16,184,160
s_nop 0
Y_ 82,6656
Y_ 83,6912
Mvaa 16,188,200
s_nop 0
Ta 196,v3,28672
Ta 198,v3,30720
Mvaa 20,180,124
s_nop 0
Y_ 76,5120
Y_ 77,5376
Mvaa 20,184,164
s_nop 0
Ta 220,v3,32768
Ta 222,v3,34816
Mvaa 20,188,204
s_nop 0
Y_ 78,5632
Y_ 79,5888
Mvaa 24,180,128
Mvaa 24,184,168
Mvaa 24,188,208
s_waitcnt vmcnt(14) lgkmcnt(0)
s_barrier
Mvaa 28,180,132
S_ s55,s71,122880
ds_read2st64_b32 v[96:97], v5 offset0:0 offset1:1
Mvaa 28,184,172
S_ m0,s51,36864
ds_read2st64_b32 v[98:99], v5 offset0:2 offset1:3
Mvaa 28,188,212
L_ v7,24,s55,0
Mvaa 32,180,136
S_ s55,s71,133120
ds_read2st64_b32 v[104:105], v5 offset0:4 offset1:5
Mvaa 32,184,176
s_waitcnt vmcnt(11)
ds_read2st64_b32 v[106:107], v5 offset0:6 offset1:7
S_ m0,s51,37888
Mvaa 32,188,216
ds_read2st64_b32 v[112:113], v5 offset0:8 offset1:9
v_cmp_eq_u32 vcc, s62, v11
Mvaa 52,220,216
s_mul_i32 s76, s75, 204800
Ta 180,%0,0
Ta 182,%0,2048
Mvaa 52,196,176
L_ v7,24,s55,0
Mvaa 52,192,136
W_ 5
Ta 184,%0,4096
Ta 186,%0,6144
Mvaa 48,220,212
s_bitcmp1_b32 vcc_lo, s75
s_cbranch_scc0 .Lspin4
.Lspun4:
ds_read2st64_b32 v[114:115], v5 offset0:10 offset1:11
Mvaa 48,196,172
S_ s55,s71,143360
Ta 188,%0,8192
Ta 190,%0,10240
Mvaa 48,192,132
S_ m0,s51,38912
ds_read2st64_b32 v[120:121], v5 offset0:12 offset1:13
A_ v96,v16,v96
Mvaa 44,220,208
L_ v7,24,s55,0
Mvaa 44,196,168
S_ s55,s71,153600
ds_read2st64_b32 v[122:123], v5 offset0:14 offset1:15
A_ v97,v17,v97
Mvaa 44,192,128
S_ m0,s51,39936
ds_read2st64_b32 v[128:129], v5 offset0:16 offset1:17
A_ v98,v18,v98
Mvaa 40,220,204
ds_read2st64_b32 v[130:131], v5 offset0:18 offset1:19
A_ v99,v19,v99
Mvaa 40,196,164
L_ v7,24,s55,0
Mvaa 40,192,124
W_ 12
ds_read2st64_b32 v[132:133], v5 offset0:36 offset1:37
A_ v104,v20,v104
Mvaa 36,220,200
S_ s55,s71,163840
ds_read2st64_b32 v[134:135], v5 offset0:38 offset1:39
A_ v105,v21,v105
Mvaa 36,196,160
S_ m0,s51,40960
ds_read2st64_b32 v[124:125], v5 offset0:32 offset1:33
A_ v106,v22,v106
Mvaa 36,192,120
L_ v7,24,s55,0
s_waitcnt vmcnt(26)
Maaa 100,70,20
S_ s55,s71,174080
ds_read2st64_b32 v[126:127], v5 offset0:34 offset1:35
A_ v107,v23,v107
Maaa 140,68,20
W_ 10
ds_read2st64_b32 v[116:117], v5 offset0:28 offset1:29
A_ v112,v24,v112
Maaa 104,70,24
S_ m0,s51,41984
ds_read2st64_b32 v[118:119], v5 offset0:30 offset1:31
A_ v113,v25,v113
Maaa 144,68,24
L_ v7,24,s55,0
Maaa 108,70,28
ds_read2st64_b32 v[108:109], v5 offset0:24 offset1:25
A_ v114,v26,v114
Maaa 148,68,28
ds_read2st64_b32 v[110:111], v5 offset0:26 offset1:27
A_ v115,v27,v115
Maaa 112,70,32
W_ 10
ds_read2st64_b32 v[100:101], v5 offset0:20 offset1:21
A_ v120,v28,v120
Maaa 152,68,32
ds_read2st64_b32 v[102:103], v5 offset0:22 offset1:23
A_ v121,v29,v121
Maaa 116,70,36
A_ v122,v30,v122
A_ v123,v31,v123
Maaa 156,68,36
W_ 10
A_ v128,v32,v128
A_ v129,v33,v129
Maaa 224,70,60
A_ v130,v34,v130
A_ v131,v35,v131
Maaa 228,68,60
W_ 8
A_ v132,v52,v132
A_ v133,v53,v133
Maaa 232,70,64
A_ v134,v54,v134
A_ v135,v55,v135
Maaa 236,68,64
W_ 7
A_ v124,v48,v124
W_ 6
Zvav 56,180,136
s_nop 0
Ta 192,v1,0
Ta 194,v1,2048
Mvav 56,184,156
A_ v125,v49,v125
A_ v126,v50,v126
Mvav 56,188,176
S_ s56,s76,s84
Ta 196,v1,4096
Ta 198,v1,6144
Zvav 60,180,140
buffer_load_dwordx4 a[20:23], v6, s[28:31], s56 offen offset:0 sc1
Mvav 60,184,160
s_nop 0
Ta 220,v1,8192
Ta 222,v1,10240
Mvav 60,188,180
A_ v127,v51,v127
C_ 131,130,131
Zvav 64,180,144
W_ 10
A_ v116,v44,v116
A_ v117,v45,v117
Mvav 64,184,164
buffer_load_dwordx4 a[24:27], v6, s[28:31], s56 offen offset:1024 sc1
Mvav 64,188,184
A_ v118,v46,v118
A_ v119,v47,v119
Zvav 68,180,148
W_ 8
A_ v108,v40,v108
A_ v109,v41,v109
Mvav 68,184,168
A_ v110,v42,v110
A_ v111,v43,v111
Mvav 68,188,188
buffer_load_dwordx4 a[28:31], v6, s[28:31], s56 offen offset:2048 sc1
Zvav 72,180,152
W_ 6
A_ v100,v36,v100
A_ v101,v37,v101
Mvav 72,184,172
A_ v102,v38,v102
A_ v103,v39,v103
Mvav 72,188,192
W_ 0
C_ 130,128,129
C_ 132,132,133
Zvav 92,220,192
s_nop 0
Ta 180,v2,0
Ta 182,v2,2048
Mvav 92,196,172
buffer_load_dwordx4 a[32:35], v6, s[28:31], s56 offen offset:3072 sc1
Mvav 92,192,152
S_ s56,s56,4096
Ta 184,v2,4096
Ta 186,v2,6144
Zvav 88,220,188
C_ 133,134,135
Mvav 88,196,168
s_nop 0
Ta 188,v2,8192
Ta 190,v2,10240
Mvav 88,192,148
buffer_load_dwordx4 a[36:39], v6, s[28:31], s56 offen offset:0 sc1
Zvav 84,220,184
S_ s56,s76,s74
Mvav 84,196,164
Mvav 84,192,144
Zvav 80,220,180
buffer_load_dwordx4 a[60:63], v6, s[28:31], s56 offen offset:0 sc1
Mvav 80,196,160
S_ s56,s76,s82
Mvav 80,192,140
Zvav 76,220,176
Mvav 76,196,156
buffer_load_dwordx4 a[64:67], v6, s[28:31], s56 offen offset:0 sc1
Mvav 76,192,136
W_ 0
s_sub_u32 s75, s6, 6
Maav 100,180,136
s_and_b32 s75, s75, 7
Ta 192,v3,0
Ta 194,v3,2048
Maav 100,184,156
Maav 100,188,176
s_nop 0
Ta 196,v3,4096
Ta 198,v3,6144
Maav 104,180,140
buffer_load_dword v11, v8, s[92:95], s96 offen offset:384 sc1
Maav 104,184,160
s_nop 0
Ta 220,v3,8192
Ta 222,v3,10240
Maav 104,188,180
Maav 108,180,144
Maav 108,184,164
buffer_store_dwordx4 v[130:133], v6, s[28:31], s73 offen offset:0 sc1
Maav 108,188,184
s_sub_u32 s73, s73, 4096
Maav 112,180,148
C_ 123,122,123
C_ 122,120,121
Maav 112,184,168
C_ 124,124,125
C_ 125,126,127
Maav 112,188,188
buffer_store_dwordx4 v[122:125], v6, s[28:31], s73 offen offset:3072 sc1
Maav 116,180,152
C_ 115,114,115
C_ 114,112,113
Maav 116,184,172
C_ 116,116,117
C_ 117,118,119
Maav 116,188,192
s_waitcnt vmcnt(23) lgkmcnt(0)
s_barrier
Maav 156,220,192
s_nop 0
Ta 180,%0,12288
Ta 182,%0,14336
Maav 156,196,172
buffer_store_dwordx4 v[114:117], v6, s[28:31], s73 offen offset:2048 sc1
Maav 156,192,152
s_nop 0
Ta 184,%0,16384
Ta 186,%0,18432
Maav 152,220,188
C_ 107,106,107
C_ 106,104,105
Maav 152,196,168
s_nop 0
Ta 188,%0,20480
Ta 190,%0,22528
Maav 152,192,148
C_ 108,108,109
C_ 109,110,111
Maav 148,220,184
buffer_store_dwordx4 v[106:109], v6, s[28:31], s73 offen offset:1024 sc1
Maav 148,196,164
C_ 99,98,99
C_ 98,96,97
Maav 148,192,144
C_ 100,100,101
C_ 101,102,103
Maav 144,220,180
Maav 144,196,160
buffer_store_dwordx4 v[98:101], v6, s[28:31], s73 offen offset:0 sc1
Maav 144,192,140
Maav 140,220,176
Maav 140,196,156
Maav 140,192,136
W_ 0
Mvav 56,180,196
s_nop 0
Ta 192,v1,12288
Ta 194,v1,14336
Mvav 56,184,216
Mvav 56,188,236
s_nop 0
Ta 196,v1,16384
Ta 198,v1,18432
Mvav 60,180,200
Mvav 60,184,220
s_nop 0
Ta 220,v1,20480
Ta 222,v1,22528
Mvav 60,188,240
v_bfe_u32 v4, v6, 4, 4
Mvav 64,180,204
v_mul_u32_u24 v4, 2560, v4
Mvav 64,184,224
v_bfe_u32 v10, v6, 8, 1
Mvav 64,188,244
v_lshl_add_u32 v4, v10, 5, v4
Mvav 68,180,208
v_lshrrev_b32 v10, 9, v6
Mvav 68,184,228
v_lshl_add_u32 v4, v10, 4, v4
Mvav 68,188,248
Mvav 72,180,212
Mvav 72,184,232
Mvav 72,188,252
W_ 0
Mvav 92,220,252
s_nop 0
Ta 180,v2,12288
Ta 182,v2,14336
Mvav 92,196,232
Mvav 92,192,212
s_nop 0
Ta 184,v2,16384
Ta 186,v2,18432
Mvav 88,220,248
Mvav 88,196,228
s_nop 0
Ta 188,v2,20480
Ta 190,v2,22528
Mvav 88,192,208
Mvav 84,220,244
Mvav 84,196,224
Mvav 84,192,204
Mvav 80,220,240
Mvav 80,196,220
Mvav 80,192,200
Mvav 76,220,236
Mvav 76,196,216
Mvav 76,192,196
W_ 0
Maav 100,180,196
s_nop 0
Ta 192,v3,12288
Ta 194,v3,14336
Maav 100,184,216
Maav 100,188,236
s_nop 0
Ta 196,v3,16384
Ta 198,v3,18432
Maav 104,180,200
Maav 104,184,220
s_nop 0
Ta 220,v3,20480
Ta 222,v3,22528
Maav 104,188,240
Maav 108,180,204
S_ s86,s6,1
Maav 108,184,224
s_and_b32 s86, s86, 7
Maav 108,188,244
s_waitcnt vmcnt(5)
Maav 112,180,208
v_cmp_eq_u32 vcc, s62, v11
Maav 112,184,228
s_mul_i32 s76, s75, 204800
Maav 112,188,248
s_bitcmp1_b32 vcc_lo, s75
s_cbranch_scc0 .Lspin5
.Lspun5:
Maav 116,180,212
s_bitcmp1_b32 vcc_lo, s86
s_cbranch_scc0 .Lspin6
.Lspun6:
Maav 116,184,232
S_ s56,s76,s84
Maav 116,188,252
s_waitcnt vmcnt(20) lgkmcnt(0)
s_barrier
Maav 156,220,252
s_nop 0
Ta 180,%0,49152
Ta 182,%0,51200
Maav 156,196,232
buffer_load_dwordx4 v[16:19], v6, s[28:31], s56 offen offset:0 sc1
Maav 156,192,212
s_nop 0
Ta 184,%0,53248
Ta 186,%0,55296
Maav 152,220,248
Maav 152,196,228
s_nop 0
Ta 188,%0,57344
Ta 190,%0,59392
Maav 152,192,208
buffer_load_dwordx4 v[20:23], v6, s[28:31], s56 offen offset:1024 sc1
Maav 148,220,244
Maav 148,196,224
Maav 148,192,204
Maav 144,220,240
buffer_load_dwordx4 v[24:27], v6, s[28:31], s56 offen offset:2048 sc1
Maav 144,196,220
Maav 144,192,200
Maav 140,220,236
Maav 140,196,216
buffer_load_dwordx4 v[28:31], v6, s[28:31], s56 offen offset:3072 sc1
Maav 140,192,196
W_ 0
S_ s56,s56,4096
Mvaa 56,180,0
.Lpad_o_11:
s_cbranch_execz .Lhop_o_r
Ta 192,v1,49152
Ta 194,v1,51200
Mvaa 56,184,40
R_ v164,a224
R_ v165,a225
Mvaa 56,188,80
s_nop 0
Ta 196,v1,53248
Ta 198,v1,55296
Mvaa 60,180,4
buffer_load_dwordx4 v[32:35], v6, s[28:31], s56 offen offset:0 sc1
Mvaa 60,184,44
S_ s56,s76,s74
Ta 220,v1,57344
Ta 222,v1,59392
Mvaa 60,188,84
R_ v166,a226
R_ v167,a227
Mvaa 64,180,8
R_ v168,a228
R_ v169,a229
Mvaa 64,184,48
buffer_load_dwordx4 v[36:39], v6, s[28:31], s56 offen offset:0 sc1
Mvaa 64,188,88
S_ s56,s76,s82
R_ v170,a230
R_ v171,a231
Mvaa 68,180,12
R_ v172,a232
R_ v173,a233
Mvaa 68,184,52
R_ v174,a234
R_ v175,a235
Mvaa 68,188,240
buffer_load_dwordx4 v[40:43], v6, s[28:31], s56 offen offset:0 sc1
Mvaa 72,180,16
s_mul_i32 s76, s86, 204800
R_ v176,a236
R_ v177,a237
Mvaa 72,184,56
S_ s80,s76,s84
R_ v178,a238
R_ v179,a239
Mvaa 72,188,244
W_ 0
S_ s56,s76,s74
Mvaa 92,220,244
s_nop 0
Ta 180,v2,49152
Ta 182,v2,51200
Mvaa 92,196,56
buffer_load_dwordx4 v[156:159], v6, s[28:31], s56 offen offset:0 sc1
Mvaa 92,192,16
S_ s56,s76,s82
Ta 184,v2,53248
Ta 186,v2,55296
Mvaa 88,220,240
Mvaa 88,196,52
s_nop 0
Ta 188,v2,57344
Ta 190,v2,59392
Mvaa 88,192,12
buffer_load_dwordx4 v[160:163], v6, s[28:31], s56 offen offset:0 sc1
Mvaa 84,220,88
Mvaa 84,196,48
Mvaa 84,192,8
Mvaa 80,220,84
Mvaa 80,196,44
Mvaa 80,192,4
Mvaa 76,220,80
Mvaa 76,196,40
Mvaa 76,192,0
W_ 0
Maaa 100,180,0
s_nop 0
Ta 192,v3,49152
Ta 194,v3,51200
Maaa 100,184,40
Maaa 100,188,80
s_nop 0
Ta 196,v3,53248
Ta 198,v3,55296
Maaa 104,180,4
Maaa 104,184,44
s_nop 0
Ta 220,v3,57344
Ta 222,v3,59392
Maaa 104,188,84
Maaa 108,180,8
R_ v180,a100
R_ v181,a101
Maaa 108,184,48
R_ v182,a102
R_ v183,a103
Maaa 108,188,88
Maaa 112,180,12
R_ v184,a104
R_ v185,a105
Maaa 112,184,52
R_ v186,a106
R_ v187,a107
Maaa 112,188,240
Maaa 116,180,16
R_ v188,a108
R_ v189,a109
Maaa 116,184,56
R_ v190,a110
R_ v191,a111
Maaa 116,188,244
s_waitcnt vmcnt(22) lgkmcnt(0)
s_barrier
Maaa 156,220,244
s_nop 0
Ta 180,%0,36864
Ta 182,%0,38912
Maaa 156,196,56
R_ v192,a112
R_ v193,a113
Maaa 156,192,16
s_nop 0
Ta 184,%0,40960
Ta 186,%0,43008
Maaa 152,220,240
R_ v194,a114
R_ v195,a115
Maaa 152,196,52
s_nop 0
Ta 188,%0,45056
Ta 190,%0,47104
Maaa 152,192,12
R_ v196,a116
R_ v197,a117
Maaa 148,220,88
R_ v198,a118
R_ v199,a119
Maaa 148,196,48
R_ v216,a156
R_ v217,a157
Maaa 148,192,8
R_ v218,a158
R_ v219,a159
Maaa 144,220,84
R_ v212,a152
R_ v213,a153
Maaa 144,196,44
R_ v214,a154
R_ v215,a155
Maaa 144,192,4
Maaa 140,220,80
R_ v208,a148
R_ v209,a149
Maaa 140,196,40
R_ v210,a150
R_ v211,a151
Maaa 140,192,0
s_waitcnt vmcnt(15)
Mvaa 164,70,60
R_ v204,a144
R_ v205,a145
Mvaa 168,68,60
R_ v206,a146
R_ v207,a147
Mvaa 172,70,64
Mvaa 176,68,64
R_ v200,a140
R_ v201,a141
Mvaa 180,70,20
R_ v202,a142
R_ v203,a143
Mvaa 184,70,24
Mvaa 188,70,28
Mvaa 192,70,32
Mvaa 196,70,36
Mvaa 216,68,36
Mvaa 212,68,32
Mvaa 208,68,28
Mvaa 204,68,24
Mvaa 200,68,20
W_ 0
Mvaa 56,180,120
s_nop 0
Ta 192,v1,36864
Ta 194,v1,38912
Mvaa 56,184,160
Mvaa 56,188,200
s_nop 0
Ta 196,v1,40960
Ta 198,v1,43008
Mvaa 60,180,124
s_mov_b32 m0, s52
Mvaa 60,184,164
s_nop 0
Ta 220,v1,45056
Ta 222,v1,47104
Mvaa 60,188,204
s_nop 0
Y_ 56,0
Y_ 57,256
Mvaa 64,180,128
s_nop 0
Y_ 58,512
Y_ 59,768
Mvaa 64,184,168
Mvaa 64,188,208
s_nop 0
Y_ 60,1024
Y_ 61,1280
Mvaa 68,180,132
s_nop 0
Y_ 62,1536
Y_ 63,1792
Mvaa 68,184,172
Mvaa 68,188,212
s_nop 0
Y_ 64,2048
Y_ 65,2304
Mvaa 72,180,136
s_nop 0
Y_ 66,2560
Y_ 67,2816
Mvaa 72,184,176
Mvaa 72,188,216
W_ 12
Y_ 68,3072
Y_ 69,3328
Mvaa 92,220,216
s_nop 0
Ta 180,v2,36864
Ta 182,v2,38912
Mvaa 92,196,176
s_nop 0
Y_ 70,3584
Y_ 71,3840
Mvaa 92,192,136
s_nop 0
Ta 184,v2,40960
Ta 186,v2,43008
Mvaa 88,220,212
s_nop 0
Y_ 72,4096
Y_ 73,4352
Mvaa 88,196,172
s_nop 0
Ta 188,v2,45056
Ta 190,v2,47104
Mvaa 88,192,132
s_nop 0
Y_ 74,4608
Y_ 75,4864
Mvaa 84,220,208
s_nop 0
Y_ 92,9216
Y_ 93,9472
Mvaa 84,196,168
s_nop 0
Y_ 94,9728
Y_ 95,9984
Mvaa 84,192,128
s_nop 0
Y_ 88,8192
Y_ 89,8448
Mvaa 80,220,204
s_nop 0
Y_ 90,8704
Y_ 91,8960
Mvaa 80,196,164
Mvaa 80,192,124
s_nop 0
Y_ 84,7168
Y_ 85,7424
Mvaa 76,220,200
s_nop 0
Y_ 86,7680
Y_ 87,7936
Mvaa 76,196,160
Mvaa 76,192,120
W_ 14
Y_ 80,6144
Y_ 81,6400
Mvaa 180,180,120
S_ s56,s76,s84
Ta 192,v3,36864
Ta 194,v3,38912
Mvaa 180,184,160
s_nop 0
Y_ 82,6656
Y_ 83,6912
Mvaa 180,188,200
s_nop 0
Ta 196,v3,40960
Ta 198,v3,43008
Mvaa 184,180,124
s_nop 0
Y_ 76,5120
Y_ 77,5376
Mvaa 184,184,164
s_nop 0
Ta 220,v3,45056
Ta 222,v3,47104
Mvaa 184,188,204
s_nop 0
Y_ 78,5632
Y_ 79,5888
Mvaa 188,180,128
buffer_load_dwordx4 v[136:139], v6, s[28:31], s56 offen offset:0 sc1
Mvaa 188,184,168
Mvaa 188,188,208
s_waitcnt vmcnt(63) lgkmcnt(0)
s_barrier
Mvaa 192,180,132
ds_read2st64_b32 v[96:97], v5 offset0:0 offset1:1
Mvaa 192,184,172
buffer_load_dwordx4 v[140:143], v6, s[28:31], s56 offen offset:1024 sc1
Mvaa 192,188,212
ds_read2st64_b32 v[98:99], v5 offset0:2 offset1:3
Mvaa 196,180,136
ds_read2st64_b32 v[104:105], v5 offset0:4 offset1:5
Mvaa 196,184,176
ds_read2st64_b32 v[106:107], v5 offset0:6 offset1:7
Mvaa 196,188,216
buffer_load_dwordx4 v[144:147], v6, s[28:31], s56 offen offset:2048 sc1
Mvaa 216,220,216
ds_read2st64_b32 v[112:113], v5 offset0:8 offset1:9
Mvaa 216,196,176
ds_read2st64_b32 v[114:115], v5 offset0:10 offset1:11
W_ 5
A_ v96,v96,v180
Mvaa 216,192,136
ds_read2st64_b32 v[120:121], v5 offset0:12 offset1:13
A_ v97,v97,v181
Mvaa 212,220,212
buffer_load_dwordx4 v[148:151], v6, s[28:31], s56 offen offset:3072 sc1
Mvaa 212,196,172
S_ s56,s56,4096
ds_read2st64_b32 v[122:123], v5 offset0:14 offset1:15
W_ 6
A_ v98,v98,v182
Mvaa 212,192,132
ds_read2st64_b32 v[128:129], v5 offset0:16 offset1:17
A_ v99,v99,v183
Mvaa 208,220,208
ds_read2st64_b32 v[130:131], v5 offset0:18 offset1:19
W_ 7
A_ v104,v104,v184
Mvaa 208,196,168
buffer_load_dwordx4 v[152:155], v6, s[28:31], s56 offen offset:0 sc1
Mvaa 208,192,128
ds_read2st64_b32 v[132:133], v5 offset0:36 offset1:37
A_ v105,v105,v185
Mvaa 204,220,204
ds_read2st64_b32 v[134:135], v5 offset0:38 offset1:39
W_ 8
A_ v106,v106,v186
Mvaa 204,196,164
ds_read2st64_b32 v[124:125], v5 offset0:32 offset1:33
A_ v107,v107,v187
Mvaa 204,192,124
ds_read2st64_b32 v[126:127], v5 offset0:34 offset1:35
W_ 9
A_ v112,v112,v188
Mvaa 200,220,200
ds_read2st64_b32 v[116:117], v5 offset0:28 offset1:29
A_ v113,v113,v189
Mvaa 200,196,160
ds_read2st64_b32 v[118:119], v5 offset0:30 offset1:31
W_ 10
A_ v114,v114,v190
Mvaa 200,192,120
ds_read2st64_b32 v[108:109], v5 offset0:24 offset1:25
A_ v115,v115,v191
ds_read2st64_b32 v[110:111], v5 offset0:26 offset1:27
ds_read2st64_b32 v[100:101], v5 offset0:20 offset1:21
ds_read2st64_b32 v[102:103], v5 offset0:22 offset1:23
W_ 13
A_ v120,v120,v192
A_ v121,v121,v193
W_ 12
A_ v122,v122,v194
A_ v123,v123,v195
W_ 11
A_ v128,v128,v196
A_ v129,v129,v197
W_ 10
A_ v130,v130,v198
A_ v131,v131,v199
s_waitcnt vmcnt(5)
Mvav 164,70,36
W_ 9
A_ v132,v132,v216
A_ v133,v133,v217
Mvav 168,68,36
W_ 8
A_ v134,v134,v218
A_ v135,v135,v219
Mvav 172,70,40
W_ 7
A_ v124,v124,v212
A_ v125,v125,v213
Mvav 176,68,40
W_ 6
A_ v126,v126,v214
A_ v127,v127,v215
Mvav 164,70,156
W_ 5
A_ v116,v116,v208
A_ v117,v117,v209
Mvav 168,68,156
W_ 4
A_ v118,v118,v210
A_ v119,v119,v211
Mvav 172,70,160
W_ 3
A_ v108,v108,v204
A_ v109,v109,v205
Mvav 176,68,160
W_ 2
A_ v110,v110,v206
A_ v111,v111,v207
Mvav 96,70,16
W_ 1
A_ v100,v100,v200
A_ v101,v101,v201
Mvav 104,70,20
W_ 0
A_ v102,v102,v202
A_ v103,v103,v203
Mvav 112,70,24
C_ 220,164,165
C_ 221,166,167
Mvav 120,70,28
C_ 222,168,169
C_ 223,170,171
Mvav 128,70,32
v_permlane16_swap_b32 v220, v222
v_permlane16_swap_b32 v221, v223
Mvav 132,68,32
buffer_store_dwordx4 v[220:223], v4, s[32:35], s9 offen sc1
Mvav 124,68,28
C_ 228,172,173
C_ 229,174,175
Mvav 116,68,24
C_ 230,176,177
C_ 231,178,179
Mvav 108,68,20
v_permlane16_swap_b32 v228, v230
v_permlane16_swap_b32 v229, v231
Mvav 100,68,16
buffer_store_dwordx4 v[228:231], v4, s[32:35], s10 offen sc1
s_waitcnt vmcnt(2)
.Lsel6:
Nvav 180,70,136,96
Nvav 188,70,140,104
Nvav 196,70,144,112
Nvav 204,70,148,120
Nvav 212,70,152,128
Nvav 184,68,136,100
s_nop 3
A_ v10,v180,v188
Nvav 192,68,140,108
A_ v11,v196,v204
Nvav 200,68,144,116
A_ v10,v10,v212
v_cmp_u_f32 vcc, v10, v11
Nvav 208,68,148,124
s_cbranch_vccnz .Lretry
.Lgood:
Nvav 216,68,152,132
C_ 180,180,181
C_ 181,182,183
C_ 182,184,185
C_ 183,186,187
s_nop 0
v_permlane16_swap_b32 v180, v182
v_permlane16_swap_b32 v181, v183
buffer_store_dwordx4 v[180:183], v4, s[32:35], s8 offen sc1
C_ 188,188,189
C_ 189,190,191
C_ 190,192,193
C_ 191,194,195
S_ s8,s8,40960
v_permlane16_swap_b32 v188, v190
v_permlane16_swap_b32 v189, v191
buffer_store_dwordx4 v[188:191], v4, s[32:35], s8 offen sc1
C_ 196,196,197
C_ 197,198,199
C_ 198,200,201
C_ 199,202,203
S_ s8,s8,40960
v_permlane16_swap_b32 v196, v198
v_permlane16_swap_b32 v197, v199
buffer_store_dwordx4 v[196:199], v4, s[32:35], s8 offen sc1
C_ 204,204,205
C_ 205,206,207
C_ 206,208,209
C_ 207,210,211
S_ s8,s8,40960
v_permlane16_swap_b32 v204, v206
v_permlane16_swap_b32 v205, v207
buffer_store_dwordx4 v[204:207], v4, s[32:35], s8 offen sc1
C_ 212,212,213
C_ 213,214,215
C_ 214,216,217
C_ 215,218,219
S_ s8,s8,40960
v_permlane16_swap_b32 v212, v214
v_permlane16_swap_b32 v213, v215
buffer_store_dwordx4 v[212:215], v4, s[32:35], s8 offen sc1
s_endpgm
.Lspin0:
s_cmp_eq_u32 s57, 0
s_cbranch_scc1 .Lspun0
s_sub_u32 s57, s57, 1
s_sleep 4
buffer_load_dword v11, v8, s[92:95], s96 offen offset:384 sc1
s_waitcnt vmcnt(0)
v_cmp_eq_u32 vcc, s62, v11
s_nop 1
s_bitcmp1_b32 vcc_lo, s75
s_cbranch_scc0 .Lspin0
s_branch .Lspun0
.Lspin1:
s_cmp_eq_u32 s57, 0
s_cbranch_scc1 .Lspun1
s_sub_u32 s57, s57, 1
s_sleep 4
buffer_load_dword v11, v8, s[92:95], s96 offen offset:384 sc1
s_waitcnt vmcnt(0)
v_cmp_eq_u32 vcc, s62, v11
s_nop 1
s_bitcmp1_b32 vcc_lo, s75
s_cbranch_scc0 .Lspin1
s_branch .Lspun1
.Lspin2:
s_cmp_eq_u32 s57, 0
s_cbranch_scc1 .Lspun2
s_sub_u32 s57, s57, 1
s_sleep 4
buffer_load_dword v11, v8, s[92:95], s96 offen offset:384 sc1
s_waitcnt vmcnt(0)
v_cmp_eq_u32 vcc, s62, v11
s_nop 1
s_bitcmp1_b32 vcc_lo, s75
s_cbranch_scc0 .Lspin2
s_branch .Lspun2
.Lspin3:
s_cmp_eq_u32 s57, 0
s_cbranch_scc1 .Lspun3
s_sub_u32 s57, s57, 1
s_sleep 4
buffer_load_dword v11, v8, s[92:95], s96 offen offset:384 sc1
s_waitcnt vmcnt(0)
v_cmp_eq_u32 vcc, s62, v11
s_nop 1
s_bitcmp1_b32 vcc_lo, s75
s_cbranch_scc0 .Lspin3
s_branch .Lspun3
.Lspin4:
s_cmp_eq_u32 s57, 0
s_cbranch_scc1 .Lspun4
s_sub_u32 s57, s57, 1
s_sleep 4
buffer_load_dword v11, v8, s[92:95], s96 offen offset:384 sc1
s_waitcnt vmcnt(0)
v_cmp_eq_u32 vcc, s62, v11
s_nop 1
s_bitcmp1_b32 vcc_lo, s75
s_cbranch_scc0 .Lspin4
s_branch .Lspun4
.Lspin5:
s_cmp_eq_u32 s57, 0
s_cbranch_scc1 .Lspun5
s_sub_u32 s57, s57, 1
s_sleep 4
buffer_load_dword v11, v8, s[92:95], s96 offen offset:384 sc1
s_waitcnt vmcnt(0)
v_cmp_eq_u32 vcc, s62, v11
s_nop 1
s_bitcmp1_b32 vcc_lo, s75
s_cbranch_scc0 .Lspin5
s_branch .Lspun5
.Lspin6:
s_cmp_eq_u32 s57, 0
s_cbranch_scc1 .Lspun6
s_sub_u32 s57, s57, 1
s_sleep 4
buffer_load_dword v11, v8, s[92:95], s96 offen offset:384 sc1
s_waitcnt vmcnt(0)
v_cmp_eq_u32 vcc, s62, v11
s_nop 1
s_bitcmp1_b32 vcc_lo, s86
s_cbranch_scc0 .Lspin6
s_branch .Lspun6
.Lretry:
s_cmp_eq_u32 s57, 0
s_cbranch_scc1 .Lgood
s_sub_u32 s57, s57, 1
v_max_u32 v10, v136, v137
v_max3_u32 v10, v10, v138, v139
v_max3_u32 v10, v10, v140, v141
v_max3_u32 v10, v10, v142, v143
v_max3_u32 v10, v10, v144, v145
v_max3_u32 v10, v10, v146, v147
v_max3_u32 v10, v10, v148, v149
v_max3_u32 v10, v10, v150, v151
v_max3_u32 v10, v10, v152, v153
v_max3_u32 v10, v10, v154, v155
v_cmp_eq_u32 vcc, -1, v10
s_nop 4
s_cbranch_vccz .Lgood
s_nop 7
s_nop 7
S_ s53,s80,4096
buffer_load_dwordx4 v[136:139], v6, s[28:31], s80 offen offset:0 sc1
buffer_load_dwordx4 v[140:143], v6, s[28:31], s80 offen offset:1024 sc1
buffer_load_dwordx4 v[144:147], v6, s[28:31], s80 offen offset:2048 sc1
buffer_load_dwordx4 v[148:151], v6, s[28:31], s80 offen offset:3072 sc1
buffer_load_dwordx4 v[152:155], v6, s[28:31], s53 offen offset:0 sc1
s_waitcnt vmcnt(0)
s_branch .Lsel6
.Ldone:
s_waitcnt vmcnt(0)
.purgem S_
.purgem W_
.purgem L_
.purgem X_
.purgem R_
.purgem Ta
.purgem Da
.purgem Zvaa
.purgem Mvaa
.purgem U_
.purgem C_
.purgem Ln
.purgem Dv
.purgem Zvvv
.purgem Mvvv
.purgem B_
.purgem E_
.purgem Nvvv
.purgem Tv
.purgem Zaav
.purgem Zavv
.purgem A_
.purgem Mavv
.purgem Zvav
.purgem Mvav
.purgem Y_
.purgem Zaaa
.purgem Maaa
.purgem Maav
.purgem Nvav
)A"
    : "+v"(tid)
    : "s"(__builtin_amdgcn_kernarg_segment_ptr()), "s"(blockIdx.x)
    : "v1", "v2", "v3", "v4", "v5", "v6", "v7", "v8", "v9", "v10", "v11", "v12", "v13", "v14", "v15", "v16", "v17", "v18", "v19", "v20", "v21", "v22", "v23", "v24", "v25", "v26", "v27", "v28", "v29", "v30", "v31", "v32", "v33", "v34", "v35", "v36", "v37", "v38", "v39", "v40", "v41", "v42", "v43", "v44", "v45", "v46", "v47", "v48", "v49", "v50", "v51", "v52", "v53", "v54", "v55", "v56", "v57", "v58", "v59", "v60", "v61", "v62", "v63", "v64", "v65", "v66", "v67", "v68", "v69", "v70", "v71", "v72", "v73", "v74", "v75", "v76", "v77", "v78", "v79", "v80", "v81", "v82", "v83", "v84", "v85", "v86", "v87", "v88", "v89", "v90", "v91", "v92", "v93", "v94", "v95", "v96", "v97", "v98", "v99", "v100", "v101", "v102", "v103", "v104", "v105", "v106", "v107", "v108", "v109", "v110", "v111", "v112", "v113", "v114", "v115", "v116", "v117", "v118", "v119", "v120", "v121", "v122", "v123", "v124", "v125", "v126", "v127", "v128", "v129", "v130", "v131", "v132", "v133", "v134", "v135", "v136", "v137", "v138", "v139", "v140", "v141", "v142", "v143", "v144", "v145", "v146", "v147", "v148", "v149", "v150", "v151", "v152", "v153", "v154", "v155", "v156", "v157", "v158", "v159", "v160", "v161", "v162", "v163", "v164", "v165", "v166", "v167", "v168", "v169", "v170", "v171", "v172", "v173", "v174", "v175", "v176", "v177", "v178", "v179", "v180", "v181", "v182", "v183", "v184", "v185", "v186", "v187", "v188", "v189", "v190", "v191", "v192", "v193", "v194", "v195", "v196", "v197", "v198", "v199", "v200", "v201", "v202", "v203", "v204", "v205", "v206", "v207", "v208", "v209", "v210", "v211", "v212", "v213", "v214", "v215", "v216", "v217", "v218", "v219", "v220", "v221", "v222", "v223", "v224", "v225", "v226", "v227", "v228", "v229", "v230", "v231", "v232", "v233", "v234", "v235", "v236", "v237", "v238", "v239", "v240", "v241", "v242", "v243", "v244", "v245", "v246", "v247", "v248", "v249", "v250", "v251", "v252", "v253", "v254", "v255", "a0", "a1", "a2", "a3", "a4", "a5", "a6", "a7", "a8", "a9", "a10", "a11", "a12", "a13", "a14", "a15", "a16", "a17", "a18", "a19", "a20", "a21", "a22", "a23", "a24", "a25", "a26", "a27", "a28", "a29", "a30", "a31", "a32", "a33", "a34", "a35", "a36", "a37", "a38", "a39", "a40", "a41", "a42", "a43", "a44", "a45", "a46", "a47", "a48", "a49", "a50", "a51", "a52", "a53", "a54", "a55", "a56", "a57", "a58", "a59", "a60", "a61", "a62", "a63", "a64", "a65", "a66", "a67", "a68", "a69", "a70", "a71", "a72", "a73", "a74", "a75", "a76", "a77", "a78", "a79", "a80", "a81", "a82", "a83", "a84", "a85", "a86", "a87", "a88", "a89", "a90", "a91", "a92", "a93", "a94", "a95", "a96", "a97", "a98", "a99", "a100", "a101", "a102", "a103", "a104", "a105", "a106", "a107", "a108", "a109", "a110", "a111", "a112", "a113", "a114", "a115", "a116", "a117", "a118", "a119", "a120", "a121", "a122", "a123", "a124", "a125", "a126", "a127", "a128", "a129", "a130", "a131", "a132", "a133", "a134", "a135", "a136", "a137", "a138", "a139", "a140", "a141", "a142", "a143", "a144", "a145", "a146", "a147", "a148", "a149", "a150", "a151", "a152", "a153", "a154", "a155", "a156", "a157", "a158", "a159", "a160", "a161", "a162", "a163", "a164", "a165", "a166", "a167", "a168", "a169", "a170", "a171", "a172", "a173", "a174", "a175", "a176", "a177", "a178", "a179", "a180", "a181", "a182", "a183", "a184", "a185", "a186", "a187", "a188", "a189", "a190", "a191", "a192", "a193", "a194", "a195", "a196", "a197", "a198", "a199", "a200", "a201", "a202", "a203", "a204", "a205", "a206", "a207", "a208", "a209", "a210", "a211", "a212", "a213", "a214", "a215", "a216", "a217", "a218", "a219", "a220", "a221", "a222", "a223", "a224", "a225", "a226", "a227", "a228", "a229", "a230", "a231", "a232", "a233", "a234", "a235", "a236", "a237", "a238", "a239", "a240", "a241", "a242", "a243", "a244", "a245", "a246", "a247", "a248", "a249", "a250", "a251", "a252", "a253", "a254", "a255", "s3", "s4", "s5", "s6", "s7", "s8", "s9", "s10", "s11", "s12", "s13", "s14", "s15", "s16", "s17", "s18", "s19", "s20", "s21", "s22", "s23", "s24", "s25", "s26", "s27", "s28", "s29", "s30", "s31", "s32", "s33", "s34", "s35", "s36", "s37", "s38", "s39", "s40", "s41", "s42", "s43", "s44", "s45", "s46", "s47", "s48", "s49", "s50", "s51", "s52", "s53", "s54", "s55", "s56", "s57", "s58", "s59", "s60", "s61", "s62", "s63", "s64", "s65", "s66", "s67", "s68", "s69", "s70", "s71", "s72", "s73", "s74", "s75", "s76", "s77", "s78", "s79", "s80", "s81", "s82", "s83", "s84", "s85", "s86", "s87", "s88", "s89", "s90", "s91", "s92", "s93", "s94", "s95", "s96", "s97", "s98", "s99", "s100", "s101", "vcc", "scc", "memory");
}

extern "C" void kernel_launch(void* const* d_in, const int* in_sizes, int n_in, void* d_out, int out_size, void* d_ws, size_t ws_size, hipStream_t stream) {
  Args P;
  for (int br = 0; br < 2; ++br) {
    P.p[br][0] = d_in[5 * br]; P.p[br][1] = d_in[5 * br + 3]; P.p[br][2] = d_in[5 * br + 1]; P.p[br][3] = d_in[5 * br + 2]; P.p[br][4] = d_in[5 * br + 4];
    P.p[br][5] = d_ws; P.p[br][6] = (char*)d_out + br * 1280; P.p[br][7] = d_ws;
  }
  (void)hipFuncSetAttribute((const void*)fused_kernel, hipFuncAttributeMaxDynamicSharedMemorySize, LDS1);
  fused_kernel<<<256, 256, LDS1, stream>>>(P);
}
```

```cpp
#include <hip/hip_runtime.h>
#include <hip/hip_bf16.h>

constexpr unsigned LDS1 = 160 * 1024;

struct Args { const void* p[2][8]; };

__global__ __launch_bounds__(256, 1) void fused_kernel(Args P) {
  unsigned tid = threadIdx.x;
  asm volatile(
R"A(.macro S_ a,b,c
s_add_u32 \a, \b, \c
.endm
.macro W_ a
s_waitcnt lgkmcnt(\a)
.endm
.macro L_ x,r,s,o
buffer_load_dwordx4 \x, s[\r:\r+3], \s offen offset:\o lds
.endm
.macro X_ a,b
v_accvgpr_write_b32 \a, \b
.endm
.macro R_ a,b
v_accvgpr_read_b32 \a, \b
.endm
.macro Ta d,x,o
ds_read_b64_tr_b16 a[\d:\d+1], \x offset:\o
.endm
.macro Da d,x,o
ds_read_b128 a[\d:\d+3], \x offset:\o
.endm
.macro Zvaa d,a,b
v_mfma_f32_16x16x32_bf16 v[\d:\d+3], a[\a:\a+3], a[\b:\b+3], 0
.endm
.macro Mvaa d,a,b
v_mfma_f32_16x16x32_bf16 v[\d:\d+3], a[\a:\a+3], a[\b:\b+3], v[\d:\d+3]
.endm
.macro U_ a,b,c
v_mul_f32 \a, \b, \c
.endm
.macro C_ d,a,b
v_cvt_pk_bf16_f32 v\d, v\a, v\b
.endm
.macro Ln x,r,s,o
buffer_load_dwordx4 \x, s[\r:\r+3], \s offen offset:\o nt lds
.endm
.macro Dv d,x,o
ds_read_b128 v[\d:\d+3], \x offset:\o
.endm
.macro Zvvv d,a,b
v_mfma_f32_16x16x32_bf16 v[\d:\d+3], v[\a:\a+3], v[\b:\b+3], 0
.endm
.macro Mvvv d,a,b
v_mfma_f32_16x16x32_bf16 v[\d:\d+3], v[\a:\a+3], v[\b:\b+3], v[\d:\d+3]
.endm
.macro B_ a,b,c
v_sub_f32 \a, \b, \c
.endm
.macro E_ r
v_exp_f32 v\r, v\r
.endm
.macro Nvvv d,a,b,c
v_mfma_f32_16x16x32_bf16 v[\d:\d+3], v[\a:\a+3], v[\b:\b+3], v[\c:\c+3]
.endm
.macro Tv d,x,o
ds_read_b64_tr_b16 v[\d:\d+1], \x offset:\o
.endm
.macro Zaav d,a,b
v_mfma_f32_16x16x32_bf16 a[\d:\d+3], a[\a:\a+3], v[\b:\b+3], 0
.endm
.macro Zavv d,a,b
v_mfma_f32_16x16x32_bf16 a[\d:\d+3], v[\a:\a+3], v[\b:\b+3], 0
.endm
.macro A_ a,b,c
v_add_f32 \a, \b, \c
.endm
.macro Mavv d,a,b
v_mfma_f32_16x16x32_bf16 a[\d:\d+3], v[\a:\a+3], v[\b:\b+3], a[\d:\d+3]
.endm
.macro Zvav d,a,b
v_mfma_f32_16x16x32_bf16 v[\d:\d+3], a[\a:\a+3], v[\b:\b+3], 0
.endm
.macro Mvav d,a,b
v_mfma_f32_16x16x32_bf16 v[\d:\d+3], a[\a:\a+3], v[\b:\b+3], v[\d:\d+3]
.endm
.macro Y_ r,o
ds_write_addtid_b32 v\r offset:\o
.endm
.macro Zaaa d,a,b
v_mfma_f32_16x16x32_bf16 a[\d:\d+3], a[\a:\a+3], a[\b:\b+3], 0
.endm
.macro Maaa d,a,b
v_mfma_f32_16x16x32_bf16 a[\d:\d+3], a[\a:\a+3], a[\b:\b+3], a[\d:\d+3]
.endm
.macro Maav d,a,b
v_mfma_f32_16x16x32_bf16 a[\d:\d+3], a[\a:\a+3], v[\b:\b+3], a[\d:\d+3]
.endm
.macro Nvav d,a,b,c
v_mfma_f32_16x16x32_bf16 v[\d:\d+3], a[\a:\a+3], v[\b:\b+3], v[\c:\c+3]
.endm
.p2align 12
s_and_b32 s3, %2, 4
s_lshl_b32 s3, s3, 4
s_load_dwordx16 s[8:23], %1, s3
s_memrealtime s[100:101]
s_memtime s[90:91]
v_readfirstlane_b32 s4, %0
v_and_b32 v16, 63, %0
v_lshlrev_b32 v10, 4, v16
s_mov_b32 s7, %2
s_lshr_b32 s4, s4, 6
.Lcode0:
s_getpc_b64 s[44:45]
s_mov_b32 s46, .Ldone-.Lcode0
s_mov_b32 s47, 0x00020000
s_lshr_b32 s66, s7, 3
s_lshl_b32 s66, s66, 2
S_ s66,s66,s4
s_lshl_b32 s66, s66, 11
v_min_u32 v40, 15, v16
v_lshlrev_b32 v40, 7, v40
buffer_load_dword v247, v40, s[44:47], s66 offen
s_and_b32 s3, s7, 1
s_lshl_b32 s3, s3, 3
s_lshr_b32 s53, s7, 5
S_ s3,s3,s53
s_and_b32 s6, s7, 2
s_lshl_b32 s6, s6, 1
s_bfe_u32 s53, s7, 0x20003
S_ s6,s6,s53
s_and_b32 s5, s7, 4
s_lshl_b32 s5, s5, 2
S_ s5,s5,s3
s_mul_i32 s50, s4, 5120
s_mul_i32 s60, s4, 51200
s_lshr_b32 s77, s4, 1
s_and_b32 s53, s4, 1
s_mul_i32 s59, s53, 393216
s_lshl_b32 s52, s53, 11
s_mul_i32 s61, s53, 6144
s_mul_i32 s51, s77, 61440
S_ s55,s51,40960
S_ s51,s55,s61
s_sub_u32 s52, s51, s52
s_lshl_b32 s86, s53, 2
s_mul_i32 s56, s53, 10240
v_lshrrev_b32 v17, 3, v16
v_and_b32 v18, 7, v16
v_lshrrev_b32 v23, 4, v16
v_and_b32 v24, 1, v23
v_lshlrev_b32 v24, 2, v24
v_xor_b32 v24, v18, v24
v_mul_u32_u24 v6, 12288, v17
v_lshl_add_u32 v6, v24, 4, v6
v_add_u32 v24, s86, v23
v_xor_b32 v24, v18, v24
v_mul_u32_u24 v4, 1280, v17
v_lshl_add_u32 v4, v24, 4, v4
v_xor_b32 v5, 64, v4
s_getreg_b32 s53, hwreg(HW_REG_XCC_ID, 0, 4)
s_bitcmp1_b32 0xaaaa, s53
s_cselect_b32 s58, 2, 0
s_cselect_b32 s89, 1, 0
s_cselect_b32 s53, 768, 0
S_ s59,s59,s53
s_lshl_b32 s54, s6, 2
S_ s54,s54,s77
s_mul_i32 s86, s54, 384
s_mul_i32 s87, s3, 204800
s_lshl_b32 s53, s3, 5
S_ s53,s53,s54
s_mul_i32 s85, s53, 196608
s_mov_b32 s30, 7853184
s_mov_b32 s31, 0x00020000
s_mov_b32 s26, 204800
s_mov_b32 s27, 0x00020000
W_ 0
s_mov_b32 s98, s100
s_mov_b32 s99, 0
s_mov_b32 s88, s90
S_ s28,s10,s86
s_addc_u32 s29, s11, 0
S_ s24,s8,s87
s_addc_u32 s25, s9, 0
s_buffer_load_dword s67, s[24:27], s60
s_mul_i32 s64, s66, 30
s_buffer_load_dword s67, s[28:31], s64
s_mov_b32 s11, 0
S_ s65,s59,0
S_ m0,s52,8192
s_mov_b64 s[92:93], s[18:19]
s_mov_b32 s53, 251658240
L_ v6,28,s65,0
S_ s65,s59,98304
S_ m0,s52,9216
S_ s92,s92,s53
s_addc_u32 s93, s93, 0
L_ v6,28,s65,0
S_ s65,s59,196608
S_ m0,s52,10240
s_mov_b32 s94, 131072
s_mov_b32 s95, 0x00020000
L_ v6,28,s65,0
S_ s65,s59,294912
S_ m0,s52,11264
s_lshl_b32 s96, s5, 3
S_ s96,s96,s6
L_ v6,28,s65,0
S_ s65,s60,0
S_ m0,s50,0
s_lshl_b32 s96, s96, 9
v_cmp_gt_u32 vcc, 8, v16
L_ v4,24,s65,0
S_ s65,s60,10240
S_ m0,s50,1024
s_lshl_b32 s54, s5, 12
v_lshl_add_u32 v24, v16, 9, s54
L_ v5,24,s65,0
S_ s65,s60,20480
S_ m0,s50,2048
v_mov_b32 v12, 0x7fff0000
v_cndmask_b32 v12, v12, v24, vcc
L_ v4,24,s65,0
S_ s65,s60,30720
S_ m0,s50,3072
s_lshl_b32 s54, s6, 2
v_add_u32 v13, s54, v12
L_ v5,24,s65,0
S_ s65,s60,40960
S_ m0,s50,4096
v_lshrrev_b32 v19, 2, v10
v_mul_u32_u24 v25, 384, v17
L_ v4,24,s65,0
s_load_dword s67, s[12:13], s85
s_load_dword s67, s[14:15], s85
S_ s64,s85,393216
s_load_dword s67, s[12:13], s64
s_load_dword s67, s[14:15], s64
s_lshl_b32 s64, s6, 2
S_ s64,s64,s4
s_mul_i32 s64, s64, 245760
s_load_dword s67, s[16:17], s64
s_cmp_eq_u32 s4, 0
s_cbranch_scc0 .Lhs0a
buffer_load_dword v20, v19, s[92:95], s96 offen offset:128 sc1
buffer_load_dword v21, v19, s[92:95], s96 offen offset:384 sc1
buffer_load_dword a252, v19, s[92:95], s96 offen offset:256 sc1
buffer_load_dword a253, v13, s[92:95], 0 offen offset:256 sc1
buffer_load_dword v22, off, s[92:95], s96 offset:0 sc1
.Lhs0a:
S_ s65,s59,0
S_ m0,s52,16256
v_xor_b32 v24, v18, v23
v_lshl_add_u32 v7, v24, 4, v25
v_lshlrev_b32 v24, 1, v23
v_xor_b32 v24, v18, v24
v_lshl_add_u32 v9, v24, 4, v25
v_and_b32 v26, 15, v16
v_lshrrev_b32 v27, 2, v26
v_and_b32 v28, 3, v16
v_lshrrev_b32 v24, 1, v26
v_xor_b32 v24, v23, v24
L_ v6,28,s65,128
S_ s65,s59,98304
S_ m0,s52,17280
v_lshlrev_b32 v24, 4, v24
v_lshl_add_u32 v24, v18, 7, v24
v_lshrrev_b32 v25, 3, v26
v_lshl_add_u32 v2, v25, 10, v24
v_add_u32 v2, s56, v2
v_mul_u32_u24 v1, 3072, v25
v_add3_u32 v1, v1, v24, s55
v_lshlrev_b32 v24, 2, v25
v_xor_b32 v24, v28, v24
v_lshlrev_b32 v24, 4, v24
L_ v6,28,s65,128
S_ s65,s59,196608
S_ m0,s52,18304
v_lshl_add_u32 v24, v27, 7, v24
v_lshl_add_u32 %0, v23, 10, v24
v_add_u32 %0, s55, %0
v_and_b32 v29, 1, v23
v_lshl_add_u32 v24, v29, 1, v25
v_lshlrev_b32 v24, 1, v24
v_lshrrev_b32 v30, 1, v28
v_xor_b32 v24, v30, v24
v_lshlrev_b32 v24, 4, v24
v_lshl_add_u32 v29, v29, 2, v27
L_ v6,28,s65,128
S_ s65,s59,294912
S_ m0,s52,19328
v_lshl_add_u32 v24, v29, 7, v24
v_and_b32 v29, 1, v28
v_lshl_add_u32 v24, v29, 3, v24
v_lshrrev_b32 v29, 1, v23
v_mul_u32_u24 v3, 3072, v29
v_add3_u32 v3, v3, v24, s55
X_ a242,%0
X_ a244,v1
X_ a243,v2
X_ a245,v3
L_ v6,28,s65,128
S_ s65,s59,0
S_ m0,s52,24320
X_ a240,v4
X_ a248,v10
X_ a241,v12
X_ a251,v13
v_xor_b32 v1, 64, %0
v_xor_b32 v3, 64, v2
v_xor_b32 v8, 64, v7
S_ s32,s12,s85
s_addc_u32 s33, s13, 0
s_mov_b32 s34, 589824
L_ v6,28,s65,256
S_ s65,s59,98304
S_ m0,s52,25344
s_mov_b32 s35, 0x00020000
S_ s36,s14,s85
s_addc_u32 s37, s15, 0
s_mov_b32 s38, 589824
s_mov_b32 s39, 0x00020000
s_lshl_b32 s87, s87, 1
S_ s20,s20,s87
s_addc_u32 s21, s21, 0
s_mov_b32 s57, 20000
s_mov_b32 s48, 1037384597
L_ v6,28,s65,256
S_ s65,s59,196608
S_ m0,s52,26368
s_mov_b32 s49, 1037384597
s_mul_i32 s53, s5, 1638400
S_ s53,s53,134217728
S_ s22,s18,s53
s_addc_u32 s23, s19, 0
s_lshl_b32 s53, s6, 3
s_lshr_b32 s54, s7, 5
S_ s53,s53,s54
s_lshl_b32 s53, s53, 2
S_ s53,s53,s4
L_ v6,28,s65,256
S_ s65,s59,294912
S_ m0,s52,27392
s_mul_i32 s87, s53, 30720
s_lshl_b32 s53, s6, 2
S_ s53,s53,s77
s_mul_i32 s72, s53, 245760
s_and_b32 s54, s4, 1
s_mul_i32 s53, s54, 61440
S_ s72,s72,s53
s_lshl_b32 s53, s77, 6
s_xor_b32 s83, s53, 64
s_xor_b32 s53, s4, 2
L_ v6,28,s65,256
S_ s65,s60,128
S_ m0,s50,20480
s_mul_i32 s75, s53, 10240
s_cmp_eq_u32 s4, 0
s_cselect_b32 s73, 0, 2147418112
s_mul_i32 s53, s4, 5120
s_mul_i32 s78, s6, 204800
S_ s78,s78,s53
s_and_b32 s84, s6, 7
s_mul_i32 s84, s84, 20480
S_ s84,s84,s53
s_mul_i32 s53, s6, 5120
L_ v4,24,s65,0
S_ s65,s60,10368
S_ m0,s50,21504
S_ s53,s53,163840
s_lshl_b32 s54, s4, 10
S_ s74,s53,s54
S_ s53,s53,4096
s_cmp_eq_u32 s4, 0
s_cselect_b32 s82, s53, 4026531840
s_and_b32 s53, s4, 1
s_mul_i32 s53, s53, 204800
s_lshl_b32 s54, s6, 7
S_ s53,s53,s54
L_ v5,24,s65,0
S_ s65,s60,20608
S_ m0,s50,22528
s_lshl_b32 s54, s77, 6
S_ s8,s53,s54
s_and_b32 s53, s6, 1
s_mul_i32 s53, s53, 204800
s_lshr_b32 s54, s6, 1
s_lshl_b32 s54, s54, 6
S_ s53,s53,s54
S_ s53,s53,1024
s_mul_i32 s54, s4, 40960
S_ s9,s53,s54
L_ v4,24,s65,0
S_ s65,s60,30848
S_ m0,s50,23552
S_ s53,s53,163840
s_cmp_eq_u32 s4, 0
s_cselect_b32 s10, s53, 4026531840
R_ v12,a248
v_lshrrev_b32 v12, 4, v12
v_min_u32 v10, 11, v12
s_mul_i32 s56, s4, 12
v_add_u32 v10, s56, v10
v_mul_u32_u24 v11, 43691, v10
v_lshrrev_b32 v11, 18, v11
L_ v5,24,s65,0
S_ s65,s60,41088
S_ m0,s50,24576
v_lshlrev_b32 v10, 7, v10
v_mul_u32_u24 v11, 11520, v11
v_add_u32 v10, v10, v11
v_min_u32 v12, 9, v12
s_and_b32 s56, s6, 3
s_lshl_b32 s56, s56, 2
S_ s56,s56,s4
s_mul_i32 s56, s56, 10
v_add_u32 v12, s56, v12
v_mul_u32_u24 v12, 1280, v12
L_ v4,24,s65,0
S_ s65,s59,786432
S_ m0,s52,0
s_lshr_b32 s56, s7, 5
s_mul_i32 s56, s56, 98304
s_and_b32 s55, s59, 4095
S_ s56,s56,s55
s_mul_i32 s55, s77, 384
s_sub_u32 s56, s56, s55
s_mov_b32 s63, 0
L_ v6,28,s65,0
S_ s65,s59,884736
S_ m0,s52,1024
s_nop 0
L_ v6,28,s65,0
S_ s65,s59,983040
S_ m0,s52,2048
s_nop 0
L_ v6,28,s65,0
S_ s65,s59,1081344
S_ m0,s52,3072
s_nop 0
L_ v6,28,s65,0
S_ s55,s56,786432
buffer_load_dword a254, v10, s[28:31], s55 offen
S_ s55,s56,1572864
buffer_load_dword a254, v10, s[28:31], s55 offen
buffer_load_dword a254, v12, s[24:27], s63 offen offset:256
S_ s55,s56,2359296
buffer_load_dword a254, v10, s[28:31], s55 offen
buffer_load_dword a254, v12, s[24:27], s63 offen offset:384
s_waitcnt vmcnt(22) lgkmcnt(0)
s_barrier
s_memtime s[90:91]
s_memrealtime s[68:69]
Ta 20,%0,8192
Ta 22,%0,8704
Ta 24,%0,12288
Ta 26,%0,12800
Da 60,v2,0
Da 100,v3,0
Da 64,v2,2048
Da 104,v3,2048
Da 68,v2,4096
Da 108,v3,4096
Da 72,v2,6144
Da 112,v3,6144
Da 76,v2,8192
Da 116,v3,8192
Ta 28,%0,8200
Ta 30,%0,8712
Ta 32,%0,12296
Ta 34,%0,12808
s_cmp_eq_u32 s4, 0
s_cbranch_scc0 .Lhs0b
v_readfirstlane_b32 s62, v22
.Lhs0l:
s_nop 3
S_ s62,s62,1
v_cmp_eq_u32 vcc, s62, v20
s_nop 1
s_or_b32 s53, vcc_lo, vcc_hi
v_cmp_eq_u32 vcc, s62, v21
s_nop 1
s_or_b32 s54, vcc_lo, vcc_hi
s_or_b32 s53, s53, s54
s_cbranch_scc1 .Lhs0l
v_mov_b32 v22, s62
s_mov_b64 exec, 1
s_nop 1
buffer_store_dword v22, off, s[92:95], s96 sc1
s_mov_b64 exec, -1
s_nop 1
.Lhs0b:
s_cmp_eq_u32 s58, 0
s_cbranch_scc1 .Lq
.Lqy:
W_ 13
Zvaa 16,20,60
s_nop 0
Ta 36,v1,8192
Ta 38,v1,8704
W_ 14
Mvaa 16,24,100
R_ v12,a248
W_ 13
Zvaa 24,20,64
v_lshrrev_b32 v12, 4, v12
W_ 12
Mvaa 24,24,104
s_nop 0
Ta 224,v1,12288
Ta 226,v1,12800
W_ 13
Zvaa 32,20,68
v_min_u32 v10, 11, v12
W_ 12
Mvaa 32,24,108
s_mul_i32 s56, s4, 12
W_ 11
Zvaa 40,20,72
v_add_u32 v10, s56, v10
W_ 10
Mvaa 40,24,112
v_mul_u32_u24 v11, 43691, v10
W_ 9
Zvaa 48,20,76
v_lshrrev_b32 v11, 18, v11
W_ 8
Mvaa 48,24,116
W_ 4
v_lshlrev_b32 v10, 7, v10
Zvaa 52,32,116
s_nop 0
Ta 20,v1,8200
Ta 22,v1,8712
Mvaa 52,28,76
v_mul_u32_u24 v11, 11520, v11
Zvaa 44,32,112
v_add_u32 v10, v10, v11
Mvaa 44,28,72
s_nop 0
Ta 24,v1,12296
Ta 26,v1,12808
Zvaa 36,32,108
v_min_u32 v12, 9, v12
Mvaa 36,28,68
s_and_b32 s56, s6, 3
Zvaa 28,32,104
s_lshl_b32 s56, s56, 2
Mvaa 28,28,64
S_ s56,s56,s4
Zvaa 20,32,100
s_waitcnt vmcnt(18) lgkmcnt(0)
s_barrier
s_mul_i32 s56, s56, 10
Mvaa 20,28,60
S_ s65,s59,786432
v_add_u32 v12, s56, v12
Zvaa 56,36,60
S_ m0,s52,8064
Ta 28,%0,16384
Ta 30,%0,16896
Mvaa 56,224,100
L_ v6,28,s65,128
Zvaa 64,36,64
S_ s65,s59,884736
v_mul_u32_u24 v12, 1280, v12
Mvaa 64,224,104
S_ m0,s52,9088
Ta 32,%0,20480
Ta 34,%0,20992
Zvaa 72,36,68
s_lshr_b32 s56, s7, 5
Mvaa 72,224,108
L_ v6,28,s65,128
Zvaa 80,36,72
S_ s65,s59,983040
s_mul_i32 s56, s56, 98304
Mvaa 80,224,112
S_ m0,s52,10112
s_and_b32 s55, s59, 4095
Zvaa 88,36,76
S_ s56,s56,s55
Mvaa 88,224,116
s_mul_i32 s55, s77, 384
Zvaa 92,24,116
s_sub_u32 s56, s56, s55
Ta 36,%0,16392
Ta 38,%0,16904
Mvaa 92,20,76
L_ v6,28,s65,128
Zvaa 84,24,112
S_ s65,s59,1081344
s_mov_b32 s63, 0
Mvaa 84,20,72
S_ m0,s52,11136
Ta 224,%0,20488
Ta 226,%0,21000
Zvaa 76,24,108
S_ s55,s56,3145728
Mvaa 76,20,68
L_ v6,28,s65,128
Zvaa 68,24,104
S_ s65,s60,256
Mvaa 68,20,64
S_ m0,s50,0
Zvaa 60,24,100
Mvaa 60,20,60
W_ 4
Zvaa 96,28,60
s_nop 0
Ta 20,v1,16384
Ta 22,v1,16896
Mvaa 96,32,100
L_ v4,24,s65,0
Zvaa 104,28,64
S_ s65,s60,10496
Mvaa 104,32,104
S_ m0,s50,1024
Ta 24,v1,20480
Ta 26,v1,20992
Zvaa 112,28,68
Mvaa 112,32,108
L_ v5,24,s65,0
Zvaa 120,28,72
S_ s65,s60,20736
Mvaa 120,32,112
S_ m0,s50,2048
Zvaa 128,28,76
Mvaa 128,32,116
W_ 4
Zvaa 132,224,116
s_nop 0
Ta 28,v1,16392
Ta 30,v1,16904
Mvaa 132,36,76
L_ v4,24,s65,0
Zvaa 124,224,112
S_ s65,s60,30976
Mvaa 124,36,72
S_ m0,s50,3072
Ta 32,v1,20488
Ta 34,v1,21000
Zvaa 116,224,108
Mvaa 116,36,68
L_ v5,24,s65,0
Zvaa 108,224,104
S_ s65,s60,41216
Mvaa 108,36,64
S_ m0,s50,4096
Zvaa 100,224,100
s_mov_b64 exec, 0
s_bitcmp1_b32 s4, 1
s_cbranch_scc1 .Lhop_q1_b
s_bitcmp1_b32 s4, 0
s_cbranch_scc1 .Lpad_q1_1
s_branch .Lpad_q1_0
.Lhop_q1_b:
s_bitcmp1_b32 s4, 0
s_cbranch_scc1 .Lpad_q1_3
s_branch .Lpad_q1_2
.Lhop_q1_r:
s_mov_b64 exec, -1
s_nop 1
.Lqy_early:
s_waitcnt vmcnt(17) lgkmcnt(0)
s_barrier
Mvaa 100,36,60
Da 140,v2,20480
Zvaa 136,20,60
s_nop 0
Ta 36,%0,24576
Ta 38,%0,25088
Mvaa 136,24,100
L_ v4,24,s65,0
Zvaa 144,20,64
S_ s65,s59,786432
Da 180,v3,20480
Mvaa 144,24,104
S_ m0,s52,16128
Ta 224,%0,28672
Ta 226,%0,29184
Zvaa 152,20,68
Mvaa 152,24,108
Da 144,v2,22528
Zvaa 160,20,72
L_ v6,28,s65,256
Mvaa 160,24,112
S_ s65,s59,884736
Zvaa 168,20,76
S_ m0,s52,17152
Da 184,v3,22528
Mvaa 168,24,116
Zvaa 172,32,116
s_nop 0
Ta 20,%0,24584
Ta 22,%0,25096
Mvaa 172,28,76
Da 148,v2,24576
Zvaa 164,32,112
L_ v6,28,s65,256
Mvaa 164,28,72
S_ s65,s59,983040
Ta 24,%0,28680
Ta 26,%0,29192
Zvaa 156,32,108
S_ m0,s52,18176
Da 188,v3,24576
Mvaa 156,28,68
Zvaa 148,32,104
L_ v6,28,s65,256
Mvaa 148,28,64
S_ s65,s59,1081344
Da 152,v2,26624
Zvaa 140,32,100
S_ m0,s52,19200
Mvaa 140,28,60
W_ 9
Zvaa 176,36,60
s_nop 0
Ta 28,v1,24576
Ta 30,v1,25088
Mvaa 176,224,100
Da 192,v3,26624
Zvaa 184,36,64
L_ v6,28,s65,256
Mvaa 184,224,104
s_nop 0
Ta 32,v1,28672
Ta 34,v1,29184
Zvaa 192,36,68
Da 156,v2,28672
Mvaa 192,224,108
Zvaa 200,36,72
Da 196,v3,28672
Mvaa 200,224,112
buffer_load_dword a254, v10, s[28:31], s55 offen
Zvaa 208,36,76
Mvaa 208,224,116
W_ 9
Zvaa 212,24,116
s_nop 0
Ta 36,v1,24584
Ta 38,v1,25096
Mvaa 212,20,76
buffer_load_dword a254, v12, s[24:27], s63 offen offset:512
Zvaa 204,24,112
.Lpad_q1_0:
s_cbranch_execz .Lhop_q1_r
Mvaa 204,20,72
s_nop 0
Ta 224,v1,28680
Ta 226,v1,29192
Zvaa 196,24,108
Mvaa 196,20,68
Zvaa 188,24,104
Mvaa 188,20,64
Zvaa 180,24,100
s_waitcnt vmcnt(20) lgkmcnt(0)
s_barrier
Mvaa 180,20,60
S_ s65,s59,1572864
Zvaa 216,28,60
S_ m0,s52,24576
Ta 20,%0,0
Ta 22,%0,512
Mvaa 216,32,100
L_ v6,28,s65,0
Zvaa 224,28,64
S_ s65,s59,1671168
Mvaa 224,32,104
S_ m0,s52,25600
Ta 24,%0,4096
Ta 26,%0,4608
Zvaa 232,28,68
Mvaa 232,32,108
L_ v6,28,s65,0
Zvaa 240,28,72
S_ s65,s59,1769472
Mvaa 240,32,112
S_ m0,s52,26624
Zvaa 248,28,76
Mvaa 248,32,116
Zvaa 252,224,116
s_nop 0
Ta 28,%0,8
Ta 30,%0,520
Mvaa 252,36,76
L_ v6,28,s65,0
Zvaa 244,224,112
S_ s65,s59,1867776
Mvaa 244,36,72
S_ m0,s52,27648
Ta 32,%0,4104
Ta 34,%0,4616
Zvaa 236,224,108
Mvaa 236,36,68
L_ v6,28,s65,0
Zvaa 228,224,104
S_ s65,s60,384
Mvaa 228,36,64
S_ m0,s50,20480
Zvaa 220,224,100
Mvaa 220,36,60
W_ 4
Mvaa 16,20,140
s_nop 0
Ta 36,v1,0
Ta 38,v1,512
Mvaa 16,24,180
L_ v4,24,s65,0
Mvaa 24,20,144
S_ s65,s60,10624
Mvaa 24,24,184
S_ m0,s50,21504
Ta 224,v1,4096
Ta 226,v1,4608
Mvaa 32,20,148
Mvaa 32,24,188
L_ v5,24,s65,0
Mvaa 40,20,152
S_ s65,s60,20864
Mvaa 40,24,192
S_ m0,s50,22528
Mvaa 48,20,156
Mvaa 48,24,196
W_ 4
Mvaa 52,32,196
s_nop 0
Ta 20,v1,8
Ta 22,v1,520
Mvaa 52,28,156
L_ v4,24,s65,0
Mvaa 44,32,192
S_ s65,s60,31104
Mvaa 44,28,152
S_ m0,s50,23552
Ta 24,v1,4104
Ta 26,v1,4616
Mvaa 36,32,188
Mvaa 36,28,148
L_ v5,24,s65,0
Mvaa 28,32,184
S_ s65,s60,41344
Mvaa 28,28,144
S_ m0,s50,24576
Mvaa 20,32,180
s_waitcnt vmcnt(19) lgkmcnt(0)
s_barrier
Mvaa 20,28,140
L_ v4,24,s65,0
Mvaa 56,36,140
S_ s65,s59,1572864
Ta 28,%0,8192
Ta 30,%0,8704
Mvaa 56,224,180
S_ m0,s52,-128
Mvaa 64,36,144
Mvaa 64,224,184
s_nop 0
Ta 32,%0,12288
Ta 34,%0,12800
Mvaa 72,36,148
L_ v6,28,s65,128
Mvaa 72,224,188
S_ s65,s59,1671168
Mvaa 80,36,152
S_ m0,s52,896
Mvaa 80,224,192
Mvaa 88,36,156
L_ v6,28,s65,128
Mvaa 88,224,196
S_ s65,s59,1769472
Mvaa 92,24,196
S_ m0,s52,1920
Ta 36,%0,8200
Ta 38,%0,8712
Mvaa 92,20,156
S_ s55,s56,3932160
Mvaa 84,24,192
L_ v6,28,s65,128
Mvaa 84,20,152
S_ s65,s59,1867776
Ta 224,%0,12296
Ta 226,%0,12808
Mvaa 76,24,188
S_ m0,s52,2944
Mvaa 76,20,148
Mvaa 68,24,184
L_ v6,28,s65,128
Mvaa 68,20,144
Mvaa 60,24,180
Mvaa 60,20,140
W_ 4
Mvaa 96,28,140
s_nop 0
Ta 20,v1,8192
Ta 22,v1,8704
Mvaa 96,32,180
buffer_load_dword a254, v10, s[28:31], s55 offen
Mvaa 104,28,144
Mvaa 104,32,184
s_nop 0
Ta 24,v1,12288
Ta 26,v1,12800
Mvaa 112,28,148
Mvaa 112,32,188
buffer_load_dword a254, v12, s[24:27], s63 offen offset:640
Mvaa 120,28,152
Mvaa 120,32,192
Mvaa 128,28,156
Mvaa 128,32,196
W_ 4
Mvaa 132,224,196
s_nop 0
Ta 28,v1,8200
Ta 30,v1,8712
Mvaa 132,36,156
Mvaa 124,224,192
Mvaa 124,36,152
s_nop 0
Ta 32,v1,12296
Ta 34,v1,12808
Mvaa 116,224,188
Mvaa 116,36,148
Mvaa 108,224,184
Mvaa 108,36,144
Mvaa 100,224,180
s_waitcnt vmcnt(17) lgkmcnt(0)
s_barrier
Mvaa 100,36,140
S_ s65,s59,1572864
Da 60,v2,0
Mvaa 136,20,140
S_ m0,s52,7936
Ta 36,%0,16384
Ta 38,%0,16896
Mvaa 136,24,180
L_ v6,28,s65,256
Mvaa 144,20,144
S_ s65,s59,1671168
Da 100,v3,0
Mvaa 144,24,184
S_ m0,s52,8960
Ta 224,%0,20480
Ta 226,%0,20992
Mvaa 152,20,148
Mvaa 152,24,188
Da 64,v2,2048
Mvaa 160,20,152
L_ v6,28,s65,256
Mvaa 160,24,192
S_ s65,s59,1769472
Mvaa 168,20,156
S_ m0,s52,9984
Da 104,v3,2048
Mvaa 168,24,196
Mvaa 172,32,196
s_nop 0
Ta 20,%0,16392
Ta 22,%0,16904
Mvaa 172,28,156
Da 68,v2,4096
Mvaa 164,32,192
L_ v6,28,s65,256
Mvaa 164,28,152
S_ s65,s59,1867776
Ta 24,%0,20488
Ta 26,%0,21000
Mvaa 156,32,188
S_ m0,s52,11008
Da 108,v3,4096
Mvaa 156,28,148
Mvaa 148,32,184
L_ v6,28,s65,256
Mvaa 148,28,144
Da 72,v2,6144
Mvaa 140,32,180
Mvaa 140,28,140
W_ 9
Mvaa 176,36,140
s_nop 0
Ta 28,v1,16384
Ta 30,v1,16896
Mvaa 176,224,180
Da 112,v3,6144
Mvaa 184,36,144
Mvaa 184,224,184
s_nop 0
Ta 32,v1,20480
Ta 34,v1,20992
Mvaa 192,36,148
Da 76,v2,8192
Mvaa 192,224,188
Mvaa 200,36,152
Da 116,v3,8192
Mvaa 200,224,192
Mvaa 208,36,156
Mvaa 208,224,196
W_ 9
Mvaa 212,24,196
s_nop 0
Ta 36,v1,16392
Ta 38,v1,16904
Mvaa 212,20,156
Mvaa 204,24,192
Mvaa 204,20,152
s_nop 0
Ta 224,v1,20488
Ta 226,v1,21000
Mvaa 196,24,188
Mvaa 196,20,148
Mvaa 188,24,184
Mvaa 188,20,144
Mvaa 180,24,180
s_waitcnt vmcnt(15) lgkmcnt(0)
s_barrier
Mvaa 180,20,140
S_ s65,s59,2359296
Mvaa 216,28,140
S_ m0,s52,16384
Ta 20,%0,24576
Ta 22,%0,25088
Mvaa 216,32,180
L_ v6,28,s65,0
Mvaa 224,28,144
S_ s65,s59,2457600
Mvaa 224,32,184
S_ m0,s52,17408
Ta 24,%0,28672
Ta 26,%0,29184
Mvaa 232,28,148
Mvaa 232,32,188
L_ v6,28,s65,0
Mvaa 240,28,152
S_ s65,s59,2555904
Mvaa 240,32,192
S_ m0,s52,18432
Mvaa 248,28,156
Mvaa 248,32,196
Mvaa 252,224,196
s_nop 0
Ta 28,%0,24584
Ta 30,%0,25096
Mvaa 252,36,156
L_ v6,28,s65,0
Mvaa 244,224,192
S_ s65,s59,2654208
Mvaa 244,36,152
S_ m0,s52,19456
Ta 32,%0,28680
Ta 34,%0,29192
Mvaa 236,224,188
Mvaa 236,36,148
L_ v6,28,s65,0
Mvaa 228,224,184
S_ s65,s60,512
Mvaa 228,36,144
S_ m0,s50,0
Mvaa 220,224,180
Mvaa 220,36,140
W_ 4
Mvaa 16,20,60
s_nop 0
Ta 36,v1,24576
Ta 38,v1,25088
Mvaa 16,24,100
L_ v4,24,s65,0
Mvaa 24,20,64
S_ s65,s60,10752
Mvaa 24,24,104
S_ m0,s50,1024
Ta 224,v1,28672
Ta 226,v1,29184
Mvaa 32,20,68
Mvaa 32,24,108
L_ v5,24,s65,0
Mvaa 40,20,72
S_ s65,s60,20992
Mvaa 40,24,112
S_ m0,s50,2048
Mvaa 48,20,76
Mvaa 48,24,116
W_ 4
Mvaa 52,32,116
s_nop 0
Ta 20,v1,24584
Ta 22,v1,25096
Mvaa 52,28,76
L_ v4,24,s65,0
Mvaa 44,32,112
S_ s65,s60,31232
Mvaa 44,28,72
S_ m0,s50,3072
Ta 24,v1,28680
Ta 26,v1,29192
Mvaa 36,32,108
Mvaa 36,28,68
L_ v5,24,s65,0
Mvaa 28,32,104
S_ s65,s60,41472
Mvaa 28,28,64
S_ m0,s50,4096
Mvaa 20,32,100
s_waitcnt vmcnt(14) lgkmcnt(0)
s_barrier
Mvaa 20,28,60
L_ v4,24,s65,0
Mvaa 56,36,60
S_ s65,s59,2359296
Ta 28,%0,0
Ta 30,%0,512
Mvaa 56,224,100
S_ m0,s52,24448
Mvaa 64,36,64
Mvaa 64,224,104
s_nop 0
Ta 32,%0,4096
Ta 34,%0,4608
Mvaa 72,36,68
L_ v6,28,s65,128
Mvaa 72,224,108
S_ s65,s59,2457600
Mvaa 80,36,72
S_ m0,s52,25472
Mvaa 80,224,112
Mvaa 88,36,76
L_ v6,28,s65,128
Mvaa 88,224,116
S_ s65,s59,2555904
Mvaa 92,24,116
S_ m0,s52,26496
Ta 36,%0,8
Ta 38,%0,520
Mvaa 92,20,76
S_ s55,s56,4718592
Mvaa 84,24,112
L_ v6,28,s65,128
Mvaa 84,20,72
S_ s65,s59,2654208
Ta 224,%0,4104
Ta 226,%0,4616
Mvaa 76,24,108
S_ m0,s52,27520
Mvaa 76,20,68
Mvaa 68,24,104
L_ v6,28,s65,128
Mvaa 68,20,64
Mvaa 60,24,100
Mvaa 60,20,60
W_ 4
Mvaa 96,28,60
s_nop 0
Ta 20,v1,0
Ta 22,v1,512
Mvaa 96,32,100
buffer_load_dword a254, v10, s[28:31], s55 offen
Mvaa 104,28,64
Mvaa 104,32,104
s_nop 0
Ta 24,v1,4096
Ta 26,v1,4608
Mvaa 112,28,68
Mvaa 112,32,108
buffer_load_dword a254, v12, s[24:27], s63 offen offset:768
Mvaa 120,28,72
Mvaa 120,32,112
Mvaa 128,28,76
Mvaa 128,32,116
W_ 4
Mvaa 132,224,116
s_nop 0
Ta 28,v1,8
Ta 30,v1,520
Mvaa 132,36,76
Mvaa 124,224,112
Mvaa 124,36,72
s_nop 0
Ta 32,v1,4104
Ta 34,v1,4616
Mvaa 116,224,108
Mvaa 116,36,68
Mvaa 108,224,104
Mvaa 108,36,64
Mvaa 100,224,100
s_waitcnt vmcnt(15) lgkmcnt(0)
s_barrier
Mvaa 100,36,60
S_ s65,s59,2359296
Da 140,v2,20480
Mvaa 136,20,60
S_ m0,s52,-256
Ta 36,%0,8192
Ta 38,%0,8704
Mvaa 136,24,100
L_ v6,28,s65,256
Mvaa 144,20,64
S_ s65,s59,2457600
Da 180,v3,20480
Mvaa 144,24,104
S_ m0,s52,768
Ta 224,%0,12288
Ta 226,%0,12800
Mvaa 152,20,68
Mvaa 152,24,108
Da 144,v2,22528
Mvaa 160,20,72
L_ v6,28,s65,256
Mvaa 160,24,112
S_ s65,s59,2555904
Mvaa 168,20,76
S_ m0,s52,1792
Da 184,v3,22528
Mvaa 168,24,116
Mvaa 172,32,116
s_nop 0
Ta 20,%0,8200
Ta 22,%0,8712
Mvaa 172,28,76
Da 148,v2,24576
Mvaa 164,32,112
L_ v6,28,s65,256
Mvaa 164,28,72
S_ s65,s59,2654208
Ta 24,%0,12296
Ta 26,%0,12808
Mvaa 156,32,108
S_ m0,s52,2816
Da 188,v3,24576
Mvaa 156,28,68
Mvaa 148,32,104
L_ v6,28,s65,256
Mvaa 148,28,64
Da 152,v2,26624
Mvaa 140,32,100
Mvaa 140,28,60
W_ 9
Mvaa 176,36,60
s_nop 0
Ta 28,v1,8192
Ta 30,v1,8704
Mvaa 176,224,100
Da 192,v3,26624
Mvaa 184,36,64
Mvaa 184,224,104
s_nop 0
Ta 32,v1,12288
Ta 34,v1,12800
Mvaa 192,36,68
Da 156,v2,28672
Mvaa 192,224,108
Mvaa 200,36,72
Da 196,v3,28672
Mvaa 200,224,112
Mvaa 208,36,76
Mvaa 208,224,116
W_ 9
Mvaa 212,24,116
s_nop 0
Ta 36,v1,8200
Ta 38,v1,8712
Mvaa 212,20,76
Mvaa 204,24,112
Mvaa 204,20,72
s_nop 0
Ta 224,v1,12296
Ta 226,v1,12808
Mvaa 196,24,108
Mvaa 196,20,68
Mvaa 188,24,104
Mvaa 188,20,64
Mvaa 180,24,100
s_waitcnt vmcnt(15) lgkmcnt(0)
s_barrier
Mvaa 180,20,60
S_ s65,s59,3145728
Mvaa 216,28,60
S_ m0,s52,8192
Ta 20,%0,16384
Ta 22,%0,16896
Mvaa 216,32,100
L_ v6,28,s65,0
Mvaa 224,28,64
S_ s65,s59,3244032
Mvaa 224,32,104
S_ m0,s52,9216
Ta 24,%0,20480
Ta 26,%0,20992
Mvaa 232,28,68
.Lpad_q1_1:
s_cbranch_execz .Lhop_q1_r
Mvaa 232,32,108
L_ v6,28,s65,0
Mvaa 240,28,72
S_ s65,s59,3342336
Mvaa 240,32,112
S_ m0,s52,10240
Mvaa 248,28,76
Mvaa 248,32,116
Mvaa 252,224,116
s_nop 0
Ta 28,%0,16392
Ta 30,%0,16904
Mvaa 252,36,76
L_ v6,28,s65,0
Mvaa 244,224,112
S_ s65,s59,3440640
Mvaa 244,36,72
S_ m0,s52,11264
Ta 32,%0,20488
Ta 34,%0,21000
Mvaa 236,224,108
Mvaa 236,36,68
L_ v6,28,s65,0
Mvaa 228,224,104
S_ s65,s60,640
Mvaa 228,36,64
S_ m0,s50,20480
Mvaa 220,224,100
Mvaa 220,36,60
W_ 4
Mvaa 16,20,140
s_nop 0
Ta 36,v1,16384
Ta 38,v1,16896
Mvaa 16,24,180
L_ v4,24,s65,0
Mvaa 24,20,144
S_ s65,s60,10880
Mvaa 24,24,184
S_ m0,s50,21504
Ta 224,v1,20480
Ta 226,v1,20992
Mvaa 32,20,148
Mvaa 32,24,188
L_ v5,24,s65,0
Mvaa 40,20,152
S_ s65,s60,21120
Mvaa 40,24,192
S_ m0,s50,22528
Mvaa 48,20,156
Mvaa 48,24,196
W_ 4
Mvaa 52,32,196
s_nop 0
Ta 20,v1,16392
Ta 22,v1,16904
Mvaa 52,28,156
L_ v4,24,s65,0
Mvaa 44,32,192
S_ s65,s60,31360
Mvaa 44,28,152
S_ m0,s50,23552
Ta 24,v1,20488
Ta 26,v1,21000
Mvaa 36,32,188
Mvaa 36,28,148
L_ v5,24,s65,0
Mvaa 28,32,184
S_ s65,s60,41600
Mvaa 28,28,144
S_ m0,s50,24576
Mvaa 20,32,180
s_waitcnt vmcnt(14) lgkmcnt(0)
s_barrier
Mvaa 20,28,140
L_ v4,24,s65,0
Mvaa 56,36,140
S_ s65,s59,3145728
Ta 28,%0,24576
Ta 30,%0,25088
Mvaa 56,224,180
S_ m0,s52,16256
Mvaa 64,36,144
Mvaa 64,224,184
s_nop 0
Ta 32,%0,28672
Ta 34,%0,29184
Mvaa 72,36,148
L_ v6,28,s65,128
Mvaa 72,224,188
S_ s65,s59,3244032
Mvaa 80,36,152
S_ m0,s52,17280
Mvaa 80,224,192
Mvaa 88,36,156
L_ v6,28,s65,128
Mvaa 88,224,196
S_ s65,s59,3342336
Mvaa 92,24,196
S_ m0,s52,18304
Ta 36,%0,24584
Ta 38,%0,25096
Mvaa 92,20,156
S_ s55,s56,5505024
Mvaa 84,24,192
L_ v6,28,s65,128
Mvaa 84,20,152
S_ s65,s59,3440640
Ta 224,%0,28680
Ta 226,%0,29192
Mvaa 76,24,188
S_ m0,s52,19328
Mvaa 76,20,148
Mvaa 68,24,184
L_ v6,28,s65,128
Mvaa 68,20,144
Mvaa 60,24,180
Mvaa 60,20,140
W_ 4
Mvaa 96,28,140
s_nop 0
Ta 20,v1,24576
Ta 22,v1,25088
Mvaa 96,32,180
buffer_load_dword a254, v10, s[28:31], s55 offen
Mvaa 104,28,144
Mvaa 104,32,184
s_nop 0
Ta 24,v1,28672
Ta 26,v1,29184
Mvaa 112,28,148
Mvaa 112,32,188
buffer_load_dword a254, v12, s[24:27], s63 offen offset:896
Mvaa 120,28,152
Mvaa 120,32,192
Mvaa 128,28,156
Mvaa 128,32,196
W_ 4
Mvaa 132,224,196
s_nop 0
Ta 28,v1,24584
Ta 30,v1,25096
Mvaa 132,36,156
Mvaa 124,224,192
Mvaa 124,36,152
s_nop 0
Ta 32,v1,28680
Ta 34,v1,29192
Mvaa 116,224,188
Mvaa 116,36,148
Mvaa 108,224,184
Mvaa 108,36,144
Mvaa 100,224,180
s_waitcnt vmcnt(15) lgkmcnt(0)
s_barrier
Mvaa 100,36,140
S_ s65,s59,3145728
Da 60,v2,0
Mvaa 136,20,140
S_ m0,s52,24320
Ta 36,%0,0
Ta 38,%0,512
Mvaa 136,24,180
L_ v6,28,s65,256
Mvaa 144,20,144
S_ s65,s59,3244032
Da 100,v3,0
Mvaa 144,24,184
S_ m0,s52,25344
Ta 224,%0,4096
Ta 226,%0,4608
Mvaa 152,20,148
Mvaa 152,24,188
Da 64,v2,2048
Mvaa 160,20,152
L_ v6,28,s65,256
Mvaa 160,24,192
S_ s65,s59,3342336
Mvaa 168,20,156
S_ m0,s52,26368
Da 104,v3,2048
Mvaa 168,24,196
Mvaa 172,32,196
s_nop 0
Ta 20,%0,8
Ta 22,%0,520
Mvaa 172,28,156
Da 68,v2,4096
Mvaa 164,32,192
L_ v6,28,s65,256
Mvaa 164,28,152
S_ s65,s59,3440640
Ta 24,%0,4104
Ta 26,%0,4616
Mvaa 156,32,188
S_ m0,s52,27392
Da 108,v3,4096
Mvaa 156,28,148
Mvaa 148,32,184
L_ v6,28,s65,256
Mvaa 148,28,144
Da 72,v2,6144
Mvaa 140,32,180
Mvaa 140,28,140
W_ 9
Mvaa 176,36,140
s_nop 0
Ta 28,v1,0
Ta 30,v1,512
Mvaa 176,224,180
Da 112,v3,6144
Mvaa 184,36,144
Mvaa 184,224,184
s_nop 0
Ta 32,v1,4096
Ta 34,v1,4608
Mvaa 192,36,148
Da 76,v2,8192
Mvaa 192,224,188
Mvaa 200,36,152
Da 116,v3,8192
Mvaa 200,224,192
Mvaa 208,36,156
Mvaa 208,224,196
W_ 9
Mvaa 212,24,196
s_nop 0
Ta 36,v1,8
Ta 38,v1,520
Mvaa 212,20,156
Mvaa 204,24,192
Mvaa 204,20,152
s_nop 0
Ta 224,v1,4104
Ta 226,v1,4616
Mvaa 196,24,188
Mvaa 196,20,148
Mvaa 188,24,184
Mvaa 188,20,144
Mvaa 180,24,180
s_waitcnt vmcnt(15) lgkmcnt(0)
s_barrier
Mvaa 180,20,140
S_ s65,s59,3932160
Mvaa 216,28,140
S_ m0,s52,0
Ta 20,%0,8192
Ta 22,%0,8704
Mvaa 216,32,180
L_ v6,28,s65,0
Mvaa 224,28,144
S_ s65,s59,4030464
Mvaa 224,32,184
S_ m0,s52,1024
Ta 24,%0,12288
Ta 26,%0,12800
Mvaa 232,28,148
Mvaa 232,32,188
L_ v6,28,s65,0
Mvaa 240,28,152
S_ s65,s59,4128768
Mvaa 240,32,192
S_ m0,s52,2048
Mvaa 248,28,156
Mvaa 248,32,196
Mvaa 252,224,196
s_nop 0
Ta 28,%0,8200
Ta 30,%0,8712
Mvaa 252,36,156
L_ v6,28,s65,0
Mvaa 244,224,192
S_ s65,s59,4227072
Mvaa 244,36,152
S_ m0,s52,3072
Ta 32,%0,12296
Ta 34,%0,12808
Mvaa 236,224,188
Mvaa 236,36,148
L_ v6,28,s65,0
Mvaa 228,224,184
S_ s65,s60,768
Mvaa 228,36,144
S_ m0,s50,0
Mvaa 220,224,180
Mvaa 220,36,140
W_ 4
Mvaa 16,20,60
s_nop 0
Ta 36,v1,8192
Ta 38,v1,8704
Mvaa 16,24,100
L_ v4,24,s65,0
Mvaa 24,20,64
S_ s65,s60,11008
R_ v13,a248
Mvaa 24,24,104
S_ m0,s50,1024
Ta 224,v1,12288
Ta 226,v1,12800
Mvaa 32,20,68
v_min_u32 v13, 240, v13
Mvaa 32,24,108
L_ v5,24,s65,0
Mvaa 40,20,72
S_ s65,s60,21248
v_lshlrev_b32 v13, 3, v13
Mvaa 40,24,112
S_ m0,s50,2048
Mvaa 48,20,76
Mvaa 48,24,116
W_ 4
Mvaa 52,32,116
s_nop 0
Ta 20,v1,8200
Ta 22,v1,8712
Mvaa 52,28,76
L_ v4,24,s65,0
Mvaa 44,32,112
S_ s65,s60,31488
Mvaa 44,28,72
S_ m0,s50,3072
Ta 24,v1,12296
Ta 26,v1,12808
Mvaa 36,32,108
Mvaa 36,28,68
L_ v5,24,s65,0
Mvaa 28,32,104
S_ s65,s60,41728
Mvaa 28,28,64
S_ m0,s50,4096
Mvaa 20,32,100
s_waitcnt vmcnt(14) lgkmcnt(0)
s_barrier
Mvaa 20,28,60
L_ v4,24,s65,0
Mvaa 56,36,60
S_ s65,s59,3932160
Ta 28,%0,16384
Ta 30,%0,16896
Mvaa 56,224,100
S_ m0,s52,8064
Mvaa 64,36,64
Mvaa 64,224,104
s_nop 0
Ta 32,%0,20480
Ta 34,%0,20992
Mvaa 72,36,68
L_ v6,28,s65,128
Mvaa 72,224,108
S_ s65,s59,4030464
Mvaa 80,36,72
S_ m0,s52,9088
Mvaa 80,224,112
Mvaa 88,36,76
L_ v6,28,s65,128
Mvaa 88,224,116
S_ s65,s59,4128768
Mvaa 92,24,116
S_ m0,s52,10112
Ta 36,%0,16392
Ta 38,%0,16904
Mvaa 92,20,76
S_ s55,s56,6291456
Mvaa 84,24,112
L_ v6,28,s65,128
Mvaa 84,20,72
S_ s65,s59,4227072
Ta 224,%0,20488
Ta 226,%0,21000
Mvaa 76,24,108
S_ m0,s52,11136
Mvaa 76,20,68
Mvaa 68,24,104
L_ v6,28,s65,128
Mvaa 68,20,64
Mvaa 60,24,100
Mvaa 60,20,60
W_ 4
Mvaa 96,28,60
s_nop 0
Ta 20,v1,16384
Ta 22,v1,16896
Mvaa 96,32,100
buffer_load_dword a220, v13, s[44:47], s66 offen
Mvaa 104,28,64
Mvaa 104,32,104
s_nop 0
Ta 24,v1,20480
Ta 26,v1,20992
Mvaa 112,28,68
Mvaa 112,32,108
buffer_load_dword a254, v10, s[28:31], s55 offen
Mvaa 120,28,72
Mvaa 120,32,112
Mvaa 128,28,76
Mvaa 128,32,116
W_ 4
Mvaa 132,224,116
s_nop 0
Ta 28,v1,16392
Ta 30,v1,16904
Mvaa 132,36,76
buffer_load_dword a254, v12, s[24:27], s63 offen offset:1024
Mvaa 124,224,112
Mvaa 124,36,72
s_nop 0
Ta 32,v1,20488
Ta 34,v1,21000
Mvaa 116,224,108
Mvaa 116,36,68
Mvaa 108,224,104
Mvaa 108,36,64
Mvaa 100,224,100
s_waitcnt vmcnt(16) lgkmcnt(0)
s_barrier
Mvaa 100,36,60
S_ s65,s59,3932160
Da 140,v2,20480
Mvaa 136,20,60
S_ m0,s52,16128
Ta 36,%0,24576
Ta 38,%0,25088
Mvaa 136,24,100
L_ v6,28,s65,256
Mvaa 144,20,64
S_ s65,s59,4030464
Da 180,v3,20480
Mvaa 144,24,104
S_ m0,s52,17152
Ta 224,%0,28672
Ta 226,%0,29184
Mvaa 152,20,68
Mvaa 152,24,108
Da 144,v2,22528
Mvaa 160,20,72
L_ v6,28,s65,256
Mvaa 160,24,112
S_ s65,s59,4128768
Mvaa 168,20,76
S_ m0,s52,18176
Da 184,v3,22528
Mvaa 168,24,116
Mvaa 172,32,116
s_nop 0
Ta 20,%0,24584
Ta 22,%0,25096
Mvaa 172,28,76
Da 148,v2,24576
Mvaa 164,32,112
L_ v6,28,s65,256
Mvaa 164,28,72
S_ s65,s59,4227072
Ta 24,%0,28680
Ta 26,%0,29192
Mvaa 156,32,108
S_ m0,s52,19200
Da 188,v3,24576
Mvaa 156,28,68
Mvaa 148,32,104
L_ v6,28,s65,256
Mvaa 148,28,64
Da 152,v2,26624
Mvaa 140,32,100
Mvaa 140,28,60
W_ 9
Mvaa 176,36,60
s_nop 0
Ta 28,v1,24576
Ta 30,v1,25088
Mvaa 176,224,100
Da 192,v3,26624
Mvaa 184,36,64
Mvaa 184,224,104
s_nop 0
Ta 32,v1,28672
Ta 34,v1,29184
Mvaa 192,36,68
Da 156,v2,28672
Mvaa 192,224,108
Mvaa 200,36,72
Da 196,v3,28672
Mvaa 200,224,112
Mvaa 208,36,76
Mvaa 208,224,116
W_ 9
Mvaa 212,24,116
s_nop 0
Ta 36,v1,24584
Ta 38,v1,25096
Mvaa 212,20,76
Mvaa 204,24,112
Mvaa 204,20,72
s_nop 0
Ta 224,v1,28680
Ta 226,v1,29192
Mvaa 196,24,108
Mvaa 196,20,68
Mvaa 188,24,104
Mvaa 188,20,64
Mvaa 180,24,100
s_waitcnt vmcnt(16) lgkmcnt(0)
s_barrier
Mvaa 180,20,60
S_ s65,s59,4718592
Mvaa 216,28,60
S_ m0,s52,24576
Ta 20,%0,0
Ta 22,%0,512
Mvaa 216,32,100
L_ v6,28,s65,0
Mvaa 224,28,64
S_ s65,s59,4816896
Mvaa 224,32,104
S_ m0,s52,25600
Ta 24,%0,4096
Ta 26,%0,4608
Mvaa 232,28,68
Mvaa 232,32,108
L_ v6,28,s65,0
Mvaa 240,28,72
S_ s65,s59,4915200
Mvaa 240,32,112
S_ m0,s52,26624
Mvaa 248,28,76
Mvaa 248,32,116
Mvaa 252,224,116
s_nop 0
Ta 28,%0,8
Ta 30,%0,520
Mvaa 252,36,76
L_ v6,28,s65,0
Mvaa 244,224,112
S_ s65,s59,5013504
Mvaa 244,36,72
S_ m0,s52,27648
Ta 32,%0,4104
Ta 34,%0,4616
Mvaa 236,224,108
.Lpad_q1_2:
s_cbranch_execz .Lhop_q1_r
Mvaa 236,36,68
L_ v6,28,s65,0
Mvaa 228,224,104
S_ s65,s60,896
Mvaa 228,36,64
S_ m0,s50,20480
Mvaa 220,224,100
Mvaa 220,36,60
W_ 4
Mvaa 16,20,140
s_nop 0
Ta 36,v1,0
Ta 38,v1,512
Mvaa 16,24,180
L_ v4,24,s65,0
Mvaa 24,20,144
S_ s65,s60,11136
Mvaa 24,24,184
S_ m0,s50,21504
Ta 224,v1,4096
Ta 226,v1,4608
Mvaa 32,20,148
Mvaa 32,24,188
L_ v5,24,s65,0
Mvaa 40,20,152
S_ s65,s60,21376
Mvaa 40,24,192
S_ m0,s50,22528
Mvaa 48,20,156
Mvaa 48,24,196
W_ 4
Mvaa 52,32,196
s_nop 0
Ta 20,v1,8
Ta 22,v1,520
Mvaa 52,28,156
L_ v4,24,s65,0
Mvaa 44,32,192
S_ s65,s60,31616
Mvaa 44,28,152
S_ m0,s50,23552
Ta 24,v1,4104
Ta 26,v1,4616
Mvaa 36,32,188
Mvaa 36,28,148
L_ v5,24,s65,0
Mvaa 28,32,184
S_ s65,s60,41856
Mvaa 28,28,144
S_ m0,s50,24576
Mvaa 20,32,180
s_waitcnt vmcnt(15) lgkmcnt(0)
s_barrier
Mvaa 20,28,140
L_ v4,24,s65,0
Mvaa 56,36,140
S_ s65,s59,4718592
Ta 28,%0,8192
Ta 30,%0,8704
Mvaa 56,224,180
S_ m0,s52,-128
Mvaa 64,36,144
Mvaa 64,224,184
s_nop 0
Ta 32,%0,12288
Ta 34,%0,12800
Mvaa 72,36,148
L_ v6,28,s65,128
Mvaa 72,224,188
S_ s65,s59,4816896
Mvaa 80,36,152
S_ m0,s52,896
Mvaa 80,224,192
Mvaa 88,36,156
L_ v6,28,s65,128
Mvaa 88,224,196
S_ s65,s59,4915200
Mvaa 92,24,196
S_ m0,s52,1920
Ta 36,%0,8200
Ta 38,%0,8712
Mvaa 92,20,156
S_ s55,s56,7077888
Mvaa 84,24,192
L_ v6,28,s65,128
Mvaa 84,20,152
S_ s65,s59,5013504
Ta 224,%0,12296
Ta 226,%0,12808
Mvaa 76,24,188
S_ m0,s52,2944
Mvaa 76,20,148
Mvaa 68,24,184
L_ v6,28,s65,128
Mvaa 68,20,144
Mvaa 60,24,180
Mvaa 60,20,140
W_ 4
Mvaa 96,28,140
s_nop 0
Ta 20,v1,8192
Ta 22,v1,8704
Mvaa 96,32,180
buffer_load_dword a254, v10, s[28:31], s55 offen
Mvaa 104,28,144
Mvaa 104,32,184
s_nop 0
Ta 24,v1,12288
Ta 26,v1,12800
Mvaa 112,28,148
Mvaa 112,32,188
buffer_load_dword a254, v12, s[24:27], s63 offen offset:1152
Mvaa 120,28,152
Mvaa 120,32,192
Mvaa 128,28,156
Mvaa 128,32,196
W_ 4
Mvaa 132,224,196
s_nop 0
Ta 28,v1,8200
Ta 30,v1,8712
Mvaa 132,36,156
Mvaa 124,224,192
Mvaa 124,36,152
s_nop 0
Ta 32,v1,12296
Ta 34,v1,12808
Mvaa 116,224,188
Mvaa 116,36,148
Mvaa 108,224,184
Mvaa 108,36,144
Mvaa 100,224,180
s_waitcnt vmcnt(15) lgkmcnt(0)
s_barrier
Mvaa 100,36,140
S_ s65,s59,4718592
Da 60,v2,0
Mvaa 136,20,140
S_ m0,s52,7936
Ta 36,%0,16384
Ta 38,%0,16896
Mvaa 136,24,180
L_ v6,28,s65,256
Mvaa 144,20,144
S_ s65,s59,4816896
Da 100,v3,0
Mvaa 144,24,184
S_ m0,s52,8960
Ta 224,%0,20480
Ta 226,%0,20992
Mvaa 152,20,148
Mvaa 152,24,188
Da 64,v2,2048
Mvaa 160,20,152
L_ v6,28,s65,256
Mvaa 160,24,192
S_ s65,s59,4915200
Mvaa 168,20,156
S_ m0,s52,9984
Da 104,v3,2048
Mvaa 168,24,196
Mvaa 172,32,196
s_nop 0
Ta 20,%0,16392
Ta 22,%0,16904
Mvaa 172,28,156
Da 68,v2,4096
Mvaa 164,32,192
L_ v6,28,s65,256
Mvaa 164,28,152
S_ s65,s59,5013504
Ta 24,%0,20488
Ta 26,%0,21000
Mvaa 156,32,188
S_ m0,s52,11008
Da 108,v3,4096
Mvaa 156,28,148
Mvaa 148,32,184
L_ v6,28,s65,256
)A"
R"A(Mvaa 148,28,144
Da 72,v2,6144
Mvaa 140,32,180
Mvaa 140,28,140
W_ 9
Mvaa 176,36,140
s_nop 0
Ta 28,v1,16384
Ta 30,v1,16896
Mvaa 176,224,180
Da 112,v3,6144
Mvaa 184,36,144
Mvaa 184,224,184
s_nop 0
Ta 32,v1,20480
Ta 34,v1,20992
Mvaa 192,36,148
Da 76,v2,8192
Mvaa 192,224,188
Mvaa 200,36,152
Da 116,v3,8192
Mvaa 200,224,192
Mvaa 208,36,156
Mvaa 208,224,196
W_ 9
Mvaa 212,24,196
s_nop 0
Ta 36,v1,16392
Ta 38,v1,16904
Mvaa 212,20,156
Mvaa 204,24,192
Mvaa 204,20,152
s_nop 0
Ta 224,v1,20488
Ta 226,v1,21000
Mvaa 196,24,188
Mvaa 196,20,148
Mvaa 188,24,184
Mvaa 188,20,144
Mvaa 180,24,180
s_waitcnt vmcnt(15) lgkmcnt(0)
s_barrier
Mvaa 180,20,140
S_ s65,s59,5505024
Mvaa 216,28,140
S_ m0,s52,16384
Ta 20,%0,24576
Ta 22,%0,25088
Mvaa 216,32,180
L_ v6,28,s65,0
Mvaa 224,28,144
S_ s65,s59,5603328
Mvaa 224,32,184
S_ m0,s52,17408
Ta 24,%0,28672
Ta 26,%0,29184
Mvaa 232,28,148
Mvaa 232,32,188
L_ v6,28,s65,0
Mvaa 240,28,152
S_ s65,s59,5701632
Mvaa 240,32,192
S_ m0,s52,18432
Mvaa 248,28,156
Mvaa 248,32,196
Mvaa 252,224,196
s_nop 0
Ta 28,%0,24584
Ta 30,%0,25096
Mvaa 252,36,156
L_ v6,28,s65,0
Mvaa 244,224,192
S_ s65,s59,5799936
Mvaa 244,36,152
S_ m0,s52,19456
Ta 32,%0,28680
Ta 34,%0,29192
Mvaa 236,224,188
Mvaa 236,36,148
L_ v6,28,s65,0
Mvaa 228,224,184
S_ s65,s60,1024
Mvaa 228,36,144
S_ m0,s50,0
Mvaa 220,224,180
Mvaa 220,36,140
W_ 4
Mvaa 16,20,60
s_nop 0
Ta 36,v1,24576
Ta 38,v1,25088
Mvaa 16,24,100
L_ v4,24,s65,0
Mvaa 24,20,64
S_ s65,s60,11264
Mvaa 24,24,104
S_ m0,s50,1024
Ta 224,v1,28672
Ta 226,v1,29184
Mvaa 32,20,68
Mvaa 32,24,108
L_ v5,24,s65,0
Mvaa 40,20,72
S_ s65,s60,21504
Mvaa 40,24,112
S_ m0,s50,2048
Mvaa 48,20,76
Mvaa 48,24,116
W_ 4
Mvaa 52,32,116
s_nop 0
Ta 20,v1,24584
Ta 22,v1,25096
Mvaa 52,28,76
L_ v4,24,s65,0
Mvaa 44,32,112
S_ s65,s60,31744
Mvaa 44,28,72
S_ m0,s50,3072
Ta 24,v1,28680
Ta 26,v1,29192
Mvaa 36,32,108
Mvaa 36,28,68
L_ v5,24,s65,0
Mvaa 28,32,104
S_ s65,s60,41984
Mvaa 28,28,64
S_ m0,s50,4096
Mvaa 20,32,100
s_waitcnt vmcnt(14) lgkmcnt(0)
s_barrier
Mvaa 20,28,60
L_ v4,24,s65,0
Mvaa 56,36,60
S_ s65,s59,5505024
Ta 28,%0,0
Ta 30,%0,512
Mvaa 56,224,100
S_ m0,s52,24448
Mvaa 64,36,64
Mvaa 64,224,104
s_nop 0
Ta 32,%0,4096
Ta 34,%0,4608
Mvaa 72,36,68
L_ v6,28,s65,128
Mvaa 72,224,108
S_ s65,s59,5603328
Mvaa 80,36,72
S_ m0,s52,25472
Mvaa 80,224,112
Mvaa 88,36,76
L_ v6,28,s65,128
Mvaa 88,224,116
S_ s65,s59,5701632
Mvaa 92,24,116
S_ m0,s52,26496
Ta 36,%0,8
Ta 38,%0,520
Mvaa 92,20,76
Mvaa 84,24,112
L_ v6,28,s65,128
Mvaa 84,20,72
S_ s65,s59,5799936
Ta 224,%0,4104
Ta 226,%0,4616
Mvaa 76,24,108
S_ m0,s52,27520
Mvaa 76,20,68
Mvaa 68,24,104
L_ v6,28,s65,128
Mvaa 68,20,64
Mvaa 60,24,100
Mvaa 60,20,60
W_ 4
Mvaa 96,28,60
s_nop 0
Ta 20,v1,0
Ta 22,v1,512
Mvaa 96,32,100
Mvaa 104,28,64
Mvaa 104,32,104
s_nop 0
Ta 24,v1,4096
Ta 26,v1,4608
Mvaa 112,28,68
Mvaa 112,32,108
Mvaa 120,28,72
Mvaa 120,32,112
Mvaa 128,28,76
Mvaa 128,32,116
W_ 4
Mvaa 132,224,116
s_nop 0
Ta 28,v1,8
Ta 30,v1,520
Mvaa 132,36,76
Mvaa 124,224,112
Mvaa 124,36,72
s_nop 0
Ta 32,v1,4104
Ta 34,v1,4616
Mvaa 116,224,108
Mvaa 116,36,68
Mvaa 108,224,104
Mvaa 108,36,64
Mvaa 100,224,100
s_waitcnt vmcnt(13) lgkmcnt(0)
s_barrier
Mvaa 100,36,60
S_ s65,s59,5505024
Da 140,v2,20480
Mvaa 136,20,60
S_ m0,s52,-256
Ta 36,%0,8192
Ta 38,%0,8704
Mvaa 136,24,100
L_ v6,28,s65,256
Mvaa 144,20,64
S_ s65,s59,5603328
Da 180,v3,20480
Mvaa 144,24,104
S_ m0,s52,768
Ta 224,%0,12288
Ta 226,%0,12800
Mvaa 152,20,68
Mvaa 152,24,108
Da 144,v2,22528
Mvaa 160,20,72
L_ v6,28,s65,256
Mvaa 160,24,112
S_ s65,s59,5701632
Mvaa 168,20,76
S_ m0,s52,1792
Da 184,v3,22528
Mvaa 168,24,116
Mvaa 172,32,116
s_nop 0
Ta 20,%0,8200
Ta 22,%0,8712
Mvaa 172,28,76
Da 148,v2,24576
Mvaa 164,32,112
L_ v6,28,s65,256
Mvaa 164,28,72
S_ s65,s59,5799936
Ta 24,%0,12296
Ta 26,%0,12808
Mvaa 156,32,108
S_ m0,s52,2816
Da 188,v3,24576
Mvaa 156,28,68
Mvaa 148,32,104
L_ v6,28,s65,256
Mvaa 148,28,64
Da 152,v2,26624
Mvaa 140,32,100
Mvaa 140,28,60
W_ 9
Mvaa 176,36,60
s_nop 0
Ta 28,v1,8192
Ta 30,v1,8704
Mvaa 176,224,100
Da 192,v3,26624
Mvaa 184,36,64
Mvaa 184,224,104
s_nop 0
Ta 32,v1,12288
Ta 34,v1,12800
Mvaa 192,36,68
Da 156,v2,28672
Mvaa 192,224,108
Mvaa 200,36,72
Da 196,v3,28672
Mvaa 200,224,112
Mvaa 208,36,76
Mvaa 208,224,116
W_ 9
Mvaa 212,24,116
s_nop 0
Ta 36,v1,8200
Ta 38,v1,8712
Mvaa 212,20,76
Mvaa 204,24,112
Mvaa 204,20,72
s_nop 0
Ta 224,v1,12296
Ta 226,v1,12808
Mvaa 196,24,108
Mvaa 196,20,68
Mvaa 188,24,104
Mvaa 188,20,64
Mvaa 180,24,100
s_waitcnt vmcnt(13) lgkmcnt(0)
s_barrier
Mvaa 180,20,60
S_ s65,s59,6291456
Mvaa 216,28,60
S_ m0,s52,7936
Ta 20,%0,16384
Ta 22,%0,16896
Mvaa 216,32,100
L_ v6,28,s65,256
Mvaa 224,28,64
S_ s65,s59,6389760
Mvaa 224,32,104
S_ m0,s52,8960
Ta 24,%0,20480
Ta 26,%0,20992
Mvaa 232,28,68
Mvaa 232,32,108
L_ v6,28,s65,256
Mvaa 240,28,72
S_ s65,s59,6488064
Mvaa 240,32,112
S_ m0,s52,9984
Mvaa 248,28,76
Mvaa 248,32,116
Mvaa 252,224,116
s_nop 0
Ta 28,%0,16392
Ta 30,%0,16904
Mvaa 252,36,76
L_ v6,28,s65,256
Mvaa 244,224,112
S_ s65,s59,6586368
Mvaa 244,36,72
S_ m0,s52,11008
Ta 32,%0,20488
Ta 34,%0,21000
Mvaa 236,224,108
Mvaa 236,36,68
L_ v6,28,s65,256
Mvaa 228,224,104
S_ s65,s60,1152
Mvaa 228,36,64
S_ m0,s50,20480
Mvaa 220,224,100
Mvaa 220,36,60
W_ 4
Mvaa 16,20,140
s_nop 0
Ta 36,v1,16384
Ta 38,v1,16896
Mvaa 16,24,180
L_ v4,24,s65,0
Mvaa 24,20,144
S_ s65,s60,11392
Mvaa 24,24,184
S_ m0,s50,21504
Ta 224,v1,20480
Ta 226,v1,20992
Mvaa 32,20,148
.Lpad_q1_3:
s_cbranch_execz .Lhop_q1_r
Mvaa 32,24,188
L_ v5,24,s65,0
Mvaa 40,20,152
S_ s65,s60,21632
Mvaa 40,24,192
S_ m0,s50,22528
Mvaa 48,20,156
Mvaa 48,24,196
W_ 4
Mvaa 52,32,196
s_nop 0
Ta 20,v1,16392
Ta 22,v1,16904
Mvaa 52,28,156
L_ v4,24,s65,0
Mvaa 44,32,192
S_ s65,s60,31872
Mvaa 44,28,152
S_ m0,s50,23552
Ta 24,v1,20488
Ta 26,v1,21000
Mvaa 36,32,188
Mvaa 36,28,148
L_ v5,24,s65,0
Mvaa 28,32,184
S_ s65,s60,42112
Mvaa 28,28,144
S_ m0,s50,24576
Mvaa 20,32,180
s_waitcnt vmcnt(12) lgkmcnt(0)
s_barrier
Mvaa 20,28,140
L_ v4,24,s65,0
Mvaa 56,36,140
S_ s65,s59,7077888
Ta 28,%0,24576
Ta 30,%0,25088
Mvaa 56,224,180
S_ m0,s52,16128
Mvaa 64,36,144
Mvaa 64,224,184
s_nop 0
Ta 32,%0,28672
Ta 34,%0,29184
Mvaa 72,36,148
L_ v6,28,s65,256
Mvaa 72,224,188
S_ s65,s59,7176192
Mvaa 80,36,152
S_ m0,s52,17152
Mvaa 80,224,192
Mvaa 88,36,156
L_ v6,28,s65,256
Mvaa 88,224,196
S_ s65,s59,7274496
Mvaa 92,24,196
S_ m0,s52,18176
Ta 36,%0,24584
Ta 38,%0,25096
Mvaa 92,20,156
Mvaa 84,24,192
L_ v6,28,s65,256
Mvaa 84,20,152
S_ s65,s59,7372800
Ta 224,%0,28680
Ta 226,%0,29192
Mvaa 76,24,188
S_ m0,s52,19200
Mvaa 76,20,148
Mvaa 68,24,184
L_ v6,28,s65,256
Mvaa 68,20,144
Mvaa 60,24,180
Mvaa 60,20,140
W_ 4
Mvaa 96,28,140
s_nop 0
Ta 20,v1,24576
Ta 22,v1,25088
Mvaa 96,32,180
Mvaa 104,28,144
Mvaa 104,32,184
s_nop 0
Ta 24,v1,28672
Ta 26,v1,29184
Mvaa 112,28,148
Mvaa 112,32,188
Mvaa 120,28,152
Mvaa 120,32,192
Mvaa 128,28,156
Mvaa 128,32,196
W_ 4
Mvaa 132,224,196
s_nop 0
Ta 28,v1,24584
Ta 30,v1,25096
Mvaa 132,36,156
Mvaa 124,224,192
Mvaa 124,36,152
s_nop 0
Ta 32,v1,28680
Ta 34,v1,29192
Mvaa 116,224,188
Mvaa 116,36,148
Mvaa 108,224,184
Mvaa 108,36,144
Mvaa 100,224,180
s_waitcnt vmcnt(13) lgkmcnt(0)
s_barrier
Mvaa 100,36,140
S_ s65,s59,6291456
Da 60,v2,0
Mvaa 136,20,140
S_ m0,s52,24448
Ta 36,%0,0
Ta 38,%0,512
Mvaa 136,24,180
L_ v6,28,s65,128
Mvaa 144,20,144
S_ s65,s59,6389760
Da 100,v3,0
Mvaa 144,24,184
S_ m0,s52,25472
Ta 224,%0,4096
Ta 226,%0,4608
Mvaa 152,20,148
Mvaa 152,24,188
Da 64,v2,2048
Mvaa 160,20,152
L_ v6,28,s65,128
Mvaa 160,24,192
S_ s65,s59,6488064
Mvaa 168,20,156
S_ m0,s52,26496
Da 104,v3,2048
Mvaa 168,24,196
Mvaa 172,32,196
s_nop 0
Ta 20,%0,8
Ta 22,%0,520
Mvaa 172,28,156
Da 68,v2,4096
Mvaa 164,32,192
L_ v6,28,s65,128
Mvaa 164,28,152
S_ s65,s59,6586368
Ta 24,%0,4104
Ta 26,%0,4616
Mvaa 156,32,188
S_ m0,s52,27520
Da 108,v3,4096
Mvaa 156,28,148
Mvaa 148,32,184
L_ v6,28,s65,128
Mvaa 148,28,144
Da 72,v2,6144
Mvaa 140,32,180
Mvaa 140,28,140
W_ 9
Mvaa 176,36,140
s_nop 0
Ta 28,v1,0
Ta 30,v1,512
Mvaa 176,224,180
Da 112,v3,6144
Mvaa 184,36,144
Mvaa 184,224,184
s_nop 0
Ta 32,v1,4096
Ta 34,v1,4608
Mvaa 192,36,148
Da 76,v2,8192
Mvaa 192,224,188
Mvaa 200,36,152
Da 116,v3,8192
Mvaa 200,224,192
Mvaa 208,36,156
Mvaa 208,224,196
W_ 9
Mvaa 212,24,196
s_nop 0
Ta 36,v1,8
Ta 38,v1,520
Mvaa 212,20,156
Mvaa 204,24,192
Mvaa 204,20,152
s_nop 0
Ta 224,v1,4104
Ta 226,v1,4616
Mvaa 196,24,188
Mvaa 196,20,148
Mvaa 188,24,184
Mvaa 188,20,144
Mvaa 180,24,180
s_waitcnt vmcnt(8) lgkmcnt(0)
s_barrier
Mvaa 180,20,140
S_ s65,s59,7077888
Mvaa 216,28,140
S_ m0,s52,-128
Ta 20,v1,8200
Ta 22,v1,8712
Mvaa 216,32,180
L_ v6,28,s65,128
Mvaa 224,28,144
S_ s65,s59,7176192
Mvaa 224,32,184
S_ m0,s52,896
Ta 24,v1,12296
Ta 26,v1,12808
Mvaa 232,28,148
Mvaa 232,32,188
L_ v6,28,s65,128
Mvaa 240,28,152
S_ s65,s59,7274496
Mvaa 240,32,192
S_ m0,s52,1920
Mvaa 248,28,156
Mvaa 248,32,196
Mvaa 252,224,196
s_nop 0
Ta 28,v1,8192
Ta 30,v1,8704
Mvaa 252,36,156
L_ v6,28,s65,128
Mvaa 244,224,192
S_ s65,s59,7372800
Mvaa 244,36,152
S_ m0,s52,2944
Ta 32,v1,12288
Ta 34,v1,12800
Mvaa 236,224,188
Mvaa 236,36,148
L_ v6,28,s65,128
Mvaa 228,224,184
S_ s65,s60,0
Mvaa 228,36,144
S_ m0,s50,0
Mvaa 220,224,180
Mvaa 220,36,140
W_ 4
Mvaa 220,20,60
s_nop 0
Ta 36,%0,8200
Ta 38,%0,8712
Mvaa 220,24,100
Da 140,v2,20480
Mvaa 228,20,64
L_ v4,24,s65,0
Mvaa 228,24,104
S_ s65,s60,10240
Ta 224,%0,12296
Ta 226,%0,12808
Mvaa 236,20,68
S_ m0,s50,1024
Da 180,v3,20480
Mvaa 236,24,108
Mvaa 244,20,72
Da 144,v2,22528
Mvaa 244,24,112
L_ v5,24,s65,0
Mvaa 252,20,76
S_ s65,s60,20480
Mvaa 252,24,116
W_ 7
Da 184,v3,22528
S_ m0,s50,2048
Mvaa 248,32,116
s_nop 0
Ta 20,%0,8192
Ta 22,%0,8704
Mvaa 248,28,76
L_ v4,24,s65,0
Mvaa 240,32,112
S_ s65,s60,30720
Da 148,v2,24576
Mvaa 240,28,72
S_ m0,s50,3072
Ta 24,%0,12288
Ta 26,%0,12800
Mvaa 232,32,108
Mvaa 232,28,68
Da 188,v3,24576
Mvaa 224,32,104
L_ v5,24,s65,0
Mvaa 224,28,64
S_ s65,s60,40960
Mvaa 216,32,100
s_mov_b64 exec, 0
s_bitcmp1_b32 s4, 1
s_cbranch_scc1 .Lhop_q2_b
s_bitcmp1_b32 s4, 0
s_cbranch_scc1 .Lpad_q2_1
s_branch .Lpad_q2_0
.Lhop_q2_b:
s_bitcmp1_b32 s4, 0
s_cbranch_scc1 .Lpad_q2_3
s_branch .Lpad_q2_2
.Lhop_q2_r:
s_mov_b64 exec, -1
s_nop 1
.Lqy_late:
s_waitcnt vmcnt(12) lgkmcnt(0)
s_barrier
Da 152,v2,26624
Mvaa 216,28,60
S_ m0,s50,4096
Mvaa 180,36,60
s_nop 0
Ta 28,v1,16392
Ta 30,v1,16904
Mvaa 180,224,100
Da 192,v3,26624
Mvaa 188,36,64
L_ v4,24,s65,0
Mvaa 188,224,104
S_ s65,s59,6291456
Ta 32,v1,20488
Ta 34,v1,21000
Mvaa 196,36,68
S_ m0,s52,8192
Da 156,v2,28672
Mvaa 196,224,108
Mvaa 204,36,72
L_ v6,28,s65,0
Mvaa 204,224,112
S_ s65,s59,6389760
Da 196,v3,28672
Mvaa 212,36,76
S_ m0,s52,9216
Mvaa 212,224,116
Mvaa 208,24,116
s_nop 0
Ta 36,v1,16384
Ta 38,v1,16896
Mvaa 208,20,76
L_ v6,28,s65,0
Mvaa 200,24,112
S_ s65,s59,6488064
Mvaa 200,20,72
S_ m0,s52,10240
Ta 224,v1,20480
Ta 226,v1,20992
Mvaa 192,24,108
Mvaa 192,20,68
L_ v6,28,s65,0
Mvaa 184,24,104
S_ s65,s59,6586368
Mvaa 184,20,64
S_ m0,s52,11264
Mvaa 176,24,100
Mvaa 176,20,60
W_ 6
Mvaa 220,28,140
s_nop 0
Ta 20,%0,16392
Ta 22,%0,16904
Mvaa 220,32,180
L_ v6,28,s65,0
Mvaa 228,28,144
Mvaa 228,32,184
s_nop 0
Ta 24,%0,20488
Ta 26,%0,21000
Mvaa 236,28,148
Mvaa 236,32,188
Mvaa 244,28,152
Mvaa 244,32,192
W_ 9
Mvaa 252,28,156
W_ 8
Mvaa 252,32,196
W_ 4
Mvaa 248,224,196
s_nop 0
Ta 28,%0,16384
Ta 30,%0,16896
Mvaa 248,36,156
Mvaa 240,224,192
Mvaa 240,36,152
s_nop 0
Ta 32,%0,20480
Ta 34,%0,20992
Mvaa 232,224,188
Mvaa 232,36,148
Mvaa 224,224,184
Mvaa 224,36,144
Mvaa 216,224,180
s_waitcnt vmcnt(13) lgkmcnt(0)
s_barrier
Mvaa 216,36,140
S_ s65,s59,7077888
Mvaa 180,20,140
S_ m0,s52,16384
Ta 36,v1,24584
Ta 38,v1,25096
Mvaa 180,24,180
L_ v6,28,s65,0
Mvaa 188,20,144
S_ s65,s59,7176192
Mvaa 188,24,184
S_ m0,s52,17408
Ta 224,v1,28680
Ta 226,v1,29192
Mvaa 196,20,148
U_ v248,s48,v248
U_ v249,s48,v249
Mvaa 196,24,188
L_ v6,28,s65,0
Mvaa 204,20,152
S_ s65,s59,7274496
U_ v250,s48,v250
U_ v251,s48,v251
Mvaa 204,24,192
S_ m0,s52,18432
U_ v252,s48,v252
U_ v253,s48,v253
Mvaa 212,20,156
U_ v254,s48,v254
U_ v255,s48,v255
Mvaa 212,24,196
C_ 255,254,255
C_ 254,252,253
Mvaa 208,32,196
s_nop 0
Ta 20,v1,24576
Ta 22,v1,25088
Mvaa 208,28,156
L_ v6,28,s65,0
Mvaa 200,32,192
S_ s65,s59,7372800
C_ 253,250,251
C_ 252,248,249
Mvaa 200,28,152
S_ m0,s52,19456
Ta 24,v1,28672
Ta 26,v1,29184
Mvaa 192,32,188
X_ a216,v252
X_ a217,v253
Mvaa 192,28,148
L_ v6,28,s65,0
Mvaa 184,32,184
X_ a218,v254
X_ a219,v255
Mvaa 184,28,144
U_ v240,s48,v240
U_ v241,s48,v241
Mvaa 176,32,180
U_ v242,s48,v242
U_ v243,s48,v243
Mvaa 176,28,140
W_ 4
U_ v244,s48,v244
U_ v245,s48,v245
Mvaa 140,36,60
s_nop 0
Ta 28,%0,24584
Ta 30,%0,25096
Mvaa 140,224,100
U_ v246,s48,v246
U_ v247,s48,v247
Mvaa 148,36,64
C_ 251,246,247
C_ 250,244,245
Mvaa 148,224,104
s_nop 0
Ta 32,%0,28680
Ta 34,%0,29192
Mvaa 156,36,68
C_ 249,242,243
C_ 248,240,241
Mvaa 156,224,108
X_ a212,v248
X_ a213,v249
Mvaa 164,36,72
X_ a214,v250
X_ a215,v251
Mvaa 164,224,112
U_ v232,s48,v232
U_ v233,s48,v233
Mvaa 172,36,76
U_ v234,s48,v234
U_ v235,s48,v235
Mvaa 172,224,116
W_ 4
U_ v236,s48,v236
U_ v237,s48,v237
Mvaa 168,24,116
s_nop 0
Ta 36,%0,24576
Ta 38,%0,25088
Mvaa 168,20,76
U_ v238,s48,v238
U_ v239,s48,v239
Mvaa 160,24,112
C_ 247,238,239
C_ 246,236,237
Mvaa 160,20,72
s_nop 0
Ta 224,%0,28672
Ta 226,%0,29184
Mvaa 152,24,108
C_ 245,234,235
C_ 244,232,233
Mvaa 152,20,68
X_ a208,v244
X_ a209,v245
Mvaa 144,24,104
X_ a210,v246
X_ a211,v247
Mvaa 144,20,64
U_ v224,s48,v224
U_ v225,s48,v225
Mvaa 136,24,100
s_waitcnt vmcnt(13) lgkmcnt(0)
s_barrier
U_ v226,s48,v226
Mvaa 136,20,60
S_ s65,s59,-768
U_ v227,s48,v227
Mvaa 100,28,60
S_ m0,s52,24320
Ta 20,v1,8
Ta 22,v1,520
Mvaa 100,32,100
L_ v6,28,s65,256
Mvaa 108,28,64
S_ s65,s59,97536
U_ v228,s48,v228
U_ v229,s48,v229
Mvaa 108,32,104
S_ m0,s52,25344
Ta 24,v1,4104
Ta 26,v1,4616
Mvaa 116,28,68
U_ v230,s48,v230
U_ v231,s48,v231
Mvaa 116,32,108
L_ v6,28,s65,256
Mvaa 124,28,72
S_ s65,s59,195840
C_ 243,230,231
C_ 242,228,229
Mvaa 124,32,112
S_ m0,s52,26368
C_ 241,226,227
C_ 240,224,225
Mvaa 132,28,76
X_ a204,v240
X_ a205,v241
Mvaa 132,32,116
X_ a206,v242
X_ a207,v243
Mvaa 128,224,116
s_nop 0
Ta 28,v1,0
Ta 30,v1,512
Mvaa 128,36,76
L_ v6,28,s65,256
Mvaa 120,224,112
S_ s65,s59,294144
U_ v216,s48,v216
U_ v217,s48,v217
Mvaa 120,36,72
S_ m0,s52,27392
Ta 32,v1,4096
Ta 34,v1,4608
Mvaa 112,224,108
.Lpad_q2_0:
s_cbranch_execz .Lpad_q2_4
U_ v218,s48,v218
U_ v219,s48,v219
Mvaa 112,36,68
L_ v6,28,s65,256
Mvaa 104,224,104
S_ s65,s60,128
U_ v220,s48,v220
U_ v221,s48,v221
Mvaa 104,36,64
S_ m0,s50,20480
U_ v222,s48,v222
U_ v223,s48,v223
Mvaa 96,224,100
C_ 239,222,223
C_ 238,220,221
Mvaa 96,36,60
W_ 4
C_ 237,218,219
C_ 236,216,217
Mvaa 140,20,140
s_nop 0
Ta 36,%0,8
Ta 38,%0,520
Mvaa 140,24,180
L_ v4,24,s65,0
Mvaa 148,20,144
S_ s65,s60,10368
X_ a200,v236
X_ a201,v237
Mvaa 148,24,184
S_ m0,s50,21504
Ta 224,%0,4104
Ta 226,%0,4616
Mvaa 156,20,148
X_ a202,v238
X_ a203,v239
Mvaa 156,24,188
L_ v5,24,s65,0
Mvaa 164,20,152
S_ s65,s60,20608
U_ v208,s48,v208
U_ v209,s48,v209
Mvaa 164,24,192
S_ m0,s50,22528
U_ v210,s48,v210
U_ v211,s48,v211
Mvaa 172,20,156
U_ v212,s48,v212
U_ v213,s48,v213
Mvaa 172,24,196
W_ 4
U_ v214,s48,v214
U_ v215,s48,v215
Mvaa 168,32,196
s_nop 0
Ta 20,%0,0
Ta 22,%0,512
Mvaa 168,28,156
L_ v4,24,s65,0
Mvaa 160,32,192
S_ s65,s60,30848
C_ 235,214,215
C_ 234,212,213
Mvaa 160,28,152
S_ m0,s50,23552
Ta 24,%0,4096
Ta 26,%0,4608
Mvaa 152,32,188
C_ 233,210,211
C_ 232,208,209
Mvaa 152,28,148
L_ v5,24,s65,0
Mvaa 144,32,184
S_ s65,s60,41088
X_ a176,v232
X_ a177,v233
Mvaa 144,28,144
S_ m0,s50,24576
X_ a178,v234
X_ a179,v235
Mvaa 136,32,180
s_waitcnt vmcnt(12) lgkmcnt(0)
s_barrier
U_ v200,s48,v200
Mvaa 136,28,140
L_ v4,24,s65,0
Mvaa 100,36,140
S_ s65,s59,785664
Ta 28,v1,8200
Ta 30,v1,8712
Mvaa 100,224,180
S_ m0,s52,0
U_ v201,s48,v201
Mvaa 108,36,144
U_ v202,s48,v202
U_ v203,s48,v203
Mvaa 108,224,184
s_nop 0
Ta 32,v1,12296
Ta 34,v1,12808
Mvaa 116,36,148
L_ v6,28,s65,0
Mvaa 116,224,188
S_ s65,s59,883968
U_ v204,s48,v204
U_ v205,s48,v205
Mvaa 124,36,152
S_ m0,s52,1024
U_ v206,s48,v206
U_ v207,s48,v207
Mvaa 124,224,192
C_ 231,206,207
C_ 230,204,205
Mvaa 132,36,156
L_ v6,28,s65,0
Mvaa 132,224,196
C_ 229,202,203
S_ s65,s59,982272
Mvaa 128,24,196
S_ m0,s52,2048
Ta 36,v1,8192
Ta 38,v1,8704
Mvaa 128,20,156
C_ 228,200,201
Mvaa 120,24,192
L_ v6,28,s65,0
Mvaa 120,20,152
S_ s65,s59,1080576
Ta 224,v1,12288
Ta 226,v1,12800
Mvaa 112,24,188
S_ m0,s52,3072
X_ a172,v228
X_ a173,v229
Mvaa 112,20,148
X_ a174,v230
X_ a175,v231
Mvaa 104,24,184
L_ v6,28,s65,0
Mvaa 104,20,144
U_ v192,s48,v192
U_ v193,s48,v193
Mvaa 96,24,180
U_ v194,s48,v194
U_ v195,s48,v195
Mvaa 96,20,140
W_ 4
U_ v196,s48,v196
U_ v197,s48,v197
Mvaa 60,28,60
s_nop 0
Ta 20,%0,8200
Ta 22,%0,8712
Mvaa 60,32,100
U_ v198,s48,v198
U_ v199,s48,v199
Mvaa 68,28,64
C_ 227,198,199
C_ 226,196,197
Mvaa 68,32,104
s_nop 0
Ta 24,%0,12296
Ta 26,%0,12808
Mvaa 76,28,68
C_ 225,194,195
C_ 224,192,193
Mvaa 76,32,108
X_ a168,v224
X_ a169,v225
Mvaa 84,28,72
X_ a170,v226
X_ a171,v227
Mvaa 84,32,112
U_ v184,s48,v184
U_ v185,s48,v185
Mvaa 92,28,76
U_ v186,s48,v186
U_ v187,s48,v187
Mvaa 92,32,116
W_ 4
U_ v188,s48,v188
U_ v189,s48,v189
Mvaa 88,224,116
s_nop 0
Ta 28,%0,8192
Ta 30,%0,8704
Mvaa 88,36,76
U_ v190,s48,v190
U_ v191,s48,v191
Mvaa 80,224,112
C_ 223,190,191
C_ 222,188,189
Mvaa 80,36,72
s_nop 0
Ta 32,%0,12288
Ta 34,%0,12800
Mvaa 72,224,108
C_ 221,186,187
C_ 220,184,185
Mvaa 72,36,68
X_ a164,v220
X_ a165,v221
Mvaa 64,224,104
X_ a166,v222
X_ a167,v223
Mvaa 64,36,64
U_ v176,s48,v176
U_ v177,s48,v177
Mvaa 56,224,100
s_waitcnt vmcnt(13) lgkmcnt(0)
s_barrier
U_ v178,s48,v178
Mvaa 56,36,60
S_ s65,s59,-768
U_ v179,s48,v179
Mvaa 20,20,60
S_ m0,s52,8192
Ta 36,v1,16392
Ta 38,v1,16904
Mvaa 20,24,100
L_ v6,28,s65,0
Mvaa 28,20,64
S_ s65,s59,97536
U_ v180,s48,v180
U_ v181,s48,v181
Mvaa 28,24,104
S_ m0,s52,9216
Ta 224,v1,20488
Ta 226,v1,21000
Mvaa 36,20,68
U_ v182,s48,v182
U_ v183,s48,v183
Mvaa 36,24,108
L_ v6,28,s65,0
Mvaa 44,20,72
S_ s65,s59,195840
C_ 219,182,183
C_ 218,180,181
Mvaa 44,24,112
S_ m0,s52,10240
C_ 217,178,179
C_ 216,176,177
Mvaa 52,20,76
X_ a160,v216
X_ a161,v217
Mvaa 52,24,116
X_ a162,v218
X_ a163,v219
Mvaa 48,32,116
s_nop 0
Ta 20,v1,16384
Ta 22,v1,16896
Mvaa 48,28,76
L_ v6,28,s65,0
Mvaa 40,32,112
S_ s65,s59,294144
U_ v168,s48,v168
U_ v169,s48,v169
Mvaa 40,28,72
S_ m0,s52,11264
Ta 24,v1,20480
Ta 26,v1,20992
Mvaa 32,32,108
U_ v170,s48,v170
U_ v171,s48,v171
Mvaa 32,28,68
L_ v6,28,s65,0
Mvaa 24,32,104
U_ v172,s48,v172
U_ v173,s48,v173
Mvaa 24,28,64
U_ v174,s48,v174
U_ v175,s48,v175
Mvaa 16,32,100
C_ 215,174,175
C_ 214,172,173
Mvaa 16,28,60
W_ 4
C_ 213,170,171
C_ 212,168,169
Mvaa 60,36,140
s_nop 0
Ta 28,%0,16392
Ta 30,%0,16904
Mvaa 60,224,180
X_ a136,v212
X_ a137,v213
Mvaa 68,36,144
X_ a138,v214
X_ a139,v215
Mvaa 68,224,184
s_nop 0
Ta 32,%0,20488
Ta 34,%0,21000
Mvaa 76,36,148
U_ v160,s48,v160
U_ v161,s48,v161
Mvaa 76,224,188
U_ v162,s48,v162
U_ v163,s48,v163
Mvaa 84,36,152
U_ v164,s48,v164
U_ v165,s48,v165
Mvaa 84,224,192
U_ v166,s48,v166
U_ v167,s48,v167
Mvaa 92,36,156
C_ 211,166,167
C_ 210,164,165
Mvaa 92,224,196
W_ 4
C_ 209,162,163
C_ 208,160,161
Mvaa 88,24,196
s_nop 0
Ta 36,%0,16384
Ta 38,%0,16896
Mvaa 88,20,156
X_ a132,v208
X_ a133,v209
Mvaa 80,24,192
X_ a134,v210
X_ a135,v211
Mvaa 80,20,152
s_nop 0
Ta 224,%0,20480
Ta 226,%0,20992
Mvaa 72,24,188
U_ v152,s48,v152
U_ v153,s48,v153
Mvaa 72,20,148
U_ v154,s48,v154
U_ v155,s48,v155
Mvaa 64,24,184
U_ v156,s48,v156
U_ v157,s48,v157
Mvaa 64,20,144
U_ v158,s48,v158
U_ v159,s48,v159
Mvaa 56,24,180
s_waitcnt vmcnt(63) lgkmcnt(0)
s_barrier
C_ 207,158,159
Mvaa 56,20,140
S_ s65,s59,-768
C_ 206,156,157
Mvaa 20,28,140
S_ m0,s52,16256
C_ 205,154,155
C_ 204,152,153
Mvaa 20,32,180
L_ v6,28,s65,128
Mvaa 28,28,144
S_ s65,s59,97536
X_ a128,v204
X_ a129,v205
Mvaa 28,32,184
S_ m0,s52,17280
X_ a130,v206
X_ a131,v207
Mvaa 36,28,148
U_ v144,s48,v144
U_ v145,s48,v145
Mvaa 36,32,188
L_ v6,28,s65,128
Mvaa 44,28,152
S_ s65,s59,195840
U_ v146,s48,v146
U_ v147,s48,v147
Mvaa 44,32,192
S_ m0,s52,18304
U_ v148,s48,v148
U_ v149,s48,v149
Mvaa 52,28,156
U_ v150,s48,v150
U_ v151,s48,v151
Mvaa 52,32,196
C_ 203,150,151
C_ 202,148,149
Mvaa 48,224,196
L_ v6,28,s65,128
Mvaa 48,36,156
S_ s65,s59,294144
C_ 201,146,147
C_ 200,144,145
Mvaa 40,224,192
S_ m0,s52,19328
X_ a124,v200
X_ a125,v201
Mvaa 40,36,152
X_ a126,v202
X_ a127,v203
Mvaa 32,224,188
L_ v6,28,s65,128
Mvaa 32,36,148
U_ v136,s48,v136
U_ v137,s48,v137
Mvaa 24,224,184
U_ v138,s48,v138
U_ v139,s48,v139
Mvaa 24,36,144
U_ v140,s48,v140
U_ v141,s48,v141
Mvaa 16,224,180
U_ v142,s48,v142
U_ v143,s48,v143
Mvaa 16,36,140
C_ 199,142,143
C_ 198,140,141
C_ 197,138,139
C_ 196,136,137
X_ a120,v196
X_ a121,v197
X_ a122,v198
X_ a123,v199
U_ v128,s48,v128
U_ v129,s48,v129
U_ v130,s48,v130
U_ v131,s48,v131
U_ v132,s48,v132
U_ v133,s48,v133
U_ v134,s48,v134
U_ v135,s48,v135
C_ 195,134,135
C_ 194,132,133
C_ 193,130,131
C_ 192,128,129
X_ a96,v192
X_ a97,v193
X_ a98,v194
X_ a99,v195
U_ v120,s48,v120
U_ v121,s48,v121
U_ v122,s48,v122
U_ v123,s48,v123
U_ v124,s48,v124
U_ v125,s48,v125
U_ v126,s48,v126
U_ v127,s48,v127
C_ 191,126,127
C_ 190,124,125
C_ 189,122,123
C_ 188,120,121
X_ a92,v188
X_ a93,v189
X_ a94,v190
X_ a95,v191
U_ v112,s48,v112
U_ v113,s48,v113
U_ v114,s48,v114
U_ v115,s48,v115
U_ v116,s48,v116
U_ v117,s48,v117
U_ v118,s48,v118
U_ v119,s48,v119
C_ 187,118,119
C_ 186,116,117
C_ 185,114,115
C_ 184,112,113
X_ a88,v184
X_ a89,v185
X_ a90,v186
X_ a91,v187
U_ v104,s48,v104
U_ v105,s48,v105
U_ v106,s48,v106
U_ v107,s48,v107
U_ v108,s48,v108
U_ v109,s48,v109
U_ v110,s48,v110
U_ v111,s48,v111
C_ 183,110,111
C_ 182,108,109
C_ 181,106,107
C_ 180,104,105
X_ a84,v180
X_ a85,v181
X_ a86,v182
X_ a87,v183
U_ v96,s48,v96
U_ v97,s48,v97
U_ v98,s48,v98
U_ v99,s48,v99
U_ v100,s48,v100
U_ v101,s48,v101
U_ v102,s48,v102
U_ v103,s48,v103
C_ 179,102,103
C_ 178,100,101
C_ 177,98,99
C_ 176,96,97
X_ a80,v176
X_ a81,v177
X_ a82,v178
X_ a83,v179
v_pk_mul_f32 v[88:89], v[88:89], s[48:49]
v_pk_mul_f32 v[90:91], v[90:91], s[48:49]
v_pk_mul_f32 v[92:93], v[92:93], s[48:49]
v_pk_mul_f32 v[94:95], v[94:95], s[48:49]
C_ 175,94,95
C_ 174,92,93
C_ 173,90,91
C_ 172,88,89
X_ a56,v172
X_ a57,v173
X_ a58,v174
X_ a59,v175
v_pk_mul_f32 v[80:81], v[80:81], s[48:49]
v_pk_mul_f32 v[82:83], v[82:83], s[48:49]
v_pk_mul_f32 v[84:85], v[84:85], s[48:49]
v_pk_mul_f32 v[86:87], v[86:87], s[48:49]
C_ 171,86,87
C_ 170,84,85
C_ 169,82,83
C_ 168,80,81
X_ a52,v168
X_ a53,v169
X_ a54,v170
X_ a55,v171
v_pk_mul_f32 v[72:73], v[72:73], s[48:49]
v_pk_mul_f32 v[74:75], v[74:75], s[48:49]
v_pk_mul_f32 v[76:77], v[76:77], s[48:49]
v_pk_mul_f32 v[78:79], v[78:79], s[48:49]
C_ 167,78,79
C_ 166,76,77
C_ 165,74,75
C_ 164,72,73
X_ a48,v164
X_ a49,v165
X_ a50,v166
X_ a51,v167
v_pk_mul_f32 v[64:65], v[64:65], s[48:49]
v_pk_mul_f32 v[66:67], v[66:67], s[48:49]
v_pk_mul_f32 v[68:69], v[68:69], s[48:49]
v_pk_mul_f32 v[70:71], v[70:71], s[48:49]
C_ 163,70,71
C_ 162,68,69
C_ 161,66,67
C_ 160,64,65
X_ a44,v160
X_ a45,v161
X_ a46,v162
X_ a47,v163
v_pk_mul_f32 v[56:57], v[56:57], s[48:49]
v_pk_mul_f32 v[58:59], v[58:59], s[48:49]
v_pk_mul_f32 v[60:61], v[60:61], s[48:49]
v_pk_mul_f32 v[62:63], v[62:63], s[48:49]
C_ 159,62,63
C_ 158,60,61
C_ 157,58,59
C_ 156,56,57
X_ a40,v156
X_ a41,v157
X_ a42,v158
X_ a43,v159
v_pk_mul_f32 v[48:49], v[48:49], s[48:49]
v_pk_mul_f32 v[50:51], v[50:51], s[48:49]
v_pk_mul_f32 v[52:53], v[52:53], s[48:49]
v_pk_mul_f32 v[54:55], v[54:55], s[48:49]
C_ 155,54,55
C_ 154,52,53
C_ 153,50,51
C_ 152,48,49
X_ a16,v152
X_ a17,v153
X_ a18,v154
X_ a19,v155
v_pk_mul_f32 v[40:41], v[40:41], s[48:49]
v_pk_mul_f32 v[42:43], v[42:43], s[48:49]
v_pk_mul_f32 v[44:45], v[44:45], s[48:49]
v_pk_mul_f32 v[46:47], v[46:47], s[48:49]
C_ 151,46,47
C_ 150,44,45
C_ 149,42,43
C_ 148,40,41
X_ a12,v148
X_ a13,v149
X_ a14,v150
X_ a15,v151
s_sub_u32 s59, s59, 768
s_waitcnt vmcnt(0) lgkmcnt(0)
s_barrier
Ta 20,%0,8192
Ta 22,%0,8704
Ta 24,%0,12288
Ta 26,%0,12800
Da 60,v2,0
Da 100,v3,0
Da 64,v2,2048
Da 104,v3,2048
.Lpad_q2_1:
s_cbranch_execz .Lpad_q2_5
Da 68,v2,4096
Da 108,v3,4096
Da 72,v2,6144
Da 112,v3,6144
Da 76,v2,8192
Da 116,v3,8192
Ta 28,%0,8200
Ta 30,%0,8712
Ta 32,%0,12296
Ta 34,%0,12808
v_pk_mul_f32 v[32:33], v[32:33], s[48:49]
v_pk_mul_f32 v[34:35], v[34:35], s[48:49]
v_pk_mul_f32 v[36:37], v[36:37], s[48:49]
v_pk_mul_f32 v[38:39], v[38:39], s[48:49]
C_ 147,38,39
C_ 146,36,37
C_ 145,34,35
C_ 144,32,33
X_ a8,v144
X_ a9,v145
X_ a10,v146
X_ a11,v147
v_pk_mul_f32 v[24:25], v[24:25], s[48:49]
v_pk_mul_f32 v[26:27], v[26:27], s[48:49]
v_pk_mul_f32 v[28:29], v[28:29], s[48:49]
v_pk_mul_f32 v[30:31], v[30:31], s[48:49]
C_ 143,30,31
C_ 142,28,29
C_ 141,26,27
C_ 140,24,25
X_ a4,v140
X_ a5,v141
X_ a6,v142
X_ a7,v143
v_pk_mul_f32 v[16:17], v[16:17], s[48:49]
v_pk_mul_f32 v[18:19], v[18:19], s[48:49]
v_pk_mul_f32 v[20:21], v[20:21], s[48:49]
v_pk_mul_f32 v[22:23], v[22:23], s[48:49]
C_ 139,22,23
C_ 138,20,21
C_ 137,18,19
C_ 136,16,17
X_ a0,v136
X_ a1,v137
X_ a2,v138
X_ a3,v139
s_sub_u32 s90, s90, s88
s_cmp_gt_u32 s90, 4000
s_cselect_b32 s11, 1, 0
s_sub_u32 s12, s68, s98
S_ s12,s12,1252
s_mov_b32 s13, 110
s_nop 1
.p2align 3
.Lq:
W_ 13
Zvaa 16,20,60
s_nop 0
Ta 36,v1,8192
Ta 38,v1,8704
W_ 14
Mvaa 16,24,100
R_ v12,a248
W_ 13
Zvaa 24,20,64
v_lshrrev_b32 v12, 4, v12
W_ 12
Mvaa 24,24,104
s_nop 0
Ta 224,v1,12288
Ta 226,v1,12800
W_ 13
Zvaa 32,20,68
v_min_u32 v10, 11, v12
W_ 12
Mvaa 32,24,108
s_mul_i32 s56, s4, 12
W_ 11
Zvaa 40,20,72
v_add_u32 v10, s56, v10
W_ 10
Mvaa 40,24,112
v_mul_u32_u24 v11, 43691, v10
W_ 9
Zvaa 48,20,76
v_lshrrev_b32 v11, 18, v11
W_ 8
Mvaa 48,24,116
W_ 4
v_lshlrev_b32 v10, 7, v10
Zvaa 52,32,116
s_nop 0
Ta 20,v1,8200
Ta 22,v1,8712
Mvaa 52,28,76
v_mul_u32_u24 v11, 11520, v11
Zvaa 44,32,112
v_add_u32 v10, v10, v11
Mvaa 44,28,72
s_nop 0
Ta 24,v1,12296
Ta 26,v1,12808
Zvaa 36,32,108
v_min_u32 v12, 9, v12
Mvaa 36,28,68
s_and_b32 s56, s6, 3
Zvaa 28,32,104
s_lshl_b32 s56, s56, 2
Mvaa 28,28,64
S_ s56,s56,s4
Zvaa 20,32,100
s_waitcnt vmcnt(18) lgkmcnt(0)
s_barrier
s_mul_i32 s56, s56, 10
Mvaa 20,28,60
S_ s65,s59,786432
v_add_u32 v12, s56, v12
Zvaa 56,36,60
S_ m0,s52,8064
Ta 28,%0,16384
Ta 30,%0,16896
Mvaa 56,224,100
L_ v6,28,s65,128
Zvaa 64,36,64
S_ s65,s59,884736
v_mul_u32_u24 v12, 1280, v12
Mvaa 64,224,104
S_ m0,s52,9088
Ta 32,%0,20480
Ta 34,%0,20992
Zvaa 72,36,68
s_lshr_b32 s56, s7, 5
Mvaa 72,224,108
L_ v6,28,s65,128
Zvaa 80,36,72
S_ s65,s59,983040
s_mul_i32 s56, s56, 98304
Mvaa 80,224,112
S_ m0,s52,10112
s_and_b32 s55, s59, 4095
Zvaa 88,36,76
S_ s56,s56,s55
Mvaa 88,224,116
s_mul_i32 s55, s77, 384
Zvaa 92,24,116
s_sub_u32 s56, s56, s55
Ta 36,%0,16392
Ta 38,%0,16904
Mvaa 92,20,76
L_ v6,28,s65,128
Zvaa 84,24,112
S_ s65,s59,1081344
s_mov_b32 s63, 0
Mvaa 84,20,72
S_ m0,s52,11136
Ta 224,%0,20488
Ta 226,%0,21000
Zvaa 76,24,108
s_bitcmp1_b32 1, s58
s_cselect_b32 s63, 0, 4026531840
Mvaa 76,20,68
L_ v6,28,s65,128
Zvaa 68,24,104
S_ s65,s60,256
S_ s56,s56,s63
Mvaa 68,20,64
S_ m0,s50,0
S_ s55,s56,3145728
Zvaa 60,24,100
Mvaa 60,20,60
W_ 4
Zvaa 96,28,60
s_nop 0
Ta 20,v1,16384
Ta 22,v1,16896
Mvaa 96,32,100
L_ v4,24,s65,0
Zvaa 104,28,64
S_ s65,s60,10496
Mvaa 104,32,104
S_ m0,s50,1024
Ta 24,v1,20480
Ta 26,v1,20992
Zvaa 112,28,68
Mvaa 112,32,108
L_ v5,24,s65,0
Zvaa 120,28,72
S_ s65,s60,20736
Mvaa 120,32,112
S_ m0,s50,2048
Zvaa 128,28,76
Mvaa 128,32,116
W_ 4
Zvaa 132,224,116
s_nop 0
Ta 28,v1,16392
Ta 30,v1,16904
Mvaa 132,36,76
L_ v4,24,s65,0
Zvaa 124,224,112
S_ s65,s60,30976
Mvaa 124,36,72
S_ m0,s50,3072
Ta 32,v1,20488
Ta 34,v1,21000
Zvaa 116,224,108
Mvaa 116,36,68
L_ v5,24,s65,0
Zvaa 108,224,104
S_ s65,s60,41216
Mvaa 108,36,64
S_ m0,s50,4096
Zvaa 100,224,100
s_cmp_eq_u32 s58, 0
s_cbranch_scc0 .Lq_early
s_mov_b64 exec, 0
s_bitcmp1_b32 s4, 1
s_cbranch_scc1 .Lhop_qx_b
s_bitcmp1_b32 s4, 0
s_cbranch_scc1 .Lpad_qx_1
s_branch .Lpad_qx_0
.Lhop_qx_b:
s_bitcmp1_b32 s4, 0
s_cbranch_scc1 .Lpad_qx_3
s_branch .Lpad_qx_2
.Lhop_qx_r:
s_mov_b64 exec, -1
s_nop 1
.Lq_early:
s_waitcnt vmcnt(17) lgkmcnt(0)
s_barrier
s_cmp_eq_u32 s11, 1
s_cbranch_scc1 .Lpace1
.Lpace1_back:
Mvaa 100,36,60
Da 140,v2,20480
Zvaa 136,20,60
s_nop 0
Ta 36,%0,24576
Ta 38,%0,25088
Mvaa 136,24,100
L_ v4,24,s65,0
Zvaa 144,20,64
S_ s65,s59,786432
Da 180,v3,20480
Mvaa 144,24,104
S_ m0,s52,16128
Ta 224,%0,28672
Ta 226,%0,29184
Zvaa 152,20,68
Mvaa 152,24,108
Da 144,v2,22528
Zvaa 160,20,72
L_ v6,28,s65,256
Mvaa 160,24,112
S_ s65,s59,884736
Zvaa 168,20,76
S_ m0,s52,17152
Da 184,v3,22528
Mvaa 168,24,116
Zvaa 172,32,116
s_nop 0
Ta 20,%0,24584
Ta 22,%0,25096
Mvaa 172,28,76
Da 148,v2,24576
Zvaa 164,32,112
L_ v6,28,s65,256
Mvaa 164,28,72
S_ s65,s59,983040
Ta 24,%0,28680
Ta 26,%0,29192
Zvaa 156,32,108
S_ m0,s52,18176
Da 188,v3,24576
Mvaa 156,28,68
Zvaa 148,32,104
L_ v6,28,s65,256
Mvaa 148,28,64
S_ s65,s59,1081344
Da 152,v2,26624
Zvaa 140,32,100
S_ m0,s52,19200
Mvaa 140,28,60
W_ 9
Zvaa 176,36,60
s_nop 0
Ta 28,v1,24576
Ta 30,v1,25088
Mvaa 176,224,100
Da 192,v3,26624
Zvaa 184,36,64
L_ v6,28,s65,256
Mvaa 184,224,104
s_nop 0
Ta 32,v1,28672
Ta 34,v1,29184
Zvaa 192,36,68
Da 156,v2,28672
Mvaa 192,224,108
Zvaa 200,36,72
Da 196,v3,28672
Mvaa 200,224,112
buffer_load_dword a254, v10, s[28:31], s55 offen
Zvaa 208,36,76
Mvaa 208,224,116
W_ 9
Zvaa 212,24,116
s_nop 0
Ta 36,v1,24584
Ta 38,v1,25096
Mvaa 212,20,76
buffer_load_dword a254, v12, s[24:27], s63 offen offset:512
Zvaa 204,24,112
Mvaa 204,20,72
s_nop 0
Ta 224,v1,28680
Ta 226,v1,29192
Zvaa 196,24,108
Mvaa 196,20,68
Zvaa 188,24,104
Mvaa 188,20,64
Zvaa 180,24,100
s_waitcnt vmcnt(20) lgkmcnt(0)
s_barrier
s_cmp_eq_u32 s11, 2
s_cbranch_scc1 .Lqsl2
.Lqsl2_back:
Mvaa 180,20,60
S_ s65,s59,1572864
Zvaa 216,28,60
S_ m0,s52,24576
Ta 20,%0,0
Ta 22,%0,512
Mvaa 216,32,100
L_ v6,28,s65,0
Zvaa 224,28,64
S_ s65,s59,1671168
Mvaa 224,32,104
S_ m0,s52,25600
Ta 24,%0,4096
Ta 26,%0,4608
Zvaa 232,28,68
Mvaa 232,32,108
L_ v6,28,s65,0
Zvaa 240,28,72
S_ s65,s59,1769472
Mvaa 240,32,112
S_ m0,s52,26624
Zvaa 248,28,76
Mvaa 248,32,116
Zvaa 252,224,116
s_nop 0
Ta 28,%0,8
Ta 30,%0,520
Mvaa 252,36,76
L_ v6,28,s65,0
Zvaa 244,224,112
S_ s65,s59,1867776
Mvaa 244,36,72
S_ m0,s52,27648
Ta 32,%0,4104
Ta 34,%0,4616
Zvaa 236,224,108
Mvaa 236,36,68
L_ v6,28,s65,0
Zvaa 228,224,104
S_ s65,s60,384
Mvaa 228,36,64
S_ m0,s50,20480
Zvaa 220,224,100
Mvaa 220,36,60
W_ 4
Mvaa 16,20,140
s_nop 0
Ta 36,v1,0
Ta 38,v1,512
Mvaa 16,24,180
L_ v4,24,s65,0
Mvaa 24,20,144
S_ s65,s60,10624
Mvaa 24,24,184
S_ m0,s50,21504
Ta 224,v1,4096
Ta 226,v1,4608
Mvaa 32,20,148
Mvaa 32,24,188
L_ v5,24,s65,0
Mvaa 40,20,152
S_ s65,s60,20864
Mvaa 40,24,192
S_ m0,s50,22528
Mvaa 48,20,156
Mvaa 48,24,196
W_ 4
Mvaa 52,32,196
s_nop 0
Ta 20,v1,8
Ta 22,v1,520
Mvaa 52,28,156
L_ v4,24,s65,0
Mvaa 44,32,192
S_ s65,s60,31104
Mvaa 44,28,152
S_ m0,s50,23552
Ta 24,v1,4104
Ta 26,v1,4616
Mvaa 36,32,188
Mvaa 36,28,148
L_ v5,24,s65,0
Mvaa 28,32,184
S_ s65,s60,41344
Mvaa 28,28,144
S_ m0,s50,24576
Mvaa 20,32,180
s_waitcnt vmcnt(19) lgkmcnt(0)
s_barrier
s_cmp_eq_u32 s11, 1
s_cbranch_scc1 .Lpace3
.Lpace3_back:
Mvaa 20,28,140
L_ v4,24,s65,0
Mvaa 56,36,140
S_ s65,s59,1572864
Ta 28,%0,8192
Ta 30,%0,8704
Mvaa 56,224,180
S_ m0,s52,-128
Mvaa 64,36,144
Mvaa 64,224,184
s_nop 0
Ta 32,%0,12288
Ta 34,%0,12800
Mvaa 72,36,148
L_ v6,28,s65,128
Mvaa 72,224,188
S_ s65,s59,1671168
Mvaa 80,36,152
S_ m0,s52,896
Mvaa 80,224,192
Mvaa 88,36,156
L_ v6,28,s65,128
Mvaa 88,224,196
S_ s65,s59,1769472
Mvaa 92,24,196
S_ m0,s52,1920
Ta 36,%0,8200
Ta 38,%0,8712
Mvaa 92,20,156
S_ s55,s56,3932160
Mvaa 84,24,192
L_ v6,28,s65,128
Mvaa 84,20,152
S_ s65,s59,1867776
Ta 224,%0,12296
Ta 226,%0,12808
Mvaa 76,24,188
S_ m0,s52,2944
Mvaa 76,20,148
Mvaa 68,24,184
L_ v6,28,s65,128
Mvaa 68,20,144
Mvaa 60,24,180
Mvaa 60,20,140
W_ 4
Mvaa 96,28,140
s_nop 0
Ta 20,v1,8192
Ta 22,v1,8704
Mvaa 96,32,180
buffer_load_dword a254, v10, s[28:31], s55 offen
Mvaa 104,28,144
Mvaa 104,32,184
s_nop 0
Ta 24,v1,12288
Ta 26,v1,12800
Mvaa 112,28,148
Mvaa 112,32,188
buffer_load_dword a254, v12, s[24:27], s63 offen offset:640
Mvaa 120,28,152
Mvaa 120,32,192
)A"
R"A(Mvaa 128,28,156
Mvaa 128,32,196
W_ 4
Mvaa 132,224,196
s_nop 0
Ta 28,v1,8200
Ta 30,v1,8712
Mvaa 132,36,156
Mvaa 124,224,192
Mvaa 124,36,152
s_nop 0
Ta 32,v1,12296
Ta 34,v1,12808
Mvaa 116,224,188
Mvaa 116,36,148
Mvaa 108,224,184
Mvaa 108,36,144
Mvaa 100,224,180
s_waitcnt vmcnt(17) lgkmcnt(0)
s_barrier
s_cmp_eq_u32 s11, 2
s_cbranch_scc1 .Lqsl4
.Lqsl4_back:
Mvaa 100,36,140
S_ s65,s59,1572864
Da 60,v2,0
Mvaa 136,20,140
S_ m0,s52,7936
Ta 36,%0,16384
Ta 38,%0,16896
Mvaa 136,24,180
L_ v6,28,s65,256
Mvaa 144,20,144
S_ s65,s59,1671168
Da 100,v3,0
Mvaa 144,24,184
S_ m0,s52,8960
Ta 224,%0,20480
Ta 226,%0,20992
Mvaa 152,20,148
Mvaa 152,24,188
Da 64,v2,2048
Mvaa 160,20,152
L_ v6,28,s65,256
Mvaa 160,24,192
S_ s65,s59,1769472
Mvaa 168,20,156
S_ m0,s52,9984
Da 104,v3,2048
Mvaa 168,24,196
Mvaa 172,32,196
s_nop 0
Ta 20,%0,16392
Ta 22,%0,16904
Mvaa 172,28,156
Da 68,v2,4096
Mvaa 164,32,192
L_ v6,28,s65,256
Mvaa 164,28,152
S_ s65,s59,1867776
Ta 24,%0,20488
Ta 26,%0,21000
Mvaa 156,32,188
S_ m0,s52,11008
Da 108,v3,4096
Mvaa 156,28,148
Mvaa 148,32,184
L_ v6,28,s65,256
Mvaa 148,28,144
Da 72,v2,6144
Mvaa 140,32,180
Mvaa 140,28,140
W_ 9
Mvaa 176,36,140
s_nop 0
Ta 28,v1,16384
Ta 30,v1,16896
Mvaa 176,224,180
Da 112,v3,6144
Mvaa 184,36,144
Mvaa 184,224,184
.Lpad_q2_2:
s_cbranch_execz .Lpad_q2_6
Ta 32,v1,20480
Ta 34,v1,20992
Mvaa 192,36,148
.Lpad_qx_0:
s_cbranch_execz .Lpad_qx_4
Da 76,v2,8192
Mvaa 192,224,188
Mvaa 200,36,152
Da 116,v3,8192
Mvaa 200,224,192
Mvaa 208,36,156
Mvaa 208,224,196
W_ 9
Mvaa 212,24,196
s_nop 0
Ta 36,v1,16392
Ta 38,v1,16904
Mvaa 212,20,156
Mvaa 204,24,192
Mvaa 204,20,152
s_nop 0
Ta 224,v1,20488
Ta 226,v1,21000
Mvaa 196,24,188
Mvaa 196,20,148
Mvaa 188,24,184
Mvaa 188,20,144
Mvaa 180,24,180
s_waitcnt vmcnt(15) lgkmcnt(0)
s_barrier
s_cmp_eq_u32 s11, 1
s_cbranch_scc1 .Lpace5
.Lpace5_back:
Mvaa 180,20,140
S_ s65,s59,2359296
Mvaa 216,28,140
S_ m0,s52,16384
Ta 20,%0,24576
Ta 22,%0,25088
Mvaa 216,32,180
L_ v6,28,s65,0
Mvaa 224,28,144
S_ s65,s59,2457600
Mvaa 224,32,184
S_ m0,s52,17408
Ta 24,%0,28672
Ta 26,%0,29184
Mvaa 232,28,148
Mvaa 232,32,188
L_ v6,28,s65,0
Mvaa 240,28,152
S_ s65,s59,2555904
Mvaa 240,32,192
S_ m0,s52,18432
Mvaa 248,28,156
Mvaa 248,32,196
Mvaa 252,224,196
s_nop 0
Ta 28,%0,24584
Ta 30,%0,25096
Mvaa 252,36,156
L_ v6,28,s65,0
Mvaa 244,224,192
S_ s65,s59,2654208
Mvaa 244,36,152
S_ m0,s52,19456
Ta 32,%0,28680
Ta 34,%0,29192
Mvaa 236,224,188
Mvaa 236,36,148
L_ v6,28,s65,0
Mvaa 228,224,184
S_ s65,s60,512
Mvaa 228,36,144
S_ m0,s50,0
Mvaa 220,224,180
Mvaa 220,36,140
W_ 4
Mvaa 16,20,60
s_nop 0
Ta 36,v1,24576
Ta 38,v1,25088
Mvaa 16,24,100
L_ v4,24,s65,0
Mvaa 24,20,64
S_ s65,s60,10752
Mvaa 24,24,104
S_ m0,s50,1024
Ta 224,v1,28672
Ta 226,v1,29184
Mvaa 32,20,68
Mvaa 32,24,108
L_ v5,24,s65,0
Mvaa 40,20,72
S_ s65,s60,20992
Mvaa 40,24,112
S_ m0,s50,2048
Mvaa 48,20,76
Mvaa 48,24,116
W_ 4
Mvaa 52,32,116
s_nop 0
Ta 20,v1,24584
Ta 22,v1,25096
Mvaa 52,28,76
L_ v4,24,s65,0
Mvaa 44,32,112
S_ s65,s60,31232
Mvaa 44,28,72
S_ m0,s50,3072
Ta 24,v1,28680
Ta 26,v1,29192
Mvaa 36,32,108
Mvaa 36,28,68
L_ v5,24,s65,0
Mvaa 28,32,104
S_ s65,s60,41472
Mvaa 28,28,64
S_ m0,s50,4096
Mvaa 20,32,100
s_waitcnt vmcnt(14) lgkmcnt(0)
s_barrier
s_cmp_eq_u32 s11, 2
s_cbranch_scc1 .Lqsl6
.Lqsl6_back:
Mvaa 20,28,60
L_ v4,24,s65,0
Mvaa 56,36,60
S_ s65,s59,2359296
Ta 28,%0,0
Ta 30,%0,512
Mvaa 56,224,100
S_ m0,s52,24448
Mvaa 64,36,64
Mvaa 64,224,104
s_nop 0
Ta 32,%0,4096
Ta 34,%0,4608
Mvaa 72,36,68
L_ v6,28,s65,128
Mvaa 72,224,108
S_ s65,s59,2457600
Mvaa 80,36,72
S_ m0,s52,25472
Mvaa 80,224,112
Mvaa 88,36,76
L_ v6,28,s65,128
Mvaa 88,224,116
S_ s65,s59,2555904
Mvaa 92,24,116
S_ m0,s52,26496
Ta 36,%0,8
Ta 38,%0,520
Mvaa 92,20,76
S_ s55,s56,4718592
Mvaa 84,24,112
L_ v6,28,s65,128
Mvaa 84,20,72
S_ s65,s59,2654208
Ta 224,%0,4104
Ta 226,%0,4616
Mvaa 76,24,108
S_ m0,s52,27520
Mvaa 76,20,68
Mvaa 68,24,104
L_ v6,28,s65,128
Mvaa 68,20,64
Mvaa 60,24,100
Mvaa 60,20,60
W_ 4
Mvaa 96,28,60
s_nop 0
Ta 20,v1,0
Ta 22,v1,512
Mvaa 96,32,100
buffer_load_dword a254, v10, s[28:31], s55 offen
Mvaa 104,28,64
Mvaa 104,32,104
s_nop 0
Ta 24,v1,4096
Ta 26,v1,4608
Mvaa 112,28,68
Mvaa 112,32,108
buffer_load_dword a254, v12, s[24:27], s63 offen offset:768
Mvaa 120,28,72
Mvaa 120,32,112
Mvaa 128,28,76
Mvaa 128,32,116
W_ 4
Mvaa 132,224,116
s_nop 0
Ta 28,v1,8
Ta 30,v1,520
Mvaa 132,36,76
Mvaa 124,224,112
Mvaa 124,36,72
s_nop 0
Ta 32,v1,4104
Ta 34,v1,4616
Mvaa 116,224,108
Mvaa 116,36,68
Mvaa 108,224,104
Mvaa 108,36,64
Mvaa 100,224,100
s_waitcnt vmcnt(15) lgkmcnt(0)
s_barrier
s_cmp_eq_u32 s11, 1
s_cbranch_scc1 .Lpace7
.Lpace7_back:
Mvaa 100,36,60
S_ s65,s59,2359296
Da 140,v2,20480
Mvaa 136,20,60
S_ m0,s52,-256
Ta 36,%0,8192
Ta 38,%0,8704
Mvaa 136,24,100
L_ v6,28,s65,256
Mvaa 144,20,64
S_ s65,s59,2457600
Da 180,v3,20480
Mvaa 144,24,104
S_ m0,s52,768
Ta 224,%0,12288
Ta 226,%0,12800
Mvaa 152,20,68
Mvaa 152,24,108
Da 144,v2,22528
Mvaa 160,20,72
L_ v6,28,s65,256
Mvaa 160,24,112
S_ s65,s59,2555904
Mvaa 168,20,76
S_ m0,s52,1792
Da 184,v3,22528
Mvaa 168,24,116
Mvaa 172,32,116
s_nop 0
Ta 20,%0,8200
Ta 22,%0,8712
Mvaa 172,28,76
Da 148,v2,24576
Mvaa 164,32,112
L_ v6,28,s65,256
Mvaa 164,28,72
S_ s65,s59,2654208
Ta 24,%0,12296
Ta 26,%0,12808
Mvaa 156,32,108
S_ m0,s52,2816
Da 188,v3,24576
Mvaa 156,28,68
Mvaa 148,32,104
L_ v6,28,s65,256
Mvaa 148,28,64
Da 152,v2,26624
Mvaa 140,32,100
Mvaa 140,28,60
W_ 9
Mvaa 176,36,60
s_nop 0
Ta 28,v1,8192
Ta 30,v1,8704
Mvaa 176,224,100
Da 192,v3,26624
Mvaa 184,36,64
Mvaa 184,224,104
s_nop 0
Ta 32,v1,12288
Ta 34,v1,12800
Mvaa 192,36,68
Da 156,v2,28672
Mvaa 192,224,108
Mvaa 200,36,72
Da 196,v3,28672
Mvaa 200,224,112
Mvaa 208,36,76
Mvaa 208,224,116
W_ 9
Mvaa 212,24,116
s_nop 0
Ta 36,v1,8200
Ta 38,v1,8712
Mvaa 212,20,76
Mvaa 204,24,112
Mvaa 204,20,72
s_nop 0
Ta 224,v1,12296
Ta 226,v1,12808
Mvaa 196,24,108
Mvaa 196,20,68
Mvaa 188,24,104
Mvaa 188,20,64
Mvaa 180,24,100
s_waitcnt vmcnt(15) lgkmcnt(0)
s_barrier
s_cmp_eq_u32 s11, 2
s_cbranch_scc1 .Lqsl8
.Lqsl8_back:
Mvaa 180,20,60
S_ s65,s59,3145728
Mvaa 216,28,60
S_ m0,s52,8192
Ta 20,%0,16384
Ta 22,%0,16896
Mvaa 216,32,100
L_ v6,28,s65,0
Mvaa 224,28,64
S_ s65,s59,3244032
Mvaa 224,32,104
S_ m0,s52,9216
Ta 24,%0,20480
Ta 26,%0,20992
Mvaa 232,28,68
Mvaa 232,32,108
L_ v6,28,s65,0
Mvaa 240,28,72
S_ s65,s59,3342336
Mvaa 240,32,112
S_ m0,s52,10240
Mvaa 248,28,76
Mvaa 248,32,116
Mvaa 252,224,116
s_nop 0
Ta 28,%0,16392
Ta 30,%0,16904
Mvaa 252,36,76
L_ v6,28,s65,0
Mvaa 244,224,112
S_ s65,s59,3440640
Mvaa 244,36,72
S_ m0,s52,11264
Ta 32,%0,20488
Ta 34,%0,21000
Mvaa 236,224,108
Mvaa 236,36,68
L_ v6,28,s65,0
Mvaa 228,224,104
S_ s65,s60,640
Mvaa 228,36,64
S_ m0,s50,20480
Mvaa 220,224,100
Mvaa 220,36,60
W_ 4
Mvaa 16,20,140
s_nop 0
Ta 36,v1,16384
Ta 38,v1,16896
Mvaa 16,24,180
L_ v4,24,s65,0
Mvaa 24,20,144
S_ s65,s60,10880
Mvaa 24,24,184
S_ m0,s50,21504
Ta 224,v1,20480
Ta 226,v1,20992
Mvaa 32,20,148
Mvaa 32,24,188
L_ v5,24,s65,0
Mvaa 40,20,152
S_ s65,s60,21120
Mvaa 40,24,192
S_ m0,s50,22528
Mvaa 48,20,156
Mvaa 48,24,196
W_ 4
Mvaa 52,32,196
s_nop 0
Ta 20,v1,16392
Ta 22,v1,16904
Mvaa 52,28,156
L_ v4,24,s65,0
Mvaa 44,32,192
S_ s65,s60,31360
Mvaa 44,28,152
S_ m0,s50,23552
Ta 24,v1,20488
Ta 26,v1,21000
Mvaa 36,32,188
Mvaa 36,28,148
L_ v5,24,s65,0
Mvaa 28,32,184
S_ s65,s60,41600
Mvaa 28,28,144
S_ m0,s50,24576
Mvaa 20,32,180
s_waitcnt vmcnt(14) lgkmcnt(0)
s_barrier
s_cmp_eq_u32 s11, 1
s_cbranch_scc1 .Lpace9
.Lpace9_back:
Mvaa 20,28,140
L_ v4,24,s65,0
Mvaa 56,36,140
S_ s65,s59,3145728
Ta 28,%0,24576
Ta 30,%0,25088
Mvaa 56,224,180
S_ m0,s52,16256
Mvaa 64,36,144
Mvaa 64,224,184
s_nop 0
Ta 32,%0,28672
Ta 34,%0,29184
Mvaa 72,36,148
L_ v6,28,s65,128
Mvaa 72,224,188
S_ s65,s59,3244032
Mvaa 80,36,152
S_ m0,s52,17280
Mvaa 80,224,192
Mvaa 88,36,156
L_ v6,28,s65,128
Mvaa 88,224,196
S_ s65,s59,3342336
Mvaa 92,24,196
S_ m0,s52,18304
Ta 36,%0,24584
Ta 38,%0,25096
Mvaa 92,20,156
S_ s55,s56,5505024
Mvaa 84,24,192
L_ v6,28,s65,128
Mvaa 84,20,152
S_ s65,s59,3440640
Ta 224,%0,28680
Ta 226,%0,29192
Mvaa 76,24,188
S_ m0,s52,19328
Mvaa 76,20,148
Mvaa 68,24,184
L_ v6,28,s65,128
Mvaa 68,20,144
Mvaa 60,24,180
Mvaa 60,20,140
W_ 4
Mvaa 96,28,140
s_nop 0
Ta 20,v1,24576
Ta 22,v1,25088
Mvaa 96,32,180
buffer_load_dword a254, v10, s[28:31], s55 offen
Mvaa 104,28,144
Mvaa 104,32,184
s_nop 0
Ta 24,v1,28672
Ta 26,v1,29184
Mvaa 112,28,148
Mvaa 112,32,188
buffer_load_dword a254, v12, s[24:27], s63 offen offset:896
Mvaa 120,28,152
Mvaa 120,32,192
Mvaa 128,28,156
Mvaa 128,32,196
W_ 4
Mvaa 132,224,196
s_nop 0
Ta 28,v1,24584
Ta 30,v1,25096
Mvaa 132,36,156
Mvaa 124,224,192
Mvaa 124,36,152
s_nop 0
Ta 32,v1,28680
Ta 34,v1,29192
Mvaa 116,224,188
Mvaa 116,36,148
Mvaa 108,224,184
Mvaa 108,36,144
Mvaa 100,224,180
s_waitcnt vmcnt(15) lgkmcnt(0)
s_barrier
s_cmp_eq_u32 s11, 2
s_cbranch_scc1 .Lqsl10
.Lqsl10_back:
Mvaa 100,36,140
S_ s65,s59,3145728
Da 60,v2,0
Mvaa 136,20,140
S_ m0,s52,24320
Ta 36,%0,0
Ta 38,%0,512
Mvaa 136,24,180
L_ v6,28,s65,256
Mvaa 144,20,144
S_ s65,s59,3244032
Da 100,v3,0
Mvaa 144,24,184
S_ m0,s52,25344
Ta 224,%0,4096
Ta 226,%0,4608
Mvaa 152,20,148
Mvaa 152,24,188
Da 64,v2,2048
Mvaa 160,20,152
L_ v6,28,s65,256
Mvaa 160,24,192
S_ s65,s59,3342336
Mvaa 168,20,156
S_ m0,s52,26368
Da 104,v3,2048
Mvaa 168,24,196
Mvaa 172,32,196
s_nop 0
Ta 20,%0,8
Ta 22,%0,520
Mvaa 172,28,156
Da 68,v2,4096
Mvaa 164,32,192
L_ v6,28,s65,256
Mvaa 164,28,152
S_ s65,s59,3440640
Ta 24,%0,4104
Ta 26,%0,4616
Mvaa 156,32,188
S_ m0,s52,27392
Da 108,v3,4096
Mvaa 156,28,148
Mvaa 148,32,184
L_ v6,28,s65,256
Mvaa 148,28,144
Da 72,v2,6144
Mvaa 140,32,180
Mvaa 140,28,140
W_ 9
Mvaa 176,36,140
s_nop 0
Ta 28,v1,0
Ta 30,v1,512
Mvaa 176,224,180
Da 112,v3,6144
Mvaa 184,36,144
Mvaa 184,224,184
s_nop 0
Ta 32,v1,4096
Ta 34,v1,4608
Mvaa 192,36,148
Da 76,v2,8192
Mvaa 192,224,188
Mvaa 200,36,152
Da 116,v3,8192
Mvaa 200,224,192
Mvaa 208,36,156
Mvaa 208,224,196
W_ 9
Mvaa 212,24,196
s_nop 0
Ta 36,v1,8
Ta 38,v1,520
Mvaa 212,20,156
Mvaa 204,24,192
.Lpad_q2_3:
s_cbranch_execz .Lhop_q2_r
Mvaa 204,20,152
.Lpad_qx_1:
s_cbranch_execz .Lhop_qx_r
Ta 224,v1,4104
Ta 226,v1,4616
Mvaa 196,24,188
Mvaa 196,20,148
Mvaa 188,24,184
Mvaa 188,20,144
Mvaa 180,24,180
s_waitcnt vmcnt(15) lgkmcnt(0)
s_barrier
s_cmp_eq_u32 s11, 1
s_cbranch_scc1 .Lpace11
.Lpace11_back:
Mvaa 180,20,140
S_ s65,s59,3932160
Mvaa 216,28,140
S_ m0,s52,0
Ta 20,%0,8192
Ta 22,%0,8704
Mvaa 216,32,180
L_ v6,28,s65,0
Mvaa 224,28,144
S_ s65,s59,4030464
Mvaa 224,32,184
S_ m0,s52,1024
Ta 24,%0,12288
Ta 26,%0,12800
Mvaa 232,28,148
Mvaa 232,32,188
L_ v6,28,s65,0
Mvaa 240,28,152
S_ s65,s59,4128768
Mvaa 240,32,192
S_ m0,s52,2048
Mvaa 248,28,156
Mvaa 248,32,196
Mvaa 252,224,196
s_nop 0
Ta 28,%0,8200
Ta 30,%0,8712
Mvaa 252,36,156
L_ v6,28,s65,0
Mvaa 244,224,192
S_ s65,s59,4227072
Mvaa 244,36,152
S_ m0,s52,3072
Ta 32,%0,12296
Ta 34,%0,12808
Mvaa 236,224,188
Mvaa 236,36,148
L_ v6,28,s65,0
Mvaa 228,224,184
S_ s65,s60,768
Mvaa 228,36,144
S_ m0,s50,0
Mvaa 220,224,180
Mvaa 220,36,140
W_ 4
Mvaa 16,20,60
s_nop 0
Ta 36,v1,8192
Ta 38,v1,8704
Mvaa 16,24,100
L_ v4,24,s65,0
Mvaa 24,20,64
S_ s65,s60,11008
R_ v13,a248
Mvaa 24,24,104
S_ m0,s50,1024
Ta 224,v1,12288
Ta 226,v1,12800
Mvaa 32,20,68
v_min_u32 v13, 240, v13
Mvaa 32,24,108
L_ v5,24,s65,0
Mvaa 40,20,72
S_ s65,s60,21248
v_lshlrev_b32 v13, 3, v13
Mvaa 40,24,112
S_ m0,s50,2048
Mvaa 48,20,76
Mvaa 48,24,116
W_ 4
Mvaa 52,32,116
s_nop 0
Ta 20,v1,8200
Ta 22,v1,8712
Mvaa 52,28,76
L_ v4,24,s65,0
Mvaa 44,32,112
S_ s65,s60,31488
Mvaa 44,28,72
S_ m0,s50,3072
Ta 24,v1,12296
Ta 26,v1,12808
Mvaa 36,32,108
Mvaa 36,28,68
L_ v5,24,s65,0
Mvaa 28,32,104
S_ s65,s60,41728
Mvaa 28,28,64
S_ m0,s50,4096
Mvaa 20,32,100
s_waitcnt vmcnt(14) lgkmcnt(0)
s_barrier
s_cmp_eq_u32 s11, 2
s_cbranch_scc1 .Lqsl12
.Lqsl12_back:
Mvaa 20,28,60
L_ v4,24,s65,0
Mvaa 56,36,60
S_ s65,s59,3932160
Ta 28,%0,16384
Ta 30,%0,16896
Mvaa 56,224,100
S_ m0,s52,8064
Mvaa 64,36,64
Mvaa 64,224,104
s_nop 0
Ta 32,%0,20480
Ta 34,%0,20992
Mvaa 72,36,68
L_ v6,28,s65,128
Mvaa 72,224,108
S_ s65,s59,4030464
Mvaa 80,36,72
S_ m0,s52,9088
Mvaa 80,224,112
Mvaa 88,36,76
L_ v6,28,s65,128
Mvaa 88,224,116
S_ s65,s59,4128768
Mvaa 92,24,116
S_ m0,s52,10112
Ta 36,%0,16392
Ta 38,%0,16904
Mvaa 92,20,76
S_ s55,s56,6291456
Mvaa 84,24,112
L_ v6,28,s65,128
Mvaa 84,20,72
S_ s65,s59,4227072
Ta 224,%0,20488
Ta 226,%0,21000
Mvaa 76,24,108
S_ m0,s52,11136
Mvaa 76,20,68
Mvaa 68,24,104
L_ v6,28,s65,128
Mvaa 68,20,64
Mvaa 60,24,100
Mvaa 60,20,60
W_ 4
Mvaa 96,28,60
s_nop 0
Ta 20,v1,16384
Ta 22,v1,16896
Mvaa 96,32,100
buffer_load_dword a220, v13, s[44:47], s66 offen
Mvaa 104,28,64
Mvaa 104,32,104
s_nop 0
Ta 24,v1,20480
Ta 26,v1,20992
Mvaa 112,28,68
Mvaa 112,32,108
buffer_load_dword a254, v10, s[28:31], s55 offen
Mvaa 120,28,72
Mvaa 120,32,112
Mvaa 128,28,76
Mvaa 128,32,116
W_ 4
Mvaa 132,224,116
s_nop 0
Ta 28,v1,16392
Ta 30,v1,16904
Mvaa 132,36,76
buffer_load_dword a254, v12, s[24:27], s63 offen offset:1024
Mvaa 124,224,112
Mvaa 124,36,72
s_nop 0
Ta 32,v1,20488
Ta 34,v1,21000
Mvaa 116,224,108
Mvaa 116,36,68
Mvaa 108,224,104
Mvaa 108,36,64
Mvaa 100,224,100
s_waitcnt vmcnt(16) lgkmcnt(0)
s_barrier
s_cmp_eq_u32 s11, 1
s_cbranch_scc1 .Lpace13
.Lpace13_back:
Mvaa 100,36,60
S_ s65,s59,3932160
Da 140,v2,20480
Mvaa 136,20,60
S_ m0,s52,16128
Ta 36,%0,24576
Ta 38,%0,25088
Mvaa 136,24,100
L_ v6,28,s65,256
Mvaa 144,20,64
S_ s65,s59,4030464
Da 180,v3,20480
Mvaa 144,24,104
S_ m0,s52,17152
Ta 224,%0,28672
Ta 226,%0,29184
Mvaa 152,20,68
Mvaa 152,24,108
Da 144,v2,22528
Mvaa 160,20,72
L_ v6,28,s65,256
Mvaa 160,24,112
S_ s65,s59,4128768
Mvaa 168,20,76
S_ m0,s52,18176
Da 184,v3,22528
Mvaa 168,24,116
Mvaa 172,32,116
s_nop 0
Ta 20,%0,24584
Ta 22,%0,25096
Mvaa 172,28,76
Da 148,v2,24576
Mvaa 164,32,112
L_ v6,28,s65,256
Mvaa 164,28,72
S_ s65,s59,4227072
Ta 24,%0,28680
Ta 26,%0,29192
Mvaa 156,32,108
S_ m0,s52,19200
Da 188,v3,24576
Mvaa 156,28,68
Mvaa 148,32,104
L_ v6,28,s65,256
Mvaa 148,28,64
Da 152,v2,26624
Mvaa 140,32,100
Mvaa 140,28,60
W_ 9
Mvaa 176,36,60
s_nop 0
Ta 28,v1,24576
Ta 30,v1,25088
Mvaa 176,224,100
Da 192,v3,26624
Mvaa 184,36,64
Mvaa 184,224,104
s_nop 0
Ta 32,v1,28672
Ta 34,v1,29184
Mvaa 192,36,68
Da 156,v2,28672
Mvaa 192,224,108
Mvaa 200,36,72
Da 196,v3,28672
Mvaa 200,224,112
Mvaa 208,36,76
Mvaa 208,224,116
W_ 9
Mvaa 212,24,116
s_nop 0
Ta 36,v1,24584
Ta 38,v1,25096
Mvaa 212,20,76
Mvaa 204,24,112
Mvaa 204,20,72
s_nop 0
Ta 224,v1,28680
Ta 226,v1,29192
Mvaa 196,24,108
Mvaa 196,20,68
Mvaa 188,24,104
Mvaa 188,20,64
Mvaa 180,24,100
s_waitcnt vmcnt(16) lgkmcnt(0)
s_barrier
s_cmp_eq_u32 s11, 2
s_cbranch_scc1 .Lqsl14
.Lqsl14_back:
Mvaa 180,20,60
S_ s65,s59,4718592
Mvaa 216,28,60
S_ m0,s52,24576
Ta 20,%0,0
Ta 22,%0,512
Mvaa 216,32,100
L_ v6,28,s65,0
Mvaa 224,28,64
S_ s65,s59,4816896
Mvaa 224,32,104
S_ m0,s52,25600
Ta 24,%0,4096
Ta 26,%0,4608
Mvaa 232,28,68
Mvaa 232,32,108
L_ v6,28,s65,0
Mvaa 240,28,72
S_ s65,s59,4915200
Mvaa 240,32,112
S_ m0,s52,26624
Mvaa 248,28,76
Mvaa 248,32,116
Mvaa 252,224,116
s_nop 0
Ta 28,%0,8
Ta 30,%0,520
Mvaa 252,36,76
L_ v6,28,s65,0
Mvaa 244,224,112
S_ s65,s59,5013504
Mvaa 244,36,72
S_ m0,s52,27648
Ta 32,%0,4104
Ta 34,%0,4616
Mvaa 236,224,108
Mvaa 236,36,68
L_ v6,28,s65,0
Mvaa 228,224,104
S_ s65,s60,896
Mvaa 228,36,64
S_ m0,s50,20480
Mvaa 220,224,100
Mvaa 220,36,60
W_ 4
Mvaa 16,20,140
s_nop 0
Ta 36,v1,0
Ta 38,v1,512
Mvaa 16,24,180
L_ v4,24,s65,0
Mvaa 24,20,144
S_ s65,s60,11136
Mvaa 24,24,184
S_ m0,s50,21504
Ta 224,v1,4096
Ta 226,v1,4608
Mvaa 32,20,148
Mvaa 32,24,188
L_ v5,24,s65,0
Mvaa 40,20,152
S_ s65,s60,21376
Mvaa 40,24,192
S_ m0,s50,22528
Mvaa 48,20,156
Mvaa 48,24,196
W_ 4
Mvaa 52,32,196
s_nop 0
Ta 20,v1,8
Ta 22,v1,520
Mvaa 52,28,156
L_ v4,24,s65,0
Mvaa 44,32,192
S_ s65,s60,31616
Mvaa 44,28,152
S_ m0,s50,23552
Ta 24,v1,4104
Ta 26,v1,4616
Mvaa 36,32,188
Mvaa 36,28,148
L_ v5,24,s65,0
Mvaa 28,32,184
S_ s65,s60,41856
Mvaa 28,28,144
S_ m0,s50,24576
Mvaa 20,32,180
s_waitcnt vmcnt(15) lgkmcnt(0)
s_barrier
s_cmp_eq_u32 s11, 1
s_cbranch_scc1 .Lpace15
.Lpace15_back:
Mvaa 20,28,140
L_ v4,24,s65,0
Mvaa 56,36,140
S_ s65,s59,4718592
Ta 28,%0,8192
Ta 30,%0,8704
Mvaa 56,224,180
S_ m0,s52,-128
Mvaa 64,36,144
Mvaa 64,224,184
s_nop 0
Ta 32,%0,12288
Ta 34,%0,12800
Mvaa 72,36,148
L_ v6,28,s65,128
Mvaa 72,224,188
S_ s65,s59,4816896
Mvaa 80,36,152
S_ m0,s52,896
Mvaa 80,224,192
Mvaa 88,36,156
L_ v6,28,s65,128
Mvaa 88,224,196
S_ s65,s59,4915200
Mvaa 92,24,196
S_ m0,s52,1920
Ta 36,%0,8200
Ta 38,%0,8712
Mvaa 92,20,156
S_ s55,s56,7077888
Mvaa 84,24,192
L_ v6,28,s65,128
Mvaa 84,20,152
S_ s65,s59,5013504
Ta 224,%0,12296
Ta 226,%0,12808
Mvaa 76,24,188
S_ m0,s52,2944
Mvaa 76,20,148
Mvaa 68,24,184
L_ v6,28,s65,128
Mvaa 68,20,144
Mvaa 60,24,180
Mvaa 60,20,140
W_ 4
Mvaa 96,28,140
s_nop 0
Ta 20,v1,8192
Ta 22,v1,8704
Mvaa 96,32,180
buffer_load_dword a254, v10, s[28:31], s55 offen
Mvaa 104,28,144
Mvaa 104,32,184
s_nop 0
Ta 24,v1,12288
Ta 26,v1,12800
Mvaa 112,28,148
Mvaa 112,32,188
buffer_load_dword a254, v12, s[24:27], s63 offen offset:1152
Mvaa 120,28,152
Mvaa 120,32,192
Mvaa 128,28,156
Mvaa 128,32,196
W_ 4
Mvaa 132,224,196
s_nop 0
Ta 28,v1,8200
Ta 30,v1,8712
Mvaa 132,36,156
Mvaa 124,224,192
Mvaa 124,36,152
s_nop 0
Ta 32,v1,12296
Ta 34,v1,12808
Mvaa 116,224,188
Mvaa 116,36,148
Mvaa 108,224,184
Mvaa 108,36,144
Mvaa 100,224,180
s_waitcnt vmcnt(15) lgkmcnt(0)
s_barrier
Mvaa 100,36,140
S_ s65,s59,4718592
Da 60,v2,0
Mvaa 136,20,140
S_ m0,s52,7936
Ta 36,%0,16384
Ta 38,%0,16896
Mvaa 136,24,180
L_ v6,28,s65,256
Mvaa 144,20,144
S_ s65,s59,4816896
Da 100,v3,0
Mvaa 144,24,184
S_ m0,s52,8960
Ta 224,%0,20480
Ta 226,%0,20992
Mvaa 152,20,148
Mvaa 152,24,188
Da 64,v2,2048
Mvaa 160,20,152
L_ v6,28,s65,256
Mvaa 160,24,192
S_ s65,s59,4915200
Mvaa 168,20,156
S_ m0,s52,9984
Da 104,v3,2048
Mvaa 168,24,196
Mvaa 172,32,196
s_nop 0
Ta 20,%0,16392
Ta 22,%0,16904
Mvaa 172,28,156
Da 68,v2,4096
Mvaa 164,32,192
L_ v6,28,s65,256
Mvaa 164,28,152
S_ s65,s59,5013504
Ta 24,%0,20488
Ta 26,%0,21000
Mvaa 156,32,188
S_ m0,s52,11008
Da 108,v3,4096
Mvaa 156,28,148
Mvaa 148,32,184
L_ v6,28,s65,256
Mvaa 148,28,144
Da 72,v2,6144
Mvaa 140,32,180
Mvaa 140,28,140
W_ 9
Mvaa 176,36,140
s_nop 0
Ta 28,v1,16384
Ta 30,v1,16896
Mvaa 176,224,180
Da 112,v3,6144
Mvaa 184,36,144
Mvaa 184,224,184
s_nop 0
Ta 32,v1,20480
Ta 34,v1,20992
Mvaa 192,36,148
Da 76,v2,8192
Mvaa 192,224,188
Mvaa 200,36,152
Da 116,v3,8192
Mvaa 200,224,192
Mvaa 208,36,156
Mvaa 208,224,196
W_ 9
Mvaa 212,24,196
s_nop 0
Ta 36,v1,16392
Ta 38,v1,16904
Mvaa 212,20,156
Mvaa 204,24,192
Mvaa 204,20,152
s_nop 0
Ta 224,v1,20488
Ta 226,v1,21000
Mvaa 196,24,188
Mvaa 196,20,148
Mvaa 188,24,184
Mvaa 188,20,144
Mvaa 180,24,180
s_waitcnt vmcnt(15) lgkmcnt(0)
s_barrier
s_cmp_eq_u32 s11, 1
s_cbranch_scc1 .Lpace17
.Lpace17_back:
Mvaa 180,20,140
S_ s65,s59,5505024
Mvaa 216,28,140
S_ m0,s52,16384
Ta 20,%0,24576
Ta 22,%0,25088
Mvaa 216,32,180
L_ v6,28,s65,0
Mvaa 224,28,144
S_ s65,s59,5603328
Mvaa 224,32,184
.Lpad_q2_4:
s_cbranch_execz .Lhop_q2_r
.Lpad_qx_2:
s_cbranch_execz .Lhop_qx_r
S_ m0,s52,17408
Ta 24,%0,28672
Ta 26,%0,29184
Mvaa 232,28,148
Mvaa 232,32,188
L_ v6,28,s65,0
Mvaa 240,28,152
S_ s65,s59,5701632
Mvaa 240,32,192
S_ m0,s52,18432
Mvaa 248,28,156
Mvaa 248,32,196
Mvaa 252,224,196
s_nop 0
Ta 28,%0,24584
Ta 30,%0,25096
Mvaa 252,36,156
L_ v6,28,s65,0
Mvaa 244,224,192
S_ s65,s59,5799936
Mvaa 244,36,152
S_ m0,s52,19456
Ta 32,%0,28680
Ta 34,%0,29192
Mvaa 236,224,188
Mvaa 236,36,148
L_ v6,28,s65,0
Mvaa 228,224,184
S_ s65,s60,1024
Mvaa 228,36,144
S_ m0,s50,0
Mvaa 220,224,180
Mvaa 220,36,140
W_ 4
Mvaa 16,20,60
s_nop 0
Ta 36,v1,24576
Ta 38,v1,25088
Mvaa 16,24,100
L_ v4,24,s65,0
Mvaa 24,20,64
S_ s65,s60,11264
Mvaa 24,24,104
S_ m0,s50,1024
Ta 224,v1,28672
Ta 226,v1,29184
Mvaa 32,20,68
Mvaa 32,24,108
L_ v5,24,s65,0
Mvaa 40,20,72
S_ s65,s60,21504
Mvaa 40,24,112
S_ m0,s50,2048
Mvaa 48,20,76
Mvaa 48,24,116
W_ 4
Mvaa 52,32,116
s_nop 0
Ta 20,v1,24584
Ta 22,v1,25096
Mvaa 52,28,76
L_ v4,24,s65,0
Mvaa 44,32,112
S_ s65,s60,31744
Mvaa 44,28,72
S_ m0,s50,3072
Ta 24,v1,28680
Ta 26,v1,29192
Mvaa 36,32,108
Mvaa 36,28,68
L_ v5,24,s65,0
Mvaa 28,32,104
S_ s65,s60,41984
Mvaa 28,28,64
S_ m0,s50,4096
Mvaa 20,32,100
s_waitcnt vmcnt(14) lgkmcnt(0)
s_barrier
Mvaa 20,28,60
L_ v4,24,s65,0
Mvaa 56,36,60
S_ s65,s59,5505024
Ta 28,%0,0
Ta 30,%0,512
Mvaa 56,224,100
S_ m0,s52,24448
Mvaa 64,36,64
Mvaa 64,224,104
s_nop 0
Ta 32,%0,4096
Ta 34,%0,4608
Mvaa 72,36,68
L_ v6,28,s65,128
Mvaa 72,224,108
S_ s65,s59,5603328
Mvaa 80,36,72
S_ m0,s52,25472
Mvaa 80,224,112
Mvaa 88,36,76
L_ v6,28,s65,128
Mvaa 88,224,116
S_ s65,s59,5701632
Mvaa 92,24,116
S_ m0,s52,26496
Ta 36,%0,8
Ta 38,%0,520
Mvaa 92,20,76
Mvaa 84,24,112
L_ v6,28,s65,128
Mvaa 84,20,72
S_ s65,s59,5799936
Ta 224,%0,4104
Ta 226,%0,4616
Mvaa 76,24,108
S_ m0,s52,27520
Mvaa 76,20,68
Mvaa 68,24,104
L_ v6,28,s65,128
Mvaa 68,20,64
S_ s65,s61,0
Mvaa 60,24,100
S_ m0,s51,36864
Mvaa 60,20,60
W_ 4
Mvaa 96,28,60
s_nop 0
Ta 20,v1,0
Ta 22,v1,512
Mvaa 96,32,100
Ln v7,32,s65,0
Mvaa 104,28,64
Mvaa 104,32,104
s_nop 0
Ta 24,v1,4096
Ta 26,v1,4608
Mvaa 112,28,68
Mvaa 112,32,108
Ln v8,32,s65,3072
Mvaa 120,28,72
S_ m0,s51,37760
Mvaa 120,32,112
Mvaa 128,28,76
Mvaa 128,32,116
W_ 4
Mvaa 132,224,116
s_nop 0
Ta 28,v1,8
Ta 30,v1,520
Mvaa 132,36,76
Ln v7,32,s65,128
Mvaa 124,224,112
Mvaa 124,36,72
s_nop 0
Ta 32,v1,4104
Ta 34,v1,4616
Mvaa 116,224,108
Mvaa 116,36,68
Ln v8,32,s65,3200
Mvaa 108,224,104
S_ m0,s51,38656
Mvaa 108,36,64
Mvaa 100,224,100
s_waitcnt vmcnt(17) lgkmcnt(0)
s_barrier
s_cmp_eq_u32 s11, 1
s_cbranch_scc1 .Lpace19
.Lpace19_back:
Mvaa 100,36,60
Da 140,v2,20480
Mvaa 136,20,60
s_nop 0
Ta 36,%0,8192
Ta 38,%0,8704
Mvaa 136,24,100
Ln v7,32,s65,256
Mvaa 144,20,64
Da 180,v3,20480
Mvaa 144,24,104
s_nop 0
Ta 224,%0,12288
Ta 226,%0,12800
Mvaa 152,20,68
Mvaa 152,24,108
Da 144,v2,22528
Mvaa 160,20,72
Ln v8,32,s65,3328
Mvaa 160,24,112
S_ s65,s59,5505024
Mvaa 168,20,76
S_ m0,s52,-256
Da 184,v3,22528
Mvaa 168,24,116
Mvaa 172,32,116
s_nop 0
Ta 20,%0,8200
Ta 22,%0,8712
Mvaa 172,28,76
Da 148,v2,24576
Mvaa 164,32,112
L_ v6,28,s65,256
Mvaa 164,28,72
S_ s65,s59,5603328
Ta 24,%0,12296
Ta 26,%0,12808
Mvaa 156,32,108
S_ m0,s52,768
Da 188,v3,24576
Mvaa 156,28,68
Mvaa 148,32,104
L_ v6,28,s65,256
Mvaa 148,28,64
S_ s65,s59,5701632
Da 152,v2,26624
Mvaa 140,32,100
S_ m0,s52,1792
Mvaa 140,28,60
W_ 9
Mvaa 176,36,60
s_nop 0
Ta 28,v1,8192
Ta 30,v1,8704
Mvaa 176,224,100
Da 192,v3,26624
Mvaa 184,36,64
L_ v6,28,s65,256
Mvaa 184,224,104
S_ s65,s59,5799936
Ta 32,v1,12288
Ta 34,v1,12800
Mvaa 192,36,68
S_ m0,s52,2816
Da 156,v2,28672
Mvaa 192,224,108
Mvaa 200,36,72
Da 196,v3,28672
Mvaa 200,224,112
L_ v6,28,s65,256
Mvaa 208,36,76
Mvaa 208,224,116
W_ 9
Mvaa 212,24,116
s_nop 0
Ta 36,v1,8200
Ta 38,v1,8712
Mvaa 212,20,76
Mvaa 204,24,112
Mvaa 204,20,72
s_nop 0
Ta 224,v1,12296
Ta 226,v1,12808
Mvaa 196,24,108
Mvaa 196,20,68
Mvaa 188,24,104
Mvaa 188,20,64
Mvaa 180,24,100
s_waitcnt vmcnt(19) lgkmcnt(0)
s_barrier
Mvaa 180,20,60
S_ s65,s59,6291456
Mvaa 216,28,60
S_ m0,s52,7936
Ta 20,%0,16384
Ta 22,%0,16896
Mvaa 216,32,100
L_ v6,28,s65,256
Mvaa 224,28,64
S_ s65,s59,6389760
Mvaa 224,32,104
S_ m0,s52,8960
Ta 24,%0,20480
Ta 26,%0,20992
Mvaa 232,28,68
Mvaa 232,32,108
L_ v6,28,s65,256
Mvaa 240,28,72
S_ s65,s59,6488064
Mvaa 240,32,112
S_ m0,s52,9984
Mvaa 248,28,76
Mvaa 248,32,116
Mvaa 252,224,116
s_nop 0
Ta 28,%0,16392
Ta 30,%0,16904
Mvaa 252,36,76
L_ v6,28,s65,256
Mvaa 244,224,112
S_ s65,s59,6586368
Mvaa 244,36,72
S_ m0,s52,11008
Ta 32,%0,20488
Ta 34,%0,21000
Mvaa 236,224,108
Mvaa 236,36,68
L_ v6,28,s65,256
Mvaa 228,224,104
S_ s65,s60,1152
Mvaa 228,36,64
S_ m0,s50,20480
Mvaa 220,224,100
Mvaa 220,36,60
W_ 4
Mvaa 16,20,140
s_nop 0
Ta 36,v1,16384
Ta 38,v1,16896
Mvaa 16,24,180
L_ v4,24,s65,0
Mvaa 24,20,144
S_ s65,s60,11392
Mvaa 24,24,184
S_ m0,s50,21504
Ta 224,v1,20480
Ta 226,v1,20992
Mvaa 32,20,148
Mvaa 32,24,188
L_ v5,24,s65,0
Mvaa 40,20,152
S_ s65,s60,21632
Mvaa 40,24,192
S_ m0,s50,22528
Mvaa 48,20,156
Mvaa 48,24,196
W_ 4
Mvaa 52,32,196
s_nop 0
Ta 20,v1,16392
Ta 22,v1,16904
Mvaa 52,28,156
L_ v4,24,s65,0
Mvaa 44,32,192
S_ s65,s60,31872
Mvaa 44,28,152
S_ m0,s50,23552
Ta 24,v1,20488
Ta 26,v1,21000
Mvaa 36,32,188
Mvaa 36,28,148
L_ v5,24,s65,0
Mvaa 28,32,184
S_ s65,s60,42112
Mvaa 28,28,144
S_ m0,s50,24576
Mvaa 20,32,180
s_waitcnt vmcnt(18) lgkmcnt(0)
s_barrier
s_cmp_eq_u32 s11, 1
s_cbranch_scc1 .Lpace21
.Lpace21_back:
Mvaa 20,28,140
L_ v4,24,s65,0
Mvaa 56,36,140
S_ s65,s59,7077888
Ta 28,%0,24576
Ta 30,%0,25088
Mvaa 56,224,180
S_ m0,s52,16128
Mvaa 64,36,144
Mvaa 64,224,184
s_nop 0
Ta 32,%0,28672
Ta 34,%0,29184
Mvaa 72,36,148
L_ v6,28,s65,256
Mvaa 72,224,188
S_ s65,s59,7176192
Mvaa 80,36,152
S_ m0,s52,17152
Mvaa 80,224,192
Mvaa 88,36,156
L_ v6,28,s65,256
Mvaa 88,224,196
S_ s65,s59,7274496
Mvaa 92,24,196
S_ m0,s52,18176
Ta 36,%0,24584
Ta 38,%0,25096
Mvaa 92,20,156
Mvaa 84,24,192
L_ v6,28,s65,256
Mvaa 84,20,152
S_ s65,s59,7372800
Ta 224,%0,28680
Ta 226,%0,29192
Mvaa 76,24,188
S_ m0,s52,19200
Mvaa 76,20,148
Mvaa 68,24,184
L_ v6,28,s65,256
Mvaa 68,20,144
Mvaa 60,24,180
Mvaa 60,20,140
W_ 4
Mvaa 96,28,140
s_nop 0
Ta 20,v1,24576
Ta 22,v1,25088
Mvaa 96,32,180
Mvaa 104,28,144
Mvaa 104,32,184
s_nop 0
Ta 24,v1,28672
Ta 26,v1,29184
Mvaa 112,28,148
Mvaa 112,32,188
Mvaa 120,28,152
Mvaa 120,32,192
Mvaa 128,28,156
Mvaa 128,32,196
W_ 4
Mvaa 132,224,196
s_nop 0
Ta 28,v1,24584
Ta 30,v1,25096
Mvaa 132,36,156
Mvaa 124,224,192
Mvaa 124,36,152
s_nop 0
Ta 32,v1,28680
Ta 34,v1,29192
Mvaa 116,224,188
Mvaa 116,36,148
Mvaa 108,224,184
Mvaa 108,36,144
Mvaa 100,224,180
s_waitcnt vmcnt(13) lgkmcnt(0)
s_barrier
Mvaa 100,36,140
S_ s65,s59,6291456
Da 60,v2,0
Mvaa 136,20,140
S_ m0,s52,24448
Ta 36,%0,0
Ta 38,%0,512
Mvaa 136,24,180
L_ v6,28,s65,128
Mvaa 144,20,144
S_ s65,s59,6389760
Da 100,v3,0
Mvaa 144,24,184
S_ m0,s52,25472
Ta 224,%0,4096
Ta 226,%0,4608
Mvaa 152,20,148
Mvaa 152,24,188
Da 64,v2,2048
Mvaa 160,20,152
L_ v6,28,s65,128
Mvaa 160,24,192
S_ s65,s59,6488064
Mvaa 168,20,156
S_ m0,s52,26496
Da 104,v3,2048
Mvaa 168,24,196
Mvaa 172,32,196
s_nop 0
Ta 20,%0,8
Ta 22,%0,520
Mvaa 172,28,156
Da 68,v2,4096
Mvaa 164,32,192
L_ v6,28,s65,128
Mvaa 164,28,152
S_ s65,s59,6586368
Ta 24,%0,4104
Ta 26,%0,4616
Mvaa 156,32,188
S_ m0,s52,27520
Da 108,v3,4096
Mvaa 156,28,148
Mvaa 148,32,184
L_ v6,28,s65,128
Mvaa 148,28,144
S_ s65,s61,12288
Da 72,v2,6144
Mvaa 140,32,180
S_ m0,s51,49152
Mvaa 140,28,140
W_ 9
Mvaa 176,36,140
s_nop 0
Ta 28,v1,0
Ta 30,v1,512
Mvaa 176,224,180
Da 112,v3,6144
Mvaa 184,36,144
Ln v7,32,s65,0
Mvaa 184,224,184
s_nop 0
Ta 32,v1,4096
Ta 34,v1,4608
Mvaa 192,36,148
Da 76,v2,8192
Mvaa 192,224,188
Mvaa 200,36,152
Da 116,v3,8192
Mvaa 200,224,192
Ln v8,32,s65,3072
Mvaa 208,36,156
S_ m0,s51,50048
Mvaa 208,224,196
W_ 9
Mvaa 212,24,196
s_nop 0
Ta 36,v1,8
Ta 38,v1,520
Mvaa 212,20,156
Ln v7,32,s65,128
Mvaa 204,24,192
Mvaa 204,20,152
s_nop 0
Ta 224,v1,4104
Ta 226,v1,4616
Mvaa 196,24,188
Mvaa 196,20,148
Ln v8,32,s65,3200
Mvaa 188,24,184
S_ m0,s51,50944
Mvaa 188,20,144
Mvaa 180,24,180
s_waitcnt vmcnt(12) lgkmcnt(0)
s_barrier
s_cmp_eq_u32 s11, 1
s_cbranch_scc1 .Lpace23
.Lpace23_back:
Mvaa 180,20,140
Ln v7,32,s65,256
Mvaa 216,28,140
s_nop 0
Ta 20,v1,8200
Ta 22,v1,8712
Mvaa 216,32,180
Mvaa 224,28,144
Mvaa 224,32,184
s_nop 0
Ta 24,v1,12296
Ta 26,v1,12808
Mvaa 232,28,148
Ln v8,32,s65,3328
Mvaa 232,32,188
S_ s65,s59,7077888
Mvaa 240,28,152
S_ m0,s52,-128
Mvaa 240,32,192
.Lpad_q2_5:
s_cbranch_execz .Lhop_q2_r
Mvaa 248,28,156
.Lpad_qx_3:
s_cbranch_execz .Lhop_qx_r
L_ v6,28,s65,128
Mvaa 248,32,196
S_ s65,s59,7176192
Mvaa 252,224,196
S_ m0,s52,896
Ta 28,v1,8192
Ta 30,v1,8704
Mvaa 252,36,156
Mvaa 244,224,192
L_ v6,28,s65,128
Mvaa 244,36,152
S_ s65,s59,7274496
Ta 32,v1,12288
Ta 34,v1,12800
Mvaa 236,224,188
S_ m0,s52,1920
Mvaa 236,36,148
Mvaa 228,224,184
L_ v6,28,s65,128
Mvaa 228,36,144
S_ s65,s59,7372800
Mvaa 220,224,180
S_ m0,s52,2944
Mvaa 220,36,140
W_ 4
Mvaa 220,20,60
s_nop 0
Ta 36,%0,8200
Ta 38,%0,8712
Mvaa 220,24,100
Da 140,v2,20480
Mvaa 228,20,64
L_ v6,28,s65,128
Mvaa 228,24,104
s_nop 0
Ta 224,%0,12296
Ta 226,%0,12808
Mvaa 236,20,68
Da 180,v3,20480
Mvaa 236,24,108
Mvaa 244,20,72
Da 144,v2,22528
Mvaa 244,24,112
Mvaa 252,20,76
Mvaa 252,24,116
W_ 7
Da 184,v3,22528
Mvaa 248,32,116
s_nop 0
Ta 20,%0,8192
Ta 22,%0,8704
Mvaa 248,28,76
Mvaa 240,32,112
Da 148,v2,24576
Mvaa 240,28,72
s_nop 0
Ta 24,%0,12288
Ta 26,%0,12800
Mvaa 232,32,108
Mvaa 232,28,68
Da 188,v3,24576
Mvaa 224,32,104
Mvaa 224,28,64
Mvaa 216,32,100
s_waitcnt vmcnt(14) lgkmcnt(0)
s_barrier
Da 152,v2,26624
Mvaa 216,28,60
S_ s65,s59,6291456
Mvaa 180,36,60
S_ m0,s52,8192
Ta 28,v1,16392
Ta 30,v1,16904
Mvaa 180,224,100
Da 192,v3,26624
Mvaa 188,36,64
L_ v6,28,s65,0
Mvaa 188,224,104
S_ s65,s59,6389760
Ta 32,v1,20488
Ta 34,v1,21000
Mvaa 196,36,68
S_ m0,s52,9216
Da 156,v2,28672
Mvaa 196,224,108
Mvaa 204,36,72
L_ v6,28,s65,0
Mvaa 204,224,112
S_ s65,s59,6488064
Da 196,v3,28672
Mvaa 212,36,76
S_ m0,s52,10240
Mvaa 212,224,116
Mvaa 208,24,116
s_nop 0
Ta 36,v1,16384
Ta 38,v1,16896
Mvaa 208,20,76
L_ v6,28,s65,0
Mvaa 200,24,112
S_ s65,s59,6586368
Mvaa 200,20,72
S_ m0,s52,11264
Ta 224,v1,20480
Ta 226,v1,20992
Mvaa 192,24,108
Mvaa 192,20,68
L_ v6,28,s65,0
Mvaa 184,24,104
Mvaa 184,20,64
Mvaa 176,24,100
Mvaa 176,20,60
W_ 6
Mvaa 220,28,140
s_nop 0
Ta 20,%0,16392
Ta 22,%0,16904
Mvaa 220,32,180
Mvaa 228,28,144
Mvaa 228,32,184
s_nop 0
Ta 24,%0,20488
Ta 26,%0,21000
Mvaa 236,28,148
Mvaa 236,32,188
Mvaa 244,28,152
Mvaa 244,32,192
W_ 9
Mvaa 252,28,156
W_ 8
Mvaa 252,32,196
W_ 4
Mvaa 248,224,196
s_nop 0
Ta 28,%0,16384
Ta 30,%0,16896
Mvaa 248,36,156
Mvaa 240,224,192
Mvaa 240,36,152
s_nop 0
Ta 32,%0,20480
Ta 34,%0,20992
Mvaa 232,224,188
Mvaa 232,36,148
Mvaa 224,224,184
Mvaa 224,36,144
Mvaa 216,224,180
s_cmp_eq_u32 s58, 0
s_cbranch_scc0 .Lhq_0
s_mov_b64 exec, 0
s_bitcmp1_b32 s4, 1
s_cbranch_scc1 .Lhop_a0_b
s_bitcmp1_b32 s4, 0
s_cbranch_scc1 .Lpad_a0_1
s_branch .Lpad_a0_0
.Lhop_a0_b:
s_bitcmp1_b32 s4, 0
s_cbranch_scc1 .Lpad_a0_3
s_branch .Lpad_a0_2
.Lhop_a0_r:
s_mov_b64 exec, -1
s_nop 1
.Lhq_0:
s_cmp_eq_u32 s58, 1
s_cbranch_scc0 .Lhq_1
s_mov_b64 exec, 0
s_bitcmp1_b32 s4, 1
s_cbranch_scc1 .Lhop_a1_b
s_bitcmp1_b32 s4, 0
s_cbranch_scc1 .Lpad_a1_1
s_branch .Lpad_a1_0
.Lhop_a1_b:
s_bitcmp1_b32 s4, 0
s_cbranch_scc1 .Lpad_a1_3
s_branch .Lpad_a1_2
.Lhop_a1_r:
s_mov_b64 exec, -1
s_nop 1
.Lhq_1:
s_cmp_eq_u32 s58, 2
s_cbranch_scc0 .Lhq_2
s_mov_b64 exec, 0
s_bitcmp1_b32 s4, 1
s_cbranch_scc1 .Lhop_a2_b
s_bitcmp1_b32 s4, 0
s_cbranch_scc1 .Lpad_a2_1
s_branch .Lpad_a2_0
.Lhop_a2_b:
s_bitcmp1_b32 s4, 0
s_cbranch_scc1 .Lpad_a2_3
s_branch .Lpad_a2_2
.Lhop_a2_r:
s_mov_b64 exec, -1
s_nop 1
.Lhq_2:
s_waitcnt vmcnt(14) lgkmcnt(0)
s_barrier
s_cmp_eq_u32 s11, 1
s_cbranch_scc1 .Lpace25
.Lpace25_back:
Mvaa 216,36,140
S_ s65,s59,7077888
Mvaa 180,20,140
S_ m0,s52,16384
Ta 36,v1,24584
Ta 38,v1,25096
Mvaa 180,24,180
L_ v6,28,s65,0
Mvaa 188,20,144
S_ s65,s59,7176192
Mvaa 188,24,184
S_ m0,s52,17408
Ta 224,v1,28680
Ta 226,v1,29192
Mvaa 196,20,148
U_ v248,s48,v248
U_ v249,s48,v249
Mvaa 196,24,188
L_ v6,28,s65,0
Mvaa 204,20,152
S_ s65,s59,7274496
U_ v250,s48,v250
U_ v251,s48,v251
Mvaa 204,24,192
S_ m0,s52,18432
U_ v252,s48,v252
U_ v253,s48,v253
Mvaa 212,20,156
U_ v254,s48,v254
U_ v255,s48,v255
Mvaa 212,24,196
C_ 255,254,255
C_ 254,252,253
Mvaa 208,32,196
s_nop 0
Ta 20,v1,24576
Ta 22,v1,25088
Mvaa 208,28,156
L_ v6,28,s65,0
Mvaa 200,32,192
S_ s65,s59,7372800
C_ 253,250,251
C_ 252,248,249
Mvaa 200,28,152
S_ m0,s52,19456
Ta 24,v1,28672
Ta 26,v1,29184
Mvaa 192,32,188
U_ v240,s48,v240
U_ v241,s48,v241
Mvaa 192,28,148
L_ v6,28,s65,0
)A"
R"A(Mvaa 184,32,184
U_ v242,s48,v242
U_ v243,s48,v243
Mvaa 184,28,144
U_ v244,s48,v244
U_ v245,s48,v245
Mvaa 176,32,180
U_ v246,s48,v246
U_ v247,s48,v247
Mvaa 176,28,140
W_ 4
C_ 251,246,247
C_ 250,244,245
Mvaa 140,36,60
s_nop 0
Ta 28,%0,24584
Ta 30,%0,25096
Mvaa 140,224,100
C_ 249,242,243
C_ 248,240,241
Mvaa 148,36,64
U_ v232,s48,v232
U_ v233,s48,v233
Mvaa 148,224,104
s_nop 0
Ta 32,%0,28680
Ta 34,%0,29192
Mvaa 156,36,68
U_ v234,s48,v234
U_ v235,s48,v235
Mvaa 156,224,108
U_ v236,s48,v236
U_ v237,s48,v237
Mvaa 164,36,72
U_ v238,s48,v238
U_ v239,s48,v239
Mvaa 164,224,112
C_ 247,238,239
C_ 246,236,237
Mvaa 172,36,76
C_ 245,234,235
C_ 244,232,233
Mvaa 172,224,116
W_ 4
U_ v224,s48,v224
U_ v225,s48,v225
Mvaa 168,24,116
s_nop 0
Ta 36,%0,24576
Ta 38,%0,25088
Mvaa 168,20,76
U_ v226,s48,v226
U_ v227,s48,v227
Mvaa 160,24,112
U_ v228,s48,v228
U_ v229,s48,v229
Mvaa 160,20,72
s_nop 0
Ta 224,%0,28672
Ta 226,%0,29184
Mvaa 152,24,108
U_ v230,s48,v230
U_ v231,s48,v231
Mvaa 152,20,68
C_ 243,230,231
C_ 242,228,229
Mvaa 144,24,104
C_ 241,226,227
C_ 240,224,225
Mvaa 144,20,64
U_ v216,s48,v216
U_ v217,s48,v217
Mvaa 136,24,100
s_waitcnt vmcnt(8) lgkmcnt(0)
s_barrier
U_ v218,s48,v218
Mvaa 136,20,60
S_ s65,s61,0
U_ v219,s48,v219
Mvaa 100,28,60
S_ m0,s51,24576
Ta 20,v1,8
Ta 22,v1,520
Mvaa 100,32,100
Ln v9,36,s65,0
Mvaa 108,28,64
U_ v220,s48,v220
U_ v221,s48,v221
Mvaa 108,32,104
s_nop 0
Ta 24,v1,4104
Ta 26,v1,4616
Mvaa 116,28,68
U_ v222,s48,v222
U_ v223,s48,v223
Mvaa 116,32,108
Ln v9,36,s65,3072
Mvaa 124,28,72
S_ m0,s51,25472
C_ 239,222,223
C_ 238,220,221
Mvaa 124,32,112
C_ 237,218,219
C_ 236,216,217
Mvaa 132,28,76
U_ v208,s48,v208
U_ v209,s48,v209
Mvaa 132,32,116
U_ v210,s48,v210
U_ v211,s48,v211
Mvaa 128,224,116
s_nop 0
Ta 28,v1,0
Ta 30,v1,512
Mvaa 128,36,76
Ln v9,36,s65,128
Mvaa 120,224,112
U_ v212,s48,v212
U_ v213,s48,v213
Mvaa 120,36,72
s_nop 0
Ta 32,v1,4096
Ta 34,v1,4608
Mvaa 112,224,108
U_ v214,s48,v214
U_ v215,s48,v215
Mvaa 112,36,68
Ln v9,36,s65,3200
Mvaa 104,224,104
S_ m0,s51,26368
C_ 235,214,215
C_ 234,212,213
Mvaa 104,36,64
C_ 233,210,211
C_ 232,208,209
Mvaa 96,224,100
U_ v200,s48,v200
U_ v201,s48,v201
Mvaa 96,36,60
W_ 4
U_ v202,s48,v202
U_ v203,s48,v203
Mvaa 140,20,140
s_nop 0
Ta 36,%0,8
Ta 38,%0,520
Mvaa 140,24,180
Ln v9,36,s65,256
Mvaa 148,20,144
U_ v204,s48,v204
U_ v205,s48,v205
Mvaa 148,24,184
s_nop 0
Ta 224,%0,4104
Ta 226,%0,4616
Mvaa 156,20,148
U_ v206,s48,v206
U_ v207,s48,v207
Mvaa 156,24,188
Ln v9,36,s65,3328
Mvaa 164,20,152
C_ 231,206,207
C_ 230,204,205
Mvaa 164,24,192
C_ 229,202,203
C_ 228,200,201
Mvaa 172,20,156
U_ v192,s48,v192
U_ v193,s48,v193
Mvaa 172,24,196
W_ 4
U_ v194,s48,v194
U_ v195,s48,v195
Mvaa 168,32,196
s_nop 0
Ta 20,%0,0
Ta 22,%0,512
Mvaa 168,28,156
U_ v196,s48,v196
U_ v197,s48,v197
Mvaa 160,32,192
U_ v198,s48,v198
U_ v199,s48,v199
Mvaa 160,28,152
s_nop 0
Ta 24,%0,4096
Ta 26,%0,4608
Mvaa 152,32,188
C_ 227,198,199
C_ 226,196,197
Mvaa 152,28,148
C_ 225,194,195
C_ 224,192,193
Mvaa 144,32,184
U_ v184,s48,v184
U_ v185,s48,v185
Mvaa 144,28,144
U_ v186,s48,v186
U_ v187,s48,v187
Mvaa 136,32,180
s_waitcnt vmcnt(10) lgkmcnt(0)
s_barrier
s_cmp_eq_u32 s11, 1
s_cbranch_scc1 .Lpace27
.Lpace27_back:
U_ v188,s48,v188
Mvaa 136,28,140
U_ v189,s48,v189
Mvaa 100,36,140
s_nop 0
Ta 28,v1,8200
Ta 30,v1,8712
Mvaa 100,224,180
U_ v190,s48,v190
U_ v191,s48,v191
Mvaa 108,36,144
C_ 223,190,191
C_ 222,188,189
Mvaa 108,224,184
s_nop 0
Ta 32,v1,12296
Ta 34,v1,12808
Mvaa 116,36,148
C_ 221,186,187
C_ 220,184,185
Mvaa 116,224,188
U_ v176,s48,v176
U_ v177,s48,v177
Mvaa 124,36,152
U_ v178,s48,v178
U_ v179,s48,v179
Mvaa 124,224,192
U_ v180,s48,v180
U_ v181,s48,v181
Mvaa 132,36,156
U_ v182,s48,v182
U_ v183,s48,v183
Mvaa 132,224,196
C_ 219,182,183
C_ 218,180,181
Mvaa 128,24,196
s_nop 0
Ta 36,v1,8192
Ta 38,v1,8704
Mvaa 128,20,156
C_ 217,178,179
C_ 216,176,177
Mvaa 120,24,192
U_ v168,s48,v168
U_ v169,s48,v169
Mvaa 120,20,152
s_nop 0
Ta 224,v1,12288
Ta 226,v1,12800
Mvaa 112,24,188
U_ v170,s48,v170
U_ v171,s48,v171
Mvaa 112,20,148
U_ v172,s48,v172
U_ v173,s48,v173
Mvaa 104,24,184
U_ v174,s48,v174
U_ v175,s48,v175
Mvaa 104,20,144
C_ 215,174,175
C_ 214,172,173
Mvaa 96,24,180
C_ 213,170,171
C_ 212,168,169
Mvaa 96,20,140
W_ 4
U_ v160,s48,v160
U_ v161,s48,v161
Mvaa 60,28,60
s_nop 0
Ta 20,%0,8200
Ta 22,%0,8712
Mvaa 60,32,100
U_ v162,s48,v162
U_ v163,s48,v163
Mvaa 68,28,64
U_ v164,s48,v164
U_ v165,s48,v165
Mvaa 68,32,104
s_nop 0
Ta 24,%0,12296
Ta 26,%0,12808
Mvaa 76,28,68
U_ v166,s48,v166
U_ v167,s48,v167
Mvaa 76,32,108
C_ 211,166,167
C_ 210,164,165
Mvaa 84,28,72
C_ 209,162,163
C_ 208,160,161
Mvaa 84,32,112
U_ v152,s48,v152
U_ v153,s48,v153
Mvaa 92,28,76
U_ v154,s48,v154
U_ v155,s48,v155
Mvaa 92,32,116
W_ 4
U_ v156,s48,v156
U_ v157,s48,v157
Mvaa 88,224,116
s_nop 0
Ta 28,%0,8192
Ta 30,%0,8704
Mvaa 88,36,76
U_ v158,s48,v158
U_ v159,s48,v159
Mvaa 80,224,112
C_ 207,158,159
C_ 206,156,157
Mvaa 80,36,72
s_nop 0
Ta 32,%0,12288
Ta 34,%0,12800
Mvaa 72,224,108
C_ 205,154,155
C_ 204,152,153
Mvaa 72,36,68
U_ v144,s48,v144
U_ v145,s48,v145
Mvaa 64,224,104
U_ v146,s48,v146
U_ v147,s48,v147
Mvaa 64,36,64
U_ v148,s48,v148
U_ v149,s48,v149
Mvaa 56,224,100
s_waitcnt vmcnt(6) lgkmcnt(0)
s_barrier
U_ v150,s48,v150
Mvaa 56,36,60
S_ s65,s61,24576
U_ v151,s48,v151
Mvaa 20,20,60
S_ m0,s51,0
Ta 36,v1,16392
Ta 38,v1,16904
Mvaa 20,24,100
Ln v7,32,s65,0
Mvaa 28,20,64
C_ 203,150,151
C_ 202,148,149
Mvaa 28,24,104
s_nop 0
Ta 224,v1,20488
Ta 226,v1,21000
Mvaa 36,20,68
C_ 201,146,147
C_ 200,144,145
Mvaa 36,24,108
Ln v8,32,s65,3072
Mvaa 44,20,72
S_ m0,s51,896
U_ v136,s48,v136
U_ v137,s48,v137
Mvaa 44,24,112
U_ v138,s48,v138
U_ v139,s48,v139
Mvaa 52,20,76
U_ v140,s48,v140
U_ v141,s48,v141
Mvaa 52,24,116
U_ v142,s48,v142
U_ v143,s48,v143
Mvaa 48,32,116
s_nop 0
Ta 20,v1,16384
Ta 22,v1,16896
Mvaa 48,28,76
Ln v7,32,s65,128
Mvaa 40,32,112
.Lpad_q2_6:
s_cbranch_execz .Lhop_q2_r
C_ 199,142,143
C_ 198,140,141
.Lpad_qx_4:
s_cbranch_execz .Lhop_qx_r
Mvaa 40,28,72
s_nop 0
Ta 24,v1,20480
Ta 26,v1,20992
Mvaa 32,32,108
C_ 197,138,139
C_ 196,136,137
Mvaa 32,28,68
Ln v8,32,s65,3200
Mvaa 24,32,104
S_ m0,s51,1792
U_ v128,s48,v128
U_ v129,s48,v129
Mvaa 24,28,64
U_ v130,s48,v130
U_ v131,s48,v131
Mvaa 16,32,100
U_ v132,s48,v132
U_ v133,s48,v133
Mvaa 16,28,60
W_ 4
U_ v134,s48,v134
U_ v135,s48,v135
Mvaa 60,36,140
s_nop 0
Ta 28,%0,16392
Ta 30,%0,16904
Mvaa 60,224,180
Ln v7,32,s65,256
Mvaa 68,36,144
C_ 195,134,135
C_ 194,132,133
Mvaa 68,224,184
s_nop 0
Ta 32,%0,20488
Ta 34,%0,21000
Mvaa 76,36,148
C_ 193,130,131
C_ 192,128,129
Mvaa 76,224,188
Ln v8,32,s65,3328
Mvaa 84,36,152
U_ v120,s48,v120
U_ v121,s48,v121
Mvaa 84,224,192
U_ v122,s48,v122
U_ v123,s48,v123
Mvaa 92,36,156
U_ v124,s48,v124
U_ v125,s48,v125
Mvaa 92,224,196
W_ 4
U_ v126,s48,v126
U_ v127,s48,v127
Mvaa 88,24,196
s_nop 0
Ta 36,%0,16384
Ta 38,%0,16896
Mvaa 88,20,156
C_ 191,126,127
C_ 190,124,125
Mvaa 80,24,192
C_ 189,122,123
C_ 188,120,121
Mvaa 80,20,152
s_nop 0
Ta 224,%0,20480
Ta 226,%0,20992
Mvaa 72,24,188
U_ v112,s48,v112
U_ v113,s48,v113
Mvaa 72,20,148
U_ v114,s48,v114
U_ v115,s48,v115
Mvaa 64,24,184
U_ v116,s48,v116
U_ v117,s48,v117
Mvaa 64,20,144
U_ v118,s48,v118
U_ v119,s48,v119
Mvaa 56,24,180
s_waitcnt vmcnt(51) lgkmcnt(0)
s_barrier
s_cmp_eq_u32 s11, 1
s_cbranch_scc1 .Lpace29
.Lpace29_back:
C_ 187,118,119
Mvaa 56,20,140
S_ s65,s61,12288
C_ 186,116,117
Mvaa 20,28,140
S_ m0,s51,12288
C_ 185,114,115
C_ 184,112,113
Mvaa 20,32,180
Ln v9,36,s65,0
Mvaa 28,28,144
U_ v104,s48,v104
U_ v105,s48,v105
Mvaa 28,32,184
U_ v106,s48,v106
U_ v107,s48,v107
Mvaa 36,28,148
U_ v108,s48,v108
U_ v109,s48,v109
Mvaa 36,32,188
Ln v9,36,s65,3072
Mvaa 44,28,152
S_ m0,s51,13184
U_ v110,s48,v110
U_ v111,s48,v111
Mvaa 44,32,192
C_ 183,110,111
C_ 182,108,109
Mvaa 52,28,156
C_ 181,106,107
C_ 180,104,105
Mvaa 52,32,196
U_ v96,s48,v96
U_ v97,s48,v97
Mvaa 48,224,196
Ln v9,36,s65,128
Mvaa 48,36,156
U_ v98,s48,v98
U_ v99,s48,v99
Mvaa 40,224,192
U_ v100,s48,v100
U_ v101,s48,v101
Mvaa 40,36,152
U_ v102,s48,v102
U_ v103,s48,v103
Mvaa 32,224,188
Ln v9,36,s65,3200
Mvaa 32,36,148
S_ m0,s51,14080
C_ 179,102,103
C_ 178,100,101
Mvaa 24,224,184
C_ 177,98,99
C_ 176,96,97
Mvaa 24,36,144
v_pk_mul_f32 v[88:89], v[88:89], s[48:49]
v_pk_mul_f32 v[90:91], v[90:91], s[48:49]
Mvaa 16,224,180
Ln v9,36,s65,256
Mvaa 16,36,140
v_pk_mul_f32 v[92:93], v[92:93], s[48:49]
v_pk_mul_f32 v[94:95], v[94:95], s[48:49]
Ln v9,36,s65,3328
C_ 175,94,95
C_ 174,92,93
C_ 173,90,91
C_ 172,88,89
v_pk_mul_f32 v[80:81], v[80:81], s[48:49]
v_pk_mul_f32 v[82:83], v[82:83], s[48:49]
v_pk_mul_f32 v[84:85], v[84:85], s[48:49]
v_pk_mul_f32 v[86:87], v[86:87], s[48:49]
C_ 171,86,87
C_ 170,84,85
C_ 169,82,83
C_ 168,80,81
v_pk_mul_f32 v[72:73], v[72:73], s[48:49]
v_pk_mul_f32 v[74:75], v[74:75], s[48:49]
v_pk_mul_f32 v[76:77], v[76:77], s[48:49]
v_pk_mul_f32 v[78:79], v[78:79], s[48:49]
C_ 167,78,79
C_ 166,76,77
C_ 165,74,75
C_ 164,72,73
v_pk_mul_f32 v[40:41], v[40:41], s[48:49]
v_pk_mul_f32 v[42:43], v[42:43], s[48:49]
v_pk_mul_f32 v[44:45], v[44:45], s[48:49]
v_pk_mul_f32 v[46:47], v[46:47], s[48:49]
C_ 151,46,47
C_ 150,44,45
C_ 149,42,43
C_ 148,40,41
v_pk_mul_f32 v[32:33], v[32:33], s[48:49]
v_pk_mul_f32 v[34:35], v[34:35], s[48:49]
v_pk_mul_f32 v[36:37], v[36:37], s[48:49]
v_pk_mul_f32 v[38:39], v[38:39], s[48:49]
C_ 147,38,39
C_ 146,36,37
C_ 145,34,35
C_ 144,32,33
v_pk_mul_f32 v[24:25], v[24:25], s[48:49]
v_pk_mul_f32 v[26:27], v[26:27], s[48:49]
v_pk_mul_f32 v[28:29], v[28:29], s[48:49]
v_pk_mul_f32 v[30:31], v[30:31], s[48:49]
C_ 143,30,31
C_ 142,28,29
C_ 141,26,27
C_ 140,24,25
R_ %0,a244
v_xor_b32 v1, 64, %0
Dv 30,%0,43008
Dv 34,v1,43008
Dv 38,%0,44032
v_pk_mul_f32 v[64:65], v[64:65], s[48:49]
v_pk_mul_f32 v[66:67], v[66:67], s[48:49]
v_pk_mul_f32 v[68:69], v[68:69], s[48:49]
v_pk_mul_f32 v[70:71], v[70:71], s[48:49]
C_ 163,70,71
C_ 162,68,69
C_ 161,66,67
C_ 160,64,65
v_pk_mul_f32 v[56:57], v[56:57], s[48:49]
v_pk_mul_f32 v[58:59], v[58:59], s[48:49]
v_pk_mul_f32 v[60:61], v[60:61], s[48:49]
v_pk_mul_f32 v[62:63], v[62:63], s[48:49]
C_ 159,62,63
C_ 158,60,61
C_ 157,58,59
C_ 156,56,57
v_pk_mul_f32 v[48:49], v[48:49], s[48:49]
v_pk_mul_f32 v[50:51], v[50:51], s[48:49]
v_pk_mul_f32 v[52:53], v[52:53], s[48:49]
v_pk_mul_f32 v[54:55], v[54:55], s[48:49]
C_ 155,54,55
C_ 154,52,53
C_ 153,50,51
C_ 152,48,49
v_pk_mul_f32 v[16:17], v[16:17], s[48:49]
v_pk_mul_f32 v[18:19], v[18:19], s[48:49]
v_pk_mul_f32 v[20:21], v[20:21], s[48:49]
v_pk_mul_f32 v[22:23], v[22:23], s[48:49]
C_ 139,22,23
C_ 138,20,21
C_ 137,18,19
C_ 136,16,17
s_cmp_eq_u32 s58, 1
s_cbranch_scc1 .Latt1
s_cmp_eq_u32 s58, 2
s_cbranch_scc1 .Latt2
.Latt0:
s_cmp_eq_u32 s4, 0
s_cbranch_scc0 .Lhs_tok0
R_ v11,a241
buffer_load_dword a255, v11, s[92:95], 0 offen offset:0 sc1
.Lhs_tok0:
S_ s59,s59,768
W_ 2
Zvvv 96,30,136
Dv 42,v1,44032
R_ v2,a245
Zvvv 100,30,140
v_xor_b32 v3, 32, v2
v_xor_b32 v4, 64, v2
Zvvv 104,30,144
v_xor_b32 v5, 96, v2
Zvvv 108,30,148
s_cmp_eq_u32 s4, 0
s_cselect_b32 s67, 13, 0
Zvvv 112,30,152
W_ 2
s_or_b32 s67, s67, 8192
Mvvv 96,34,156
Dv 30,%0,45056
Mvvv 100,34,160
Mvvv 104,34,164
Mvvv 108,34,168
Mvvv 112,34,172
W_ 2
Mvvv 96,38,176
Dv 34,v1,45056
Mvvv 100,38,180
Mvvv 104,38,184
Mvvv 108,38,188
Mvvv 112,38,192
W_ 2
Mvvv 96,42,196
Dv 38,%0,36864
Mvvv 100,42,200
Mvvv 104,42,204
Mvvv 108,42,208
Mvvv 112,42,212
W_ 2
Mvvv 96,30,216
Dv 42,v1,36864
Mvvv 100,30,220
Mvvv 104,30,224
Mvvv 108,30,228
Mvvv 112,30,232
W_ 2
Mvvv 96,34,236
Dv 30,%0,37888
Mvvv 100,34,240
Mvvv 104,34,244
Mvvv 108,34,248
Mvvv 112,34,252
W_ 2
s_nop 1
v_max3_f32 v56, v96, v97, v98
v_max_f32 v56, v56, v99
Zvvv 76,38,136
s_memtime s[12:13]
Dv 34,v1,37888
v_max3_f32 v60, v100, v101, v102
Zvvv 80,38,140
s_memrealtime s[100:101]
v_max_f32 v60, v60, v103
Zvvv 84,38,144
v_max3_f32 v64, v104, v105, v106
v_max_f32 v64, v64, v107
Zvvv 88,38,148
v_max3_f32 v68, v108, v109, v110
v_max_f32 v68, v68, v111
Zvvv 92,38,152
W_ 2
v_max3_f32 v72, v112, v113, v114
v_max_f32 v72, v72, v115
Mvvv 76,42,156
Dv 38,%0,38912
Mvvv 80,42,160
Mvvv 84,42,164
Mvvv 88,42,168
Mvvv 92,42,172
W_ 2
Mvvv 76,30,176
Dv 42,v1,38912
Mvvv 80,30,180
Mvvv 84,30,184
Mvvv 88,30,188
Mvvv 92,30,192
s_waitcnt vmcnt(30) lgkmcnt(0)
s_barrier
Mvvv 76,34,196
S_ s65,s61,184320
Dv 30,%0,55296
Mvvv 76,38,216
S_ m0,s51,36864
s_sub_u32 s12, s12, s90
Mvvv 76,42,236
L_ v9,36,s65,0
Mvvv 80,34,200
s_sub_u32 s100, s100, s68
Mvvv 80,38,220
L_ v9,36,s65,3072
Mvvv 80,42,240
S_ m0,s51,37760
s_sub_u32 s101, s68, s98
Mvvv 84,34,204
L_ v9,36,s65,128
Mvvv 84,38,224
s_mul_i32 s12, s12, s101
v_max3_f32 v56, v56, v76, v77
v_max3_f32 v56, v56, v78, v79
Mvvv 84,42,244
L_ v9,36,s65,3200
Mvvv 88,34,208
S_ m0,s51,38656
v_max3_f32 v60, v60, v80, v81
s_sub_u32 s101, s90, s88
Mvvv 88,38,228
L_ v9,36,s65,256
Mvvv 88,42,248
s_mul_i32 s100, s100, s101
v_max3_f32 v60, v60, v82, v83
Mvvv 92,34,212
L_ v9,36,s65,3328
Mvvv 92,38,232
s_lshr_b32 s100, s100, 6
v_max3_f32 v64, v64, v84, v85
v_max3_f32 v64, v64, v86, v87
Mvvv 92,42,252
s_mul_i32 s100, s100, 47
s_cmp_gt_u32 s101, 4000
s_cselect_b32 s13, 2, 0
s_cmp_gt_u32 s12, s100
s_cselect_b32 s13, s13, 0
Dv 34,v1,55296
.p2align 3
v_mov_b32 v11, 0x3f803f80
v_permlane32_swap_b32 v56, v60
v_max3_f32 v68, v68, v88, v89
v_max3_f32 v68, v68, v90, v91
v_max_f32 v56, v56, v60
v_max3_f32 v72, v72, v92, v93
v_max3_f32 v72, v72, v94, v95
v_mov_b32 v73, v72
v_permlane32_swap_b32 v64, v68
v_max_f32 v64, v64, v68
v_permlane32_swap_b32 v72, v73
v_max_f32 v72, v72, v73
v_mov_b32 v73, v72
v_permlane16_swap_b32 v56, v64
v_max_f32 v56, v56, v64
v_permlane16_swap_b32 v72, v73
B_ v56,0xc0e00000,v56
v_max_f32 v72, v72, v73
v_mov_b32 v64, v56
B_ v72,0xc0e00000,v72
v_mov_b32 v73, v72
v_permlane16_swap_b32 v56, v64
v_mov_b32 v60, v56
v_mov_b32 v68, v64
v_mov_b64 v[74:75], v[72:73]
v_permlane32_swap_b32 v56, v60
v_permlane32_swap_b32 v64, v68
v_mov_b32 v57, v56
v_mov_b32 v61, v60
v_mov_b32 v65, v64
v_mov_b32 v69, v68
v_pk_add_f32 v[96:97], v[96:97], v[56:57]
v_pk_add_f32 v[98:99], v[98:99], v[56:57]
v_pk_add_f32 v[76:77], v[76:77], v[56:57]
v_pk_add_f32 v[78:79], v[78:79], v[56:57]
v_mov_b64 v[58:59], v[56:57]
v_pk_add_f32 v[100:101], v[100:101], v[60:61]
v_pk_add_f32 v[102:103], v[102:103], v[60:61]
v_pk_add_f32 v[80:81], v[80:81], v[60:61]
v_pk_add_f32 v[82:83], v[82:83], v[60:61]
v_mov_b64 v[62:63], v[60:61]
v_pk_add_f32 v[104:105], v[104:105], v[64:65]
v_pk_add_f32 v[106:107], v[106:107], v[64:65]
v_pk_add_f32 v[84:85], v[84:85], v[64:65]
v_pk_add_f32 v[86:87], v[86:87], v[64:65]
v_mov_b64 v[66:67], v[64:65]
v_pk_add_f32 v[108:109], v[108:109], v[68:69]
v_pk_add_f32 v[110:111], v[110:111], v[68:69]
v_pk_add_f32 v[88:89], v[88:89], v[68:69]
v_pk_add_f32 v[90:91], v[90:91], v[68:69]
v_mov_b64 v[70:71], v[68:69]
v_pk_add_f32 v[112:113], v[112:113], v[72:73]
v_pk_add_f32 v[114:115], v[114:115], v[72:73]
v_pk_add_f32 v[92:93], v[92:93], v[72:73]
v_pk_add_f32 v[94:95], v[94:95], v[72:73]
E_ 96
E_ 97
E_ 98
E_ 99
E_ 76
E_ 77
E_ 78
E_ 79
C_ 99,98,99
C_ 98,96,97
C_ 96,76,77
C_ 97,78,79
E_ 100
E_ 101
E_ 102
E_ 103
E_ 80
E_ 81
E_ 82
E_ 83
C_ 103,102,103
C_ 102,100,101
C_ 100,80,81
C_ 101,82,83
.Lstep0_1:
W_ 1
Nvvv 116,30,136,56
s_nop 0
Dv 38,%0,56320
Dv 42,v1,56320
W_ 2
Mvvv 116,34,156
E_ 104
Nvvv 120,30,140,60
E_ 105
Mvvv 120,34,160
E_ 106
Nvvv 124,30,144,64
E_ 107
Mvvv 124,34,164
E_ 84
Nvvv 128,30,148,68
E_ 85
Mvvv 128,34,168
E_ 86
Nvvv 132,30,152,72
E_ 87
Mvvv 132,34,172
W_ 0
C_ 107,106,107
C_ 106,104,105
Mvvv 132,38,192
s_nop 0
Dv 30,%0,57344
Dv 34,v1,57344
Mvvv 132,42,212
C_ 104,84,85
C_ 105,86,87
Mvvv 128,38,188
E_ 108
Mvvv 128,42,208
E_ 109
Mvvv 124,38,184
E_ 110
Mvvv 124,42,204
E_ 111
Mvvv 120,38,180
E_ 88
Mvvv 120,42,200
E_ 89
Mvvv 116,38,176
E_ 90
Mvvv 116,42,196
W_ 0
E_ 91
Mvvv 116,30,216
s_nop 0
Dv 38,%0,49152
Dv 42,v1,49152
Mvvv 116,34,236
C_ 111,110,111
C_ 110,108,109
Mvvv 120,30,220
C_ 108,88,89
C_ 109,90,91
Mvvv 120,34,240
E_ 112
Mvvv 124,30,224
E_ 113
Mvvv 124,34,244
E_ 114
Mvvv 128,30,228
E_ 115
Mvvv 128,34,248
E_ 92
Mvvv 132,30,232
E_ 93
Mvvv 132,34,252
W_ 0
E_ 94
Nvvv 76,38,136,56
s_nop 0
Dv 30,%0,50176
Dv 34,v1,50176
Mvvv 76,42,156
E_ 95
Nvvv 80,38,140,60
C_ 115,114,115
C_ 114,112,113
Mvvv 80,42,160
C_ 112,92,93
C_ 113,94,95
Nvvv 84,38,144,64
E_ 116
Mvvv 84,42,164
E_ 117
Nvvv 88,38,148,68
E_ 118
Mvvv 88,42,168
E_ 119
Nvvv 92,38,152,72
C_ 119,118,119
C_ 118,116,117
Mvvv 92,42,172
W_ 0
E_ 120
Mvvv 92,30,192
s_nop 0
Dv 38,%0,51200
Dv 42,v1,51200
Mvvv 92,34,212
X_ a180,v11
X_ a181,v11
Mvvv 88,30,188
X_ a182,v11
X_ a183,v11
Mvvv 88,34,208
E_ 121
Mvvv 84,30,184
E_ 122
Mvvv 84,34,204
E_ 123
Mvvv 80,30,180
C_ 123,122,123
C_ 122,120,121
Mvvv 80,34,200
E_ 124
Mvvv 76,30,176
E_ 125
Mvvv 76,34,196
S_ s65,s61,36864
E_ 126
W_ 1
Mvvv 76,38,216
S_ m0,s51,49152
E_ 127
W_ 0
Mvvv 76,42,236
s_waitcnt vmcnt(18) lgkmcnt(0)
s_barrier
C_ 127,126,127
Mvvv 80,38,220
Ln v7,32,s65,0
Mvvv 80,42,240
C_ 126,124,125
Mvvv 84,38,224
Ln v8,32,s65,3072
S_ m0,s51,50048
Mvvv 84,42,244
s_nop 0
Tv 30,v2,24576
Tv 32,v2,30720
Mvvv 88,38,228
Ln v7,32,s65,128
Mvvv 88,42,248
.Lpad_a0_0:
s_cbranch_execz .Lhop_a0_r
Tv 34,v3,24576
Tv 36,v3,30720
Mvvv 92,38,232
Ln v8,32,s65,3200
S_ m0,s51,50944
Mvvv 92,42,252
E_ 128
Zaav 220,180,96
Ln v7,32,s65,256
Zaav 224,180,100
E_ 129
Zaav 228,180,104
Ln v8,32,s65,3328
Zaav 232,180,108
E_ 130
Zaav 236,180,112
W_ 0
E_ 131
Zavv 0,30,96
s_nop 0
Tv 38,v4,24576
Tv 40,v4,30720
Zavv 20,34,96
C_ 131,130,131
C_ 130,128,129
Zavv 24,34,100
s_nop 0
Tv 42,v5,24576
Tv 44,v5,30720
Zavv 4,30,100
E_ 132
Zavv 8,30,104
E_ 133
Zavv 28,34,104
R_ v46,a220
R_ v47,a224
Zavv 32,34,108
R_ v48,a228
R_ v49,a232
Zavv 12,30,108
R_ v50,a236
Zavv 16,30,112
E_ 134
Zavv 36,34,112
W_ 0
E_ 135
Zavv 56,38,112
s_nop 0
Tv 30,v2,25600
Tv 32,v2,31744
Zavv 76,42,112
C_ 135,134,135
C_ 134,132,133
Zavv 72,42,108
s_nop 0
Tv 34,v3,25600
Tv 36,v3,31744
Zavv 52,38,108
E_ 76
Zavv 48,38,104
E_ 77
Zavv 68,42,104
E_ 78
Zavv 64,42,100
E_ 79
Zavv 44,38,100
C_ 116,76,77
C_ 117,78,79
Zavv 40,38,96
E_ 80
Zavv 60,42,96
W_ 0
E_ 81
Zavv 80,30,96
s_nop 0
Tv 38,v4,25600
Tv 40,v4,31744
Zavv 100,34,96
E_ 82
Zavv 104,34,100
s_nop 0
Tv 42,v5,25600
Tv 44,v5,31744
Zavv 84,30,100
E_ 83
Zavv 88,30,104
C_ 120,80,81
C_ 121,82,83
Zavv 108,34,104
E_ 84
Zavv 112,34,108
E_ 85
Zavv 92,30,108
E_ 86
Zavv 96,30,112
E_ 87
Zavv 116,34,112
W_ 0
C_ 124,84,85
C_ 125,86,87
Zavv 136,38,112
s_nop 0
Tv 30,v2,26624
Tv 32,v2,32768
Zavv 156,42,112
E_ 88
Zavv 152,42,108
s_nop 0
Tv 34,v3,26624
Tv 36,v3,32768
Zavv 132,38,108
E_ 89
Zavv 128,38,104
E_ 90
Zavv 148,42,104
E_ 91
Zavv 144,42,100
C_ 128,88,89
C_ 129,90,91
Zavv 124,38,100
E_ 92
Zavv 120,38,96
E_ 93
Zavv 140,42,96
W_ 0
E_ 94
Zavv 160,30,96
s_nop 0
Tv 38,v4,26624
Tv 40,v4,32768
Zavv 180,34,96
E_ 95
Zavv 184,34,100
s_nop 0
Tv 42,v5,26624
Tv 44,v5,32768
Zavv 164,30,100
C_ 132,92,93
C_ 133,94,95
Zavv 168,30,104
v_mov_b32 v92, 0x3f803f80
v_mov_b32 v93, 0x3f803f80
Zavv 188,34,104
S_ s65,s61,24576
v_mov_b32 v94, 0x3f803f80
v_mov_b32 v95, 0x3f803f80
Zavv 192,34,108
S_ m0,s51,24576
Zavv 172,30,108
s_waitcnt vmcnt(18) lgkmcnt(0)
s_barrier
Zavv 176,30,112
L_ v9,36,s65,0
Zavv 196,34,112
Zavv 216,38,112
Dv 30,%0,6144
Zavv 236,42,112
Dv 34,v1,6144
Zavv 232,42,108
L_ v9,36,s65,3072
S_ m0,s51,25472
Zavv 212,38,108
Zavv 208,38,104
L_ v9,36,s65,128
Zavv 228,42,104
Zavv 224,42,100
L_ v9,36,s65,3200
S_ m0,s51,26368
Zavv 204,38,100
Zavv 200,38,96
L_ v9,36,s65,256
Zavv 220,42,96
.Lstep0_2:
Zvvv 76,92,116
Dv 38,%0,7168
Zvvv 80,92,120
Dv 42,v1,7168
Zvvv 84,92,124
Zvvv 88,92,128
Zvvv 92,92,132
W_ 2
Nvvv 96,30,136,56
Mvvv 96,34,156
Nvvv 100,30,140,60
s_nop 1
v_max3_f32 v10, v76, v80, v84
s_nop 0
v_max3_f32 v10, v10, v88, v92
Mvvv 100,34,160
v_cmp_lt_f32 vcc, 0x42000000, v10
s_cbranch_vccnz .Ltramp0_1
Nvvv 104,30,144,64
A_ v46,v46,v76
A_ v47,v47,v80
Mvvv 104,34,164
A_ v48,v48,v84
A_ v49,v49,v88
Nvvv 108,30,148,68
A_ v50,v50,v92
Mvvv 108,34,168
L_ v9,36,s65,3328
Nvvv 112,30,152,72
R_ v11,a248
Mvvv 112,34,172
W_ 0
v_min_u32 v11, 240, v11
Mvvv 112,38,192
Dv 30,%0,8192
v_lshlrev_b32 v11, 3, v11
Mvvv 112,42,212
Dv 34,v1,8192
Mvvv 108,38,188
buffer_load_dword a246, v11, s[44:47], s66 offen
Mvvv 108,42,208
Mvvv 104,38,184
Mvvv 104,42,204
Mvvv 100,38,180
Mvvv 100,42,200
Mvvv 96,38,176
Mvvv 96,42,196
W_ 0
Mvvv 96,30,216
Dv 38,%0,0
Mvvv 96,34,236
Dv 42,v1,0
Mvvv 100,30,220
Mvvv 100,34,240
Mvvv 104,30,224
Mvvv 104,34,244
Mvvv 108,30,228
Mvvv 108,34,248
s_nop 0
E_ 96
Mvvv 112,30,232
E_ 97
Mvvv 112,34,252
W_ 0
E_ 98
Nvvv 76,38,136,56
Dv 30,%0,1024
Mvvv 76,42,156
Dv 34,v1,1024
Nvvv 80,38,140,60
E_ 99
Mvvv 80,42,160
C_ 99,98,99
C_ 98,96,97
Nvvv 84,38,144,64
E_ 100
Mvvv 84,42,164
E_ 101
Nvvv 88,38,148,68
E_ 102
Mvvv 88,42,168
E_ 103
Nvvv 92,38,152,72
C_ 103,102,103
C_ 102,100,101
Mvvv 92,42,172
W_ 0
E_ 104
Mvvv 92,30,192
Dv 38,%0,2048
Mvvv 92,34,212
Dv 42,v1,2048
Mvvv 88,30,188
E_ 105
Mvvv 88,34,208
E_ 106
Mvvv 84,30,184
S_ s65,s61,49152
E_ 107
Mvvv 84,34,204
S_ m0,s51,0
C_ 107,106,107
C_ 106,104,105
Mvvv 80,30,180
s_waitcnt vmcnt(13) lgkmcnt(0)
s_barrier
s_cmp_eq_u32 s11, 2
s_cbranch_scc1 .Lxs2m
.Lxs2m_back:
Mvvv 80,34,200
Ln v7,32,s65,0
Mvvv 76,30,176
E_ 108
Mvvv 76,34,196
Ln v8,32,s65,3072
S_ m0,s51,896
Mvvv 76,38,216
Tv 30,v2,12288
Tv 32,v2,18432
Mvvv 76,42,236
Ln v7,32,s65,128
Mvvv 80,38,220
s_nop 0
Tv 34,v3,12288
Tv 36,v3,18432
Mvvv 80,42,240
Ln v8,32,s65,3200
S_ m0,s51,1792
Mvvv 84,38,224
E_ 109
Mvvv 84,42,244
Ln v7,32,s65,256
Mvvv 88,38,228
E_ 110
Mvvv 88,42,248
Ln v8,32,s65,3328
Mvvv 92,38,232
E_ 111
Mvvv 92,42,252
W_ 0
C_ 111,110,111
C_ 110,108,109
Mavv 0,30,116
s_nop 0
Tv 38,v4,12288
Tv 40,v4,18432
Mavv 20,34,116
E_ 112
Mavv 24,34,120
s_nop 0
Tv 42,v5,12288
Tv 44,v5,18432
Mavv 4,30,120
E_ 113
Mavv 8,30,124
E_ 114
Mavv 28,34,124
E_ 115
Mavv 32,34,128
C_ 115,114,115
C_ 114,112,113
Mavv 12,30,128
E_ 76
Mavv 16,30,132
E_ 77
Mavv 36,34,132
W_ 0
E_ 78
Mavv 56,38,132
s_nop 0
Tv 30,v2,13312
Tv 32,v2,19456
Mavv 76,42,132
E_ 79
Mavv 72,42,128
s_nop 0
Tv 34,v3,13312
Tv 36,v3,19456
Mavv 52,38,128
C_ 96,76,77
C_ 97,78,79
Mavv 48,38,124
E_ 80
Mavv 68,42,124
E_ 81
Mavv 64,42,120
E_ 82
Mavv 44,38,120
E_ 83
Mavv 40,38,116
C_ 100,80,81
C_ 101,82,83
Mavv 60,42,116
W_ 0
E_ 84
Mavv 80,30,116
s_nop 0
Tv 38,v4,13312
Tv 40,v4,19456
Mavv 100,34,116
E_ 85
Mavv 104,34,120
s_nop 0
Tv 42,v5,13312
Tv 44,v5,19456
Mavv 84,30,120
E_ 86
Mavv 88,30,124
E_ 87
Mavv 108,34,124
C_ 104,84,85
C_ 105,86,87
Mavv 112,34,128
E_ 88
Mavv 92,30,128
E_ 89
Mavv 96,30,132
E_ 90
Mavv 116,34,132
W_ 0
E_ 91
Mavv 136,38,132
s_nop 0
Tv 30,v2,14336
Tv 32,v2,20480
Mavv 156,42,132
C_ 108,88,89
C_ 109,90,91
Mavv 152,42,128
s_nop 0
Tv 34,v3,14336
Tv 36,v3,20480
Mavv 132,38,128
E_ 92
Mavv 128,38,124
E_ 93
Mavv 148,42,124
E_ 94
Mavv 144,42,120
E_ 95
Mavv 124,38,120
C_ 112,92,93
C_ 113,94,95
Mavv 120,38,116
v_mov_b32 v92, 0x3f803f80
v_mov_b32 v93, 0x3f803f80
Mavv 140,42,116
W_ 0
v_mov_b32 v94, 0x3f803f80
v_mov_b32 v95, 0x3f803f80
Mavv 160,30,116
s_nop 0
Tv 38,v4,14336
Tv 40,v4,20480
Mavv 180,34,116
Mavv 184,34,120
s_nop 0
Tv 42,v5,14336
Tv 44,v5,20480
Mavv 164,30,120
Mavv 168,30,124
Mavv 188,34,124
S_ s65,s61,36864
Mavv 192,34,128
S_ m0,s51,12288
Mavv 172,30,128
s_waitcnt vmcnt(13) lgkmcnt(0)
s_barrier
s_cmp_eq_u32 s11, 2
s_cbranch_scc1 .Lxs2e
.Lxs2e_back:
Mavv 176,30,132
L_ v9,36,s65,0
Mavv 196,34,132
Mavv 216,38,132
Dv 30,%0,55296
Mavv 236,42,132
Dv 34,v1,55296
Mavv 232,42,128
L_ v9,36,s65,3072
S_ m0,s51,13184
Mavv 212,38,128
Mavv 208,38,124
L_ v9,36,s65,128
Mavv 228,42,124
Mavv 224,42,120
L_ v9,36,s65,3200
S_ m0,s51,14080
Mavv 204,38,120
Mavv 200,38,116
L_ v9,36,s65,256
Mavv 220,42,116
s_bitcmp1_b32 s67, 0
s_cbranch_scc1 .Lhsd0_0
.Lhsd0_0_back:
.Lstep0_3:
Zvvv 76,92,96
Dv 38,%0,56320
Zvvv 80,92,100
Dv 42,v1,56320
Zvvv 84,92,104
Zvvv 88,92,108
Zvvv 92,92,112
W_ 2
Nvvv 116,30,136,56
Mvvv 116,34,156
Nvvv 120,30,140,60
s_nop 1
v_max3_f32 v10, v76, v80, v84
s_nop 0
v_max3_f32 v10, v10, v88, v92
Mvvv 120,34,160
v_cmp_lt_f32 vcc, 0x42000000, v10
s_cbranch_vccnz .Ltramp0_2
Nvvv 124,30,144,64
A_ v46,v46,v76
A_ v47,v47,v80
Mvvv 124,34,164
A_ v48,v48,v84
A_ v49,v49,v88
Nvvv 128,30,148,68
A_ v50,v50,v92
Mvvv 128,34,168
L_ v9,36,s65,3328
Nvvv 132,30,152,72
Mvvv 132,34,172
W_ 0
Mvvv 132,38,192
Dv 30,%0,57344
Mvvv 132,42,212
Dv 34,v1,57344
Mvvv 128,38,188
Mvvv 128,42,208
Mvvv 124,38,184
Mvvv 124,42,204
Mvvv 120,38,180
Mvvv 120,42,200
Mvvv 116,38,176
Mvvv 116,42,196
W_ 0
Mvvv 116,30,216
Dv 38,%0,49152
Mvvv 116,34,236
Dv 42,v1,49152
Mvvv 120,30,220
Mvvv 120,34,240
Mvvv 124,30,224
Mvvv 124,34,244
Mvvv 128,30,228
Mvvv 128,34,248
s_nop 0
E_ 116
Mvvv 132,30,232
E_ 117
Mvvv 132,34,252
W_ 0
E_ 118
Nvvv 76,38,136,56
Dv 30,%0,50176
Mvvv 76,42,156
Dv 34,v1,50176
Nvvv 80,38,140,60
E_ 119
Mvvv 80,42,160
C_ 119,118,119
C_ 118,116,117
Nvvv 84,38,144,64
E_ 120
Mvvv 84,42,164
E_ 121
Nvvv 88,38,148,68
E_ 122
Mvvv 88,42,168
E_ 123
Nvvv 92,38,152,72
C_ 123,122,123
C_ 122,120,121
Mvvv 92,42,172
W_ 0
E_ 124
Mvvv 92,30,192
Dv 38,%0,51200
Mvvv 92,34,212
Dv 42,v1,51200
Mvvv 88,30,188
E_ 125
Mvvv 88,34,208
E_ 126
Mvvv 84,30,184
S_ s65,s61,61440
E_ 127
Mvvv 84,34,204
S_ m0,s51,49152
C_ 127,126,127
C_ 126,124,125
Mvvv 80,30,180
s_waitcnt vmcnt(13) lgkmcnt(0)
s_barrier
s_cmp_eq_u32 s11, 2
s_cbranch_scc1 .Lxs3m
.Lxs3m_back:
Mvvv 80,34,200
Ln v7,32,s65,0
Mvvv 76,30,176
E_ 128
Mvvv 76,34,196
Ln v8,32,s65,3072
S_ m0,s51,50048
Mvvv 76,38,216
Tv 30,v2,24576
Tv 32,v2,30720
Mvvv 76,42,236
Ln v7,32,s65,128
Mvvv 80,38,220
s_nop 0
Tv 34,v3,24576
Tv 36,v3,30720
Mvvv 80,42,240
Ln v8,32,s65,3200
S_ m0,s51,50944
Mvvv 84,38,224
E_ 129
Mvvv 84,42,244
Ln v7,32,s65,256
Mvvv 88,38,228
.Lpad_a0_1:
s_cbranch_execz .Lhop_a0_r
E_ 130
Mvvv 88,42,248
Ln v8,32,s65,3328
Mvvv 92,38,232
E_ 131
Mvvv 92,42,252
W_ 0
C_ 131,130,131
C_ 130,128,129
Mavv 0,30,96
s_nop 0
Tv 38,v4,24576
Tv 40,v4,30720
Mavv 20,34,96
E_ 132
Mavv 24,34,100
s_nop 0
Tv 42,v5,24576
Tv 44,v5,30720
Mavv 4,30,100
E_ 133
Mavv 8,30,104
E_ 134
Mavv 28,34,104
E_ 135
Mavv 32,34,108
C_ 135,134,135
C_ 134,132,133
Mavv 12,30,108
E_ 76
Mavv 16,30,112
E_ 77
Mavv 36,34,112
W_ 0
E_ 78
Mavv 56,38,112
s_nop 0
Tv 30,v2,25600
Tv 32,v2,31744
Mavv 76,42,112
E_ 79
Mavv 72,42,108
s_nop 0
Tv 34,v3,25600
Tv 36,v3,31744
Mavv 52,38,108
C_ 116,76,77
C_ 117,78,79
Mavv 48,38,104
E_ 80
Mavv 68,42,104
E_ 81
Mavv 64,42,100
E_ 82
Mavv 44,38,100
E_ 83
Mavv 40,38,96
C_ 120,80,81
C_ 121,82,83
Mavv 60,42,96
W_ 0
E_ 84
Mavv 80,30,96
s_nop 0
Tv 38,v4,25600
Tv 40,v4,31744
Mavv 100,34,96
E_ 85
Mavv 104,34,100
s_nop 0
Tv 42,v5,25600
Tv 44,v5,31744
Mavv 84,30,100
E_ 86
Mavv 88,30,104
E_ 87
Mavv 108,34,104
C_ 124,84,85
C_ 125,86,87
Mavv 112,34,108
E_ 88
Mavv 92,30,108
E_ 89
Mavv 96,30,112
E_ 90
Mavv 116,34,112
W_ 0
E_ 91
Mavv 136,38,112
s_nop 0
Tv 30,v2,26624
Tv 32,v2,32768
Mavv 156,42,112
C_ 128,88,89
C_ 129,90,91
Mavv 152,42,108
s_nop 0
Tv 34,v3,26624
Tv 36,v3,32768
Mavv 132,38,108
E_ 92
Mavv 128,38,104
E_ 93
Mavv 148,42,104
E_ 94
Mavv 144,42,100
E_ 95
Mavv 124,38,100
C_ 132,92,93
C_ 133,94,95
Mavv 120,38,96
v_mov_b32 v92, 0x3f803f80
v_mov_b32 v93, 0x3f803f80
Mavv 140,42,96
W_ 0
v_mov_b32 v94, 0x3f803f80
v_mov_b32 v95, 0x3f803f80
Mavv 160,30,96
s_nop 0
Tv 38,v4,26624
Tv 40,v4,32768
Mavv 180,34,96
Mavv 184,34,100
s_nop 0
Tv 42,v5,26624
Tv 44,v5,32768
Mavv 164,30,100
Mavv 168,30,104
Mavv 188,34,104
S_ s65,s61,49152
Mavv 192,34,108
S_ m0,s51,24576
Mavv 172,30,108
s_waitcnt vmcnt(12) lgkmcnt(0)
s_barrier
s_cmp_eq_u32 s11, 2
s_cbranch_scc1 .Lxs3e
.Lxs3e_back:
Mavv 176,30,112
L_ v9,36,s65,0
Mavv 196,34,112
S_ s61,s61,24576
Mavv 216,38,112
s_lshr_b32 s67, s67, 1
Dv 30,%0,6144
Mavv 236,42,112
s_bitcmp1_b32 s67, 9
s_cselect_b32 s11, s13, s11
Dv 34,v1,6144
Mavv 232,42,108
L_ v9,36,s65,3072
S_ m0,s51,25472
Mavv 212,38,108
Mavv 208,38,104
L_ v9,36,s65,128
Mavv 228,42,104
Mavv 224,42,100
L_ v9,36,s65,3200
S_ m0,s51,26368
Mavv 204,38,100
Mavv 200,38,96
L_ v9,36,s65,256
Mavv 220,42,96
s_cmp_ge_u32 s67, 256
s_cbranch_scc1 .Lstep0_2
s_sub_u32 s61, s61, 147456
.Lstep0_14:
Zvvv 76,92,116
Dv 38,%0,7168
Zvvv 80,92,120
Dv 42,v1,7168
Zvvv 84,92,124
Zvvv 88,92,128
Zvvv 92,92,132
W_ 2
Nvvv 96,30,136,56
Mvvv 96,34,156
Nvvv 100,30,140,60
s_nop 1
v_max3_f32 v10, v76, v80, v84
s_nop 0
v_max3_f32 v10, v10, v88, v92
Mvvv 100,34,160
v_cmp_lt_f32 vcc, 0x42000000, v10
s_cbranch_vccnz .Ltramp0_13
Nvvv 104,30,144,64
A_ v46,v46,v76
A_ v47,v47,v80
Mvvv 104,34,164
A_ v48,v48,v84
A_ v49,v49,v88
Nvvv 108,30,148,68
A_ v50,v50,v92
Mvvv 108,34,168
L_ v9,36,s65,3328
Nvvv 112,30,152,72
R_ v20,a240
Mvvv 112,34,172
W_ 0
v_xor_b32 v21, 64, v20
Mvvv 112,38,192
S_ s65,s60,0
Dv 30,%0,8192
Mvvv 112,42,212
S_ m0,s50,0
Dv 34,v1,8192
Mvvv 108,38,188
L_ v20,24,s65,0
Mvvv 108,42,208
S_ s65,s60,10240
Mvvv 104,38,184
S_ m0,s50,1024
Mvvv 104,42,204
L_ v21,24,s65,0
Mvvv 100,38,180
S_ s65,s60,20480
Mvvv 100,42,200
S_ m0,s50,2048
Mvvv 96,38,176
L_ v20,24,s65,0
Mvvv 96,42,196
S_ s65,s60,30720
W_ 0
Mvvv 96,30,216
S_ m0,s50,3072
Dv 38,%0,0
Mvvv 96,34,236
Dv 42,v1,0
Mvvv 100,30,220
L_ v21,24,s65,0
Mvvv 100,34,240
S_ s65,s60,40960
Mvvv 104,30,224
S_ m0,s50,4096
Mvvv 104,34,244
L_ v20,24,s65,0
Mvvv 108,30,228
Mvvv 108,34,248
E_ 96
Mvvv 112,30,232
E_ 97
Mvvv 112,34,252
W_ 0
E_ 98
Nvvv 76,38,136,56
Dv 30,%0,1024
Mvvv 76,42,156
Dv 34,v1,1024
Nvvv 80,38,140,60
E_ 99
Mvvv 80,42,160
C_ 99,98,99
C_ 98,96,97
Nvvv 84,38,144,64
E_ 100
Mvvv 84,42,164
E_ 101
Nvvv 88,38,148,68
E_ 102
Mvvv 88,42,168
E_ 103
Nvvv 92,38,152,72
C_ 103,102,103
C_ 102,100,101
Mvvv 92,42,172
W_ 0
E_ 104
Mvvv 92,30,192
Dv 38,%0,2048
Mvvv 92,34,212
Dv 42,v1,2048
Mvvv 88,30,188
E_ 105
Mvvv 88,34,208
E_ 106
Mvvv 84,30,184
E_ 107
Mvvv 84,34,204
C_ 107,106,107
C_ 106,104,105
Mvvv 80,30,180
s_waitcnt vmcnt(17) lgkmcnt(0)
s_barrier
s_cmp_eq_u32 s11, 2
s_cbranch_scc1 .Lxs14m
.Lxs14m_back:
Mvvv 80,34,200
E_ 108
Mvvv 76,30,176
E_ 109
Mvvv 76,34,196
E_ 110
Mvvv 76,38,216
s_nop 0
Tv 30,v2,12288
Tv 32,v2,18432
Mvvv 76,42,236
E_ 111
Mvvv 80,38,220
s_nop 0
Tv 34,v3,12288
Tv 36,v3,18432
Mvvv 80,42,240
C_ 111,110,111
C_ 110,108,109
Mvvv 84,38,224
E_ 112
Mvvv 84,42,244
E_ 113
Mvvv 88,38,228
E_ 114
Mvvv 88,42,248
E_ 115
Mvvv 92,38,232
C_ 115,114,115
C_ 114,112,113
Mvvv 92,42,252
W_ 0
E_ 76
Mavv 0,30,116
s_nop 0
Tv 38,v4,12288
Tv 40,v4,18432
Mavv 20,34,116
E_ 77
Mavv 24,34,120
s_nop 0
Tv 42,v5,12288
Tv 44,v5,18432
Mavv 4,30,120
E_ 78
Mavv 8,30,124
E_ 79
Mavv 28,34,124
C_ 96,76,77
C_ 97,78,79
Mavv 32,34,128
E_ 80
Mavv 12,30,128
E_ 81
Mavv 16,30,132
E_ 82
Mavv 36,34,132
W_ 0
E_ 83
Mavv 56,38,132
s_nop 0
Tv 30,v2,13312
Tv 32,v2,19456
Mavv 76,42,132
C_ 100,80,81
C_ 101,82,83
Mavv 72,42,128
s_nop 0
Tv 34,v3,13312
Tv 36,v3,19456
Mavv 52,38,128
E_ 84
Mavv 48,38,124
E_ 85
Mavv 68,42,124
E_ 86
Mavv 64,42,120
E_ 87
Mavv 44,38,120
C_ 104,84,85
C_ 105,86,87
Mavv 40,38,116
E_ 88
Mavv 60,42,116
W_ 0
E_ 89
Mavv 80,30,116
s_nop 0
Tv 38,v4,13312
Tv 40,v4,19456
Mavv 100,34,116
E_ 90
Mavv 104,34,120
s_nop 0
Tv 42,v5,13312
Tv 44,v5,19456
Mavv 84,30,120
E_ 91
Mavv 88,30,124
C_ 108,88,89
C_ 109,90,91
Mavv 108,34,124
E_ 92
Mavv 112,34,128
E_ 93
Mavv 92,30,128
E_ 94
Mavv 96,30,132
E_ 95
Mavv 116,34,132
W_ 0
C_ 112,92,93
C_ 113,94,95
Mavv 136,38,132
s_nop 0
Tv 30,v2,14336
Tv 32,v2,20480
Mavv 156,42,132
v_mov_b32 v92, 0x3f803f80
v_mov_b32 v93, 0x3f803f80
Mavv 152,42,128
s_nop 0
Tv 34,v3,14336
Tv 36,v3,20480
Mavv 132,38,128
v_mov_b32 v94, 0x3f803f80
v_mov_b32 v95, 0x3f803f80
)A"
R"A(Mavv 128,38,124
Mavv 148,42,124
Mavv 144,42,120
Mavv 124,38,120
Mavv 120,38,116
Mavv 140,42,116
W_ 0
Mavv 160,30,116
s_nop 0
Tv 38,v4,14336
Tv 40,v4,20480
Mavv 180,34,116
Mavv 184,34,120
s_nop 0
Tv 42,v5,14336
Tv 44,v5,20480
Mavv 164,30,120
Mavv 168,30,124
Mavv 188,34,124
Mavv 192,34,128
Mavv 172,30,128
s_waitcnt vmcnt(11) lgkmcnt(0)
s_barrier
s_cmp_eq_u32 s11, 2
s_cbranch_scc1 .Lxs14e
.Lxs14e_back:
Mavv 176,30,132
S_ s65,s59,0
Mavv 196,34,132
S_ m0,s52,8192
Mavv 216,38,132
Dv 30,%0,55296
Mavv 236,42,132
Dv 34,v1,55296
Mavv 232,42,128
L_ v6,28,s65,0
Mavv 212,38,128
S_ s65,s59,98304
Mavv 208,38,124
S_ m0,s52,9216
Mavv 228,42,124
L_ v6,28,s65,0
Mavv 224,42,120
S_ s65,s59,196608
Mavv 204,38,120
S_ m0,s52,10240
Mavv 200,38,116
L_ v6,28,s65,0
Mavv 220,42,116
S_ s65,s59,294912
.Lstep0_15:
Zvvv 76,92,96
Dv 38,%0,56320
Zvvv 80,92,100
Dv 42,v1,56320
Zvvv 84,92,104
Zvvv 88,92,108
Zvvv 92,92,112
W_ 2
Nvvv 116,30,136,56
Mvvv 116,34,156
Nvvv 120,30,140,60
s_nop 1
v_max3_f32 v10, v76, v80, v84
s_nop 0
v_max3_f32 v10, v10, v88, v92
Mvvv 120,34,160
v_cmp_lt_f32 vcc, 0x42000000, v10
s_cbranch_vccnz .Ltramp0_14
Nvvv 124,30,144,64
S_ m0,s52,11264
A_ v46,v46,v76
A_ v47,v47,v80
Mvvv 124,34,164
A_ v48,v48,v84
A_ v49,v49,v88
Nvvv 128,30,148,68
A_ v50,v50,v92
Mvvv 128,34,168
L_ v6,28,s65,0
Nvvv 132,30,152,72
S_ s65,s59,0
Mvvv 132,34,172
S_ m0,s52,16256
W_ 0
Mvvv 132,38,192
Dv 30,%0,57344
Mvvv 132,42,212
Dv 34,v1,57344
Mvvv 128,38,188
L_ v6,28,s65,128
Mvvv 128,42,208
S_ s65,s59,98304
Mvvv 124,38,184
S_ m0,s52,17280
Mvvv 124,42,204
L_ v6,28,s65,128
Mvvv 120,38,180
S_ s65,s59,196608
Mvvv 120,42,200
S_ m0,s52,18304
Mvvv 116,38,176
L_ v6,28,s65,128
Mvvv 116,42,196
S_ s65,s59,294912
W_ 0
Mvvv 116,30,216
S_ m0,s52,19328
Dv 38,%0,49152
Mvvv 116,34,236
Dv 42,v1,49152
Mvvv 120,30,220
L_ v6,28,s65,128
Mvvv 120,34,240
S_ s65,s59,786432
Mvvv 124,30,224
S_ m0,s52,0
Mvvv 124,34,244
L_ v6,28,s65,0
Mvvv 128,30,228
S_ s65,s59,884736
Mvvv 128,34,248
S_ m0,s52,1024
E_ 116
Mvvv 132,30,232
L_ v6,28,s65,0
Mvvv 132,34,252
S_ s65,s59,983040
E_ 117
W_ 1
Nvvv 76,38,136,56
S_ m0,s52,2048
Dv 30,%0,50176
W_ 1
Mvvv 76,42,156
Dv 34,v1,50176
Nvvv 80,38,140,60
L_ v6,28,s65,0
Mvvv 80,42,160
S_ s65,s59,1081344
E_ 118
Nvvv 84,38,144,64
S_ m0,s52,3072
E_ 119
Mvvv 84,42,164
L_ v6,28,s65,0
Nvvv 88,38,148,68
S_ s65,s60,128
C_ 119,118,119
C_ 118,116,117
Mvvv 88,42,168
S_ m0,s50,20480
E_ 120
Nvvv 92,38,152,72
L_ v20,24,s65,0
Mvvv 92,42,172
S_ s65,s60,10368
E_ 121
W_ 1
Mvvv 92,30,192
S_ m0,s50,21504
Dv 38,%0,51200
W_ 1
Mvvv 92,34,212
Dv 42,v1,51200
Mvvv 88,30,188
L_ v21,24,s65,0
Mvvv 88,34,208
S_ s65,s60,20608
E_ 122
Mvvv 84,30,184
S_ m0,s50,22528
E_ 123
Mvvv 84,34,204
L_ v20,24,s65,0
Mvvv 80,30,180
s_waitcnt vmcnt(20) lgkmcnt(0)
s_barrier
s_cmp_eq_u32 s11, 2
s_cbranch_scc1 .Lxs15m
.Lxs15m_back:
S_ s65,s60,30848
Mvvv 80,34,200
S_ m0,s50,23552
C_ 123,122,123
C_ 122,120,121
Mvvv 76,30,176
L_ v21,24,s65,0
Mvvv 76,34,196
S_ s65,s60,41088
E_ 124
Mvvv 76,38,216
.Lpad_a0_2:
s_cbranch_execz .Lhop_a0_r
S_ m0,s50,24576
Tv 30,v2,24576
Tv 32,v2,30720
Mvvv 76,42,236
L_ v20,24,s65,0
Mvvv 80,38,220
Tv 34,v3,24576
Tv 36,v3,30720
Mvvv 80,42,240
E_ 125
Mvvv 84,38,224
E_ 126
Mvvv 84,42,244
E_ 127
Mvvv 88,38,228
C_ 127,126,127
C_ 126,124,125
Mvvv 88,42,248
E_ 128
Mvvv 92,38,232
E_ 129
Mvvv 92,42,252
W_ 0
E_ 130
Mavv 0,30,96
s_nop 0
Tv 38,v4,24576
Tv 40,v4,30720
Mavv 20,34,96
E_ 131
Mavv 24,34,100
s_nop 0
Tv 42,v5,24576
Tv 44,v5,30720
Mavv 4,30,100
C_ 131,130,131
C_ 130,128,129
Mavv 8,30,104
E_ 132
Mavv 28,34,104
E_ 133
Mavv 32,34,108
E_ 134
Mavv 12,30,108
E_ 135
Mavv 16,30,112
C_ 135,134,135
C_ 134,132,133
Mavv 36,34,112
W_ 0
E_ 76
Mavv 56,38,112
s_nop 0
Tv 30,v2,25600
Tv 32,v2,31744
Mavv 76,42,112
E_ 77
Mavv 72,42,108
s_nop 0
Tv 34,v3,25600
Tv 36,v3,31744
Mavv 52,38,108
E_ 78
Mavv 48,38,104
E_ 79
Mavv 68,42,104
C_ 116,76,77
C_ 117,78,79
Mavv 64,42,100
E_ 80
Mavv 44,38,100
E_ 81
Mavv 40,38,96
E_ 82
Mavv 60,42,96
W_ 0
E_ 83
Mavv 80,30,96
s_nop 0
Tv 38,v4,25600
Tv 40,v4,31744
Mavv 100,34,96
C_ 120,80,81
C_ 121,82,83
Mavv 104,34,100
s_nop 0
Tv 42,v5,25600
Tv 44,v5,31744
Mavv 84,30,100
E_ 84
Mavv 88,30,104
E_ 85
Mavv 108,34,104
E_ 86
Mavv 112,34,108
E_ 87
Mavv 92,30,108
C_ 124,84,85
C_ 125,86,87
Mavv 96,30,112
E_ 88
Mavv 116,34,112
W_ 0
E_ 89
Mavv 136,38,112
s_nop 0
Tv 30,v2,26624
Tv 32,v2,32768
Mavv 156,42,112
E_ 90
Mavv 152,42,108
s_nop 0
Tv 34,v3,26624
Tv 36,v3,32768
Mavv 132,38,108
E_ 91
Mavv 128,38,104
C_ 128,88,89
C_ 129,90,91
Mavv 148,42,104
E_ 92
Mavv 144,42,100
E_ 93
Mavv 124,38,100
E_ 94
Mavv 120,38,96
E_ 95
Mavv 140,42,96
W_ 0
C_ 132,92,93
C_ 133,94,95
Mavv 160,30,96
s_nop 0
Tv 38,v4,26624
Tv 40,v4,32768
Mavv 180,34,96
v_mov_b32 v92, 0x3f803f80
v_mov_b32 v93, 0x3f803f80
Mavv 184,34,100
s_nop 0
Tv 42,v5,26624
Tv 44,v5,32768
Mavv 164,30,100
v_mov_b32 v94, 0x3f803f80
v_mov_b32 v95, 0x3f803f80
Mavv 168,30,104
Mavv 188,34,104
Mavv 192,34,108
Mavv 172,30,108
s_waitcnt vmcnt(63) lgkmcnt(0)
s_barrier
s_cmp_eq_u32 s11, 2
s_cbranch_scc1 .Lxs15e
.Lxs15e_back:
Mavv 176,30,112
S_ s65,s59,0
Mavv 196,34,112
S_ m0,s52,24320
Mavv 216,38,112
s_nop 0
Tv 30,v2,36864
Tv 32,v2,43008
Zvvv 76,92,116
L_ v6,28,s65,256
Zvvv 80,92,120
S_ s65,s59,98304
Tv 34,v3,36864
Tv 36,v3,43008
Zvvv 84,92,124
S_ m0,s52,25344
Zvvv 88,92,128
L_ v6,28,s65,256
Zvvv 92,92,132
S_ s65,s59,196608
Mavv 236,42,112
S_ m0,s52,26368
Mavv 232,42,108
L_ v6,28,s65,256
Mavv 212,38,108
S_ s65,s59,294912
Mavv 208,38,104
S_ m0,s52,27392
v_max3_f32 v10, v76, v80, v84
Mavv 228,42,104
v_max3_f32 v10, v10, v88, v92
v_cmp_lt_f32 vcc, 0x42000000, v10
Mavv 224,42,100
L_ v6,28,s65,256
Mavv 204,38,100
Mavv 200,38,96
Mavv 220,42,96
s_cbranch_vccnz .Ltramp0_15
.Ljoin0:
W_ 2
Mavv 0,30,116
s_mov_b32 s58, 1
Tv 38,v4,36864
Tv 40,v4,43008
W_ 2
Mavv 20,34,116
S_ s61,s61,393216
R_ v12,a248
s_mov_b32 s85, 4026531840
Mavv 24,34,120
s_nop 0
Tv 42,v5,36864
Tv 44,v5,43008
Mavv 4,30,120
A_ v46,v46,v76
A_ v47,v47,v80
Mavv 8,30,124
A_ v48,v48,v84
A_ v49,v49,v88
Mavv 28,34,124
A_ v50,v50,v92
Mavv 32,34,128
v_rcp_f32 v46, v46
Mavv 12,30,128
v_rcp_f32 v47, v47
Mavv 16,30,132
v_rcp_f32 v48, v48
Mavv 36,34,132
W_ 0
v_rcp_f32 v49, v49
Mavv 56,38,132
s_nop 0
Tv 30,v2,37888
Tv 32,v2,44032
Mavv 76,42,132
v_rcp_f32 v50, v50
Mavv 72,42,128
s_nop 0
Tv 34,v3,37888
Tv 36,v3,44032
Mavv 52,38,128
R_ v96,a0
R_ v97,a1
Mavv 48,38,124
R_ v98,a2
R_ v99,a3
Mavv 68,42,124
R_ v100,a20
R_ v101,a21
Mavv 64,42,120
R_ v102,a22
R_ v103,a23
Mavv 44,38,120
U_ v96,v96,v46
U_ v97,v97,v46
Mavv 40,38,116
U_ v98,v98,v46
U_ v99,v99,v46
Mavv 60,42,116
W_ 0
U_ v100,v100,v46
U_ v101,v101,v46
Mavv 80,30,116
s_nop 0
Tv 38,v4,37888
Tv 40,v4,44032
Mavv 100,34,116
U_ v102,v102,v46
U_ v103,v103,v46
Mavv 104,34,120
s_nop 0
Tv 42,v5,37888
Tv 44,v5,44032
Mavv 84,30,120
C_ 76,96,97
C_ 77,98,99
Mavv 88,30,124
C_ 78,100,101
C_ 79,102,103
Mavv 108,34,124
X_ a0,v76
X_ a1,v77
Mavv 112,34,128
X_ a2,v78
X_ a3,v79
Mavv 92,30,128
R_ v104,a4
R_ v105,a5
Mavv 96,30,132
R_ v106,a6
R_ v107,a7
Mavv 116,34,132
W_ 0
R_ v108,a24
R_ v109,a25
Mavv 136,38,132
s_nop 0
Tv 30,v2,38912
Tv 32,v2,45056
Mavv 156,42,132
R_ v110,a26
R_ v111,a27
Mavv 152,42,128
s_nop 0
Tv 34,v3,38912
Tv 36,v3,45056
Mavv 132,38,128
U_ v104,v104,v47
U_ v105,v105,v47
Mavv 128,38,124
U_ v106,v106,v47
U_ v107,v107,v47
Mavv 148,42,124
U_ v108,v108,v47
U_ v109,v109,v47
Mavv 144,42,120
U_ v110,v110,v47
U_ v111,v111,v47
Mavv 124,38,120
C_ 80,104,105
C_ 81,106,107
Mavv 120,38,116
C_ 82,108,109
C_ 83,110,111
Mavv 140,42,116
W_ 0
X_ a4,v80
X_ a5,v81
Mavv 160,30,116
s_nop 0
Tv 38,v4,38912
Tv 40,v4,45056
Mavv 180,34,116
X_ a6,v82
X_ a7,v83
Mavv 184,34,120
s_nop 0
Tv 42,v5,38912
Tv 44,v5,45056
Mavv 164,30,120
R_ v96,a8
R_ v97,a9
Mavv 168,30,124
R_ v98,a10
R_ v99,a11
Mavv 188,34,124
R_ v100,a28
R_ v101,a29
Mavv 192,34,128
R_ v102,a30
R_ v103,a31
Mavv 172,30,128
U_ v96,v96,v48
U_ v97,v97,v48
Mavv 176,30,132
U_ v98,v98,v48
U_ v99,v99,v48
Mavv 196,34,132
W_ 0
U_ v100,v100,v48
U_ v101,v101,v48
Mavv 216,38,132
U_ v102,v102,v48
U_ v103,v103,v48
Mavv 236,42,132
C_ 84,96,97
C_ 85,98,99
Mavv 232,42,128
C_ 86,100,101
C_ 87,102,103
Mavv 212,38,128
X_ a8,v84
X_ a9,v85
Mavv 208,38,124
X_ a10,v86
X_ a11,v87
Mavv 228,42,124
Mavv 224,42,120
Mavv 204,38,120
Mavv 200,38,116
Mavv 220,42,116
.p2align 3
R_ v104,a12
R_ v105,a13
R_ v106,a14
R_ v107,a15
R_ v108,a32
R_ v109,a33
R_ v110,a34
R_ v111,a35
v_pk_mul_f32 v[104:105], v[104:105], v[48:49] op_sel:[0,1]
v_pk_mul_f32 v[106:107], v[106:107], v[48:49] op_sel:[0,1]
v_pk_mul_f32 v[108:109], v[108:109], v[48:49] op_sel:[0,1]
v_pk_mul_f32 v[110:111], v[110:111], v[48:49] op_sel:[0,1]
C_ 88,104,105
C_ 89,106,107
C_ 90,108,109
C_ 91,110,111
X_ a12,v88
X_ a13,v89
X_ a14,v90
X_ a15,v91
.p2align 3
R_ v96,a16
R_ v97,a17
R_ v98,a18
R_ v99,a19
R_ v100,a36
R_ v101,a37
R_ v102,a38
R_ v103,a39
v_pk_mul_f32 v[96:97], v[96:97], v[50:51] op_sel_hi:[1,0]
v_pk_mul_f32 v[98:99], v[98:99], v[50:51] op_sel_hi:[1,0]
v_pk_mul_f32 v[100:101], v[100:101], v[50:51] op_sel_hi:[1,0]
v_pk_mul_f32 v[102:103], v[102:103], v[50:51] op_sel_hi:[1,0]
C_ 92,96,97
C_ 93,98,99
C_ 94,100,101
C_ 95,102,103
X_ a16,v92
X_ a17,v93
X_ a18,v94
X_ a19,v95
.p2align 3
R_ v104,a40
R_ v105,a41
R_ v106,a42
R_ v107,a43
R_ v108,a60
R_ v109,a61
R_ v110,a62
R_ v111,a63
v_pk_mul_f32 v[104:105], v[104:105], v[46:47] op_sel_hi:[1,0]
v_pk_mul_f32 v[106:107], v[106:107], v[46:47] op_sel_hi:[1,0]
v_pk_mul_f32 v[108:109], v[108:109], v[46:47] op_sel_hi:[1,0]
v_pk_mul_f32 v[110:111], v[110:111], v[46:47] op_sel_hi:[1,0]
C_ 76,104,105
C_ 77,106,107
C_ 78,108,109
C_ 79,110,111
X_ a40,v76
X_ a41,v77
X_ a42,v78
X_ a43,v79
.p2align 3
R_ v96,a44
R_ v97,a45
R_ v98,a46
R_ v99,a47
R_ v100,a64
R_ v101,a65
R_ v102,a66
R_ v103,a67
v_pk_mul_f32 v[96:97], v[96:97], v[46:47] op_sel:[0,1]
v_pk_mul_f32 v[98:99], v[98:99], v[46:47] op_sel:[0,1]
v_pk_mul_f32 v[100:101], v[100:101], v[46:47] op_sel:[0,1]
v_pk_mul_f32 v[102:103], v[102:103], v[46:47] op_sel:[0,1]
C_ 80,96,97
C_ 81,98,99
C_ 82,100,101
C_ 83,102,103
X_ a44,v80
X_ a45,v81
X_ a46,v82
X_ a47,v83
.p2align 3
R_ v104,a48
R_ v105,a49
R_ v106,a50
R_ v107,a51
R_ v108,a68
R_ v109,a69
R_ v110,a70
R_ v111,a71
v_pk_mul_f32 v[104:105], v[104:105], v[48:49] op_sel_hi:[1,0]
v_pk_mul_f32 v[106:107], v[106:107], v[48:49] op_sel_hi:[1,0]
v_pk_mul_f32 v[108:109], v[108:109], v[48:49] op_sel_hi:[1,0]
v_pk_mul_f32 v[110:111], v[110:111], v[48:49] op_sel_hi:[1,0]
C_ 84,104,105
C_ 85,106,107
C_ 86,108,109
C_ 87,110,111
X_ a48,v84
X_ a49,v85
X_ a50,v86
X_ a51,v87
.p2align 3
R_ v96,a52
R_ v97,a53
R_ v98,a54
R_ v99,a55
R_ v100,a72
R_ v101,a73
R_ v102,a74
R_ v103,a75
v_pk_mul_f32 v[96:97], v[96:97], v[48:49] op_sel:[0,1]
v_pk_mul_f32 v[98:99], v[98:99], v[48:49] op_sel:[0,1]
v_pk_mul_f32 v[100:101], v[100:101], v[48:49] op_sel:[0,1]
v_pk_mul_f32 v[102:103], v[102:103], v[48:49] op_sel:[0,1]
C_ 88,96,97
C_ 89,98,99
C_ 90,100,101
C_ 91,102,103
X_ a52,v88
X_ a53,v89
X_ a54,v90
X_ a55,v91
.p2align 3
R_ v104,a56
R_ v105,a57
R_ v106,a58
R_ v107,a59
R_ v108,a76
R_ v109,a77
R_ v110,a78
R_ v111,a79
v_pk_mul_f32 v[104:105], v[104:105], v[50:51] op_sel_hi:[1,0]
v_pk_mul_f32 v[106:107], v[106:107], v[50:51] op_sel_hi:[1,0]
v_pk_mul_f32 v[108:109], v[108:109], v[50:51] op_sel_hi:[1,0]
v_pk_mul_f32 v[110:111], v[110:111], v[50:51] op_sel_hi:[1,0]
C_ 92,104,105
C_ 93,106,107
C_ 94,108,109
C_ 95,110,111
X_ a56,v92
X_ a57,v93
X_ a58,v94
X_ a59,v95
.p2align 3
R_ v96,a80
R_ v97,a81
R_ v98,a82
R_ v99,a83
R_ v100,a100
R_ v101,a101
R_ v102,a102
R_ v103,a103
v_pk_mul_f32 v[96:97], v[96:97], v[46:47] op_sel_hi:[1,0]
v_pk_mul_f32 v[98:99], v[98:99], v[46:47] op_sel_hi:[1,0]
v_pk_mul_f32 v[100:101], v[100:101], v[46:47] op_sel_hi:[1,0]
v_pk_mul_f32 v[102:103], v[102:103], v[46:47] op_sel_hi:[1,0]
C_ 76,96,97
C_ 77,98,99
C_ 78,100,101
C_ 79,102,103
X_ a80,v76
X_ a81,v77
X_ a82,v78
X_ a83,v79
.p2align 3
R_ v104,a84
R_ v105,a85
R_ v106,a86
R_ v107,a87
R_ v108,a104
R_ v109,a105
R_ v110,a106
R_ v111,a107
v_pk_mul_f32 v[104:105], v[104:105], v[46:47] op_sel:[0,1]
v_pk_mul_f32 v[106:107], v[106:107], v[46:47] op_sel:[0,1]
v_pk_mul_f32 v[108:109], v[108:109], v[46:47] op_sel:[0,1]
v_pk_mul_f32 v[110:111], v[110:111], v[46:47] op_sel:[0,1]
C_ 80,104,105
C_ 81,106,107
C_ 82,108,109
C_ 83,110,111
X_ a84,v80
X_ a85,v81
X_ a86,v82
X_ a87,v83
.p2align 3
R_ v96,a88
R_ v97,a89
R_ v98,a90
R_ v99,a91
R_ v100,a108
R_ v101,a109
R_ v102,a110
R_ v103,a111
v_pk_mul_f32 v[96:97], v[96:97], v[48:49] op_sel_hi:[1,0]
v_pk_mul_f32 v[98:99], v[98:99], v[48:49] op_sel_hi:[1,0]
.Lpad_a0_3:
s_cbranch_execz .Lhop_a0_r
v_pk_mul_f32 v[100:101], v[100:101], v[48:49] op_sel_hi:[1,0]
v_pk_mul_f32 v[102:103], v[102:103], v[48:49] op_sel_hi:[1,0]
C_ 84,96,97
C_ 85,98,99
C_ 86,100,101
C_ 87,102,103
X_ a88,v84
X_ a89,v85
X_ a90,v86
X_ a91,v87
.p2align 3
R_ v104,a92
R_ v105,a93
R_ v106,a94
R_ v107,a95
R_ v108,a112
R_ v109,a113
R_ v110,a114
R_ v111,a115
v_pk_mul_f32 v[104:105], v[104:105], v[48:49] op_sel:[0,1]
v_pk_mul_f32 v[106:107], v[106:107], v[48:49] op_sel:[0,1]
v_pk_mul_f32 v[108:109], v[108:109], v[48:49] op_sel:[0,1]
v_pk_mul_f32 v[110:111], v[110:111], v[48:49] op_sel:[0,1]
C_ 88,104,105
C_ 89,106,107
C_ 90,108,109
C_ 91,110,111
X_ a92,v88
X_ a93,v89
X_ a94,v90
X_ a95,v91
.p2align 3
R_ v96,a96
R_ v97,a97
R_ v98,a98
R_ v99,a99
R_ v100,a116
R_ v101,a117
R_ v102,a118
R_ v103,a119
v_pk_mul_f32 v[96:97], v[96:97], v[50:51] op_sel_hi:[1,0]
v_pk_mul_f32 v[98:99], v[98:99], v[50:51] op_sel_hi:[1,0]
v_pk_mul_f32 v[100:101], v[100:101], v[50:51] op_sel_hi:[1,0]
v_pk_mul_f32 v[102:103], v[102:103], v[50:51] op_sel_hi:[1,0]
C_ 92,96,97
C_ 93,98,99
C_ 94,100,101
C_ 95,102,103
X_ a96,v92
X_ a97,v93
X_ a98,v94
X_ a99,v95
.p2align 3
R_ v104,a120
R_ v105,a121
R_ v106,a122
R_ v107,a123
R_ v108,a140
R_ v109,a141
R_ v110,a142
R_ v111,a143
v_pk_mul_f32 v[104:105], v[104:105], v[46:47] op_sel_hi:[1,0]
v_pk_mul_f32 v[106:107], v[106:107], v[46:47] op_sel_hi:[1,0]
v_pk_mul_f32 v[108:109], v[108:109], v[46:47] op_sel_hi:[1,0]
v_pk_mul_f32 v[110:111], v[110:111], v[46:47] op_sel_hi:[1,0]
C_ 76,104,105
C_ 77,106,107
C_ 78,108,109
C_ 79,110,111
X_ a120,v76
X_ a121,v77
X_ a122,v78
X_ a123,v79
.p2align 3
R_ v96,a124
R_ v97,a125
R_ v98,a126
R_ v99,a127
R_ v100,a144
R_ v101,a145
R_ v102,a146
R_ v103,a147
v_pk_mul_f32 v[96:97], v[96:97], v[46:47] op_sel:[0,1]
v_pk_mul_f32 v[98:99], v[98:99], v[46:47] op_sel:[0,1]
v_pk_mul_f32 v[100:101], v[100:101], v[46:47] op_sel:[0,1]
v_pk_mul_f32 v[102:103], v[102:103], v[46:47] op_sel:[0,1]
C_ 80,96,97
C_ 81,98,99
C_ 82,100,101
C_ 83,102,103
X_ a124,v80
X_ a125,v81
X_ a126,v82
X_ a127,v83
.p2align 3
R_ v104,a128
R_ v105,a129
R_ v106,a130
R_ v107,a131
R_ v108,a148
R_ v109,a149
R_ v110,a150
R_ v111,a151
v_pk_mul_f32 v[104:105], v[104:105], v[48:49] op_sel_hi:[1,0]
v_pk_mul_f32 v[106:107], v[106:107], v[48:49] op_sel_hi:[1,0]
v_pk_mul_f32 v[108:109], v[108:109], v[48:49] op_sel_hi:[1,0]
v_pk_mul_f32 v[110:111], v[110:111], v[48:49] op_sel_hi:[1,0]
C_ 84,104,105
C_ 85,106,107
C_ 86,108,109
C_ 87,110,111
X_ a128,v84
X_ a129,v85
X_ a130,v86
X_ a131,v87
.p2align 3
R_ v96,a132
R_ v97,a133
R_ v98,a134
R_ v99,a135
R_ v100,a152
R_ v101,a153
R_ v102,a154
R_ v103,a155
v_pk_mul_f32 v[96:97], v[96:97], v[48:49] op_sel:[0,1]
v_pk_mul_f32 v[98:99], v[98:99], v[48:49] op_sel:[0,1]
v_pk_mul_f32 v[100:101], v[100:101], v[48:49] op_sel:[0,1]
v_pk_mul_f32 v[102:103], v[102:103], v[48:49] op_sel:[0,1]
C_ 88,96,97
C_ 89,98,99
C_ 90,100,101
C_ 91,102,103
X_ a132,v88
X_ a133,v89
X_ a134,v90
X_ a135,v91
.p2align 3
R_ v104,a136
R_ v105,a137
R_ v106,a138
R_ v107,a139
R_ v108,a156
R_ v109,a157
R_ v110,a158
R_ v111,a159
v_pk_mul_f32 v[104:105], v[104:105], v[50:51] op_sel_hi:[1,0]
v_pk_mul_f32 v[106:107], v[106:107], v[50:51] op_sel_hi:[1,0]
v_pk_mul_f32 v[108:109], v[108:109], v[50:51] op_sel_hi:[1,0]
v_pk_mul_f32 v[110:111], v[110:111], v[50:51] op_sel_hi:[1,0]
C_ 92,104,105
C_ 93,106,107
C_ 94,108,109
C_ 95,110,111
X_ a136,v92
X_ a137,v93
X_ a138,v94
X_ a139,v95
.p2align 3
R_ v96,a160
R_ v97,a161
R_ v98,a162
R_ v99,a163
R_ v100,a180
R_ v101,a181
R_ v102,a182
R_ v103,a183
v_pk_mul_f32 v[96:97], v[96:97], v[46:47] op_sel_hi:[1,0]
v_pk_mul_f32 v[98:99], v[98:99], v[46:47] op_sel_hi:[1,0]
v_pk_mul_f32 v[100:101], v[100:101], v[46:47] op_sel_hi:[1,0]
v_pk_mul_f32 v[102:103], v[102:103], v[46:47] op_sel_hi:[1,0]
C_ 76,96,97
C_ 77,98,99
C_ 78,100,101
C_ 79,102,103
X_ a160,v76
X_ a161,v77
X_ a162,v78
X_ a163,v79
.p2align 3
R_ v104,a164
R_ v105,a165
R_ v106,a166
R_ v107,a167
R_ v108,a184
R_ v109,a185
R_ v110,a186
R_ v111,a187
v_pk_mul_f32 v[104:105], v[104:105], v[46:47] op_sel:[0,1]
v_pk_mul_f32 v[106:107], v[106:107], v[46:47] op_sel:[0,1]
v_pk_mul_f32 v[108:109], v[108:109], v[46:47] op_sel:[0,1]
v_pk_mul_f32 v[110:111], v[110:111], v[46:47] op_sel:[0,1]
C_ 80,104,105
C_ 81,106,107
C_ 82,108,109
C_ 83,110,111
X_ a164,v80
X_ a165,v81
X_ a166,v82
X_ a167,v83
.p2align 3
R_ v96,a168
R_ v97,a169
R_ v98,a170
R_ v99,a171
R_ v100,a188
R_ v101,a189
R_ v102,a190
R_ v103,a191
v_pk_mul_f32 v[96:97], v[96:97], v[48:49] op_sel_hi:[1,0]
v_pk_mul_f32 v[98:99], v[98:99], v[48:49] op_sel_hi:[1,0]
v_pk_mul_f32 v[100:101], v[100:101], v[48:49] op_sel_hi:[1,0]
v_pk_mul_f32 v[102:103], v[102:103], v[48:49] op_sel_hi:[1,0]
C_ 84,96,97
C_ 85,98,99
C_ 86,100,101
C_ 87,102,103
X_ a168,v84
X_ a169,v85
X_ a170,v86
X_ a171,v87
.p2align 3
R_ v104,a172
R_ v105,a173
R_ v106,a174
R_ v107,a175
R_ v108,a192
R_ v109,a193
R_ v110,a194
R_ v111,a195
v_pk_mul_f32 v[104:105], v[104:105], v[48:49] op_sel:[0,1]
v_pk_mul_f32 v[106:107], v[106:107], v[48:49] op_sel:[0,1]
v_pk_mul_f32 v[108:109], v[108:109], v[48:49] op_sel:[0,1]
v_pk_mul_f32 v[110:111], v[110:111], v[48:49] op_sel:[0,1]
C_ 88,104,105
C_ 89,106,107
C_ 90,108,109
C_ 91,110,111
X_ a172,v88
X_ a173,v89
X_ a174,v90
X_ a175,v91
.p2align 3
R_ v96,a176
R_ v97,a177
R_ v98,a178
R_ v99,a179
R_ v100,a196
R_ v101,a197
R_ v102,a198
R_ v103,a199
v_pk_mul_f32 v[96:97], v[96:97], v[50:51] op_sel_hi:[1,0]
v_pk_mul_f32 v[98:99], v[98:99], v[50:51] op_sel_hi:[1,0]
v_pk_mul_f32 v[100:101], v[100:101], v[50:51] op_sel_hi:[1,0]
v_pk_mul_f32 v[102:103], v[102:103], v[50:51] op_sel_hi:[1,0]
C_ 92,96,97
C_ 93,98,99
C_ 94,100,101
C_ 95,102,103
X_ a176,v92
X_ a177,v93
X_ a178,v94
X_ a179,v95
.p2align 3
R_ v104,a200
R_ v105,a201
R_ v106,a202
R_ v107,a203
R_ v108,a220
R_ v109,a221
R_ v110,a222
R_ v111,a223
v_pk_mul_f32 v[104:105], v[104:105], v[46:47] op_sel_hi:[1,0]
v_pk_mul_f32 v[106:107], v[106:107], v[46:47] op_sel_hi:[1,0]
v_pk_mul_f32 v[108:109], v[108:109], v[46:47] op_sel_hi:[1,0]
v_pk_mul_f32 v[110:111], v[110:111], v[46:47] op_sel_hi:[1,0]
C_ 76,104,105
C_ 77,106,107
C_ 78,108,109
C_ 79,110,111
X_ a200,v76
X_ a201,v77
X_ a202,v78
X_ a203,v79
.p2align 3
R_ v96,a204
R_ v97,a205
R_ v98,a206
R_ v99,a207
R_ v100,a224
R_ v101,a225
R_ v102,a226
R_ v103,a227
v_pk_mul_f32 v[96:97], v[96:97], v[46:47] op_sel:[0,1]
v_pk_mul_f32 v[98:99], v[98:99], v[46:47] op_sel:[0,1]
v_pk_mul_f32 v[100:101], v[100:101], v[46:47] op_sel:[0,1]
v_pk_mul_f32 v[102:103], v[102:103], v[46:47] op_sel:[0,1]
C_ 80,96,97
C_ 81,98,99
C_ 82,100,101
C_ 83,102,103
X_ a204,v80
X_ a205,v81
X_ a206,v82
X_ a207,v83
v_mov_b32 v4, v20
v_mov_b32 v5, v21
R_ %0,a242
R_ v2,a243
v_xor_b32 v1, 64, %0
v_xor_b32 v3, 64, v2
s_waitcnt vmcnt(0) lgkmcnt(0)
s_barrier
Ta 20,%0,8192
Ta 22,%0,8704
Ta 24,%0,12288
Ta 26,%0,12800
Da 60,v2,0
Da 100,v3,0
Da 64,v2,2048
Da 104,v3,2048
Da 68,v2,4096
Da 108,v3,4096
Da 72,v2,6144
Da 112,v3,6144
Da 76,v2,8192
Da 116,v3,8192
Ta 28,%0,8200
Ta 30,%0,8712
Ta 32,%0,12296
Ta 34,%0,12808
.p2align 3
R_ v104,a208
R_ v105,a209
R_ v106,a210
R_ v107,a211
R_ v108,a228
R_ v109,a229
R_ v110,a230
R_ v111,a231
v_pk_mul_f32 v[104:105], v[104:105], v[48:49] op_sel_hi:[1,0]
v_pk_mul_f32 v[106:107], v[106:107], v[48:49] op_sel_hi:[1,0]
v_pk_mul_f32 v[108:109], v[108:109], v[48:49] op_sel_hi:[1,0]
v_pk_mul_f32 v[110:111], v[110:111], v[48:49] op_sel_hi:[1,0]
C_ 84,104,105
C_ 85,106,107
C_ 86,108,109
C_ 87,110,111
X_ a208,v84
X_ a209,v85
X_ a210,v86
X_ a211,v87
.p2align 3
R_ v96,a212
R_ v97,a213
R_ v98,a214
R_ v99,a215
R_ v100,a232
R_ v101,a233
R_ v102,a234
R_ v103,a235
v_pk_mul_f32 v[96:97], v[96:97], v[48:49] op_sel:[0,1]
v_pk_mul_f32 v[98:99], v[98:99], v[48:49] op_sel:[0,1]
v_pk_mul_f32 v[100:101], v[100:101], v[48:49] op_sel:[0,1]
v_pk_mul_f32 v[102:103], v[102:103], v[48:49] op_sel:[0,1]
C_ 88,96,97
C_ 89,98,99
C_ 90,100,101
C_ 91,102,103
X_ a212,v88
X_ a213,v89
X_ a214,v90
X_ a215,v91
.p2align 3
R_ v104,a216
R_ v105,a217
R_ v106,a218
R_ v107,a219
R_ v108,a236
R_ v109,a237
R_ v110,a238
R_ v111,a239
v_pk_mul_f32 v[104:105], v[104:105], v[50:51] op_sel_hi:[1,0]
v_pk_mul_f32 v[106:107], v[106:107], v[50:51] op_sel_hi:[1,0]
v_pk_mul_f32 v[108:109], v[108:109], v[50:51] op_sel_hi:[1,0]
v_pk_mul_f32 v[110:111], v[110:111], v[50:51] op_sel_hi:[1,0]
C_ 92,104,105
C_ 93,106,107
C_ 94,108,109
C_ 95,110,111
X_ a216,v92
X_ a217,v93
X_ a218,v94
X_ a219,v95
s_branch .Lq
.Ltramp0_1:
s_mov_b32 s63, 1
s_branch .Lslow1
.Ltramp0_2:
s_mov_b32 s63, 2
s_branch .Lslow0
.Ltramp0_13:
s_mov_b32 s63, 13
s_branch .Lslow1
.Ltramp0_14:
s_mov_b32 s63, 14
s_branch .Lslow0
.Ltramp0_15:
s_mov_b32 s63, 15
s_branch .Lslow1
.Lstep0_16:
Zvvv 76,92,116
Zvvv 80,92,120
Zvvv 84,92,124
Zvvv 88,92,128
Zvvv 92,92,132
s_nop 5
v_max3_f32 v10, v76, v80, v84
s_nop 0
v_max3_f32 v10, v10, v88, v92
v_cmp_lt_f32 vcc, 0x42000000, v10
s_cbranch_vccnz .Ltramp0_15
s_branch .Ljoin0
.Ltramp1_1:
s_mov_b32 s63, 1
s_branch .Lslow1
.Ltramp1_2:
s_mov_b32 s63, 2
s_branch .Lslow0
.Ltramp1_3:
s_mov_b32 s63, 3
s_branch .Lslow1
.Ltramp1_4:
s_mov_b32 s63, 4
s_branch .Lslow0
.Ltramp1_5:
s_mov_b32 s63, 5
s_branch .Lslow1
.Ltramp1_6:
s_mov_b32 s63, 6
s_branch .Lslow0
.Ltramp1_7:
s_mov_b32 s63, 7
s_branch .Lslow1
.Ltramp1_8:
s_mov_b32 s63, 8
s_branch .Lslow0
.Ltramp1_9:
s_mov_b32 s63, 9
s_branch .Lslow1
.Ltramp1_10:
s_mov_b32 s63, 10
s_branch .Lslow0
.Ltramp1_11:
s_mov_b32 s63, 11
s_branch .Lslow1
.Ltramp1_12:
s_mov_b32 s63, 12
s_branch .Lslow0
.Ltramp1_13:
s_mov_b32 s63, 13
s_branch .Lslow1
.Ltramp1_14:
s_mov_b32 s63, 14
s_branch .Lslow0
.Ltramp1_15:
s_mov_b32 s63, 15
s_branch .Lslow1
.Lstep1_16:
Zvvv 76,92,116
Zvvv 80,92,120
Zvvv 84,92,124
Zvvv 88,92,128
Zvvv 92,92,132
s_nop 5
v_max3_f32 v10, v76, v80, v84
s_nop 0
v_max3_f32 v10, v10, v88, v92
v_cmp_lt_f32 vcc, 0x42000000, v10
s_cbranch_vccnz .Ltramp1_15
s_branch .Ljoin1
.Ltramp2_1:
s_mov_b32 s63, 1
s_branch .Lslow1
.Ltramp2_2:
s_mov_b32 s63, 2
s_branch .Lslow0
.Ltramp2_3:
s_mov_b32 s63, 3
s_branch .Lslow1
.Ltramp2_4:
s_mov_b32 s63, 4
s_branch .Lslow0
.Ltramp2_5:
s_mov_b32 s63, 5
s_branch .Lslow1
.Ltramp2_6:
s_mov_b32 s63, 6
s_branch .Lslow0
.Ltramp2_7:
s_mov_b32 s63, 7
s_branch .Lslow1
.Ltramp2_8:
s_mov_b32 s63, 8
s_branch .Lslow0
.Ltramp2_9:
s_mov_b32 s63, 9
s_branch .Lslow1
.Ltramp2_10:
s_mov_b32 s63, 10
s_branch .Lslow0
.Ltramp2_11:
s_mov_b32 s63, 11
s_branch .Lslow1
.Ltramp2_12:
s_mov_b32 s63, 12
s_branch .Lslow0
.Ltramp2_13:
s_mov_b32 s63, 13
s_branch .Lslow1
.Ltramp2_14:
s_mov_b32 s63, 14
s_branch .Lslow0
.Ltramp2_15:
s_mov_b32 s63, 15
s_branch .Lslow1
.Lstep2_16:
Zvvv 76,92,116
Zvvv 80,92,120
Zvvv 84,92,124
Zvvv 88,92,128
Zvvv 92,92,132
s_nop 5
v_max3_f32 v10, v76, v80, v84
s_nop 0
v_max3_f32 v10, v10, v88, v92
v_cmp_lt_f32 vcc, 0x42000000, v10
s_cbranch_vccnz .Ltramp2_15
s_branch .Ljoin2
.Lslow0:
W_ 0
s_nop 7
s_nop 7
s_and_b32 s64, s61, 4294959104
s_mul_i32 s53, s63, 12288
S_ s64,s64,s53
R_ v11,a248
v_and_b32 v10, 0xf0, v11
v_mul_u32_u24 v10, 24, v10
v_lshrrev_b32 v11, 8, v11
v_lshl_add_u32 v10, v11, 4, v10
S_ s53,s64,6144
buffer_load_dwordx4 v[116:119], v10, s[32:35], s53 offen offset:0
S_ s53,s64,6144
buffer_load_dwordx4 v[120:123], v10, s[32:35], s53 offen offset:64
S_ s53,s64,6144
buffer_load_dwordx4 v[124:127], v10, s[32:35], s53 offen offset:128
S_ s53,s64,6144
buffer_load_dwordx4 v[128:131], v10, s[32:35], s53 offen offset:192
s_waitcnt vmcnt(0)
Nvvv 96,116,136,56
Nvvv 100,116,140,60
Nvvv 104,116,144,64
Nvvv 108,116,148,68
Nvvv 112,116,152,72
Mvvv 96,120,156
Mvvv 100,120,160
Mvvv 104,120,164
Mvvv 108,120,168
Mvvv 112,120,172
Mvvv 96,124,176
Mvvv 100,124,180
Mvvv 104,124,184
Mvvv 108,124,188
Mvvv 112,124,192
Mvvv 96,128,196
Mvvv 100,128,200
Mvvv 104,128,204
Mvvv 108,128,208
Mvvv 112,128,212
s_nop 7
S_ s53,s64,6144
buffer_load_dwordx4 v[116:119], v10, s[32:35], s53 offen offset:256
S_ s53,s64,6144
buffer_load_dwordx4 v[120:123], v10, s[32:35], s53 offen offset:320
S_ s53,s64,0
buffer_load_dwordx4 v[124:127], v10, s[32:35], s53 offen offset:0
S_ s53,s64,0
buffer_load_dwordx4 v[128:131], v10, s[32:35], s53 offen offset:64
s_waitcnt vmcnt(0)
Mvvv 96,116,216
Mvvv 100,116,220
Mvvv 104,116,224
Mvvv 108,116,228
Mvvv 112,116,232
Mvvv 96,120,236
Mvvv 100,120,240
Mvvv 104,120,244
Mvvv 108,120,248
Mvvv 112,120,252
Nvvv 76,124,136,56
Nvvv 80,124,140,60
Nvvv 84,124,144,64
Nvvv 88,124,148,68
Nvvv 92,124,152,72
Mvvv 76,128,156
Mvvv 80,128,160
Mvvv 84,128,164
Mvvv 88,128,168
Mvvv 92,128,172
s_nop 7
S_ s53,s64,0
buffer_load_dwordx4 v[116:119], v10, s[32:35], s53 offen offset:128
S_ s53,s64,0
buffer_load_dwordx4 v[120:123], v10, s[32:35], s53 offen offset:192
S_ s53,s64,0
buffer_load_dwordx4 v[124:127], v10, s[32:35], s53 offen offset:256
S_ s53,s64,0
buffer_load_dwordx4 v[128:131], v10, s[32:35], s53 offen offset:320
s_waitcnt vmcnt(0)
Mvvv 76,116,176
Mvvv 80,116,180
Mvvv 84,116,184
Mvvv 88,116,188
Mvvv 92,116,192
Mvvv 76,120,196
Mvvv 80,120,200
Mvvv 84,120,204
Mvvv 88,120,208
Mvvv 92,120,212
Mvvv 76,124,216
Mvvv 80,124,220
Mvvv 84,124,224
Mvvv 88,124,228
Mvvv 92,124,232
Mvvv 76,128,236
Mvvv 80,128,240
Mvvv 84,128,244
Mvvv 88,128,248
Mvvv 92,128,252
s_nop 7
s_nop 2
v_max3_f32 v10, v76, v77, v78
v_max3_f32 v10, v10, v79, v96
v_max3_f32 v10, v10, v97, v98
v_max_f32 v10, v10, v99
v_mov_b32 v11, v10
s_nop 1
v_permlane16_swap_b32 v10, v11
v_max_f32 v10, v10, v11
v_mov_b32 v11, v10
s_nop 1
v_permlane32_swap_b32 v10, v11
v_max_f32 v10, v10, v11
A_ v10,0x40e00000,v10
v_max_f32 v10, 0, v10
v_exp_f32 v11, -v10
s_nop 0
U_ v46,v46,v11
R_ v12,a0
U_ v12,v12,v11
X_ a0,v12
R_ v13,a1
U_ v13,v13,v11
X_ a1,v13
R_ v12,a2
U_ v12,v12,v11
X_ a2,v12
R_ v13,a3
U_ v13,v13,v11
X_ a3,v13
R_ v12,a20
U_ v12,v12,v11
X_ a20,v12
R_ v13,a21
U_ v13,v13,v11
X_ a21,v13
R_ v12,a22
U_ v12,v12,v11
X_ a22,v12
R_ v13,a23
U_ v13,v13,v11
X_ a23,v13
R_ v12,a40
U_ v12,v12,v11
X_ a40,v12
R_ v13,a41
U_ v13,v13,v11
X_ a41,v13
R_ v12,a42
U_ v12,v12,v11
X_ a42,v12
R_ v13,a43
U_ v13,v13,v11
X_ a43,v13
R_ v12,a60
U_ v12,v12,v11
X_ a60,v12
R_ v13,a61
U_ v13,v13,v11
X_ a61,v13
R_ v12,a62
U_ v12,v12,v11
X_ a62,v12
R_ v13,a63
U_ v13,v13,v11
X_ a63,v13
R_ v12,a80
U_ v12,v12,v11
X_ a80,v12
R_ v13,a81
U_ v13,v13,v11
X_ a81,v13
R_ v12,a82
U_ v12,v12,v11
X_ a82,v12
R_ v13,a83
U_ v13,v13,v11
X_ a83,v13
R_ v12,a100
U_ v12,v12,v11
X_ a100,v12
R_ v13,a101
U_ v13,v13,v11
X_ a101,v13
R_ v12,a102
U_ v12,v12,v11
X_ a102,v12
R_ v13,a103
U_ v13,v13,v11
X_ a103,v13
R_ v12,a120
U_ v12,v12,v11
X_ a120,v12
R_ v13,a121
U_ v13,v13,v11
X_ a121,v13
R_ v12,a122
U_ v12,v12,v11
X_ a122,v12
R_ v13,a123
U_ v13,v13,v11
X_ a123,v13
R_ v12,a140
U_ v12,v12,v11
X_ a140,v12
R_ v13,a141
U_ v13,v13,v11
X_ a141,v13
R_ v12,a142
U_ v12,v12,v11
X_ a142,v12
R_ v13,a143
U_ v13,v13,v11
X_ a143,v13
R_ v12,a160
U_ v12,v12,v11
X_ a160,v12
R_ v13,a161
U_ v13,v13,v11
X_ a161,v13
R_ v12,a162
U_ v12,v12,v11
X_ a162,v12
R_ v13,a163
U_ v13,v13,v11
X_ a163,v13
R_ v12,a180
U_ v12,v12,v11
X_ a180,v12
R_ v13,a181
U_ v13,v13,v11
X_ a181,v13
R_ v12,a182
U_ v12,v12,v11
X_ a182,v12
R_ v13,a183
U_ v13,v13,v11
X_ a183,v13
R_ v12,a200
U_ v12,v12,v11
X_ a200,v12
R_ v13,a201
U_ v13,v13,v11
X_ a201,v13
R_ v12,a202
U_ v12,v12,v11
X_ a202,v12
R_ v13,a203
U_ v13,v13,v11
X_ a203,v13
R_ v12,a220
U_ v12,v12,v11
X_ a220,v12
R_ v13,a221
U_ v13,v13,v11
X_ a221,v13
R_ v12,a222
U_ v12,v12,v11
X_ a222,v12
R_ v13,a223
U_ v13,v13,v11
X_ a223,v13
B_ v56,v56,v10
B_ v57,v57,v10
B_ v58,v58,v10
B_ v59,v59,v10
B_ v76,v76,v10
B_ v77,v77,v10
B_ v78,v78,v10
B_ v79,v79,v10
B_ v96,v96,v10
B_ v97,v97,v10
B_ v98,v98,v10
B_ v99,v99,v10
E_ 76
E_ 77
E_ 78
E_ 79
E_ 96
E_ 97
E_ 98
E_ 99
s_nop 0
C_ 99,98,99
C_ 98,96,97
C_ 96,76,77
C_ 97,78,79
v_max3_f32 v10, v80, v81, v82
v_max3_f32 v10, v10, v83, v100
v_max3_f32 v10, v10, v101, v102
v_max_f32 v10, v10, v103
v_mov_b32 v11, v10
s_nop 1
v_permlane16_swap_b32 v10, v11
v_max_f32 v10, v10, v11
v_mov_b32 v11, v10
s_nop 1
v_permlane32_swap_b32 v10, v11
v_max_f32 v10, v10, v11
A_ v10,0x40e00000,v10
v_max_f32 v10, 0, v10
v_exp_f32 v11, -v10
s_nop 0
U_ v47,v47,v11
R_ v12,a4
U_ v12,v12,v11
X_ a4,v12
R_ v13,a5
U_ v13,v13,v11
X_ a5,v13
R_ v12,a6
U_ v12,v12,v11
X_ a6,v12
R_ v13,a7
U_ v13,v13,v11
X_ a7,v13
R_ v12,a24
U_ v12,v12,v11
X_ a24,v12
R_ v13,a25
U_ v13,v13,v11
X_ a25,v13
R_ v12,a26
U_ v12,v12,v11
X_ a26,v12
R_ v13,a27
U_ v13,v13,v11
X_ a27,v13
R_ v12,a44
U_ v12,v12,v11
X_ a44,v12
R_ v13,a45
U_ v13,v13,v11
X_ a45,v13
R_ v12,a46
U_ v12,v12,v11
X_ a46,v12
R_ v13,a47
U_ v13,v13,v11
X_ a47,v13
R_ v12,a64
U_ v12,v12,v11
X_ a64,v12
R_ v13,a65
U_ v13,v13,v11
X_ a65,v13
R_ v12,a66
U_ v12,v12,v11
X_ a66,v12
R_ v13,a67
U_ v13,v13,v11
X_ a67,v13
R_ v12,a84
U_ v12,v12,v11
X_ a84,v12
R_ v13,a85
U_ v13,v13,v11
X_ a85,v13
R_ v12,a86
U_ v12,v12,v11
X_ a86,v12
R_ v13,a87
U_ v13,v13,v11
X_ a87,v13
R_ v12,a104
U_ v12,v12,v11
X_ a104,v12
R_ v13,a105
U_ v13,v13,v11
X_ a105,v13
R_ v12,a106
U_ v12,v12,v11
X_ a106,v12
R_ v13,a107
U_ v13,v13,v11
X_ a107,v13
R_ v12,a124
U_ v12,v12,v11
X_ a124,v12
R_ v13,a125
U_ v13,v13,v11
X_ a125,v13
R_ v12,a126
U_ v12,v12,v11
X_ a126,v12
R_ v13,a127
U_ v13,v13,v11
X_ a127,v13
R_ v12,a144
U_ v12,v12,v11
X_ a144,v12
R_ v13,a145
U_ v13,v13,v11
X_ a145,v13
R_ v12,a146
U_ v12,v12,v11
X_ a146,v12
R_ v13,a147
U_ v13,v13,v11
X_ a147,v13
R_ v12,a164
U_ v12,v12,v11
X_ a164,v12
R_ v13,a165
U_ v13,v13,v11
X_ a165,v13
R_ v12,a166
U_ v12,v12,v11
X_ a166,v12
R_ v13,a167
U_ v13,v13,v11
X_ a167,v13
R_ v12,a184
U_ v12,v12,v11
X_ a184,v12
R_ v13,a185
U_ v13,v13,v11
X_ a185,v13
R_ v12,a186
U_ v12,v12,v11
X_ a186,v12
R_ v13,a187
U_ v13,v13,v11
X_ a187,v13
R_ v12,a204
U_ v12,v12,v11
X_ a204,v12
R_ v13,a205
U_ v13,v13,v11
X_ a205,v13
R_ v12,a206
U_ v12,v12,v11
X_ a206,v12
R_ v13,a207
U_ v13,v13,v11
X_ a207,v13
R_ v12,a224
U_ v12,v12,v11
X_ a224,v12
R_ v13,a225
U_ v13,v13,v11
X_ a225,v13
R_ v12,a226
U_ v12,v12,v11
X_ a226,v12
R_ v13,a227
U_ v13,v13,v11
X_ a227,v13
B_ v60,v60,v10
B_ v61,v61,v10
B_ v62,v62,v10
B_ v63,v63,v10
B_ v80,v80,v10
B_ v81,v81,v10
B_ v82,v82,v10
B_ v83,v83,v10
B_ v100,v100,v10
B_ v101,v101,v10
B_ v102,v102,v10
B_ v103,v103,v10
E_ 80
E_ 81
E_ 82
E_ 83
E_ 100
E_ 101
E_ 102
E_ 103
s_nop 0
C_ 103,102,103
C_ 102,100,101
C_ 100,80,81
C_ 101,82,83
v_max3_f32 v10, v84, v85, v86
v_max3_f32 v10, v10, v87, v104
v_max3_f32 v10, v10, v105, v106
v_max_f32 v10, v10, v107
v_mov_b32 v11, v10
s_nop 1
v_permlane16_swap_b32 v10, v11
v_max_f32 v10, v10, v11
v_mov_b32 v11, v10
s_nop 1
v_permlane32_swap_b32 v10, v11
v_max_f32 v10, v10, v11
A_ v10,0x40e00000,v10
v_max_f32 v10, 0, v10
v_exp_f32 v11, -v10
s_nop 0
U_ v48,v48,v11
R_ v12,a8
U_ v12,v12,v11
X_ a8,v12
R_ v13,a9
U_ v13,v13,v11
X_ a9,v13
R_ v12,a10
U_ v12,v12,v11
X_ a10,v12
R_ v13,a11
U_ v13,v13,v11
X_ a11,v13
R_ v12,a28
U_ v12,v12,v11
X_ a28,v12
R_ v13,a29
U_ v13,v13,v11
X_ a29,v13
R_ v12,a30
U_ v12,v12,v11
X_ a30,v12
R_ v13,a31
U_ v13,v13,v11
X_ a31,v13
R_ v12,a48
U_ v12,v12,v11
X_ a48,v12
R_ v13,a49
U_ v13,v13,v11
X_ a49,v13
R_ v12,a50
U_ v12,v12,v11
X_ a50,v12
R_ v13,a51
U_ v13,v13,v11
X_ a51,v13
R_ v12,a68
U_ v12,v12,v11
X_ a68,v12
R_ v13,a69
U_ v13,v13,v11
X_ a69,v13
R_ v12,a70
U_ v12,v12,v11
X_ a70,v12
R_ v13,a71
U_ v13,v13,v11
X_ a71,v13
R_ v12,a88
U_ v12,v12,v11
X_ a88,v12
R_ v13,a89
U_ v13,v13,v11
X_ a89,v13
R_ v12,a90
U_ v12,v12,v11
X_ a90,v12
R_ v13,a91
U_ v13,v13,v11
X_ a91,v13
R_ v12,a108
U_ v12,v12,v11
X_ a108,v12
R_ v13,a109
U_ v13,v13,v11
X_ a109,v13
R_ v12,a110
U_ v12,v12,v11
X_ a110,v12
R_ v13,a111
U_ v13,v13,v11
X_ a111,v13
R_ v12,a128
U_ v12,v12,v11
X_ a128,v12
R_ v13,a129
U_ v13,v13,v11
X_ a129,v13
R_ v12,a130
U_ v12,v12,v11
X_ a130,v12
R_ v13,a131
U_ v13,v13,v11
X_ a131,v13
R_ v12,a148
U_ v12,v12,v11
X_ a148,v12
R_ v13,a149
U_ v13,v13,v11
X_ a149,v13
R_ v12,a150
U_ v12,v12,v11
X_ a150,v12
R_ v13,a151
U_ v13,v13,v11
X_ a151,v13
R_ v12,a168
U_ v12,v12,v11
X_ a168,v12
R_ v13,a169
U_ v13,v13,v11
X_ a169,v13
R_ v12,a170
U_ v12,v12,v11
X_ a170,v12
R_ v13,a171
U_ v13,v13,v11
X_ a171,v13
R_ v12,a188
U_ v12,v12,v11
X_ a188,v12
R_ v13,a189
U_ v13,v13,v11
X_ a189,v13
R_ v12,a190
U_ v12,v12,v11
X_ a190,v12
R_ v13,a191
U_ v13,v13,v11
X_ a191,v13
R_ v12,a208
U_ v12,v12,v11
X_ a208,v12
R_ v13,a209
U_ v13,v13,v11
X_ a209,v13
R_ v12,a210
U_ v12,v12,v11
X_ a210,v12
R_ v13,a211
U_ v13,v13,v11
X_ a211,v13
R_ v12,a228
U_ v12,v12,v11
X_ a228,v12
R_ v13,a229
U_ v13,v13,v11
X_ a229,v13
R_ v12,a230
U_ v12,v12,v11
X_ a230,v12
R_ v13,a231
U_ v13,v13,v11
X_ a231,v13
B_ v64,v64,v10
B_ v65,v65,v10
B_ v66,v66,v10
B_ v67,v67,v10
B_ v84,v84,v10
B_ v85,v85,v10
B_ v86,v86,v10
B_ v87,v87,v10
B_ v104,v104,v10
B_ v105,v105,v10
B_ v106,v106,v10
B_ v107,v107,v10
E_ 84
E_ 85
E_ 86
E_ 87
E_ 104
E_ 105
E_ 106
E_ 107
s_nop 0
C_ 107,106,107
C_ 106,104,105
C_ 104,84,85
C_ 105,86,87
v_max3_f32 v10, v88, v89, v90
v_max3_f32 v10, v10, v91, v108
v_max3_f32 v10, v10, v109, v110
v_max_f32 v10, v10, v111
v_mov_b32 v11, v10
s_nop 1
v_permlane16_swap_b32 v10, v11
v_max_f32 v10, v10, v11
v_mov_b32 v11, v10
s_nop 1
v_permlane32_swap_b32 v10, v11
v_max_f32 v10, v10, v11
A_ v10,0x40e00000,v10
v_max_f32 v10, 0, v10
v_exp_f32 v11, -v10
s_nop 0
U_ v49,v49,v11
R_ v12,a12
U_ v12,v12,v11
X_ a12,v12
R_ v13,a13
U_ v13,v13,v11
X_ a13,v13
R_ v12,a14
U_ v12,v12,v11
X_ a14,v12
R_ v13,a15
U_ v13,v13,v11
X_ a15,v13
R_ v12,a32
)A"
R"A(U_ v12,v12,v11
X_ a32,v12
R_ v13,a33
U_ v13,v13,v11
X_ a33,v13
R_ v12,a34
U_ v12,v12,v11
X_ a34,v12
R_ v13,a35
U_ v13,v13,v11
X_ a35,v13
R_ v12,a52
U_ v12,v12,v11
X_ a52,v12
R_ v13,a53
U_ v13,v13,v11
X_ a53,v13
R_ v12,a54
U_ v12,v12,v11
X_ a54,v12
R_ v13,a55
U_ v13,v13,v11
X_ a55,v13
R_ v12,a72
U_ v12,v12,v11
X_ a72,v12
R_ v13,a73
U_ v13,v13,v11
X_ a73,v13
R_ v12,a74
U_ v12,v12,v11
X_ a74,v12
R_ v13,a75
U_ v13,v13,v11
X_ a75,v13
R_ v12,a92
U_ v12,v12,v11
X_ a92,v12
R_ v13,a93
U_ v13,v13,v11
X_ a93,v13
R_ v12,a94
U_ v12,v12,v11
X_ a94,v12
R_ v13,a95
U_ v13,v13,v11
X_ a95,v13
R_ v12,a112
U_ v12,v12,v11
X_ a112,v12
R_ v13,a113
U_ v13,v13,v11
X_ a113,v13
R_ v12,a114
U_ v12,v12,v11
X_ a114,v12
R_ v13,a115
U_ v13,v13,v11
X_ a115,v13
R_ v12,a132
U_ v12,v12,v11
X_ a132,v12
R_ v13,a133
U_ v13,v13,v11
X_ a133,v13
R_ v12,a134
U_ v12,v12,v11
X_ a134,v12
R_ v13,a135
U_ v13,v13,v11
X_ a135,v13
R_ v12,a152
U_ v12,v12,v11
X_ a152,v12
R_ v13,a153
U_ v13,v13,v11
X_ a153,v13
R_ v12,a154
U_ v12,v12,v11
X_ a154,v12
R_ v13,a155
U_ v13,v13,v11
X_ a155,v13
R_ v12,a172
U_ v12,v12,v11
X_ a172,v12
R_ v13,a173
U_ v13,v13,v11
X_ a173,v13
R_ v12,a174
U_ v12,v12,v11
X_ a174,v12
R_ v13,a175
U_ v13,v13,v11
X_ a175,v13
R_ v12,a192
U_ v12,v12,v11
X_ a192,v12
R_ v13,a193
U_ v13,v13,v11
X_ a193,v13
R_ v12,a194
U_ v12,v12,v11
X_ a194,v12
R_ v13,a195
U_ v13,v13,v11
X_ a195,v13
R_ v12,a212
U_ v12,v12,v11
X_ a212,v12
R_ v13,a213
U_ v13,v13,v11
X_ a213,v13
R_ v12,a214
U_ v12,v12,v11
X_ a214,v12
R_ v13,a215
U_ v13,v13,v11
X_ a215,v13
R_ v12,a232
U_ v12,v12,v11
X_ a232,v12
R_ v13,a233
U_ v13,v13,v11
X_ a233,v13
R_ v12,a234
U_ v12,v12,v11
X_ a234,v12
R_ v13,a235
U_ v13,v13,v11
X_ a235,v13
B_ v68,v68,v10
B_ v69,v69,v10
B_ v70,v70,v10
B_ v71,v71,v10
B_ v88,v88,v10
B_ v89,v89,v10
B_ v90,v90,v10
B_ v91,v91,v10
B_ v108,v108,v10
B_ v109,v109,v10
B_ v110,v110,v10
B_ v111,v111,v10
E_ 88
E_ 89
E_ 90
E_ 91
E_ 108
E_ 109
E_ 110
E_ 111
s_nop 0
C_ 111,110,111
C_ 110,108,109
C_ 108,88,89
C_ 109,90,91
v_max3_f32 v10, v92, v93, v94
v_max3_f32 v10, v10, v95, v112
v_max3_f32 v10, v10, v113, v114
v_max_f32 v10, v10, v115
v_mov_b32 v11, v10
s_nop 1
v_permlane16_swap_b32 v10, v11
v_max_f32 v10, v10, v11
v_mov_b32 v11, v10
s_nop 1
v_permlane32_swap_b32 v10, v11
v_max_f32 v10, v10, v11
A_ v10,0x40e00000,v10
v_max_f32 v10, 0, v10
v_exp_f32 v11, -v10
s_nop 0
U_ v50,v50,v11
R_ v12,a16
U_ v12,v12,v11
X_ a16,v12
R_ v13,a17
U_ v13,v13,v11
X_ a17,v13
R_ v12,a18
U_ v12,v12,v11
X_ a18,v12
R_ v13,a19
U_ v13,v13,v11
X_ a19,v13
R_ v12,a36
U_ v12,v12,v11
X_ a36,v12
R_ v13,a37
U_ v13,v13,v11
X_ a37,v13
R_ v12,a38
U_ v12,v12,v11
X_ a38,v12
R_ v13,a39
U_ v13,v13,v11
X_ a39,v13
R_ v12,a56
U_ v12,v12,v11
X_ a56,v12
R_ v13,a57
U_ v13,v13,v11
X_ a57,v13
R_ v12,a58
U_ v12,v12,v11
X_ a58,v12
R_ v13,a59
U_ v13,v13,v11
X_ a59,v13
R_ v12,a76
U_ v12,v12,v11
X_ a76,v12
R_ v13,a77
U_ v13,v13,v11
X_ a77,v13
R_ v12,a78
U_ v12,v12,v11
X_ a78,v12
R_ v13,a79
U_ v13,v13,v11
X_ a79,v13
R_ v12,a96
U_ v12,v12,v11
X_ a96,v12
R_ v13,a97
U_ v13,v13,v11
X_ a97,v13
R_ v12,a98
U_ v12,v12,v11
X_ a98,v12
R_ v13,a99
U_ v13,v13,v11
X_ a99,v13
R_ v12,a116
U_ v12,v12,v11
X_ a116,v12
R_ v13,a117
U_ v13,v13,v11
X_ a117,v13
R_ v12,a118
U_ v12,v12,v11
X_ a118,v12
R_ v13,a119
U_ v13,v13,v11
X_ a119,v13
R_ v12,a136
U_ v12,v12,v11
X_ a136,v12
R_ v13,a137
U_ v13,v13,v11
X_ a137,v13
R_ v12,a138
U_ v12,v12,v11
X_ a138,v12
R_ v13,a139
U_ v13,v13,v11
X_ a139,v13
R_ v12,a156
U_ v12,v12,v11
X_ a156,v12
R_ v13,a157
U_ v13,v13,v11
X_ a157,v13
R_ v12,a158
U_ v12,v12,v11
X_ a158,v12
R_ v13,a159
U_ v13,v13,v11
X_ a159,v13
R_ v12,a176
U_ v12,v12,v11
X_ a176,v12
R_ v13,a177
U_ v13,v13,v11
X_ a177,v13
R_ v12,a178
U_ v12,v12,v11
X_ a178,v12
R_ v13,a179
U_ v13,v13,v11
X_ a179,v13
R_ v12,a196
U_ v12,v12,v11
X_ a196,v12
R_ v13,a197
U_ v13,v13,v11
X_ a197,v13
R_ v12,a198
U_ v12,v12,v11
X_ a198,v12
R_ v13,a199
U_ v13,v13,v11
X_ a199,v13
R_ v12,a216
U_ v12,v12,v11
X_ a216,v12
R_ v13,a217
U_ v13,v13,v11
X_ a217,v13
R_ v12,a218
U_ v12,v12,v11
X_ a218,v12
R_ v13,a219
U_ v13,v13,v11
X_ a219,v13
R_ v12,a236
U_ v12,v12,v11
X_ a236,v12
R_ v13,a237
U_ v13,v13,v11
X_ a237,v13
R_ v12,a238
U_ v12,v12,v11
X_ a238,v12
R_ v13,a239
U_ v13,v13,v11
X_ a239,v13
B_ v72,v72,v10
B_ v73,v73,v10
B_ v74,v74,v10
B_ v75,v75,v10
B_ v92,v92,v10
B_ v93,v93,v10
B_ v94,v94,v10
B_ v95,v95,v10
B_ v112,v112,v10
B_ v113,v113,v10
B_ v114,v114,v10
B_ v115,v115,v10
E_ 92
E_ 93
E_ 94
E_ 95
E_ 112
E_ 113
E_ 114
E_ 115
s_nop 0
C_ 115,114,115
C_ 114,112,113
C_ 112,92,93
C_ 113,94,95
v_mov_b32 v92, 0x3f803f80
v_mov_b32 v93, 0x3f803f80
v_mov_b32 v94, 0x3f803f80
v_mov_b32 v95, 0x3f803f80
s_nop 7
s_lshl_b32 s53, s58, 4
S_ s53,s53,s63
s_cmp_eq_u32 s53, 2
s_cbranch_scc1 .Lstep0_3
s_cmp_eq_u32 s53, 14
s_cbranch_scc1 .Lstep0_15
s_cmp_eq_u32 s53, 18
s_cbranch_scc1 .Lstep1_3
s_cmp_eq_u32 s53, 20
s_cbranch_scc1 .Lstep1_5
s_cmp_eq_u32 s53, 22
s_cbranch_scc1 .Lstep1_7
s_cmp_eq_u32 s53, 24
s_cbranch_scc1 .Lstep1_9
s_cmp_eq_u32 s53, 26
s_cbranch_scc1 .Lstep1_11
s_cmp_eq_u32 s53, 28
s_cbranch_scc1 .Lstep1_13
s_cmp_eq_u32 s53, 30
s_cbranch_scc1 .Lstep1_15
s_cmp_eq_u32 s53, 34
s_cbranch_scc1 .Lstep2_3
s_cmp_eq_u32 s53, 36
s_cbranch_scc1 .Lstep2_5
s_cmp_eq_u32 s53, 38
s_cbranch_scc1 .Lstep2_7
s_cmp_eq_u32 s53, 40
s_cbranch_scc1 .Lstep2_9
s_cmp_eq_u32 s53, 42
s_cbranch_scc1 .Lstep2_11
s_cmp_eq_u32 s53, 44
s_cbranch_scc1 .Lstep2_13
s_cmp_eq_u32 s53, 46
s_cbranch_scc1 .Lstep2_15
s_endpgm
.Lslow1:
W_ 0
s_nop 7
s_nop 7
s_and_b32 s64, s61, 4294959104
s_mul_i32 s53, s63, 12288
S_ s64,s64,s53
R_ v11,a248
v_and_b32 v10, 0xf0, v11
v_mul_u32_u24 v10, 24, v10
v_lshrrev_b32 v11, 8, v11
v_lshl_add_u32 v10, v11, 4, v10
S_ s53,s64,6144
buffer_load_dwordx4 v[96:99], v10, s[32:35], s53 offen offset:0
S_ s53,s64,6144
buffer_load_dwordx4 v[100:103], v10, s[32:35], s53 offen offset:64
S_ s53,s64,6144
buffer_load_dwordx4 v[104:107], v10, s[32:35], s53 offen offset:128
S_ s53,s64,6144
buffer_load_dwordx4 v[108:111], v10, s[32:35], s53 offen offset:192
s_waitcnt vmcnt(0)
Nvvv 116,96,136,56
Nvvv 120,96,140,60
Nvvv 124,96,144,64
Nvvv 128,96,148,68
Nvvv 132,96,152,72
Mvvv 116,100,156
Mvvv 120,100,160
Mvvv 124,100,164
Mvvv 128,100,168
Mvvv 132,100,172
Mvvv 116,104,176
Mvvv 120,104,180
Mvvv 124,104,184
Mvvv 128,104,188
Mvvv 132,104,192
Mvvv 116,108,196
Mvvv 120,108,200
Mvvv 124,108,204
Mvvv 128,108,208
Mvvv 132,108,212
s_nop 7
S_ s53,s64,6144
buffer_load_dwordx4 v[96:99], v10, s[32:35], s53 offen offset:256
S_ s53,s64,6144
buffer_load_dwordx4 v[100:103], v10, s[32:35], s53 offen offset:320
S_ s53,s64,0
buffer_load_dwordx4 v[104:107], v10, s[32:35], s53 offen offset:0
S_ s53,s64,0
buffer_load_dwordx4 v[108:111], v10, s[32:35], s53 offen offset:64
s_waitcnt vmcnt(0)
Mvvv 116,96,216
Mvvv 120,96,220
Mvvv 124,96,224
Mvvv 128,96,228
Mvvv 132,96,232
Mvvv 116,100,236
Mvvv 120,100,240
Mvvv 124,100,244
Mvvv 128,100,248
Mvvv 132,100,252
Nvvv 76,104,136,56
Nvvv 80,104,140,60
Nvvv 84,104,144,64
Nvvv 88,104,148,68
Nvvv 92,104,152,72
Mvvv 76,108,156
Mvvv 80,108,160
Mvvv 84,108,164
Mvvv 88,108,168
Mvvv 92,108,172
s_nop 7
S_ s53,s64,0
buffer_load_dwordx4 v[96:99], v10, s[32:35], s53 offen offset:128
S_ s53,s64,0
buffer_load_dwordx4 v[100:103], v10, s[32:35], s53 offen offset:192
S_ s53,s64,0
buffer_load_dwordx4 v[104:107], v10, s[32:35], s53 offen offset:256
S_ s53,s64,0
buffer_load_dwordx4 v[108:111], v10, s[32:35], s53 offen offset:320
s_waitcnt vmcnt(0)
Mvvv 76,96,176
Mvvv 80,96,180
Mvvv 84,96,184
Mvvv 88,96,188
Mvvv 92,96,192
Mvvv 76,100,196
Mvvv 80,100,200
Mvvv 84,100,204
Mvvv 88,100,208
Mvvv 92,100,212
Mvvv 76,104,216
Mvvv 80,104,220
Mvvv 84,104,224
Mvvv 88,104,228
Mvvv 92,104,232
Mvvv 76,108,236
Mvvv 80,108,240
Mvvv 84,108,244
Mvvv 88,108,248
Mvvv 92,108,252
s_nop 7
s_nop 2
v_max3_f32 v10, v76, v77, v78
v_max3_f32 v10, v10, v79, v116
v_max3_f32 v10, v10, v117, v118
v_max_f32 v10, v10, v119
v_mov_b32 v11, v10
s_nop 1
v_permlane16_swap_b32 v10, v11
v_max_f32 v10, v10, v11
v_mov_b32 v11, v10
s_nop 1
v_permlane32_swap_b32 v10, v11
v_max_f32 v10, v10, v11
A_ v10,0x40e00000,v10
v_max_f32 v10, 0, v10
v_exp_f32 v11, -v10
s_nop 0
U_ v46,v46,v11
R_ v12,a0
U_ v12,v12,v11
X_ a0,v12
R_ v13,a1
U_ v13,v13,v11
X_ a1,v13
R_ v12,a2
U_ v12,v12,v11
X_ a2,v12
R_ v13,a3
U_ v13,v13,v11
X_ a3,v13
R_ v12,a20
U_ v12,v12,v11
X_ a20,v12
R_ v13,a21
U_ v13,v13,v11
X_ a21,v13
R_ v12,a22
U_ v12,v12,v11
X_ a22,v12
R_ v13,a23
U_ v13,v13,v11
X_ a23,v13
R_ v12,a40
U_ v12,v12,v11
X_ a40,v12
R_ v13,a41
U_ v13,v13,v11
X_ a41,v13
R_ v12,a42
U_ v12,v12,v11
X_ a42,v12
R_ v13,a43
U_ v13,v13,v11
X_ a43,v13
R_ v12,a60
U_ v12,v12,v11
X_ a60,v12
R_ v13,a61
U_ v13,v13,v11
X_ a61,v13
R_ v12,a62
U_ v12,v12,v11
X_ a62,v12
R_ v13,a63
U_ v13,v13,v11
X_ a63,v13
R_ v12,a80
U_ v12,v12,v11
X_ a80,v12
R_ v13,a81
U_ v13,v13,v11
X_ a81,v13
R_ v12,a82
U_ v12,v12,v11
X_ a82,v12
R_ v13,a83
U_ v13,v13,v11
X_ a83,v13
R_ v12,a100
U_ v12,v12,v11
X_ a100,v12
R_ v13,a101
U_ v13,v13,v11
X_ a101,v13
R_ v12,a102
U_ v12,v12,v11
X_ a102,v12
R_ v13,a103
U_ v13,v13,v11
X_ a103,v13
R_ v12,a120
U_ v12,v12,v11
X_ a120,v12
R_ v13,a121
U_ v13,v13,v11
X_ a121,v13
R_ v12,a122
U_ v12,v12,v11
X_ a122,v12
R_ v13,a123
U_ v13,v13,v11
X_ a123,v13
R_ v12,a140
U_ v12,v12,v11
X_ a140,v12
R_ v13,a141
U_ v13,v13,v11
X_ a141,v13
R_ v12,a142
U_ v12,v12,v11
X_ a142,v12
R_ v13,a143
U_ v13,v13,v11
X_ a143,v13
R_ v12,a160
U_ v12,v12,v11
X_ a160,v12
R_ v13,a161
U_ v13,v13,v11
X_ a161,v13
R_ v12,a162
U_ v12,v12,v11
X_ a162,v12
R_ v13,a163
U_ v13,v13,v11
X_ a163,v13
R_ v12,a180
U_ v12,v12,v11
X_ a180,v12
R_ v13,a181
U_ v13,v13,v11
X_ a181,v13
R_ v12,a182
U_ v12,v12,v11
X_ a182,v12
R_ v13,a183
U_ v13,v13,v11
X_ a183,v13
R_ v12,a200
U_ v12,v12,v11
X_ a200,v12
R_ v13,a201
U_ v13,v13,v11
X_ a201,v13
R_ v12,a202
U_ v12,v12,v11
X_ a202,v12
R_ v13,a203
U_ v13,v13,v11
X_ a203,v13
R_ v12,a220
U_ v12,v12,v11
X_ a220,v12
R_ v13,a221
U_ v13,v13,v11
X_ a221,v13
R_ v12,a222
U_ v12,v12,v11
X_ a222,v12
R_ v13,a223
U_ v13,v13,v11
X_ a223,v13
B_ v56,v56,v10
B_ v57,v57,v10
B_ v58,v58,v10
B_ v59,v59,v10
B_ v76,v76,v10
B_ v77,v77,v10
B_ v78,v78,v10
B_ v79,v79,v10
B_ v116,v116,v10
B_ v117,v117,v10
B_ v118,v118,v10
B_ v119,v119,v10
E_ 76
E_ 77
E_ 78
E_ 79
E_ 116
E_ 117
E_ 118
E_ 119
s_nop 0
C_ 119,118,119
C_ 118,116,117
C_ 116,76,77
C_ 117,78,79
v_max3_f32 v10, v80, v81, v82
v_max3_f32 v10, v10, v83, v120
v_max3_f32 v10, v10, v121, v122
v_max_f32 v10, v10, v123
v_mov_b32 v11, v10
s_nop 1
v_permlane16_swap_b32 v10, v11
v_max_f32 v10, v10, v11
v_mov_b32 v11, v10
s_nop 1
v_permlane32_swap_b32 v10, v11
v_max_f32 v10, v10, v11
A_ v10,0x40e00000,v10
v_max_f32 v10, 0, v10
v_exp_f32 v11, -v10
s_nop 0
U_ v47,v47,v11
R_ v12,a4
U_ v12,v12,v11
X_ a4,v12
R_ v13,a5
U_ v13,v13,v11
X_ a5,v13
R_ v12,a6
U_ v12,v12,v11
X_ a6,v12
R_ v13,a7
U_ v13,v13,v11
X_ a7,v13
R_ v12,a24
U_ v12,v12,v11
X_ a24,v12
R_ v13,a25
U_ v13,v13,v11
X_ a25,v13
R_ v12,a26
U_ v12,v12,v11
X_ a26,v12
R_ v13,a27
U_ v13,v13,v11
X_ a27,v13
R_ v12,a44
U_ v12,v12,v11
X_ a44,v12
R_ v13,a45
U_ v13,v13,v11
X_ a45,v13
R_ v12,a46
U_ v12,v12,v11
X_ a46,v12
R_ v13,a47
U_ v13,v13,v11
X_ a47,v13
R_ v12,a64
U_ v12,v12,v11
X_ a64,v12
R_ v13,a65
U_ v13,v13,v11
X_ a65,v13
R_ v12,a66
U_ v12,v12,v11
X_ a66,v12
R_ v13,a67
U_ v13,v13,v11
X_ a67,v13
R_ v12,a84
U_ v12,v12,v11
X_ a84,v12
R_ v13,a85
U_ v13,v13,v11
X_ a85,v13
R_ v12,a86
U_ v12,v12,v11
X_ a86,v12
R_ v13,a87
U_ v13,v13,v11
X_ a87,v13
R_ v12,a104
U_ v12,v12,v11
X_ a104,v12
R_ v13,a105
U_ v13,v13,v11
X_ a105,v13
R_ v12,a106
U_ v12,v12,v11
X_ a106,v12
R_ v13,a107
U_ v13,v13,v11
X_ a107,v13
R_ v12,a124
U_ v12,v12,v11
X_ a124,v12
R_ v13,a125
U_ v13,v13,v11
X_ a125,v13
R_ v12,a126
U_ v12,v12,v11
X_ a126,v12
R_ v13,a127
U_ v13,v13,v11
X_ a127,v13
R_ v12,a144
U_ v12,v12,v11
X_ a144,v12
R_ v13,a145
U_ v13,v13,v11
X_ a145,v13
R_ v12,a146
U_ v12,v12,v11
X_ a146,v12
R_ v13,a147
U_ v13,v13,v11
X_ a147,v13
R_ v12,a164
U_ v12,v12,v11
X_ a164,v12
R_ v13,a165
U_ v13,v13,v11
X_ a165,v13
R_ v12,a166
U_ v12,v12,v11
X_ a166,v12
R_ v13,a167
U_ v13,v13,v11
X_ a167,v13
R_ v12,a184
U_ v12,v12,v11
X_ a184,v12
R_ v13,a185
U_ v13,v13,v11
X_ a185,v13
R_ v12,a186
U_ v12,v12,v11
X_ a186,v12
R_ v13,a187
U_ v13,v13,v11
X_ a187,v13
R_ v12,a204
U_ v12,v12,v11
X_ a204,v12
R_ v13,a205
U_ v13,v13,v11
X_ a205,v13
R_ v12,a206
U_ v12,v12,v11
X_ a206,v12
R_ v13,a207
U_ v13,v13,v11
X_ a207,v13
R_ v12,a224
U_ v12,v12,v11
X_ a224,v12
R_ v13,a225
U_ v13,v13,v11
X_ a225,v13
R_ v12,a226
U_ v12,v12,v11
X_ a226,v12
R_ v13,a227
U_ v13,v13,v11
X_ a227,v13
B_ v60,v60,v10
B_ v61,v61,v10
B_ v62,v62,v10
B_ v63,v63,v10
B_ v80,v80,v10
B_ v81,v81,v10
B_ v82,v82,v10
B_ v83,v83,v10
B_ v120,v120,v10
B_ v121,v121,v10
B_ v122,v122,v10
B_ v123,v123,v10
E_ 80
E_ 81
E_ 82
E_ 83
E_ 120
E_ 121
E_ 122
E_ 123
s_nop 0
C_ 123,122,123
C_ 122,120,121
C_ 120,80,81
C_ 121,82,83
v_max3_f32 v10, v84, v85, v86
v_max3_f32 v10, v10, v87, v124
v_max3_f32 v10, v10, v125, v126
v_max_f32 v10, v10, v127
v_mov_b32 v11, v10
s_nop 1
v_permlane16_swap_b32 v10, v11
v_max_f32 v10, v10, v11
v_mov_b32 v11, v10
s_nop 1
v_permlane32_swap_b32 v10, v11
v_max_f32 v10, v10, v11
A_ v10,0x40e00000,v10
v_max_f32 v10, 0, v10
v_exp_f32 v11, -v10
s_nop 0
U_ v48,v48,v11
R_ v12,a8
U_ v12,v12,v11
X_ a8,v12
R_ v13,a9
U_ v13,v13,v11
X_ a9,v13
R_ v12,a10
U_ v12,v12,v11
X_ a10,v12
R_ v13,a11
U_ v13,v13,v11
X_ a11,v13
R_ v12,a28
U_ v12,v12,v11
X_ a28,v12
R_ v13,a29
U_ v13,v13,v11
X_ a29,v13
R_ v12,a30
U_ v12,v12,v11
X_ a30,v12
R_ v13,a31
U_ v13,v13,v11
X_ a31,v13
R_ v12,a48
U_ v12,v12,v11
X_ a48,v12
R_ v13,a49
U_ v13,v13,v11
X_ a49,v13
R_ v12,a50
U_ v12,v12,v11
X_ a50,v12
R_ v13,a51
U_ v13,v13,v11
X_ a51,v13
R_ v12,a68
U_ v12,v12,v11
X_ a68,v12
R_ v13,a69
U_ v13,v13,v11
X_ a69,v13
R_ v12,a70
U_ v12,v12,v11
X_ a70,v12
R_ v13,a71
U_ v13,v13,v11
X_ a71,v13
R_ v12,a88
U_ v12,v12,v11
X_ a88,v12
R_ v13,a89
U_ v13,v13,v11
X_ a89,v13
R_ v12,a90
U_ v12,v12,v11
X_ a90,v12
R_ v13,a91
U_ v13,v13,v11
X_ a91,v13
R_ v12,a108
U_ v12,v12,v11
X_ a108,v12
R_ v13,a109
U_ v13,v13,v11
X_ a109,v13
R_ v12,a110
U_ v12,v12,v11
X_ a110,v12
R_ v13,a111
U_ v13,v13,v11
X_ a111,v13
R_ v12,a128
U_ v12,v12,v11
X_ a128,v12
R_ v13,a129
U_ v13,v13,v11
X_ a129,v13
R_ v12,a130
U_ v12,v12,v11
X_ a130,v12
R_ v13,a131
U_ v13,v13,v11
X_ a131,v13
R_ v12,a148
U_ v12,v12,v11
X_ a148,v12
R_ v13,a149
U_ v13,v13,v11
X_ a149,v13
R_ v12,a150
U_ v12,v12,v11
X_ a150,v12
R_ v13,a151
U_ v13,v13,v11
X_ a151,v13
R_ v12,a168
U_ v12,v12,v11
X_ a168,v12
R_ v13,a169
U_ v13,v13,v11
X_ a169,v13
R_ v12,a170
U_ v12,v12,v11
X_ a170,v12
R_ v13,a171
U_ v13,v13,v11
X_ a171,v13
R_ v12,a188
U_ v12,v12,v11
X_ a188,v12
R_ v13,a189
U_ v13,v13,v11
X_ a189,v13
R_ v12,a190
U_ v12,v12,v11
X_ a190,v12
R_ v13,a191
U_ v13,v13,v11
X_ a191,v13
R_ v12,a208
U_ v12,v12,v11
X_ a208,v12
R_ v13,a209
U_ v13,v13,v11
X_ a209,v13
R_ v12,a210
U_ v12,v12,v11
X_ a210,v12
R_ v13,a211
U_ v13,v13,v11
X_ a211,v13
R_ v12,a228
U_ v12,v12,v11
X_ a228,v12
R_ v13,a229
U_ v13,v13,v11
X_ a229,v13
R_ v12,a230
U_ v12,v12,v11
X_ a230,v12
R_ v13,a231
U_ v13,v13,v11
X_ a231,v13
B_ v64,v64,v10
B_ v65,v65,v10
B_ v66,v66,v10
B_ v67,v67,v10
B_ v84,v84,v10
B_ v85,v85,v10
B_ v86,v86,v10
B_ v87,v87,v10
B_ v124,v124,v10
B_ v125,v125,v10
B_ v126,v126,v10
B_ v127,v127,v10
E_ 84
E_ 85
E_ 86
E_ 87
E_ 124
E_ 125
E_ 126
E_ 127
s_nop 0
C_ 127,126,127
C_ 126,124,125
C_ 124,84,85
C_ 125,86,87
v_max3_f32 v10, v88, v89, v90
v_max3_f32 v10, v10, v91, v128
v_max3_f32 v10, v10, v129, v130
v_max_f32 v10, v10, v131
v_mov_b32 v11, v10
s_nop 1
v_permlane16_swap_b32 v10, v11
v_max_f32 v10, v10, v11
v_mov_b32 v11, v10
s_nop 1
v_permlane32_swap_b32 v10, v11
v_max_f32 v10, v10, v11
A_ v10,0x40e00000,v10
v_max_f32 v10, 0, v10
v_exp_f32 v11, -v10
s_nop 0
U_ v49,v49,v11
R_ v12,a12
U_ v12,v12,v11
X_ a12,v12
R_ v13,a13
U_ v13,v13,v11
X_ a13,v13
R_ v12,a14
U_ v12,v12,v11
X_ a14,v12
R_ v13,a15
U_ v13,v13,v11
X_ a15,v13
R_ v12,a32
U_ v12,v12,v11
X_ a32,v12
R_ v13,a33
U_ v13,v13,v11
X_ a33,v13
R_ v12,a34
U_ v12,v12,v11
X_ a34,v12
R_ v13,a35
U_ v13,v13,v11
X_ a35,v13
R_ v12,a52
U_ v12,v12,v11
X_ a52,v12
R_ v13,a53
U_ v13,v13,v11
X_ a53,v13
R_ v12,a54
U_ v12,v12,v11
X_ a54,v12
R_ v13,a55
U_ v13,v13,v11
X_ a55,v13
R_ v12,a72
U_ v12,v12,v11
X_ a72,v12
R_ v13,a73
U_ v13,v13,v11
X_ a73,v13
R_ v12,a74
U_ v12,v12,v11
X_ a74,v12
R_ v13,a75
U_ v13,v13,v11
X_ a75,v13
R_ v12,a92
U_ v12,v12,v11
X_ a92,v12
R_ v13,a93
U_ v13,v13,v11
X_ a93,v13
R_ v12,a94
U_ v12,v12,v11
X_ a94,v12
R_ v13,a95
U_ v13,v13,v11
X_ a95,v13
R_ v12,a112
U_ v12,v12,v11
X_ a112,v12
R_ v13,a113
U_ v13,v13,v11
X_ a113,v13
R_ v12,a114
U_ v12,v12,v11
X_ a114,v12
R_ v13,a115
U_ v13,v13,v11
X_ a115,v13
R_ v12,a132
U_ v12,v12,v11
X_ a132,v12
R_ v13,a133
U_ v13,v13,v11
X_ a133,v13
R_ v12,a134
U_ v12,v12,v11
X_ a134,v12
R_ v13,a135
U_ v13,v13,v11
X_ a135,v13
R_ v12,a152
U_ v12,v12,v11
X_ a152,v12
R_ v13,a153
U_ v13,v13,v11
X_ a153,v13
R_ v12,a154
U_ v12,v12,v11
X_ a154,v12
R_ v13,a155
U_ v13,v13,v11
X_ a155,v13
R_ v12,a172
U_ v12,v12,v11
X_ a172,v12
R_ v13,a173
U_ v13,v13,v11
X_ a173,v13
R_ v12,a174
U_ v12,v12,v11
X_ a174,v12
R_ v13,a175
U_ v13,v13,v11
X_ a175,v13
R_ v12,a192
U_ v12,v12,v11
X_ a192,v12
R_ v13,a193
U_ v13,v13,v11
X_ a193,v13
R_ v12,a194
U_ v12,v12,v11
X_ a194,v12
R_ v13,a195
U_ v13,v13,v11
X_ a195,v13
R_ v12,a212
U_ v12,v12,v11
X_ a212,v12
R_ v13,a213
U_ v13,v13,v11
X_ a213,v13
R_ v12,a214
U_ v12,v12,v11
X_ a214,v12
R_ v13,a215
U_ v13,v13,v11
X_ a215,v13
R_ v12,a232
U_ v12,v12,v11
X_ a232,v12
R_ v13,a233
U_ v13,v13,v11
X_ a233,v13
R_ v12,a234
U_ v12,v12,v11
X_ a234,v12
R_ v13,a235
U_ v13,v13,v11
X_ a235,v13
B_ v68,v68,v10
B_ v69,v69,v10
B_ v70,v70,v10
B_ v71,v71,v10
B_ v88,v88,v10
B_ v89,v89,v10
B_ v90,v90,v10
B_ v91,v91,v10
B_ v128,v128,v10
B_ v129,v129,v10
B_ v130,v130,v10
B_ v131,v131,v10
E_ 88
E_ 89
E_ 90
E_ 91
E_ 128
E_ 129
E_ 130
E_ 131
s_nop 0
C_ 131,130,131
C_ 130,128,129
C_ 128,88,89
C_ 129,90,91
v_max3_f32 v10, v92, v93, v94
v_max3_f32 v10, v10, v95, v132
v_max3_f32 v10, v10, v133, v134
v_max_f32 v10, v10, v135
v_mov_b32 v11, v10
s_nop 1
v_permlane16_swap_b32 v10, v11
v_max_f32 v10, v10, v11
v_mov_b32 v11, v10
s_nop 1
v_permlane32_swap_b32 v10, v11
v_max_f32 v10, v10, v11
A_ v10,0x40e00000,v10
v_max_f32 v10, 0, v10
v_exp_f32 v11, -v10
s_nop 0
U_ v50,v50,v11
R_ v12,a16
U_ v12,v12,v11
X_ a16,v12
R_ v13,a17
U_ v13,v13,v11
X_ a17,v13
R_ v12,a18
U_ v12,v12,v11
X_ a18,v12
R_ v13,a19
U_ v13,v13,v11
X_ a19,v13
R_ v12,a36
U_ v12,v12,v11
X_ a36,v12
R_ v13,a37
U_ v13,v13,v11
X_ a37,v13
R_ v12,a38
U_ v12,v12,v11
X_ a38,v12
R_ v13,a39
U_ v13,v13,v11
X_ a39,v13
R_ v12,a56
U_ v12,v12,v11
X_ a56,v12
R_ v13,a57
U_ v13,v13,v11
X_ a57,v13
R_ v12,a58
U_ v12,v12,v11
X_ a58,v12
R_ v13,a59
U_ v13,v13,v11
X_ a59,v13
R_ v12,a76
U_ v12,v12,v11
X_ a76,v12
R_ v13,a77
U_ v13,v13,v11
X_ a77,v13
R_ v12,a78
U_ v12,v12,v11
X_ a78,v12
R_ v13,a79
U_ v13,v13,v11
X_ a79,v13
R_ v12,a96
U_ v12,v12,v11
X_ a96,v12
R_ v13,a97
U_ v13,v13,v11
X_ a97,v13
R_ v12,a98
U_ v12,v12,v11
X_ a98,v12
R_ v13,a99
U_ v13,v13,v11
X_ a99,v13
R_ v12,a116
U_ v12,v12,v11
X_ a116,v12
R_ v13,a117
U_ v13,v13,v11
X_ a117,v13
R_ v12,a118
U_ v12,v12,v11
X_ a118,v12
R_ v13,a119
U_ v13,v13,v11
X_ a119,v13
R_ v12,a136
U_ v12,v12,v11
X_ a136,v12
R_ v13,a137
U_ v13,v13,v11
X_ a137,v13
R_ v12,a138
U_ v12,v12,v11
X_ a138,v12
R_ v13,a139
U_ v13,v13,v11
X_ a139,v13
R_ v12,a156
U_ v12,v12,v11
X_ a156,v12
R_ v13,a157
U_ v13,v13,v11
X_ a157,v13
R_ v12,a158
U_ v12,v12,v11
X_ a158,v12
R_ v13,a159
U_ v13,v13,v11
X_ a159,v13
R_ v12,a176
U_ v12,v12,v11
X_ a176,v12
R_ v13,a177
U_ v13,v13,v11
X_ a177,v13
R_ v12,a178
U_ v12,v12,v11
X_ a178,v12
R_ v13,a179
U_ v13,v13,v11
X_ a179,v13
R_ v12,a196
U_ v12,v12,v11
X_ a196,v12
R_ v13,a197
U_ v13,v13,v11
X_ a197,v13
R_ v12,a198
U_ v12,v12,v11
X_ a198,v12
R_ v13,a199
U_ v13,v13,v11
X_ a199,v13
R_ v12,a216
U_ v12,v12,v11
X_ a216,v12
R_ v13,a217
U_ v13,v13,v11
X_ a217,v13
R_ v12,a218
U_ v12,v12,v11
X_ a218,v12
R_ v13,a219
U_ v13,v13,v11
X_ a219,v13
R_ v12,a236
U_ v12,v12,v11
X_ a236,v12
R_ v13,a237
U_ v13,v13,v11
X_ a237,v13
R_ v12,a238
U_ v12,v12,v11
X_ a238,v12
R_ v13,a239
U_ v13,v13,v11
X_ a239,v13
B_ v72,v72,v10
B_ v73,v73,v10
B_ v74,v74,v10
B_ v75,v75,v10
B_ v92,v92,v10
B_ v93,v93,v10
B_ v94,v94,v10
B_ v95,v95,v10
B_ v132,v132,v10
B_ v133,v133,v10
B_ v134,v134,v10
B_ v135,v135,v10
E_ 92
E_ 93
E_ 94
E_ 95
E_ 132
E_ 133
E_ 134
E_ 135
s_nop 0
C_ 135,134,135
C_ 134,132,133
C_ 132,92,93
C_ 133,94,95
v_mov_b32 v92, 0x3f803f80
v_mov_b32 v93, 0x3f803f80
v_mov_b32 v94, 0x3f803f80
v_mov_b32 v95, 0x3f803f80
s_nop 7
s_lshl_b32 s53, s58, 4
S_ s53,s53,s63
s_cmp_eq_u32 s53, 1
s_cbranch_scc1 .Lstep0_2
s_cmp_eq_u32 s53, 13
s_cbranch_scc1 .Lstep0_14
s_cmp_eq_u32 s53, 15
s_cbranch_scc1 .Lstep0_16
s_cmp_eq_u32 s53, 17
s_cbranch_scc1 .Lstep1_2
s_cmp_eq_u32 s53, 19
s_cbranch_scc1 .Lstep1_4
s_cmp_eq_u32 s53, 21
s_cbranch_scc1 .Lstep1_6
s_cmp_eq_u32 s53, 23
s_cbranch_scc1 .Lstep1_8
s_cmp_eq_u32 s53, 25
s_cbranch_scc1 .Lstep1_10
s_cmp_eq_u32 s53, 27
s_cbranch_scc1 .Lstep1_12
s_cmp_eq_u32 s53, 29
s_cbranch_scc1 .Lstep1_14
s_cmp_eq_u32 s53, 31
s_cbranch_scc1 .Lstep1_16
s_cmp_eq_u32 s53, 33
s_cbranch_scc1 .Lstep2_2
s_cmp_eq_u32 s53, 35
s_cbranch_scc1 .Lstep2_4
s_cmp_eq_u32 s53, 37
s_cbranch_scc1 .Lstep2_6
s_cmp_eq_u32 s53, 39
s_cbranch_scc1 .Lstep2_8
s_cmp_eq_u32 s53, 41
s_cbranch_scc1 .Lstep2_10
s_cmp_eq_u32 s53, 43
s_cbranch_scc1 .Lstep2_12
s_cmp_eq_u32 s53, 45
s_cbranch_scc1 .Lstep2_14
s_cmp_eq_u32 s53, 47
s_cbranch_scc1 .Lstep2_16
s_endpgm
.Lxs2m:
s_sleep 5
s_branch .Lxs2m_back
.Lxs2e:
s_sleep 5
s_branch .Lxs2e_back
.Lxs3m:
s_sleep 5
s_branch .Lxs3m_back
.Lxs3e:
s_sleep 5
s_branch .Lxs3e_back
.Lxs14m:
s_sleep 7
s_branch .Lxs14m_back
.Lxs14e:
s_sleep 7
s_branch .Lxs14e_back
.Lxs15m:
s_sleep 7
s_branch .Lxs15m_back
.Lxs15e:
s_sleep 7
s_branch .Lxs15e_back
.Lqsl2:
s_sleep 2
s_branch .Lqsl2_back
.Lqsl4:
s_sleep 2
s_branch .Lqsl4_back
.Lqsl6:
s_sleep 2
s_branch .Lqsl6_back
.Lqsl8:
s_sleep 2
s_branch .Lqsl8_back
.Lqsl10:
s_sleep 2
s_branch .Lqsl10_back
.Lqsl12:
s_sleep 2
s_branch .Lqsl12_back
.Lqsl14:
s_sleep 2
s_branch .Lqsl14_back
.Lpace1:
S_ s12,s12,s13
.Lpace1_l:
s_memrealtime s[100:101]
W_ 0
s_sub_u32 s100, s100, s98
s_cmp_ge_u32 s100, s12
s_cbranch_scc1 .Lpace1_back
s_sleep 2
s_branch .Lpace1_l
.Lpace3:
S_ s12,s12,s13
.Lpace3_l:
s_memrealtime s[100:101]
W_ 0
s_sub_u32 s100, s100, s98
s_cmp_ge_u32 s100, s12
s_cbranch_scc1 .Lpace3_back
s_sleep 2
s_branch .Lpace3_l
.Lpace5:
S_ s12,s12,s13
.Lpace5_l:
s_memrealtime s[100:101]
W_ 0
s_sub_u32 s100, s100, s98
s_cmp_ge_u32 s100, s12
s_cbranch_scc1 .Lpace5_back
s_sleep 2
s_branch .Lpace5_l
.Lpace7:
S_ s12,s12,s13
.Lpace7_l:
s_memrealtime s[100:101]
W_ 0
s_sub_u32 s100, s100, s98
s_cmp_ge_u32 s100, s12
s_cbranch_scc1 .Lpace7_back
s_sleep 2
s_branch .Lpace7_l
.Lpace9:
S_ s12,s12,s13
.Lpace9_l:
s_memrealtime s[100:101]
W_ 0
s_sub_u32 s100, s100, s98
s_cmp_ge_u32 s100, s12
s_cbranch_scc1 .Lpace9_back
s_sleep 2
s_branch .Lpace9_l
.Lpace11:
S_ s12,s12,s13
.Lpace11_l:
s_memrealtime s[100:101]
W_ 0
s_sub_u32 s100, s100, s98
s_cmp_ge_u32 s100, s12
s_cbranch_scc1 .Lpace11_back
s_sleep 2
s_branch .Lpace11_l
.Lpace13:
S_ s12,s12,s13
.Lpace13_l:
s_memrealtime s[100:101]
W_ 0
s_sub_u32 s100, s100, s98
s_cmp_ge_u32 s100, s12
s_cbranch_scc1 .Lpace13_back
s_sleep 2
s_branch .Lpace13_l
.Lpace15:
S_ s12,s12,s13
.Lpace15_l:
s_memrealtime s[100:101]
W_ 0
s_sub_u32 s100, s100, s98
s_cmp_ge_u32 s100, s12
s_cbranch_scc1 .Lpace15_back
s_sleep 2
s_branch .Lpace15_l
.Lpace17:
S_ s12,s12,s13
.Lpace17_l:
s_memrealtime s[100:101]
W_ 0
s_sub_u32 s100, s100, s98
s_cmp_ge_u32 s100, s12
s_cbranch_scc1 .Lpace17_back
s_sleep 2
s_branch .Lpace17_l
.Lpace19:
S_ s12,s12,s13
.Lpace19_l:
s_memrealtime s[100:101]
W_ 0
s_sub_u32 s100, s100, s98
s_cmp_ge_u32 s100, s12
s_cbranch_scc1 .Lpace19_back
s_sleep 2
s_branch .Lpace19_l
.Lpace21:
S_ s12,s12,s13
.Lpace21_l:
s_memrealtime s[100:101]
W_ 0
s_sub_u32 s100, s100, s98
s_cmp_ge_u32 s100, s12
s_cbranch_scc1 .Lpace21_back
s_sleep 2
s_branch .Lpace21_l
.Lpace23:
S_ s12,s12,s13
.Lpace23_l:
s_memrealtime s[100:101]
W_ 0
s_sub_u32 s100, s100, s98
s_cmp_ge_u32 s100, s12
s_cbranch_scc1 .Lpace23_back
s_sleep 2
s_branch .Lpace23_l
.Lpace25:
S_ s12,s12,s13
.Lpace25_l:
s_memrealtime s[100:101]
W_ 0
s_sub_u32 s100, s100, s98
s_cmp_ge_u32 s100, s12
s_cbranch_scc1 .Lpace25_back
s_sleep 2
s_branch .Lpace25_l
.Lpace27:
S_ s12,s12,s13
.Lpace27_l:
s_memrealtime s[100:101]
W_ 0
s_sub_u32 s100, s100, s98
s_cmp_ge_u32 s100, s12
s_cbranch_scc1 .Lpace27_back
s_sleep 2
s_branch .Lpace27_l
.Lpace29:
S_ s12,s12,s13
.Lpace29_l:
s_memrealtime s[100:101]
W_ 0
s_sub_u32 s100, s100, s98
s_cmp_ge_u32 s100, s12
s_cbranch_scc1 .Lpace29_back
s_sleep 2
s_branch .Lpace29_l
.Lhsd0_0:
s_bitcmp1_b32 s67, 13
s_cbranch_scc1 .Lhs_a0
s_bitcmp1_b32 s67, 11
s_cbranch_scc1 .Lhs_c0
s_branch .Lhs_d0
.Lhs_a0:
s_nop 4
s_waitcnt vmcnt(12)
R_ v10,a251
buffer_store_dword a255, v10, s[92:95], 0 offen offset:128 sc1
s_branch .Lhsd0_0_back
.Lhs_a1:
s_nop 4
R_ v11,a248
v_lshrrev_b32 v11, 2, v11
buffer_load_dword a254, v11, s[92:95], s96 offen offset:128 sc1
s_branch .Lhs_a1_back
.Lhs_a2:
s_nop 4
s_waitcnt vmcnt(12)
R_ v10,a251
buffer_store_dword a255, v10, s[92:95], 0 offen offset:128 sc1
s_branch .Lhs_a2_back
.Lhs_b1:
s_nop 4
s_waitcnt vmcnt(12)
R_ v11,a254
v_cmp_eq_u32 vcc, s62, v11
R_ v10,a251
v_mov_b32 v11, 0x7fff0000
v_cndmask_b32 v10, v11, v10, vcc
R_ v11,a253
v_add_u32 v11, 1, v11
buffer_store_dword v11, v10, s[92:95], 0 offen offset:256 sc1
s_and_b32 s53, vcc_lo, 255
s_cmp_eq_u32 s53, 0xFF
s_cselect_b32 s97, 1, 0
s_branch .Lhs_b1_back
.Lhs_c0:
s_nop 4
R_ v11,a248
v_lshrrev_b32 v11, 2, v11
buffer_load_dword a254, v11, s[92:95], s96 offen offset:128 sc1
s_branch .Lhsd0_0_back
.Lhs_c1:
s_nop 4
R_ v11,a248
v_lshrrev_b32 v11, 2, v11
buffer_load_dword a254, v11, s[92:95], s96 offen offset:256 sc1
s_branch .Lhs_c1_back
.Lhs_c2:
s_nop 4
R_ v11,a248
v_lshrrev_b32 v11, 2, v11
buffer_load_dword a254, v11, s[92:95], s96 offen offset:128 sc1
s_branch .Lhs_c2_back
.Lhs_d0:
s_nop 4
s_waitcnt vmcnt(12)
R_ v11,a254
v_cmp_eq_u32 vcc, s62, v11
R_ v10,a251
v_mov_b32 v11, 0x7fff0000
v_cndmask_b32 v10, v11, v10, vcc
R_ v11,a253
v_add_u32 v11, 1, v11
buffer_store_dword v11, v10, s[92:95], 0 offen offset:256 sc1
s_and_b32 s53, vcc_lo, 255
s_cmp_eq_u32 s53, 0xFF
s_cselect_b32 s97, 1, 0
s_branch .Lhsd0_0_back
.Lhs_d2:
s_nop 4
s_waitcnt vmcnt(12)
R_ v11,a254
v_cmp_eq_u32 vcc, s62, v11
R_ v10,a251
v_mov_b32 v11, 0x7fff0000
v_cndmask_b32 v10, v11, v10, vcc
R_ v11,a253
v_add_u32 v11, 1, v11
buffer_store_dword v11, v10, s[92:95], 0 offen offset:256 sc1
s_and_b32 s53, vcc_lo, 255
s_cmp_eq_u32 s53, 0xFF
s_cselect_b32 s97, 1, 0
s_branch .Lhs_d2_back
.Lhs_d1:
s_nop 4
s_waitcnt vmcnt(12)
R_ v11,a254
R_ v10,a252
v_cmp_ne_u32 vcc, v11, v10
s_nop 1
s_and_b32 s53, vcc_lo, 255
s_cmp_eq_u32 s53, 0xFF
s_cselect_b32 s53, s97, 0
s_cmp_eq_u32 s53, 1
s_cbranch_scc1 .Lhs_ok
.Lhs_spin:
s_cmp_eq_u32 s57, 0
s_cbranch_scc1 .Lhs_ok
s_sub_u32 s57, s57, 1
s_sleep 8
R_ v10,a251
s_lshl_b32 s53, s6, 2
v_subrev_u32 v10, s53, v10
buffer_load_dword v11, v10, s[92:95], 0 offen offset:0 sc1
R_ v10,a248
v_lshrrev_b32 v10, 2, v10
buffer_load_dword v12, v10, s[92:95], s96 offen offset:128 sc1
buffer_load_dword v13, v10, s[92:95], s96 offen offset:256 sc1
s_waitcnt vmcnt(0)
X_ a255,v11
R_ v10,a251
buffer_store_dword v11, v10, s[92:95], 0 offen offset:128 sc1
s_nop 1
v_cmp_eq_u32 vcc, s62, v12
R_ v10,a251
v_mov_b32 v12, 0x7fff0000
v_cndmask_b32 v10, v12, v10, vcc
R_ v12,a253
v_add_u32 v12, 1, v12
buffer_store_dword v12, v10, s[92:95], 0 offen offset:256 sc1
s_and_b32 s53, vcc_lo, 255
s_cmp_eq_u32 s53, 0xFF
s_cselect_b32 s97, 1, 0
s_nop 1
R_ v10,a252
v_cmp_ne_u32 vcc, v13, v10
s_nop 1
s_and_b32 s53, vcc_lo, 255
s_cmp_eq_u32 s53, 0xFF
s_cselect_b32 s53, s97, 0
s_cmp_eq_u32 s53, 1
s_cbranch_scc0 .Lhs_spin
.Lhs_ok:
s_branch .Lhs_d1_back
.Latt2:
R_ v13,a248
S_ s53,s51,36864
v_add_u32 v12, s53, v13
s_mul_i32 s53, s4, 10240
S_ s53,s53,0
v_add_u32 v13, s53, v13
s_cmp_eq_u32 s4, 0
s_cbranch_scc0 .Lhs_tok2
.Lpad_a2_0:
s_cbranch_execz .Lpad_a2_4
R_ v11,a241
buffer_load_dword a255, v11, s[92:95], 0 offen offset:0 sc1
.Lhs_tok2:
W_ 2
Zvvv 96,30,136
Dv 42,v1,44032
R_ v2,a245
Zvvv 100,30,140
ds_write_b128 v13, a[0:3] offset:0
Zvvv 104,30,144
v_xor_b32 v3, 32, v2
v_xor_b32 v4, 64, v2
Zvvv 108,30,148
ds_write_b128 v13, a[4:7] offset:1024
Zvvv 112,30,152
W_ 4
v_xor_b32 v5, 96, v2
s_mov_b64 s[24:25], s[16:17]
Mvvv 96,34,156
s_mov_b32 s26, 7864320
Dv 30,%0,45056
s_mov_b64 s[28:29], s[22:23]
Mvvv 100,34,160
ds_write_b128 v13, a[8:11] offset:2048
Mvvv 104,34,164
s_mov_b32 s30, 1638400
R_ v20,a80
s_mul_i32 s52, s4, 10240
Mvvv 108,34,168
ds_write_b128 v13, a[12:15] offset:3072
Mvvv 112,34,172
W_ 6
R_ v21,a81
R_ v22,a82
Mvvv 96,38,176
Dv 34,v1,45056
R_ v23,a83
Mvvv 100,38,180
ds_write_b128 v13, a[16:19] offset:4096
Mvvv 104,38,184
R_ v24,a84
R_ v25,a85
Mvvv 108,38,188
ds_write_b128 v13, a[40:43] offset:5120
Mvvv 112,38,192
W_ 8
R_ v26,a86
R_ v27,a87
Mvvv 96,42,196
Dv 38,%0,36864
R_ v52,a88
Mvvv 100,42,200
ds_write_b128 v13, a[44:47] offset:6144
Mvvv 104,42,204
R_ v53,a89
R_ v54,a90
Mvvv 108,42,208
ds_write_b128 v13, a[48:51] offset:7168
Mvvv 112,42,212
W_ 8
R_ v55,a91
v_accvgpr_mov_b32 a240, a92
Mvvv 96,30,216
Dv 42,v1,36864
v_accvgpr_mov_b32 a241, a93
Mvvv 100,30,220
ds_write_b128 v13, a[52:55] offset:8192
Mvvv 104,30,224
v_accvgpr_mov_b32 a242, a94
v_accvgpr_mov_b32 a243, a95
Mvvv 108,30,228
ds_write_b128 v13, a[56:59] offset:9216
Mvvv 112,30,232
W_ 8
v_accvgpr_mov_b32 a244, a96
v_accvgpr_mov_b32 a245, a97
Mvvv 96,34,236
Dv 30,%0,37888
v_accvgpr_mov_b32 a246, a98
Mvvv 100,34,240
v_accvgpr_mov_b32 a247, a99
R_ v16,a120
Mvvv 104,34,244
R_ v17,a121
R_ v18,a122
Mvvv 108,34,248
R_ v19,a123
R_ v28,a124
Mvvv 112,34,252
W_ 6
R_ v29,a125
R_ v51,a126
Zvvv 76,38,136
Dv 34,v1,37888
R_ v6,a127
Zvvv 80,38,140
R_ v14,a128
R_ v15,a129
Zvvv 84,38,144
v_accvgpr_mov_b32 a249, a130
v_accvgpr_mov_b32 a250, a131
Zvvv 88,38,148
v_max3_f32 v56, v96, v97, v98
v_max_f32 v56, v56, v99
Zvvv 92,38,152
W_ 4
v_max3_f32 v60, v100, v101, v102
v_max_f32 v60, v60, v103
Mvvv 76,42,156
Dv 38,%0,38912
v_max3_f32 v64, v104, v105, v106
Mvvv 80,42,160
v_max_f32 v64, v64, v107
Mvvv 84,42,164
v_max3_f32 v68, v108, v109, v110
v_max_f32 v68, v68, v111
Mvvv 88,42,168
v_max3_f32 v72, v112, v113, v114
v_max_f32 v72, v72, v115
Mvvv 92,42,172
W_ 2
Mvvv 76,30,176
Dv 42,v1,38912
Mvvv 80,30,180
Mvvv 84,30,184
Mvvv 88,30,188
Mvvv 92,30,192
s_waitcnt vmcnt(30) lgkmcnt(0)
s_barrier
Mvvv 76,34,196
Dv 30,%0,55296
Mvvv 76,38,216
Mvvv 76,42,236
Mvvv 80,34,200
Mvvv 80,38,220
Mvvv 80,42,240
Mvvv 84,34,204
s_nop 3
v_max3_f32 v56, v56, v76, v77
v_max3_f32 v56, v56, v78, v79
Mvvv 84,38,224
Mvvv 84,42,244
Mvvv 88,34,208
v_max3_f32 v60, v60, v80, v81
v_max3_f32 v60, v60, v82, v83
Mvvv 88,38,228
Mvvv 88,42,248
Mvvv 92,34,212
s_nop 1
v_max3_f32 v64, v64, v84, v85
v_max3_f32 v64, v64, v86, v87
Mvvv 92,38,232
Mvvv 92,42,252
Dv 34,v1,55296
.p2align 3
v_mov_b32 v11, 0x3f803f80
v_permlane32_swap_b32 v56, v60
v_max3_f32 v68, v68, v88, v89
v_max3_f32 v68, v68, v90, v91
v_max_f32 v56, v56, v60
s_nop 1
v_max3_f32 v72, v72, v92, v93
v_max3_f32 v72, v72, v94, v95
v_mov_b32 v73, v72
v_permlane32_swap_b32 v64, v68
v_max_f32 v64, v64, v68
v_permlane32_swap_b32 v72, v73
v_max_f32 v72, v72, v73
v_mov_b32 v73, v72
v_permlane16_swap_b32 v56, v64
v_max_f32 v56, v56, v64
v_permlane16_swap_b32 v72, v73
B_ v56,0xc0e00000,v56
v_max_f32 v72, v72, v73
v_mov_b32 v64, v56
B_ v72,0xc0e00000,v72
v_mov_b32 v73, v72
v_permlane16_swap_b32 v56, v64
v_mov_b32 v60, v56
v_mov_b32 v68, v64
v_mov_b64 v[74:75], v[72:73]
v_permlane32_swap_b32 v56, v60
v_permlane32_swap_b32 v64, v68
v_mov_b32 v57, v56
v_mov_b32 v61, v60
v_mov_b32 v65, v64
v_mov_b32 v69, v68
v_pk_add_f32 v[96:97], v[96:97], v[56:57]
v_pk_add_f32 v[98:99], v[98:99], v[56:57]
v_pk_add_f32 v[76:77], v[76:77], v[56:57]
v_pk_add_f32 v[78:79], v[78:79], v[56:57]
v_mov_b64 v[58:59], v[56:57]
v_pk_add_f32 v[100:101], v[100:101], v[60:61]
v_pk_add_f32 v[102:103], v[102:103], v[60:61]
v_pk_add_f32 v[80:81], v[80:81], v[60:61]
)A"
R"A(v_pk_add_f32 v[82:83], v[82:83], v[60:61]
v_mov_b64 v[62:63], v[60:61]
v_pk_add_f32 v[104:105], v[104:105], v[64:65]
v_pk_add_f32 v[106:107], v[106:107], v[64:65]
v_pk_add_f32 v[84:85], v[84:85], v[64:65]
v_pk_add_f32 v[86:87], v[86:87], v[64:65]
v_mov_b64 v[66:67], v[64:65]
v_pk_add_f32 v[108:109], v[108:109], v[68:69]
v_pk_add_f32 v[110:111], v[110:111], v[68:69]
v_pk_add_f32 v[88:89], v[88:89], v[68:69]
v_pk_add_f32 v[90:91], v[90:91], v[68:69]
v_mov_b64 v[70:71], v[68:69]
v_pk_add_f32 v[112:113], v[112:113], v[72:73]
v_pk_add_f32 v[114:115], v[114:115], v[72:73]
v_pk_add_f32 v[92:93], v[92:93], v[72:73]
v_pk_add_f32 v[94:95], v[94:95], v[72:73]
E_ 96
E_ 97
E_ 98
E_ 99
E_ 76
E_ 77
E_ 78
E_ 79
C_ 99,98,99
C_ 98,96,97
C_ 96,76,77
C_ 97,78,79
E_ 100
E_ 101
E_ 102
E_ 103
E_ 80
E_ 81
E_ 82
E_ 83
C_ 103,102,103
C_ 102,100,101
C_ 100,80,81
C_ 101,82,83
.Lstep2_1:
W_ 1
Nvvv 116,30,136,56
s_nop 0
Dv 38,%0,56320
Dv 42,v1,56320
W_ 2
Mvvv 116,34,156
E_ 104
Nvvv 120,30,140,60
E_ 105
Mvvv 120,34,160
E_ 106
Nvvv 124,30,144,64
E_ 107
Mvvv 124,34,164
E_ 84
Nvvv 128,30,148,68
E_ 85
Mvvv 128,34,168
E_ 86
Nvvv 132,30,152,72
E_ 87
Mvvv 132,34,172
W_ 0
C_ 107,106,107
C_ 106,104,105
Mvvv 132,38,192
s_nop 0
Dv 30,%0,57344
Dv 34,v1,57344
Mvvv 132,42,212
C_ 104,84,85
C_ 105,86,87
Mvvv 128,38,188
E_ 108
Mvvv 128,42,208
E_ 109
Mvvv 124,38,184
E_ 110
Mvvv 124,42,204
E_ 111
Mvvv 120,38,180
E_ 88
Mvvv 120,42,200
E_ 89
Mvvv 116,38,176
E_ 90
Mvvv 116,42,196
W_ 0
E_ 91
Mvvv 116,30,216
s_nop 0
Dv 38,%0,49152
Dv 42,v1,49152
Mvvv 116,34,236
C_ 111,110,111
C_ 110,108,109
Mvvv 120,30,220
C_ 108,88,89
C_ 109,90,91
Mvvv 120,34,240
E_ 112
Mvvv 124,30,224
E_ 113
Mvvv 124,34,244
E_ 114
Mvvv 128,30,228
.Lpad_a2_1:
s_cbranch_execz .Lpad_a2_5
E_ 115
Mvvv 128,34,248
E_ 92
Mvvv 132,30,232
E_ 93
Mvvv 132,34,252
W_ 0
E_ 94
Nvvv 76,38,136,56
s_nop 0
Dv 30,%0,50176
Dv 34,v1,50176
Mvvv 76,42,156
E_ 95
Nvvv 80,38,140,60
C_ 115,114,115
C_ 114,112,113
Mvvv 80,42,160
C_ 112,92,93
C_ 113,94,95
Nvvv 84,38,144,64
E_ 116
Mvvv 84,42,164
E_ 117
Nvvv 88,38,148,68
E_ 118
Mvvv 88,42,168
E_ 119
Nvvv 92,38,152,72
C_ 119,118,119
C_ 118,116,117
Mvvv 92,42,172
W_ 0
E_ 120
Mvvv 92,30,192
s_nop 0
Dv 38,%0,51200
Dv 42,v1,51200
Mvvv 92,34,212
X_ a180,v11
X_ a181,v11
Mvvv 88,30,188
X_ a182,v11
X_ a183,v11
Mvvv 88,34,208
ds_write_b128 v12, a[132:135] offset:0
Mvvv 84,30,184
E_ 121
Mvvv 84,34,204
E_ 122
Mvvv 80,30,180
ds_write_b128 v12, a[136:139] offset:1024
Mvvv 80,34,200
E_ 123
Mvvv 76,30,176
C_ 123,122,123
C_ 122,120,121
Mvvv 76,34,196
ds_write_b128 v12, a[160:163] offset:2048
W_ 4
Mvvv 76,38,216
W_ 3
E_ 124
Mvvv 76,42,236
s_waitcnt vmcnt(12) lgkmcnt(0)
s_barrier
Mvvv 80,38,220
ds_write_b128 v12, a[164:167] offset:3072
Mvvv 80,42,240
E_ 125
Mvvv 84,38,224
E_ 126
Mvvv 84,42,244
s_nop 0
Tv 30,v2,24576
Tv 32,v2,30720
Mvvv 88,38,228
ds_write_b128 v12, a[168:171] offset:4096
Mvvv 88,42,248
s_nop 0
Tv 34,v3,24576
Tv 36,v3,30720
Mvvv 92,38,232
E_ 127
Mvvv 92,42,252
ds_write_b128 v12, a[172:175] offset:5120
Zaav 220,180,96
C_ 127,126,127
C_ 126,124,125
Zaav 224,180,100
E_ 128
Zaav 228,180,104
ds_write_b128 v12, a[176:179] offset:12288
Zaav 232,180,108
E_ 129
Zaav 236,180,112
W_ 2
E_ 130
Zavv 0,30,96
s_nop 0
Tv 38,v4,24576
Tv 40,v4,30720
Zavv 20,34,96
ds_write_b128 v12, a[200:203] offset:13312
Zavv 24,34,100
s_nop 0
Tv 42,v5,24576
Tv 44,v5,30720
Zavv 4,30,100
E_ 131
Zavv 8,30,104
ds_write_b128 v12, a[204:207] offset:14336
Zavv 28,34,104
R_ v46,a220
R_ v47,a224
Zavv 32,34,108
R_ v48,a228
R_ v49,a232
Zavv 12,30,108
R_ v50,a236
C_ 131,130,131
Zavv 16,30,112
ds_write_b128 v12, a[208:211] offset:15360
Zavv 36,34,112
W_ 2
C_ 130,128,129
Zavv 56,38,112
s_nop 0
Tv 30,v2,25600
Tv 32,v2,31744
Zavv 76,42,112
ds_write_b128 v12, a[212:215] offset:16384
Zavv 72,42,108
s_nop 0
Tv 34,v3,25600
Tv 36,v3,31744
Zavv 52,38,108
E_ 132
Zavv 48,38,104
ds_write_b128 v12, a[216:219] offset:17408
Zavv 68,42,104
E_ 133
Zavv 64,42,100
E_ 134
Zavv 44,38,100
E_ 135
Zavv 40,38,96
C_ 135,134,135
C_ 134,132,133
Zavv 60,42,96
W_ 1
E_ 76
Zavv 80,30,96
s_nop 0
Tv 38,v4,25600
Tv 40,v4,31744
Zavv 100,34,96
E_ 77
Zavv 104,34,100
s_nop 0
Tv 42,v5,25600
Tv 44,v5,31744
Zavv 84,30,100
E_ 78
Zavv 88,30,104
E_ 79
Zavv 108,34,104
C_ 116,76,77
C_ 117,78,79
Zavv 112,34,108
E_ 80
Zavv 92,30,108
E_ 81
Zavv 96,30,112
E_ 82
Zavv 116,34,112
W_ 0
E_ 83
Zavv 136,38,112
s_nop 0
Tv 30,v2,26624
Tv 32,v2,32768
Zavv 156,42,112
C_ 120,80,81
C_ 121,82,83
Zavv 152,42,108
s_nop 0
Tv 34,v3,26624
Tv 36,v3,32768
Zavv 132,38,108
E_ 84
Zavv 128,38,104
E_ 85
Zavv 148,42,104
E_ 86
Zavv 144,42,100
E_ 87
Zavv 124,38,100
C_ 124,84,85
C_ 125,86,87
Zavv 120,38,96
E_ 88
Zavv 140,42,96
W_ 0
E_ 89
Zavv 160,30,96
s_nop 0
Tv 38,v4,26624
Tv 40,v4,32768
Zavv 180,34,96
E_ 90
Zavv 184,34,100
s_nop 0
Tv 42,v5,26624
Tv 44,v5,32768
Zavv 164,30,100
E_ 91
Zavv 168,30,104
C_ 128,88,89
C_ 129,90,91
Zavv 188,34,104
S_ s65,s61,36864
E_ 92
Zavv 192,34,108
S_ m0,s51,24576
E_ 93
Zavv 172,30,108
s_waitcnt vmcnt(6) lgkmcnt(0)
s_barrier
Zavv 176,30,112
Ln v7,32,s65,0
Zavv 196,34,112
E_ 94
Zavv 216,38,112
Dv 30,%0,6144
Zavv 236,42,112
Dv 34,v1,6144
Zavv 232,42,108
Ln v8,32,s65,3072
S_ m0,s51,25472
Zavv 212,38,108
E_ 95
Zavv 208,38,104
Ln v7,32,s65,128
Zavv 228,42,104
C_ 132,92,93
C_ 133,94,95
Zavv 224,42,100
Ln v8,32,s65,3200
S_ m0,s51,26368
Zavv 204,38,100
v_mov_b32 v92, 0x3f803f80
v_mov_b32 v93, 0x3f803f80
Zavv 200,38,96
Ln v7,32,s65,256
Zavv 220,42,96
v_mov_b32 v94, 0x3f803f80
v_mov_b32 v95, 0x3f803f80
Ln v8,32,s65,3328
.Lstep2_2:
s_nop 0
Zvvv 76,92,116
Dv 38,%0,7168
Zvvv 80,92,120
Dv 42,v1,7168
Zvvv 84,92,124
Zvvv 88,92,128
Zvvv 92,92,132
W_ 2
Nvvv 96,30,136,56
Mvvv 96,34,156
Nvvv 100,30,140,60
s_nop 1
v_max3_f32 v10, v76, v80, v84
s_nop 0
v_max3_f32 v10, v10, v88, v92
Mvvv 100,34,160
v_cmp_lt_f32 vcc, 0x42000000, v10
s_cbranch_vccnz .Ltramp2_1
Nvvv 104,30,144,64
A_ v46,v46,v76
A_ v47,v47,v80
Mvvv 104,34,164
A_ v48,v48,v84
A_ v49,v49,v88
Nvvv 108,30,148,68
A_ v50,v50,v92
Mvvv 108,34,168
Nvvv 112,30,152,72
Mvvv 112,34,172
W_ 0
Mvvv 112,38,192
Dv 30,%0,8192
Mvvv 112,42,212
Dv 34,v1,8192
Mvvv 108,38,188
Mvvv 108,42,208
Mvvv 104,38,184
Mvvv 104,42,204
Mvvv 100,38,180
Mvvv 100,42,200
Mvvv 96,38,176
Mvvv 96,42,196
W_ 0
Mvvv 96,30,216
Dv 38,%0,0
Mvvv 96,34,236
Dv 42,v1,0
Mvvv 100,30,220
Mvvv 100,34,240
Mvvv 104,30,224
Mvvv 104,34,244
Mvvv 108,30,228
Mvvv 108,34,248
s_nop 0
E_ 96
Mvvv 112,30,232
E_ 97
Mvvv 112,34,252
W_ 0
E_ 98
Nvvv 76,38,136,56
Dv 30,%0,1024
Mvvv 76,42,156
Dv 34,v1,1024
Nvvv 80,38,140,60
E_ 99
Mvvv 80,42,160
C_ 99,98,99
C_ 98,96,97
Nvvv 84,38,144,64
E_ 100
Mvvv 84,42,164
E_ 101
Nvvv 88,38,148,68
E_ 102
Mvvv 88,42,168
E_ 103
Nvvv 92,38,152,72
C_ 103,102,103
C_ 102,100,101
Mvvv 92,42,172
W_ 0
E_ 104
Mvvv 92,30,192
Dv 38,%0,2048
Mvvv 92,34,212
Dv 42,v1,2048
Mvvv 88,30,188
E_ 105
Mvvv 88,34,208
E_ 106
Mvvv 84,30,184
S_ s65,s61,24576
E_ 107
Mvvv 84,34,204
S_ m0,s51,0
C_ 107,106,107
C_ 106,104,105
Mvvv 80,30,180
s_waitcnt vmcnt(6) lgkmcnt(0)
s_barrier
Mvvv 80,34,200
L_ v9,36,s65,0
Mvvv 76,30,176
E_ 108
Mvvv 76,34,196
L_ v9,36,s65,3072
S_ m0,s51,896
Mvvv 76,38,216
Tv 30,v2,12288
Tv 32,v2,18432
Mvvv 76,42,236
L_ v9,36,s65,128
Mvvv 80,38,220
s_nop 0
Tv 34,v3,12288
Tv 36,v3,18432
Mvvv 80,42,240
L_ v9,36,s65,3200
S_ m0,s51,1792
Mvvv 84,38,224
E_ 109
Mvvv 84,42,244
L_ v9,36,s65,256
Mvvv 88,38,228
E_ 110
Mvvv 88,42,248
L_ v9,36,s65,3328
Mvvv 92,38,232
E_ 111
Mvvv 92,42,252
W_ 0
C_ 111,110,111
C_ 110,108,109
Mavv 0,30,116
s_nop 0
Tv 38,v4,12288
Tv 40,v4,18432
Mavv 20,34,116
E_ 112
Mavv 24,34,120
s_nop 0
Tv 42,v5,12288
Tv 44,v5,18432
Mavv 4,30,120
E_ 113
Mavv 8,30,124
E_ 114
Mavv 28,34,124
E_ 115
Mavv 32,34,128
C_ 115,114,115
C_ 114,112,113
Mavv 12,30,128
E_ 76
Mavv 16,30,132
E_ 77
Mavv 36,34,132
W_ 0
E_ 78
Mavv 56,38,132
s_nop 0
Tv 30,v2,13312
Tv 32,v2,19456
Mavv 76,42,132
E_ 79
Mavv 72,42,128
s_nop 0
Tv 34,v3,13312
Tv 36,v3,19456
Mavv 52,38,128
C_ 96,76,77
C_ 97,78,79
Mavv 48,38,124
E_ 80
Mavv 68,42,124
E_ 81
Mavv 64,42,120
E_ 82
Mavv 44,38,120
E_ 83
Mavv 40,38,116
C_ 100,80,81
C_ 101,82,83
Mavv 60,42,116
W_ 0
E_ 84
Mavv 80,30,116
s_nop 0
Tv 38,v4,13312
Tv 40,v4,19456
Mavv 100,34,116
E_ 85
Mavv 104,34,120
s_nop 0
Tv 42,v5,13312
Tv 44,v5,19456
Mavv 84,30,120
E_ 86
Mavv 88,30,124
E_ 87
Mavv 108,34,124
C_ 104,84,85
C_ 105,86,87
Mavv 112,34,128
E_ 88
Mavv 92,30,128
E_ 89
Mavv 96,30,132
E_ 90
Mavv 116,34,132
W_ 0
E_ 91
Mavv 136,38,132
s_nop 0
Tv 30,v2,14336
Tv 32,v2,20480
Mavv 156,42,132
C_ 108,88,89
C_ 109,90,91
Mavv 152,42,128
s_nop 0
Tv 34,v3,14336
Tv 36,v3,20480
Mavv 132,38,128
E_ 92
Mavv 128,38,124
E_ 93
Mavv 148,42,124
E_ 94
Mavv 144,42,120
E_ 95
Mavv 124,38,120
C_ 112,92,93
C_ 113,94,95
Mavv 120,38,116
v_mov_b32 v92, 0x3f803f80
v_mov_b32 v93, 0x3f803f80
Mavv 140,42,116
W_ 0
v_mov_b32 v94, 0x3f803f80
v_mov_b32 v95, 0x3f803f80
Mavv 160,30,116
s_nop 0
Tv 38,v4,14336
Tv 40,v4,20480
Mavv 180,34,116
Mavv 184,34,120
s_nop 0
Tv 42,v5,14336
Tv 44,v5,20480
Mavv 164,30,120
Mavv 168,30,124
Mavv 188,34,124
S_ s65,s61,49152
Mavv 192,34,128
S_ m0,s51,12288
Mavv 172,30,128
s_waitcnt vmcnt(6) lgkmcnt(0)
s_barrier
Mavv 176,30,132
Ln v7,32,s65,0
Mavv 196,34,132
Mavv 216,38,132
Dv 30,%0,30720
Mavv 236,42,132
Dv 34,v1,30720
Mavv 232,42,128
Ln v8,32,s65,3072
S_ m0,s51,13184
Mavv 212,38,128
Mavv 208,38,124
Ln v7,32,s65,128
Mavv 228,42,124
Mavv 224,42,120
Ln v8,32,s65,3200
S_ m0,s51,14080
Mavv 204,38,120
Mavv 200,38,116
Ln v7,32,s65,256
Mavv 220,42,116
s_cmp_eq_u32 s4, 0
s_cbranch_scc1 .Lhs_a2
.Lhs_a2_back:
.Lstep2_3:
Zvvv 76,92,96
Dv 38,%0,31744
Zvvv 80,92,100
Dv 42,v1,31744
Zvvv 84,92,104
Zvvv 88,92,108
Zvvv 92,92,112
W_ 2
Nvvv 116,30,136,56
Mvvv 116,34,156
Nvvv 120,30,140,60
s_nop 1
v_max3_f32 v10, v76, v80, v84
s_nop 0
v_max3_f32 v10, v10, v88, v92
Mvvv 120,34,160
v_cmp_lt_f32 vcc, 0x42000000, v10
s_cbranch_vccnz .Ltramp2_2
Nvvv 124,30,144,64
A_ v46,v46,v76
A_ v47,v47,v80
Mvvv 124,34,164
A_ v48,v48,v84
A_ v49,v49,v88
Nvvv 128,30,148,68
A_ v50,v50,v92
Mvvv 128,34,168
Ln v8,32,s65,3328
Nvvv 132,30,152,72
Mvvv 132,34,172
W_ 0
Mvvv 132,38,192
Dv 30,%0,32768
Mvvv 132,42,212
Dv 34,v1,32768
Mvvv 128,38,188
Mvvv 128,42,208
Mvvv 124,38,184
Mvvv 124,42,204
Mvvv 120,38,180
Mvvv 120,42,200
Mvvv 116,38,176
Mvvv 116,42,196
W_ 0
Mvvv 116,30,216
Dv 38,%0,24576
Mvvv 116,34,236
Dv 42,v1,24576
Mvvv 120,30,220
.Lpad_a2_2:
s_cbranch_execz .Lpad_a2_6
Mvvv 120,34,240
Mvvv 124,30,224
Mvvv 124,34,244
Mvvv 128,30,228
Mvvv 128,34,248
s_nop 0
E_ 116
Mvvv 132,30,232
E_ 117
Mvvv 132,34,252
W_ 0
E_ 118
Nvvv 76,38,136,56
Dv 30,%0,25600
Mvvv 76,42,156
Dv 34,v1,25600
Nvvv 80,38,140,60
E_ 119
Mvvv 80,42,160
C_ 119,118,119
C_ 118,116,117
Nvvv 84,38,144,64
E_ 120
Mvvv 84,42,164
E_ 121
Nvvv 88,38,148,68
E_ 122
Mvvv 88,42,168
E_ 123
Nvvv 92,38,152,72
C_ 123,122,123
C_ 122,120,121
Mvvv 92,42,172
W_ 0
E_ 124
Mvvv 92,30,192
Dv 38,%0,26624
Mvvv 92,34,212
Dv 42,v1,26624
Mvvv 88,30,188
E_ 125
Mvvv 88,34,208
E_ 126
Mvvv 84,30,184
S_ s65,s61,36864
E_ 127
Mvvv 84,34,204
S_ m0,s51,24576
C_ 127,126,127
C_ 126,124,125
Mvvv 80,30,180
s_waitcnt vmcnt(6) lgkmcnt(0)
s_barrier
Mvvv 80,34,200
L_ v9,36,s65,0
Mvvv 76,30,176
E_ 128
Mvvv 76,34,196
L_ v9,36,s65,3072
S_ m0,s51,25472
Mvvv 76,38,216
Tv 30,v2,0
Tv 32,v2,6144
Mvvv 76,42,236
L_ v9,36,s65,128
Mvvv 80,38,220
s_nop 0
Tv 34,v3,0
Tv 36,v3,6144
Mvvv 80,42,240
L_ v9,36,s65,3200
S_ m0,s51,26368
Mvvv 84,38,224
E_ 129
Mvvv 84,42,244
L_ v9,36,s65,256
Mvvv 88,38,228
E_ 130
Mvvv 88,42,248
L_ v9,36,s65,3328
Mvvv 92,38,232
E_ 131
Mvvv 92,42,252
W_ 0
C_ 131,130,131
C_ 130,128,129
Mavv 0,30,96
s_nop 0
Tv 38,v4,0
Tv 40,v4,6144
Mavv 20,34,96
E_ 132
Mavv 24,34,100
s_nop 0
Tv 42,v5,0
Tv 44,v5,6144
Mavv 4,30,100
E_ 133
Mavv 8,30,104
E_ 134
Mavv 28,34,104
E_ 135
Mavv 32,34,108
C_ 135,134,135
C_ 134,132,133
Mavv 12,30,108
E_ 76
Mavv 16,30,112
E_ 77
Mavv 36,34,112
W_ 0
E_ 78
Mavv 56,38,112
s_nop 0
Tv 30,v2,1024
Tv 32,v2,7168
Mavv 76,42,112
E_ 79
Mavv 72,42,108
s_nop 0
Tv 34,v3,1024
Tv 36,v3,7168
Mavv 52,38,108
C_ 116,76,77
C_ 117,78,79
Mavv 48,38,104
E_ 80
Mavv 68,42,104
E_ 81
Mavv 64,42,100
E_ 82
Mavv 44,38,100
E_ 83
Mavv 40,38,96
C_ 120,80,81
C_ 121,82,83
Mavv 60,42,96
W_ 0
E_ 84
Mavv 80,30,96
s_nop 0
Tv 38,v4,1024
Tv 40,v4,7168
Mavv 100,34,96
E_ 85
Mavv 104,34,100
s_nop 0
Tv 42,v5,1024
Tv 44,v5,7168
Mavv 84,30,100
E_ 86
Mavv 88,30,104
E_ 87
Mavv 108,34,104
C_ 124,84,85
C_ 125,86,87
Mavv 112,34,108
E_ 88
Mavv 92,30,108
E_ 89
Mavv 96,30,112
E_ 90
Mavv 116,34,112
W_ 0
E_ 91
Mavv 136,38,112
s_nop 0
Tv 30,v2,2048
Tv 32,v2,8192
Mavv 156,42,112
C_ 128,88,89
C_ 129,90,91
Mavv 152,42,108
s_nop 0
Tv 34,v3,2048
Tv 36,v3,8192
Mavv 132,38,108
E_ 92
Mavv 128,38,104
E_ 93
Mavv 148,42,104
E_ 94
Mavv 144,42,100
E_ 95
Mavv 124,38,100
C_ 132,92,93
C_ 133,94,95
Mavv 120,38,96
v_mov_b32 v92, 0x3f803f80
v_mov_b32 v93, 0x3f803f80
Mavv 140,42,96
W_ 0
v_mov_b32 v94, 0x3f803f80
v_mov_b32 v95, 0x3f803f80
Mavv 160,30,96
s_nop 0
Tv 38,v4,2048
Tv 40,v4,8192
Mavv 180,34,96
Mavv 184,34,100
s_nop 0
Tv 42,v5,2048
Tv 44,v5,8192
Mavv 164,30,100
Mavv 168,30,104
Mavv 188,34,104
S_ s65,s61,61440
Mavv 192,34,108
S_ m0,s51,0
Mavv 172,30,108
s_waitcnt vmcnt(6) lgkmcnt(0)
s_barrier
Mavv 176,30,112
Ln v7,32,s65,0
Mavv 196,34,112
Mavv 216,38,112
Dv 30,%0,18432
Mavv 236,42,112
Dv 34,v1,18432
Mavv 232,42,108
Ln v8,32,s65,3072
S_ m0,s51,896
Mavv 212,38,108
Mavv 208,38,104
Ln v7,32,s65,128
Mavv 228,42,104
Mavv 224,42,100
Ln v8,32,s65,3200
S_ m0,s51,1792
Mavv 204,38,100
Mavv 200,38,96
Ln v7,32,s65,256
Mavv 220,42,96
.Lstep2_4:
Zvvv 76,92,116
Dv 38,%0,19456
Zvvv 80,92,120
Dv 42,v1,19456
Zvvv 84,92,124
Zvvv 88,92,128
Zvvv 92,92,132
W_ 2
Nvvv 96,30,136,56
Mvvv 96,34,156
Nvvv 100,30,140,60
s_nop 1
v_max3_f32 v10, v76, v80, v84
s_nop 0
v_max3_f32 v10, v10, v88, v92
Mvvv 100,34,160
v_cmp_lt_f32 vcc, 0x42000000, v10
s_cbranch_vccnz .Ltramp2_3
Nvvv 104,30,144,64
A_ v46,v46,v76
A_ v47,v47,v80
Mvvv 104,34,164
A_ v48,v48,v84
A_ v49,v49,v88
Nvvv 108,30,148,68
A_ v50,v50,v92
Mvvv 108,34,168
Ln v8,32,s65,3328
Nvvv 112,30,152,72
Mvvv 112,34,172
W_ 0
Mvvv 112,38,192
Dv 30,%0,20480
Mvvv 112,42,212
Dv 34,v1,20480
Mvvv 108,38,188
Mvvv 108,42,208
Mvvv 104,38,184
Mvvv 104,42,204
Mvvv 100,38,180
Mvvv 100,42,200
Mvvv 96,38,176
Mvvv 96,42,196
W_ 0
Mvvv 96,30,216
Dv 38,%0,12288
Mvvv 96,34,236
Dv 42,v1,12288
Mvvv 100,30,220
Mvvv 100,34,240
Mvvv 104,30,224
Mvvv 104,34,244
Mvvv 108,30,228
Mvvv 108,34,248
s_nop 0
E_ 96
Mvvv 112,30,232
E_ 97
Mvvv 112,34,252
W_ 0
E_ 98
Nvvv 76,38,136,56
Dv 30,%0,13312
Mvvv 76,42,156
Dv 34,v1,13312
Nvvv 80,38,140,60
E_ 99
Mvvv 80,42,160
C_ 99,98,99
C_ 98,96,97
Nvvv 84,38,144,64
E_ 100
Mvvv 84,42,164
E_ 101
Nvvv 88,38,148,68
E_ 102
Mvvv 88,42,168
E_ 103
Nvvv 92,38,152,72
C_ 103,102,103
C_ 102,100,101
Mvvv 92,42,172
W_ 0
E_ 104
Mvvv 92,30,192
Dv 38,%0,14336
Mvvv 92,34,212
Dv 42,v1,14336
Mvvv 88,30,188
E_ 105
Mvvv 88,34,208
E_ 106
Mvvv 84,30,184
S_ s65,s61,49152
E_ 107
Mvvv 84,34,204
S_ m0,s51,12288
C_ 107,106,107
C_ 106,104,105
Mvvv 80,30,180
s_waitcnt vmcnt(6) lgkmcnt(0)
s_barrier
Mvvv 80,34,200
L_ v9,36,s65,0
Mvvv 76,30,176
E_ 108
Mvvv 76,34,196
L_ v9,36,s65,3072
S_ m0,s51,13184
Mvvv 76,38,216
Tv 30,v2,24576
Tv 32,v2,30720
Mvvv 76,42,236
L_ v9,36,s65,128
Mvvv 80,38,220
s_nop 0
Tv 34,v3,24576
Tv 36,v3,30720
Mvvv 80,42,240
L_ v9,36,s65,3200
S_ m0,s51,14080
Mvvv 84,38,224
E_ 109
Mvvv 84,42,244
L_ v9,36,s65,256
Mvvv 88,38,228
E_ 110
Mvvv 88,42,248
L_ v9,36,s65,3328
Mvvv 92,38,232
E_ 111
Mvvv 92,42,252
W_ 0
C_ 111,110,111
C_ 110,108,109
Mavv 0,30,116
s_nop 0
Tv 38,v4,24576
Tv 40,v4,30720
Mavv 20,34,116
E_ 112
Mavv 24,34,120
s_nop 0
Tv 42,v5,24576
Tv 44,v5,30720
Mavv 4,30,120
E_ 113
Mavv 8,30,124
E_ 114
Mavv 28,34,124
E_ 115
Mavv 32,34,128
C_ 115,114,115
C_ 114,112,113
Mavv 12,30,128
E_ 76
Mavv 16,30,132
E_ 77
Mavv 36,34,132
W_ 0
E_ 78
Mavv 56,38,132
s_nop 0
Tv 30,v2,25600
Tv 32,v2,31744
Mavv 76,42,132
E_ 79
Mavv 72,42,128
s_nop 0
Tv 34,v3,25600
Tv 36,v3,31744
Mavv 52,38,128
C_ 96,76,77
C_ 97,78,79
Mavv 48,38,124
E_ 80
Mavv 68,42,124
E_ 81
Mavv 64,42,120
E_ 82
Mavv 44,38,120
E_ 83
Mavv 40,38,116
C_ 100,80,81
C_ 101,82,83
Mavv 60,42,116
W_ 0
E_ 84
Mavv 80,30,116
s_nop 0
Tv 38,v4,25600
Tv 40,v4,31744
Mavv 100,34,116
E_ 85
Mavv 104,34,120
s_nop 0
Tv 42,v5,25600
Tv 44,v5,31744
Mavv 84,30,120
E_ 86
Mavv 88,30,124
E_ 87
Mavv 108,34,124
C_ 104,84,85
C_ 105,86,87
Mavv 112,34,128
E_ 88
Mavv 92,30,128
E_ 89
Mavv 96,30,132
E_ 90
Mavv 116,34,132
W_ 0
E_ 91
Mavv 136,38,132
s_nop 0
Tv 30,v2,26624
Tv 32,v2,32768
Mavv 156,42,132
C_ 108,88,89
C_ 109,90,91
Mavv 152,42,128
s_nop 0
Tv 34,v3,26624
Tv 36,v3,32768
Mavv 132,38,128
E_ 92
Mavv 128,38,124
E_ 93
Mavv 148,42,124
E_ 94
Mavv 144,42,120
E_ 95
Mavv 124,38,120
C_ 112,92,93
C_ 113,94,95
Mavv 120,38,116
v_mov_b32 v92, 0x3f803f80
v_mov_b32 v93, 0x3f803f80
Mavv 140,42,116
W_ 0
v_mov_b32 v94, 0x3f803f80
v_mov_b32 v95, 0x3f803f80
Mavv 160,30,116
s_nop 0
Tv 38,v4,26624
Tv 40,v4,32768
Mavv 180,34,116
Mavv 184,34,120
s_nop 0
Tv 42,v5,26624
Tv 44,v5,32768
Mavv 164,30,120
Mavv 168,30,124
Mavv 188,34,124
S_ s65,s61,73728
Mavv 192,34,128
S_ m0,s51,24576
Mavv 172,30,128
s_waitcnt vmcnt(6) lgkmcnt(0)
s_barrier
Mavv 176,30,132
Ln v7,32,s65,0
Mavv 196,34,132
Mavv 216,38,132
Dv 30,%0,6144
Mavv 236,42,132
Dv 34,v1,6144
Mavv 232,42,128
Ln v8,32,s65,3072
S_ m0,s51,25472
Mavv 212,38,128
Mavv 208,38,124
Ln v7,32,s65,128
Mavv 228,42,124
Mavv 224,42,120
Ln v8,32,s65,3200
S_ m0,s51,26368
Mavv 204,38,120
Mavv 200,38,116
Ln v7,32,s65,256
Mavv 220,42,116
.Lstep2_5:
Zvvv 76,92,96
Dv 38,%0,7168
Zvvv 80,92,100
Dv 42,v1,7168
Zvvv 84,92,104
Zvvv 88,92,108
Zvvv 92,92,112
W_ 2
Nvvv 116,30,136,56
Mvvv 116,34,156
Nvvv 120,30,140,60
s_nop 1
v_max3_f32 v10, v76, v80, v84
s_nop 0
v_max3_f32 v10, v10, v88, v92
Mvvv 120,34,160
v_cmp_lt_f32 vcc, 0x42000000, v10
s_cbranch_vccnz .Ltramp2_4
Nvvv 124,30,144,64
A_ v46,v46,v76
A_ v47,v47,v80
Mvvv 124,34,164
A_ v48,v48,v84
A_ v49,v49,v88
Nvvv 128,30,148,68
A_ v50,v50,v92
Mvvv 128,34,168
Ln v8,32,s65,3328
Nvvv 132,30,152,72
Mvvv 132,34,172
W_ 0
Mvvv 132,38,192
Dv 30,%0,8192
Mvvv 132,42,212
Dv 34,v1,8192
Mvvv 128,38,188
Mvvv 128,42,208
Mvvv 124,38,184
Mvvv 124,42,204
Mvvv 120,38,180
Mvvv 120,42,200
Mvvv 116,38,176
Mvvv 116,42,196
W_ 0
Mvvv 116,30,216
Dv 38,%0,0
Mvvv 116,34,236
Dv 42,v1,0
Mvvv 120,30,220
Mvvv 120,34,240
Mvvv 124,30,224
Mvvv 124,34,244
Mvvv 128,30,228
Mvvv 128,34,248
s_nop 0
E_ 116
Mvvv 132,30,232
E_ 117
Mvvv 132,34,252
W_ 0
E_ 118
Nvvv 76,38,136,56
Dv 30,%0,1024
Mvvv 76,42,156
Dv 34,v1,1024
Nvvv 80,38,140,60
E_ 119
Mvvv 80,42,160
C_ 119,118,119
C_ 118,116,117
Nvvv 84,38,144,64
E_ 120
Mvvv 84,42,164
E_ 121
Nvvv 88,38,148,68
.Lpad_a2_3:
s_cbranch_execz .Lpad_a2_7
E_ 122
Mvvv 88,42,168
E_ 123
Nvvv 92,38,152,72
C_ 123,122,123
C_ 122,120,121
Mvvv 92,42,172
W_ 0
E_ 124
Mvvv 92,30,192
Dv 38,%0,2048
Mvvv 92,34,212
Dv 42,v1,2048
Mvvv 88,30,188
E_ 125
Mvvv 88,34,208
E_ 126
Mvvv 84,30,184
S_ s65,s61,61440
E_ 127
Mvvv 84,34,204
S_ m0,s51,0
C_ 127,126,127
C_ 126,124,125
Mvvv 80,30,180
s_waitcnt vmcnt(6) lgkmcnt(0)
s_barrier
Mvvv 80,34,200
L_ v9,36,s65,0
Mvvv 76,30,176
E_ 128
Mvvv 76,34,196
L_ v9,36,s65,3072
S_ m0,s51,896
Mvvv 76,38,216
Tv 30,v2,12288
Tv 32,v2,18432
Mvvv 76,42,236
L_ v9,36,s65,128
Mvvv 80,38,220
s_nop 0
Tv 34,v3,12288
Tv 36,v3,18432
Mvvv 80,42,240
L_ v9,36,s65,3200
S_ m0,s51,1792
Mvvv 84,38,224
E_ 129
Mvvv 84,42,244
L_ v9,36,s65,256
Mvvv 88,38,228
E_ 130
Mvvv 88,42,248
L_ v9,36,s65,3328
Mvvv 92,38,232
E_ 131
Mvvv 92,42,252
W_ 0
C_ 131,130,131
C_ 130,128,129
Mavv 0,30,96
s_nop 0
Tv 38,v4,12288
Tv 40,v4,18432
Mavv 20,34,96
E_ 132
Mavv 24,34,100
s_nop 0
Tv 42,v5,12288
Tv 44,v5,18432
Mavv 4,30,100
E_ 133
Mavv 8,30,104
E_ 134
Mavv 28,34,104
E_ 135
Mavv 32,34,108
C_ 135,134,135
C_ 134,132,133
Mavv 12,30,108
E_ 76
Mavv 16,30,112
E_ 77
Mavv 36,34,112
W_ 0
E_ 78
Mavv 56,38,112
s_nop 0
Tv 30,v2,13312
Tv 32,v2,19456
Mavv 76,42,112
E_ 79
Mavv 72,42,108
s_nop 0
Tv 34,v3,13312
Tv 36,v3,19456
Mavv 52,38,108
C_ 116,76,77
C_ 117,78,79
Mavv 48,38,104
E_ 80
Mavv 68,42,104
E_ 81
Mavv 64,42,100
E_ 82
Mavv 44,38,100
E_ 83
Mavv 40,38,96
C_ 120,80,81
C_ 121,82,83
Mavv 60,42,96
W_ 0
E_ 84
Mavv 80,30,96
s_nop 0
Tv 38,v4,13312
Tv 40,v4,19456
Mavv 100,34,96
E_ 85
Mavv 104,34,100
s_nop 0
Tv 42,v5,13312
Tv 44,v5,19456
Mavv 84,30,100
E_ 86
Mavv 88,30,104
E_ 87
Mavv 108,34,104
C_ 124,84,85
C_ 125,86,87
Mavv 112,34,108
E_ 88
Mavv 92,30,108
E_ 89
Mavv 96,30,112
E_ 90
Mavv 116,34,112
W_ 0
E_ 91
Mavv 136,38,112
s_nop 0
Tv 30,v2,14336
Tv 32,v2,20480
Mavv 156,42,112
C_ 128,88,89
C_ 129,90,91
Mavv 152,42,108
s_nop 0
Tv 34,v3,14336
Tv 36,v3,20480
Mavv 132,38,108
E_ 92
Mavv 128,38,104
E_ 93
Mavv 148,42,104
E_ 94
Mavv 144,42,100
E_ 95
Mavv 124,38,100
C_ 132,92,93
C_ 133,94,95
Mavv 120,38,96
v_mov_b32 v92, 0x3f803f80
v_mov_b32 v93, 0x3f803f80
Mavv 140,42,96
W_ 0
v_mov_b32 v94, 0x3f803f80
v_mov_b32 v95, 0x3f803f80
Mavv 160,30,96
s_nop 0
Tv 38,v4,14336
Tv 40,v4,20480
Mavv 180,34,96
Mavv 184,34,100
s_nop 0
Tv 42,v5,14336
Tv 44,v5,20480
Mavv 164,30,100
Mavv 168,30,104
Mavv 188,34,104
S_ s65,s61,86016
Mavv 192,34,108
S_ m0,s51,12288
Mavv 172,30,108
s_waitcnt vmcnt(6) lgkmcnt(0)
s_barrier
Mavv 176,30,112
Ln v7,32,s65,0
Mavv 196,34,112
Mavv 216,38,112
Dv 30,%0,30720
Mavv 236,42,112
Dv 34,v1,30720
Mavv 232,42,108
Ln v8,32,s65,3072
S_ m0,s51,13184
Mavv 212,38,108
Mavv 208,38,104
Ln v7,32,s65,128
Mavv 228,42,104
Mavv 224,42,100
Ln v8,32,s65,3200
S_ m0,s51,14080
Mavv 204,38,100
Mavv 200,38,96
Ln v7,32,s65,256
Mavv 220,42,96
.Lstep2_6:
Zvvv 76,92,116
Dv 38,%0,31744
Zvvv 80,92,120
Dv 42,v1,31744
Zvvv 84,92,124
Zvvv 88,92,128
Zvvv 92,92,132
W_ 2
Nvvv 96,30,136,56
Mvvv 96,34,156
Nvvv 100,30,140,60
s_nop 1
v_max3_f32 v10, v76, v80, v84
s_nop 0
v_max3_f32 v10, v10, v88, v92
Mvvv 100,34,160
v_cmp_lt_f32 vcc, 0x42000000, v10
s_cbranch_vccnz .Ltramp2_5
Nvvv 104,30,144,64
A_ v46,v46,v76
A_ v47,v47,v80
Mvvv 104,34,164
A_ v48,v48,v84
A_ v49,v49,v88
Nvvv 108,30,148,68
A_ v50,v50,v92
Mvvv 108,34,168
Ln v8,32,s65,3328
Nvvv 112,30,152,72
R_ v11,a248
Mvvv 112,34,172
W_ 0
v_min_u32 v11, 240, v11
Mvvv 112,38,192
Dv 30,%0,32768
v_lshlrev_b32 v11, 3, v11
Mvvv 112,42,212
Dv 34,v1,32768
Mvvv 108,38,188
buffer_load_dword a254, v11, s[44:47], s66 offen
Mvvv 108,42,208
Mvvv 104,38,184
Mvvv 104,42,204
Mvvv 100,38,180
Mvvv 100,42,200
Mvvv 96,38,176
Mvvv 96,42,196
W_ 0
Mvvv 96,30,216
Dv 38,%0,24576
Mvvv 96,34,236
Dv 42,v1,24576
Mvvv 100,30,220
Mvvv 100,34,240
Mvvv 104,30,224
Mvvv 104,34,244
Mvvv 108,30,228
Mvvv 108,34,248
s_nop 0
E_ 96
Mvvv 112,30,232
E_ 97
Mvvv 112,34,252
W_ 0
E_ 98
Nvvv 76,38,136,56
Dv 30,%0,25600
Mvvv 76,42,156
Dv 34,v1,25600
Nvvv 80,38,140,60
E_ 99
Mvvv 80,42,160
C_ 99,98,99
C_ 98,96,97
Nvvv 84,38,144,64
E_ 100
Mvvv 84,42,164
E_ 101
Nvvv 88,38,148,68
E_ 102
Mvvv 88,42,168
E_ 103
Nvvv 92,38,152,72
C_ 103,102,103
C_ 102,100,101
Mvvv 92,42,172
W_ 0
E_ 104
Mvvv 92,30,192
Dv 38,%0,26624
Mvvv 92,34,212
Dv 42,v1,26624
Mvvv 88,30,188
E_ 105
Mvvv 88,34,208
E_ 106
Mvvv 84,30,184
S_ s65,s61,73728
E_ 107
Mvvv 84,34,204
S_ m0,s51,24576
C_ 107,106,107
C_ 106,104,105
Mvvv 80,30,180
s_waitcnt vmcnt(7) lgkmcnt(0)
s_barrier
Mvvv 80,34,200
L_ v9,36,s65,0
Mvvv 76,30,176
E_ 108
Mvvv 76,34,196
L_ v9,36,s65,3072
S_ m0,s51,25472
Mvvv 76,38,216
Tv 30,v2,0
Tv 32,v2,6144
Mvvv 76,42,236
L_ v9,36,s65,128
Mvvv 80,38,220
s_nop 0
Tv 34,v3,0
Tv 36,v3,6144
Mvvv 80,42,240
L_ v9,36,s65,3200
S_ m0,s51,26368
Mvvv 84,38,224
E_ 109
Mvvv 84,42,244
L_ v9,36,s65,256
Mvvv 88,38,228
E_ 110
Mvvv 88,42,248
L_ v9,36,s65,3328
Mvvv 92,38,232
E_ 111
Mvvv 92,42,252
W_ 0
C_ 111,110,111
C_ 110,108,109
Mavv 0,30,116
s_nop 0
Tv 38,v4,0
Tv 40,v4,6144
Mavv 20,34,116
E_ 112
Mavv 24,34,120
s_nop 0
Tv 42,v5,0
Tv 44,v5,6144
Mavv 4,30,120
E_ 113
Mavv 8,30,124
E_ 114
Mavv 28,34,124
E_ 115
Mavv 32,34,128
C_ 115,114,115
C_ 114,112,113
Mavv 12,30,128
E_ 76
Mavv 16,30,132
E_ 77
Mavv 36,34,132
W_ 0
E_ 78
Mavv 56,38,132
s_nop 0
Tv 30,v2,1024
Tv 32,v2,7168
Mavv 76,42,132
E_ 79
Mavv 72,42,128
s_nop 0
Tv 34,v3,1024
Tv 36,v3,7168
Mavv 52,38,128
C_ 96,76,77
C_ 97,78,79
Mavv 48,38,124
E_ 80
Mavv 68,42,124
E_ 81
Mavv 64,42,120
E_ 82
Mavv 44,38,120
E_ 83
Mavv 40,38,116
C_ 100,80,81
C_ 101,82,83
Mavv 60,42,116
W_ 0
E_ 84
Mavv 80,30,116
s_nop 0
Tv 38,v4,1024
Tv 40,v4,7168
Mavv 100,34,116
E_ 85
Mavv 104,34,120
s_nop 0
Tv 42,v5,1024
Tv 44,v5,7168
Mavv 84,30,120
E_ 86
Mavv 88,30,124
E_ 87
Mavv 108,34,124
C_ 104,84,85
C_ 105,86,87
Mavv 112,34,128
E_ 88
Mavv 92,30,128
E_ 89
Mavv 96,30,132
E_ 90
Mavv 116,34,132
W_ 0
E_ 91
Mavv 136,38,132
s_nop 0
Tv 30,v2,2048
Tv 32,v2,8192
Mavv 156,42,132
C_ 108,88,89
C_ 109,90,91
Mavv 152,42,128
s_nop 0
Tv 34,v3,2048
Tv 36,v3,8192
Mavv 132,38,128
E_ 92
Mavv 128,38,124
E_ 93
Mavv 148,42,124
E_ 94
Mavv 144,42,120
E_ 95
Mavv 124,38,120
C_ 112,92,93
C_ 113,94,95
Mavv 120,38,116
v_mov_b32 v92, 0x3f803f80
v_mov_b32 v93, 0x3f803f80
Mavv 140,42,116
W_ 0
v_mov_b32 v94, 0x3f803f80
v_mov_b32 v95, 0x3f803f80
Mavv 160,30,116
s_nop 0
Tv 38,v4,2048
Tv 40,v4,8192
Mavv 180,34,116
Mavv 184,34,120
s_nop 0
Tv 42,v5,2048
Tv 44,v5,8192
Mavv 164,30,120
Mavv 168,30,124
Mavv 188,34,124
S_ s65,s61,98304
Mavv 192,34,128
S_ m0,s51,0
Mavv 172,30,128
s_waitcnt vmcnt(7) lgkmcnt(0)
s_barrier
Mavv 176,30,132
Ln v7,32,s65,0
Mavv 196,34,132
Mavv 216,38,132
Dv 30,%0,18432
Mavv 236,42,132
Dv 34,v1,18432
Mavv 232,42,128
Ln v8,32,s65,3072
S_ m0,s51,896
Mavv 212,38,128
Mavv 208,38,124
Ln v7,32,s65,128
Mavv 228,42,124
Mavv 224,42,120
Ln v8,32,s65,3200
S_ m0,s51,1792
Mavv 204,38,120
Mavv 200,38,116
Ln v7,32,s65,256
Mavv 220,42,116
s_cmp_eq_u32 s4, 0
s_cbranch_scc1 .Lhs_c2
.Lhs_c2_back:
.Lstep2_7:
Zvvv 76,92,96
Dv 38,%0,19456
Zvvv 80,92,100
Dv 42,v1,19456
Zvvv 84,92,104
Zvvv 88,92,108
Zvvv 92,92,112
W_ 2
Nvvv 116,30,136,56
Mvvv 116,34,156
Nvvv 120,30,140,60
s_nop 1
v_max3_f32 v10, v76, v80, v84
s_nop 0
v_max3_f32 v10, v10, v88, v92
Mvvv 120,34,160
v_cmp_lt_f32 vcc, 0x42000000, v10
s_cbranch_vccnz .Ltramp2_6
Nvvv 124,30,144,64
A_ v46,v46,v76
A_ v47,v47,v80
Mvvv 124,34,164
A_ v48,v48,v84
A_ v49,v49,v88
Nvvv 128,30,148,68
A_ v50,v50,v92
Mvvv 128,34,168
Ln v8,32,s65,3328
Nvvv 132,30,152,72
Mvvv 132,34,172
W_ 0
Mvvv 132,38,192
Dv 30,%0,20480
Mvvv 132,42,212
Dv 34,v1,20480
Mvvv 128,38,188
Mvvv 128,42,208
Mvvv 124,38,184
Mvvv 124,42,204
Mvvv 120,38,180
Mvvv 120,42,200
Mvvv 116,38,176
Mvvv 116,42,196
W_ 0
Mvvv 116,30,216
Dv 38,%0,12288
Mvvv 116,34,236
Dv 42,v1,12288
Mvvv 120,30,220
Mvvv 120,34,240
Mvvv 124,30,224
Mvvv 124,34,244
Mvvv 128,30,228
Mvvv 128,34,248
s_nop 0
E_ 116
Mvvv 132,30,232
E_ 117
Mvvv 132,34,252
W_ 0
E_ 118
Nvvv 76,38,136,56
Dv 30,%0,13312
Mvvv 76,42,156
Dv 34,v1,13312
Nvvv 80,38,140,60
E_ 119
Mvvv 80,42,160
C_ 119,118,119
C_ 118,116,117
Nvvv 84,38,144,64
E_ 120
Mvvv 84,42,164
E_ 121
Nvvv 88,38,148,68
E_ 122
Mvvv 88,42,168
E_ 123
Nvvv 92,38,152,72
C_ 123,122,123
C_ 122,120,121
Mvvv 92,42,172
W_ 0
E_ 124
Mvvv 92,30,192
Dv 38,%0,14336
Mvvv 92,34,212
Dv 42,v1,14336
Mvvv 88,30,188
E_ 125
Mvvv 88,34,208
E_ 126
Mvvv 84,30,184
S_ s65,s61,86016
E_ 127
Mvvv 84,34,204
S_ m0,s51,12288
C_ 127,126,127
C_ 126,124,125
Mvvv 80,30,180
s_waitcnt vmcnt(6) lgkmcnt(0)
s_barrier
Mvvv 80,34,200
L_ v9,36,s65,0
.Lpad_a2_4:
s_cbranch_execz .Lhop_a2_r
Mvvv 76,30,176
E_ 128
Mvvv 76,34,196
L_ v9,36,s65,3072
S_ m0,s51,13184
Mvvv 76,38,216
Tv 30,v2,24576
Tv 32,v2,30720
Mvvv 76,42,236
L_ v9,36,s65,128
Mvvv 80,38,220
s_nop 0
Tv 34,v3,24576
Tv 36,v3,30720
Mvvv 80,42,240
L_ v9,36,s65,3200
S_ m0,s51,14080
Mvvv 84,38,224
E_ 129
Mvvv 84,42,244
L_ v9,36,s65,256
Mvvv 88,38,228
E_ 130
Mvvv 88,42,248
L_ v9,36,s65,3328
Mvvv 92,38,232
E_ 131
Mvvv 92,42,252
W_ 0
C_ 131,130,131
C_ 130,128,129
Mavv 0,30,96
s_nop 0
Tv 38,v4,24576
Tv 40,v4,30720
Mavv 20,34,96
E_ 132
Mavv 24,34,100
s_nop 0
Tv 42,v5,24576
Tv 44,v5,30720
Mavv 4,30,100
E_ 133
Mavv 8,30,104
E_ 134
Mavv 28,34,104
E_ 135
Mavv 32,34,108
C_ 135,134,135
C_ 134,132,133
Mavv 12,30,108
E_ 76
Mavv 16,30,112
E_ 77
Mavv 36,34,112
W_ 0
E_ 78
Mavv 56,38,112
s_nop 0
Tv 30,v2,25600
Tv 32,v2,31744
Mavv 76,42,112
E_ 79
Mavv 72,42,108
s_nop 0
Tv 34,v3,25600
Tv 36,v3,31744
Mavv 52,38,108
C_ 116,76,77
C_ 117,78,79
Mavv 48,38,104
E_ 80
Mavv 68,42,104
E_ 81
Mavv 64,42,100
E_ 82
Mavv 44,38,100
E_ 83
Mavv 40,38,96
C_ 120,80,81
C_ 121,82,83
Mavv 60,42,96
W_ 0
E_ 84
Mavv 80,30,96
s_nop 0
Tv 38,v4,25600
Tv 40,v4,31744
Mavv 100,34,96
E_ 85
Mavv 104,34,100
s_nop 0
Tv 42,v5,25600
Tv 44,v5,31744
Mavv 84,30,100
)A"
R"A(E_ 86
Mavv 88,30,104
E_ 87
Mavv 108,34,104
C_ 124,84,85
C_ 125,86,87
Mavv 112,34,108
E_ 88
Mavv 92,30,108
E_ 89
Mavv 96,30,112
E_ 90
Mavv 116,34,112
W_ 0
E_ 91
Mavv 136,38,112
s_nop 0
Tv 30,v2,26624
Tv 32,v2,32768
Mavv 156,42,112
C_ 128,88,89
C_ 129,90,91
Mavv 152,42,108
s_nop 0
Tv 34,v3,26624
Tv 36,v3,32768
Mavv 132,38,108
E_ 92
Mavv 128,38,104
E_ 93
Mavv 148,42,104
E_ 94
Mavv 144,42,100
E_ 95
Mavv 124,38,100
C_ 132,92,93
C_ 133,94,95
Mavv 120,38,96
v_mov_b32 v92, 0x3f803f80
v_mov_b32 v93, 0x3f803f80
Mavv 140,42,96
W_ 0
v_mov_b32 v94, 0x3f803f80
v_mov_b32 v95, 0x3f803f80
Mavv 160,30,96
s_nop 0
Tv 38,v4,26624
Tv 40,v4,32768
Mavv 180,34,96
Mavv 184,34,100
s_nop 0
Tv 42,v5,26624
Tv 44,v5,32768
Mavv 164,30,100
Mavv 168,30,104
Mavv 188,34,104
S_ s65,s61,110592
Mavv 192,34,108
S_ m0,s51,24576
Mavv 172,30,108
s_waitcnt vmcnt(6) lgkmcnt(0)
s_barrier
Mavv 176,30,112
Ln v7,32,s65,0
Mavv 196,34,112
Mavv 216,38,112
Dv 30,%0,6144
Mavv 236,42,112
Dv 34,v1,6144
Mavv 232,42,108
Ln v8,32,s65,3072
S_ m0,s51,25472
Mavv 212,38,108
Mavv 208,38,104
Ln v7,32,s65,128
Mavv 228,42,104
Mavv 224,42,100
Ln v8,32,s65,3200
S_ m0,s51,26368
Mavv 204,38,100
Mavv 200,38,96
Ln v7,32,s65,256
Mavv 220,42,96
.Lstep2_8:
Zvvv 76,92,116
Dv 38,%0,7168
Zvvv 80,92,120
Dv 42,v1,7168
Zvvv 84,92,124
Zvvv 88,92,128
Zvvv 92,92,132
W_ 2
Nvvv 96,30,136,56
Mvvv 96,34,156
Nvvv 100,30,140,60
s_nop 1
v_max3_f32 v10, v76, v80, v84
s_nop 0
v_max3_f32 v10, v10, v88, v92
Mvvv 100,34,160
v_cmp_lt_f32 vcc, 0x42000000, v10
s_cbranch_vccnz .Ltramp2_7
Nvvv 104,30,144,64
A_ v46,v46,v76
A_ v47,v47,v80
Mvvv 104,34,164
A_ v48,v48,v84
A_ v49,v49,v88
Nvvv 108,30,148,68
A_ v50,v50,v92
Mvvv 108,34,168
Ln v8,32,s65,3328
Nvvv 112,30,152,72
Mvvv 112,34,172
W_ 0
Mvvv 112,38,192
Dv 30,%0,8192
Mvvv 112,42,212
Dv 34,v1,8192
Mvvv 108,38,188
Mvvv 108,42,208
Mvvv 104,38,184
Mvvv 104,42,204
Mvvv 100,38,180
Mvvv 100,42,200
Mvvv 96,38,176
Mvvv 96,42,196
W_ 0
Mvvv 96,30,216
Dv 38,%0,0
Mvvv 96,34,236
Dv 42,v1,0
Mvvv 100,30,220
Mvvv 100,34,240
Mvvv 104,30,224
Mvvv 104,34,244
Mvvv 108,30,228
Mvvv 108,34,248
s_nop 0
E_ 96
Mvvv 112,30,232
E_ 97
Mvvv 112,34,252
W_ 0
E_ 98
Nvvv 76,38,136,56
Dv 30,%0,1024
Mvvv 76,42,156
Dv 34,v1,1024
Nvvv 80,38,140,60
E_ 99
Mvvv 80,42,160
C_ 99,98,99
C_ 98,96,97
Nvvv 84,38,144,64
E_ 100
Mvvv 84,42,164
E_ 101
Nvvv 88,38,148,68
E_ 102
Mvvv 88,42,168
E_ 103
Nvvv 92,38,152,72
C_ 103,102,103
C_ 102,100,101
Mvvv 92,42,172
W_ 0
E_ 104
Mvvv 92,30,192
Dv 38,%0,2048
Mvvv 92,34,212
Dv 42,v1,2048
Mvvv 88,30,188
E_ 105
Mvvv 88,34,208
E_ 106
Mvvv 84,30,184
S_ s65,s61,98304
E_ 107
Mvvv 84,34,204
S_ m0,s51,0
C_ 107,106,107
C_ 106,104,105
Mvvv 80,30,180
s_waitcnt vmcnt(6) lgkmcnt(0)
s_barrier
Mvvv 80,34,200
L_ v9,36,s65,0
Mvvv 76,30,176
E_ 108
Mvvv 76,34,196
L_ v9,36,s65,3072
S_ m0,s51,896
Mvvv 76,38,216
Tv 30,v2,12288
Tv 32,v2,18432
Mvvv 76,42,236
L_ v9,36,s65,128
Mvvv 80,38,220
s_nop 0
Tv 34,v3,12288
Tv 36,v3,18432
Mvvv 80,42,240
L_ v9,36,s65,3200
S_ m0,s51,1792
Mvvv 84,38,224
E_ 109
Mvvv 84,42,244
L_ v9,36,s65,256
Mvvv 88,38,228
E_ 110
Mvvv 88,42,248
L_ v9,36,s65,3328
Mvvv 92,38,232
E_ 111
Mvvv 92,42,252
W_ 0
C_ 111,110,111
C_ 110,108,109
Mavv 0,30,116
s_nop 0
Tv 38,v4,12288
Tv 40,v4,18432
Mavv 20,34,116
E_ 112
Mavv 24,34,120
s_nop 0
Tv 42,v5,12288
Tv 44,v5,18432
Mavv 4,30,120
E_ 113
Mavv 8,30,124
E_ 114
Mavv 28,34,124
E_ 115
Mavv 32,34,128
C_ 115,114,115
C_ 114,112,113
Mavv 12,30,128
E_ 76
Mavv 16,30,132
E_ 77
Mavv 36,34,132
W_ 0
E_ 78
Mavv 56,38,132
s_nop 0
Tv 30,v2,13312
Tv 32,v2,19456
Mavv 76,42,132
E_ 79
Mavv 72,42,128
s_nop 0
Tv 34,v3,13312
Tv 36,v3,19456
Mavv 52,38,128
C_ 96,76,77
C_ 97,78,79
Mavv 48,38,124
E_ 80
Mavv 68,42,124
E_ 81
Mavv 64,42,120
E_ 82
Mavv 44,38,120
E_ 83
Mavv 40,38,116
C_ 100,80,81
C_ 101,82,83
Mavv 60,42,116
W_ 0
E_ 84
Mavv 80,30,116
s_nop 0
Tv 38,v4,13312
Tv 40,v4,19456
Mavv 100,34,116
E_ 85
Mavv 104,34,120
s_nop 0
Tv 42,v5,13312
Tv 44,v5,19456
Mavv 84,30,120
E_ 86
Mavv 88,30,124
E_ 87
Mavv 108,34,124
C_ 104,84,85
C_ 105,86,87
Mavv 112,34,128
E_ 88
Mavv 92,30,128
E_ 89
Mavv 96,30,132
E_ 90
Mavv 116,34,132
W_ 0
E_ 91
Mavv 136,38,132
s_nop 0
Tv 30,v2,14336
Tv 32,v2,20480
Mavv 156,42,132
C_ 108,88,89
C_ 109,90,91
Mavv 152,42,128
s_nop 0
Tv 34,v3,14336
Tv 36,v3,20480
Mavv 132,38,128
E_ 92
Mavv 128,38,124
E_ 93
Mavv 148,42,124
E_ 94
Mavv 144,42,120
E_ 95
Mavv 124,38,120
C_ 112,92,93
C_ 113,94,95
Mavv 120,38,116
v_mov_b32 v92, 0x3f803f80
v_mov_b32 v93, 0x3f803f80
Mavv 140,42,116
W_ 0
v_mov_b32 v94, 0x3f803f80
v_mov_b32 v95, 0x3f803f80
Mavv 160,30,116
s_nop 0
Tv 38,v4,14336
Tv 40,v4,20480
Mavv 180,34,116
Mavv 184,34,120
s_nop 0
Tv 42,v5,14336
Tv 44,v5,20480
Mavv 164,30,120
Mavv 168,30,124
Mavv 188,34,124
S_ s65,s61,122880
Mavv 192,34,128
S_ m0,s51,12288
Mavv 172,30,128
s_waitcnt vmcnt(6) lgkmcnt(0)
s_barrier
Mavv 176,30,132
Ln v7,32,s65,0
Mavv 196,34,132
Mavv 216,38,132
Dv 30,%0,30720
Mavv 236,42,132
Dv 34,v1,30720
Mavv 232,42,128
Ln v8,32,s65,3072
S_ m0,s51,13184
Mavv 212,38,128
Mavv 208,38,124
Ln v7,32,s65,128
Mavv 228,42,124
Mavv 224,42,120
Ln v8,32,s65,3200
S_ m0,s51,14080
Mavv 204,38,120
Mavv 200,38,116
Ln v7,32,s65,256
Mavv 220,42,116
s_cmp_eq_u32 s4, 0
s_cbranch_scc1 .Lhs_d2
.Lhs_d2_back:
.Lstep2_9:
Zvvv 76,92,96
Dv 38,%0,31744
Zvvv 80,92,100
Dv 42,v1,31744
Zvvv 84,92,104
Zvvv 88,92,108
Zvvv 92,92,112
W_ 2
Nvvv 116,30,136,56
Mvvv 116,34,156
Nvvv 120,30,140,60
s_nop 1
v_max3_f32 v10, v76, v80, v84
s_nop 0
v_max3_f32 v10, v10, v88, v92
Mvvv 120,34,160
v_cmp_lt_f32 vcc, 0x42000000, v10
s_cbranch_vccnz .Ltramp2_8
Nvvv 124,30,144,64
A_ v46,v46,v76
A_ v47,v47,v80
Mvvv 124,34,164
A_ v48,v48,v84
A_ v49,v49,v88
Nvvv 128,30,148,68
A_ v50,v50,v92
Mvvv 128,34,168
Ln v8,32,s65,3328
Nvvv 132,30,152,72
Mvvv 132,34,172
W_ 0
Mvvv 132,38,192
Dv 30,%0,32768
Mvvv 132,42,212
Dv 34,v1,32768
Mvvv 128,38,188
Mvvv 128,42,208
Mvvv 124,38,184
Mvvv 124,42,204
Mvvv 120,38,180
Mvvv 120,42,200
Mvvv 116,38,176
Mvvv 116,42,196
W_ 0
Mvvv 116,30,216
Dv 38,%0,24576
Mvvv 116,34,236
Dv 42,v1,24576
Mvvv 120,30,220
Mvvv 120,34,240
Mvvv 124,30,224
Mvvv 124,34,244
Mvvv 128,30,228
Mvvv 128,34,248
s_nop 0
E_ 116
Mvvv 132,30,232
E_ 117
Mvvv 132,34,252
W_ 0
E_ 118
Nvvv 76,38,136,56
Dv 30,%0,25600
Mvvv 76,42,156
Dv 34,v1,25600
Nvvv 80,38,140,60
E_ 119
Mvvv 80,42,160
C_ 119,118,119
C_ 118,116,117
Nvvv 84,38,144,64
E_ 120
Mvvv 84,42,164
E_ 121
Nvvv 88,38,148,68
E_ 122
Mvvv 88,42,168
E_ 123
Nvvv 92,38,152,72
C_ 123,122,123
C_ 122,120,121
Mvvv 92,42,172
W_ 0
E_ 124
Mvvv 92,30,192
Dv 38,%0,26624
Mvvv 92,34,212
Dv 42,v1,26624
Mvvv 88,30,188
E_ 125
Mvvv 88,34,208
E_ 126
Mvvv 84,30,184
S_ s65,s61,110592
E_ 127
Mvvv 84,34,204
S_ m0,s51,24576
C_ 127,126,127
C_ 126,124,125
Mvvv 80,30,180
s_waitcnt vmcnt(6) lgkmcnt(0)
s_barrier
Mvvv 80,34,200
L_ v9,36,s65,0
Mvvv 76,30,176
E_ 128
Mvvv 76,34,196
L_ v9,36,s65,3072
S_ m0,s51,25472
Mvvv 76,38,216
Tv 30,v2,0
Tv 32,v2,6144
Mvvv 76,42,236
L_ v9,36,s65,128
Mvvv 80,38,220
s_nop 0
Tv 34,v3,0
Tv 36,v3,6144
Mvvv 80,42,240
L_ v9,36,s65,3200
S_ m0,s51,26368
Mvvv 84,38,224
.Lpad_a2_5:
s_cbranch_execz .Lhop_a2_r
E_ 129
Mvvv 84,42,244
L_ v9,36,s65,256
Mvvv 88,38,228
E_ 130
Mvvv 88,42,248
L_ v9,36,s65,3328
Mvvv 92,38,232
E_ 131
Mvvv 92,42,252
W_ 0
C_ 131,130,131
C_ 130,128,129
Mavv 0,30,96
s_nop 0
Tv 38,v4,0
Tv 40,v4,6144
Mavv 20,34,96
E_ 132
Mavv 24,34,100
s_nop 0
Tv 42,v5,0
Tv 44,v5,6144
Mavv 4,30,100
E_ 133
Mavv 8,30,104
E_ 134
Mavv 28,34,104
E_ 135
Mavv 32,34,108
C_ 135,134,135
C_ 134,132,133
Mavv 12,30,108
E_ 76
Mavv 16,30,112
E_ 77
Mavv 36,34,112
W_ 0
E_ 78
Mavv 56,38,112
s_nop 0
Tv 30,v2,1024
Tv 32,v2,7168
Mavv 76,42,112
E_ 79
Mavv 72,42,108
s_nop 0
Tv 34,v3,1024
Tv 36,v3,7168
Mavv 52,38,108
C_ 116,76,77
C_ 117,78,79
Mavv 48,38,104
E_ 80
Mavv 68,42,104
E_ 81
Mavv 64,42,100
E_ 82
Mavv 44,38,100
E_ 83
Mavv 40,38,96
C_ 120,80,81
C_ 121,82,83
Mavv 60,42,96
W_ 0
E_ 84
Mavv 80,30,96
s_nop 0
Tv 38,v4,1024
Tv 40,v4,7168
Mavv 100,34,96
E_ 85
Mavv 104,34,100
s_nop 0
Tv 42,v5,1024
Tv 44,v5,7168
Mavv 84,30,100
E_ 86
Mavv 88,30,104
E_ 87
Mavv 108,34,104
C_ 124,84,85
C_ 125,86,87
Mavv 112,34,108
E_ 88
Mavv 92,30,108
E_ 89
Mavv 96,30,112
E_ 90
Mavv 116,34,112
W_ 0
E_ 91
Mavv 136,38,112
s_nop 0
Tv 30,v2,2048
Tv 32,v2,8192
Mavv 156,42,112
C_ 128,88,89
C_ 129,90,91
Mavv 152,42,108
s_nop 0
Tv 34,v3,2048
Tv 36,v3,8192
Mavv 132,38,108
E_ 92
Mavv 128,38,104
E_ 93
Mavv 148,42,104
E_ 94
Mavv 144,42,100
E_ 95
Mavv 124,38,100
C_ 132,92,93
C_ 133,94,95
Mavv 120,38,96
v_mov_b32 v92, 0x3f803f80
v_mov_b32 v93, 0x3f803f80
Mavv 140,42,96
W_ 0
v_mov_b32 v94, 0x3f803f80
v_mov_b32 v95, 0x3f803f80
Mavv 160,30,96
s_nop 0
Tv 38,v4,2048
Tv 40,v4,8192
Mavv 180,34,96
Mavv 184,34,100
s_nop 0
Tv 42,v5,2048
Tv 44,v5,8192
Mavv 164,30,100
Mavv 168,30,104
Mavv 188,34,104
S_ s65,s61,135168
Mavv 192,34,108
S_ m0,s51,0
Mavv 172,30,108
s_waitcnt vmcnt(6) lgkmcnt(0)
s_barrier
Mavv 176,30,112
Ln v7,32,s65,0
Mavv 196,34,112
Mavv 216,38,112
Dv 30,%0,18432
Mavv 236,42,112
Dv 34,v1,18432
Mavv 232,42,108
Ln v8,32,s65,3072
S_ m0,s51,896
Mavv 212,38,108
Mavv 208,38,104
Ln v7,32,s65,128
Mavv 228,42,104
Mavv 224,42,100
Ln v8,32,s65,3200
S_ m0,s51,1792
Mavv 204,38,100
Mavv 200,38,96
Ln v7,32,s65,256
Mavv 220,42,96
.Lstep2_10:
Zvvv 76,92,116
Dv 38,%0,19456
Zvvv 80,92,120
Dv 42,v1,19456
Zvvv 84,92,124
Zvvv 88,92,128
Zvvv 92,92,132
W_ 2
Nvvv 96,30,136,56
Mvvv 96,34,156
Nvvv 100,30,140,60
s_nop 1
v_max3_f32 v10, v76, v80, v84
s_nop 0
v_max3_f32 v10, v10, v88, v92
Mvvv 100,34,160
v_cmp_lt_f32 vcc, 0x42000000, v10
s_cbranch_vccnz .Ltramp2_9
Nvvv 104,30,144,64
A_ v46,v46,v76
A_ v47,v47,v80
Mvvv 104,34,164
A_ v48,v48,v84
A_ v49,v49,v88
Nvvv 108,30,148,68
A_ v50,v50,v92
Mvvv 108,34,168
Ln v8,32,s65,3328
Nvvv 112,30,152,72
Mvvv 112,34,172
W_ 0
Mvvv 112,38,192
Dv 30,%0,20480
Mvvv 112,42,212
Dv 34,v1,20480
Mvvv 108,38,188
Mvvv 108,42,208
Mvvv 104,38,184
Mvvv 104,42,204
Mvvv 100,38,180
Mvvv 100,42,200
Mvvv 96,38,176
Mvvv 96,42,196
W_ 0
Mvvv 96,30,216
Dv 38,%0,12288
Mvvv 96,34,236
Dv 42,v1,12288
Mvvv 100,30,220
Mvvv 100,34,240
Mvvv 104,30,224
Mvvv 104,34,244
Mvvv 108,30,228
Mvvv 108,34,248
s_nop 0
E_ 96
Mvvv 112,30,232
E_ 97
Mvvv 112,34,252
W_ 0
E_ 98
Nvvv 76,38,136,56
Dv 30,%0,13312
Mvvv 76,42,156
Dv 34,v1,13312
Nvvv 80,38,140,60
E_ 99
Mvvv 80,42,160
C_ 99,98,99
C_ 98,96,97
Nvvv 84,38,144,64
E_ 100
Mvvv 84,42,164
E_ 101
Nvvv 88,38,148,68
E_ 102
Mvvv 88,42,168
E_ 103
Nvvv 92,38,152,72
C_ 103,102,103
C_ 102,100,101
Mvvv 92,42,172
W_ 0
E_ 104
Mvvv 92,30,192
Dv 38,%0,14336
Mvvv 92,34,212
Dv 42,v1,14336
Mvvv 88,30,188
E_ 105
Mvvv 88,34,208
E_ 106
Mvvv 84,30,184
S_ s65,s61,122880
E_ 107
Mvvv 84,34,204
S_ m0,s51,12288
C_ 107,106,107
C_ 106,104,105
Mvvv 80,30,180
s_waitcnt vmcnt(6) lgkmcnt(0)
s_barrier
Mvvv 80,34,200
L_ v9,36,s65,0
Mvvv 76,30,176
E_ 108
Mvvv 76,34,196
L_ v9,36,s65,3072
S_ m0,s51,13184
Mvvv 76,38,216
Tv 30,v2,24576
Tv 32,v2,30720
Mvvv 76,42,236
L_ v9,36,s65,128
Mvvv 80,38,220
s_nop 0
Tv 34,v3,24576
Tv 36,v3,30720
Mvvv 80,42,240
L_ v9,36,s65,3200
S_ m0,s51,14080
Mvvv 84,38,224
E_ 109
Mvvv 84,42,244
L_ v9,36,s65,256
Mvvv 88,38,228
E_ 110
Mvvv 88,42,248
L_ v9,36,s65,3328
Mvvv 92,38,232
E_ 111
Mvvv 92,42,252
W_ 0
C_ 111,110,111
C_ 110,108,109
Mavv 0,30,116
s_nop 0
Tv 38,v4,24576
Tv 40,v4,30720
Mavv 20,34,116
E_ 112
Mavv 24,34,120
s_nop 0
Tv 42,v5,24576
Tv 44,v5,30720
Mavv 4,30,120
E_ 113
Mavv 8,30,124
E_ 114
Mavv 28,34,124
E_ 115
Mavv 32,34,128
C_ 115,114,115
C_ 114,112,113
Mavv 12,30,128
E_ 76
Mavv 16,30,132
E_ 77
Mavv 36,34,132
W_ 0
E_ 78
Mavv 56,38,132
s_nop 0
Tv 30,v2,25600
Tv 32,v2,31744
Mavv 76,42,132
E_ 79
Mavv 72,42,128
s_nop 0
Tv 34,v3,25600
Tv 36,v3,31744
Mavv 52,38,128
C_ 96,76,77
C_ 97,78,79
Mavv 48,38,124
E_ 80
Mavv 68,42,124
E_ 81
Mavv 64,42,120
E_ 82
Mavv 44,38,120
E_ 83
Mavv 40,38,116
C_ 100,80,81
C_ 101,82,83
Mavv 60,42,116
W_ 0
E_ 84
Mavv 80,30,116
s_nop 0
Tv 38,v4,25600
Tv 40,v4,31744
Mavv 100,34,116
E_ 85
Mavv 104,34,120
s_nop 0
Tv 42,v5,25600
Tv 44,v5,31744
Mavv 84,30,120
E_ 86
Mavv 88,30,124
E_ 87
Mavv 108,34,124
C_ 104,84,85
C_ 105,86,87
Mavv 112,34,128
E_ 88
Mavv 92,30,128
E_ 89
Mavv 96,30,132
E_ 90
Mavv 116,34,132
W_ 0
E_ 91
Mavv 136,38,132
s_nop 0
Tv 30,v2,26624
Tv 32,v2,32768
Mavv 156,42,132
C_ 108,88,89
C_ 109,90,91
Mavv 152,42,128
s_nop 0
Tv 34,v3,26624
Tv 36,v3,32768
Mavv 132,38,128
E_ 92
Mavv 128,38,124
E_ 93
Mavv 148,42,124
E_ 94
Mavv 144,42,120
E_ 95
Mavv 124,38,120
C_ 112,92,93
C_ 113,94,95
Mavv 120,38,116
v_mov_b32 v92, 0x3f803f80
v_mov_b32 v93, 0x3f803f80
Mavv 140,42,116
W_ 0
v_mov_b32 v94, 0x3f803f80
v_mov_b32 v95, 0x3f803f80
Mavv 160,30,116
s_nop 0
Tv 38,v4,26624
Tv 40,v4,32768
Mavv 180,34,116
Mavv 184,34,120
s_nop 0
Tv 42,v5,26624
Tv 44,v5,32768
Mavv 164,30,120
Mavv 168,30,124
Mavv 188,34,124
S_ s65,s61,147456
Mavv 192,34,128
S_ m0,s51,24576
Mavv 172,30,128
s_waitcnt vmcnt(6) lgkmcnt(0)
s_barrier
Mavv 176,30,132
Ln v7,32,s65,0
Mavv 196,34,132
Mavv 216,38,132
Dv 30,%0,6144
Mavv 236,42,132
Dv 34,v1,6144
Mavv 232,42,128
Ln v8,32,s65,3072
S_ m0,s51,25472
Mavv 212,38,128
Mavv 208,38,124
Ln v7,32,s65,128
Mavv 228,42,124
Mavv 224,42,120
Ln v8,32,s65,3200
S_ m0,s51,26368
Mavv 204,38,120
Mavv 200,38,116
Ln v7,32,s65,256
Mavv 220,42,116
.Lstep2_11:
Zvvv 76,92,96
Dv 38,%0,7168
Zvvv 80,92,100
Dv 42,v1,7168
Zvvv 84,92,104
Zvvv 88,92,108
Zvvv 92,92,112
W_ 2
Nvvv 116,30,136,56
Mvvv 116,34,156
Nvvv 120,30,140,60
s_nop 1
v_max3_f32 v10, v76, v80, v84
s_nop 0
v_max3_f32 v10, v10, v88, v92
Mvvv 120,34,160
v_cmp_lt_f32 vcc, 0x42000000, v10
s_cbranch_vccnz .Ltramp2_10
Nvvv 124,30,144,64
A_ v46,v46,v76
A_ v47,v47,v80
Mvvv 124,34,164
A_ v48,v48,v84
A_ v49,v49,v88
Nvvv 128,30,148,68
A_ v50,v50,v92
Mvvv 128,34,168
Ln v8,32,s65,3328
Nvvv 132,30,152,72
Mvvv 132,34,172
W_ 0
Mvvv 132,38,192
Dv 30,%0,8192
Mvvv 132,42,212
Dv 34,v1,8192
Mvvv 128,38,188
Mvvv 128,42,208
Mvvv 124,38,184
Mvvv 124,42,204
Mvvv 120,38,180
Mvvv 120,42,200
Mvvv 116,38,176
Mvvv 116,42,196
W_ 0
Mvvv 116,30,216
Dv 38,%0,0
Mvvv 116,34,236
Dv 42,v1,0
Mvvv 120,30,220
Mvvv 120,34,240
Mvvv 124,30,224
Mvvv 124,34,244
Mvvv 128,30,228
Mvvv 128,34,248
s_nop 0
E_ 116
Mvvv 132,30,232
E_ 117
Mvvv 132,34,252
W_ 0
E_ 118
Nvvv 76,38,136,56
Dv 30,%0,1024
Mvvv 76,42,156
Dv 34,v1,1024
Nvvv 80,38,140,60
E_ 119
Mvvv 80,42,160
C_ 119,118,119
C_ 118,116,117
Nvvv 84,38,144,64
E_ 120
Mvvv 84,42,164
E_ 121
Nvvv 88,38,148,68
E_ 122
Mvvv 88,42,168
E_ 123
Nvvv 92,38,152,72
C_ 123,122,123
C_ 122,120,121
Mvvv 92,42,172
W_ 0
E_ 124
Mvvv 92,30,192
Dv 38,%0,2048
Mvvv 92,34,212
Dv 42,v1,2048
Mvvv 88,30,188
E_ 125
Mvvv 88,34,208
E_ 126
Mvvv 84,30,184
S_ s65,s61,135168
E_ 127
Mvvv 84,34,204
S_ m0,s51,0
C_ 127,126,127
C_ 126,124,125
Mvvv 80,30,180
s_waitcnt vmcnt(6) lgkmcnt(0)
s_barrier
Mvvv 80,34,200
L_ v9,36,s65,0
Mvvv 76,30,176
E_ 128
Mvvv 76,34,196
L_ v9,36,s65,3072
S_ m0,s51,896
Mvvv 76,38,216
Tv 30,v2,12288
Tv 32,v2,18432
Mvvv 76,42,236
L_ v9,36,s65,128
Mvvv 80,38,220
s_nop 0
Tv 34,v3,12288
Tv 36,v3,18432
Mvvv 80,42,240
L_ v9,36,s65,3200
S_ m0,s51,1792
Mvvv 84,38,224
E_ 129
Mvvv 84,42,244
L_ v9,36,s65,256
Mvvv 88,38,228
E_ 130
Mvvv 88,42,248
L_ v9,36,s65,3328
Mvvv 92,38,232
E_ 131
Mvvv 92,42,252
W_ 0
C_ 131,130,131
C_ 130,128,129
Mavv 0,30,96
s_nop 0
Tv 38,v4,12288
Tv 40,v4,18432
Mavv 20,34,96
E_ 132
Mavv 24,34,100
s_nop 0
Tv 42,v5,12288
Tv 44,v5,18432
Mavv 4,30,100
.Lpad_a2_6:
s_cbranch_execz .Lhop_a2_r
E_ 133
Mavv 8,30,104
E_ 134
Mavv 28,34,104
E_ 135
Mavv 32,34,108
C_ 135,134,135
C_ 134,132,133
Mavv 12,30,108
E_ 76
Mavv 16,30,112
E_ 77
Mavv 36,34,112
W_ 0
E_ 78
Mavv 56,38,112
s_nop 0
Tv 30,v2,13312
Tv 32,v2,19456
Mavv 76,42,112
E_ 79
Mavv 72,42,108
s_nop 0
Tv 34,v3,13312
Tv 36,v3,19456
Mavv 52,38,108
C_ 116,76,77
C_ 117,78,79
Mavv 48,38,104
E_ 80
Mavv 68,42,104
E_ 81
Mavv 64,42,100
E_ 82
Mavv 44,38,100
E_ 83
Mavv 40,38,96
C_ 120,80,81
C_ 121,82,83
Mavv 60,42,96
W_ 0
E_ 84
Mavv 80,30,96
s_nop 0
Tv 38,v4,13312
Tv 40,v4,19456
Mavv 100,34,96
E_ 85
Mavv 104,34,100
s_nop 0
Tv 42,v5,13312
Tv 44,v5,19456
Mavv 84,30,100
E_ 86
Mavv 88,30,104
E_ 87
Mavv 108,34,104
C_ 124,84,85
C_ 125,86,87
Mavv 112,34,108
E_ 88
Mavv 92,30,108
E_ 89
Mavv 96,30,112
E_ 90
Mavv 116,34,112
W_ 0
E_ 91
Mavv 136,38,112
s_nop 0
Tv 30,v2,14336
Tv 32,v2,20480
Mavv 156,42,112
C_ 128,88,89
C_ 129,90,91
Mavv 152,42,108
s_nop 0
Tv 34,v3,14336
Tv 36,v3,20480
Mavv 132,38,108
E_ 92
Mavv 128,38,104
E_ 93
Mavv 148,42,104
E_ 94
Mavv 144,42,100
E_ 95
Mavv 124,38,100
C_ 132,92,93
C_ 133,94,95
Mavv 120,38,96
v_mov_b32 v92, 0x3f803f80
v_mov_b32 v93, 0x3f803f80
Mavv 140,42,96
W_ 0
v_mov_b32 v94, 0x3f803f80
v_mov_b32 v95, 0x3f803f80
Mavv 160,30,96
s_nop 0
Tv 38,v4,14336
Tv 40,v4,20480
Mavv 180,34,96
Mavv 184,34,100
s_nop 0
Tv 42,v5,14336
Tv 44,v5,20480
Mavv 164,30,100
Mavv 168,30,104
Mavv 188,34,104
S_ s65,s61,159744
Mavv 192,34,108
S_ m0,s51,12288
Mavv 172,30,108
s_waitcnt vmcnt(6) lgkmcnt(0)
s_barrier
Mavv 176,30,112
Ln v7,32,s65,0
Mavv 196,34,112
Mavv 216,38,112
Dv 30,%0,30720
Mavv 236,42,112
Dv 34,v1,30720
Mavv 232,42,108
Ln v8,32,s65,3072
S_ m0,s51,13184
Mavv 212,38,108
Mavv 208,38,104
Ln v7,32,s65,128
Mavv 228,42,104
Mavv 224,42,100
Ln v8,32,s65,3200
S_ m0,s51,14080
Mavv 204,38,100
Mavv 200,38,96
Ln v7,32,s65,256
Mavv 220,42,96
.Lstep2_12:
Zvvv 76,92,116
Dv 38,%0,31744
Zvvv 80,92,120
Dv 42,v1,31744
Zvvv 84,92,124
Zvvv 88,92,128
Zvvv 92,92,132
W_ 2
Nvvv 96,30,136,56
Mvvv 96,34,156
Nvvv 100,30,140,60
s_nop 1
v_max3_f32 v10, v76, v80, v84
s_nop 0
v_max3_f32 v10, v10, v88, v92
Mvvv 100,34,160
v_cmp_lt_f32 vcc, 0x42000000, v10
s_cbranch_vccnz .Ltramp2_11
Nvvv 104,30,144,64
A_ v46,v46,v76
A_ v47,v47,v80
Mvvv 104,34,164
A_ v48,v48,v84
A_ v49,v49,v88
Nvvv 108,30,148,68
A_ v50,v50,v92
Mvvv 108,34,168
Ln v8,32,s65,3328
Nvvv 112,30,152,72
R_ v11,a248
Mvvv 112,34,172
W_ 0
v_min_u32 v11, 240, v11
Mvvv 112,38,192
Dv 30,%0,32768
v_lshlrev_b32 v11, 3, v11
Mvvv 112,42,212
Dv 34,v1,32768
Mvvv 108,38,188
buffer_load_dword a254, v11, s[44:47], s66 offen
Mvvv 108,42,208
Mvvv 104,38,184
Mvvv 104,42,204
Mvvv 100,38,180
Mvvv 100,42,200
Mvvv 96,38,176
Mvvv 96,42,196
W_ 0
Mvvv 96,30,216
Dv 38,%0,24576
Mvvv 96,34,236
Dv 42,v1,24576
Mvvv 100,30,220
Mvvv 100,34,240
Mvvv 104,30,224
Mvvv 104,34,244
Mvvv 108,30,228
Mvvv 108,34,248
s_nop 0
E_ 96
Mvvv 112,30,232
E_ 97
Mvvv 112,34,252
W_ 0
E_ 98
Nvvv 76,38,136,56
Dv 30,%0,25600
Mvvv 76,42,156
Dv 34,v1,25600
Nvvv 80,38,140,60
E_ 99
Mvvv 80,42,160
C_ 99,98,99
C_ 98,96,97
Nvvv 84,38,144,64
E_ 100
Mvvv 84,42,164
E_ 101
Nvvv 88,38,148,68
E_ 102
Mvvv 88,42,168
E_ 103
Nvvv 92,38,152,72
C_ 103,102,103
C_ 102,100,101
Mvvv 92,42,172
W_ 0
E_ 104
Mvvv 92,30,192
Dv 38,%0,26624
Mvvv 92,34,212
Dv 42,v1,26624
Mvvv 88,30,188
E_ 105
Mvvv 88,34,208
E_ 106
Mvvv 84,30,184
S_ s65,s61,147456
E_ 107
Mvvv 84,34,204
S_ m0,s51,24576
C_ 107,106,107
C_ 106,104,105
Mvvv 80,30,180
s_waitcnt vmcnt(7) lgkmcnt(0)
s_barrier
Mvvv 80,34,200
Ln v9,36,s65,0
Mvvv 76,30,176
E_ 108
Mvvv 76,34,196
Ln v9,36,s65,3072
S_ m0,s51,25472
Mvvv 76,38,216
Tv 30,v2,0
Tv 32,v2,6144
Mvvv 76,42,236
Ln v9,36,s65,128
Mvvv 80,38,220
s_nop 0
Tv 34,v3,0
Tv 36,v3,6144
Mvvv 80,42,240
Ln v9,36,s65,3200
S_ m0,s51,26368
Mvvv 84,38,224
E_ 109
Mvvv 84,42,244
Ln v9,36,s65,256
Mvvv 88,38,228
E_ 110
Mvvv 88,42,248
Ln v9,36,s65,3328
Mvvv 92,38,232
E_ 111
Mvvv 92,42,252
W_ 0
C_ 111,110,111
C_ 110,108,109
Mavv 0,30,116
s_nop 0
Tv 38,v4,0
Tv 40,v4,6144
Mavv 20,34,116
E_ 112
Mavv 24,34,120
s_nop 0
Tv 42,v5,0
Tv 44,v5,6144
Mavv 4,30,120
E_ 113
Mavv 8,30,124
E_ 114
Mavv 28,34,124
E_ 115
Mavv 32,34,128
C_ 115,114,115
C_ 114,112,113
Mavv 12,30,128
E_ 76
Mavv 16,30,132
E_ 77
Mavv 36,34,132
W_ 0
E_ 78
Mavv 56,38,132
s_nop 0
Tv 30,v2,1024
Tv 32,v2,7168
Mavv 76,42,132
E_ 79
Mavv 72,42,128
s_nop 0
Tv 34,v3,1024
Tv 36,v3,7168
Mavv 52,38,128
C_ 96,76,77
C_ 97,78,79
Mavv 48,38,124
E_ 80
Mavv 68,42,124
E_ 81
Mavv 64,42,120
E_ 82
Mavv 44,38,120
E_ 83
Mavv 40,38,116
C_ 100,80,81
C_ 101,82,83
Mavv 60,42,116
W_ 0
E_ 84
Mavv 80,30,116
s_nop 0
Tv 38,v4,1024
Tv 40,v4,7168
Mavv 100,34,116
E_ 85
Mavv 104,34,120
s_nop 0
Tv 42,v5,1024
Tv 44,v5,7168
Mavv 84,30,120
E_ 86
Mavv 88,30,124
E_ 87
Mavv 108,34,124
C_ 104,84,85
C_ 105,86,87
Mavv 112,34,128
E_ 88
Mavv 92,30,128
E_ 89
Mavv 96,30,132
E_ 90
Mavv 116,34,132
W_ 0
E_ 91
Mavv 136,38,132
s_nop 0
Tv 30,v2,2048
Tv 32,v2,8192
Mavv 156,42,132
C_ 108,88,89
C_ 109,90,91
Mavv 152,42,128
s_nop 0
Tv 34,v3,2048
Tv 36,v3,8192
Mavv 132,38,128
E_ 92
Mavv 128,38,124
E_ 93
Mavv 148,42,124
E_ 94
Mavv 144,42,120
E_ 95
Mavv 124,38,120
C_ 112,92,93
C_ 113,94,95
Mavv 120,38,116
v_mov_b32 v92, 0x3f803f80
v_mov_b32 v93, 0x3f803f80
Mavv 140,42,116
W_ 0
v_mov_b32 v94, 0x3f803f80
v_mov_b32 v95, 0x3f803f80
Mavv 160,30,116
s_nop 0
Tv 38,v4,2048
Tv 40,v4,8192
Mavv 180,34,116
Mavv 184,34,120
s_nop 0
Tv 42,v5,2048
Tv 44,v5,8192
Mavv 164,30,120
Mavv 168,30,124
Mavv 188,34,124
S_ s65,s61,172032
Mavv 192,34,128
S_ m0,s51,0
Mavv 172,30,128
s_waitcnt vmcnt(7) lgkmcnt(0)
s_barrier
Mavv 176,30,132
Ln v7,32,s65,0
Mavv 196,34,132
Mavv 216,38,132
Dv 30,%0,18432
Mavv 236,42,132
Dv 34,v1,18432
Mavv 232,42,128
Ln v8,32,s65,3072
S_ m0,s51,896
Mavv 212,38,128
Mavv 208,38,124
Ln v7,32,s65,128
Mavv 228,42,124
Mavv 224,42,120
Ln v8,32,s65,3200
S_ m0,s51,1792
Mavv 204,38,120
Mavv 200,38,116
Ln v7,32,s65,256
Mavv 220,42,116
.Lstep2_13:
Zvvv 76,92,96
Dv 38,%0,19456
Zvvv 80,92,100
Dv 42,v1,19456
Zvvv 84,92,104
Zvvv 88,92,108
Zvvv 92,92,112
W_ 2
Nvvv 116,30,136,56
Mvvv 116,34,156
Nvvv 120,30,140,60
s_nop 1
v_max3_f32 v10, v76, v80, v84
s_nop 0
v_max3_f32 v10, v10, v88, v92
Mvvv 120,34,160
v_cmp_lt_f32 vcc, 0x42000000, v10
s_cbranch_vccnz .Ltramp2_12
Nvvv 124,30,144,64
A_ v46,v46,v76
A_ v47,v47,v80
Mvvv 124,34,164
A_ v48,v48,v84
A_ v49,v49,v88
Nvvv 128,30,148,68
A_ v50,v50,v92
Mvvv 128,34,168
Ln v8,32,s65,3328
Nvvv 132,30,152,72
Mvvv 132,34,172
W_ 0
Mvvv 132,38,192
Dv 30,%0,20480
Mvvv 132,42,212
Dv 34,v1,20480
Mvvv 128,38,188
Mvvv 128,42,208
Mvvv 124,38,184
Mvvv 124,42,204
Mvvv 120,38,180
Mvvv 120,42,200
Mvvv 116,38,176
Mvvv 116,42,196
W_ 0
Mvvv 116,30,216
Dv 38,%0,12288
Mvvv 116,34,236
Dv 42,v1,12288
Mvvv 120,30,220
Mvvv 120,34,240
Mvvv 124,30,224
Mvvv 124,34,244
Mvvv 128,30,228
Mvvv 128,34,248
s_nop 0
E_ 116
Mvvv 132,30,232
E_ 117
Mvvv 132,34,252
W_ 0
E_ 118
Nvvv 76,38,136,56
Dv 30,%0,13312
Mvvv 76,42,156
Dv 34,v1,13312
Nvvv 80,38,140,60
E_ 119
Mvvv 80,42,160
C_ 119,118,119
C_ 118,116,117
Nvvv 84,38,144,64
E_ 120
Mvvv 84,42,164
E_ 121
Nvvv 88,38,148,68
E_ 122
Mvvv 88,42,168
E_ 123
Nvvv 92,38,152,72
C_ 123,122,123
C_ 122,120,121
Mvvv 92,42,172
W_ 0
E_ 124
Mvvv 92,30,192
Dv 38,%0,14336
Mvvv 92,34,212
Dv 42,v1,14336
Mvvv 88,30,188
E_ 125
Mvvv 88,34,208
E_ 126
Mvvv 84,30,184
S_ s65,s61,159744
E_ 127
Mvvv 84,34,204
S_ m0,s51,12288
C_ 127,126,127
C_ 126,124,125
Mvvv 80,30,180
s_waitcnt vmcnt(6) lgkmcnt(0)
s_barrier
Mvvv 80,34,200
Ln v9,36,s65,0
Mvvv 76,30,176
E_ 128
Mvvv 76,34,196
Ln v9,36,s65,3072
S_ m0,s51,13184
Mvvv 76,38,216
Tv 30,v2,24576
Tv 32,v2,30720
Mvvv 76,42,236
Ln v9,36,s65,128
Mvvv 80,38,220
s_nop 0
Tv 34,v3,24576
Tv 36,v3,30720
Mvvv 80,42,240
Ln v9,36,s65,3200
S_ m0,s51,14080
Mvvv 84,38,224
E_ 129
Mvvv 84,42,244
Ln v9,36,s65,256
Mvvv 88,38,228
E_ 130
Mvvv 88,42,248
Ln v9,36,s65,3328
Mvvv 92,38,232
E_ 131
Mvvv 92,42,252
W_ 0
C_ 131,130,131
C_ 130,128,129
Mavv 0,30,96
s_nop 0
Tv 38,v4,24576
Tv 40,v4,30720
Mavv 20,34,96
E_ 132
Mavv 24,34,100
s_nop 0
Tv 42,v5,24576
Tv 44,v5,30720
Mavv 4,30,100
E_ 133
Mavv 8,30,104
E_ 134
Mavv 28,34,104
E_ 135
Mavv 32,34,108
C_ 135,134,135
C_ 134,132,133
Mavv 12,30,108
E_ 76
Mavv 16,30,112
E_ 77
Mavv 36,34,112
W_ 0
E_ 78
Mavv 56,38,112
s_nop 0
Tv 30,v2,25600
Tv 32,v2,31744
Mavv 76,42,112
E_ 79
Mavv 72,42,108
.Lpad_a2_7:
s_cbranch_execz .Lhop_a2_r
Tv 34,v3,25600
Tv 36,v3,31744
Mavv 52,38,108
C_ 116,76,77
C_ 117,78,79
Mavv 48,38,104
E_ 80
Mavv 68,42,104
E_ 81
Mavv 64,42,100
E_ 82
Mavv 44,38,100
E_ 83
Mavv 40,38,96
C_ 120,80,81
C_ 121,82,83
Mavv 60,42,96
W_ 0
E_ 84
Mavv 80,30,96
s_nop 0
Tv 38,v4,25600
Tv 40,v4,31744
Mavv 100,34,96
E_ 85
Mavv 104,34,100
s_nop 0
Tv 42,v5,25600
Tv 44,v5,31744
Mavv 84,30,100
E_ 86
Mavv 88,30,104
E_ 87
Mavv 108,34,104
C_ 124,84,85
C_ 125,86,87
Mavv 112,34,108
E_ 88
Mavv 92,30,108
E_ 89
Mavv 96,30,112
E_ 90
Mavv 116,34,112
W_ 0
E_ 91
Mavv 136,38,112
s_nop 0
Tv 30,v2,26624
Tv 32,v2,32768
Mavv 156,42,112
C_ 128,88,89
C_ 129,90,91
Mavv 152,42,108
s_nop 0
Tv 34,v3,26624
Tv 36,v3,32768
Mavv 132,38,108
E_ 92
Mavv 128,38,104
E_ 93
Mavv 148,42,104
E_ 94
Mavv 144,42,100
E_ 95
Mavv 124,38,100
C_ 132,92,93
C_ 133,94,95
Mavv 120,38,96
v_mov_b32 v92, 0x3f803f80
v_mov_b32 v93, 0x3f803f80
Mavv 140,42,96
W_ 0
v_mov_b32 v94, 0x3f803f80
v_mov_b32 v95, 0x3f803f80
Mavv 160,30,96
s_nop 0
Tv 38,v4,26624
Tv 40,v4,32768
Mavv 180,34,96
Mavv 184,34,100
s_nop 0
Tv 42,v5,26624
Tv 44,v5,32768
Mavv 164,30,100
Mavv 168,30,104
Mavv 188,34,104
S_ s65,s61,184320
Mavv 192,34,108
S_ m0,s51,24576
Mavv 172,30,108
s_waitcnt vmcnt(6) lgkmcnt(0)
s_barrier
Mavv 176,30,112
Ln v7,32,s65,0
Mavv 196,34,112
Mavv 216,38,112
Dv 30,%0,6144
Mavv 236,42,112
Dv 34,v1,6144
Mavv 232,42,108
Ln v8,32,s65,3072
S_ m0,s51,25472
Mavv 212,38,108
Mavv 208,38,104
Ln v7,32,s65,128
Mavv 228,42,104
Mavv 224,42,100
Ln v8,32,s65,3200
S_ m0,s51,26368
Mavv 204,38,100
Mavv 200,38,96
Ln v7,32,s65,256
Mavv 220,42,96
.Lstep2_14:
Zvvv 76,92,116
Dv 38,%0,7168
Zvvv 80,92,120
Dv 42,v1,7168
Zvvv 84,92,124
Zvvv 88,92,128
Zvvv 92,92,132
W_ 2
Nvvv 96,30,136,56
Mvvv 96,34,156
Nvvv 100,30,140,60
s_nop 1
v_max3_f32 v10, v76, v80, v84
s_nop 0
v_max3_f32 v10, v10, v88, v92
Mvvv 100,34,160
v_cmp_lt_f32 vcc, 0x42000000, v10
s_cbranch_vccnz .Ltramp2_13
Nvvv 104,30,144,64
A_ v46,v46,v76
A_ v47,v47,v80
Mvvv 104,34,164
A_ v48,v48,v84
A_ v49,v49,v88
Nvvv 108,30,148,68
A_ v50,v50,v92
Mvvv 108,34,168
Ln v8,32,s65,3328
Nvvv 112,30,152,72
Mvvv 112,34,172
W_ 0
Mvvv 112,38,192
Dv 30,%0,8192
Mvvv 112,42,212
Dv 34,v1,8192
Mvvv 108,38,188
Mvvv 108,42,208
Mvvv 104,38,184
Mvvv 104,42,204
Mvvv 100,38,180
Mvvv 100,42,200
Mvvv 96,38,176
Mvvv 96,42,196
W_ 0
Mvvv 96,30,216
Dv 38,%0,0
Mvvv 96,34,236
Dv 42,v1,0
Mvvv 100,30,220
Mvvv 100,34,240
Mvvv 104,30,224
Mvvv 104,34,244
Mvvv 108,30,228
Mvvv 108,34,248
s_nop 0
E_ 96
Mvvv 112,30,232
E_ 97
Mvvv 112,34,252
W_ 0
E_ 98
Nvvv 76,38,136,56
Dv 30,%0,1024
Mvvv 76,42,156
Dv 34,v1,1024
Nvvv 80,38,140,60
E_ 99
Mvvv 80,42,160
C_ 99,98,99
C_ 98,96,97
Nvvv 84,38,144,64
E_ 100
Mvvv 84,42,164
E_ 101
Nvvv 88,38,148,68
E_ 102
Mvvv 88,42,168
E_ 103
Nvvv 92,38,152,72
C_ 103,102,103
C_ 102,100,101
Mvvv 92,42,172
W_ 0
E_ 104
Mvvv 92,30,192
Dv 38,%0,2048
Mvvv 92,34,212
Dv 42,v1,2048
Mvvv 88,30,188
E_ 105
Mvvv 88,34,208
E_ 106
Mvvv 84,30,184
S_ s65,s61,172032
E_ 107
Mvvv 84,34,204
S_ m0,s51,0
C_ 107,106,107
C_ 106,104,105
Mvvv 80,30,180
s_waitcnt vmcnt(6) lgkmcnt(0)
s_barrier
Mvvv 80,34,200
Ln v9,36,s65,0
Mvvv 76,30,176
E_ 108
Mvvv 76,34,196
Ln v9,36,s65,3072
S_ m0,s51,896
Mvvv 76,38,216
Tv 30,v2,12288
Tv 32,v2,18432
Mvvv 76,42,236
Ln v9,36,s65,128
Mvvv 80,38,220
s_nop 0
Tv 34,v3,12288
Tv 36,v3,18432
Mvvv 80,42,240
Ln v9,36,s65,3200
S_ m0,s51,1792
Mvvv 84,38,224
E_ 109
Mvvv 84,42,244
Ln v9,36,s65,256
Mvvv 88,38,228
E_ 110
Mvvv 88,42,248
Ln v9,36,s65,3328
Mvvv 92,38,232
E_ 111
Mvvv 92,42,252
W_ 0
C_ 111,110,111
C_ 110,108,109
Mavv 0,30,116
s_nop 0
Tv 38,v4,12288
Tv 40,v4,18432
Mavv 20,34,116
E_ 112
Mavv 24,34,120
s_nop 0
Tv 42,v5,12288
Tv 44,v5,18432
Mavv 4,30,120
E_ 113
Mavv 8,30,124
E_ 114
Mavv 28,34,124
E_ 115
Mavv 32,34,128
C_ 115,114,115
C_ 114,112,113
Mavv 12,30,128
E_ 76
Mavv 16,30,132
E_ 77
Mavv 36,34,132
W_ 0
E_ 78
Mavv 56,38,132
s_nop 0
Tv 30,v2,13312
Tv 32,v2,19456
Mavv 76,42,132
E_ 79
Mavv 72,42,128
s_nop 0
Tv 34,v3,13312
Tv 36,v3,19456
Mavv 52,38,128
C_ 96,76,77
C_ 97,78,79
Mavv 48,38,124
E_ 80
Mavv 68,42,124
E_ 81
Mavv 64,42,120
E_ 82
Mavv 44,38,120
E_ 83
Mavv 40,38,116
C_ 100,80,81
C_ 101,82,83
Mavv 60,42,116
W_ 0
E_ 84
Mavv 80,30,116
s_nop 0
Tv 38,v4,13312
Tv 40,v4,19456
Mavv 100,34,116
E_ 85
Mavv 104,34,120
s_nop 0
Tv 42,v5,13312
Tv 44,v5,19456
Mavv 84,30,120
E_ 86
Mavv 88,30,124
E_ 87
Mavv 108,34,124
C_ 104,84,85
C_ 105,86,87
Mavv 112,34,128
)A"
R"A(E_ 88
Mavv 92,30,128
E_ 89
Mavv 96,30,132
E_ 90
Mavv 116,34,132
W_ 0
E_ 91
Mavv 136,38,132
s_nop 0
Tv 30,v2,14336
Tv 32,v2,20480
Mavv 156,42,132
C_ 108,88,89
C_ 109,90,91
Mavv 152,42,128
s_nop 0
Tv 34,v3,14336
Tv 36,v3,20480
Mavv 132,38,128
E_ 92
Mavv 128,38,124
E_ 93
Mavv 148,42,124
E_ 94
Mavv 144,42,120
E_ 95
Mavv 124,38,120
C_ 112,92,93
C_ 113,94,95
Mavv 120,38,116
v_mov_b32 v92, 0x3f803f80
v_mov_b32 v93, 0x3f803f80
Mavv 140,42,116
W_ 0
v_mov_b32 v94, 0x3f803f80
v_mov_b32 v95, 0x3f803f80
Mavv 160,30,116
s_nop 0
Tv 38,v4,14336
Tv 40,v4,20480
Mavv 180,34,116
Mavv 184,34,120
s_nop 0
Tv 42,v5,14336
Tv 44,v5,20480
Mavv 164,30,120
Mavv 168,30,124
Mavv 188,34,124
S_ s65,s61,184320
Mavv 192,34,128
S_ m0,s51,12288
Mavv 172,30,128
s_waitcnt vmcnt(6) lgkmcnt(0)
s_barrier
Mavv 176,30,132
Ln v9,36,s65,0
Mavv 196,34,132
Mavv 216,38,132
Dv 30,%0,30720
Mavv 236,42,132
Dv 34,v1,30720
Mavv 232,42,128
Ln v9,36,s65,3072
S_ m0,s51,13184
Mavv 212,38,128
Mavv 208,38,124
Ln v9,36,s65,128
Mavv 228,42,124
Mavv 224,42,120
Ln v9,36,s65,3200
S_ m0,s51,14080
Mavv 204,38,120
Mavv 200,38,116
Ln v9,36,s65,256
Mavv 220,42,116
.Lstep2_15:
Zvvv 76,92,96
Dv 38,%0,31744
Zvvv 80,92,100
Dv 42,v1,31744
Zvvv 84,92,104
Zvvv 88,92,108
Zvvv 92,92,112
W_ 2
Nvvv 116,30,136,56
Mvvv 116,34,156
Nvvv 120,30,140,60
s_nop 1
v_max3_f32 v10, v76, v80, v84
s_nop 0
v_max3_f32 v10, v10, v88, v92
Mvvv 120,34,160
v_cmp_lt_f32 vcc, 0x42000000, v10
s_cbranch_vccnz .Ltramp2_14
Nvvv 124,30,144,64
A_ v46,v46,v76
A_ v47,v47,v80
Mvvv 124,34,164
A_ v48,v48,v84
A_ v49,v49,v88
Nvvv 128,30,148,68
A_ v50,v50,v92
Mvvv 128,34,168
Ln v9,36,s65,3328
Nvvv 132,30,152,72
Mvvv 132,34,172
W_ 0
Mvvv 132,38,192
Dv 30,%0,32768
Mvvv 132,42,212
Dv 34,v1,32768
Mvvv 128,38,188
Mvvv 128,42,208
Mvvv 124,38,184
Mvvv 124,42,204
Mvvv 120,38,180
Mvvv 120,42,200
Mvvv 116,38,176
Mvvv 116,42,196
W_ 0
Mvvv 116,30,216
Dv 38,%0,24576
Mvvv 116,34,236
Dv 42,v1,24576
Mvvv 120,30,220
Mvvv 120,34,240
Mvvv 124,30,224
Mvvv 124,34,244
Mvvv 128,30,228
Mvvv 128,34,248
s_nop 0
E_ 116
Mvvv 132,30,232
E_ 117
Mvvv 132,34,252
W_ 0
E_ 118
Nvvv 76,38,136,56
Dv 30,%0,25600
Mvvv 76,42,156
Dv 34,v1,25600
Nvvv 80,38,140,60
E_ 119
Mvvv 80,42,160
C_ 119,118,119
C_ 118,116,117
Nvvv 84,38,144,64
E_ 120
Mvvv 84,42,164
E_ 121
Nvvv 88,38,148,68
E_ 122
Mvvv 88,42,168
E_ 123
Nvvv 92,38,152,72
C_ 123,122,123
C_ 122,120,121
Mvvv 92,42,172
W_ 0
E_ 124
Mvvv 92,30,192
Dv 38,%0,26624
Mvvv 92,34,212
Dv 42,v1,26624
Mvvv 88,30,188
E_ 125
Mvvv 88,34,208
E_ 126
Mvvv 84,30,184
S_ s65,s61,393216
E_ 127
Mvvv 84,34,204
S_ m0,s51,24576
C_ 127,126,127
C_ 126,124,125
Mvvv 80,30,180
s_waitcnt vmcnt(6) lgkmcnt(0)
s_barrier
Mvvv 80,34,200
Ln v9,36,s65,0
Mvvv 76,30,176
E_ 128
Mvvv 76,34,196
Ln v9,36,s65,3072
S_ m0,s51,25472
Mvvv 76,38,216
Tv 30,v2,0
Tv 32,v2,6144
Mvvv 76,42,236
Ln v9,36,s65,128
Mvvv 80,38,220
s_nop 0
Tv 34,v3,0
Tv 36,v3,6144
Mvvv 80,42,240
Ln v9,36,s65,3200
S_ m0,s51,26368
Mvvv 84,38,224
E_ 129
Mvvv 84,42,244
Ln v9,36,s65,256
Mvvv 88,38,228
E_ 130
Mvvv 88,42,248
Ln v9,36,s65,3328
Mvvv 92,38,232
E_ 131
Mvvv 92,42,252
W_ 0
C_ 131,130,131
C_ 130,128,129
Mavv 0,30,96
s_nop 0
Tv 38,v4,0
Tv 40,v4,6144
Mavv 20,34,96
E_ 132
Mavv 24,34,100
s_nop 0
Tv 42,v5,0
Tv 44,v5,6144
Mavv 4,30,100
E_ 133
Mavv 8,30,104
E_ 134
Mavv 28,34,104
E_ 135
Mavv 32,34,108
C_ 135,134,135
C_ 134,132,133
Mavv 12,30,108
E_ 76
Mavv 16,30,112
E_ 77
Mavv 36,34,112
W_ 0
E_ 78
Mavv 56,38,112
s_nop 0
Tv 30,v2,1024
Tv 32,v2,7168
Mavv 76,42,112
E_ 79
Mavv 72,42,108
s_nop 0
Tv 34,v3,1024
Tv 36,v3,7168
Mavv 52,38,108
C_ 116,76,77
C_ 117,78,79
Mavv 48,38,104
E_ 80
Mavv 68,42,104
E_ 81
Mavv 64,42,100
E_ 82
Mavv 44,38,100
E_ 83
Mavv 40,38,96
C_ 120,80,81
C_ 121,82,83
Mavv 60,42,96
W_ 0
E_ 84
Mavv 80,30,96
s_nop 0
Tv 38,v4,1024
Tv 40,v4,7168
Mavv 100,34,96
E_ 85
Mavv 104,34,100
s_nop 0
Tv 42,v5,1024
Tv 44,v5,7168
Mavv 84,30,100
E_ 86
Mavv 88,30,104
E_ 87
Mavv 108,34,104
C_ 124,84,85
C_ 125,86,87
Mavv 112,34,108
E_ 88
Mavv 92,30,108
E_ 89
Mavv 96,30,112
E_ 90
Mavv 116,34,112
W_ 0
E_ 91
Mavv 136,38,112
s_nop 0
Tv 30,v2,2048
Tv 32,v2,8192
Mavv 156,42,112
C_ 128,88,89
C_ 129,90,91
Mavv 152,42,108
s_nop 0
Tv 34,v3,2048
Tv 36,v3,8192
Mavv 132,38,108
E_ 92
Mavv 128,38,104
E_ 93
Mavv 148,42,104
E_ 94
Mavv 144,42,100
E_ 95
Mavv 124,38,100
C_ 132,92,93
C_ 133,94,95
Mavv 120,38,96
v_mov_b32 v92, 0x3f803f80
v_mov_b32 v93, 0x3f803f80
Mavv 140,42,96
W_ 0
v_mov_b32 v94, 0x3f803f80
v_mov_b32 v95, 0x3f803f80
Mavv 160,30,96
s_nop 0
Tv 38,v4,2048
Tv 40,v4,8192
Mavv 180,34,96
Mavv 184,34,100
s_nop 0
Tv 42,v5,2048
Tv 44,v5,8192
Mavv 164,30,100
Mavv 168,30,104
Mavv 188,34,104
S_ s65,s61,393216
Mavv 192,34,108
S_ m0,s51,0
Mavv 172,30,108
s_waitcnt vmcnt(6) lgkmcnt(0)
s_barrier
Mavv 176,30,112
Ln v7,32,s65,0
Mavv 196,34,112
Mavv 216,38,112
s_nop 0
Tv 30,v2,12288
Tv 32,v2,18432
Zvvv 76,92,116
Ln v8,32,s65,3072
S_ m0,s51,896
Zvvv 80,92,120
s_nop 0
Tv 34,v3,12288
Tv 36,v3,18432
Zvvv 84,92,124
Ln v7,32,s65,128
Zvvv 88,92,128
Zvvv 92,92,132
Ln v8,32,s65,3200
S_ m0,s51,1792
Mavv 236,42,112
Mavv 232,42,108
Ln v7,32,s65,256
Mavv 212,38,108
Mavv 208,38,104
v_max3_f32 v10, v76, v80, v84
Mavv 228,42,104
v_max3_f32 v10, v10, v88, v92
v_cmp_lt_f32 vcc, 0x42000000, v10
Mavv 224,42,100
Ln v8,32,s65,3328
Mavv 204,38,100
Mavv 200,38,96
Mavv 220,42,96
s_cbranch_vccnz .Ltramp2_15
.Ljoin2:
W_ 2
Mavv 0,30,116
s_mov_b32 s58, 1
Tv 38,v4,12288
Tv 40,v4,18432
W_ 2
Mavv 20,34,116
R_ v11,a248
R_ v13,a248
Mavv 24,34,120
S_ s53,s51,36864
Tv 42,v5,12288
Tv 44,v5,18432
Mavv 4,30,120
v_add_u32 v12, s53, v13
s_mul_i32 s53, s4, 10240
Mavv 8,30,124
v_add_u32 v13, s53, v13
A_ v46,v46,v76
Mavv 28,34,124
A_ v47,v47,v80
Mavv 32,34,128
A_ v48,v48,v84
A_ v49,v49,v88
Mavv 12,30,128
A_ v50,v50,v92
Mavv 16,30,132
v_rcp_f32 v46, v46
Mavv 36,34,132
W_ 0
v_rcp_f32 v47, v47
Mavv 56,38,132
s_nop 0
Tv 30,v2,13312
Tv 32,v2,19456
Mavv 76,42,132
v_rcp_f32 v48, v48
Mavv 72,42,128
s_nop 0
Tv 34,v3,13312
Tv 36,v3,19456
Mavv 52,38,128
v_rcp_f32 v49, v49
Mavv 48,38,124
v_rcp_f32 v50, v50
Mavv 68,42,124
s_mov_b32 s56, 12288
v_mov_b32 v176, v20
v_mov_b32 v177, v21
Mavv 64,42,120
Dv 136,v13,0
v_mov_b32 v178, v22
Mavv 44,38,120
Dv 140,v13,1024
v_mov_b32 v179, v23
Mavv 40,38,116
Dv 144,v13,2048
v_mov_b32 v180, v24
Mavv 60,42,116
W_ 3
Dv 148,v13,3072
v_mov_b32 v181, v25
Mavv 80,30,116
s_nop 0
Tv 38,v4,13312
Tv 40,v4,19456
Mavv 100,34,116
Dv 152,v13,4096
v_mov_b32 v182, v26
Mavv 104,34,120
s_nop 0
Tv 42,v5,13312
Tv 44,v5,19456
Mavv 84,30,120
Dv 156,v13,5120
v_mov_b32 v183, v27
Mavv 88,30,124
Dv 160,v13,6144
v_mov_b32 v184, v52
Mavv 108,34,124
Dv 164,v13,7168
v_mov_b32 v185, v53
Mavv 112,34,128
Dv 168,v13,8192
v_mov_b32 v186, v54
Mavv 92,30,128
Dv 172,v13,9216
v_mov_b32 v187, v55
Mavv 96,30,132
Dv 208,v12,0
R_ v188,a240
Mavv 116,34,132
W_ 6
Dv 212,v12,1024
R_ v189,a241
Mavv 136,38,132
s_nop 0
Tv 30,v2,14336
Tv 32,v2,20480
Mavv 156,42,132
Dv 216,v12,2048
R_ v190,a242
Mavv 152,42,128
s_nop 0
Tv 34,v3,14336
Tv 36,v3,20480
Mavv 132,38,128
Dv 220,v12,3072
R_ v191,a243
Mavv 128,38,124
Dv 224,v12,4096
R_ v192,a244
Mavv 148,42,124
Dv 228,v12,5120
R_ v193,a245
Mavv 144,42,120
Dv 232,v12,12288
R_ v194,a246
Mavv 124,38,120
Dv 236,v12,13312
R_ v195,a247
Mavv 120,38,116
Dv 240,v12,14336
v_mov_b32 v196, v16
Mavv 140,42,116
W_ 6
Dv 244,v12,15360
v_mov_b32 v197, v17
Mavv 160,30,116
s_nop 0
Tv 38,v4,14336
Tv 40,v4,20480
Mavv 180,34,116
Dv 248,v12,16384
v_mov_b32 v198, v18
Mavv 184,34,120
s_nop 0
Tv 42,v5,14336
Tv 44,v5,20480
Mavv 164,30,120
Dv 252,v12,17408
v_mov_b32 v199, v19
Mavv 168,30,124
v_mov_b32 v200, v28
v_mov_b32 v201, v29
Mavv 188,34,124
v_mov_b32 v202, v51
v_mov_b32 v203, v6
Mavv 192,34,128
v_mov_b32 v204, v14
v_mov_b32 v205, v15
Mavv 172,30,128
S_ s65,s61,405504
R_ v206,a249
R_ v207,a250
Mavv 176,30,132
S_ m0,s51,12288
R_ v96,a0
R_ v97,a1
Mavv 196,34,132
s_waitcnt vmcnt(63) lgkmcnt(0)
s_barrier
R_ v98,a2
Mavv 216,38,132
Ln v7,32,s65,0
Mavv 236,42,132
R_ v99,a3
Mavv 232,42,128
Ln v8,32,s65,3072
S_ m0,s51,13184
Mavv 212,38,128
R_ v100,a20
R_ v101,a21
Mavv 208,38,124
Ln v7,32,s65,128
Mavv 228,42,124
R_ v102,a22
R_ v103,a23
Mavv 224,42,120
Ln v8,32,s65,3200
S_ m0,s51,14080
Mavv 204,38,120
U_ v96,v96,v46
U_ v97,v97,v46
Mavv 200,38,116
Ln v7,32,s65,256
Mavv 220,42,116
U_ v98,v98,v46
U_ v99,v99,v46
U_ v100,v100,v46
U_ v101,v101,v46
U_ v102,v102,v46
U_ v103,v103,v46
C_ 76,96,97
C_ 77,98,99
C_ 78,100,101
C_ 79,102,103
ds_write_b128 v13, v[76:79] offset:0
Ln v8,32,s65,3328
S_ s61,s61,393216
.p2align 3
R_ v104,a4
R_ v105,a5
R_ v106,a6
R_ v107,a7
R_ v108,a24
R_ v109,a25
R_ v110,a26
R_ v111,a27
v_pk_mul_f32 v[104:105], v[104:105], v[46:47] op_sel:[0,1]
v_pk_mul_f32 v[106:107], v[106:107], v[46:47] op_sel:[0,1]
v_pk_mul_f32 v[108:109], v[108:109], v[46:47] op_sel:[0,1]
v_pk_mul_f32 v[110:111], v[110:111], v[46:47] op_sel:[0,1]
C_ 80,104,105
C_ 81,106,107
C_ 82,108,109
C_ 83,110,111
ds_write_b128 v13, v[80:83] offset:1024
.p2align 3
R_ v96,a8
R_ v97,a9
R_ v98,a10
R_ v99,a11
R_ v100,a28
R_ v101,a29
R_ v102,a30
R_ v103,a31
v_pk_mul_f32 v[96:97], v[96:97], v[48:49] op_sel_hi:[1,0]
v_pk_mul_f32 v[98:99], v[98:99], v[48:49] op_sel_hi:[1,0]
v_pk_mul_f32 v[100:101], v[100:101], v[48:49] op_sel_hi:[1,0]
v_pk_mul_f32 v[102:103], v[102:103], v[48:49] op_sel_hi:[1,0]
C_ 84,96,97
C_ 85,98,99
C_ 86,100,101
C_ 87,102,103
ds_write_b128 v13, v[84:87] offset:2048
.p2align 3
R_ v104,a12
R_ v105,a13
R_ v106,a14
R_ v107,a15
R_ v108,a32
R_ v109,a33
R_ v110,a34
R_ v111,a35
v_pk_mul_f32 v[104:105], v[104:105], v[48:49] op_sel:[0,1]
v_pk_mul_f32 v[106:107], v[106:107], v[48:49] op_sel:[0,1]
v_pk_mul_f32 v[108:109], v[108:109], v[48:49] op_sel:[0,1]
v_pk_mul_f32 v[110:111], v[110:111], v[48:49] op_sel:[0,1]
C_ 88,104,105
C_ 89,106,107
C_ 90,108,109
C_ 91,110,111
ds_write_b128 v13, v[88:91] offset:3072
.p2align 3
R_ v96,a16
R_ v97,a17
R_ v98,a18
R_ v99,a19
R_ v100,a36
R_ v101,a37
R_ v102,a38
R_ v103,a39
v_pk_mul_f32 v[96:97], v[96:97], v[50:51] op_sel_hi:[1,0]
v_pk_mul_f32 v[98:99], v[98:99], v[50:51] op_sel_hi:[1,0]
v_pk_mul_f32 v[100:101], v[100:101], v[50:51] op_sel_hi:[1,0]
v_pk_mul_f32 v[102:103], v[102:103], v[50:51] op_sel_hi:[1,0]
C_ 92,96,97
C_ 93,98,99
C_ 94,100,101
C_ 95,102,103
ds_write_b128 v13, v[92:95] offset:4096
.p2align 3
R_ v104,a40
R_ v105,a41
R_ v106,a42
R_ v107,a43
R_ v108,a60
R_ v109,a61
R_ v110,a62
R_ v111,a63
v_pk_mul_f32 v[104:105], v[104:105], v[46:47] op_sel_hi:[1,0]
v_pk_mul_f32 v[106:107], v[106:107], v[46:47] op_sel_hi:[1,0]
v_pk_mul_f32 v[108:109], v[108:109], v[46:47] op_sel_hi:[1,0]
v_pk_mul_f32 v[110:111], v[110:111], v[46:47] op_sel_hi:[1,0]
C_ 76,104,105
C_ 77,106,107
C_ 78,108,109
C_ 79,110,111
ds_write_b128 v13, v[76:79] offset:5120
.p2align 3
R_ v96,a44
R_ v97,a45
R_ v98,a46
R_ v99,a47
R_ v100,a64
R_ v101,a65
R_ v102,a66
R_ v103,a67
v_pk_mul_f32 v[96:97], v[96:97], v[46:47] op_sel:[0,1]
v_pk_mul_f32 v[98:99], v[98:99], v[46:47] op_sel:[0,1]
v_pk_mul_f32 v[100:101], v[100:101], v[46:47] op_sel:[0,1]
v_pk_mul_f32 v[102:103], v[102:103], v[46:47] op_sel:[0,1]
C_ 80,96,97
C_ 81,98,99
C_ 82,100,101
C_ 83,102,103
ds_write_b128 v13, v[80:83] offset:6144
.p2align 3
R_ v104,a48
R_ v105,a49
R_ v106,a50
R_ v107,a51
R_ v108,a68
R_ v109,a69
R_ v110,a70
R_ v111,a71
v_pk_mul_f32 v[104:105], v[104:105], v[48:49] op_sel_hi:[1,0]
v_pk_mul_f32 v[106:107], v[106:107], v[48:49] op_sel_hi:[1,0]
v_pk_mul_f32 v[108:109], v[108:109], v[48:49] op_sel_hi:[1,0]
v_pk_mul_f32 v[110:111], v[110:111], v[48:49] op_sel_hi:[1,0]
C_ 84,104,105
C_ 85,106,107
C_ 86,108,109
C_ 87,110,111
ds_write_b128 v13, v[84:87] offset:7168
.p2align 3
R_ v96,a52
R_ v97,a53
R_ v98,a54
R_ v99,a55
R_ v100,a72
R_ v101,a73
R_ v102,a74
R_ v103,a75
v_pk_mul_f32 v[96:97], v[96:97], v[48:49] op_sel:[0,1]
v_pk_mul_f32 v[98:99], v[98:99], v[48:49] op_sel:[0,1]
v_pk_mul_f32 v[100:101], v[100:101], v[48:49] op_sel:[0,1]
v_pk_mul_f32 v[102:103], v[102:103], v[48:49] op_sel:[0,1]
C_ 88,96,97
C_ 89,98,99
C_ 90,100,101
C_ 91,102,103
ds_write_b128 v13, v[88:91] offset:8192
.p2align 3
R_ v104,a56
R_ v105,a57
R_ v106,a58
R_ v107,a59
R_ v108,a76
R_ v109,a77
R_ v110,a78
R_ v111,a79
v_pk_mul_f32 v[104:105], v[104:105], v[50:51] op_sel_hi:[1,0]
v_pk_mul_f32 v[106:107], v[106:107], v[50:51] op_sel_hi:[1,0]
v_pk_mul_f32 v[108:109], v[108:109], v[50:51] op_sel_hi:[1,0]
v_pk_mul_f32 v[110:111], v[110:111], v[50:51] op_sel_hi:[1,0]
C_ 92,104,105
C_ 93,106,107
C_ 94,108,109
C_ 95,110,111
ds_write_b128 v13, v[92:95] offset:9216
.p2align 3
R_ v104,a92
R_ v105,a93
R_ v106,a94
R_ v107,a95
R_ v108,a112
R_ v109,a113
R_ v110,a114
R_ v111,a115
v_pk_mul_f32 v[104:105], v[104:105], v[48:49] op_sel:[0,1]
v_pk_mul_f32 v[106:107], v[106:107], v[48:49] op_sel:[0,1]
v_pk_mul_f32 v[108:109], v[108:109], v[48:49] op_sel:[0,1]
v_pk_mul_f32 v[110:111], v[110:111], v[48:49] op_sel:[0,1]
C_ 88,104,105
C_ 89,106,107
C_ 90,108,109
C_ 91,110,111
X_ a240,v88
X_ a241,v89
X_ a242,v90
X_ a243,v91
.p2align 3
R_ v96,a96
R_ v97,a97
R_ v98,a98
R_ v99,a99
R_ v100,a116
R_ v101,a117
R_ v102,a118
R_ v103,a119
v_pk_mul_f32 v[96:97], v[96:97], v[50:51] op_sel_hi:[1,0]
v_pk_mul_f32 v[98:99], v[98:99], v[50:51] op_sel_hi:[1,0]
v_pk_mul_f32 v[100:101], v[100:101], v[50:51] op_sel_hi:[1,0]
v_pk_mul_f32 v[102:103], v[102:103], v[50:51] op_sel_hi:[1,0]
C_ 92,96,97
C_ 93,98,99
C_ 94,100,101
C_ 95,102,103
X_ a244,v92
X_ a245,v93
X_ a246,v94
X_ a247,v95
.p2align 3
R_ v96,a124
R_ v97,a125
R_ v98,a126
R_ v99,a127
R_ v100,a144
R_ v101,a145
R_ v102,a146
R_ v103,a147
v_pk_mul_f32 v[96:97], v[96:97], v[46:47] op_sel:[0,1]
v_pk_mul_f32 v[98:99], v[98:99], v[46:47] op_sel:[0,1]
v_pk_mul_f32 v[100:101], v[100:101], v[46:47] op_sel:[0,1]
v_pk_mul_f32 v[102:103], v[102:103], v[46:47] op_sel:[0,1]
C_ 80,96,97
C_ 81,98,99
C_ 82,100,101
C_ 83,102,103
v_mov_b32 v28, v80
v_mov_b32 v29, v81
v_mov_b32 v51, v82
v_mov_b32 v6, v83
.p2align 3
R_ v104,a128
R_ v105,a129
R_ v106,a130
R_ v107,a131
R_ v108,a148
R_ v109,a149
R_ v110,a150
R_ v111,a151
v_pk_mul_f32 v[104:105], v[104:105], v[48:49] op_sel_hi:[1,0]
v_pk_mul_f32 v[106:107], v[106:107], v[48:49] op_sel_hi:[1,0]
v_pk_mul_f32 v[108:109], v[108:109], v[48:49] op_sel_hi:[1,0]
v_pk_mul_f32 v[110:111], v[110:111], v[48:49] op_sel_hi:[1,0]
C_ 84,104,105
C_ 85,106,107
C_ 86,108,109
C_ 87,110,111
v_mov_b32 v14, v84
v_mov_b32 v15, v85
X_ a249,v86
X_ a250,v87
.p2align 3
R_ v96,a132
R_ v97,a133
R_ v98,a134
R_ v99,a135
R_ v100,a152
R_ v101,a153
R_ v102,a154
R_ v103,a155
v_pk_mul_f32 v[96:97], v[96:97], v[48:49] op_sel:[0,1]
v_pk_mul_f32 v[98:99], v[98:99], v[48:49] op_sel:[0,1]
v_pk_mul_f32 v[100:101], v[100:101], v[48:49] op_sel:[0,1]
v_pk_mul_f32 v[102:103], v[102:103], v[48:49] op_sel:[0,1]
C_ 88,96,97
C_ 89,98,99
C_ 90,100,101
C_ 91,102,103
ds_write_b128 v12, v[88:91] offset:0
.p2align 3
R_ v104,a136
R_ v105,a137
R_ v106,a138
R_ v107,a139
R_ v108,a156
R_ v109,a157
R_ v110,a158
R_ v111,a159
v_pk_mul_f32 v[104:105], v[104:105], v[50:51] op_sel_hi:[1,0]
v_pk_mul_f32 v[106:107], v[106:107], v[50:51] op_sel_hi:[1,0]
v_pk_mul_f32 v[108:109], v[108:109], v[50:51] op_sel_hi:[1,0]
v_pk_mul_f32 v[110:111], v[110:111], v[50:51] op_sel_hi:[1,0]
C_ 92,104,105
C_ 93,106,107
C_ 94,108,109
C_ 95,110,111
ds_write_b128 v12, v[92:95] offset:1024
.p2align 3
R_ v96,a160
R_ v97,a161
R_ v98,a162
R_ v99,a163
R_ v100,a180
R_ v101,a181
R_ v102,a182
R_ v103,a183
v_pk_mul_f32 v[96:97], v[96:97], v[46:47] op_sel_hi:[1,0]
v_pk_mul_f32 v[98:99], v[98:99], v[46:47] op_sel_hi:[1,0]
v_pk_mul_f32 v[100:101], v[100:101], v[46:47] op_sel_hi:[1,0]
v_pk_mul_f32 v[102:103], v[102:103], v[46:47] op_sel_hi:[1,0]
C_ 76,96,97
C_ 77,98,99
C_ 78,100,101
C_ 79,102,103
ds_write_b128 v12, v[76:79] offset:2048
.p2align 3
R_ v104,a164
R_ v105,a165
R_ v106,a166
R_ v107,a167
R_ v108,a184
R_ v109,a185
R_ v110,a186
R_ v111,a187
v_pk_mul_f32 v[104:105], v[104:105], v[46:47] op_sel:[0,1]
v_pk_mul_f32 v[106:107], v[106:107], v[46:47] op_sel:[0,1]
v_pk_mul_f32 v[108:109], v[108:109], v[46:47] op_sel:[0,1]
v_pk_mul_f32 v[110:111], v[110:111], v[46:47] op_sel:[0,1]
C_ 80,104,105
C_ 81,106,107
C_ 82,108,109
C_ 83,110,111
ds_write_b128 v12, v[80:83] offset:3072
.p2align 3
R_ v96,a168
R_ v97,a169
R_ v98,a170
R_ v99,a171
R_ v100,a188
R_ v101,a189
R_ v102,a190
R_ v103,a191
v_pk_mul_f32 v[96:97], v[96:97], v[48:49] op_sel_hi:[1,0]
v_pk_mul_f32 v[98:99], v[98:99], v[48:49] op_sel_hi:[1,0]
v_pk_mul_f32 v[100:101], v[100:101], v[48:49] op_sel_hi:[1,0]
v_pk_mul_f32 v[102:103], v[102:103], v[48:49] op_sel_hi:[1,0]
C_ 84,96,97
C_ 85,98,99
C_ 86,100,101
C_ 87,102,103
ds_write_b128 v12, v[84:87] offset:4096
.p2align 3
R_ v104,a172
R_ v105,a173
R_ v106,a174
R_ v107,a175
R_ v108,a192
R_ v109,a193
R_ v110,a194
R_ v111,a195
v_pk_mul_f32 v[104:105], v[104:105], v[48:49] op_sel:[0,1]
v_pk_mul_f32 v[106:107], v[106:107], v[48:49] op_sel:[0,1]
v_pk_mul_f32 v[108:109], v[108:109], v[48:49] op_sel:[0,1]
v_pk_mul_f32 v[110:111], v[110:111], v[48:49] op_sel:[0,1]
C_ 88,104,105
C_ 89,106,107
C_ 90,108,109
C_ 91,110,111
ds_write_b128 v12, v[88:91] offset:5120
.p2align 3
R_ v96,a176
R_ v97,a177
R_ v98,a178
R_ v99,a179
R_ v100,a196
R_ v101,a197
R_ v102,a198
R_ v103,a199
v_pk_mul_f32 v[96:97], v[96:97], v[50:51] op_sel_hi:[1,0]
v_pk_mul_f32 v[98:99], v[98:99], v[50:51] op_sel_hi:[1,0]
v_pk_mul_f32 v[100:101], v[100:101], v[50:51] op_sel_hi:[1,0]
v_pk_mul_f32 v[102:103], v[102:103], v[50:51] op_sel_hi:[1,0]
C_ 92,96,97
C_ 93,98,99
C_ 94,100,101
C_ 95,102,103
ds_write_b128 v12, v[92:95] offset:12288
.p2align 3
R_ v104,a200
R_ v105,a201
R_ v106,a202
R_ v107,a203
R_ v108,a220
R_ v109,a221
R_ v110,a222
R_ v111,a223
v_pk_mul_f32 v[104:105], v[104:105], v[46:47] op_sel_hi:[1,0]
v_pk_mul_f32 v[106:107], v[106:107], v[46:47] op_sel_hi:[1,0]
v_pk_mul_f32 v[108:109], v[108:109], v[46:47] op_sel_hi:[1,0]
v_pk_mul_f32 v[110:111], v[110:111], v[46:47] op_sel_hi:[1,0]
C_ 76,104,105
C_ 77,106,107
C_ 78,108,109
C_ 79,110,111
ds_write_b128 v12, v[76:79] offset:13312
.p2align 3
R_ v96,a204
R_ v97,a205
R_ v98,a206
R_ v99,a207
R_ v100,a224
R_ v101,a225
R_ v102,a226
R_ v103,a227
v_pk_mul_f32 v[96:97], v[96:97], v[46:47] op_sel:[0,1]
v_pk_mul_f32 v[98:99], v[98:99], v[46:47] op_sel:[0,1]
v_pk_mul_f32 v[100:101], v[100:101], v[46:47] op_sel:[0,1]
v_pk_mul_f32 v[102:103], v[102:103], v[46:47] op_sel:[0,1]
C_ 80,96,97
C_ 81,98,99
C_ 82,100,101
C_ 83,102,103
ds_write_b128 v12, v[80:83] offset:14336
.p2align 3
R_ v104,a208
R_ v105,a209
R_ v106,a210
R_ v107,a211
R_ v108,a228
R_ v109,a229
R_ v110,a230
R_ v111,a231
v_pk_mul_f32 v[104:105], v[104:105], v[48:49] op_sel_hi:[1,0]
v_pk_mul_f32 v[106:107], v[106:107], v[48:49] op_sel_hi:[1,0]
v_pk_mul_f32 v[108:109], v[108:109], v[48:49] op_sel_hi:[1,0]
v_pk_mul_f32 v[110:111], v[110:111], v[48:49] op_sel_hi:[1,0]
C_ 84,104,105
C_ 85,106,107
C_ 86,108,109
C_ 87,110,111
ds_write_b128 v12, v[84:87] offset:15360
s_waitcnt vmcnt(6) lgkmcnt(0)
s_barrier
Dv 30,%0,6144
Dv 34,v1,6144
Dv 38,%0,7168
.p2align 3
R_ v96,a212
R_ v97,a213
R_ v98,a214
R_ v99,a215
R_ v100,a232
R_ v101,a233
R_ v102,a234
R_ v103,a235
v_pk_mul_f32 v[96:97], v[96:97], v[48:49] op_sel:[0,1]
v_pk_mul_f32 v[98:99], v[98:99], v[48:49] op_sel:[0,1]
v_pk_mul_f32 v[100:101], v[100:101], v[48:49] op_sel:[0,1]
v_pk_mul_f32 v[102:103], v[102:103], v[48:49] op_sel:[0,1]
C_ 88,96,97
C_ 89,98,99
C_ 90,100,101
C_ 91,102,103
ds_write_b128 v12, v[88:91] offset:16384
.p2align 3
R_ v104,a216
R_ v105,a217
R_ v106,a218
R_ v107,a219
R_ v108,a236
R_ v109,a237
R_ v110,a238
R_ v111,a239
v_pk_mul_f32 v[104:105], v[104:105], v[50:51] op_sel_hi:[1,0]
v_pk_mul_f32 v[106:107], v[106:107], v[50:51] op_sel_hi:[1,0]
v_pk_mul_f32 v[108:109], v[108:109], v[50:51] op_sel_hi:[1,0]
v_pk_mul_f32 v[110:111], v[110:111], v[50:51] op_sel_hi:[1,0]
C_ 92,104,105
C_ 93,106,107
C_ 94,108,109
C_ 95,110,111
ds_write_b128 v12, v[92:95] offset:17408
.Latt3:
W_ 4
Zvvv 96,30,136
Dv 42,v1,7168
R_ v116,a80
Zvvv 100,30,140
R_ v117,a81
R_ v118,a82
Zvvv 104,30,144
R_ v119,a83
R_ v120,a100
Zvvv 108,30,148
R_ v121,a101
R_ v122,a102
Zvvv 112,30,152
W_ 4
R_ v123,a103
U_ v116,v116,v46
Mvvv 96,34,156
Dv 30,%0,8192
U_ v117,v117,v46
Mvvv 100,34,160
U_ v118,v118,v46
U_ v119,v119,v46
Mvvv 104,34,164
U_ v120,v120,v46
U_ v121,v121,v46
Mvvv 108,34,168
U_ v122,v122,v46
U_ v123,v123,v46
Mvvv 112,34,172
W_ 4
C_ 20,116,117
C_ 21,118,119
Mvvv 96,38,176
Dv 34,v1,8192
C_ 22,120,121
Mvvv 100,38,180
C_ 23,122,123
R_ v124,a84
Mvvv 104,38,184
R_ v125,a85
R_ v126,a86
Mvvv 108,38,188
R_ v127,a87
R_ v128,a104
Mvvv 112,38,192
W_ 2
R_ v129,a105
R_ v130,a106
Mvvv 96,42,196
Dv 38,%0,0
R_ v131,a107
Mvvv 100,42,200
U_ v124,v124,v47
U_ v125,v125,v47
Mvvv 104,42,204
U_ v126,v126,v47
U_ v127,v127,v47
Mvvv 108,42,208
U_ v128,v128,v47
U_ v129,v129,v47
Mvvv 112,42,212
W_ 2
U_ v130,v130,v47
U_ v131,v131,v47
Mvvv 96,30,216
Dv 42,v1,0
C_ 24,124,125
Mvvv 100,30,220
C_ 25,126,127
C_ 26,128,129
Mvvv 104,30,224
C_ 27,130,131
R_ v116,a88
Mvvv 108,30,228
R_ v117,a89
R_ v118,a90
Mvvv 112,30,232
W_ 2
R_ v119,a91
R_ v120,a108
Mvvv 96,34,236
Dv 30,%0,1024
R_ v121,a109
Mvvv 100,34,240
R_ v122,a110
R_ v123,a111
Mvvv 104,34,244
U_ v116,v116,v48
U_ v117,v117,v48
Mvvv 108,34,248
U_ v118,v118,v48
U_ v119,v119,v48
Mvvv 112,34,252
W_ 2
U_ v120,v120,v48
U_ v121,v121,v48
Zvvv 76,38,136
Dv 34,v1,1024
U_ v122,v122,v48
Zvvv 80,38,140
U_ v123,v123,v48
C_ 52,116,117
Zvvv 84,38,144
C_ 53,118,119
C_ 54,120,121
Zvvv 88,38,148
C_ 55,122,123
R_ v124,a120
Zvvv 92,38,152
W_ 2
R_ v125,a121
R_ v126,a122
Mvvv 76,42,156
Dv 38,%0,2048
R_ v127,a123
Mvvv 80,42,160
R_ v128,a140
R_ v129,a141
Mvvv 84,42,164
R_ v130,a142
R_ v131,a143
Mvvv 88,42,168
U_ v124,v124,v46
U_ v125,v125,v46
Mvvv 92,42,172
W_ 2
U_ v126,v126,v46
U_ v127,v127,v46
Mvvv 76,30,176
Dv 42,v1,2048
U_ v128,v128,v46
Mvvv 80,30,180
U_ v129,v129,v46
U_ v130,v130,v46
Mvvv 84,30,184
U_ v131,v131,v46
C_ 16,124,125
Mvvv 88,30,188
C_ 17,126,127
C_ 18,128,129
Mvvv 92,30,192
s_waitcnt vmcnt(0) lgkmcnt(0)
s_barrier
Mvvv 76,34,196
S_ s65,s61,24576
Dv 30,%0,18432
C_ 19,130,131
Mvvv 76,38,216
S_ m0,s51,0
v_max3_f32 v56, v96, v97, v98
v_max_f32 v56, v56, v99
Mvvv 76,42,236
Ln v7,32,s65,0
Mvvv 80,34,200
v_max3_f32 v60, v100, v101, v102
v_max_f32 v60, v60, v103
Mvvv 80,38,220
Ln v8,32,s65,3072
Mvvv 80,42,240
S_ m0,s51,896
v_max3_f32 v64, v104, v105, v106
v_max_f32 v64, v64, v107
Mvvv 84,34,204
Ln v7,32,s65,128
Mvvv 84,38,224
v_max3_f32 v68, v108, v109, v110
v_max_f32 v68, v68, v111
Mvvv 84,42,244
Ln v8,32,s65,3200
Mvvv 88,34,208
S_ m0,s51,1792
v_max3_f32 v72, v112, v113, v114
v_max_f32 v72, v72, v115
Mvvv 88,38,228
Ln v7,32,s65,256
Mvvv 88,42,248
v_max3_f32 v56, v56, v76, v77
v_max3_f32 v56, v56, v78, v79
Mvvv 92,34,212
Ln v8,32,s65,3328
Mvvv 92,38,232
v_max3_f32 v60, v60, v80, v81
v_max3_f32 v60, v60, v82, v83
Mvvv 92,42,252
v_max3_f32 v64, v64, v84, v85
v_max3_f32 v64, v64, v86, v87
Dv 34,v1,18432
.p2align 3
v_mov_b32 v11, 0x3f803f80
v_permlane32_swap_b32 v56, v60
v_max3_f32 v68, v68, v88, v89
v_max3_f32 v68, v68, v90, v91
v_max_f32 v56, v56, v60
v_max3_f32 v72, v72, v92, v93
v_max3_f32 v72, v72, v94, v95
v_mov_b32 v73, v72
v_permlane32_swap_b32 v64, v68
v_max_f32 v64, v64, v68
v_permlane32_swap_b32 v72, v73
v_max_f32 v72, v72, v73
v_mov_b32 v73, v72
v_permlane16_swap_b32 v56, v64
v_max_f32 v56, v56, v64
v_permlane16_swap_b32 v72, v73
B_ v56,0xc0e00000,v56
v_max_f32 v72, v72, v73
v_mov_b32 v64, v56
B_ v72,0xc0e00000,v72
v_mov_b32 v73, v72
v_permlane16_swap_b32 v56, v64
v_mov_b32 v60, v56
v_mov_b32 v68, v64
v_mov_b64 v[74:75], v[72:73]
v_permlane32_swap_b32 v56, v60
v_permlane32_swap_b32 v64, v68
v_mov_b32 v57, v56
v_mov_b32 v61, v60
v_mov_b32 v65, v64
v_mov_b32 v69, v68
v_pk_add_f32 v[96:97], v[96:97], v[56:57]
v_pk_add_f32 v[98:99], v[98:99], v[56:57]
v_pk_add_f32 v[76:77], v[76:77], v[56:57]
v_pk_add_f32 v[78:79], v[78:79], v[56:57]
v_mov_b64 v[58:59], v[56:57]
v_pk_add_f32 v[100:101], v[100:101], v[60:61]
v_pk_add_f32 v[102:103], v[102:103], v[60:61]
v_pk_add_f32 v[80:81], v[80:81], v[60:61]
v_pk_add_f32 v[82:83], v[82:83], v[60:61]
v_mov_b64 v[62:63], v[60:61]
v_pk_add_f32 v[104:105], v[104:105], v[64:65]
v_pk_add_f32 v[106:107], v[106:107], v[64:65]
v_pk_add_f32 v[84:85], v[84:85], v[64:65]
v_pk_add_f32 v[86:87], v[86:87], v[64:65]
v_mov_b64 v[66:67], v[64:65]
v_pk_add_f32 v[108:109], v[108:109], v[68:69]
v_pk_add_f32 v[110:111], v[110:111], v[68:69]
v_pk_add_f32 v[88:89], v[88:89], v[68:69]
v_pk_add_f32 v[90:91], v[90:91], v[68:69]
v_mov_b64 v[70:71], v[68:69]
v_pk_add_f32 v[112:113], v[112:113], v[72:73]
v_pk_add_f32 v[114:115], v[114:115], v[72:73]
v_pk_add_f32 v[92:93], v[92:93], v[72:73]
v_pk_add_f32 v[94:95], v[94:95], v[72:73]
E_ 96
E_ 97
E_ 98
E_ 99
E_ 76
E_ 77
E_ 78
E_ 79
C_ 99,98,99
C_ 98,96,97
C_ 96,76,77
C_ 97,78,79
E_ 100
E_ 101
E_ 102
E_ 103
E_ 80
E_ 81
E_ 82
E_ 83
C_ 103,102,103
C_ 102,100,101
C_ 100,80,81
C_ 101,82,83
.Lstep3_1:
W_ 1
Nvvv 116,30,136,56
s_nop 0
Dv 38,%0,19456
Dv 42,v1,19456
W_ 2
Mvvv 116,34,156
E_ 104
Nvvv 120,30,140,60
E_ 105
Mvvv 120,34,160
E_ 106
Nvvv 124,30,144,64
E_ 107
Mvvv 124,34,164
E_ 84
Nvvv 128,30,148,68
E_ 85
Mvvv 128,34,168
E_ 86
Nvvv 132,30,152,72
E_ 87
Mvvv 132,34,172
W_ 0
C_ 107,106,107
C_ 106,104,105
Mvvv 132,38,192
s_nop 0
Dv 30,%0,20480
Dv 34,v1,20480
Mvvv 132,42,212
C_ 104,84,85
C_ 105,86,87
Mvvv 128,38,188
E_ 108
Mvvv 128,42,208
E_ 109
Mvvv 124,38,184
E_ 110
Mvvv 124,42,204
E_ 111
Mvvv 120,38,180
E_ 88
Mvvv 120,42,200
E_ 89
Mvvv 116,38,176
E_ 90
Mvvv 116,42,196
W_ 0
E_ 91
Mvvv 116,30,216
s_nop 0
Dv 38,%0,12288
Dv 42,v1,12288
Mvvv 116,34,236
C_ 111,110,111
C_ 110,108,109
Mvvv 120,30,220
C_ 108,88,89
C_ 109,90,91
Mvvv 120,34,240
E_ 112
Mvvv 124,30,224
E_ 113
Mvvv 124,34,244
E_ 114
Mvvv 128,30,228
E_ 115
Mvvv 128,34,248
E_ 92
Mvvv 132,30,232
E_ 93
Mvvv 132,34,252
W_ 0
E_ 94
Nvvv 76,38,136,56
s_nop 0
Dv 30,%0,13312
Dv 34,v1,13312
Mvvv 76,42,156
E_ 95
Nvvv 80,38,140,60
C_ 115,114,115
C_ 114,112,113
Mvvv 80,42,160
C_ 112,92,93
C_ 113,94,95
Nvvv 84,38,144,64
E_ 116
Mvvv 84,42,164
E_ 117
Nvvv 88,38,148,68
E_ 118
Mvvv 88,42,168
E_ 119
Nvvv 92,38,152,72
C_ 119,118,119
C_ 118,116,117
Mvvv 92,42,172
W_ 0
E_ 120
Mvvv 92,30,192
s_nop 0
Dv 38,%0,14336
Dv 42,v1,14336
Mvvv 92,34,212
X_ a180,v11
X_ a181,v11
Mvvv 88,30,188
X_ a182,v11
X_ a183,v11
Mvvv 88,34,208
E_ 121
Mvvv 84,30,184
E_ 122
Mvvv 84,34,204
E_ 123
Mvvv 80,30,180
C_ 123,122,123
C_ 122,120,121
Mvvv 80,34,200
E_ 124
Mvvv 76,30,176
E_ 125
Mvvv 76,34,196
S_ s65,s61,12288
E_ 126
W_ 1
Mvvv 76,38,216
S_ m0,s51,12288
E_ 127
W_ 0
Mvvv 76,42,236
s_waitcnt vmcnt(18) lgkmcnt(0)
s_barrier
C_ 127,126,127
Mvvv 80,38,220
Ln v9,36,s65,0
Mvvv 80,42,240
C_ 126,124,125
Mvvv 84,38,224
Ln v9,36,s65,3072
S_ m0,s51,13184
Mvvv 84,42,244
s_nop 0
Tv 30,v2,24576
Tv 32,v2,30720
Mvvv 88,38,228
Ln v9,36,s65,128
Mvvv 88,42,248
s_nop 0
Tv 34,v3,24576
Tv 36,v3,30720
Mvvv 92,38,232
Ln v9,36,s65,3200
S_ m0,s51,14080
Mvvv 92,42,252
E_ 128
Zaav 220,180,96
Ln v9,36,s65,256
Zaav 224,180,100
E_ 129
Zaav 228,180,104
Ln v9,36,s65,3328
Zaav 232,180,108
E_ 130
Zaav 236,180,112
W_ 0
E_ 131
Zavv 0,30,96
s_nop 0
Tv 38,v4,24576
Tv 40,v4,30720
Zavv 20,34,96
C_ 131,130,131
C_ 130,128,129
Zavv 24,34,100
s_nop 0
Tv 42,v5,24576
Tv 44,v5,30720
Zavv 4,30,100
E_ 132
Zavv 8,30,104
E_ 133
Zavv 28,34,104
R_ v46,a220
R_ v47,a224
Zavv 32,34,108
R_ v48,a228
R_ v49,a232
Zavv 12,30,108
R_ v50,a236
Zavv 16,30,112
E_ 134
Zavv 36,34,112
W_ 0
E_ 135
Zavv 56,38,112
s_nop 0
Tv 30,v2,25600
Tv 32,v2,31744
Zavv 76,42,112
C_ 135,134,135
C_ 134,132,133
Zavv 72,42,108
s_nop 0
Tv 34,v3,25600
Tv 36,v3,31744
Zavv 52,38,108
E_ 76
Zavv 48,38,104
E_ 77
Zavv 68,42,104
E_ 78
Zavv 64,42,100
E_ 79
Zavv 44,38,100
C_ 116,76,77
C_ 117,78,79
Zavv 40,38,96
E_ 80
Zavv 60,42,96
W_ 0
E_ 81
Zavv 80,30,96
s_nop 0
Tv 38,v4,25600
Tv 40,v4,31744
Zavv 100,34,96
E_ 82
Zavv 104,34,100
s_nop 0
Tv 42,v5,25600
Tv 44,v5,31744
Zavv 84,30,100
E_ 83
Zavv 88,30,104
C_ 120,80,81
C_ 121,82,83
Zavv 108,34,104
E_ 84
Zavv 112,34,108
E_ 85
Zavv 92,30,108
E_ 86
Zavv 96,30,112
E_ 87
Zavv 116,34,112
W_ 0
C_ 124,84,85
C_ 125,86,87
Zavv 136,38,112
s_nop 0
Tv 30,v2,26624
Tv 32,v2,32768
Zavv 156,42,112
E_ 88
Zavv 152,42,108
s_nop 0
Tv 34,v3,26624
Tv 36,v3,32768
Zavv 132,38,108
E_ 89
Zavv 128,38,104
E_ 90
Zavv 148,42,104
E_ 91
Zavv 144,42,100
C_ 128,88,89
C_ 129,90,91
Zavv 124,38,100
E_ 92
Zavv 120,38,96
E_ 93
Zavv 140,42,96
W_ 0
E_ 94
Zavv 160,30,96
s_nop 0
Tv 38,v4,26624
Tv 40,v4,32768
Zavv 180,34,96
E_ 95
Zavv 184,34,100
s_nop 0
Tv 42,v5,26624
Tv 44,v5,32768
Zavv 164,30,100
C_ 132,92,93
C_ 133,94,95
Zavv 168,30,104
v_mov_b32 v92, 0x3f803f80
v_mov_b32 v93, 0x3f803f80
Zavv 188,34,104
S_ s65,s61,36864
v_mov_b32 v94, 0x3f803f80
v_mov_b32 v95, 0x3f803f80
Zavv 192,34,108
S_ m0,s51,24576
Zavv 172,30,108
s_waitcnt vmcnt(6) lgkmcnt(0)
s_barrier
Zavv 176,30,112
Ln v7,32,s65,0
Zavv 196,34,112
Zavv 216,38,112
Dv 30,%0,6144
Zavv 236,42,112
Dv 34,v1,6144
Zavv 232,42,108
Ln v8,32,s65,3072
S_ m0,s51,25472
Zavv 212,38,108
Zavv 208,38,104
Ln v7,32,s65,128
Zavv 228,42,104
Zavv 224,42,100
Ln v8,32,s65,3200
S_ m0,s51,26368
Zavv 204,38,100
Zavv 200,38,96
Ln v7,32,s65,256
Zavv 220,42,96
Ln v8,32,s65,3328
W_ 0
s_nop 7
s_branch .Lstep1_2
.Latt1:
R_ v13,a248
S_ s53,s51,36864
v_add_u32 v12, s53, v13
s_mul_i32 s53, s4, 10240
S_ s53,s53,0
.Lpad_a1_0:
s_cbranch_execz .Lhop_a1_r
v_add_u32 v13, s53, v13
W_ 2
Zvvv 96,30,136
Dv 42,v1,44032
R_ v2,a245
Zvvv 100,30,140
ds_write_b128 v13, a[0:3] offset:0
Zvvv 104,30,144
v_xor_b32 v3, 32, v2
v_xor_b32 v4, 64, v2
Zvvv 108,30,148
ds_write_b128 v13, a[4:7] offset:1024
Zvvv 112,30,152
W_ 4
v_xor_b32 v5, 96, v2
s_mov_b64 s[24:25], s[16:17]
Mvvv 96,34,156
s_mov_b32 s26, 7864320
Dv 30,%0,45056
s_mov_b64 s[28:29], s[22:23]
Mvvv 100,34,160
ds_write_b128 v13, a[8:11] offset:2048
Mvvv 104,34,164
s_mov_b32 s30, 1638400
R_ v20,a80
s_mul_i32 s52, s4, 10240
Mvvv 108,34,168
ds_write_b128 v13, a[12:15] offset:3072
Mvvv 112,34,172
W_ 6
R_ v21,a81
R_ v22,a82
Mvvv 96,38,176
Dv 34,v1,45056
R_ v23,a83
Mvvv 100,38,180
ds_write_b128 v13, a[16:19] offset:4096
Mvvv 104,38,184
R_ v24,a84
R_ v25,a85
Mvvv 108,38,188
ds_write_b128 v13, a[40:43] offset:5120
Mvvv 112,38,192
W_ 8
R_ v26,a86
R_ v27,a87
Mvvv 96,42,196
Dv 38,%0,36864
R_ v52,a88
Mvvv 100,42,200
ds_write_b128 v13, a[44:47] offset:6144
Mvvv 104,42,204
R_ v53,a89
R_ v54,a90
Mvvv 108,42,208
ds_write_b128 v13, a[48:51] offset:7168
Mvvv 112,42,212
W_ 8
R_ v55,a91
v_accvgpr_mov_b32 a240, a92
Mvvv 96,30,216
Dv 42,v1,36864
v_accvgpr_mov_b32 a241, a93
Mvvv 100,30,220
ds_write_b128 v13, a[52:55] offset:8192
Mvvv 104,30,224
v_accvgpr_mov_b32 a242, a94
v_accvgpr_mov_b32 a243, a95
Mvvv 108,30,228
ds_write_b128 v13, a[56:59] offset:9216
Mvvv 112,30,232
W_ 8
v_accvgpr_mov_b32 a244, a96
v_accvgpr_mov_b32 a245, a97
Mvvv 96,34,236
Dv 30,%0,37888
v_accvgpr_mov_b32 a246, a98
Mvvv 100,34,240
v_accvgpr_mov_b32 a247, a99
R_ v16,a120
Mvvv 104,34,244
R_ v17,a121
R_ v18,a122
Mvvv 108,34,248
R_ v19,a123
R_ v28,a124
Mvvv 112,34,252
W_ 6
R_ v29,a125
R_ v51,a126
Zvvv 76,38,136
Dv 34,v1,37888
R_ v6,a127
Zvvv 80,38,140
R_ v14,a128
R_ v15,a129
Zvvv 84,38,144
v_accvgpr_mov_b32 a249, a130
v_accvgpr_mov_b32 a250, a131
Zvvv 88,38,148
v_max3_f32 v56, v96, v97, v98
v_max_f32 v56, v56, v99
Zvvv 92,38,152
W_ 4
v_max3_f32 v60, v100, v101, v102
v_max_f32 v60, v60, v103
Mvvv 76,42,156
Dv 38,%0,38912
v_max3_f32 v64, v104, v105, v106
Mvvv 80,42,160
v_max_f32 v64, v64, v107
Mvvv 84,42,164
v_max3_f32 v68, v108, v109, v110
v_max_f32 v68, v68, v111
Mvvv 88,42,168
v_max3_f32 v72, v112, v113, v114
v_max_f32 v72, v72, v115
Mvvv 92,42,172
W_ 2
Mvvv 76,30,176
Dv 42,v1,38912
Mvvv 80,30,180
Mvvv 84,30,184
Mvvv 88,30,188
Mvvv 92,30,192
s_waitcnt vmcnt(30) lgkmcnt(0)
s_barrier
Mvvv 76,34,196
Dv 30,%0,55296
Mvvv 76,38,216
Mvvv 76,42,236
Mvvv 80,34,200
Mvvv 80,38,220
Mvvv 80,42,240
Mvvv 84,34,204
s_nop 3
v_max3_f32 v56, v56, v76, v77
v_max3_f32 v56, v56, v78, v79
Mvvv 84,38,224
Mvvv 84,42,244
Mvvv 88,34,208
v_max3_f32 v60, v60, v80, v81
v_max3_f32 v60, v60, v82, v83
Mvvv 88,38,228
Mvvv 88,42,248
Mvvv 92,34,212
s_nop 1
v_max3_f32 v64, v64, v84, v85
v_max3_f32 v64, v64, v86, v87
Mvvv 92,38,232
Mvvv 92,42,252
Dv 34,v1,55296
.p2align 3
v_mov_b32 v11, 0x3f803f80
v_permlane32_swap_b32 v56, v60
v_max3_f32 v68, v68, v88, v89
v_max3_f32 v68, v68, v90, v91
v_max_f32 v56, v56, v60
s_nop 1
v_max3_f32 v72, v72, v92, v93
v_max3_f32 v72, v72, v94, v95
v_mov_b32 v73, v72
v_permlane32_swap_b32 v64, v68
v_max_f32 v64, v64, v68
v_permlane32_swap_b32 v72, v73
v_max_f32 v72, v72, v73
v_mov_b32 v73, v72
v_permlane16_swap_b32 v56, v64
v_max_f32 v56, v56, v64
v_permlane16_swap_b32 v72, v73
B_ v56,0xc0e00000,v56
v_max_f32 v72, v72, v73
v_mov_b32 v64, v56
B_ v72,0xc0e00000,v72
v_mov_b32 v73, v72
v_permlane16_swap_b32 v56, v64
v_mov_b32 v60, v56
v_mov_b32 v68, v64
v_mov_b64 v[74:75], v[72:73]
v_permlane32_swap_b32 v56, v60
v_permlane32_swap_b32 v64, v68
v_mov_b32 v57, v56
v_mov_b32 v61, v60
v_mov_b32 v65, v64
v_mov_b32 v69, v68
v_pk_add_f32 v[96:97], v[96:97], v[56:57]
v_pk_add_f32 v[98:99], v[98:99], v[56:57]
v_pk_add_f32 v[76:77], v[76:77], v[56:57]
v_pk_add_f32 v[78:79], v[78:79], v[56:57]
v_mov_b64 v[58:59], v[56:57]
v_pk_add_f32 v[100:101], v[100:101], v[60:61]
v_pk_add_f32 v[102:103], v[102:103], v[60:61]
v_pk_add_f32 v[80:81], v[80:81], v[60:61]
v_pk_add_f32 v[82:83], v[82:83], v[60:61]
v_mov_b64 v[62:63], v[60:61]
v_pk_add_f32 v[104:105], v[104:105], v[64:65]
v_pk_add_f32 v[106:107], v[106:107], v[64:65]
v_pk_add_f32 v[84:85], v[84:85], v[64:65]
v_pk_add_f32 v[86:87], v[86:87], v[64:65]
v_mov_b64 v[66:67], v[64:65]
v_pk_add_f32 v[108:109], v[108:109], v[68:69]
v_pk_add_f32 v[110:111], v[110:111], v[68:69]
v_pk_add_f32 v[88:89], v[88:89], v[68:69]
v_pk_add_f32 v[90:91], v[90:91], v[68:69]
v_mov_b64 v[70:71], v[68:69]
v_pk_add_f32 v[112:113], v[112:113], v[72:73]
v_pk_add_f32 v[114:115], v[114:115], v[72:73]
v_pk_add_f32 v[92:93], v[92:93], v[72:73]
v_pk_add_f32 v[94:95], v[94:95], v[72:73]
E_ 96
E_ 97
E_ 98
E_ 99
E_ 76
E_ 77
E_ 78
E_ 79
C_ 99,98,99
C_ 98,96,97
C_ 96,76,77
C_ 97,78,79
E_ 100
E_ 101
E_ 102
E_ 103
E_ 80
E_ 81
E_ 82
E_ 83
C_ 103,102,103
C_ 102,100,101
C_ 100,80,81
C_ 101,82,83
.Lstep1_1:
W_ 1
Nvvv 116,30,136,56
s_nop 0
Dv 38,%0,56320
Dv 42,v1,56320
W_ 2
Mvvv 116,34,156
E_ 104
Nvvv 120,30,140,60
E_ 105
Mvvv 120,34,160
E_ 106
Nvvv 124,30,144,64
E_ 107
Mvvv 124,34,164
E_ 84
Nvvv 128,30,148,68
E_ 85
Mvvv 128,34,168
E_ 86
Nvvv 132,30,152,72
E_ 87
Mvvv 132,34,172
W_ 0
C_ 107,106,107
C_ 106,104,105
Mvvv 132,38,192
s_nop 0
Dv 30,%0,57344
Dv 34,v1,57344
Mvvv 132,42,212
C_ 104,84,85
C_ 105,86,87
Mvvv 128,38,188
E_ 108
Mvvv 128,42,208
E_ 109
Mvvv 124,38,184
E_ 110
Mvvv 124,42,204
E_ 111
Mvvv 120,38,180
E_ 88
Mvvv 120,42,200
E_ 89
Mvvv 116,38,176
E_ 90
Mvvv 116,42,196
W_ 0
E_ 91
Mvvv 116,30,216
s_nop 0
Dv 38,%0,49152
Dv 42,v1,49152
Mvvv 116,34,236
C_ 111,110,111
C_ 110,108,109
Mvvv 120,30,220
C_ 108,88,89
C_ 109,90,91
Mvvv 120,34,240
E_ 112
Mvvv 124,30,224
E_ 113
Mvvv 124,34,244
E_ 114
Mvvv 128,30,228
E_ 115
Mvvv 128,34,248
E_ 92
Mvvv 132,30,232
E_ 93
Mvvv 132,34,252
W_ 0
E_ 94
Nvvv 76,38,136,56
s_nop 0
Dv 30,%0,50176
Dv 34,v1,50176
Mvvv 76,42,156
E_ 95
Nvvv 80,38,140,60
C_ 115,114,115
C_ 114,112,113
Mvvv 80,42,160
C_ 112,92,93
C_ 113,94,95
Nvvv 84,38,144,64
E_ 116
Mvvv 84,42,164
E_ 117
Nvvv 88,38,148,68
E_ 118
Mvvv 88,42,168
E_ 119
Nvvv 92,38,152,72
C_ 119,118,119
C_ 118,116,117
Mvvv 92,42,172
W_ 0
E_ 120
Mvvv 92,30,192
s_nop 0
Dv 38,%0,51200
Dv 42,v1,51200
Mvvv 92,34,212
X_ a180,v11
X_ a181,v11
Mvvv 88,30,188
X_ a182,v11
X_ a183,v11
Mvvv 88,34,208
ds_write_b128 v12, a[132:135] offset:0
Mvvv 84,30,184
E_ 121
Mvvv 84,34,204
E_ 122
Mvvv 80,30,180
ds_write_b128 v12, a[136:139] offset:1024
Mvvv 80,34,200
)A"
R"A(E_ 123
Mvvv 76,30,176
C_ 123,122,123
C_ 122,120,121
Mvvv 76,34,196
ds_write_b128 v12, a[160:163] offset:2048
W_ 4
Mvvv 76,38,216
W_ 3
E_ 124
Mvvv 76,42,236
s_waitcnt vmcnt(12) lgkmcnt(0)
s_barrier
Mvvv 80,38,220
ds_write_b128 v12, a[164:167] offset:3072
Mvvv 80,42,240
E_ 125
Mvvv 84,38,224
E_ 126
Mvvv 84,42,244
s_nop 0
Tv 30,v2,24576
Tv 32,v2,30720
Mvvv 88,38,228
ds_write_b128 v12, a[168:171] offset:4096
Mvvv 88,42,248
s_nop 0
Tv 34,v3,24576
Tv 36,v3,30720
Mvvv 92,38,232
E_ 127
Mvvv 92,42,252
ds_write_b128 v12, a[172:175] offset:5120
Zaav 220,180,96
C_ 127,126,127
C_ 126,124,125
Zaav 224,180,100
E_ 128
Zaav 228,180,104
ds_write_b128 v12, a[176:179] offset:12288
Zaav 232,180,108
E_ 129
Zaav 236,180,112
W_ 2
E_ 130
Zavv 0,30,96
s_nop 0
Tv 38,v4,24576
Tv 40,v4,30720
Zavv 20,34,96
ds_write_b128 v12, a[200:203] offset:13312
Zavv 24,34,100
.Lpad_a1_1:
s_cbranch_execz .Lhop_a1_r
Tv 42,v5,24576
Tv 44,v5,30720
Zavv 4,30,100
E_ 131
Zavv 8,30,104
ds_write_b128 v12, a[204:207] offset:14336
Zavv 28,34,104
R_ v46,a220
R_ v47,a224
Zavv 32,34,108
R_ v48,a228
R_ v49,a232
Zavv 12,30,108
R_ v50,a236
C_ 131,130,131
Zavv 16,30,112
ds_write_b128 v12, a[208:211] offset:15360
Zavv 36,34,112
W_ 2
C_ 130,128,129
Zavv 56,38,112
s_nop 0
Tv 30,v2,25600
Tv 32,v2,31744
Zavv 76,42,112
ds_write_b128 v12, a[212:215] offset:16384
Zavv 72,42,108
s_nop 0
Tv 34,v3,25600
Tv 36,v3,31744
Zavv 52,38,108
E_ 132
Zavv 48,38,104
ds_write_b128 v12, a[216:219] offset:17408
Zavv 68,42,104
E_ 133
Zavv 64,42,100
E_ 134
Zavv 44,38,100
E_ 135
Zavv 40,38,96
C_ 135,134,135
C_ 134,132,133
Zavv 60,42,96
W_ 1
E_ 76
Zavv 80,30,96
s_nop 0
Tv 38,v4,25600
Tv 40,v4,31744
Zavv 100,34,96
E_ 77
Zavv 104,34,100
s_nop 0
Tv 42,v5,25600
Tv 44,v5,31744
Zavv 84,30,100
E_ 78
Zavv 88,30,104
E_ 79
Zavv 108,34,104
C_ 116,76,77
C_ 117,78,79
Zavv 112,34,108
E_ 80
Zavv 92,30,108
E_ 81
Zavv 96,30,112
E_ 82
Zavv 116,34,112
W_ 0
E_ 83
Zavv 136,38,112
s_nop 0
Tv 30,v2,26624
Tv 32,v2,32768
Zavv 156,42,112
C_ 120,80,81
C_ 121,82,83
Zavv 152,42,108
s_nop 0
Tv 34,v3,26624
Tv 36,v3,32768
Zavv 132,38,108
E_ 84
Zavv 128,38,104
E_ 85
Zavv 148,42,104
E_ 86
Zavv 144,42,100
E_ 87
Zavv 124,38,100
C_ 124,84,85
C_ 125,86,87
Zavv 120,38,96
E_ 88
Zavv 140,42,96
W_ 0
E_ 89
Zavv 160,30,96
s_nop 0
Tv 38,v4,26624
Tv 40,v4,32768
Zavv 180,34,96
E_ 90
Zavv 184,34,100
s_nop 0
Tv 42,v5,26624
Tv 44,v5,32768
Zavv 164,30,100
E_ 91
Zavv 168,30,104
C_ 128,88,89
C_ 129,90,91
Zavv 188,34,104
S_ s65,s61,36864
E_ 92
Zavv 192,34,108
S_ m0,s51,24576
E_ 93
Zavv 172,30,108
s_waitcnt vmcnt(6) lgkmcnt(0)
s_barrier
Zavv 176,30,112
Ln v7,32,s65,0
Zavv 196,34,112
E_ 94
Zavv 216,38,112
Dv 30,%0,6144
Zavv 236,42,112
Dv 34,v1,6144
Zavv 232,42,108
Ln v8,32,s65,3072
S_ m0,s51,25472
Zavv 212,38,108
E_ 95
Zavv 208,38,104
Ln v7,32,s65,128
Zavv 228,42,104
C_ 132,92,93
C_ 133,94,95
Zavv 224,42,100
Ln v8,32,s65,3200
S_ m0,s51,26368
Zavv 204,38,100
v_mov_b32 v92, 0x3f803f80
v_mov_b32 v93, 0x3f803f80
Zavv 200,38,96
Ln v7,32,s65,256
Zavv 220,42,96
v_mov_b32 v94, 0x3f803f80
v_mov_b32 v95, 0x3f803f80
Ln v8,32,s65,3328
.Lstep1_2:
s_nop 0
Zvvv 76,92,116
Dv 38,%0,7168
Zvvv 80,92,120
Dv 42,v1,7168
Zvvv 84,92,124
Zvvv 88,92,128
Zvvv 92,92,132
W_ 2
Nvvv 96,30,136,56
Mvvv 96,34,156
Nvvv 100,30,140,60
s_nop 1
v_max3_f32 v10, v76, v80, v84
s_nop 0
v_max3_f32 v10, v10, v88, v92
Mvvv 100,34,160
v_cmp_lt_f32 vcc, 0x42000000, v10
s_cbranch_vccnz .Ltramp1_1
Nvvv 104,30,144,64
A_ v46,v46,v76
A_ v47,v47,v80
Mvvv 104,34,164
A_ v48,v48,v84
A_ v49,v49,v88
Nvvv 108,30,148,68
A_ v50,v50,v92
Mvvv 108,34,168
Nvvv 112,30,152,72
Mvvv 112,34,172
W_ 0
Mvvv 112,38,192
Dv 30,%0,8192
Mvvv 112,42,212
Dv 34,v1,8192
Mvvv 108,38,188
Mvvv 108,42,208
Mvvv 104,38,184
Mvvv 104,42,204
Mvvv 100,38,180
Mvvv 100,42,200
Mvvv 96,38,176
Mvvv 96,42,196
W_ 0
Mvvv 96,30,216
Dv 38,%0,0
Mvvv 96,34,236
Dv 42,v1,0
Mvvv 100,30,220
Mvvv 100,34,240
Mvvv 104,30,224
Mvvv 104,34,244
Mvvv 108,30,228
Mvvv 108,34,248
s_nop 0
E_ 96
Mvvv 112,30,232
E_ 97
Mvvv 112,34,252
W_ 0
E_ 98
Nvvv 76,38,136,56
Dv 30,%0,1024
Mvvv 76,42,156
Dv 34,v1,1024
Nvvv 80,38,140,60
E_ 99
Mvvv 80,42,160
C_ 99,98,99
C_ 98,96,97
Nvvv 84,38,144,64
E_ 100
Mvvv 84,42,164
E_ 101
Nvvv 88,38,148,68
E_ 102
Mvvv 88,42,168
E_ 103
Nvvv 92,38,152,72
C_ 103,102,103
C_ 102,100,101
Mvvv 92,42,172
W_ 0
E_ 104
Mvvv 92,30,192
Dv 38,%0,2048
Mvvv 92,34,212
Dv 42,v1,2048
Mvvv 88,30,188
E_ 105
Mvvv 88,34,208
E_ 106
Mvvv 84,30,184
S_ s65,s61,24576
E_ 107
Mvvv 84,34,204
S_ m0,s51,0
C_ 107,106,107
C_ 106,104,105
Mvvv 80,30,180
s_waitcnt vmcnt(6) lgkmcnt(0)
s_barrier
Mvvv 80,34,200
Ln v9,36,s65,0
Mvvv 76,30,176
E_ 108
Mvvv 76,34,196
Ln v9,36,s65,3072
S_ m0,s51,896
Mvvv 76,38,216
Tv 30,v2,12288
Tv 32,v2,18432
Mvvv 76,42,236
Ln v9,36,s65,128
Mvvv 80,38,220
s_nop 0
Tv 34,v3,12288
Tv 36,v3,18432
Mvvv 80,42,240
Ln v9,36,s65,3200
S_ m0,s51,1792
Mvvv 84,38,224
E_ 109
Mvvv 84,42,244
Ln v9,36,s65,256
Mvvv 88,38,228
E_ 110
Mvvv 88,42,248
Ln v9,36,s65,3328
Mvvv 92,38,232
E_ 111
Mvvv 92,42,252
W_ 0
C_ 111,110,111
C_ 110,108,109
Mavv 0,30,116
s_nop 0
Tv 38,v4,12288
Tv 40,v4,18432
Mavv 20,34,116
E_ 112
Mavv 24,34,120
s_nop 0
Tv 42,v5,12288
Tv 44,v5,18432
Mavv 4,30,120
E_ 113
Mavv 8,30,124
E_ 114
Mavv 28,34,124
E_ 115
Mavv 32,34,128
C_ 115,114,115
C_ 114,112,113
Mavv 12,30,128
E_ 76
Mavv 16,30,132
E_ 77
Mavv 36,34,132
W_ 0
E_ 78
Mavv 56,38,132
s_nop 0
Tv 30,v2,13312
Tv 32,v2,19456
Mavv 76,42,132
E_ 79
Mavv 72,42,128
s_nop 0
Tv 34,v3,13312
Tv 36,v3,19456
Mavv 52,38,128
C_ 96,76,77
C_ 97,78,79
Mavv 48,38,124
E_ 80
Mavv 68,42,124
E_ 81
Mavv 64,42,120
E_ 82
Mavv 44,38,120
E_ 83
Mavv 40,38,116
C_ 100,80,81
C_ 101,82,83
Mavv 60,42,116
W_ 0
E_ 84
Mavv 80,30,116
s_nop 0
Tv 38,v4,13312
Tv 40,v4,19456
Mavv 100,34,116
E_ 85
Mavv 104,34,120
s_nop 0
Tv 42,v5,13312
Tv 44,v5,19456
Mavv 84,30,120
E_ 86
Mavv 88,30,124
E_ 87
Mavv 108,34,124
C_ 104,84,85
C_ 105,86,87
Mavv 112,34,128
E_ 88
Mavv 92,30,128
E_ 89
Mavv 96,30,132
E_ 90
Mavv 116,34,132
W_ 0
E_ 91
Mavv 136,38,132
s_nop 0
Tv 30,v2,14336
Tv 32,v2,20480
Mavv 156,42,132
C_ 108,88,89
C_ 109,90,91
Mavv 152,42,128
s_nop 0
Tv 34,v3,14336
Tv 36,v3,20480
Mavv 132,38,128
E_ 92
Mavv 128,38,124
E_ 93
Mavv 148,42,124
E_ 94
Mavv 144,42,120
E_ 95
Mavv 124,38,120
C_ 112,92,93
C_ 113,94,95
Mavv 120,38,116
v_mov_b32 v92, 0x3f803f80
v_mov_b32 v93, 0x3f803f80
Mavv 140,42,116
W_ 0
v_mov_b32 v94, 0x3f803f80
v_mov_b32 v95, 0x3f803f80
Mavv 160,30,116
s_nop 0
Tv 38,v4,14336
Tv 40,v4,20480
Mavv 180,34,116
Mavv 184,34,120
s_nop 0
Tv 42,v5,14336
Tv 44,v5,20480
Mavv 164,30,120
Mavv 168,30,124
Mavv 188,34,124
S_ s65,s61,49152
Mavv 192,34,128
S_ m0,s51,12288
Mavv 172,30,128
s_waitcnt vmcnt(6) lgkmcnt(0)
s_barrier
Mavv 176,30,132
Ln v7,32,s65,0
Mavv 196,34,132
Mavv 216,38,132
Dv 30,%0,30720
Mavv 236,42,132
Dv 34,v1,30720
Mavv 232,42,128
Ln v8,32,s65,3072
S_ m0,s51,13184
Mavv 212,38,128
Mavv 208,38,124
Ln v7,32,s65,128
Mavv 228,42,124
Mavv 224,42,120
Ln v8,32,s65,3200
S_ m0,s51,14080
Mavv 204,38,120
Mavv 200,38,116
Ln v7,32,s65,256
Mavv 220,42,116
s_cmp_eq_u32 s4, 0
s_cbranch_scc1 .Lhs_a1
.Lhs_a1_back:
.Lstep1_3:
Zvvv 76,92,96
Dv 38,%0,31744
Zvvv 80,92,100
Dv 42,v1,31744
Zvvv 84,92,104
Zvvv 88,92,108
Zvvv 92,92,112
W_ 2
Nvvv 116,30,136,56
Mvvv 116,34,156
Nvvv 120,30,140,60
s_nop 1
v_max3_f32 v10, v76, v80, v84
s_nop 0
v_max3_f32 v10, v10, v88, v92
Mvvv 120,34,160
v_cmp_lt_f32 vcc, 0x42000000, v10
s_cbranch_vccnz .Ltramp1_2
Nvvv 124,30,144,64
A_ v46,v46,v76
A_ v47,v47,v80
Mvvv 124,34,164
A_ v48,v48,v84
A_ v49,v49,v88
Nvvv 128,30,148,68
A_ v50,v50,v92
Mvvv 128,34,168
Ln v8,32,s65,3328
Nvvv 132,30,152,72
Mvvv 132,34,172
W_ 0
Mvvv 132,38,192
Dv 30,%0,32768
Mvvv 132,42,212
Dv 34,v1,32768
Mvvv 128,38,188
Mvvv 128,42,208
Mvvv 124,38,184
Mvvv 124,42,204
Mvvv 120,38,180
Mvvv 120,42,200
Mvvv 116,38,176
Mvvv 116,42,196
W_ 0
Mvvv 116,30,216
Dv 38,%0,24576
Mvvv 116,34,236
Dv 42,v1,24576
Mvvv 120,30,220
Mvvv 120,34,240
Mvvv 124,30,224
Mvvv 124,34,244
Mvvv 128,30,228
Mvvv 128,34,248
s_nop 0
E_ 116
Mvvv 132,30,232
E_ 117
Mvvv 132,34,252
W_ 0
E_ 118
Nvvv 76,38,136,56
Dv 30,%0,25600
Mvvv 76,42,156
Dv 34,v1,25600
Nvvv 80,38,140,60
E_ 119
Mvvv 80,42,160
C_ 119,118,119
C_ 118,116,117
Nvvv 84,38,144,64
E_ 120
Mvvv 84,42,164
E_ 121
Nvvv 88,38,148,68
E_ 122
Mvvv 88,42,168
E_ 123
Nvvv 92,38,152,72
C_ 123,122,123
C_ 122,120,121
Mvvv 92,42,172
W_ 0
E_ 124
Mvvv 92,30,192
Dv 38,%0,26624
Mvvv 92,34,212
Dv 42,v1,26624
Mvvv 88,30,188
E_ 125
Mvvv 88,34,208
E_ 126
Mvvv 84,30,184
S_ s65,s61,36864
E_ 127
Mvvv 84,34,204
S_ m0,s51,24576
C_ 127,126,127
C_ 126,124,125
Mvvv 80,30,180
s_waitcnt vmcnt(6) lgkmcnt(0)
s_barrier
Mvvv 80,34,200
Ln v9,36,s65,0
Mvvv 76,30,176
E_ 128
Mvvv 76,34,196
Ln v9,36,s65,3072
S_ m0,s51,25472
Mvvv 76,38,216
Tv 30,v2,0
Tv 32,v2,6144
Mvvv 76,42,236
Ln v9,36,s65,128
Mvvv 80,38,220
s_nop 0
Tv 34,v3,0
Tv 36,v3,6144
Mvvv 80,42,240
Ln v9,36,s65,3200
S_ m0,s51,26368
Mvvv 84,38,224
E_ 129
Mvvv 84,42,244
Ln v9,36,s65,256
Mvvv 88,38,228
E_ 130
Mvvv 88,42,248
Ln v9,36,s65,3328
Mvvv 92,38,232
E_ 131
Mvvv 92,42,252
W_ 0
C_ 131,130,131
C_ 130,128,129
Mavv 0,30,96
s_nop 0
Tv 38,v4,0
Tv 40,v4,6144
Mavv 20,34,96
E_ 132
Mavv 24,34,100
s_nop 0
Tv 42,v5,0
Tv 44,v5,6144
Mavv 4,30,100
E_ 133
Mavv 8,30,104
E_ 134
Mavv 28,34,104
E_ 135
Mavv 32,34,108
C_ 135,134,135
C_ 134,132,133
Mavv 12,30,108
.Lpad_a1_2:
s_cbranch_execz .Lhop_a1_r
E_ 76
Mavv 16,30,112
E_ 77
Mavv 36,34,112
W_ 0
E_ 78
Mavv 56,38,112
s_nop 0
Tv 30,v2,1024
Tv 32,v2,7168
Mavv 76,42,112
E_ 79
Mavv 72,42,108
s_nop 0
Tv 34,v3,1024
Tv 36,v3,7168
Mavv 52,38,108
C_ 116,76,77
C_ 117,78,79
Mavv 48,38,104
E_ 80
Mavv 68,42,104
E_ 81
Mavv 64,42,100
E_ 82
Mavv 44,38,100
E_ 83
Mavv 40,38,96
C_ 120,80,81
C_ 121,82,83
Mavv 60,42,96
W_ 0
E_ 84
Mavv 80,30,96
s_nop 0
Tv 38,v4,1024
Tv 40,v4,7168
Mavv 100,34,96
E_ 85
Mavv 104,34,100
s_nop 0
Tv 42,v5,1024
Tv 44,v5,7168
Mavv 84,30,100
E_ 86
Mavv 88,30,104
E_ 87
Mavv 108,34,104
C_ 124,84,85
C_ 125,86,87
Mavv 112,34,108
E_ 88
Mavv 92,30,108
E_ 89
Mavv 96,30,112
E_ 90
Mavv 116,34,112
W_ 0
E_ 91
Mavv 136,38,112
s_nop 0
Tv 30,v2,2048
Tv 32,v2,8192
Mavv 156,42,112
C_ 128,88,89
C_ 129,90,91
Mavv 152,42,108
s_nop 0
Tv 34,v3,2048
Tv 36,v3,8192
Mavv 132,38,108
E_ 92
Mavv 128,38,104
E_ 93
Mavv 148,42,104
E_ 94
Mavv 144,42,100
E_ 95
Mavv 124,38,100
C_ 132,92,93
C_ 133,94,95
Mavv 120,38,96
v_mov_b32 v92, 0x3f803f80
v_mov_b32 v93, 0x3f803f80
Mavv 140,42,96
W_ 0
v_mov_b32 v94, 0x3f803f80
v_mov_b32 v95, 0x3f803f80
Mavv 160,30,96
s_nop 0
Tv 38,v4,2048
Tv 40,v4,8192
Mavv 180,34,96
Mavv 184,34,100
s_nop 0
Tv 42,v5,2048
Tv 44,v5,8192
Mavv 164,30,100
Mavv 168,30,104
Mavv 188,34,104
S_ s65,s61,61440
Mavv 192,34,108
S_ m0,s51,0
Mavv 172,30,108
s_waitcnt vmcnt(6) lgkmcnt(0)
s_barrier
Mavv 176,30,112
Ln v7,32,s65,0
Mavv 196,34,112
Mavv 216,38,112
Dv 30,%0,18432
Mavv 236,42,112
Dv 34,v1,18432
Mavv 232,42,108
Ln v8,32,s65,3072
S_ m0,s51,896
Mavv 212,38,108
Mavv 208,38,104
Ln v7,32,s65,128
Mavv 228,42,104
Mavv 224,42,100
Ln v8,32,s65,3200
S_ m0,s51,1792
Mavv 204,38,100
Mavv 200,38,96
Ln v7,32,s65,256
Mavv 220,42,96
.Lstep1_4:
Zvvv 76,92,116
Dv 38,%0,19456
Zvvv 80,92,120
Dv 42,v1,19456
Zvvv 84,92,124
Zvvv 88,92,128
Zvvv 92,92,132
W_ 2
Nvvv 96,30,136,56
Mvvv 96,34,156
Nvvv 100,30,140,60
s_nop 1
v_max3_f32 v10, v76, v80, v84
s_nop 0
v_max3_f32 v10, v10, v88, v92
Mvvv 100,34,160
v_cmp_lt_f32 vcc, 0x42000000, v10
s_cbranch_vccnz .Ltramp1_3
Nvvv 104,30,144,64
A_ v46,v46,v76
A_ v47,v47,v80
Mvvv 104,34,164
A_ v48,v48,v84
A_ v49,v49,v88
Nvvv 108,30,148,68
A_ v50,v50,v92
Mvvv 108,34,168
Ln v8,32,s65,3328
Nvvv 112,30,152,72
Mvvv 112,34,172
W_ 0
Mvvv 112,38,192
Dv 30,%0,20480
Mvvv 112,42,212
Dv 34,v1,20480
Mvvv 108,38,188
Mvvv 108,42,208
Mvvv 104,38,184
Mvvv 104,42,204
Mvvv 100,38,180
Mvvv 100,42,200
Mvvv 96,38,176
Mvvv 96,42,196
W_ 0
Mvvv 96,30,216
Dv 38,%0,12288
Mvvv 96,34,236
Dv 42,v1,12288
Mvvv 100,30,220
Mvvv 100,34,240
Mvvv 104,30,224
Mvvv 104,34,244
Mvvv 108,30,228
Mvvv 108,34,248
s_nop 0
E_ 96
Mvvv 112,30,232
E_ 97
Mvvv 112,34,252
W_ 0
E_ 98
Nvvv 76,38,136,56
Dv 30,%0,13312
Mvvv 76,42,156
Dv 34,v1,13312
Nvvv 80,38,140,60
E_ 99
Mvvv 80,42,160
C_ 99,98,99
C_ 98,96,97
Nvvv 84,38,144,64
E_ 100
Mvvv 84,42,164
E_ 101
Nvvv 88,38,148,68
E_ 102
Mvvv 88,42,168
E_ 103
Nvvv 92,38,152,72
C_ 103,102,103
C_ 102,100,101
Mvvv 92,42,172
W_ 0
E_ 104
Mvvv 92,30,192
Dv 38,%0,14336
Mvvv 92,34,212
Dv 42,v1,14336
Mvvv 88,30,188
E_ 105
Mvvv 88,34,208
E_ 106
Mvvv 84,30,184
S_ s65,s61,49152
E_ 107
Mvvv 84,34,204
S_ m0,s51,12288
C_ 107,106,107
C_ 106,104,105
Mvvv 80,30,180
s_waitcnt vmcnt(6) lgkmcnt(0)
s_barrier
Mvvv 80,34,200
L_ v9,36,s65,0
Mvvv 76,30,176
E_ 108
Mvvv 76,34,196
L_ v9,36,s65,3072
S_ m0,s51,13184
Mvvv 76,38,216
Tv 30,v2,24576
Tv 32,v2,30720
Mvvv 76,42,236
L_ v9,36,s65,128
Mvvv 80,38,220
s_nop 0
Tv 34,v3,24576
Tv 36,v3,30720
Mvvv 80,42,240
L_ v9,36,s65,3200
S_ m0,s51,14080
Mvvv 84,38,224
E_ 109
Mvvv 84,42,244
L_ v9,36,s65,256
Mvvv 88,38,228
E_ 110
Mvvv 88,42,248
L_ v9,36,s65,3328
Mvvv 92,38,232
E_ 111
Mvvv 92,42,252
W_ 0
C_ 111,110,111
C_ 110,108,109
Mavv 0,30,116
s_nop 0
Tv 38,v4,24576
Tv 40,v4,30720
Mavv 20,34,116
E_ 112
Mavv 24,34,120
s_nop 0
Tv 42,v5,24576
Tv 44,v5,30720
Mavv 4,30,120
E_ 113
Mavv 8,30,124
E_ 114
Mavv 28,34,124
E_ 115
Mavv 32,34,128
C_ 115,114,115
C_ 114,112,113
Mavv 12,30,128
E_ 76
Mavv 16,30,132
E_ 77
Mavv 36,34,132
W_ 0
E_ 78
Mavv 56,38,132
s_nop 0
Tv 30,v2,25600
Tv 32,v2,31744
Mavv 76,42,132
E_ 79
Mavv 72,42,128
s_nop 0
Tv 34,v3,25600
Tv 36,v3,31744
Mavv 52,38,128
C_ 96,76,77
C_ 97,78,79
Mavv 48,38,124
E_ 80
Mavv 68,42,124
E_ 81
Mavv 64,42,120
E_ 82
Mavv 44,38,120
E_ 83
Mavv 40,38,116
C_ 100,80,81
C_ 101,82,83
Mavv 60,42,116
W_ 0
E_ 84
Mavv 80,30,116
s_nop 0
Tv 38,v4,25600
Tv 40,v4,31744
Mavv 100,34,116
E_ 85
Mavv 104,34,120
s_nop 0
Tv 42,v5,25600
Tv 44,v5,31744
Mavv 84,30,120
E_ 86
Mavv 88,30,124
E_ 87
Mavv 108,34,124
C_ 104,84,85
C_ 105,86,87
Mavv 112,34,128
E_ 88
Mavv 92,30,128
E_ 89
Mavv 96,30,132
E_ 90
Mavv 116,34,132
W_ 0
E_ 91
Mavv 136,38,132
s_nop 0
Tv 30,v2,26624
Tv 32,v2,32768
Mavv 156,42,132
C_ 108,88,89
C_ 109,90,91
Mavv 152,42,128
s_nop 0
Tv 34,v3,26624
Tv 36,v3,32768
Mavv 132,38,128
E_ 92
Mavv 128,38,124
E_ 93
Mavv 148,42,124
E_ 94
Mavv 144,42,120
E_ 95
Mavv 124,38,120
C_ 112,92,93
C_ 113,94,95
Mavv 120,38,116
v_mov_b32 v92, 0x3f803f80
v_mov_b32 v93, 0x3f803f80
Mavv 140,42,116
W_ 0
v_mov_b32 v94, 0x3f803f80
v_mov_b32 v95, 0x3f803f80
Mavv 160,30,116
s_nop 0
Tv 38,v4,26624
Tv 40,v4,32768
Mavv 180,34,116
Mavv 184,34,120
s_nop 0
Tv 42,v5,26624
Tv 44,v5,32768
Mavv 164,30,120
Mavv 168,30,124
Mavv 188,34,124
S_ s65,s61,73728
Mavv 192,34,128
S_ m0,s51,24576
Mavv 172,30,128
s_waitcnt vmcnt(6) lgkmcnt(0)
s_barrier
Mavv 176,30,132
Ln v7,32,s65,0
Mavv 196,34,132
Mavv 216,38,132
Dv 30,%0,6144
Mavv 236,42,132
Dv 34,v1,6144
Mavv 232,42,128
Ln v8,32,s65,3072
S_ m0,s51,25472
Mavv 212,38,128
Mavv 208,38,124
Ln v7,32,s65,128
Mavv 228,42,124
Mavv 224,42,120
Ln v8,32,s65,3200
S_ m0,s51,26368
Mavv 204,38,120
Mavv 200,38,116
Ln v7,32,s65,256
Mavv 220,42,116
s_cmp_eq_u32 s4, 0
s_cbranch_scc1 .Lhs_b1
.Lhs_b1_back:
.Lstep1_5:
Zvvv 76,92,96
Dv 38,%0,7168
Zvvv 80,92,100
Dv 42,v1,7168
Zvvv 84,92,104
Zvvv 88,92,108
Zvvv 92,92,112
W_ 2
Nvvv 116,30,136,56
Mvvv 116,34,156
Nvvv 120,30,140,60
s_nop 1
v_max3_f32 v10, v76, v80, v84
s_nop 0
v_max3_f32 v10, v10, v88, v92
Mvvv 120,34,160
v_cmp_lt_f32 vcc, 0x42000000, v10
s_cbranch_vccnz .Ltramp1_4
Nvvv 124,30,144,64
A_ v46,v46,v76
A_ v47,v47,v80
Mvvv 124,34,164
A_ v48,v48,v84
A_ v49,v49,v88
Nvvv 128,30,148,68
A_ v50,v50,v92
Mvvv 128,34,168
Ln v8,32,s65,3328
Nvvv 132,30,152,72
Mvvv 132,34,172
W_ 0
Mvvv 132,38,192
Dv 30,%0,8192
Mvvv 132,42,212
Dv 34,v1,8192
Mvvv 128,38,188
Mvvv 128,42,208
Mvvv 124,38,184
Mvvv 124,42,204
Mvvv 120,38,180
Mvvv 120,42,200
Mvvv 116,38,176
Mvvv 116,42,196
W_ 0
Mvvv 116,30,216
Dv 38,%0,0
Mvvv 116,34,236
Dv 42,v1,0
Mvvv 120,30,220
Mvvv 120,34,240
Mvvv 124,30,224
Mvvv 124,34,244
Mvvv 128,30,228
Mvvv 128,34,248
s_nop 0
E_ 116
Mvvv 132,30,232
E_ 117
Mvvv 132,34,252
W_ 0
E_ 118
Nvvv 76,38,136,56
Dv 30,%0,1024
Mvvv 76,42,156
Dv 34,v1,1024
Nvvv 80,38,140,60
E_ 119
Mvvv 80,42,160
C_ 119,118,119
C_ 118,116,117
Nvvv 84,38,144,64
E_ 120
Mvvv 84,42,164
E_ 121
Nvvv 88,38,148,68
E_ 122
Mvvv 88,42,168
E_ 123
Nvvv 92,38,152,72
C_ 123,122,123
C_ 122,120,121
Mvvv 92,42,172
W_ 0
E_ 124
Mvvv 92,30,192
Dv 38,%0,2048
Mvvv 92,34,212
Dv 42,v1,2048
Mvvv 88,30,188
E_ 125
Mvvv 88,34,208
E_ 126
Mvvv 84,30,184
S_ s65,s61,61440
E_ 127
Mvvv 84,34,204
S_ m0,s51,0
C_ 127,126,127
C_ 126,124,125
Mvvv 80,30,180
s_waitcnt vmcnt(6) lgkmcnt(0)
s_barrier
Mvvv 80,34,200
L_ v9,36,s65,0
Mvvv 76,30,176
E_ 128
Mvvv 76,34,196
L_ v9,36,s65,3072
S_ m0,s51,896
Mvvv 76,38,216
Tv 30,v2,12288
Tv 32,v2,18432
Mvvv 76,42,236
L_ v9,36,s65,128
Mvvv 80,38,220
s_nop 0
Tv 34,v3,12288
Tv 36,v3,18432
Mvvv 80,42,240
L_ v9,36,s65,3200
S_ m0,s51,1792
Mvvv 84,38,224
E_ 129
Mvvv 84,42,244
L_ v9,36,s65,256
Mvvv 88,38,228
E_ 130
Mvvv 88,42,248
L_ v9,36,s65,3328
Mvvv 92,38,232
E_ 131
Mvvv 92,42,252
W_ 0
C_ 131,130,131
C_ 130,128,129
Mavv 0,30,96
s_nop 0
Tv 38,v4,12288
Tv 40,v4,18432
Mavv 20,34,96
E_ 132
Mavv 24,34,100
s_nop 0
Tv 42,v5,12288
Tv 44,v5,18432
Mavv 4,30,100
E_ 133
Mavv 8,30,104
E_ 134
Mavv 28,34,104
E_ 135
Mavv 32,34,108
C_ 135,134,135
C_ 134,132,133
Mavv 12,30,108
E_ 76
Mavv 16,30,112
E_ 77
Mavv 36,34,112
W_ 0
E_ 78
Mavv 56,38,112
s_nop 0
Tv 30,v2,13312
Tv 32,v2,19456
Mavv 76,42,112
E_ 79
Mavv 72,42,108
s_nop 0
Tv 34,v3,13312
Tv 36,v3,19456
Mavv 52,38,108
C_ 116,76,77
C_ 117,78,79
Mavv 48,38,104
E_ 80
Mavv 68,42,104
E_ 81
Mavv 64,42,100
.Lpad_a1_3:
s_cbranch_execz .Lhop_a1_r
E_ 82
Mavv 44,38,100
E_ 83
Mavv 40,38,96
C_ 120,80,81
C_ 121,82,83
Mavv 60,42,96
W_ 0
E_ 84
Mavv 80,30,96
s_nop 0
Tv 38,v4,13312
Tv 40,v4,19456
Mavv 100,34,96
E_ 85
Mavv 104,34,100
s_nop 0
Tv 42,v5,13312
Tv 44,v5,19456
Mavv 84,30,100
E_ 86
Mavv 88,30,104
E_ 87
Mavv 108,34,104
C_ 124,84,85
C_ 125,86,87
Mavv 112,34,108
E_ 88
Mavv 92,30,108
E_ 89
Mavv 96,30,112
E_ 90
Mavv 116,34,112
W_ 0
E_ 91
Mavv 136,38,112
s_nop 0
Tv 30,v2,14336
Tv 32,v2,20480
Mavv 156,42,112
C_ 128,88,89
C_ 129,90,91
Mavv 152,42,108
s_nop 0
Tv 34,v3,14336
Tv 36,v3,20480
Mavv 132,38,108
E_ 92
Mavv 128,38,104
E_ 93
Mavv 148,42,104
E_ 94
Mavv 144,42,100
E_ 95
Mavv 124,38,100
C_ 132,92,93
C_ 133,94,95
Mavv 120,38,96
v_mov_b32 v92, 0x3f803f80
v_mov_b32 v93, 0x3f803f80
Mavv 140,42,96
W_ 0
v_mov_b32 v94, 0x3f803f80
v_mov_b32 v95, 0x3f803f80
Mavv 160,30,96
s_nop 0
Tv 38,v4,14336
Tv 40,v4,20480
Mavv 180,34,96
Mavv 184,34,100
s_nop 0
Tv 42,v5,14336
Tv 44,v5,20480
Mavv 164,30,100
Mavv 168,30,104
Mavv 188,34,104
S_ s65,s61,86016
Mavv 192,34,108
S_ m0,s51,12288
Mavv 172,30,108
s_waitcnt vmcnt(6) lgkmcnt(0)
s_barrier
Mavv 176,30,112
Ln v7,32,s65,0
Mavv 196,34,112
Mavv 216,38,112
Dv 30,%0,30720
Mavv 236,42,112
Dv 34,v1,30720
Mavv 232,42,108
Ln v8,32,s65,3072
S_ m0,s51,13184
Mavv 212,38,108
Mavv 208,38,104
Ln v7,32,s65,128
Mavv 228,42,104
Mavv 224,42,100
Ln v8,32,s65,3200
S_ m0,s51,14080
Mavv 204,38,100
Mavv 200,38,96
Ln v7,32,s65,256
Mavv 220,42,96
.Lstep1_6:
Zvvv 76,92,116
Dv 38,%0,31744
Zvvv 80,92,120
Dv 42,v1,31744
Zvvv 84,92,124
Zvvv 88,92,128
Zvvv 92,92,132
W_ 2
Nvvv 96,30,136,56
Mvvv 96,34,156
Nvvv 100,30,140,60
s_nop 1
v_max3_f32 v10, v76, v80, v84
s_nop 0
v_max3_f32 v10, v10, v88, v92
Mvvv 100,34,160
v_cmp_lt_f32 vcc, 0x42000000, v10
s_cbranch_vccnz .Ltramp1_5
Nvvv 104,30,144,64
A_ v46,v46,v76
A_ v47,v47,v80
Mvvv 104,34,164
A_ v48,v48,v84
A_ v49,v49,v88
Nvvv 108,30,148,68
A_ v50,v50,v92
Mvvv 108,34,168
Ln v8,32,s65,3328
Nvvv 112,30,152,72
R_ v11,a248
Mvvv 112,34,172
W_ 0
v_min_u32 v11, 240, v11
Mvvv 112,38,192
Dv 30,%0,32768
v_lshlrev_b32 v11, 3, v11
Mvvv 112,42,212
Dv 34,v1,32768
Mvvv 108,38,188
buffer_load_dword a254, v11, s[44:47], s66 offen
Mvvv 108,42,208
Mvvv 104,38,184
Mvvv 104,42,204
Mvvv 100,38,180
Mvvv 100,42,200
Mvvv 96,38,176
Mvvv 96,42,196
W_ 0
Mvvv 96,30,216
Dv 38,%0,24576
Mvvv 96,34,236
Dv 42,v1,24576
Mvvv 100,30,220
Mvvv 100,34,240
Mvvv 104,30,224
Mvvv 104,34,244
Mvvv 108,30,228
Mvvv 108,34,248
s_nop 0
E_ 96
Mvvv 112,30,232
E_ 97
Mvvv 112,34,252
W_ 0
E_ 98
Nvvv 76,38,136,56
Dv 30,%0,25600
Mvvv 76,42,156
Dv 34,v1,25600
Nvvv 80,38,140,60
E_ 99
Mvvv 80,42,160
C_ 99,98,99
C_ 98,96,97
Nvvv 84,38,144,64
E_ 100
Mvvv 84,42,164
E_ 101
Nvvv 88,38,148,68
E_ 102
Mvvv 88,42,168
E_ 103
Nvvv 92,38,152,72
C_ 103,102,103
C_ 102,100,101
Mvvv 92,42,172
W_ 0
E_ 104
Mvvv 92,30,192
Dv 38,%0,26624
Mvvv 92,34,212
Dv 42,v1,26624
Mvvv 88,30,188
E_ 105
Mvvv 88,34,208
E_ 106
Mvvv 84,30,184
S_ s65,s61,73728
E_ 107
Mvvv 84,34,204
S_ m0,s51,24576
C_ 107,106,107
C_ 106,104,105
Mvvv 80,30,180
s_waitcnt vmcnt(7) lgkmcnt(0)
s_barrier
Mvvv 80,34,200
L_ v9,36,s65,0
Mvvv 76,30,176
E_ 108
Mvvv 76,34,196
L_ v9,36,s65,3072
S_ m0,s51,25472
Mvvv 76,38,216
Tv 30,v2,0
Tv 32,v2,6144
Mvvv 76,42,236
L_ v9,36,s65,128
Mvvv 80,38,220
s_nop 0
Tv 34,v3,0
Tv 36,v3,6144
Mvvv 80,42,240
L_ v9,36,s65,3200
S_ m0,s51,26368
Mvvv 84,38,224
E_ 109
Mvvv 84,42,244
L_ v9,36,s65,256
Mvvv 88,38,228
E_ 110
Mvvv 88,42,248
L_ v9,36,s65,3328
Mvvv 92,38,232
E_ 111
Mvvv 92,42,252
W_ 0
C_ 111,110,111
C_ 110,108,109
Mavv 0,30,116
s_nop 0
Tv 38,v4,0
Tv 40,v4,6144
Mavv 20,34,116
E_ 112
Mavv 24,34,120
s_nop 0
Tv 42,v5,0
Tv 44,v5,6144
Mavv 4,30,120
E_ 113
Mavv 8,30,124
E_ 114
Mavv 28,34,124
E_ 115
Mavv 32,34,128
C_ 115,114,115
C_ 114,112,113
Mavv 12,30,128
E_ 76
Mavv 16,30,132
E_ 77
Mavv 36,34,132
W_ 0
E_ 78
Mavv 56,38,132
s_nop 0
Tv 30,v2,1024
Tv 32,v2,7168
Mavv 76,42,132
E_ 79
Mavv 72,42,128
s_nop 0
Tv 34,v3,1024
Tv 36,v3,7168
Mavv 52,38,128
C_ 96,76,77
C_ 97,78,79
Mavv 48,38,124
E_ 80
Mavv 68,42,124
E_ 81
Mavv 64,42,120
E_ 82
Mavv 44,38,120
E_ 83
Mavv 40,38,116
C_ 100,80,81
C_ 101,82,83
Mavv 60,42,116
W_ 0
E_ 84
Mavv 80,30,116
s_nop 0
Tv 38,v4,1024
Tv 40,v4,7168
Mavv 100,34,116
E_ 85
Mavv 104,34,120
s_nop 0
Tv 42,v5,1024
Tv 44,v5,7168
Mavv 84,30,120
E_ 86
Mavv 88,30,124
E_ 87
Mavv 108,34,124
C_ 104,84,85
C_ 105,86,87
Mavv 112,34,128
E_ 88
Mavv 92,30,128
E_ 89
Mavv 96,30,132
E_ 90
Mavv 116,34,132
W_ 0
E_ 91
Mavv 136,38,132
s_nop 0
Tv 30,v2,2048
Tv 32,v2,8192
Mavv 156,42,132
C_ 108,88,89
C_ 109,90,91
Mavv 152,42,128
s_nop 0
Tv 34,v3,2048
Tv 36,v3,8192
Mavv 132,38,128
E_ 92
Mavv 128,38,124
E_ 93
Mavv 148,42,124
E_ 94
Mavv 144,42,120
E_ 95
Mavv 124,38,120
C_ 112,92,93
C_ 113,94,95
Mavv 120,38,116
v_mov_b32 v92, 0x3f803f80
v_mov_b32 v93, 0x3f803f80
Mavv 140,42,116
W_ 0
v_mov_b32 v94, 0x3f803f80
v_mov_b32 v95, 0x3f803f80
Mavv 160,30,116
s_nop 0
Tv 38,v4,2048
Tv 40,v4,8192
Mavv 180,34,116
Mavv 184,34,120
s_nop 0
Tv 42,v5,2048
Tv 44,v5,8192
Mavv 164,30,120
Mavv 168,30,124
Mavv 188,34,124
S_ s65,s61,98304
Mavv 192,34,128
S_ m0,s51,0
Mavv 172,30,128
s_waitcnt vmcnt(7) lgkmcnt(0)
s_barrier
Mavv 176,30,132
Ln v7,32,s65,0
Mavv 196,34,132
Mavv 216,38,132
Dv 30,%0,18432
Mavv 236,42,132
Dv 34,v1,18432
Mavv 232,42,128
Ln v8,32,s65,3072
S_ m0,s51,896
Mavv 212,38,128
Mavv 208,38,124
Ln v7,32,s65,128
Mavv 228,42,124
Mavv 224,42,120
Ln v8,32,s65,3200
S_ m0,s51,1792
Mavv 204,38,120
Mavv 200,38,116
Ln v7,32,s65,256
Mavv 220,42,116
s_cmp_eq_u32 s4, 0
s_cbranch_scc1 .Lhs_c1
.Lhs_c1_back:
.Lstep1_7:
Zvvv 76,92,96
Dv 38,%0,19456
Zvvv 80,92,100
Dv 42,v1,19456
Zvvv 84,92,104
Zvvv 88,92,108
Zvvv 92,92,112
W_ 2
Nvvv 116,30,136,56
Mvvv 116,34,156
Nvvv 120,30,140,60
s_nop 1
v_max3_f32 v10, v76, v80, v84
s_nop 0
v_max3_f32 v10, v10, v88, v92
Mvvv 120,34,160
v_cmp_lt_f32 vcc, 0x42000000, v10
s_cbranch_vccnz .Ltramp1_6
Nvvv 124,30,144,64
A_ v46,v46,v76
A_ v47,v47,v80
Mvvv 124,34,164
A_ v48,v48,v84
A_ v49,v49,v88
Nvvv 128,30,148,68
A_ v50,v50,v92
Mvvv 128,34,168
Ln v8,32,s65,3328
Nvvv 132,30,152,72
Mvvv 132,34,172
W_ 0
Mvvv 132,38,192
Dv 30,%0,20480
Mvvv 132,42,212
Dv 34,v1,20480
Mvvv 128,38,188
Mvvv 128,42,208
Mvvv 124,38,184
Mvvv 124,42,204
Mvvv 120,38,180
Mvvv 120,42,200
Mvvv 116,38,176
Mvvv 116,42,196
W_ 0
Mvvv 116,30,216
Dv 38,%0,12288
Mvvv 116,34,236
Dv 42,v1,12288
Mvvv 120,30,220
Mvvv 120,34,240
Mvvv 124,30,224
Mvvv 124,34,244
Mvvv 128,30,228
Mvvv 128,34,248
s_nop 0
E_ 116
Mvvv 132,30,232
E_ 117
Mvvv 132,34,252
W_ 0
E_ 118
Nvvv 76,38,136,56
Dv 30,%0,13312
Mvvv 76,42,156
Dv 34,v1,13312
Nvvv 80,38,140,60
E_ 119
Mvvv 80,42,160
C_ 119,118,119
C_ 118,116,117
Nvvv 84,38,144,64
E_ 120
Mvvv 84,42,164
E_ 121
Nvvv 88,38,148,68
E_ 122
Mvvv 88,42,168
E_ 123
Nvvv 92,38,152,72
C_ 123,122,123
C_ 122,120,121
Mvvv 92,42,172
W_ 0
E_ 124
Mvvv 92,30,192
Dv 38,%0,14336
Mvvv 92,34,212
Dv 42,v1,14336
Mvvv 88,30,188
E_ 125
Mvvv 88,34,208
E_ 126
Mvvv 84,30,184
S_ s65,s61,86016
E_ 127
Mvvv 84,34,204
S_ m0,s51,12288
C_ 127,126,127
C_ 126,124,125
Mvvv 80,30,180
s_waitcnt vmcnt(6) lgkmcnt(0)
s_barrier
Mvvv 80,34,200
L_ v9,36,s65,0
Mvvv 76,30,176
E_ 128
Mvvv 76,34,196
L_ v9,36,s65,3072
S_ m0,s51,13184
Mvvv 76,38,216
Tv 30,v2,24576
Tv 32,v2,30720
Mvvv 76,42,236
L_ v9,36,s65,128
Mvvv 80,38,220
s_nop 0
Tv 34,v3,24576
Tv 36,v3,30720
Mvvv 80,42,240
L_ v9,36,s65,3200
S_ m0,s51,14080
Mvvv 84,38,224
E_ 129
Mvvv 84,42,244
L_ v9,36,s65,256
Mvvv 88,38,228
E_ 130
Mvvv 88,42,248
L_ v9,36,s65,3328
Mvvv 92,38,232
E_ 131
Mvvv 92,42,252
W_ 0
C_ 131,130,131
C_ 130,128,129
Mavv 0,30,96
s_nop 0
Tv 38,v4,24576
Tv 40,v4,30720
Mavv 20,34,96
E_ 132
Mavv 24,34,100
s_nop 0
Tv 42,v5,24576
Tv 44,v5,30720
Mavv 4,30,100
E_ 133
Mavv 8,30,104
E_ 134
Mavv 28,34,104
E_ 135
Mavv 32,34,108
C_ 135,134,135
C_ 134,132,133
Mavv 12,30,108
E_ 76
Mavv 16,30,112
E_ 77
Mavv 36,34,112
W_ 0
E_ 78
Mavv 56,38,112
s_nop 0
Tv 30,v2,25600
Tv 32,v2,31744
Mavv 76,42,112
E_ 79
Mavv 72,42,108
s_nop 0
Tv 34,v3,25600
Tv 36,v3,31744
Mavv 52,38,108
C_ 116,76,77
C_ 117,78,79
Mavv 48,38,104
E_ 80
Mavv 68,42,104
E_ 81
Mavv 64,42,100
E_ 82
Mavv 44,38,100
E_ 83
Mavv 40,38,96
C_ 120,80,81
C_ 121,82,83
Mavv 60,42,96
W_ 0
E_ 84
Mavv 80,30,96
s_nop 0
Tv 38,v4,25600
Tv 40,v4,31744
Mavv 100,34,96
E_ 85
Mavv 104,34,100
s_nop 0
Tv 42,v5,25600
Tv 44,v5,31744
Mavv 84,30,100
E_ 86
Mavv 88,30,104
E_ 87
Mavv 108,34,104
C_ 124,84,85
C_ 125,86,87
Mavv 112,34,108
E_ 88
Mavv 92,30,108
E_ 89
Mavv 96,30,112
E_ 90
Mavv 116,34,112
W_ 0
E_ 91
Mavv 136,38,112
s_nop 0
Tv 30,v2,26624
Tv 32,v2,32768
Mavv 156,42,112
C_ 128,88,89
C_ 129,90,91
Mavv 152,42,108
s_nop 0
Tv 34,v3,26624
Tv 36,v3,32768
Mavv 132,38,108
E_ 92
Mavv 128,38,104
E_ 93
Mavv 148,42,104
E_ 94
Mavv 144,42,100
E_ 95
Mavv 124,38,100
C_ 132,92,93
C_ 133,94,95
Mavv 120,38,96
v_mov_b32 v92, 0x3f803f80
v_mov_b32 v93, 0x3f803f80
Mavv 140,42,96
W_ 0
v_mov_b32 v94, 0x3f803f80
v_mov_b32 v95, 0x3f803f80
Mavv 160,30,96
s_nop 0
Tv 38,v4,26624
Tv 40,v4,32768
Mavv 180,34,96
Mavv 184,34,100
s_nop 0
Tv 42,v5,26624
Tv 44,v5,32768
Mavv 164,30,100
Mavv 168,30,104
Mavv 188,34,104
S_ s65,s61,110592
Mavv 192,34,108
S_ m0,s51,24576
Mavv 172,30,108
s_waitcnt vmcnt(6) lgkmcnt(0)
s_barrier
Mavv 176,30,112
Ln v7,32,s65,0
Mavv 196,34,112
Mavv 216,38,112
Dv 30,%0,6144
Mavv 236,42,112
Dv 34,v1,6144
Mavv 232,42,108
Ln v8,32,s65,3072
S_ m0,s51,25472
Mavv 212,38,108
Mavv 208,38,104
Ln v7,32,s65,128
Mavv 228,42,104
Mavv 224,42,100
Ln v8,32,s65,3200
S_ m0,s51,26368
Mavv 204,38,100
Mavv 200,38,96
Ln v7,32,s65,256
Mavv 220,42,96
.Lstep1_8:
Zvvv 76,92,116
Dv 38,%0,7168
Zvvv 80,92,120
Dv 42,v1,7168
Zvvv 84,92,124
Zvvv 88,92,128
Zvvv 92,92,132
W_ 2
Nvvv 96,30,136,56
Mvvv 96,34,156
Nvvv 100,30,140,60
s_nop 1
v_max3_f32 v10, v76, v80, v84
s_nop 0
v_max3_f32 v10, v10, v88, v92
Mvvv 100,34,160
v_cmp_lt_f32 vcc, 0x42000000, v10
s_cbranch_vccnz .Ltramp1_7
Nvvv 104,30,144,64
A_ v46,v46,v76
A_ v47,v47,v80
Mvvv 104,34,164
A_ v48,v48,v84
A_ v49,v49,v88
Nvvv 108,30,148,68
A_ v50,v50,v92
Mvvv 108,34,168
Ln v8,32,s65,3328
Nvvv 112,30,152,72
Mvvv 112,34,172
W_ 0
Mvvv 112,38,192
Dv 30,%0,8192
Mvvv 112,42,212
Dv 34,v1,8192
Mvvv 108,38,188
Mvvv 108,42,208
Mvvv 104,38,184
Mvvv 104,42,204
Mvvv 100,38,180
Mvvv 100,42,200
Mvvv 96,38,176
Mvvv 96,42,196
W_ 0
Mvvv 96,30,216
Dv 38,%0,0
Mvvv 96,34,236
Dv 42,v1,0
Mvvv 100,30,220
Mvvv 100,34,240
Mvvv 104,30,224
Mvvv 104,34,244
Mvvv 108,30,228
Mvvv 108,34,248
s_nop 0
E_ 96
Mvvv 112,30,232
E_ 97
Mvvv 112,34,252
W_ 0
E_ 98
Nvvv 76,38,136,56
Dv 30,%0,1024
Mvvv 76,42,156
Dv 34,v1,1024
Nvvv 80,38,140,60
E_ 99
Mvvv 80,42,160
C_ 99,98,99
C_ 98,96,97
Nvvv 84,38,144,64
E_ 100
Mvvv 84,42,164
E_ 101
Nvvv 88,38,148,68
E_ 102
Mvvv 88,42,168
)A"
R"A(E_ 103
Nvvv 92,38,152,72
C_ 103,102,103
C_ 102,100,101
Mvvv 92,42,172
W_ 0
E_ 104
Mvvv 92,30,192
Dv 38,%0,2048
Mvvv 92,34,212
Dv 42,v1,2048
Mvvv 88,30,188
E_ 105
Mvvv 88,34,208
E_ 106
Mvvv 84,30,184
S_ s65,s61,98304
E_ 107
Mvvv 84,34,204
S_ m0,s51,0
C_ 107,106,107
C_ 106,104,105
Mvvv 80,30,180
s_waitcnt vmcnt(6) lgkmcnt(0)
s_barrier
Mvvv 80,34,200
L_ v9,36,s65,0
Mvvv 76,30,176
E_ 108
Mvvv 76,34,196
L_ v9,36,s65,3072
S_ m0,s51,896
Mvvv 76,38,216
Tv 30,v2,12288
Tv 32,v2,18432
Mvvv 76,42,236
L_ v9,36,s65,128
Mvvv 80,38,220
s_nop 0
Tv 34,v3,12288
Tv 36,v3,18432
Mvvv 80,42,240
L_ v9,36,s65,3200
S_ m0,s51,1792
Mvvv 84,38,224
E_ 109
Mvvv 84,42,244
L_ v9,36,s65,256
Mvvv 88,38,228
E_ 110
Mvvv 88,42,248
L_ v9,36,s65,3328
Mvvv 92,38,232
E_ 111
Mvvv 92,42,252
W_ 0
C_ 111,110,111
C_ 110,108,109
Mavv 0,30,116
s_nop 0
Tv 38,v4,12288
Tv 40,v4,18432
Mavv 20,34,116
E_ 112
Mavv 24,34,120
s_nop 0
Tv 42,v5,12288
Tv 44,v5,18432
Mavv 4,30,120
E_ 113
Mavv 8,30,124
E_ 114
Mavv 28,34,124
E_ 115
Mavv 32,34,128
C_ 115,114,115
C_ 114,112,113
Mavv 12,30,128
E_ 76
Mavv 16,30,132
E_ 77
Mavv 36,34,132
W_ 0
E_ 78
Mavv 56,38,132
s_nop 0
Tv 30,v2,13312
Tv 32,v2,19456
Mavv 76,42,132
E_ 79
Mavv 72,42,128
s_nop 0
Tv 34,v3,13312
Tv 36,v3,19456
Mavv 52,38,128
C_ 96,76,77
C_ 97,78,79
Mavv 48,38,124
E_ 80
Mavv 68,42,124
E_ 81
Mavv 64,42,120
E_ 82
Mavv 44,38,120
E_ 83
Mavv 40,38,116
C_ 100,80,81
C_ 101,82,83
Mavv 60,42,116
W_ 0
E_ 84
Mavv 80,30,116
s_nop 0
Tv 38,v4,13312
Tv 40,v4,19456
Mavv 100,34,116
E_ 85
Mavv 104,34,120
s_nop 0
Tv 42,v5,13312
Tv 44,v5,19456
Mavv 84,30,120
E_ 86
Mavv 88,30,124
E_ 87
Mavv 108,34,124
C_ 104,84,85
C_ 105,86,87
Mavv 112,34,128
E_ 88
Mavv 92,30,128
E_ 89
Mavv 96,30,132
E_ 90
Mavv 116,34,132
W_ 0
E_ 91
Mavv 136,38,132
s_nop 0
Tv 30,v2,14336
Tv 32,v2,20480
Mavv 156,42,132
C_ 108,88,89
C_ 109,90,91
Mavv 152,42,128
s_nop 0
Tv 34,v3,14336
Tv 36,v3,20480
Mavv 132,38,128
E_ 92
Mavv 128,38,124
E_ 93
Mavv 148,42,124
E_ 94
Mavv 144,42,120
E_ 95
Mavv 124,38,120
C_ 112,92,93
C_ 113,94,95
Mavv 120,38,116
v_mov_b32 v92, 0x3f803f80
v_mov_b32 v93, 0x3f803f80
Mavv 140,42,116
W_ 0
v_mov_b32 v94, 0x3f803f80
v_mov_b32 v95, 0x3f803f80
Mavv 160,30,116
s_nop 0
Tv 38,v4,14336
Tv 40,v4,20480
Mavv 180,34,116
Mavv 184,34,120
s_nop 0
Tv 42,v5,14336
Tv 44,v5,20480
Mavv 164,30,120
Mavv 168,30,124
Mavv 188,34,124
S_ s65,s61,122880
Mavv 192,34,128
S_ m0,s51,12288
Mavv 172,30,128
s_waitcnt vmcnt(6) lgkmcnt(0)
s_barrier
Mavv 176,30,132
Ln v7,32,s65,0
Mavv 196,34,132
Mavv 216,38,132
Dv 30,%0,30720
Mavv 236,42,132
Dv 34,v1,30720
Mavv 232,42,128
Ln v8,32,s65,3072
S_ m0,s51,13184
Mavv 212,38,128
Mavv 208,38,124
Ln v7,32,s65,128
Mavv 228,42,124
Mavv 224,42,120
Ln v8,32,s65,3200
S_ m0,s51,14080
Mavv 204,38,120
Mavv 200,38,116
Ln v7,32,s65,256
Mavv 220,42,116
s_cmp_eq_u32 s4, 0
s_cbranch_scc1 .Lhs_d1
.Lhs_d1_back:
.Lstep1_9:
Zvvv 76,92,96
Dv 38,%0,31744
Zvvv 80,92,100
Dv 42,v1,31744
Zvvv 84,92,104
Zvvv 88,92,108
Zvvv 92,92,112
W_ 2
Nvvv 116,30,136,56
Mvvv 116,34,156
Nvvv 120,30,140,60
s_nop 1
v_max3_f32 v10, v76, v80, v84
s_nop 0
v_max3_f32 v10, v10, v88, v92
Mvvv 120,34,160
v_cmp_lt_f32 vcc, 0x42000000, v10
s_cbranch_vccnz .Ltramp1_8
Nvvv 124,30,144,64
A_ v46,v46,v76
A_ v47,v47,v80
Mvvv 124,34,164
A_ v48,v48,v84
A_ v49,v49,v88
Nvvv 128,30,148,68
A_ v50,v50,v92
Mvvv 128,34,168
Ln v8,32,s65,3328
Nvvv 132,30,152,72
Mvvv 132,34,172
W_ 0
Mvvv 132,38,192
Dv 30,%0,32768
Mvvv 132,42,212
Dv 34,v1,32768
Mvvv 128,38,188
Mvvv 128,42,208
Mvvv 124,38,184
Mvvv 124,42,204
Mvvv 120,38,180
Mvvv 120,42,200
Mvvv 116,38,176
Mvvv 116,42,196
W_ 0
Mvvv 116,30,216
Dv 38,%0,24576
Mvvv 116,34,236
Dv 42,v1,24576
Mvvv 120,30,220
Mvvv 120,34,240
Mvvv 124,30,224
Mvvv 124,34,244
Mvvv 128,30,228
Mvvv 128,34,248
s_nop 0
E_ 116
Mvvv 132,30,232
E_ 117
Mvvv 132,34,252
W_ 0
E_ 118
Nvvv 76,38,136,56
Dv 30,%0,25600
Mvvv 76,42,156
Dv 34,v1,25600
Nvvv 80,38,140,60
E_ 119
Mvvv 80,42,160
C_ 119,118,119
C_ 118,116,117
Nvvv 84,38,144,64
E_ 120
Mvvv 84,42,164
E_ 121
Nvvv 88,38,148,68
E_ 122
Mvvv 88,42,168
E_ 123
Nvvv 92,38,152,72
C_ 123,122,123
C_ 122,120,121
Mvvv 92,42,172
W_ 0
E_ 124
Mvvv 92,30,192
Dv 38,%0,26624
Mvvv 92,34,212
Dv 42,v1,26624
Mvvv 88,30,188
E_ 125
Mvvv 88,34,208
E_ 126
Mvvv 84,30,184
S_ s65,s61,110592
E_ 127
Mvvv 84,34,204
S_ m0,s51,24576
C_ 127,126,127
C_ 126,124,125
Mvvv 80,30,180
s_waitcnt vmcnt(6) lgkmcnt(0)
s_barrier
Mvvv 80,34,200
L_ v9,36,s65,0
Mvvv 76,30,176
E_ 128
Mvvv 76,34,196
L_ v9,36,s65,3072
S_ m0,s51,25472
Mvvv 76,38,216
Tv 30,v2,0
Tv 32,v2,6144
Mvvv 76,42,236
L_ v9,36,s65,128
Mvvv 80,38,220
s_nop 0
Tv 34,v3,0
Tv 36,v3,6144
Mvvv 80,42,240
L_ v9,36,s65,3200
S_ m0,s51,26368
Mvvv 84,38,224
E_ 129
Mvvv 84,42,244
L_ v9,36,s65,256
Mvvv 88,38,228
E_ 130
Mvvv 88,42,248
L_ v9,36,s65,3328
Mvvv 92,38,232
E_ 131
Mvvv 92,42,252
W_ 0
C_ 131,130,131
C_ 130,128,129
Mavv 0,30,96
s_nop 0
Tv 38,v4,0
Tv 40,v4,6144
Mavv 20,34,96
E_ 132
Mavv 24,34,100
s_nop 0
Tv 42,v5,0
Tv 44,v5,6144
Mavv 4,30,100
E_ 133
Mavv 8,30,104
E_ 134
Mavv 28,34,104
E_ 135
Mavv 32,34,108
C_ 135,134,135
C_ 134,132,133
Mavv 12,30,108
E_ 76
Mavv 16,30,112
E_ 77
Mavv 36,34,112
W_ 0
E_ 78
Mavv 56,38,112
s_nop 0
Tv 30,v2,1024
Tv 32,v2,7168
Mavv 76,42,112
E_ 79
Mavv 72,42,108
s_nop 0
Tv 34,v3,1024
Tv 36,v3,7168
Mavv 52,38,108
C_ 116,76,77
C_ 117,78,79
Mavv 48,38,104
E_ 80
Mavv 68,42,104
E_ 81
Mavv 64,42,100
E_ 82
Mavv 44,38,100
E_ 83
Mavv 40,38,96
C_ 120,80,81
C_ 121,82,83
Mavv 60,42,96
W_ 0
E_ 84
Mavv 80,30,96
s_nop 0
Tv 38,v4,1024
Tv 40,v4,7168
Mavv 100,34,96
E_ 85
Mavv 104,34,100
s_nop 0
Tv 42,v5,1024
Tv 44,v5,7168
Mavv 84,30,100
E_ 86
Mavv 88,30,104
E_ 87
Mavv 108,34,104
C_ 124,84,85
C_ 125,86,87
Mavv 112,34,108
E_ 88
Mavv 92,30,108
E_ 89
Mavv 96,30,112
E_ 90
Mavv 116,34,112
W_ 0
E_ 91
Mavv 136,38,112
s_nop 0
Tv 30,v2,2048
Tv 32,v2,8192
Mavv 156,42,112
C_ 128,88,89
C_ 129,90,91
Mavv 152,42,108
s_nop 0
Tv 34,v3,2048
Tv 36,v3,8192
Mavv 132,38,108
E_ 92
Mavv 128,38,104
E_ 93
Mavv 148,42,104
E_ 94
Mavv 144,42,100
E_ 95
Mavv 124,38,100
C_ 132,92,93
C_ 133,94,95
Mavv 120,38,96
v_mov_b32 v92, 0x3f803f80
v_mov_b32 v93, 0x3f803f80
Mavv 140,42,96
W_ 0
v_mov_b32 v94, 0x3f803f80
v_mov_b32 v95, 0x3f803f80
Mavv 160,30,96
s_nop 0
Tv 38,v4,2048
Tv 40,v4,8192
Mavv 180,34,96
Mavv 184,34,100
s_nop 0
Tv 42,v5,2048
Tv 44,v5,8192
Mavv 164,30,100
Mavv 168,30,104
Mavv 188,34,104
S_ s65,s61,135168
Mavv 192,34,108
S_ m0,s51,0
Mavv 172,30,108
s_waitcnt vmcnt(6) lgkmcnt(0)
s_barrier
Mavv 176,30,112
Ln v7,32,s65,0
Mavv 196,34,112
Mavv 216,38,112
Dv 30,%0,18432
Mavv 236,42,112
Dv 34,v1,18432
Mavv 232,42,108
Ln v8,32,s65,3072
S_ m0,s51,896
Mavv 212,38,108
Mavv 208,38,104
Ln v7,32,s65,128
Mavv 228,42,104
Mavv 224,42,100
Ln v8,32,s65,3200
S_ m0,s51,1792
Mavv 204,38,100
Mavv 200,38,96
Ln v7,32,s65,256
Mavv 220,42,96
.Lstep1_10:
Zvvv 76,92,116
Dv 38,%0,19456
Zvvv 80,92,120
Dv 42,v1,19456
Zvvv 84,92,124
Zvvv 88,92,128
Zvvv 92,92,132
W_ 2
Nvvv 96,30,136,56
Mvvv 96,34,156
Nvvv 100,30,140,60
s_nop 1
v_max3_f32 v10, v76, v80, v84
s_nop 0
v_max3_f32 v10, v10, v88, v92
Mvvv 100,34,160
v_cmp_lt_f32 vcc, 0x42000000, v10
s_cbranch_vccnz .Ltramp1_9
Nvvv 104,30,144,64
A_ v46,v46,v76
A_ v47,v47,v80
Mvvv 104,34,164
A_ v48,v48,v84
A_ v49,v49,v88
Nvvv 108,30,148,68
A_ v50,v50,v92
Mvvv 108,34,168
Ln v8,32,s65,3328
Nvvv 112,30,152,72
Mvvv 112,34,172
W_ 0
Mvvv 112,38,192
Dv 30,%0,20480
Mvvv 112,42,212
Dv 34,v1,20480
Mvvv 108,38,188
Mvvv 108,42,208
Mvvv 104,38,184
Mvvv 104,42,204
Mvvv 100,38,180
Mvvv 100,42,200
Mvvv 96,38,176
Mvvv 96,42,196
W_ 0
Mvvv 96,30,216
Dv 38,%0,12288
Mvvv 96,34,236
Dv 42,v1,12288
Mvvv 100,30,220
Mvvv 100,34,240
Mvvv 104,30,224
Mvvv 104,34,244
Mvvv 108,30,228
Mvvv 108,34,248
s_nop 0
E_ 96
Mvvv 112,30,232
E_ 97
Mvvv 112,34,252
W_ 0
E_ 98
Nvvv 76,38,136,56
Dv 30,%0,13312
Mvvv 76,42,156
Dv 34,v1,13312
Nvvv 80,38,140,60
E_ 99
Mvvv 80,42,160
C_ 99,98,99
C_ 98,96,97
Nvvv 84,38,144,64
E_ 100
Mvvv 84,42,164
E_ 101
Nvvv 88,38,148,68
E_ 102
Mvvv 88,42,168
E_ 103
Nvvv 92,38,152,72
C_ 103,102,103
C_ 102,100,101
Mvvv 92,42,172
W_ 0
E_ 104
Mvvv 92,30,192
Dv 38,%0,14336
Mvvv 92,34,212
Dv 42,v1,14336
Mvvv 88,30,188
E_ 105
Mvvv 88,34,208
E_ 106
Mvvv 84,30,184
S_ s65,s61,122880
E_ 107
Mvvv 84,34,204
S_ m0,s51,12288
C_ 107,106,107
C_ 106,104,105
Mvvv 80,30,180
s_waitcnt vmcnt(6) lgkmcnt(0)
s_barrier
Mvvv 80,34,200
L_ v9,36,s65,0
Mvvv 76,30,176
E_ 108
Mvvv 76,34,196
L_ v9,36,s65,3072
S_ m0,s51,13184
Mvvv 76,38,216
Tv 30,v2,24576
Tv 32,v2,30720
Mvvv 76,42,236
L_ v9,36,s65,128
Mvvv 80,38,220
s_nop 0
Tv 34,v3,24576
Tv 36,v3,30720
Mvvv 80,42,240
L_ v9,36,s65,3200
S_ m0,s51,14080
Mvvv 84,38,224
E_ 109
Mvvv 84,42,244
L_ v9,36,s65,256
Mvvv 88,38,228
E_ 110
Mvvv 88,42,248
L_ v9,36,s65,3328
Mvvv 92,38,232
E_ 111
Mvvv 92,42,252
W_ 0
C_ 111,110,111
C_ 110,108,109
Mavv 0,30,116
s_nop 0
Tv 38,v4,24576
Tv 40,v4,30720
Mavv 20,34,116
E_ 112
Mavv 24,34,120
s_nop 0
Tv 42,v5,24576
Tv 44,v5,30720
Mavv 4,30,120
E_ 113
Mavv 8,30,124
E_ 114
Mavv 28,34,124
E_ 115
Mavv 32,34,128
C_ 115,114,115
C_ 114,112,113
Mavv 12,30,128
E_ 76
Mavv 16,30,132
E_ 77
Mavv 36,34,132
W_ 0
E_ 78
Mavv 56,38,132
s_nop 0
Tv 30,v2,25600
Tv 32,v2,31744
Mavv 76,42,132
E_ 79
Mavv 72,42,128
s_nop 0
Tv 34,v3,25600
Tv 36,v3,31744
Mavv 52,38,128
C_ 96,76,77
C_ 97,78,79
Mavv 48,38,124
E_ 80
Mavv 68,42,124
E_ 81
Mavv 64,42,120
E_ 82
Mavv 44,38,120
E_ 83
Mavv 40,38,116
C_ 100,80,81
C_ 101,82,83
Mavv 60,42,116
W_ 0
E_ 84
Mavv 80,30,116
s_nop 0
Tv 38,v4,25600
Tv 40,v4,31744
Mavv 100,34,116
E_ 85
Mavv 104,34,120
s_nop 0
Tv 42,v5,25600
Tv 44,v5,31744
Mavv 84,30,120
E_ 86
Mavv 88,30,124
E_ 87
Mavv 108,34,124
C_ 104,84,85
C_ 105,86,87
Mavv 112,34,128
E_ 88
Mavv 92,30,128
E_ 89
Mavv 96,30,132
E_ 90
Mavv 116,34,132
W_ 0
E_ 91
Mavv 136,38,132
s_nop 0
Tv 30,v2,26624
Tv 32,v2,32768
Mavv 156,42,132
C_ 108,88,89
C_ 109,90,91
Mavv 152,42,128
s_nop 0
Tv 34,v3,26624
Tv 36,v3,32768
Mavv 132,38,128
E_ 92
Mavv 128,38,124
E_ 93
Mavv 148,42,124
E_ 94
Mavv 144,42,120
E_ 95
Mavv 124,38,120
C_ 112,92,93
C_ 113,94,95
Mavv 120,38,116
v_mov_b32 v92, 0x3f803f80
v_mov_b32 v93, 0x3f803f80
Mavv 140,42,116
W_ 0
v_mov_b32 v94, 0x3f803f80
v_mov_b32 v95, 0x3f803f80
Mavv 160,30,116
s_nop 0
Tv 38,v4,26624
Tv 40,v4,32768
Mavv 180,34,116
Mavv 184,34,120
s_nop 0
Tv 42,v5,26624
Tv 44,v5,32768
Mavv 164,30,120
Mavv 168,30,124
Mavv 188,34,124
S_ s65,s61,147456
Mavv 192,34,128
S_ m0,s51,24576
Mavv 172,30,128
s_waitcnt vmcnt(6) lgkmcnt(0)
s_barrier
Mavv 176,30,132
Ln v7,32,s65,0
Mavv 196,34,132
Mavv 216,38,132
Dv 30,%0,6144
Mavv 236,42,132
Dv 34,v1,6144
Mavv 232,42,128
Ln v8,32,s65,3072
S_ m0,s51,25472
Mavv 212,38,128
Mavv 208,38,124
Ln v7,32,s65,128
Mavv 228,42,124
Mavv 224,42,120
Ln v8,32,s65,3200
S_ m0,s51,26368
Mavv 204,38,120
Mavv 200,38,116
Ln v7,32,s65,256
Mavv 220,42,116
.Lstep1_11:
Zvvv 76,92,96
Dv 38,%0,7168
Zvvv 80,92,100
Dv 42,v1,7168
Zvvv 84,92,104
Zvvv 88,92,108
Zvvv 92,92,112
W_ 2
Nvvv 116,30,136,56
Mvvv 116,34,156
Nvvv 120,30,140,60
s_nop 1
v_max3_f32 v10, v76, v80, v84
s_nop 0
v_max3_f32 v10, v10, v88, v92
Mvvv 120,34,160
v_cmp_lt_f32 vcc, 0x42000000, v10
s_cbranch_vccnz .Ltramp1_10
Nvvv 124,30,144,64
A_ v46,v46,v76
A_ v47,v47,v80
Mvvv 124,34,164
A_ v48,v48,v84
A_ v49,v49,v88
Nvvv 128,30,148,68
A_ v50,v50,v92
Mvvv 128,34,168
Ln v8,32,s65,3328
Nvvv 132,30,152,72
Mvvv 132,34,172
W_ 0
Mvvv 132,38,192
Dv 30,%0,8192
Mvvv 132,42,212
Dv 34,v1,8192
Mvvv 128,38,188
Mvvv 128,42,208
Mvvv 124,38,184
Mvvv 124,42,204
Mvvv 120,38,180
Mvvv 120,42,200
Mvvv 116,38,176
Mvvv 116,42,196
W_ 0
Mvvv 116,30,216
Dv 38,%0,0
Mvvv 116,34,236
Dv 42,v1,0
Mvvv 120,30,220
Mvvv 120,34,240
Mvvv 124,30,224
Mvvv 124,34,244
Mvvv 128,30,228
Mvvv 128,34,248
s_nop 0
E_ 116
Mvvv 132,30,232
E_ 117
Mvvv 132,34,252
W_ 0
E_ 118
Nvvv 76,38,136,56
Dv 30,%0,1024
Mvvv 76,42,156
Dv 34,v1,1024
Nvvv 80,38,140,60
E_ 119
Mvvv 80,42,160
C_ 119,118,119
C_ 118,116,117
Nvvv 84,38,144,64
E_ 120
Mvvv 84,42,164
E_ 121
Nvvv 88,38,148,68
E_ 122
Mvvv 88,42,168
E_ 123
Nvvv 92,38,152,72
C_ 123,122,123
C_ 122,120,121
Mvvv 92,42,172
W_ 0
E_ 124
Mvvv 92,30,192
Dv 38,%0,2048
Mvvv 92,34,212
Dv 42,v1,2048
Mvvv 88,30,188
E_ 125
Mvvv 88,34,208
E_ 126
Mvvv 84,30,184
S_ s65,s61,135168
E_ 127
Mvvv 84,34,204
S_ m0,s51,0
C_ 127,126,127
C_ 126,124,125
Mvvv 80,30,180
s_waitcnt vmcnt(6) lgkmcnt(0)
s_barrier
Mvvv 80,34,200
L_ v9,36,s65,0
Mvvv 76,30,176
E_ 128
Mvvv 76,34,196
L_ v9,36,s65,3072
S_ m0,s51,896
Mvvv 76,38,216
Tv 30,v2,12288
Tv 32,v2,18432
Mvvv 76,42,236
L_ v9,36,s65,128
Mvvv 80,38,220
s_nop 0
Tv 34,v3,12288
Tv 36,v3,18432
Mvvv 80,42,240
L_ v9,36,s65,3200
S_ m0,s51,1792
Mvvv 84,38,224
E_ 129
Mvvv 84,42,244
L_ v9,36,s65,256
Mvvv 88,38,228
E_ 130
Mvvv 88,42,248
L_ v9,36,s65,3328
Mvvv 92,38,232
E_ 131
Mvvv 92,42,252
W_ 0
C_ 131,130,131
C_ 130,128,129
Mavv 0,30,96
s_nop 0
Tv 38,v4,12288
Tv 40,v4,18432
Mavv 20,34,96
E_ 132
Mavv 24,34,100
s_nop 0
Tv 42,v5,12288
Tv 44,v5,18432
Mavv 4,30,100
E_ 133
Mavv 8,30,104
E_ 134
Mavv 28,34,104
E_ 135
Mavv 32,34,108
C_ 135,134,135
C_ 134,132,133
Mavv 12,30,108
E_ 76
Mavv 16,30,112
E_ 77
Mavv 36,34,112
W_ 0
E_ 78
Mavv 56,38,112
s_nop 0
Tv 30,v2,13312
Tv 32,v2,19456
Mavv 76,42,112
E_ 79
Mavv 72,42,108
s_nop 0
Tv 34,v3,13312
Tv 36,v3,19456
Mavv 52,38,108
C_ 116,76,77
C_ 117,78,79
Mavv 48,38,104
E_ 80
Mavv 68,42,104
E_ 81
Mavv 64,42,100
E_ 82
Mavv 44,38,100
E_ 83
Mavv 40,38,96
C_ 120,80,81
C_ 121,82,83
Mavv 60,42,96
W_ 0
E_ 84
Mavv 80,30,96
s_nop 0
Tv 38,v4,13312
Tv 40,v4,19456
Mavv 100,34,96
E_ 85
Mavv 104,34,100
s_nop 0
Tv 42,v5,13312
Tv 44,v5,19456
Mavv 84,30,100
E_ 86
Mavv 88,30,104
E_ 87
Mavv 108,34,104
C_ 124,84,85
C_ 125,86,87
Mavv 112,34,108
E_ 88
Mavv 92,30,108
E_ 89
Mavv 96,30,112
E_ 90
Mavv 116,34,112
W_ 0
E_ 91
Mavv 136,38,112
s_nop 0
Tv 30,v2,14336
Tv 32,v2,20480
Mavv 156,42,112
C_ 128,88,89
C_ 129,90,91
Mavv 152,42,108
s_nop 0
Tv 34,v3,14336
Tv 36,v3,20480
Mavv 132,38,108
E_ 92
Mavv 128,38,104
E_ 93
Mavv 148,42,104
E_ 94
Mavv 144,42,100
E_ 95
Mavv 124,38,100
C_ 132,92,93
C_ 133,94,95
Mavv 120,38,96
v_mov_b32 v92, 0x3f803f80
v_mov_b32 v93, 0x3f803f80
Mavv 140,42,96
W_ 0
v_mov_b32 v94, 0x3f803f80
v_mov_b32 v95, 0x3f803f80
Mavv 160,30,96
s_nop 0
Tv 38,v4,14336
Tv 40,v4,20480
Mavv 180,34,96
Mavv 184,34,100
s_nop 0
Tv 42,v5,14336
Tv 44,v5,20480
Mavv 164,30,100
Mavv 168,30,104
Mavv 188,34,104
S_ s65,s61,159744
Mavv 192,34,108
S_ m0,s51,12288
Mavv 172,30,108
s_waitcnt vmcnt(6) lgkmcnt(0)
s_barrier
Mavv 176,30,112
Ln v7,32,s65,0
Mavv 196,34,112
Mavv 216,38,112
Dv 30,%0,30720
Mavv 236,42,112
Dv 34,v1,30720
Mavv 232,42,108
Ln v8,32,s65,3072
S_ m0,s51,13184
Mavv 212,38,108
Mavv 208,38,104
Ln v7,32,s65,128
Mavv 228,42,104
Mavv 224,42,100
Ln v8,32,s65,3200
S_ m0,s51,14080
Mavv 204,38,100
Mavv 200,38,96
Ln v7,32,s65,256
Mavv 220,42,96
.Lstep1_12:
Zvvv 76,92,116
Dv 38,%0,31744
Zvvv 80,92,120
Dv 42,v1,31744
Zvvv 84,92,124
Zvvv 88,92,128
Zvvv 92,92,132
W_ 2
Nvvv 96,30,136,56
Mvvv 96,34,156
Nvvv 100,30,140,60
s_nop 1
v_max3_f32 v10, v76, v80, v84
s_nop 0
v_max3_f32 v10, v10, v88, v92
Mvvv 100,34,160
v_cmp_lt_f32 vcc, 0x42000000, v10
s_cbranch_vccnz .Ltramp1_11
Nvvv 104,30,144,64
A_ v46,v46,v76
A_ v47,v47,v80
Mvvv 104,34,164
A_ v48,v48,v84
A_ v49,v49,v88
Nvvv 108,30,148,68
A_ v50,v50,v92
Mvvv 108,34,168
Ln v8,32,s65,3328
Nvvv 112,30,152,72
R_ v11,a248
Mvvv 112,34,172
W_ 0
v_min_u32 v11, 240, v11
Mvvv 112,38,192
Dv 30,%0,32768
v_lshlrev_b32 v11, 3, v11
Mvvv 112,42,212
Dv 34,v1,32768
Mvvv 108,38,188
buffer_load_dword a254, v11, s[44:47], s66 offen
Mvvv 108,42,208
Mvvv 104,38,184
Mvvv 104,42,204
Mvvv 100,38,180
Mvvv 100,42,200
Mvvv 96,38,176
Mvvv 96,42,196
W_ 0
Mvvv 96,30,216
Dv 38,%0,24576
Mvvv 96,34,236
Dv 42,v1,24576
Mvvv 100,30,220
Mvvv 100,34,240
Mvvv 104,30,224
Mvvv 104,34,244
Mvvv 108,30,228
Mvvv 108,34,248
s_nop 0
E_ 96
Mvvv 112,30,232
E_ 97
Mvvv 112,34,252
W_ 0
E_ 98
Nvvv 76,38,136,56
Dv 30,%0,25600
Mvvv 76,42,156
Dv 34,v1,25600
Nvvv 80,38,140,60
E_ 99
Mvvv 80,42,160
C_ 99,98,99
C_ 98,96,97
Nvvv 84,38,144,64
E_ 100
Mvvv 84,42,164
E_ 101
Nvvv 88,38,148,68
E_ 102
Mvvv 88,42,168
E_ 103
Nvvv 92,38,152,72
C_ 103,102,103
C_ 102,100,101
Mvvv 92,42,172
W_ 0
E_ 104
Mvvv 92,30,192
Dv 38,%0,26624
Mvvv 92,34,212
Dv 42,v1,26624
Mvvv 88,30,188
E_ 105
Mvvv 88,34,208
E_ 106
Mvvv 84,30,184
S_ s65,s61,147456
E_ 107
Mvvv 84,34,204
S_ m0,s51,24576
C_ 107,106,107
C_ 106,104,105
Mvvv 80,30,180
s_waitcnt vmcnt(7) lgkmcnt(0)
s_barrier
Mvvv 80,34,200
L_ v9,36,s65,0
Mvvv 76,30,176
E_ 108
Mvvv 76,34,196
L_ v9,36,s65,3072
S_ m0,s51,25472
Mvvv 76,38,216
Tv 30,v2,0
Tv 32,v2,6144
Mvvv 76,42,236
L_ v9,36,s65,128
Mvvv 80,38,220
s_nop 0
Tv 34,v3,0
Tv 36,v3,6144
Mvvv 80,42,240
L_ v9,36,s65,3200
S_ m0,s51,26368
Mvvv 84,38,224
E_ 109
Mvvv 84,42,244
L_ v9,36,s65,256
Mvvv 88,38,228
E_ 110
Mvvv 88,42,248
L_ v9,36,s65,3328
Mvvv 92,38,232
E_ 111
Mvvv 92,42,252
W_ 0
C_ 111,110,111
C_ 110,108,109
Mavv 0,30,116
s_nop 0
Tv 38,v4,0
Tv 40,v4,6144
Mavv 20,34,116
E_ 112
Mavv 24,34,120
s_nop 0
Tv 42,v5,0
Tv 44,v5,6144
Mavv 4,30,120
E_ 113
Mavv 8,30,124
E_ 114
Mavv 28,34,124
E_ 115
Mavv 32,34,128
C_ 115,114,115
C_ 114,112,113
Mavv 12,30,128
E_ 76
Mavv 16,30,132
E_ 77
Mavv 36,34,132
W_ 0
E_ 78
Mavv 56,38,132
s_nop 0
Tv 30,v2,1024
Tv 32,v2,7168
Mavv 76,42,132
E_ 79
Mavv 72,42,128
s_nop 0
Tv 34,v3,1024
Tv 36,v3,7168
Mavv 52,38,128
C_ 96,76,77
C_ 97,78,79
Mavv 48,38,124
E_ 80
Mavv 68,42,124
E_ 81
Mavv 64,42,120
E_ 82
Mavv 44,38,120
E_ 83
Mavv 40,38,116
C_ 100,80,81
C_ 101,82,83
Mavv 60,42,116
W_ 0
E_ 84
Mavv 80,30,116
s_nop 0
Tv 38,v4,1024
Tv 40,v4,7168
Mavv 100,34,116
E_ 85
Mavv 104,34,120
s_nop 0
Tv 42,v5,1024
Tv 44,v5,7168
Mavv 84,30,120
E_ 86
Mavv 88,30,124
E_ 87
Mavv 108,34,124
C_ 104,84,85
C_ 105,86,87
Mavv 112,34,128
E_ 88
Mavv 92,30,128
E_ 89
Mavv 96,30,132
E_ 90
Mavv 116,34,132
W_ 0
E_ 91
Mavv 136,38,132
s_nop 0
Tv 30,v2,2048
Tv 32,v2,8192
Mavv 156,42,132
C_ 108,88,89
C_ 109,90,91
Mavv 152,42,128
s_nop 0
Tv 34,v3,2048
Tv 36,v3,8192
Mavv 132,38,128
E_ 92
Mavv 128,38,124
E_ 93
Mavv 148,42,124
E_ 94
Mavv 144,42,120
E_ 95
Mavv 124,38,120
C_ 112,92,93
C_ 113,94,95
Mavv 120,38,116
v_mov_b32 v92, 0x3f803f80
v_mov_b32 v93, 0x3f803f80
Mavv 140,42,116
W_ 0
v_mov_b32 v94, 0x3f803f80
v_mov_b32 v95, 0x3f803f80
Mavv 160,30,116
s_nop 0
Tv 38,v4,2048
Tv 40,v4,8192
Mavv 180,34,116
Mavv 184,34,120
s_nop 0
Tv 42,v5,2048
Tv 44,v5,8192
Mavv 164,30,120
Mavv 168,30,124
Mavv 188,34,124
S_ s65,s61,172032
Mavv 192,34,128
S_ m0,s51,0
Mavv 172,30,128
s_waitcnt vmcnt(7) lgkmcnt(0)
s_barrier
Mavv 176,30,132
Ln v7,32,s65,0
Mavv 196,34,132
Mavv 216,38,132
Dv 30,%0,18432
Mavv 236,42,132
Dv 34,v1,18432
Mavv 232,42,128
Ln v8,32,s65,3072
S_ m0,s51,896
Mavv 212,38,128
Mavv 208,38,124
Ln v7,32,s65,128
Mavv 228,42,124
Mavv 224,42,120
Ln v8,32,s65,3200
S_ m0,s51,1792
Mavv 204,38,120
Mavv 200,38,116
Ln v7,32,s65,256
Mavv 220,42,116
.Lstep1_13:
Zvvv 76,92,96
Dv 38,%0,19456
Zvvv 80,92,100
Dv 42,v1,19456
Zvvv 84,92,104
Zvvv 88,92,108
Zvvv 92,92,112
W_ 2
Nvvv 116,30,136,56
Mvvv 116,34,156
Nvvv 120,30,140,60
s_nop 1
v_max3_f32 v10, v76, v80, v84
s_nop 0
v_max3_f32 v10, v10, v88, v92
Mvvv 120,34,160
v_cmp_lt_f32 vcc, 0x42000000, v10
s_cbranch_vccnz .Ltramp1_12
Nvvv 124,30,144,64
A_ v46,v46,v76
A_ v47,v47,v80
Mvvv 124,34,164
A_ v48,v48,v84
A_ v49,v49,v88
Nvvv 128,30,148,68
A_ v50,v50,v92
Mvvv 128,34,168
Ln v8,32,s65,3328
Nvvv 132,30,152,72
Mvvv 132,34,172
W_ 0
Mvvv 132,38,192
Dv 30,%0,20480
Mvvv 132,42,212
Dv 34,v1,20480
Mvvv 128,38,188
Mvvv 128,42,208
Mvvv 124,38,184
Mvvv 124,42,204
Mvvv 120,38,180
Mvvv 120,42,200
Mvvv 116,38,176
Mvvv 116,42,196
W_ 0
Mvvv 116,30,216
Dv 38,%0,12288
Mvvv 116,34,236
Dv 42,v1,12288
Mvvv 120,30,220
Mvvv 120,34,240
Mvvv 124,30,224
Mvvv 124,34,244
Mvvv 128,30,228
Mvvv 128,34,248
s_nop 0
E_ 116
Mvvv 132,30,232
E_ 117
Mvvv 132,34,252
W_ 0
E_ 118
Nvvv 76,38,136,56
Dv 30,%0,13312
Mvvv 76,42,156
Dv 34,v1,13312
Nvvv 80,38,140,60
E_ 119
Mvvv 80,42,160
C_ 119,118,119
C_ 118,116,117
Nvvv 84,38,144,64
E_ 120
Mvvv 84,42,164
E_ 121
Nvvv 88,38,148,68
E_ 122
Mvvv 88,42,168
E_ 123
Nvvv 92,38,152,72
C_ 123,122,123
C_ 122,120,121
Mvvv 92,42,172
W_ 0
E_ 124
Mvvv 92,30,192
Dv 38,%0,14336
Mvvv 92,34,212
Dv 42,v1,14336
Mvvv 88,30,188
E_ 125
Mvvv 88,34,208
E_ 126
Mvvv 84,30,184
S_ s65,s61,159744
E_ 127
Mvvv 84,34,204
S_ m0,s51,12288
C_ 127,126,127
C_ 126,124,125
Mvvv 80,30,180
s_waitcnt vmcnt(6) lgkmcnt(0)
s_barrier
Mvvv 80,34,200
L_ v9,36,s65,0
Mvvv 76,30,176
E_ 128
Mvvv 76,34,196
L_ v9,36,s65,3072
S_ m0,s51,13184
Mvvv 76,38,216
Tv 30,v2,24576
Tv 32,v2,30720
Mvvv 76,42,236
L_ v9,36,s65,128
Mvvv 80,38,220
s_nop 0
Tv 34,v3,24576
Tv 36,v3,30720
Mvvv 80,42,240
L_ v9,36,s65,3200
S_ m0,s51,14080
Mvvv 84,38,224
E_ 129
Mvvv 84,42,244
L_ v9,36,s65,256
Mvvv 88,38,228
E_ 130
Mvvv 88,42,248
L_ v9,36,s65,3328
Mvvv 92,38,232
E_ 131
Mvvv 92,42,252
W_ 0
C_ 131,130,131
C_ 130,128,129
Mavv 0,30,96
s_nop 0
Tv 38,v4,24576
Tv 40,v4,30720
Mavv 20,34,96
E_ 132
Mavv 24,34,100
s_nop 0
Tv 42,v5,24576
Tv 44,v5,30720
Mavv 4,30,100
E_ 133
Mavv 8,30,104
E_ 134
Mavv 28,34,104
E_ 135
Mavv 32,34,108
C_ 135,134,135
C_ 134,132,133
Mavv 12,30,108
E_ 76
Mavv 16,30,112
E_ 77
Mavv 36,34,112
W_ 0
E_ 78
Mavv 56,38,112
s_nop 0
Tv 30,v2,25600
Tv 32,v2,31744
Mavv 76,42,112
E_ 79
Mavv 72,42,108
s_nop 0
Tv 34,v3,25600
Tv 36,v3,31744
Mavv 52,38,108
C_ 116,76,77
C_ 117,78,79
Mavv 48,38,104
E_ 80
Mavv 68,42,104
E_ 81
Mavv 64,42,100
E_ 82
Mavv 44,38,100
E_ 83
Mavv 40,38,96
C_ 120,80,81
C_ 121,82,83
Mavv 60,42,96
W_ 0
E_ 84
Mavv 80,30,96
s_nop 0
Tv 38,v4,25600
Tv 40,v4,31744
Mavv 100,34,96
E_ 85
Mavv 104,34,100
s_nop 0
Tv 42,v5,25600
Tv 44,v5,31744
Mavv 84,30,100
E_ 86
Mavv 88,30,104
E_ 87
Mavv 108,34,104
C_ 124,84,85
C_ 125,86,87
Mavv 112,34,108
E_ 88
Mavv 92,30,108
E_ 89
Mavv 96,30,112
E_ 90
Mavv 116,34,112
W_ 0
E_ 91
Mavv 136,38,112
s_nop 0
Tv 30,v2,26624
Tv 32,v2,32768
Mavv 156,42,112
C_ 128,88,89
C_ 129,90,91
Mavv 152,42,108
s_nop 0
Tv 34,v3,26624
Tv 36,v3,32768
Mavv 132,38,108
E_ 92
Mavv 128,38,104
E_ 93
Mavv 148,42,104
E_ 94
Mavv 144,42,100
E_ 95
Mavv 124,38,100
C_ 132,92,93
C_ 133,94,95
Mavv 120,38,96
v_mov_b32 v92, 0x3f803f80
v_mov_b32 v93, 0x3f803f80
Mavv 140,42,96
W_ 0
v_mov_b32 v94, 0x3f803f80
v_mov_b32 v95, 0x3f803f80
Mavv 160,30,96
s_nop 0
Tv 38,v4,26624
Tv 40,v4,32768
Mavv 180,34,96
Mavv 184,34,100
s_nop 0
Tv 42,v5,26624
Tv 44,v5,32768
Mavv 164,30,100
Mavv 168,30,104
Mavv 188,34,104
S_ s65,s61,184320
Mavv 192,34,108
S_ m0,s51,24576
Mavv 172,30,108
s_waitcnt vmcnt(6) lgkmcnt(0)
s_barrier
Mavv 176,30,112
Ln v7,32,s65,0
Mavv 196,34,112
Mavv 216,38,112
Dv 30,%0,6144
Mavv 236,42,112
Dv 34,v1,6144
Mavv 232,42,108
Ln v8,32,s65,3072
S_ m0,s51,25472
Mavv 212,38,108
Mavv 208,38,104
Ln v7,32,s65,128
Mavv 228,42,104
Mavv 224,42,100
Ln v8,32,s65,3200
S_ m0,s51,26368
Mavv 204,38,100
Mavv 200,38,96
Ln v7,32,s65,256
Mavv 220,42,96
.Lstep1_14:
Zvvv 76,92,116
Dv 38,%0,7168
Zvvv 80,92,120
Dv 42,v1,7168
Zvvv 84,92,124
Zvvv 88,92,128
Zvvv 92,92,132
W_ 2
Nvvv 96,30,136,56
Mvvv 96,34,156
Nvvv 100,30,140,60
s_nop 1
v_max3_f32 v10, v76, v80, v84
s_nop 0
v_max3_f32 v10, v10, v88, v92
Mvvv 100,34,160
v_cmp_lt_f32 vcc, 0x42000000, v10
s_cbranch_vccnz .Ltramp1_13
Nvvv 104,30,144,64
A_ v46,v46,v76
A_ v47,v47,v80
Mvvv 104,34,164
A_ v48,v48,v84
A_ v49,v49,v88
Nvvv 108,30,148,68
A_ v50,v50,v92
Mvvv 108,34,168
Ln v8,32,s65,3328
Nvvv 112,30,152,72
Mvvv 112,34,172
W_ 0
Mvvv 112,38,192
Dv 30,%0,8192
Mvvv 112,42,212
Dv 34,v1,8192
Mvvv 108,38,188
Mvvv 108,42,208
Mvvv 104,38,184
Mvvv 104,42,204
Mvvv 100,38,180
Mvvv 100,42,200
Mvvv 96,38,176
Mvvv 96,42,196
W_ 0
Mvvv 96,30,216
Dv 38,%0,0
Mvvv 96,34,236
Dv 42,v1,0
Mvvv 100,30,220
Mvvv 100,34,240
Mvvv 104,30,224
Mvvv 104,34,244
Mvvv 108,30,228
Mvvv 108,34,248
s_nop 0
E_ 96
Mvvv 112,30,232
E_ 97
Mvvv 112,34,252
W_ 0
E_ 98
Nvvv 76,38,136,56
Dv 30,%0,1024
Mvvv 76,42,156
Dv 34,v1,1024
Nvvv 80,38,140,60
E_ 99
Mvvv 80,42,160
C_ 99,98,99
C_ 98,96,97
Nvvv 84,38,144,64
E_ 100
Mvvv 84,42,164
E_ 101
Nvvv 88,38,148,68
E_ 102
Mvvv 88,42,168
E_ 103
Nvvv 92,38,152,72
C_ 103,102,103
C_ 102,100,101
Mvvv 92,42,172
W_ 0
E_ 104
Mvvv 92,30,192
Dv 38,%0,2048
Mvvv 92,34,212
Dv 42,v1,2048
Mvvv 88,30,188
E_ 105
Mvvv 88,34,208
E_ 106
Mvvv 84,30,184
S_ s65,s61,172032
E_ 107
Mvvv 84,34,204
S_ m0,s51,0
C_ 107,106,107
C_ 106,104,105
Mvvv 80,30,180
s_mov_b64 exec, 0
s_bitcmp1_b32 s4, 1
s_cbranch_scc1 .Lhop_o_b
s_bitcmp1_b32 s4, 0
s_cbranch_scc1 .Lpad_o_1
s_branch .Lpad_o_0
.Lhop_o_b:
s_bitcmp1_b32 s4, 0
s_cbranch_scc1 .Lpad_o_3
s_branch .Lpad_o_2
.Lhop_o_r:
s_mov_b64 exec, -1
s_nop 1
s_waitcnt vmcnt(6) lgkmcnt(0)
s_barrier
Mvvv 80,34,200
L_ v9,36,s65,0
Mvvv 76,30,176
E_ 108
Mvvv 76,34,196
L_ v9,36,s65,3072
S_ m0,s51,896
Mvvv 76,38,216
Tv 30,v2,12288
Tv 32,v2,18432
Mvvv 76,42,236
L_ v9,36,s65,128
Mvvv 80,38,220
s_nop 0
Tv 34,v3,12288
Tv 36,v3,18432
Mvvv 80,42,240
L_ v9,36,s65,3200
S_ m0,s51,1792
Mvvv 84,38,224
E_ 109
Mvvv 84,42,244
L_ v9,36,s65,256
Mvvv 88,38,228
E_ 110
Mvvv 88,42,248
L_ v9,36,s65,3328
Mvvv 92,38,232
E_ 111
Mvvv 92,42,252
W_ 0
C_ 111,110,111
C_ 110,108,109
Mavv 0,30,116
s_nop 0
Tv 38,v4,12288
Tv 40,v4,18432
Mavv 20,34,116
E_ 112
Mavv 24,34,120
s_nop 0
Tv 42,v5,12288
Tv 44,v5,18432
Mavv 4,30,120
E_ 113
Mavv 8,30,124
E_ 114
Mavv 28,34,124
E_ 115
Mavv 32,34,128
C_ 115,114,115
C_ 114,112,113
Mavv 12,30,128
E_ 76
Mavv 16,30,132
E_ 77
Mavv 36,34,132
W_ 0
E_ 78
Mavv 56,38,132
s_nop 0
Tv 30,v2,13312
Tv 32,v2,19456
Mavv 76,42,132
E_ 79
Mavv 72,42,128
s_nop 0
Tv 34,v3,13312
Tv 36,v3,19456
Mavv 52,38,128
C_ 96,76,77
C_ 97,78,79
Mavv 48,38,124
E_ 80
Mavv 68,42,124
E_ 81
Mavv 64,42,120
E_ 82
Mavv 44,38,120
E_ 83
Mavv 40,38,116
C_ 100,80,81
C_ 101,82,83
Mavv 60,42,116
W_ 0
E_ 84
Mavv 80,30,116
s_nop 0
Tv 38,v4,13312
Tv 40,v4,19456
Mavv 100,34,116
E_ 85
Mavv 104,34,120
s_nop 0
Tv 42,v5,13312
Tv 44,v5,19456
Mavv 84,30,120
E_ 86
Mavv 88,30,124
E_ 87
Mavv 108,34,124
C_ 104,84,85
C_ 105,86,87
Mavv 112,34,128
E_ 88
Mavv 92,30,128
E_ 89
Mavv 96,30,132
E_ 90
Mavv 116,34,132
W_ 0
E_ 91
Mavv 136,38,132
s_nop 0
Tv 30,v2,14336
Tv 32,v2,20480
Mavv 156,42,132
C_ 108,88,89
C_ 109,90,91
Mavv 152,42,128
s_nop 0
Tv 34,v3,14336
Tv 36,v3,20480
Mavv 132,38,128
E_ 92
Mavv 128,38,124
E_ 93
Mavv 148,42,124
E_ 94
Mavv 144,42,120
E_ 95
Mavv 124,38,120
C_ 112,92,93
C_ 113,94,95
Mavv 120,38,116
v_mov_b32 v92, 0x3f803f80
v_mov_b32 v93, 0x3f803f80
Mavv 140,42,116
W_ 0
v_mov_b32 v94, 0x3f803f80
v_mov_b32 v95, 0x3f803f80
Mavv 160,30,116
s_nop 0
Tv 38,v4,14336
Tv 40,v4,20480
Mavv 180,34,116
Mavv 184,34,120
s_nop 0
Tv 42,v5,14336
Tv 44,v5,20480
Mavv 164,30,120
Mavv 168,30,124
Mavv 188,34,124
S_ s65,s61,184320
Mavv 192,34,128
S_ m0,s51,12288
Mavv 172,30,128
s_waitcnt vmcnt(6) lgkmcnt(0)
s_barrier
Mavv 176,30,132
L_ v9,36,s65,0
Mavv 196,34,132
Mavv 216,38,132
Dv 30,%0,30720
Mavv 236,42,132
Dv 34,v1,30720
Mavv 232,42,128
L_ v9,36,s65,3072
S_ m0,s51,13184
Mavv 212,38,128
Mavv 208,38,124
L_ v9,36,s65,128
Mavv 228,42,124
Mavv 224,42,120
L_ v9,36,s65,3200
S_ m0,s51,14080
Mavv 204,38,120
Mavv 200,38,116
L_ v9,36,s65,256
Mavv 220,42,116
.Lstep1_15:
Zvvv 76,92,96
Dv 38,%0,31744
Zvvv 80,92,100
Dv 42,v1,31744
Zvvv 84,92,104
Zvvv 88,92,108
Zvvv 92,92,112
W_ 2
Nvvv 116,30,136,56
Mvvv 116,34,156
Nvvv 120,30,140,60
s_nop 1
v_max3_f32 v10, v76, v80, v84
s_nop 0
v_max3_f32 v10, v10, v88, v92
Mvvv 120,34,160
v_cmp_lt_f32 vcc, 0x42000000, v10
s_cbranch_vccnz .Ltramp1_14
Nvvv 124,30,144,64
A_ v46,v46,v76
A_ v47,v47,v80
Mvvv 124,34,164
A_ v48,v48,v84
A_ v49,v49,v88
Nvvv 128,30,148,68
A_ v50,v50,v92
Mvvv 128,34,168
L_ v9,36,s65,3328
Nvvv 132,30,152,72
Mvvv 132,34,172
W_ 0
Mvvv 132,38,192
Dv 30,%0,32768
Mvvv 132,42,212
Dv 34,v1,32768
Mvvv 128,38,188
Mvvv 128,42,208
Mvvv 124,38,184
Mvvv 124,42,204
Mvvv 120,38,180
Mvvv 120,42,200
Mvvv 116,38,176
Mvvv 116,42,196
W_ 0
Mvvv 116,30,216
Dv 38,%0,24576
Mvvv 116,34,236
Dv 42,v1,24576
Mvvv 120,30,220
Mvvv 120,34,240
Mvvv 124,30,224
Mvvv 124,34,244
Mvvv 128,30,228
Mvvv 128,34,248
s_nop 0
E_ 116
Mvvv 132,30,232
E_ 117
Mvvv 132,34,252
W_ 0
E_ 118
Nvvv 76,38,136,56
Dv 30,%0,25600
Mvvv 76,42,156
Dv 34,v1,25600
Nvvv 80,38,140,60
E_ 119
Mvvv 80,42,160
C_ 119,118,119
C_ 118,116,117
Nvvv 84,38,144,64
E_ 120
Mvvv 84,42,164
E_ 121
Nvvv 88,38,148,68
E_ 122
)A"
R"A(Mvvv 88,42,168
E_ 123
Nvvv 92,38,152,72
C_ 123,122,123
C_ 122,120,121
Mvvv 92,42,172
W_ 0
E_ 124
Mvvv 92,30,192
Dv 38,%0,26624
Mvvv 92,34,212
Dv 42,v1,26624
Mvvv 88,30,188
E_ 125
Mvvv 88,34,208
E_ 126
Mvvv 84,30,184
E_ 127
Mvvv 84,34,204
C_ 127,126,127
C_ 126,124,125
Mvvv 80,30,180
s_waitcnt vmcnt(6) lgkmcnt(0)
s_barrier
Mvvv 80,34,200
R_ v7,a248
Mvvv 76,30,176
v_lshrrev_b32 v7, 7, v7
Mvvv 76,34,196
v_mul_u32_u24 v7, 896, v7
Mvvv 76,38,216
s_nop 0
Tv 30,v2,0
Tv 32,v2,6144
Mvvv 76,42,236
v_add_u32 v7, v7, v9
Mvvv 80,38,220
S_ s71,s72,1024
Tv 34,v3,0
Tv 36,v3,6144
Mvvv 80,42,240
S_ s55,s71,491520
E_ 128
Mvvv 84,38,224
S_ m0,s51,24576
E_ 129
Mvvv 84,42,244
L_ v7,24,s55,0
Mvvv 88,38,228
S_ s55,s71,501760
E_ 130
Mvvv 88,42,248
S_ m0,s51,25600
E_ 131
Mvvv 92,38,232
L_ v7,24,s55,0
Mvvv 92,42,252
S_ s55,s71,512000
C_ 131,130,131
C_ 130,128,129
W_ 2
Mavv 0,30,96
S_ m0,s51,26624
Tv 38,v4,0
Tv 40,v4,6144
W_ 2
Mavv 20,34,96
L_ v7,24,s55,0
Mavv 24,34,100
S_ s55,s71,522240
Tv 42,v5,0
Tv 44,v5,6144
Mavv 4,30,100
S_ m0,s51,27648
E_ 132
Mavv 8,30,104
L_ v7,24,s55,0
Mavv 28,34,104
S_ s55,s71,532480
E_ 133
Mavv 32,34,108
S_ m0,s51,28672
E_ 134
Mavv 12,30,108
L_ v7,24,s55,0
Mavv 16,30,112
S_ s55,s71,542720
E_ 135
Mavv 36,34,112
S_ m0,s51,29696
C_ 135,134,135
C_ 134,132,133
W_ 2
Mavv 56,38,112
Tv 30,v2,1024
Tv 32,v2,7168
W_ 2
Mavv 76,42,112
L_ v7,24,s55,0
Mavv 72,42,108
Tv 34,v3,1024
Tv 36,v3,7168
Mavv 52,38,108
E_ 76
Mavv 48,38,104
E_ 77
Mavv 68,42,104
E_ 78
Mavv 64,42,100
E_ 79
Mavv 44,38,100
C_ 116,76,77
C_ 117,78,79
Mavv 40,38,96
E_ 80
Mavv 60,42,96
W_ 0
E_ 81
Mavv 80,30,96
s_nop 0
Tv 38,v4,1024
Tv 40,v4,7168
Mavv 100,34,96
E_ 82
Mavv 104,34,100
s_nop 0
Tv 42,v5,1024
Tv 44,v5,7168
Mavv 84,30,100
E_ 83
Mavv 88,30,104
C_ 120,80,81
C_ 121,82,83
Mavv 108,34,104
E_ 84
Mavv 112,34,108
E_ 85
Mavv 92,30,108
E_ 86
Mavv 96,30,112
E_ 87
Mavv 116,34,112
W_ 0
C_ 124,84,85
C_ 125,86,87
Mavv 136,38,112
s_nop 0
Tv 30,v2,2048
Tv 32,v2,8192
Mavv 156,42,112
E_ 88
Mavv 152,42,108
s_nop 0
Tv 34,v3,2048
Tv 36,v3,8192
Mavv 132,38,108
E_ 89
Mavv 128,38,104
E_ 90
Mavv 148,42,104
E_ 91
Mavv 144,42,100
C_ 128,88,89
C_ 129,90,91
Mavv 124,38,100
E_ 92
Mavv 120,38,96
E_ 93
Mavv 140,42,96
W_ 0
E_ 94
Mavv 160,30,96
s_nop 0
Tv 38,v4,2048
Tv 40,v4,8192
Mavv 180,34,96
E_ 95
Mavv 184,34,100
s_nop 0
Tv 42,v5,2048
Tv 44,v5,8192
Mavv 164,30,100
C_ 132,92,93
C_ 133,94,95
Mavv 168,30,104
v_mov_b32 v92, 0x3f803f80
v_mov_b32 v93, 0x3f803f80
Mavv 188,34,104
v_mov_b32 v94, 0x3f803f80
v_mov_b32 v95, 0x3f803f80
Mavv 192,34,108
Mavv 172,30,108
s_waitcnt vmcnt(6) lgkmcnt(0)
s_barrier
Mavv 176,30,112
S_ s55,s71,614400
Mavv 196,34,112
S_ m0,s51,0
Mavv 216,38,112
s_nop 0
Tv 30,v2,12288
Tv 32,v2,18432
Zvvv 76,92,116
L_ v7,24,s55,0
Zvvv 80,92,120
S_ s55,s71,624640
Tv 34,v3,12288
Tv 36,v3,18432
Zvvv 84,92,124
S_ m0,s51,1024
R_ v11,a248
Zvvv 88,92,128
L_ v7,24,s55,0
Zvvv 92,92,132
S_ s55,s71,634880
v_mul_u32_u24 v11, 80, v11
Mavv 236,42,112
S_ m0,s51,2048
v_min_u32 v11, 29440, v11
Mavv 232,42,108
L_ v7,24,s55,0
Mavv 212,38,108
S_ s55,s71,645120
S_ s85,s87,1024
Mavv 208,38,104
S_ m0,s51,3072
v_max3_f32 v10, v76, v80, v84
Mavv 228,42,104
v_max3_f32 v10, v10, v88, v92
v_cmp_lt_f32 vcc, 0x42000000, v10
Mavv 224,42,100
L_ v7,24,s55,0
Mavv 204,38,100
S_ s55,s71,655360
Mavv 200,38,96
buffer_load_dword a254, v11, s[24:27], s85 offen
Mavv 220,42,96
S_ m0,s51,4096
S_ s85,s87,1152
buffer_load_dword a254, v11, s[24:27], s85 offen
s_cbranch_vccnz .Ltramp1_15
.Ljoin1:
W_ 2
Mavv 0,30,116
s_nop 0
Tv 38,v4,12288
Tv 40,v4,18432
W_ 2
Mavv 20,34,116
L_ v7,24,s55,0
Mavv 24,34,120
S_ s55,s71,665600
Tv 42,v5,12288
Tv 44,v5,18432
Mavv 4,30,120
S_ m0,s51,5120
R_ v13,a248
S_ s53,s51,36864
Mavv 8,30,124
L_ v7,24,s55,0
Mavv 28,34,124
v_add_u32 v12, s53, v13
s_mul_i32 s53, s4, 10240
Mavv 32,34,128
v_add_u32 v13, s53, v13
R_ v10,a248
Mavv 12,30,128
buffer_load_dword v11, off, s[92:95], s96 offset:0 sc1
Mavv 16,30,132
A_ v46,v46,v76
A_ v47,v47,v80
Mavv 36,34,132
W_ 0
A_ v48,v48,v84
A_ v49,v49,v88
Mavv 56,38,132
s_nop 0
Tv 30,v2,13312
Tv 32,v2,19456
Mavv 76,42,132
A_ v50,v50,v92
Mavv 72,42,128
s_nop 0
Tv 34,v3,13312
Tv 36,v3,19456
Mavv 52,38,128
v_rcp_f32 v46, v46
Mavv 48,38,124
v_rcp_f32 v47, v47
Mavv 68,42,124
v_rcp_f32 v48, v48
Mavv 64,42,120
v_rcp_f32 v49, v49
Mavv 44,38,120
v_rcp_f32 v50, v50
Mavv 40,38,116
R_ v96,a0
R_ v97,a1
Mavv 60,42,116
W_ 0
R_ v98,a2
R_ v99,a3
Mavv 80,30,116
s_nop 0
Tv 38,v4,13312
Tv 40,v4,19456
Mavv 100,34,116
R_ v100,a20
R_ v101,a21
Mavv 104,34,120
s_nop 0
Tv 42,v5,13312
Tv 44,v5,19456
Mavv 84,30,120
R_ v102,a22
R_ v103,a23
Mavv 88,30,124
U_ v96,v96,v46
U_ v97,v97,v46
Mavv 108,34,124
U_ v98,v98,v46
U_ v99,v99,v46
Mavv 112,34,128
U_ v100,v100,v46
U_ v101,v101,v46
Mavv 92,30,128
U_ v102,v102,v46
U_ v103,v103,v46
Mavv 96,30,132
C_ 136,96,97
C_ 137,98,99
Mavv 116,34,132
W_ 0
C_ 138,100,101
C_ 139,102,103
Mavv 136,38,132
s_nop 0
Tv 30,v2,14336
Tv 32,v2,20480
Mavv 156,42,132
Da 0,v13,0
Mavv 152,42,128
s_nop 0
Tv 34,v3,14336
Tv 36,v3,20480
Mavv 132,38,128
R_ v104,a4
R_ v105,a5
Mavv 128,38,124
R_ v106,a6
R_ v107,a7
Mavv 148,42,124
R_ v108,a24
R_ v109,a25
Mavv 144,42,120
R_ v110,a26
R_ v111,a27
Mavv 124,38,120
U_ v104,v104,v47
U_ v105,v105,v47
Mavv 120,38,116
U_ v106,v106,v47
U_ v107,v107,v47
Mavv 140,42,116
W_ 0
U_ v108,v108,v47
U_ v109,v109,v47
Mavv 160,30,116
s_nop 0
Tv 38,v4,14336
Tv 40,v4,20480
Mavv 180,34,116
U_ v110,v110,v47
U_ v111,v111,v47
Mavv 184,34,120
s_nop 0
Tv 42,v5,14336
Tv 44,v5,20480
Mavv 164,30,120
C_ 140,104,105
C_ 141,106,107
Mavv 168,30,124
C_ 142,108,109
C_ 143,110,111
Mavv 188,34,124
Da 4,v13,1024
Mavv 192,34,128
R_ v96,a8
R_ v97,a9
Mavv 172,30,128
S_ s55,s71,0
R_ v98,a10
R_ v99,a11
Mavv 176,30,132
S_ m0,s51,12288
R_ v100,a28
R_ v101,a29
Mavv 196,34,132
s_waitcnt vmcnt(63) lgkmcnt(0)
s_barrier
R_ v102,a30
Mavv 216,38,132
L_ v7,24,s55,0
S_ s55,s71,10240
S_ m0,s51,13312
Mavv 236,42,132
R_ v103,a31
Mavv 232,42,128
L_ v7,24,s55,0
S_ s55,s71,20480
S_ m0,s51,14336
Mavv 212,38,128
U_ v96,v96,v48
U_ v97,v97,v48
Mavv 208,38,124
L_ v7,24,s55,0
S_ s55,s71,30720
S_ m0,s51,15360
Mavv 228,42,124
U_ v98,v98,v48
U_ v99,v99,v48
Mavv 224,42,120
L_ v7,24,s55,0
S_ s55,s71,40960
S_ m0,s51,16384
Mavv 204,38,120
U_ v100,v100,v48
U_ v101,v101,v48
Mavv 200,38,116
L_ v7,24,s55,0
S_ s55,s71,51200
S_ m0,s51,17408
Mavv 220,42,116
U_ v102,v102,v48
U_ v103,v103,v48
C_ 144,96,97
C_ 145,98,99
C_ 146,100,101
C_ 147,102,103
Da 8,v13,2048
L_ v7,24,s55,0
.p2align 3
R_ v104,a12
R_ v105,a13
R_ v106,a14
R_ v107,a15
R_ v108,a32
R_ v109,a33
R_ v110,a34
R_ v111,a35
v_pk_mul_f32 v[104:105], v[104:105], v[48:49] op_sel:[0,1]
v_pk_mul_f32 v[106:107], v[106:107], v[48:49] op_sel:[0,1]
v_pk_mul_f32 v[108:109], v[108:109], v[48:49] op_sel:[0,1]
v_pk_mul_f32 v[110:111], v[110:111], v[48:49] op_sel:[0,1]
C_ 148,104,105
C_ 149,106,107
C_ 150,108,109
C_ 151,110,111
Da 12,v13,3072
.p2align 3
R_ v96,a16
R_ v97,a17
R_ v98,a18
R_ v99,a19
R_ v100,a36
R_ v101,a37
R_ v102,a38
R_ v103,a39
v_pk_mul_f32 v[96:97], v[96:97], v[50:51] op_sel_hi:[1,0]
v_pk_mul_f32 v[98:99], v[98:99], v[50:51] op_sel_hi:[1,0]
v_pk_mul_f32 v[100:101], v[100:101], v[50:51] op_sel_hi:[1,0]
v_pk_mul_f32 v[102:103], v[102:103], v[50:51] op_sel_hi:[1,0]
C_ 152,96,97
C_ 153,98,99
C_ 154,100,101
C_ 155,102,103
Da 16,v13,4096
.p2align 3
R_ v104,a40
R_ v105,a41
R_ v106,a42
R_ v107,a43
R_ v108,a60
R_ v109,a61
R_ v110,a62
R_ v111,a63
v_pk_mul_f32 v[104:105], v[104:105], v[46:47] op_sel_hi:[1,0]
v_pk_mul_f32 v[106:107], v[106:107], v[46:47] op_sel_hi:[1,0]
v_pk_mul_f32 v[108:109], v[108:109], v[46:47] op_sel_hi:[1,0]
v_pk_mul_f32 v[110:111], v[110:111], v[46:47] op_sel_hi:[1,0]
C_ 156,104,105
C_ 157,106,107
C_ 158,108,109
C_ 159,110,111
Da 40,v13,5120
.p2align 3
R_ v96,a44
R_ v97,a45
R_ v98,a46
R_ v99,a47
R_ v100,a64
R_ v101,a65
R_ v102,a66
R_ v103,a67
v_pk_mul_f32 v[96:97], v[96:97], v[46:47] op_sel:[0,1]
v_pk_mul_f32 v[98:99], v[98:99], v[46:47] op_sel:[0,1]
v_pk_mul_f32 v[100:101], v[100:101], v[46:47] op_sel:[0,1]
v_pk_mul_f32 v[102:103], v[102:103], v[46:47] op_sel:[0,1]
C_ 160,96,97
C_ 161,98,99
C_ 162,100,101
C_ 163,102,103
Da 44,v13,6144
.p2align 3
R_ v104,a48
R_ v105,a49
R_ v106,a50
R_ v107,a51
R_ v108,a68
R_ v109,a69
R_ v110,a70
R_ v111,a71
v_pk_mul_f32 v[104:105], v[104:105], v[48:49] op_sel_hi:[1,0]
v_pk_mul_f32 v[106:107], v[106:107], v[48:49] op_sel_hi:[1,0]
v_pk_mul_f32 v[108:109], v[108:109], v[48:49] op_sel_hi:[1,0]
v_pk_mul_f32 v[110:111], v[110:111], v[48:49] op_sel_hi:[1,0]
C_ 164,104,105
C_ 165,106,107
C_ 166,108,109
C_ 167,110,111
Da 48,v13,7168
.p2align 3
R_ v96,a52
R_ v97,a53
R_ v98,a54
R_ v99,a55
R_ v100,a72
R_ v101,a73
R_ v102,a74
R_ v103,a75
v_pk_mul_f32 v[96:97], v[96:97], v[48:49] op_sel:[0,1]
v_pk_mul_f32 v[98:99], v[98:99], v[48:49] op_sel:[0,1]
v_pk_mul_f32 v[100:101], v[100:101], v[48:49] op_sel:[0,1]
v_pk_mul_f32 v[102:103], v[102:103], v[48:49] op_sel:[0,1]
C_ 168,96,97
C_ 169,98,99
C_ 170,100,101
C_ 171,102,103
Da 52,v13,8192
.p2align 3
R_ v104,a56
R_ v105,a57
R_ v106,a58
R_ v107,a59
R_ v108,a76
R_ v109,a77
R_ v110,a78
R_ v111,a79
v_pk_mul_f32 v[104:105], v[104:105], v[50:51] op_sel_hi:[1,0]
v_pk_mul_f32 v[106:107], v[106:107], v[50:51] op_sel_hi:[1,0]
v_pk_mul_f32 v[108:109], v[108:109], v[50:51] op_sel_hi:[1,0]
v_pk_mul_f32 v[110:111], v[110:111], v[50:51] op_sel_hi:[1,0]
C_ 172,104,105
C_ 173,106,107
C_ 174,108,109
C_ 175,110,111
Da 56,v13,9216
.p2align 3
R_ v96,a80
R_ v97,a81
R_ v98,a82
R_ v99,a83
R_ v100,a100
R_ v101,a101
R_ v102,a102
R_ v103,a103
v_pk_mul_f32 v[96:97], v[96:97], v[46:47] op_sel_hi:[1,0]
v_pk_mul_f32 v[98:99], v[98:99], v[46:47] op_sel_hi:[1,0]
v_pk_mul_f32 v[100:101], v[100:101], v[46:47] op_sel_hi:[1,0]
v_pk_mul_f32 v[102:103], v[102:103], v[46:47] op_sel_hi:[1,0]
C_ 176,96,97
C_ 177,98,99
C_ 178,100,101
C_ 179,102,103
X_ a80,v20
X_ a81,v21
X_ a82,v22
X_ a83,v23
.p2align 3
R_ v104,a84
R_ v105,a85
R_ v106,a86
R_ v107,a87
R_ v108,a104
R_ v109,a105
R_ v110,a106
R_ v111,a107
v_pk_mul_f32 v[104:105], v[104:105], v[46:47] op_sel:[0,1]
v_pk_mul_f32 v[106:107], v[106:107], v[46:47] op_sel:[0,1]
v_pk_mul_f32 v[108:109], v[108:109], v[46:47] op_sel:[0,1]
v_pk_mul_f32 v[110:111], v[110:111], v[46:47] op_sel:[0,1]
C_ 180,104,105
C_ 181,106,107
C_ 182,108,109
C_ 183,110,111
X_ a84,v24
X_ a85,v25
X_ a86,v26
X_ a87,v27
.p2align 3
R_ v96,a88
R_ v97,a89
R_ v98,a90
R_ v99,a91
R_ v100,a108
R_ v101,a109
R_ v102,a110
R_ v103,a111
v_pk_mul_f32 v[96:97], v[96:97], v[48:49] op_sel_hi:[1,0]
v_pk_mul_f32 v[98:99], v[98:99], v[48:49] op_sel_hi:[1,0]
v_pk_mul_f32 v[100:101], v[100:101], v[48:49] op_sel_hi:[1,0]
v_pk_mul_f32 v[102:103], v[102:103], v[48:49] op_sel_hi:[1,0]
C_ 184,96,97
C_ 185,98,99
C_ 186,100,101
C_ 187,102,103
X_ a88,v52
X_ a89,v53
X_ a90,v54
X_ a91,v55
.p2align 3
R_ v104,a92
R_ v105,a93
R_ v106,a94
R_ v107,a95
R_ v108,a112
R_ v109,a113
R_ v110,a114
R_ v111,a115
v_pk_mul_f32 v[104:105], v[104:105], v[48:49] op_sel:[0,1]
v_pk_mul_f32 v[106:107], v[106:107], v[48:49] op_sel:[0,1]
v_pk_mul_f32 v[108:109], v[108:109], v[48:49] op_sel:[0,1]
v_pk_mul_f32 v[110:111], v[110:111], v[48:49] op_sel:[0,1]
C_ 188,104,105
C_ 189,106,107
C_ 190,108,109
C_ 191,110,111
.p2align 3
R_ v96,a96
R_ v97,a97
R_ v98,a98
R_ v99,a99
R_ v100,a116
R_ v101,a117
R_ v102,a118
R_ v103,a119
v_pk_mul_f32 v[96:97], v[96:97], v[50:51] op_sel_hi:[1,0]
v_pk_mul_f32 v[98:99], v[98:99], v[50:51] op_sel_hi:[1,0]
v_pk_mul_f32 v[100:101], v[100:101], v[50:51] op_sel_hi:[1,0]
v_pk_mul_f32 v[102:103], v[102:103], v[50:51] op_sel_hi:[1,0]
C_ 192,96,97
C_ 193,98,99
C_ 194,100,101
C_ 195,102,103
.p2align 3
R_ v104,a120
R_ v105,a121
R_ v106,a122
R_ v107,a123
R_ v108,a140
R_ v109,a141
R_ v110,a142
R_ v111,a143
v_pk_mul_f32 v[104:105], v[104:105], v[46:47] op_sel_hi:[1,0]
v_pk_mul_f32 v[106:107], v[106:107], v[46:47] op_sel_hi:[1,0]
v_pk_mul_f32 v[108:109], v[108:109], v[46:47] op_sel_hi:[1,0]
v_pk_mul_f32 v[110:111], v[110:111], v[46:47] op_sel_hi:[1,0]
C_ 196,104,105
C_ 197,106,107
C_ 198,108,109
C_ 199,110,111
X_ a120,v16
X_ a121,v17
X_ a122,v18
X_ a123,v19
.p2align 3
R_ v96,a124
R_ v97,a125
R_ v98,a126
R_ v99,a127
R_ v100,a144
R_ v101,a145
R_ v102,a146
R_ v103,a147
v_pk_mul_f32 v[96:97], v[96:97], v[46:47] op_sel:[0,1]
v_pk_mul_f32 v[98:99], v[98:99], v[46:47] op_sel:[0,1]
v_pk_mul_f32 v[100:101], v[100:101], v[46:47] op_sel:[0,1]
v_pk_mul_f32 v[102:103], v[102:103], v[46:47] op_sel:[0,1]
C_ 200,96,97
C_ 201,98,99
C_ 202,100,101
C_ 203,102,103
X_ a124,v28
X_ a125,v29
X_ a126,v51
X_ a127,v6
.p2align 3
R_ v104,a128
R_ v105,a129
R_ v106,a130
R_ v107,a131
R_ v108,a148
R_ v109,a149
R_ v110,a150
R_ v111,a151
v_pk_mul_f32 v[104:105], v[104:105], v[48:49] op_sel_hi:[1,0]
v_pk_mul_f32 v[106:107], v[106:107], v[48:49] op_sel_hi:[1,0]
v_pk_mul_f32 v[108:109], v[108:109], v[48:49] op_sel_hi:[1,0]
v_pk_mul_f32 v[110:111], v[110:111], v[48:49] op_sel_hi:[1,0]
C_ 204,104,105
C_ 205,106,107
C_ 206,108,109
C_ 207,110,111
X_ a128,v14
X_ a129,v15
v_accvgpr_mov_b32 a130, a249
v_accvgpr_mov_b32 a131, a250
.p2align 3
R_ v96,a132
R_ v97,a133
R_ v98,a134
R_ v99,a135
R_ v100,a152
R_ v101,a153
R_ v102,a154
R_ v103,a155
v_pk_mul_f32 v[96:97], v[96:97], v[48:49] op_sel:[0,1]
v_pk_mul_f32 v[98:99], v[98:99], v[48:49] op_sel:[0,1]
v_pk_mul_f32 v[100:101], v[100:101], v[48:49] op_sel:[0,1]
v_pk_mul_f32 v[102:103], v[102:103], v[48:49] op_sel:[0,1]
C_ 208,96,97
C_ 209,98,99
C_ 210,100,101
C_ 211,102,103
Da 132,v12,0
.p2align 3
R_ v104,a136
R_ v105,a137
R_ v106,a138
R_ v107,a139
R_ v108,a156
R_ v109,a157
R_ v110,a158
R_ v111,a159
v_pk_mul_f32 v[104:105], v[104:105], v[50:51] op_sel_hi:[1,0]
v_pk_mul_f32 v[106:107], v[106:107], v[50:51] op_sel_hi:[1,0]
v_pk_mul_f32 v[108:109], v[108:109], v[50:51] op_sel_hi:[1,0]
v_pk_mul_f32 v[110:111], v[110:111], v[50:51] op_sel_hi:[1,0]
C_ 212,104,105
C_ 213,106,107
C_ 214,108,109
C_ 215,110,111
Da 136,v12,1024
.p2align 3
R_ v96,a160
R_ v97,a161
R_ v98,a162
R_ v99,a163
R_ v100,a180
R_ v101,a181
R_ v102,a182
R_ v103,a183
v_pk_mul_f32 v[96:97], v[96:97], v[46:47] op_sel_hi:[1,0]
v_pk_mul_f32 v[98:99], v[98:99], v[46:47] op_sel_hi:[1,0]
v_pk_mul_f32 v[100:101], v[100:101], v[46:47] op_sel_hi:[1,0]
v_pk_mul_f32 v[102:103], v[102:103], v[46:47] op_sel_hi:[1,0]
C_ 216,96,97
C_ 217,98,99
C_ 218,100,101
C_ 219,102,103
.Lpad_o_0:
s_cbranch_execz .Lpad_o_4
Da 160,v12,2048
.p2align 3
R_ v104,a164
R_ v105,a165
R_ v106,a166
R_ v107,a167
R_ v108,a184
R_ v109,a185
R_ v110,a186
R_ v111,a187
v_pk_mul_f32 v[104:105], v[104:105], v[46:47] op_sel:[0,1]
v_pk_mul_f32 v[106:107], v[106:107], v[46:47] op_sel:[0,1]
v_pk_mul_f32 v[108:109], v[108:109], v[46:47] op_sel:[0,1]
v_pk_mul_f32 v[110:111], v[110:111], v[46:47] op_sel:[0,1]
C_ 220,104,105
C_ 221,106,107
C_ 222,108,109
C_ 223,110,111
Da 164,v12,3072
.p2align 3
R_ v96,a168
R_ v97,a169
R_ v98,a170
R_ v99,a171
R_ v100,a188
R_ v101,a189
R_ v102,a190
R_ v103,a191
v_pk_mul_f32 v[96:97], v[96:97], v[48:49] op_sel_hi:[1,0]
v_pk_mul_f32 v[98:99], v[98:99], v[48:49] op_sel_hi:[1,0]
v_pk_mul_f32 v[100:101], v[100:101], v[48:49] op_sel_hi:[1,0]
v_pk_mul_f32 v[102:103], v[102:103], v[48:49] op_sel_hi:[1,0]
C_ 224,96,97
C_ 225,98,99
C_ 226,100,101
C_ 227,102,103
Da 168,v12,4096
.p2align 3
R_ v104,a172
R_ v105,a173
R_ v106,a174
R_ v107,a175
R_ v108,a192
R_ v109,a193
R_ v110,a194
R_ v111,a195
v_pk_mul_f32 v[104:105], v[104:105], v[48:49] op_sel:[0,1]
v_pk_mul_f32 v[106:107], v[106:107], v[48:49] op_sel:[0,1]
v_pk_mul_f32 v[108:109], v[108:109], v[48:49] op_sel:[0,1]
v_pk_mul_f32 v[110:111], v[110:111], v[48:49] op_sel:[0,1]
C_ 228,104,105
C_ 229,106,107
C_ 230,108,109
C_ 231,110,111
Da 172,v12,5120
.p2align 3
R_ v96,a176
R_ v97,a177
R_ v98,a178
R_ v99,a179
R_ v100,a196
R_ v101,a197
R_ v102,a198
R_ v103,a199
v_pk_mul_f32 v[96:97], v[96:97], v[50:51] op_sel_hi:[1,0]
v_pk_mul_f32 v[98:99], v[98:99], v[50:51] op_sel_hi:[1,0]
v_pk_mul_f32 v[100:101], v[100:101], v[50:51] op_sel_hi:[1,0]
v_pk_mul_f32 v[102:103], v[102:103], v[50:51] op_sel_hi:[1,0]
C_ 232,96,97
C_ 233,98,99
C_ 234,100,101
C_ 235,102,103
Da 176,v12,12288
.p2align 3
R_ v104,a200
R_ v105,a201
R_ v106,a202
R_ v107,a203
R_ v108,a220
R_ v109,a221
R_ v110,a222
R_ v111,a223
v_pk_mul_f32 v[104:105], v[104:105], v[46:47] op_sel_hi:[1,0]
v_pk_mul_f32 v[106:107], v[106:107], v[46:47] op_sel_hi:[1,0]
v_pk_mul_f32 v[108:109], v[108:109], v[46:47] op_sel_hi:[1,0]
v_pk_mul_f32 v[110:111], v[110:111], v[46:47] op_sel_hi:[1,0]
C_ 236,104,105
C_ 237,106,107
C_ 238,108,109
C_ 239,110,111
Da 200,v12,13312
.p2align 3
R_ v96,a204
R_ v97,a205
R_ v98,a206
R_ v99,a207
R_ v100,a224
R_ v101,a225
R_ v102,a226
R_ v103,a227
v_pk_mul_f32 v[96:97], v[96:97], v[46:47] op_sel:[0,1]
v_pk_mul_f32 v[98:99], v[98:99], v[46:47] op_sel:[0,1]
v_pk_mul_f32 v[100:101], v[100:101], v[46:47] op_sel:[0,1]
v_pk_mul_f32 v[102:103], v[102:103], v[46:47] op_sel:[0,1]
C_ 240,96,97
C_ 241,98,99
C_ 242,100,101
C_ 243,102,103
Da 204,v12,14336
v_mov_b32 v6, v10
v_lshrrev_b32 v10, 9, v6
v_lshlrev_b32 v10, 11, v10
v_sub_u32 %0, v2, v10
v_xor_b32 %0, s83, %0
s_waitcnt vmcnt(15) lgkmcnt(0)
s_barrier
Ta 180,%0,24576
Ta 182,%0,26624
Ta 184,%0,28672
Ta 186,%0,30720
Ta 188,%0,32768
Ta 190,%0,34816
.p2align 3
R_ v104,a208
R_ v105,a209
R_ v106,a210
R_ v107,a211
R_ v108,a228
R_ v109,a229
R_ v110,a230
R_ v111,a231
v_pk_mul_f32 v[104:105], v[104:105], v[48:49] op_sel_hi:[1,0]
v_pk_mul_f32 v[106:107], v[106:107], v[48:49] op_sel_hi:[1,0]
v_pk_mul_f32 v[108:109], v[108:109], v[48:49] op_sel_hi:[1,0]
v_pk_mul_f32 v[110:111], v[110:111], v[48:49] op_sel_hi:[1,0]
C_ 244,104,105
C_ 245,106,107
C_ 246,108,109
C_ 247,110,111
Da 208,v12,15360
.p2align 3
R_ v96,a212
R_ v97,a213
R_ v98,a214
R_ v99,a215
R_ v100,a232
R_ v101,a233
R_ v102,a234
R_ v103,a235
v_pk_mul_f32 v[96:97], v[96:97], v[48:49] op_sel:[0,1]
v_pk_mul_f32 v[98:99], v[98:99], v[48:49] op_sel:[0,1]
v_pk_mul_f32 v[100:101], v[100:101], v[48:49] op_sel:[0,1]
v_pk_mul_f32 v[102:103], v[102:103], v[48:49] op_sel:[0,1]
C_ 248,96,97
C_ 249,98,99
C_ 250,100,101
C_ 251,102,103
Da 212,v12,16384
.p2align 3
R_ v104,a216
R_ v105,a217
R_ v106,a218
R_ v107,a219
R_ v108,a236
R_ v109,a237
R_ v110,a238
R_ v111,a239
v_pk_mul_f32 v[104:105], v[104:105], v[50:51] op_sel_hi:[1,0]
v_pk_mul_f32 v[106:107], v[106:107], v[50:51] op_sel_hi:[1,0]
v_pk_mul_f32 v[108:109], v[108:109], v[50:51] op_sel_hi:[1,0]
v_pk_mul_f32 v[110:111], v[110:111], v[50:51] op_sel_hi:[1,0]
C_ 252,104,105
C_ 253,106,107
C_ 254,108,109
C_ 255,110,111
Da 216,v12,17408
v_xor_b32 v1, 32, %0
W_ 7
Zvav 56,180,136
s_mul_i32 s76, s6, 204800
Ta 192,v1,24576
Ta 194,v1,26624
W_ 7
Mvav 56,184,156
v_xor_b32 v2, 64, %0
v_xor_b32 v3, 96, %0
W_ 5
Mvav 56,188,176
s_nop 0
Ta 196,v1,28672
Ta 198,v1,30720
Zvav 60,188,180
W_ 4
R_ v4,a248
v_mul_u32_u24 v4, 80, v4
Mvav 60,184,160
S_ s55,s71,122880
Ta 220,v1,32768
Ta 222,v1,34816
Mvav 60,180,140
S_ m0,s51,49152
v_min_u32 v4, 29440, v4
s_waitcnt vmcnt(6)
Zvav 64,180,144
v_readfirstlane_b32 s62, v11
v_lshrrev_b32 v8, 2, v6
Mvav 64,184,164
v_add_u32 v5, s75, v8
R_ v9,a251
Mvav 64,188,184
v_or_b32 v9, s73, v9
v_lshrrev_b32 v10, 8, v6
Zvav 68,188,188
v_bfe_u32 v12, v6, 6, 2
v_cmp_eq_u32 vcc, v10, v12
Mvav 68,184,168
v_and_b32 v10, 16, v6
v_mov_b32 v12, 0x3f80
Mvav 68,180,148
v_lshlrev_b32 v12, v10, v12
v_cndmask_b32 v12, 0, v12, vcc
Zvav 72,180,152
v_and_b32 v10, 32, v6
v_cmp_eq_u32 vcc, 0, v10
Mvav 72,184,172
X_ a68,0
X_ a69,0
Mvav 72,188,192
W_ 0
X_ a72,0
X_ a73,0
Zvav 92,220,192
s_nop 0
Ta 180,v2,24576
Ta 182,v2,26624
Mvav 92,196,172
v_cndmask_b32 v10, 0, v12, vcc
v_sub_u32 v12, v12, v10
Mvav 92,192,152
s_nop 0
Ta 184,v2,28672
Ta 186,v2,30720
Zvav 88,192,148
X_ a70,v10
X_ a71,v12
Mvav 88,196,168
s_mov_b64 s[32:33], s[20:21]
Ta 188,v2,32768
Ta 190,v2,34816
Mvav 88,220,188
s_mov_b32 s34, 409600
S_ s85,s6,1
Zvav 84,220,184
L_ v7,24,s55,0
Mvav 84,196,164
S_ s55,s71,133120
s_and_b32 s85, s85, 7
Mvav 84,192,144
S_ m0,s51,50176
s_lshl_b32 s85, s85, 7
Zvav 80,192,140
S_ s85,s85,s87
Mvav 80,196,160
L_ v7,24,s55,0
Mvav 80,220,180
S_ s55,s71,143360
Zvav 76,220,176
S_ m0,s51,51200
Mvav 76,196,156
Mvav 76,192,136
W_ 0
Zvav 16,180,136
s_nop 0
Ta 192,v3,24576
Ta 194,v3,26624
Mvav 16,184,156
L_ v7,24,s55,0
Mvav 16,188,176
S_ s55,s71,153600
Ta 196,v3,28672
Ta 198,v3,30720
Zvav 20,188,180
S_ m0,s51,52224
Mvav 20,184,160
s_nop 0
Ta 220,v3,32768
Ta 222,v3,34816
Mvav 20,180,140
L_ v7,24,s55,0
Zvav 24,180,144
S_ s55,s71,163840
Mvav 24,184,164
S_ m0,s51,53248
Mvav 24,188,184
Zvav 28,188,188
L_ v7,24,s55,0
Mvav 28,184,168
S_ s55,s71,174080
Mvav 28,180,148
S_ m0,s51,54272
Zvav 32,180,152
Mvav 32,184,172
L_ v7,24,s55,0
Mvav 32,188,192
s_waitcnt vmcnt(13) lgkmcnt(0)
s_barrier
S_ s71,s72,1152
Zvav 52,220,192
S_ s55,s71,491520
Ta 180,%0,0
Ta 182,%0,2048
Mvav 52,196,172
S_ m0,s51,36864
Mvav 52,192,152
s_nop 0
Ta 184,%0,4096
Ta 186,%0,6144
Zvav 48,192,148
L_ v7,24,s55,0
Mvav 48,196,168
S_ s55,s71,501760
Ta 188,%0,8192
Ta 190,%0,10240
Mvav 48,220,188
S_ m0,s51,37888
Zvav 44,220,184
Mvav 44,196,164
L_ v7,24,s55,0
Mvav 44,192,144
S_ s55,s71,512000
Zvav 40,192,140
S_ m0,s51,38912
Mvav 40,196,160
Mvav 40,220,180
L_ v7,24,s55,0
Zvav 36,220,176
S_ s55,s71,522240
Mvav 36,196,156
S_ m0,s51,39936
Mvav 36,192,136
W_ 0
Mvav 56,180,196
s_nop 0
Ta 192,v1,0
Ta 194,v1,2048
Mvav 56,184,216
L_ v7,24,s55,0
Mvav 56,188,236
S_ s55,s71,532480
Ta 196,v1,4096
Ta 198,v1,6144
Mvav 60,188,240
S_ m0,s51,40960
Mvav 60,184,220
s_nop 0
Ta 220,v1,8192
Ta 222,v1,10240
Mvav 60,180,200
L_ v7,24,s55,0
Mvav 64,180,204
S_ s55,s71,542720
Mvav 64,184,224
S_ m0,s51,41984
Mvav 64,188,244
Mvav 68,188,248
L_ v7,24,s55,0
Mvav 68,184,228
S_ s55,s71,614400
Mvav 68,180,208
S_ m0,s51,24576
Mvav 72,180,212
Mvav 72,184,232
L_ v7,24,s55,0
Mvav 72,188,252
W_ 0
S_ s55,s71,624640
Mvav 92,220,252
S_ m0,s51,25600
Ta 180,v2,0
Ta 182,v2,2048
Mvav 92,196,232
Mvav 92,192,212
s_nop 0
Ta 184,v2,4096
Ta 186,v2,6144
Mvav 88,192,208
L_ v7,24,s55,0
Mvav 88,196,228
S_ s55,s71,634880
Ta 188,v2,8192
Ta 190,v2,10240
Mvav 88,220,248
S_ m0,s51,26624
Mvav 84,220,244
Mvav 84,196,224
L_ v7,24,s55,0
Mvav 84,192,204
S_ s55,s71,645120
Mvav 80,192,200
S_ m0,s51,27648
Mvav 80,196,220
Mvav 80,220,240
L_ v7,24,s55,0
Mvav 76,220,236
S_ s55,s71,655360
Mvav 76,196,216
S_ m0,s51,28672
Mvav 76,192,196
W_ 0
Mvav 16,180,196
s_nop 0
Ta 192,v3,0
Ta 194,v3,2048
Mvav 16,184,216
L_ v7,24,s55,0
Mvav 16,188,236
S_ s55,s71,665600
Ta 196,v3,4096
Ta 198,v3,6144
Mvav 20,188,240
S_ m0,s51,29696
Mvav 20,184,220
s_nop 0
Ta 220,v3,8192
Ta 222,v3,10240
Mvav 20,180,200
L_ v7,24,s55,0
Mvav 24,180,204
Mvav 24,184,224
Mvav 24,188,244
Mvav 28,188,248
buffer_load_dword a74, v4, s[24:27], s85 offen
Mvav 28,184,228
S_ s85,s6,2
Mvav 28,180,208
s_and_b32 s85, s85, 7
Mvav 32,180,212
s_lshl_b32 s85, s85, 7
Mvav 32,184,232
S_ s85,s85,s87
Mvav 32,188,252
s_waitcnt vmcnt(19) lgkmcnt(0)
s_barrier
Mvav 52,220,252
S_ s55,s71,0
Ta 180,%0,12288
Ta 182,%0,14336
Mvav 52,196,232
buffer_load_dword a74, v4, s[24:27], s85 offen
Mvav 52,192,212
S_ m0,s51,0
Ta 184,%0,16384
Ta 186,%0,18432
Mvav 48,192,208
Mvav 48,196,228
s_nop 0
Ta 188,%0,20480
Ta 190,%0,22528
Mvav 48,220,248
L_ v7,24,s55,0
Mvav 44,220,244
S_ s55,s71,10240
Mvav 44,196,224
S_ m0,s51,1024
Mvav 44,192,204
Mvav 40,192,200
L_ v7,24,s55,0
Mvav 40,196,220
S_ s55,s71,20480
Mvav 40,220,240
S_ m0,s51,2048
Mvav 36,220,236
Mvav 36,196,216
L_ v7,24,s55,0
Mvav 36,192,196
W_ 0
S_ s55,s71,30720
Mvaa 56,180,0
S_ m0,s51,3072
Ta 192,v1,12288
Ta 194,v1,14336
Mvaa 56,184,40
Mvaa 56,188,80
s_nop 0
Ta 196,v1,16384
Ta 198,v1,18432
Mvaa 60,188,84
L_ v7,24,s55,0
Mvaa 60,184,44
S_ s55,s71,40960
Ta 220,v1,20480
Ta 222,v1,22528
Mvaa 60,180,4
S_ m0,s51,4096
Mvaa 64,180,8
Mvaa 64,184,48
L_ v7,24,s55,0
Mvaa 64,188,88
S_ s55,s71,51200
Mvaa 68,188,240
S_ m0,s51,5120
Mvaa 68,184,52
Mvaa 68,180,12
L_ v7,24,s55,0
Mvaa 72,180,16
Mvaa 72,184,56
Mvaa 72,188,244
W_ 0
Mvaa 92,220,244
s_nop 0
Ta 180,v2,12288
Ta 182,v2,14336
Mvaa 92,196,56
Mvaa 92,192,16
s_nop 0
Ta 184,v2,16384
Ta 186,v2,18432
Mvaa 88,192,12
Mvaa 88,196,52
s_nop 0
Ta 188,v2,20480
Ta 190,v2,22528
Mvaa 88,220,240
Mvaa 84,220,88
Mvaa 84,196,48
Mvaa 84,192,8
Mvaa 80,192,4
Mvaa 80,196,44
Mvaa 80,220,84
Mvaa 76,220,80
Mvaa 76,196,40
Mvaa 76,192,0
W_ 0
Mvaa 16,180,0
s_nop 0
Ta 192,v3,12288
Ta 194,v3,14336
Mvaa 16,184,40
Mvaa 16,188,80
s_nop 0
Ta 196,v3,16384
Ta 198,v3,18432
Mvaa 20,188,84
Mvaa 20,184,44
s_nop 0
Ta 220,v3,20480
Ta 222,v3,22528
Mvaa 20,180,4
.Lpad_o_1:
s_cbranch_execz .Lpad_o_5
Mvaa 24,180,8
Mvaa 24,184,48
Mvaa 24,188,88
Mvaa 28,188,240
Mvaa 28,184,52
Mvaa 28,180,12
Mvaa 32,180,16
Mvaa 32,184,56
Mvaa 32,188,244
s_waitcnt vmcnt(20) lgkmcnt(0)
s_barrier
Mvaa 52,220,244
s_nop 0
Ta 180,%0,49152
Ta 182,%0,51200
Mvaa 52,196,56
Mvaa 52,192,16
s_nop 0
Ta 184,%0,53248
Ta 186,%0,55296
Mvaa 48,192,12
Mvaa 48,196,52
s_nop 0
Ta 188,%0,57344
Ta 190,%0,59392
Mvaa 48,220,240
Mvaa 44,220,88
Mvaa 44,196,48
Mvaa 44,192,8
Mvaa 40,192,4
Mvaa 40,196,44
Mvaa 40,220,84
Mvaa 36,220,80
Mvaa 36,196,40
Mvaa 36,192,0
W_ 0
Mvaa 56,180,120
S_ s73,s78,163840
Ta 192,v1,49152
Ta 194,v1,51200
Mvaa 56,184,160
S_ s73,s73,4096
Mvaa 56,188,200
s_nop 0
Ta 196,v1,53248
Ta 198,v1,55296
Mvaa 60,188,204
s_mov_b32 m0, s52
Mvaa 60,184,164
s_nop 0
Ta 220,v1,57344
Ta 222,v1,59392
Mvaa 60,180,124
s_nop 0
Y_ 56,0
Y_ 57,256
Mvaa 64,180,128
s_nop 0
Y_ 58,512
Y_ 59,768
Mvaa 64,184,168
Mvaa 64,188,208
s_nop 0
Y_ 60,1024
Y_ 61,1280
Mvaa 68,188,212
s_nop 0
Y_ 62,1536
Y_ 63,1792
Mvaa 68,184,172
Mvaa 68,180,132
s_nop 0
Y_ 64,2048
Y_ 65,2304
Mvaa 72,180,136
s_nop 0
Y_ 66,2560
Y_ 67,2816
Mvaa 72,184,176
Mvaa 72,188,216
W_ 12
Y_ 68,3072
Y_ 69,3328
Mvaa 92,220,216
s_nop 0
Ta 180,v2,49152
Ta 182,v2,51200
Mvaa 92,196,176
s_nop 0
Y_ 70,3584
Y_ 71,3840
Mvaa 92,192,136
s_nop 0
Ta 184,v2,53248
Ta 186,v2,55296
Mvaa 88,192,132
s_nop 0
Y_ 72,4096
Y_ 73,4352
Mvaa 88,196,172
s_nop 0
Ta 188,v2,57344
Ta 190,v2,59392
Mvaa 88,220,212
s_nop 0
Y_ 74,4608
Y_ 75,4864
Mvaa 84,220,208
s_nop 0
Y_ 92,9216
Y_ 93,9472
Mvaa 84,196,168
s_nop 0
Y_ 94,9728
Y_ 95,9984
Mvaa 84,192,128
s_nop 0
Y_ 88,8192
Y_ 89,8448
Mvaa 80,192,124
s_nop 0
Y_ 90,8704
Y_ 91,8960
Mvaa 80,196,164
Mvaa 80,220,204
s_nop 0
Y_ 84,7168
Y_ 85,7424
Mvaa 76,220,200
s_nop 0
Y_ 86,7680
Y_ 87,7936
Mvaa 76,196,160
Mvaa 76,192,120
W_ 14
Y_ 80,6144
Y_ 81,6400
Mvaa 16,180,120
s_nop 0
Ta 192,v3,49152
Ta 194,v3,51200
Mvaa 16,184,160
s_nop 0
Y_ 82,6656
Y_ 83,6912
Mvaa 16,188,200
s_nop 0
Ta 196,v3,53248
Ta 198,v3,55296
Mvaa 20,188,204
s_nop 0
Y_ 76,5120
Y_ 77,5376
Mvaa 20,184,164
s_nop 0
Ta 220,v3,57344
Ta 222,v3,59392
Mvaa 20,180,124
s_nop 0
Y_ 78,5632
Y_ 79,5888
Mvaa 24,180,128
Mvaa 24,184,168
Mvaa 24,188,208
s_waitcnt vmcnt(14) lgkmcnt(0)
s_barrier
Mvaa 28,188,212
S_ s55,s71,122880
ds_read2st64_b32 v[96:97], v5 offset0:0 offset1:1
Mvaa 28,184,172
S_ m0,s51,12288
ds_read2st64_b32 v[98:99], v5 offset0:2 offset1:3
Mvaa 28,180,132
L_ v7,24,s55,0
Mvaa 32,180,136
S_ s55,s71,133120
ds_read2st64_b32 v[104:105], v5 offset0:4 offset1:5
Mvaa 32,184,176
S_ m0,s51,13312
ds_read2st64_b32 v[106:107], v5 offset0:6 offset1:7
Mvaa 32,188,216
ds_read2st64_b32 v[112:113], v5 offset0:8 offset1:9
Mvaa 52,220,216
s_nop 0
Ta 180,%0,36864
Ta 182,%0,38912
Mvaa 52,196,176
L_ v7,24,s55,0
Mvaa 52,192,136
W_ 5
Ta 184,%0,40960
Ta 186,%0,43008
Mvaa 48,192,132
S_ s55,s71,143360
ds_read2st64_b32 v[114:115], v5 offset0:10 offset1:11
A_ v96,v16,v96
Mvaa 48,196,172
S_ m0,s51,14336
Ta 188,%0,45056
Ta 190,%0,47104
Mvaa 48,220,212
L_ v7,24,s55,0
Mvaa 44,220,208
S_ s55,s71,153600
ds_read2st64_b32 v[120:121], v5 offset0:12 offset1:13
A_ v97,v17,v97
Mvaa 44,196,168
S_ m0,s51,15360
ds_read2st64_b32 v[122:123], v5 offset0:14 offset1:15
A_ v98,v18,v98
Mvaa 44,192,128
ds_read2st64_b32 v[128:129], v5 offset0:16 offset1:17
A_ v99,v19,v99
Mvaa 40,192,124
L_ v7,24,s55,0
Mvaa 40,196,164
W_ 11
ds_read2st64_b32 v[130:131], v5 offset0:18 offset1:19
A_ v104,v20,v104
Mvaa 40,220,204
S_ s55,s71,163840
ds_read2st64_b32 v[132:133], v5 offset0:36 offset1:37
A_ v105,v21,v105
Mvaa 36,220,200
S_ m0,s51,16384
ds_read2st64_b32 v[134:135], v5 offset0:38 offset1:39
A_ v106,v22,v106
Mvaa 36,196,160
L_ v7,24,s55,0
Mvaa 36,192,120
W_ 6
ds_read2st64_b32 v[124:125], v5 offset0:32 offset1:33
A_ v107,v23,v107
Zvav 56,180,136
Ta 192,v1,36864
Ta 194,v1,38912
Mvav 56,184,156
S_ s55,s71,174080
ds_read2st64_b32 v[126:127], v5 offset0:34 offset1:35
A_ v112,v24,v112
Mvav 56,188,176
S_ m0,s51,17408
Ta 196,v1,40960
Ta 198,v1,43008
Zvav 60,188,180
L_ v7,24,s55,0
Mvav 60,184,160
S_ s79,s6,1
Ta 220,v1,45056
Ta 222,v1,47104
Mvav 60,180,140
s_and_b32 s79, s79, 7
ds_read2st64_b32 v[116:117], v5 offset0:28 offset1:29
A_ v113,v25,v113
Zvav 64,180,144
s_lshl_b32 s79, s79, 7
ds_read2st64_b32 v[118:119], v5 offset0:30 offset1:31
A_ v114,v26,v114
Mvav 64,184,164
S_ s71,s72,s79
ds_read2st64_b32 v[108:109], v5 offset0:24 offset1:25
A_ v115,v27,v115
Mvav 64,188,184
W_ 14
ds_read2st64_b32 v[110:111], v5 offset0:26 offset1:27
A_ v120,v28,v120
Zvav 68,188,188
S_ s55,s71,491520
ds_read2st64_b32 v[100:101], v5 offset0:20 offset1:21
A_ v121,v29,v121
Mvav 68,184,168
S_ m0,s51,49152
ds_read2st64_b32 v[102:103], v5 offset0:22 offset1:23
A_ v122,v30,v122
Mvav 68,180,148
L_ v7,24,s55,0
Zvav 72,180,152
S_ s55,s71,501760
A_ v123,v31,v123
Mvav 72,184,172
W_ 14
A_ v128,v32,v128
A_ v129,v33,v129
Mvav 72,188,192
W_ 6
A_ v130,v34,v130
A_ v131,v35,v131
Zvav 92,220,192
Ta 180,v2,36864
Ta 182,v2,38912
Mvav 92,196,172
S_ m0,s51,50176
A_ v132,v52,v132
A_ v133,v53,v133
Mvav 92,192,152
s_nop 0
Ta 184,v2,40960
Ta 186,v2,43008
Zvav 88,192,148
L_ v7,24,s55,0
Mvav 88,196,168
S_ s55,s71,512000
Ta 188,v2,45056
Ta 190,v2,47104
Mvav 88,220,188
S_ m0,s51,51200
A_ v134,v54,v134
A_ v135,v55,v135
Zvav 84,220,184
A_ v124,v48,v124
A_ v125,v49,v125
Mvav 84,196,164
L_ v7,24,s55,0
Mvav 84,192,144
S_ s55,s71,522240
A_ v126,v50,v126
A_ v127,v51,v127
Zvav 80,192,140
W_ 10
A_ v116,v44,v116
A_ v117,v45,v117
Mvav 80,196,160
S_ m0,s51,52224
A_ v118,v46,v118
A_ v119,v47,v119
Mvav 80,220,180
L_ v7,24,s55,0
Zvav 76,220,176
W_ 8
A_ v108,v40,v108
A_ v109,v41,v109
Mvav 76,196,156
S_ s55,s71,532480
A_ v110,v42,v110
A_ v111,v43,v111
Mvav 76,192,136
W_ 0
A_ v100,v36,v100
Zvav 16,180,136
S_ m0,s51,53248
Ta 192,v3,36864
Ta 194,v3,38912
Mvav 16,184,156
L_ v7,24,s55,0
Mvav 16,188,176
S_ s55,s71,542720
Ta 196,v3,40960
Ta 198,v3,43008
Zvav 20,188,180
S_ m0,s51,54272
A_ v101,v37,v101
A_ v102,v38,v102
Mvav 20,184,160
S_ s85,s6,3
Ta 220,v3,45056
Ta 222,v3,47104
Mvav 20,180,140
L_ v7,24,s55,0
Zvav 24,180,144
s_and_b32 s85, s85, 7
A_ v103,v39,v103
C_ 131,130,131
Mvav 24,184,164
s_lshl_b32 s85, s85, 7
C_ 130,128,129
C_ 132,132,133
Mvav 24,188,184
S_ s85,s85,s87
C_ 133,134,135
Zvav 28,188,188
buffer_store_dwordx4 v[130:133], v6, s[28:31], s73 offen offset:0 sc1
Mvav 28,184,168
s_sub_u32 s73, s73, 4096
Mvav 28,180,148
C_ 123,122,123
C_ 122,120,121
Zvav 32,180,152
C_ 124,124,125
C_ 125,126,127
Mvav 32,184,172
Mvav 32,188,192
s_waitcnt vmcnt(21) lgkmcnt(0)
s_barrier
Zvav 52,220,192
S_ s55,s71,614400
Ta 180,%0,24576
Ta 182,%0,26624
Mvav 52,196,172
S_ m0,s51,36864
Mvav 52,192,152
s_nop 0
Ta 184,%0,28672
Ta 186,%0,30720
Zvav 48,192,148
buffer_store_dwordx4 v[122:125], v6, s[28:31], s73 offen offset:3072 sc1
Mvav 48,196,168
s_nop 0
Ta 188,%0,32768
Ta 190,%0,34816
Mvav 48,220,188
C_ 115,114,115
C_ 114,112,113
Zvav 44,220,184
C_ 116,116,117
C_ 117,118,119
Mvav 44,196,164
Mvav 44,192,144
Zvav 40,192,140
Mvav 40,196,160
Mvav 40,220,180
buffer_store_dwordx4 v[114:117], v6, s[28:31], s73 offen offset:2048 sc1
Zvav 36,220,176
C_ 107,106,107
C_ 106,104,105
Mvav 36,196,156
C_ 108,108,109
C_ 109,110,111
Mvav 36,192,136
W_ 0
Mvav 56,180,196
s_nop 0
Ta 192,v1,24576
Ta 194,v1,26624
Mvav 56,184,216
Mvav 56,188,236
s_nop 0
Ta 196,v1,28672
Ta 198,v1,30720
Mvav 60,188,240
Mvav 60,184,220
s_nop 0
Ta 220,v1,32768
Ta 222,v1,34816
Mvav 60,180,200
buffer_store_dwordx4 v[106:109], v6, s[28:31], s73 offen offset:1024 sc1
Mvav 64,180,204
C_ 99,98,99
C_ 98,96,97
Mvav 64,184,224
C_ 100,100,101
C_ 101,102,103
Mvav 64,188,244
Mvav 68,188,248
Mvav 68,184,228
Mvav 68,180,208
Mvav 72,180,212
Mvav 72,184,232
buffer_store_dwordx4 v[98:101], v6, s[28:31], s73 offen offset:0 sc1
Mvav 72,188,252
W_ 0
Mvav 92,220,252
s_nop 0
Ta 180,v2,24576
Ta 182,v2,26624
Mvav 92,196,232
Mvav 92,192,212
s_nop 0
Ta 184,v2,28672
Ta 186,v2,30720
Mvav 88,192,208
Mvav 88,196,228
s_nop 0
Ta 188,v2,32768
Ta 190,v2,34816
Mvav 88,220,248
Mvav 84,220,244
L_ v7,24,s55,0
Mvav 84,196,224
S_ s55,s71,624640
Mvav 84,192,204
S_ m0,s51,37888
Mvav 80,192,200
Mvav 80,196,220
L_ v7,24,s55,0
Mvav 80,220,240
S_ s55,s71,634880
Mvav 76,220,236
S_ m0,s51,38912
Mvav 76,196,216
Mvav 76,192,196
W_ 0
Mvav 16,180,196
s_nop 0
Ta 192,v3,24576
Ta 194,v3,26624
Mvav 16,184,216
L_ v7,24,s55,0
Mvav 16,188,236
S_ s55,s71,645120
Ta 196,v3,28672
Ta 198,v3,30720
Mvav 20,188,240
S_ m0,s51,39936
Mvav 20,184,220
s_nop 0
Ta 220,v3,32768
Ta 222,v3,34816
Mvav 20,180,200
L_ v7,24,s55,0
Mvav 24,180,204
S_ s55,s71,655360
Mvav 24,184,224
S_ m0,s51,40960
Mvav 24,188,244
Mvav 28,188,248
L_ v7,24,s55,0
Mvav 28,184,228
S_ s55,s71,665600
Mvav 28,180,208
S_ m0,s51,41984
Mvav 32,180,212
Mvav 32,184,232
L_ v7,24,s55,0
Mvav 32,188,252
s_waitcnt vmcnt(23) lgkmcnt(0)
s_barrier
Mvav 52,220,252
S_ s55,s71,0
Ta 180,%0,0
Ta 182,%0,2048
Mvav 52,196,232
S_ m0,s51,24576
Mvav 52,192,212
s_nop 0
Ta 184,%0,4096
Ta 186,%0,6144
Mvav 48,192,208
L_ v7,24,s55,0
Mvav 48,196,228
S_ s55,s71,10240
Ta 188,%0,8192
Ta 190,%0,10240
Mvav 48,220,248
S_ m0,s51,25600
Mvav 44,220,244
Mvav 44,196,224
L_ v7,24,s55,0
Mvav 44,192,204
S_ s55,s71,20480
Mvav 40,192,200
S_ m0,s51,26624
Mvav 40,196,220
Mvav 40,220,240
L_ v7,24,s55,0
Mvav 36,220,236
S_ s55,s71,30720
Mvav 36,196,216
S_ m0,s51,27648
Mvav 36,192,196
W_ 0
Mvaa 56,180,0
s_nop 0
Ta 192,v1,0
Ta 194,v1,2048
Mvaa 56,184,40
L_ v7,24,s55,0
Mvaa 56,188,80
S_ s55,s71,40960
Ta 196,v1,4096
Ta 198,v1,6144
Mvaa 60,188,84
S_ m0,s51,28672
Mvaa 60,184,44
s_nop 0
Ta 220,v1,8192
Ta 222,v1,10240
Mvaa 60,180,4
L_ v7,24,s55,0
Mvaa 64,180,8
S_ s55,s71,51200
Mvaa 64,184,48
S_ m0,s51,29696
Mvaa 64,188,88
.Lpad_o_2:
s_cbranch_execz .Lpad_o_6
Mvaa 68,188,240
L_ v7,24,s55,0
Mvaa 68,184,52
Mvaa 68,180,12
Mvaa 72,180,16
Mvaa 72,184,56
buffer_load_dword a74, v4, s[24:27], s85 offen
Mvaa 72,188,244
W_ 0
Mvaa 92,220,244
s_nop 0
Ta 180,v2,0
Ta 182,v2,2048
Mvaa 92,196,56
Mvaa 92,192,16
s_nop 0
Ta 184,v2,4096
Ta 186,v2,6144
Mvaa 88,192,12
Mvaa 88,196,52
s_nop 0
Ta 188,v2,8192
Ta 190,v2,10240
Mvaa 88,220,240
Mvaa 84,220,88
Mvaa 84,196,48
Mvaa 84,192,8
Mvaa 80,192,4
Mvaa 80,196,44
Mvaa 80,220,84
Mvaa 76,220,80
Mvaa 76,196,40
X_ a20,-1
X_ a21,-1
Mvaa 76,192,0
W_ 0
X_ a22,-1
X_ a23,-1
Mvaa 16,180,0
S_ s81,s6,7
Ta 192,v3,0
Ta 194,v3,2048
Mvaa 16,184,40
s_and_b32 s81, s81, 7
Mvaa 16,188,80
s_mul_i32 s81, s81, 20480
Ta 196,v3,4096
Ta 198,v3,6144
Mvaa 20,188,84
S_ s81,s81,s78
Mvaa 20,184,44
s_nop 0
Ta 220,v3,8192
Ta 222,v3,10240
Mvaa 20,180,4
buffer_store_dwordx4 a[20:23], v6, s[28:31], s81 offen offset:0 sc1
Mvaa 24,180,8
Mvaa 24,184,48
Mvaa 24,188,88
Mvaa 28,188,240
buffer_store_dwordx4 a[20:23], v6, s[28:31], s81 offen offset:1024 sc1
Mvaa 28,184,52
Mvaa 28,180,12
Mvaa 32,180,16
Mvaa 32,184,56
Mvaa 32,188,244
s_waitcnt vmcnt(26) lgkmcnt(0)
s_barrier
Mvaa 52,220,244
s_nop 0
Ta 180,%0,12288
Ta 182,%0,14336
Mvaa 52,196,56
buffer_store_dwordx4 a[20:23], v6, s[28:31], s81 offen offset:2048 sc1
Mvaa 52,192,16
s_nop 0
Ta 184,%0,16384
Ta 186,%0,18432
Mvaa 48,192,12
Mvaa 48,196,52
s_nop 0
Ta 188,%0,20480
Ta 190,%0,22528
Mvaa 48,220,240
Mvaa 44,220,88
Mvaa 44,196,48
buffer_store_dwordx4 a[20:23], v6, s[28:31], s81 offen offset:3072 sc1
Mvaa 44,192,8
S_ s81,s81,4096
Mvaa 40,192,4
Mvaa 40,196,44
Mvaa 40,220,84
Mvaa 36,220,80
)A"
R"A(Mvaa 36,196,40
Mvaa 36,192,0
W_ 0
Mvaa 56,180,120
s_nop 0
Ta 192,v1,12288
Ta 194,v1,14336
Mvaa 56,184,160
buffer_store_dwordx4 a[20:23], v6, s[28:31], s81 offen offset:0 sc1
Mvaa 56,188,200
S_ s73,s78,184320
Ta 196,v1,16384
Ta 198,v1,18432
Mvaa 60,188,204
s_mov_b32 m0, s52
S_ s73,s73,4096
Mvaa 60,184,164
s_nop 0
Ta 220,v1,20480
Ta 222,v1,22528
Mvaa 60,180,124
s_nop 0
Y_ 56,0
Y_ 57,256
Mvaa 64,180,128
s_nop 0
Y_ 58,512
Y_ 59,768
Mvaa 64,184,168
Mvaa 64,188,208
s_nop 0
Y_ 60,1024
Y_ 61,1280
Mvaa 68,188,212
s_nop 0
Y_ 62,1536
Y_ 63,1792
Mvaa 68,184,172
Mvaa 68,180,132
s_nop 0
Y_ 64,2048
Y_ 65,2304
Mvaa 72,180,136
s_nop 0
Y_ 66,2560
Y_ 67,2816
Mvaa 72,184,176
Mvaa 72,188,216
W_ 12
Y_ 68,3072
Y_ 69,3328
Mvaa 92,220,216
s_nop 0
Ta 180,v2,12288
Ta 182,v2,14336
Mvaa 92,196,176
s_nop 0
Y_ 70,3584
Y_ 71,3840
Mvaa 92,192,136
s_nop 0
Ta 184,v2,16384
Ta 186,v2,18432
Mvaa 88,192,132
s_nop 0
Y_ 72,4096
Y_ 73,4352
Mvaa 88,196,172
s_nop 0
Ta 188,v2,20480
Ta 190,v2,22528
Mvaa 88,220,212
s_nop 0
Y_ 74,4608
Y_ 75,4864
Mvaa 84,220,208
s_nop 0
Y_ 92,9216
Y_ 93,9472
Mvaa 84,196,168
s_nop 0
Y_ 94,9728
Y_ 95,9984
Mvaa 84,192,128
s_nop 0
Y_ 88,8192
Y_ 89,8448
Mvaa 80,192,124
s_nop 0
Y_ 90,8704
Y_ 91,8960
Mvaa 80,196,164
Mvaa 80,220,204
s_nop 0
Y_ 84,7168
Y_ 85,7424
Mvaa 76,220,200
s_nop 0
Y_ 86,7680
Y_ 87,7936
Mvaa 76,196,160
Mvaa 76,192,120
W_ 14
Y_ 80,6144
Y_ 81,6400
Mvaa 16,180,120
s_nop 0
Ta 192,v3,12288
Ta 194,v3,14336
Mvaa 16,184,160
s_nop 0
Y_ 82,6656
Y_ 83,6912
Mvaa 16,188,200
s_nop 0
Ta 196,v3,16384
Ta 198,v3,18432
Mvaa 20,188,204
s_nop 0
Y_ 76,5120
Y_ 77,5376
Mvaa 20,184,164
s_nop 0
Ta 220,v3,20480
Ta 222,v3,22528
Mvaa 20,180,124
s_nop 0
Y_ 78,5632
Y_ 79,5888
Mvaa 24,180,128
Mvaa 24,184,168
Mvaa 24,188,208
s_waitcnt vmcnt(23) lgkmcnt(0)
s_barrier
Mvaa 28,188,212
S_ s55,s71,122880
ds_read2st64_b32 v[96:97], v5 offset0:0 offset1:1
Mvaa 28,184,172
S_ m0,s51,0
ds_read2st64_b32 v[98:99], v5 offset0:2 offset1:3
Mvaa 28,180,132
L_ v7,24,s55,0
Mvaa 32,180,136
S_ s55,s71,133120
ds_read2st64_b32 v[104:105], v5 offset0:4 offset1:5
Mvaa 32,184,176
S_ m0,s51,1024
ds_read2st64_b32 v[106:107], v5 offset0:6 offset1:7
Mvaa 32,188,216
ds_read2st64_b32 v[112:113], v5 offset0:8 offset1:9
Mvaa 52,220,216
s_nop 0
Ta 180,%0,49152
Ta 182,%0,51200
Mvaa 52,196,176
L_ v7,24,s55,0
Mvaa 52,192,136
W_ 5
Ta 184,%0,53248
Ta 186,%0,55296
Mvaa 48,192,132
S_ s55,s71,143360
ds_read2st64_b32 v[114:115], v5 offset0:10 offset1:11
A_ v96,v16,v96
Mvaa 48,196,172
S_ m0,s51,2048
Ta 188,%0,57344
Ta 190,%0,59392
Mvaa 48,220,212
L_ v7,24,s55,0
Mvaa 44,220,208
S_ s55,s71,153600
ds_read2st64_b32 v[120:121], v5 offset0:12 offset1:13
A_ v97,v17,v97
Mvaa 44,196,168
S_ m0,s51,3072
ds_read2st64_b32 v[122:123], v5 offset0:14 offset1:15
A_ v98,v18,v98
Mvaa 44,192,128
ds_read2st64_b32 v[128:129], v5 offset0:16 offset1:17
A_ v99,v19,v99
Mvaa 40,192,124
L_ v7,24,s55,0
Mvaa 40,196,164
W_ 11
ds_read2st64_b32 v[130:131], v5 offset0:18 offset1:19
A_ v104,v20,v104
Mvaa 40,220,204
S_ s55,s71,163840
ds_read2st64_b32 v[132:133], v5 offset0:36 offset1:37
A_ v105,v21,v105
Mvaa 36,220,200
S_ m0,s51,4096
ds_read2st64_b32 v[134:135], v5 offset0:38 offset1:39
A_ v106,v22,v106
Mvaa 36,196,160
L_ v7,24,s55,0
Mvaa 36,192,120
W_ 6
ds_read2st64_b32 v[124:125], v5 offset0:32 offset1:33
A_ v107,v23,v107
Zvav 56,180,136
Ta 192,v1,49152
Ta 194,v1,51200
Mvav 56,184,156
S_ s55,s71,174080
ds_read2st64_b32 v[126:127], v5 offset0:34 offset1:35
A_ v112,v24,v112
Mvav 56,188,176
S_ m0,s51,5120
Ta 196,v1,53248
Ta 198,v1,55296
Zvav 60,188,180
L_ v7,24,s55,0
Mvav 60,184,160
S_ s79,s6,2
Ta 220,v1,57344
Ta 222,v1,59392
Mvav 60,180,140
s_and_b32 s79, s79, 7
ds_read2st64_b32 v[116:117], v5 offset0:28 offset1:29
A_ v113,v25,v113
Zvav 64,180,144
s_lshl_b32 s79, s79, 7
ds_read2st64_b32 v[118:119], v5 offset0:30 offset1:31
A_ v114,v26,v114
Mvav 64,184,164
S_ s71,s72,s79
ds_read2st64_b32 v[108:109], v5 offset0:24 offset1:25
A_ v115,v27,v115
Mvav 64,188,184
W_ 14
ds_read2st64_b32 v[110:111], v5 offset0:26 offset1:27
A_ v120,v28,v120
Zvav 68,188,188
S_ s55,s71,491520
ds_read2st64_b32 v[100:101], v5 offset0:20 offset1:21
A_ v121,v29,v121
Mvav 68,184,168
S_ m0,s51,12288
ds_read2st64_b32 v[102:103], v5 offset0:22 offset1:23
A_ v122,v30,v122
Mvav 68,180,148
L_ v7,24,s55,0
Zvav 72,180,152
S_ s55,s71,501760
A_ v123,v31,v123
R_ v12,a248
Mvav 72,184,172
W_ 14
A_ v128,v32,v128
A_ v129,v33,v129
Mvav 72,188,192
W_ 6
A_ v130,v34,v130
A_ v131,v35,v131
Zvav 92,220,192
Ta 180,v2,49152
Ta 182,v2,51200
Mvav 92,196,172
S_ m0,s51,13312
A_ v132,v52,v132
A_ v133,v53,v133
Mvav 92,192,152
s_nop 0
Ta 184,v2,53248
Ta 186,v2,55296
Zvav 88,192,148
L_ v7,24,s55,0
Mvav 88,196,168
S_ s55,s71,512000
Ta 188,v2,57344
Ta 190,v2,59392
Mvav 88,220,188
S_ m0,s51,14336
A_ v134,v54,v134
A_ v135,v55,v135
Zvav 84,220,184
A_ v124,v48,v124
A_ v125,v49,v125
Mvav 84,196,164
L_ v7,24,s55,0
Mvav 84,192,144
S_ s55,s71,522240
A_ v126,v50,v126
A_ v127,v51,v127
Zvav 80,192,140
W_ 10
A_ v116,v44,v116
A_ v117,v45,v117
Mvav 80,196,160
S_ m0,s51,15360
A_ v118,v46,v118
A_ v119,v47,v119
Mvav 80,220,180
L_ v7,24,s55,0
Zvav 76,220,176
W_ 8
A_ v108,v40,v108
A_ v109,v41,v109
Mvav 76,196,156
S_ s55,s71,532480
A_ v110,v42,v110
A_ v111,v43,v111
Mvav 76,192,136
W_ 0
A_ v100,v36,v100
Zvav 16,180,136
S_ m0,s51,16384
Ta 192,v3,49152
Ta 194,v3,51200
Mvav 16,184,156
L_ v7,24,s55,0
Mvav 16,188,176
S_ s55,s71,542720
Ta 196,v3,53248
Ta 198,v3,55296
Zvav 20,188,180
S_ m0,s51,17408
A_ v101,v37,v101
A_ v102,v38,v102
Mvav 20,184,160
S_ s85,s6,4
Ta 220,v3,57344
Ta 222,v3,59392
Mvav 20,180,140
L_ v7,24,s55,0
Zvav 24,180,144
s_and_b32 s85, s85, 7
A_ v103,v39,v103
C_ 131,130,131
Mvav 24,184,164
s_lshl_b32 s85, s85, 7
C_ 130,128,129
C_ 132,132,133
Mvav 24,188,184
S_ s85,s85,s87
C_ 133,134,135
v_min_u32 v12, 240, v12
Zvav 28,188,188
buffer_store_dwordx4 v[130:133], v6, s[28:31], s73 offen offset:0 sc1
Mvav 28,184,168
s_sub_u32 s73, s73, 4096
v_lshlrev_b32 v12, 3, v12
Mvav 28,180,148
C_ 123,122,123
C_ 122,120,121
Zvav 32,180,152
C_ 124,124,125
C_ 125,126,127
Mvav 32,184,172
Mvav 32,188,192
s_waitcnt vmcnt(25) lgkmcnt(0)
s_barrier
Zvav 52,220,192
S_ s55,s71,614400
Ta 180,%0,36864
Ta 182,%0,38912
Mvav 52,196,172
S_ m0,s51,49152
Mvav 52,192,152
s_nop 0
Ta 184,%0,40960
Ta 186,%0,43008
Zvav 48,192,148
buffer_store_dwordx4 v[122:125], v6, s[28:31], s73 offen offset:3072 sc1
Mvav 48,196,168
s_nop 0
Ta 188,%0,45056
Ta 190,%0,47104
Mvav 48,220,188
C_ 115,114,115
C_ 114,112,113
Zvav 44,220,184
C_ 116,116,117
C_ 117,118,119
Mvav 44,196,164
Mvav 44,192,144
Zvav 40,192,140
Mvav 40,196,160
Mvav 40,220,180
buffer_store_dwordx4 v[114:117], v6, s[28:31], s73 offen offset:2048 sc1
Zvav 36,220,176
C_ 107,106,107
C_ 106,104,105
Mvav 36,196,156
C_ 108,108,109
C_ 109,110,111
Mvav 36,192,136
W_ 0
Mvav 56,180,196
s_nop 0
Ta 192,v1,36864
Ta 194,v1,38912
Mvav 56,184,216
Mvav 56,188,236
s_nop 0
Ta 196,v1,40960
Ta 198,v1,43008
Mvav 60,188,240
Mvav 60,184,220
s_nop 0
Ta 220,v1,45056
Ta 222,v1,47104
Mvav 60,180,200
buffer_store_dwordx4 v[106:109], v6, s[28:31], s73 offen offset:1024 sc1
Mvav 64,180,204
C_ 99,98,99
C_ 98,96,97
Mvav 64,184,224
C_ 100,100,101
C_ 101,102,103
Mvav 64,188,244
Mvav 68,188,248
Mvav 68,184,228
Mvav 68,180,208
Mvav 72,180,212
Mvav 72,184,232
buffer_store_dwordx4 v[98:101], v6, s[28:31], s73 offen offset:0 sc1
Mvav 72,188,252
W_ 0
Mvav 92,220,252
s_nop 0
Ta 180,v2,36864
Ta 182,v2,38912
Mvav 92,196,232
Mvav 92,192,212
s_nop 0
Ta 184,v2,40960
Ta 186,v2,43008
Mvav 88,192,208
Mvav 88,196,228
s_nop 0
Ta 188,v2,45056
Ta 190,v2,47104
Mvav 88,220,248
Mvav 84,220,244
L_ v7,24,s55,0
Mvav 84,196,224
S_ s55,s71,624640
Mvav 84,192,204
S_ m0,s51,50176
Mvav 80,192,200
Mvav 80,196,220
L_ v7,24,s55,0
Mvav 80,220,240
S_ s55,s71,634880
Mvav 76,220,236
S_ m0,s51,51200
Mvav 76,196,216
Mvav 76,192,196
W_ 0
Mvav 16,180,196
s_nop 0
Ta 192,v3,36864
Ta 194,v3,38912
Mvav 16,184,216
L_ v7,24,s55,0
Mvav 16,188,236
S_ s55,s71,645120
Ta 196,v3,40960
Ta 198,v3,43008
Mvav 20,188,240
S_ m0,s51,52224
Mvav 20,184,220
s_nop 0
Ta 220,v3,45056
Ta 222,v3,47104
Mvav 20,180,200
L_ v7,24,s55,0
Mvav 24,180,204
S_ s55,s71,655360
Mvav 24,184,224
S_ m0,s51,53248
Mvav 24,188,244
Mvav 28,188,248
L_ v7,24,s55,0
Mvav 28,184,228
S_ s55,s71,665600
Mvav 28,180,208
S_ m0,s51,54272
Mvav 32,180,212
.Lpad_o_3:
s_cbranch_execz .Lpad_o_7
Mvav 32,184,232
L_ v7,24,s55,0
Mvav 32,188,252
s_waitcnt vmcnt(29) lgkmcnt(0)
s_barrier
Mvav 52,220,252
S_ s55,s71,0
Ta 180,%0,24576
Ta 182,%0,26624
Mvav 52,196,232
S_ m0,s51,36864
Mvav 52,192,212
s_nop 0
Ta 184,%0,28672
Ta 186,%0,30720
Mvav 48,192,208
L_ v7,24,s55,0
Mvav 48,196,228
S_ s55,s71,10240
Ta 188,%0,32768
Ta 190,%0,34816
Mvav 48,220,248
S_ m0,s51,37888
Mvav 44,220,244
Mvav 44,196,224
L_ v7,24,s55,0
Mvav 44,192,204
S_ s55,s71,20480
Mvav 40,192,200
S_ m0,s51,38912
Mvav 40,196,220
Mvav 40,220,240
L_ v7,24,s55,0
Mvav 36,220,236
S_ s55,s71,30720
Mvav 36,196,216
S_ m0,s51,39936
Mvav 36,192,196
W_ 0
Mvaa 56,180,0
s_nop 0
Ta 192,v1,24576
Ta 194,v1,26624
Mvaa 56,184,40
L_ v7,24,s55,0
Mvaa 56,188,80
S_ s55,s71,40960
Ta 196,v1,28672
Ta 198,v1,30720
Mvaa 60,188,84
S_ m0,s51,40960
Mvaa 60,184,44
s_nop 0
Ta 220,v1,32768
Ta 222,v1,34816
Mvaa 60,180,4
L_ v7,24,s55,0
Mvaa 64,180,8
S_ s55,s71,51200
Mvaa 64,184,48
S_ m0,s51,41984
Mvaa 64,188,88
Mvaa 68,188,240
L_ v7,24,s55,0
Mvaa 68,184,52
Mvaa 68,180,12
Mvaa 72,180,16
Mvaa 72,184,56
buffer_load_dword a74, v12, s[44:47], s66 offen
Mvaa 72,188,244
W_ 0
Mvaa 92,220,244
s_nop 0
Ta 180,v2,24576
Ta 182,v2,26624
Mvaa 92,196,56
Mvaa 92,192,16
s_nop 0
Ta 184,v2,28672
Ta 186,v2,30720
Mvaa 88,192,12
buffer_load_dword a74, v4, s[24:27], s85 offen
Mvaa 88,196,52
s_nop 0
Ta 188,v2,32768
Ta 190,v2,34816
Mvaa 88,220,240
Mvaa 84,220,88
Mvaa 84,196,48
Mvaa 84,192,8
Mvaa 80,192,4
Mvaa 80,196,44
Mvaa 80,220,84
Mvaa 76,220,80
Mvaa 76,196,40
Mvaa 76,192,0
W_ 0
Mvaa 16,180,0
s_nop 0
Ta 192,v3,24576
Ta 194,v3,26624
Mvaa 16,184,40
Mvaa 16,188,80
s_nop 0
Ta 196,v3,28672
Ta 198,v3,30720
Mvaa 20,188,84
Mvaa 20,184,44
s_nop 0
Ta 220,v3,32768
Ta 222,v3,34816
Mvaa 20,180,4
Mvaa 24,180,8
Mvaa 24,184,48
Mvaa 24,188,88
Mvaa 28,188,240
Mvaa 28,184,52
Mvaa 28,180,12
Mvaa 32,180,16
Mvaa 32,184,56
Mvaa 32,188,244
s_waitcnt vmcnt(25) lgkmcnt(0)
s_barrier
Mvaa 52,220,244
s_nop 0
Ta 180,%0,0
Ta 182,%0,2048
Mvaa 52,196,56
Mvaa 52,192,16
s_nop 0
Ta 184,%0,4096
Ta 186,%0,6144
Mvaa 48,192,12
Mvaa 48,196,52
s_nop 0
Ta 188,%0,8192
Ta 190,%0,10240
Mvaa 48,220,240
Mvaa 44,220,88
Mvaa 44,196,48
Mvaa 44,192,8
Mvaa 40,192,4
Mvaa 40,196,44
Mvaa 40,220,84
Mvaa 36,220,80
Mvaa 36,196,40
Mvaa 36,192,0
W_ 0
Mvaa 56,180,120
S_ s81,s6,1
Ta 192,v1,0
Ta 194,v1,2048
Mvaa 56,184,160
s_and_b32 s81, s81, 7
Mvaa 56,188,200
s_mul_i32 s81, s81, 20480
Ta 196,v1,4096
Ta 198,v1,6144
Mvaa 60,188,204
s_mov_b32 m0, s52
S_ s73,s81,s78
Mvaa 60,184,164
S_ s73,s73,4096
Ta 220,v1,8192
Ta 222,v1,10240
Mvaa 60,180,124
s_nop 0
Y_ 56,0
Y_ 57,256
Mvaa 64,180,128
s_nop 0
Y_ 58,512
Y_ 59,768
Mvaa 64,184,168
Mvaa 64,188,208
s_nop 0
Y_ 60,1024
Y_ 61,1280
Mvaa 68,188,212
s_nop 0
Y_ 62,1536
Y_ 63,1792
Mvaa 68,184,172
Mvaa 68,180,132
s_nop 0
Y_ 64,2048
Y_ 65,2304
Mvaa 72,180,136
s_nop 0
Y_ 66,2560
Y_ 67,2816
Mvaa 72,184,176
Mvaa 72,188,216
W_ 12
Y_ 68,3072
Y_ 69,3328
Mvaa 92,220,216
s_nop 0
Ta 180,v2,0
Ta 182,v2,2048
Mvaa 92,196,176
s_nop 0
Y_ 70,3584
Y_ 71,3840
Mvaa 92,192,136
s_nop 0
Ta 184,v2,4096
Ta 186,v2,6144
Mvaa 88,192,132
s_nop 0
Y_ 72,4096
Y_ 73,4352
Mvaa 88,196,172
s_nop 0
Ta 188,v2,8192
Ta 190,v2,10240
Mvaa 88,220,212
s_nop 0
Y_ 74,4608
Y_ 75,4864
Mvaa 84,220,208
s_nop 0
Y_ 92,9216
Y_ 93,9472
Mvaa 84,196,168
s_nop 0
Y_ 94,9728
Y_ 95,9984
Mvaa 84,192,128
s_nop 0
Y_ 88,8192
Y_ 89,8448
Mvaa 80,192,124
s_nop 0
Y_ 90,8704
Y_ 91,8960
Mvaa 80,196,164
Mvaa 80,220,204
s_nop 0
Y_ 84,7168
Y_ 85,7424
Mvaa 76,220,200
s_nop 0
Y_ 86,7680
Y_ 87,7936
Mvaa 76,196,160
Mvaa 76,192,120
W_ 14
Y_ 80,6144
Y_ 81,6400
Mvaa 16,180,120
s_nop 0
Ta 192,v3,0
Ta 194,v3,2048
Mvaa 16,184,160
s_nop 0
Y_ 82,6656
Y_ 83,6912
Mvaa 16,188,200
s_nop 0
Ta 196,v3,4096
Ta 198,v3,6144
Mvaa 20,188,204
s_nop 0
Y_ 76,5120
Y_ 77,5376
Mvaa 20,184,164
s_nop 0
Ta 220,v3,8192
Ta 222,v3,10240
Mvaa 20,180,124
s_nop 0
Y_ 78,5632
Y_ 79,5888
Mvaa 24,180,128
Mvaa 24,184,168
Mvaa 24,188,208
s_waitcnt vmcnt(19) lgkmcnt(0)
s_barrier
Mvaa 28,188,212
S_ s55,s71,122880
ds_read2st64_b32 v[96:97], v5 offset0:0 offset1:1
Mvaa 28,184,172
S_ m0,s51,24576
ds_read2st64_b32 v[98:99], v5 offset0:2 offset1:3
Mvaa 28,180,132
L_ v7,24,s55,0
Mvaa 32,180,136
S_ s55,s71,133120
ds_read2st64_b32 v[104:105], v5 offset0:4 offset1:5
Mvaa 32,184,176
S_ m0,s51,25600
ds_read2st64_b32 v[106:107], v5 offset0:6 offset1:7
Mvaa 32,188,216
ds_read2st64_b32 v[112:113], v5 offset0:8 offset1:9
Mvaa 52,220,216
s_nop 0
Ta 180,%0,12288
Ta 182,%0,14336
Mvaa 52,196,176
L_ v7,24,s55,0
Mvaa 52,192,136
W_ 5
Ta 184,%0,16384
Ta 186,%0,18432
Mvaa 48,192,132
S_ s55,s71,143360
ds_read2st64_b32 v[114:115], v5 offset0:10 offset1:11
A_ v96,v16,v96
Mvaa 48,196,172
S_ m0,s51,26624
Ta 188,%0,20480
Ta 190,%0,22528
Mvaa 48,220,212
L_ v7,24,s55,0
Mvaa 44,220,208
S_ s55,s71,153600
ds_read2st64_b32 v[120:121], v5 offset0:12 offset1:13
A_ v97,v17,v97
Mvaa 44,196,168
S_ m0,s51,27648
ds_read2st64_b32 v[122:123], v5 offset0:14 offset1:15
A_ v98,v18,v98
Mvaa 44,192,128
ds_read2st64_b32 v[128:129], v5 offset0:16 offset1:17
A_ v99,v19,v99
Mvaa 40,192,124
L_ v7,24,s55,0
Mvaa 40,196,164
W_ 11
ds_read2st64_b32 v[130:131], v5 offset0:18 offset1:19
A_ v104,v20,v104
Mvaa 40,220,204
S_ s55,s71,163840
ds_read2st64_b32 v[132:133], v5 offset0:36 offset1:37
A_ v105,v21,v105
Mvaa 36,220,200
S_ m0,s51,28672
ds_read2st64_b32 v[134:135], v5 offset0:38 offset1:39
A_ v106,v22,v106
Mvaa 36,196,160
L_ v7,24,s55,0
Mvaa 36,192,120
W_ 6
ds_read2st64_b32 v[124:125], v5 offset0:32 offset1:33
A_ v107,v23,v107
Zvav 56,180,136
Ta 192,v1,12288
Ta 194,v1,14336
Mvav 56,184,156
S_ s55,s71,174080
ds_read2st64_b32 v[126:127], v5 offset0:34 offset1:35
A_ v112,v24,v112
Mvav 56,188,176
S_ m0,s51,29696
Ta 196,v1,16384
Ta 198,v1,18432
Zvav 60,188,180
L_ v7,24,s55,0
Mvav 60,184,160
S_ s79,s6,3
Ta 220,v1,20480
Ta 222,v1,22528
Mvav 60,180,140
s_and_b32 s79, s79, 7
ds_read2st64_b32 v[116:117], v5 offset0:28 offset1:29
A_ v113,v25,v113
Zvav 64,180,144
s_lshl_b32 s79, s79, 7
ds_read2st64_b32 v[118:119], v5 offset0:30 offset1:31
A_ v114,v26,v114
Mvav 64,184,164
S_ s71,s72,s79
ds_read2st64_b32 v[108:109], v5 offset0:24 offset1:25
A_ v115,v27,v115
Mvav 64,188,184
W_ 14
ds_read2st64_b32 v[110:111], v5 offset0:26 offset1:27
A_ v120,v28,v120
Zvav 68,188,188
S_ s55,s71,491520
ds_read2st64_b32 v[100:101], v5 offset0:20 offset1:21
A_ v121,v29,v121
Mvav 68,184,168
S_ m0,s51,0
ds_read2st64_b32 v[102:103], v5 offset0:22 offset1:23
A_ v122,v30,v122
Mvav 68,180,148
L_ v7,24,s55,0
Zvav 72,180,152
S_ s55,s71,501760
A_ v123,v31,v123
Mvav 72,184,172
W_ 14
A_ v128,v32,v128
A_ v129,v33,v129
Mvav 72,188,192
W_ 6
A_ v130,v34,v130
A_ v131,v35,v131
Zvav 92,220,192
Ta 180,v2,12288
Ta 182,v2,14336
Mvav 92,196,172
S_ m0,s51,1024
A_ v132,v52,v132
A_ v133,v53,v133
Mvav 92,192,152
s_nop 0
Ta 184,v2,16384
Ta 186,v2,18432
Zvav 88,192,148
L_ v7,24,s55,0
Mvav 88,196,168
S_ s55,s71,512000
Ta 188,v2,20480
Ta 190,v2,22528
Mvav 88,220,188
S_ m0,s51,2048
A_ v134,v54,v134
A_ v135,v55,v135
Zvav 84,220,184
A_ v124,v48,v124
A_ v125,v49,v125
Mvav 84,196,164
L_ v7,24,s55,0
Mvav 84,192,144
S_ s55,s71,522240
A_ v126,v50,v126
A_ v127,v51,v127
Zvav 80,192,140
W_ 10
A_ v116,v44,v116
A_ v117,v45,v117
Mvav 80,196,160
S_ m0,s51,3072
A_ v118,v46,v118
A_ v119,v47,v119
Mvav 80,220,180
L_ v7,24,s55,0
Zvav 76,220,176
W_ 8
A_ v108,v40,v108
A_ v109,v41,v109
Mvav 76,196,156
S_ s55,s71,532480
A_ v110,v42,v110
A_ v111,v43,v111
Mvav 76,192,136
W_ 0
A_ v100,v36,v100
Zvav 16,180,136
S_ m0,s51,4096
Ta 192,v3,12288
Ta 194,v3,14336
Mvav 16,184,156
L_ v7,24,s55,0
Mvav 16,188,176
S_ s55,s71,542720
Ta 196,v3,16384
Ta 198,v3,18432
Zvav 20,188,180
S_ m0,s51,5120
A_ v101,v37,v101
A_ v102,v38,v102
Mvav 20,184,160
S_ s85,s6,5
Ta 220,v3,20480
Ta 222,v3,22528
Mvav 20,180,140
L_ v7,24,s55,0
Zvav 24,180,144
s_and_b32 s85, s85, 7
A_ v103,v39,v103
C_ 131,130,131
Mvav 24,184,164
s_lshl_b32 s85, s85, 7
C_ 130,128,129
C_ 132,132,133
Mvav 24,188,184
S_ s85,s85,s87
C_ 133,134,135
Zvav 28,188,188
buffer_store_dwordx4 v[130:133], v6, s[28:31], s73 offen offset:0 sc1
Mvav 28,184,168
s_sub_u32 s73, s73, 4096
Mvav 28,180,148
C_ 123,122,123
C_ 122,120,121
Zvav 32,180,152
C_ 124,124,125
C_ 125,126,127
Mvav 32,184,172
Mvav 32,188,192
s_waitcnt vmcnt(21) lgkmcnt(0)
s_barrier
Zvav 52,220,192
S_ s56,s76,s74
Ta 180,%0,49152
Ta 182,%0,51200
Mvav 52,196,172
S_ s55,s71,614400
Mvav 52,192,152
S_ m0,s51,12288
Ta 184,%0,53248
Ta 186,%0,55296
Zvav 48,192,148
buffer_load_dwordx4 a[60:63], v6, s[28:31], s56 offen offset:0 sc1
Mvav 48,196,168
S_ s56,s76,s82
Ta 188,%0,57344
Ta 190,%0,59392
Mvav 48,220,188
Zvav 44,220,184
Mvav 44,196,164
buffer_load_dwordx4 a[64:67], v6, s[28:31], s56 offen offset:0 sc1
Mvav 44,192,144
Zvav 40,192,140
Mvav 40,196,160
Mvav 40,220,180
buffer_store_dwordx4 v[122:125], v6, s[28:31], s73 offen offset:3072 sc1
Zvav 36,220,176
C_ 115,114,115
C_ 114,112,113
Mvav 36,196,156
C_ 116,116,117
C_ 117,118,119
Mvav 36,192,136
W_ 0
Mvav 56,180,196
s_nop 0
Ta 192,v1,49152
Ta 194,v1,51200
Mvav 56,184,216
Mvav 56,188,236
s_nop 0
Ta 196,v1,53248
Ta 198,v1,55296
Mvav 60,188,240
Mvav 60,184,220
s_nop 0
Ta 220,v1,57344
Ta 222,v1,59392
Mvav 60,180,200
buffer_store_dwordx4 v[114:117], v6, s[28:31], s73 offen offset:2048 sc1
Mvav 64,180,204
C_ 107,106,107
C_ 106,104,105
Mvav 64,184,224
C_ 108,108,109
C_ 109,110,111
Mvav 64,188,244
Mvav 68,188,248
Mvav 68,184,228
Mvav 68,180,208
Mvav 72,180,212
Mvav 72,184,232
buffer_store_dwordx4 v[106:109], v6, s[28:31], s73 offen offset:1024 sc1
Mvav 72,188,252
W_ 0
C_ 99,98,99
C_ 98,96,97
Mvav 92,220,252
s_nop 0
Ta 180,v2,49152
Ta 182,v2,51200
Mvav 92,196,232
.Lpad_o_4:
s_cbranch_execz .Lpad_o_8
C_ 100,100,101
C_ 101,102,103
Mvav 92,192,212
s_nop 0
Ta 184,v2,53248
Ta 186,v2,55296
Mvav 88,192,208
Mvav 88,196,228
s_nop 0
Ta 188,v2,57344
Ta 190,v2,59392
Mvav 88,220,248
Mvav 84,220,244
buffer_store_dwordx4 v[98:101], v6, s[28:31], s73 offen offset:0 sc1
Mvav 84,196,224
Mvav 84,192,204
Mvav 80,192,200
Mvav 80,196,220
S_ s83,s6,7
Mvav 80,220,240
s_and_b32 s83, s83, 7
Mvav 76,220,236
s_lshl_b32 s83, s83, 2
Mvav 76,196,216
v_cmp_eq_u32_e64 s[36:37], s83, v8
Mvav 76,192,196
W_ 0
v_mov_b32 v10, 0x7fff0000
Mvav 16,180,196
s_nop 0
Ta 192,v3,49152
Ta 194,v3,51200
Mvav 16,184,216
L_ v7,24,s55,0
Mvav 16,188,236
S_ s55,s71,624640
Ta 196,v3,53248
Ta 198,v3,55296
Mvav 20,188,240
S_ m0,s51,13312
v_cndmask_b32_e64 v10, v10, v9, s[36:37]
Mvav 20,184,220
s_nop 0
Ta 220,v3,57344
Ta 222,v3,59392
Mvav 20,180,200
L_ v7,24,s55,0
Mvav 24,180,204
S_ s55,s71,634880
Mvav 24,184,224
S_ m0,s51,14336
Mvav 24,188,244
Mvav 28,188,248
L_ v7,24,s55,0
Mvav 28,184,228
S_ s55,s71,645120
Mvav 28,180,208
S_ m0,s51,15360
Mvav 32,180,212
Mvav 32,184,232
L_ v7,24,s55,0
Mvav 32,188,252
s_waitcnt vmcnt(25) lgkmcnt(0)
s_barrier
S_ s55,s71,655360
Mvav 52,220,252
S_ m0,s51,16384
Ta 180,%0,36864
Ta 182,%0,38912
Mvav 52,196,232
Mvav 52,192,212
s_nop 0
Ta 184,%0,40960
Ta 186,%0,43008
Mvav 48,192,208
buffer_store_dword a255, v10, s[92:95], 0 offen offset:384 sc1
Mvav 48,196,228
s_nop 0
Ta 188,%0,45056
Ta 190,%0,47104
Mvav 48,220,248
Mvav 44,220,244
Mvav 44,196,224
L_ v7,24,s55,0
Mvav 44,192,204
S_ s55,s71,665600
Mvav 40,192,200
S_ m0,s51,17408
Mvav 40,196,220
Mvav 40,220,240
L_ v7,24,s55,0
Mvav 36,220,236
S_ s55,s71,0
Mvav 36,196,216
S_ m0,s51,49152
Mvav 36,192,196
W_ 0
Mvaa 56,180,0
s_nop 0
Ta 192,v1,36864
Ta 194,v1,38912
Mvaa 56,184,40
L_ v7,24,s55,0
Mvaa 56,188,80
S_ s55,s71,10240
Ta 196,v1,40960
Ta 198,v1,43008
Mvaa 60,188,84
S_ m0,s51,50176
Mvaa 60,184,44
s_nop 0
Ta 220,v1,45056
Ta 222,v1,47104
Mvaa 60,180,4
L_ v7,24,s55,0
Mvaa 64,180,8
S_ s55,s71,20480
Mvaa 64,184,48
S_ m0,s51,51200
Mvaa 64,188,88
Mvaa 68,188,240
L_ v7,24,s55,0
Mvaa 68,184,52
S_ s55,s71,30720
Mvaa 68,180,12
S_ m0,s51,52224
Mvaa 72,180,16
Mvaa 72,184,56
L_ v7,24,s55,0
Mvaa 72,188,244
W_ 0
S_ s55,s71,40960
Mvaa 92,220,244
S_ m0,s51,53248
Ta 180,v2,36864
Ta 182,v2,38912
Mvaa 92,196,56
Mvaa 92,192,16
s_nop 0
Ta 184,v2,40960
Ta 186,v2,43008
Mvaa 88,192,12
L_ v7,24,s55,0
Mvaa 88,196,52
S_ s55,s71,51200
Ta 188,v2,45056
Ta 190,v2,47104
Mvaa 88,220,240
S_ m0,s51,54272
Mvaa 84,220,88
Mvaa 84,196,48
L_ v7,24,s55,0
Mvaa 84,192,8
Mvaa 80,192,4
Mvaa 80,196,44
Mvaa 80,220,84
buffer_load_dword a74, v4, s[24:27], s85 offen
Mvaa 76,220,80
Mvaa 76,196,40
Mvaa 76,192,0
W_ 0
Mvaa 16,180,0
s_nop 0
Ta 192,v3,36864
Ta 194,v3,38912
Mvaa 16,184,40
Mvaa 16,188,80
s_nop 0
Ta 196,v3,40960
Ta 198,v3,43008
Mvaa 20,188,84
Mvaa 20,184,44
s_nop 0
Ta 220,v3,45056
Ta 222,v3,47104
Mvaa 20,180,4
Mvaa 24,180,8
Mvaa 24,184,48
Mvaa 24,188,88
Mvaa 28,188,240
Mvaa 28,184,52
Mvaa 28,180,12
Mvaa 32,180,16
Mvaa 32,184,56
Mvaa 32,188,244
s_waitcnt vmcnt(27) lgkmcnt(0)
s_barrier
Mvaa 52,220,244
s_nop 0
Ta 180,%0,24576
Ta 182,%0,26624
Mvaa 52,196,56
Mvaa 52,192,16
s_nop 0
Ta 184,%0,28672
Ta 186,%0,30720
Mvaa 48,192,12
Mvaa 48,196,52
s_nop 0
Ta 188,%0,32768
Ta 190,%0,34816
Mvaa 48,220,240
Mvaa 44,220,88
Mvaa 44,196,48
Mvaa 44,192,8
Mvaa 40,192,4
Mvaa 40,196,44
Mvaa 40,220,84
Mvaa 36,220,80
Mvaa 36,196,40
Mvaa 36,192,0
W_ 0
Mvaa 56,180,120
S_ s81,s6,2
Ta 192,v1,24576
Ta 194,v1,26624
Mvaa 56,184,160
s_and_b32 s81, s81, 7
Mvaa 56,188,200
s_mul_i32 s81, s81, 20480
Ta 196,v1,28672
Ta 198,v1,30720
Mvaa 60,188,204
s_mov_b32 m0, s52
S_ s73,s81,s78
Mvaa 60,184,164
S_ s73,s73,4096
Ta 220,v1,32768
Ta 222,v1,34816
Mvaa 60,180,124
s_nop 0
Y_ 56,0
Y_ 57,256
Mvaa 64,180,128
s_nop 0
Y_ 58,512
Y_ 59,768
Mvaa 64,184,168
Mvaa 64,188,208
s_nop 0
Y_ 60,1024
Y_ 61,1280
Mvaa 68,188,212
s_nop 0
Y_ 62,1536
Y_ 63,1792
Mvaa 68,184,172
Mvaa 68,180,132
s_nop 0
Y_ 64,2048
Y_ 65,2304
Mvaa 72,180,136
s_nop 0
Y_ 66,2560
Y_ 67,2816
Mvaa 72,184,176
Mvaa 72,188,216
W_ 12
Y_ 68,3072
Y_ 69,3328
Mvaa 92,220,216
s_nop 0
Ta 180,v2,24576
Ta 182,v2,26624
Mvaa 92,196,176
s_nop 0
Y_ 70,3584
Y_ 71,3840
Mvaa 92,192,136
s_nop 0
Ta 184,v2,28672
Ta 186,v2,30720
Mvaa 88,192,132
s_nop 0
Y_ 72,4096
Y_ 73,4352
Mvaa 88,196,172
S_ s83,s6,1
Ta 188,v2,32768
Ta 190,v2,34816
Mvaa 88,220,212
s_and_b32 s83, s83, 7
Y_ 74,4608
Y_ 75,4864
Mvaa 84,220,208
s_lshl_b32 s83, s83, 2
Y_ 92,9216
Y_ 93,9472
Mvaa 84,196,168
s_nop 0
Y_ 94,9728
Y_ 95,9984
Mvaa 84,192,128
s_nop 0
Y_ 88,8192
Y_ 89,8448
Mvaa 80,192,124
s_nop 0
Y_ 90,8704
Y_ 91,8960
Mvaa 80,196,164
v_cmp_eq_u32_e64 s[36:37], s83, v8
Mvaa 80,220,204
s_nop 0
Y_ 84,7168
Y_ 85,7424
Mvaa 76,220,200
s_nop 0
Y_ 86,7680
Y_ 87,7936
Mvaa 76,196,160
v_mov_b32 v10, 0x7fff0000
Mvaa 76,192,120
W_ 14
Y_ 80,6144
Y_ 81,6400
Mvaa 16,180,120
s_nop 0
Ta 192,v3,24576
Ta 194,v3,26624
Mvaa 16,184,160
s_nop 0
Y_ 82,6656
Y_ 83,6912
Mvaa 16,188,200
s_nop 0
Ta 196,v3,28672
Ta 198,v3,30720
Mvaa 20,188,204
s_nop 0
Y_ 76,5120
Y_ 77,5376
Mvaa 20,184,164
s_nop 0
Ta 220,v3,32768
Ta 222,v3,34816
Mvaa 20,180,124
s_nop 0
Y_ 78,5632
Y_ 79,5888
Mvaa 24,180,128
v_cndmask_b32_e64 v10, v10, v9, s[36:37]
Mvaa 24,184,168
Mvaa 24,188,208
s_waitcnt vmcnt(14) lgkmcnt(0)
s_barrier
Mvaa 28,188,212
buffer_store_dword a255, v10, s[92:95], 0 offen offset:384 sc1
Mvaa 28,184,172
S_ s55,s71,122880
ds_read2st64_b32 v[96:97], v5 offset0:0 offset1:1
Mvaa 28,180,132
S_ m0,s51,36864
ds_read2st64_b32 v[98:99], v5 offset0:2 offset1:3
Mvaa 32,180,136
ds_read2st64_b32 v[104:105], v5 offset0:4 offset1:5
Mvaa 32,184,176
L_ v7,24,s55,0
Mvaa 32,188,216
S_ s55,s71,133120
ds_read2st64_b32 v[106:107], v5 offset0:6 offset1:7
Mvaa 52,220,216
S_ m0,s51,37888
Ta 180,%0,0
Ta 182,%0,2048
Mvaa 52,196,176
ds_read2st64_b32 v[112:113], v5 offset0:8 offset1:9
Mvaa 52,192,136
W_ 5
Ta 184,%0,4096
Ta 186,%0,6144
Mvaa 48,192,132
L_ v7,24,s55,0
Mvaa 48,196,172
S_ s55,s71,143360
Ta 188,%0,8192
Ta 190,%0,10240
Mvaa 48,220,212
S_ m0,s51,38912
ds_read2st64_b32 v[114:115], v5 offset0:10 offset1:11
A_ v96,v16,v96
Mvaa 44,220,208
ds_read2st64_b32 v[120:121], v5 offset0:12 offset1:13
A_ v97,v17,v97
Mvaa 44,196,168
L_ v7,24,s55,0
Mvaa 44,192,128
S_ s55,s71,153600
ds_read2st64_b32 v[122:123], v5 offset0:14 offset1:15
A_ v98,v18,v98
Mvaa 40,192,124
S_ m0,s51,39936
ds_read2st64_b32 v[128:129], v5 offset0:16 offset1:17
A_ v99,v19,v99
Mvaa 40,196,164
W_ 11
ds_read2st64_b32 v[130:131], v5 offset0:18 offset1:19
A_ v104,v20,v104
Mvaa 40,220,204
L_ v7,24,s55,0
Mvaa 36,220,200
S_ s55,s71,163840
ds_read2st64_b32 v[132:133], v5 offset0:36 offset1:37
A_ v105,v21,v105
Mvaa 36,196,160
S_ m0,s51,40960
ds_read2st64_b32 v[134:135], v5 offset0:38 offset1:39
A_ v106,v22,v106
Mvaa 36,192,120
ds_read2st64_b32 v[124:125], v5 offset0:32 offset1:33
A_ v107,v23,v107
s_waitcnt vmcnt(23)
Zaaa 224,70,60
L_ v7,24,s55,0
Zaaa 228,68,60
W_ 7
ds_read2st64_b32 v[126:127], v5 offset0:34 offset1:35
A_ v112,v24,v112
Zaaa 232,70,64
S_ s55,s71,174080
ds_read2st64_b32 v[116:117], v5 offset0:28 offset1:29
A_ v113,v25,v113
Zaaa 236,68,64
ds_read2st64_b32 v[118:119], v5 offset0:30 offset1:31
A_ v114,v26,v114
Zvav 56,180,136
S_ m0,s51,41984
Ta 192,v1,0
Ta 194,v1,2048
Mvav 56,184,156
L_ v7,24,s55,0
Mvav 56,188,176
S_ s79,s6,4
Ta 196,v1,4096
Ta 198,v1,6144
Zvav 60,188,180
s_and_b32 s79, s79, 7
ds_read2st64_b32 v[108:109], v5 offset0:24 offset1:25
A_ v115,v27,v115
Mvav 60,184,160
W_ 13
Ta 220,v1,8192
Ta 222,v1,10240
Mvav 60,180,140
s_lshl_b32 s79, s79, 7
ds_read2st64_b32 v[110:111], v5 offset0:26 offset1:27
A_ v120,v28,v120
Zvav 64,180,144
S_ s71,s72,s79
ds_read2st64_b32 v[100:101], v5 offset0:20 offset1:21
A_ v121,v29,v121
Mvav 64,184,164
S_ s55,s71,491520
ds_read2st64_b32 v[102:103], v5 offset0:22 offset1:23
A_ v122,v30,v122
Mvav 64,188,184
S_ m0,s51,24576
A_ v123,v31,v123
Zvav 68,188,188
L_ v7,24,s55,0
Mvav 68,184,168
W_ 14
A_ v128,v32,v128
A_ v129,v33,v129
Mvav 68,180,148
S_ s55,s71,501760
A_ v130,v34,v130
A_ v131,v35,v131
Zvav 72,180,152
A_ v132,v52,v132
A_ v133,v53,v133
Mvav 72,184,172
S_ m0,s51,25600
A_ v134,v54,v134
A_ v135,v55,v135
Mvav 72,188,192
W_ 3
A_ v124,v48,v124
Zvav 92,220,192
s_nop 0
Ta 180,v2,0
Ta 182,v2,2048
Mvav 92,196,172
L_ v7,24,s55,0
Mvav 92,192,152
S_ s55,s71,512000
Ta 184,v2,4096
Ta 186,v2,6144
Zvav 88,192,148
S_ m0,s51,26624
A_ v125,v49,v125
A_ v126,v50,v126
Mvav 88,196,168
s_nop 0
Ta 188,v2,8192
Ta 190,v2,10240
Mvav 88,220,188
L_ v7,24,s55,0
Zvav 84,220,184
S_ s55,s71,522240
A_ v127,v51,v127
C_ 131,130,131
Mvav 84,196,164
A_ v116,v44,v116
A_ v117,v45,v117
Mvav 84,192,144
S_ m0,s51,27648
A_ v118,v46,v118
A_ v119,v47,v119
Zvav 80,192,140
L_ v7,24,s55,0
Mvav 80,196,160
W_ 8
A_ v108,v40,v108
A_ v109,v41,v109
Mvav 80,220,180
S_ s55,s71,532480
A_ v110,v42,v110
A_ v111,v43,v111
Zvav 76,220,176
W_ 6
A_ v100,v36,v100
A_ v101,v37,v101
Mvav 76,196,156
S_ m0,s51,28672
A_ v102,v38,v102
A_ v103,v39,v103
Mvav 76,192,136
W_ 0
C_ 130,128,129
C_ 132,132,133
Zvav 16,180,136
S_ s85,s6,6
Ta 192,v3,0
Ta 194,v3,2048
Mvav 16,184,156
L_ v7,24,s55,0
Mvav 16,188,176
S_ s55,s71,542720
Ta 196,v3,4096
Ta 198,v3,6144
Zvav 20,188,180
S_ m0,s51,29696
C_ 133,134,135
s_and_b32 s85, s85, 7
Mvav 20,184,160
s_lshl_b32 s85, s85, 7
Ta 220,v3,8192
Ta 222,v3,10240
Mvav 20,180,140
buffer_store_dwordx4 v[130:133], v6, s[28:31], s73 offen offset:0 sc1
Zvav 24,180,144
s_sub_u32 s73, s73, 4096
S_ s85,s85,s87
Mvav 24,184,164
C_ 123,122,123
C_ 122,120,121
Mvav 24,188,184
C_ 124,124,125
C_ 125,126,127
Zvav 28,188,188
Mvav 28,184,168
Mvav 28,180,148
Zvav 32,180,152
Mvav 32,184,172
buffer_store_dwordx4 v[122:125], v6, s[28:31], s73 offen offset:3072 sc1
Mvav 32,188,192
s_waitcnt vmcnt(21) lgkmcnt(0)
s_barrier
C_ 115,114,115
Zvav 52,220,192
s_nop 0
Ta 180,%0,12288
Ta 182,%0,14336
Mvav 52,196,172
.Lpad_o_5:
s_cbranch_execz .Lpad_o_9
C_ 114,112,113
C_ 116,116,117
Mvav 52,192,152
s_nop 0
Ta 184,%0,16384
Ta 186,%0,18432
Zvav 48,192,148
C_ 117,118,119
Mvav 48,196,168
s_nop 0
Ta 188,%0,20480
Ta 190,%0,22528
Mvav 48,220,188
Zvav 44,220,184
buffer_store_dwordx4 v[114:117], v6, s[28:31], s73 offen offset:2048 sc1
Mvav 44,196,164
C_ 107,106,107
C_ 106,104,105
Mvav 44,192,144
C_ 108,108,109
C_ 109,110,111
Zvav 40,192,140
Mvav 40,196,160
Mvav 40,220,180
Zvav 36,220,176
Mvav 36,196,156
Mvav 36,192,136
W_ 0
Mvav 56,180,196
s_nop 0
Ta 192,v1,12288
Ta 194,v1,14336
Mvav 56,184,216
buffer_store_dwordx4 v[106:109], v6, s[28:31], s73 offen offset:1024 sc1
Mvav 56,188,236
s_nop 0
Ta 196,v1,16384
Ta 198,v1,18432
Mvav 60,188,240
C_ 99,98,99
C_ 98,96,97
Mvav 60,184,220
s_nop 0
Ta 220,v1,20480
Ta 222,v1,22528
Mvav 60,180,200
C_ 100,100,101
C_ 101,102,103
Mvav 64,180,204
Mvav 64,184,224
Mvav 64,188,244
Mvav 68,188,248
buffer_store_dwordx4 v[98:101], v6, s[28:31], s73 offen offset:0 sc1
Mvav 68,184,228
Mvav 68,180,208
Mvav 72,180,212
Mvav 72,184,232
Mvav 72,188,252
W_ 0
Mvav 92,220,252
s_nop 0
Ta 180,v2,12288
Ta 182,v2,14336
Mvav 92,196,232
Mvav 92,192,212
s_nop 0
Ta 184,v2,16384
Ta 186,v2,18432
Mvav 88,192,208
L_ v7,24,s55,0
Mvav 88,196,228
S_ s55,s71,614400
Ta 188,v2,20480
Ta 190,v2,22528
Mvav 88,220,248
S_ m0,s51,0
Mvav 84,220,244
Mvav 84,196,224
L_ v7,24,s55,0
Mvav 84,192,204
S_ s55,s71,624640
Mvav 80,192,200
S_ m0,s51,1024
Mvav 80,196,220
Mvav 80,220,240
L_ v7,24,s55,0
Mvav 76,220,236
S_ s55,s71,634880
Mvav 76,196,216
S_ m0,s51,2048
Mvav 76,192,196
W_ 0
Mvav 16,180,196
s_nop 0
Ta 192,v3,12288
Ta 194,v3,14336
Mvav 16,184,216
L_ v7,24,s55,0
Mvav 16,188,236
S_ s55,s71,645120
Ta 196,v3,16384
Ta 198,v3,18432
Mvav 20,188,240
S_ m0,s51,3072
Mvav 20,184,220
s_nop 0
Ta 220,v3,20480
Ta 222,v3,22528
Mvav 20,180,200
L_ v7,24,s55,0
Mvav 24,180,204
S_ s55,s71,655360
Mvav 24,184,224
S_ m0,s51,4096
Mvav 24,188,244
Mvav 28,188,248
L_ v7,24,s55,0
Mvav 28,184,228
S_ s55,s71,665600
Mvav 28,180,208
S_ m0,s51,5120
Mvav 32,180,212
Mvav 32,184,232
L_ v7,24,s55,0
Mvav 32,188,252
s_waitcnt vmcnt(25) lgkmcnt(0)
s_barrier
Mvav 52,220,252
S_ s55,s71,0
Ta 180,%0,49152
Ta 182,%0,51200
Mvav 52,196,232
S_ m0,s51,12288
Mvav 52,192,212
s_nop 0
Ta 184,%0,53248
Ta 186,%0,55296
Mvav 48,192,208
L_ v7,24,s55,0
Mvav 48,196,228
S_ s55,s71,10240
Ta 188,%0,57344
Ta 190,%0,59392
Mvav 48,220,248
S_ m0,s51,13312
Mvav 44,220,244
Mvav 44,196,224
L_ v7,24,s55,0
Mvav 44,192,204
S_ s55,s71,20480
Mvav 40,192,200
S_ m0,s51,14336
Mvav 40,196,220
Mvav 40,220,240
L_ v7,24,s55,0
Mvav 36,220,236
S_ s55,s71,30720
Mvav 36,196,216
S_ m0,s51,15360
Mvav 36,192,196
W_ 0
Mvaa 56,180,0
s_nop 0
Ta 192,v1,49152
Ta 194,v1,51200
Mvaa 56,184,40
L_ v7,24,s55,0
Mvaa 56,188,80
S_ s55,s71,40960
Ta 196,v1,53248
Ta 198,v1,55296
Mvaa 60,188,84
S_ m0,s51,16384
Mvaa 60,184,44
s_nop 0
Ta 220,v1,57344
Ta 222,v1,59392
Mvaa 60,180,4
L_ v7,24,s55,0
Mvaa 64,180,8
S_ s55,s71,51200
Mvaa 64,184,48
S_ m0,s51,17408
Mvaa 64,188,88
Mvaa 68,188,240
L_ v7,24,s55,0
Mvaa 68,184,52
Mvaa 68,180,12
Mvaa 72,180,16
Mvaa 72,184,56
buffer_load_dword a74, v4, s[24:27], s85 offen
Mvaa 72,188,244
W_ 0
Mvaa 92,220,244
s_nop 0
Ta 180,v2,49152
Ta 182,v2,51200
Mvaa 92,196,56
Mvaa 92,192,16
s_nop 0
Ta 184,v2,53248
Ta 186,v2,55296
Mvaa 88,192,12
Mvaa 88,196,52
s_nop 0
Ta 188,v2,57344
Ta 190,v2,59392
Mvaa 88,220,240
Mvaa 84,220,88
Mvaa 84,196,48
Mvaa 84,192,8
Mvaa 80,192,4
Mvaa 80,196,44
Mvaa 80,220,84
Mvaa 76,220,80
Mvaa 76,196,40
Mvaa 76,192,0
W_ 0
Mvaa 16,180,0
s_nop 0
Ta 192,v3,49152
Ta 194,v3,51200
Mvaa 16,184,40
Mvaa 16,188,80
s_nop 0
Ta 196,v3,53248
Ta 198,v3,55296
Mvaa 20,188,84
Mvaa 20,184,44
s_nop 0
Ta 220,v3,57344
Ta 222,v3,59392
Mvaa 20,180,4
Mvaa 24,180,8
Mvaa 24,184,48
Mvaa 24,188,88
Mvaa 28,188,240
Mvaa 28,184,52
Mvaa 28,180,12
Mvaa 32,180,16
Mvaa 32,184,56
Mvaa 32,188,244
s_waitcnt vmcnt(24) lgkmcnt(0)
s_barrier
Mvaa 52,220,244
s_nop 0
Ta 180,%0,36864
Ta 182,%0,38912
Mvaa 52,196,56
Mvaa 52,192,16
s_nop 0
Ta 184,%0,40960
Ta 186,%0,43008
Mvaa 48,192,12
Mvaa 48,196,52
s_nop 0
Ta 188,%0,45056
Ta 190,%0,47104
Mvaa 48,220,240
Mvaa 44,220,88
Mvaa 44,196,48
Mvaa 44,192,8
Mvaa 40,192,4
Mvaa 40,196,44
Mvaa 40,220,84
Mvaa 36,220,80
Mvaa 36,196,40
Mvaa 36,192,0
W_ 0
Mvaa 56,180,120
S_ s81,s6,3
Ta 192,v1,36864
Ta 194,v1,38912
Mvaa 56,184,160
s_and_b32 s81, s81, 7
Mvaa 56,188,200
s_mul_i32 s81, s81, 20480
Ta 196,v1,40960
Ta 198,v1,43008
Mvaa 60,188,204
s_mov_b32 m0, s52
S_ s73,s81,s78
Mvaa 60,184,164
S_ s73,s73,4096
Ta 220,v1,45056
Ta 222,v1,47104
Mvaa 60,180,124
s_nop 0
Y_ 56,0
Y_ 57,256
Mvaa 64,180,128
s_nop 0
Y_ 58,512
Y_ 59,768
Mvaa 64,184,168
Mvaa 64,188,208
s_nop 0
Y_ 60,1024
Y_ 61,1280
Mvaa 68,188,212
s_nop 0
Y_ 62,1536
Y_ 63,1792
Mvaa 68,184,172
Mvaa 68,180,132
s_nop 0
Y_ 64,2048
Y_ 65,2304
Mvaa 72,180,136
s_nop 0
Y_ 66,2560
Y_ 67,2816
Mvaa 72,184,176
Mvaa 72,188,216
W_ 12
Y_ 68,3072
Y_ 69,3328
Mvaa 92,220,216
s_nop 0
Ta 180,v2,36864
Ta 182,v2,38912
Mvaa 92,196,176
s_nop 0
Y_ 70,3584
Y_ 71,3840
Mvaa 92,192,136
s_nop 0
Ta 184,v2,40960
Ta 186,v2,43008
Mvaa 88,192,132
s_nop 0
Y_ 72,4096
Y_ 73,4352
Mvaa 88,196,172
S_ s83,s6,2
Ta 188,v2,45056
Ta 190,v2,47104
Mvaa 88,220,212
s_and_b32 s83, s83, 7
Y_ 74,4608
Y_ 75,4864
Mvaa 84,220,208
s_lshl_b32 s83, s83, 2
Y_ 92,9216
Y_ 93,9472
Mvaa 84,196,168
s_nop 0
Y_ 94,9728
Y_ 95,9984
Mvaa 84,192,128
s_nop 0
Y_ 88,8192
Y_ 89,8448
Mvaa 80,192,124
s_nop 0
Y_ 90,8704
Y_ 91,8960
Mvaa 80,196,164
v_cmp_eq_u32_e64 s[36:37], s83, v8
Mvaa 80,220,204
s_sub_u32 s75, s6, 1
Y_ 84,7168
Y_ 85,7424
Mvaa 76,220,200
s_and_b32 s75, s75, 7
Y_ 86,7680
Y_ 87,7936
Mvaa 76,196,160
v_mov_b32 v10, 0x7fff0000
Mvaa 76,192,120
)A"
R"A(W_ 14
Y_ 80,6144
Y_ 81,6400
Mvaa 16,180,120
s_nop 0
Ta 192,v3,36864
Ta 194,v3,38912
Mvaa 16,184,160
s_nop 0
Y_ 82,6656
Y_ 83,6912
Mvaa 16,188,200
s_nop 0
Ta 196,v3,40960
Ta 198,v3,43008
Mvaa 20,188,204
s_nop 0
Y_ 76,5120
Y_ 77,5376
Mvaa 20,184,164
s_nop 0
Ta 220,v3,45056
Ta 222,v3,47104
Mvaa 20,180,124
s_nop 0
Y_ 78,5632
Y_ 79,5888
Mvaa 24,180,128
buffer_load_dword v11, v8, s[92:95], s96 offen offset:384 sc1
Mvaa 24,184,168
v_cndmask_b32_e64 v10, v10, v9, s[36:37]
Mvaa 24,188,208
s_waitcnt vmcnt(14) lgkmcnt(0)
s_barrier
Mvaa 28,188,212
S_ s55,s71,122880
ds_read2st64_b32 v[96:97], v5 offset0:0 offset1:1
Mvaa 28,184,172
buffer_store_dword a255, v10, s[92:95], 0 offen offset:384 sc1
Mvaa 28,180,132
S_ m0,s51,49152
ds_read2st64_b32 v[98:99], v5 offset0:2 offset1:3
Mvaa 32,180,136
ds_read2st64_b32 v[104:105], v5 offset0:4 offset1:5
Mvaa 32,184,176
ds_read2st64_b32 v[106:107], v5 offset0:6 offset1:7
Mvaa 32,188,216
L_ v7,24,s55,0
Mvaa 52,220,216
S_ s55,s71,133120
Ta 180,%0,24576
Ta 182,%0,26624
Mvaa 52,196,176
S_ m0,s51,50176
ds_read2st64_b32 v[112:113], v5 offset0:8 offset1:9
Mvaa 52,192,136
W_ 5
Ta 184,%0,28672
Ta 186,%0,30720
Mvaa 48,192,132
L_ v7,24,s55,0
Mvaa 48,196,172
S_ s55,s71,143360
Ta 188,%0,32768
Ta 190,%0,34816
Mvaa 48,220,212
S_ m0,s51,51200
ds_read2st64_b32 v[114:115], v5 offset0:10 offset1:11
A_ v96,v16,v96
Mvaa 44,220,208
ds_read2st64_b32 v[120:121], v5 offset0:12 offset1:13
A_ v97,v17,v97
Mvaa 44,196,168
L_ v7,24,s55,0
Mvaa 44,192,128
S_ s55,s71,153600
ds_read2st64_b32 v[122:123], v5 offset0:14 offset1:15
A_ v98,v18,v98
Mvaa 40,192,124
S_ m0,s51,52224
ds_read2st64_b32 v[128:129], v5 offset0:16 offset1:17
A_ v99,v19,v99
Mvaa 40,196,164
W_ 11
ds_read2st64_b32 v[130:131], v5 offset0:18 offset1:19
A_ v104,v20,v104
Mvaa 40,220,204
L_ v7,24,s55,0
Mvaa 36,220,200
S_ s55,s71,163840
ds_read2st64_b32 v[132:133], v5 offset0:36 offset1:37
A_ v105,v21,v105
Mvaa 36,196,160
S_ m0,s51,53248
ds_read2st64_b32 v[134:135], v5 offset0:38 offset1:39
A_ v106,v22,v106
Mvaa 36,192,120
W_ 7
ds_read2st64_b32 v[124:125], v5 offset0:32 offset1:33
A_ v107,v23,v107
Zvav 56,180,136
W_ 7
Ta 192,v1,24576
Ta 194,v1,26624
Mvav 56,184,156
L_ v7,24,s55,0
Mvav 56,188,176
S_ s55,s71,174080
Ta 196,v1,28672
Ta 198,v1,30720
Zvav 60,188,180
S_ m0,s51,54272
ds_read2st64_b32 v[126:127], v5 offset0:34 offset1:35
A_ v112,v24,v112
Mvav 60,184,160
s_nop 0
Ta 220,v1,32768
Ta 222,v1,34816
Mvav 60,180,140
L_ v7,24,s55,0
Zvav 64,180,144
S_ s79,s6,5
ds_read2st64_b32 v[116:117], v5 offset0:28 offset1:29
A_ v113,v25,v113
Mvav 64,184,164
s_and_b32 s79, s79, 7
ds_read2st64_b32 v[118:119], v5 offset0:30 offset1:31
A_ v114,v26,v114
Mvav 64,188,184
s_lshl_b32 s79, s79, 7
ds_read2st64_b32 v[108:109], v5 offset0:24 offset1:25
A_ v115,v27,v115
Zvav 68,188,188
W_ 14
ds_read2st64_b32 v[110:111], v5 offset0:26 offset1:27
A_ v120,v28,v120
Mvav 68,184,168
S_ s71,s72,s79
ds_read2st64_b32 v[100:101], v5 offset0:20 offset1:21
A_ v121,v29,v121
Mvav 68,180,148
S_ s55,s71,491520
ds_read2st64_b32 v[102:103], v5 offset0:22 offset1:23
A_ v122,v30,v122
Zvav 72,180,152
s_waitcnt vmcnt(7)
A_ v123,v31,v123
S_ m0,s51,36864
Mvav 72,184,172
L_ v7,24,s55,0
Mvav 72,188,192
W_ 6
v_cmp_eq_u32 vcc, s62, v11
Zvav 92,220,192
s_mul_i32 s76, s75, 204800
Ta 180,v2,24576
Ta 182,v2,26624
Mvav 92,196,172
s_bitcmp1_b32 vcc_lo, s75
s_cbranch_scc0 .Lspin0
.Lspun0:
A_ v128,v32,v128
Mvav 92,192,152
S_ s56,s76,s84
Ta 184,v2,28672
Ta 186,v2,30720
Zvav 88,192,148
buffer_load_dwordx4 a[20:23], v6, s[28:31], s56 offen offset:0 sc1
Mvav 88,196,168
S_ s55,s71,501760
Ta 188,v2,32768
Ta 190,v2,34816
Mvav 88,220,188
S_ m0,s51,37888
A_ v129,v33,v129
A_ v130,v34,v130
Zvav 84,220,184
S_ s85,s6,7
A_ v131,v35,v131
Mvav 84,196,164
buffer_load_dwordx4 a[24:27], v6, s[28:31], s56 offen offset:1024 sc1
Mvav 84,192,144
A_ v132,v52,v132
A_ v133,v53,v133
Zvav 80,192,140
s_and_b32 s85, s85, 7
A_ v134,v54,v134
A_ v135,v55,v135
Mvav 80,196,160
A_ v124,v48,v124
A_ v125,v49,v125
Mvav 80,220,180
buffer_load_dwordx4 a[28:31], v6, s[28:31], s56 offen offset:2048 sc1
Zvav 76,220,176
s_lshl_b32 s85, s85, 7
A_ v126,v50,v126
A_ v127,v51,v127
Mvav 76,196,156
W_ 10
A_ v116,v44,v116
A_ v117,v45,v117
Mvav 76,192,136
W_ 0
A_ v118,v46,v118
A_ v119,v47,v119
Zvav 16,180,136
.Lpad_o_6:
s_cbranch_execz .Lpad_o_10
Ta 192,v3,24576
Ta 194,v3,26624
Mvav 16,184,156
buffer_load_dwordx4 a[32:35], v6, s[28:31], s56 offen offset:3072 sc1
Mvav 16,188,176
S_ s56,s56,4096
Ta 196,v3,28672
Ta 198,v3,30720
Zvav 20,188,180
S_ s85,s85,s87
A_ v108,v40,v108
A_ v109,v41,v109
Mvav 20,184,160
s_nop 0
Ta 220,v3,32768
Ta 222,v3,34816
Mvav 20,180,140
buffer_load_dwordx4 a[36:39], v6, s[28:31], s56 offen offset:0 sc1
Zvav 24,180,144
S_ s56,s76,s74
A_ v110,v42,v110
A_ v111,v43,v111
Mvav 24,184,164
A_ v100,v36,v100
A_ v101,v37,v101
Mvav 24,188,184
A_ v102,v38,v102
A_ v103,v39,v103
Zvav 28,188,188
buffer_load_dwordx4 a[60:63], v6, s[28:31], s56 offen offset:0 sc1
Mvav 28,184,168
S_ s56,s76,s82
C_ 131,130,131
C_ 130,128,129
Mvav 28,180,148
C_ 132,132,133
C_ 133,134,135
Zvav 32,180,152
Mvav 32,184,172
buffer_load_dwordx4 a[64:67], v6, s[28:31], s56 offen offset:0 sc1
Mvav 32,188,192
s_waitcnt vmcnt(23) lgkmcnt(0)
s_barrier
Zvav 52,220,192
s_nop 0
Ta 180,%0,0
Ta 182,%0,2048
Mvav 52,196,172
Mvav 52,192,152
s_nop 0
Ta 184,%0,4096
Ta 186,%0,6144
Zvav 48,192,148
buffer_store_dwordx4 v[130:133], v6, s[28:31], s73 offen offset:0 sc1
Mvav 48,196,168
s_sub_u32 s73, s73, 4096
Ta 188,%0,8192
Ta 190,%0,10240
Mvav 48,220,188
C_ 123,122,123
C_ 122,120,121
Zvav 44,220,184
C_ 124,124,125
C_ 125,126,127
Mvav 44,196,164
Mvav 44,192,144
Zvav 40,192,140
Mvav 40,196,160
Mvav 40,220,180
buffer_store_dwordx4 v[122:125], v6, s[28:31], s73 offen offset:3072 sc1
Zvav 36,220,176
C_ 115,114,115
C_ 114,112,113
Mvav 36,196,156
C_ 116,116,117
C_ 117,118,119
Mvav 36,192,136
W_ 0
Mvav 56,180,196
s_nop 0
Ta 192,v1,0
Ta 194,v1,2048
Mvav 56,184,216
Mvav 56,188,236
s_nop 0
Ta 196,v1,4096
Ta 198,v1,6144
Mvav 60,188,240
Mvav 60,184,220
s_nop 0
Ta 220,v1,8192
Ta 222,v1,10240
Mvav 60,180,200
buffer_store_dwordx4 v[114:117], v6, s[28:31], s73 offen offset:2048 sc1
Mvav 64,180,204
C_ 107,106,107
C_ 106,104,105
Mvav 64,184,224
C_ 108,108,109
C_ 109,110,111
Mvav 64,188,244
Mvav 68,188,248
Mvav 68,184,228
Mvav 68,180,208
Mvav 72,180,212
Mvav 72,184,232
buffer_store_dwordx4 v[106:109], v6, s[28:31], s73 offen offset:1024 sc1
Mvav 72,188,252
W_ 0
C_ 99,98,99
C_ 98,96,97
Mvav 92,220,252
s_nop 0
Ta 180,v2,0
Ta 182,v2,2048
Mvav 92,196,232
C_ 100,100,101
C_ 101,102,103
Mvav 92,192,212
s_nop 0
Ta 184,v2,4096
Ta 186,v2,6144
Mvav 88,192,208
Mvav 88,196,228
s_nop 0
Ta 188,v2,8192
Ta 190,v2,10240
Mvav 88,220,248
Mvav 84,220,244
buffer_store_dwordx4 v[98:101], v6, s[28:31], s73 offen offset:0 sc1
Mvav 84,196,224
Mvav 84,192,204
Mvav 80,192,200
Mvav 80,196,220
Mvav 80,220,240
Mvav 76,220,236
Mvav 76,196,216
Mvav 76,192,196
W_ 0
Mvav 16,180,196
s_nop 0
Ta 192,v3,0
Ta 194,v3,2048
Mvav 16,184,216
L_ v7,24,s55,0
Mvav 16,188,236
S_ s55,s71,512000
Ta 196,v3,4096
Ta 198,v3,6144
Mvav 20,188,240
S_ m0,s51,38912
Mvav 20,184,220
s_nop 0
Ta 220,v3,8192
Ta 222,v3,10240
Mvav 20,180,200
L_ v7,24,s55,0
Mvav 24,180,204
S_ s55,s71,522240
Mvav 24,184,224
S_ m0,s51,39936
Mvav 24,188,244
Mvav 28,188,248
L_ v7,24,s55,0
Mvav 28,184,228
S_ s55,s71,532480
Mvav 28,180,208
S_ m0,s51,40960
Mvav 32,180,212
Mvav 32,184,232
L_ v7,24,s55,0
Mvav 32,188,252
s_waitcnt vmcnt(26) lgkmcnt(0)
s_barrier
S_ s55,s71,542720
Mvav 52,220,252
S_ m0,s51,41984
Ta 180,%0,12288
Ta 182,%0,14336
Mvav 52,196,232
Mvav 52,192,212
s_nop 0
Ta 184,%0,16384
Ta 186,%0,18432
Mvav 48,192,208
L_ v7,24,s55,0
Mvav 48,196,228
S_ s55,s71,614400
Ta 188,%0,20480
Ta 190,%0,22528
Mvav 48,220,248
S_ m0,s51,24576
Mvav 44,220,244
Mvav 44,196,224
L_ v7,24,s55,0
Mvav 44,192,204
S_ s55,s71,624640
Mvav 40,192,200
S_ m0,s51,25600
Mvav 40,196,220
Mvav 40,220,240
L_ v7,24,s55,0
Mvav 36,220,236
S_ s55,s71,634880
Mvav 36,196,216
S_ m0,s51,26624
Mvav 36,192,196
W_ 0
Mvaa 56,180,0
s_nop 0
Ta 192,v1,12288
Ta 194,v1,14336
Mvaa 56,184,40
L_ v7,24,s55,0
Mvaa 56,188,80
S_ s55,s71,645120
Ta 196,v1,16384
Ta 198,v1,18432
Mvaa 60,188,84
S_ m0,s51,27648
Mvaa 60,184,44
s_nop 0
Ta 220,v1,20480
Ta 222,v1,22528
Mvaa 60,180,4
L_ v7,24,s55,0
Mvaa 64,180,8
S_ s55,s71,655360
Mvaa 64,184,48
S_ m0,s51,28672
Mvaa 64,188,88
Mvaa 68,188,240
L_ v7,24,s55,0
Mvaa 68,184,52
S_ s55,s71,665600
Mvaa 68,180,12
S_ m0,s51,29696
Mvaa 72,180,16
Mvaa 72,184,56
L_ v7,24,s55,0
Mvaa 72,188,244
W_ 0
S_ s55,s71,0
Mvaa 92,220,244
S_ m0,s51,0
Ta 180,v2,12288
Ta 182,v2,14336
Mvaa 92,196,56
Mvaa 92,192,16
s_nop 0
Ta 184,v2,16384
Ta 186,v2,18432
Mvaa 88,192,12
L_ v7,24,s55,0
Mvaa 88,196,52
S_ s55,s71,10240
Ta 188,v2,20480
Ta 190,v2,22528
Mvaa 88,220,240
S_ m0,s51,1024
Mvaa 84,220,88
Mvaa 84,196,48
L_ v7,24,s55,0
Mvaa 84,192,8
S_ s55,s71,20480
Mvaa 80,192,4
S_ m0,s51,2048
Mvaa 80,196,44
Mvaa 80,220,84
L_ v7,24,s55,0
Mvaa 76,220,80
S_ s55,s71,30720
Mvaa 76,196,40
S_ m0,s51,3072
Mvaa 76,192,0
W_ 0
Mvaa 16,180,0
s_nop 0
Ta 192,v3,12288
Ta 194,v3,14336
Mvaa 16,184,40
L_ v7,24,s55,0
Mvaa 16,188,80
S_ s55,s71,40960
Ta 196,v3,16384
Ta 198,v3,18432
Mvaa 20,188,84
S_ m0,s51,4096
Mvaa 20,184,44
s_nop 0
Ta 220,v3,20480
Ta 222,v3,22528
Mvaa 20,180,4
L_ v7,24,s55,0
Mvaa 24,180,8
S_ s55,s71,51200
Mvaa 24,184,48
S_ m0,s51,5120
Mvaa 24,188,88
Mvaa 28,188,240
L_ v7,24,s55,0
Mvaa 28,184,52
Mvaa 28,180,12
Mvaa 32,180,16
Mvaa 32,184,56
buffer_load_dword a74, v4, s[24:27], s85 offen
Mvaa 32,188,244
s_waitcnt vmcnt(31) lgkmcnt(0)
s_barrier
Mvaa 52,220,244
s_nop 0
Ta 180,%0,49152
Ta 182,%0,51200
Mvaa 52,196,56
Mvaa 52,192,16
s_nop 0
Ta 184,%0,53248
Ta 186,%0,55296
Mvaa 48,192,12
Mvaa 48,196,52
s_nop 0
Ta 188,%0,57344
Ta 190,%0,59392
Mvaa 48,220,240
Mvaa 44,220,88
Mvaa 44,196,48
Mvaa 44,192,8
Mvaa 40,192,4
Mvaa 40,196,44
Mvaa 40,220,84
Mvaa 36,220,80
Mvaa 36,196,40
Mvaa 36,192,0
W_ 0
Mvaa 56,180,120
S_ s81,s6,4
Ta 192,v1,49152
Ta 194,v1,51200
Mvaa 56,184,160
s_and_b32 s81, s81, 7
Mvaa 56,188,200
s_mul_i32 s81, s81, 20480
Ta 196,v1,53248
Ta 198,v1,55296
Mvaa 60,188,204
s_mov_b32 m0, s52
S_ s73,s81,s78
Mvaa 60,184,164
S_ s73,s73,4096
Ta 220,v1,57344
Ta 222,v1,59392
Mvaa 60,180,124
s_nop 0
Y_ 56,0
Y_ 57,256
Mvaa 64,180,128
s_nop 0
Y_ 58,512
Y_ 59,768
Mvaa 64,184,168
Mvaa 64,188,208
s_nop 0
Y_ 60,1024
Y_ 61,1280
Mvaa 68,188,212
s_nop 0
Y_ 62,1536
Y_ 63,1792
Mvaa 68,184,172
Mvaa 68,180,132
s_nop 0
Y_ 64,2048
Y_ 65,2304
Mvaa 72,180,136
s_nop 0
Y_ 66,2560
Y_ 67,2816
Mvaa 72,184,176
Mvaa 72,188,216
W_ 12
Y_ 68,3072
Y_ 69,3328
Mvaa 92,220,216
s_nop 0
Ta 180,v2,49152
Ta 182,v2,51200
Mvaa 92,196,176
s_nop 0
Y_ 70,3584
Y_ 71,3840
Mvaa 92,192,136
s_nop 0
Ta 184,v2,53248
Ta 186,v2,55296
Mvaa 88,192,132
s_nop 0
Y_ 72,4096
Y_ 73,4352
Mvaa 88,196,172
S_ s83,s6,3
Ta 188,v2,57344
Ta 190,v2,59392
Mvaa 88,220,212
s_and_b32 s83, s83, 7
Y_ 74,4608
Y_ 75,4864
Mvaa 84,220,208
s_lshl_b32 s83, s83, 2
Y_ 92,9216
Y_ 93,9472
Mvaa 84,196,168
s_nop 0
Y_ 94,9728
Y_ 95,9984
Mvaa 84,192,128
s_nop 0
Y_ 88,8192
Y_ 89,8448
Mvaa 80,192,124
s_nop 0
Y_ 90,8704
Y_ 91,8960
Mvaa 80,196,164
v_cmp_eq_u32_e64 s[36:37], s83, v8
Mvaa 80,220,204
s_sub_u32 s75, s6, 2
Y_ 84,7168
Y_ 85,7424
Mvaa 76,220,200
s_and_b32 s75, s75, 7
Y_ 86,7680
Y_ 87,7936
Mvaa 76,196,160
v_mov_b32 v10, 0x7fff0000
Mvaa 76,192,120
W_ 14
Y_ 80,6144
Y_ 81,6400
Mvaa 16,180,120
s_nop 0
Ta 192,v3,49152
Ta 194,v3,51200
Mvaa 16,184,160
s_nop 0
Y_ 82,6656
Y_ 83,6912
Mvaa 16,188,200
s_nop 0
Ta 196,v3,53248
Ta 198,v3,55296
Mvaa 20,188,204
s_nop 0
Y_ 76,5120
Y_ 77,5376
Mvaa 20,184,164
s_nop 0
Ta 220,v3,57344
Ta 222,v3,59392
Mvaa 20,180,124
s_nop 0
Y_ 78,5632
Y_ 79,5888
Mvaa 24,180,128
buffer_load_dword v11, v8, s[92:95], s96 offen offset:384 sc1
Mvaa 24,184,168
v_cndmask_b32_e64 v10, v10, v9, s[36:37]
Mvaa 24,188,208
s_waitcnt vmcnt(14) lgkmcnt(0)
s_barrier
Mvaa 28,188,212
S_ s55,s71,122880
ds_read2st64_b32 v[96:97], v5 offset0:0 offset1:1
Mvaa 28,184,172
buffer_store_dword a255, v10, s[92:95], 0 offen offset:384 sc1
Mvaa 28,180,132
S_ m0,s51,12288
ds_read2st64_b32 v[98:99], v5 offset0:2 offset1:3
Mvaa 32,180,136
ds_read2st64_b32 v[104:105], v5 offset0:4 offset1:5
Mvaa 32,184,176
ds_read2st64_b32 v[106:107], v5 offset0:6 offset1:7
Mvaa 32,188,216
L_ v7,24,s55,0
Mvaa 52,220,216
S_ s55,s71,133120
Ta 180,%0,36864
Ta 182,%0,38912
Mvaa 52,196,176
S_ m0,s51,13312
ds_read2st64_b32 v[112:113], v5 offset0:8 offset1:9
Mvaa 52,192,136
W_ 5
Ta 184,%0,40960
Ta 186,%0,43008
Mvaa 48,192,132
L_ v7,24,s55,0
Mvaa 48,196,172
S_ s55,s71,143360
Ta 188,%0,45056
Ta 190,%0,47104
Mvaa 48,220,212
S_ m0,s51,14336
ds_read2st64_b32 v[114:115], v5 offset0:10 offset1:11
A_ v96,v16,v96
Mvaa 44,220,208
ds_read2st64_b32 v[120:121], v5 offset0:12 offset1:13
A_ v97,v17,v97
Mvaa 44,196,168
L_ v7,24,s55,0
Mvaa 44,192,128
S_ s55,s71,153600
ds_read2st64_b32 v[122:123], v5 offset0:14 offset1:15
A_ v98,v18,v98
Mvaa 40,192,124
S_ m0,s51,15360
ds_read2st64_b32 v[128:129], v5 offset0:16 offset1:17
A_ v99,v19,v99
Mvaa 40,196,164
W_ 11
ds_read2st64_b32 v[130:131], v5 offset0:18 offset1:19
A_ v104,v20,v104
Mvaa 40,220,204
L_ v7,24,s55,0
Mvaa 36,220,200
S_ s55,s71,163840
ds_read2st64_b32 v[132:133], v5 offset0:36 offset1:37
A_ v105,v21,v105
Mvaa 36,196,160
S_ m0,s51,16384
ds_read2st64_b32 v[134:135], v5 offset0:38 offset1:39
A_ v106,v22,v106
Mvaa 36,192,120
ds_read2st64_b32 v[124:125], v5 offset0:32 offset1:33
A_ v107,v23,v107
s_waitcnt vmcnt(29)
Zaaa 100,70,20
L_ v7,24,s55,0
Zaaa 140,68,20
W_ 7
ds_read2st64_b32 v[126:127], v5 offset0:34 offset1:35
A_ v112,v24,v112
Zaaa 104,70,24
S_ s55,s71,174080
ds_read2st64_b32 v[116:117], v5 offset0:28 offset1:29
A_ v113,v25,v113
Zaaa 144,68,24
S_ m0,s51,17408
ds_read2st64_b32 v[118:119], v5 offset0:30 offset1:31
A_ v114,v26,v114
Zaaa 108,70,28
L_ v7,24,s55,0
Zaaa 148,68,28
S_ s79,s6,6
ds_read2st64_b32 v[108:109], v5 offset0:24 offset1:25
A_ v115,v27,v115
Zaaa 112,70,32
W_ 9
ds_read2st64_b32 v[110:111], v5 offset0:26 offset1:27
A_ v120,v28,v120
Zaaa 152,68,32
s_and_b32 s79, s79, 7
ds_read2st64_b32 v[100:101], v5 offset0:20 offset1:21
.Lpad_o_7:
s_cbranch_execz .Lpad_o_11
A_ v121,v29,v121
Zaaa 116,70,36
s_lshl_b32 s79, s79, 7
ds_read2st64_b32 v[102:103], v5 offset0:22 offset1:23
A_ v122,v30,v122
Zaaa 156,68,36
S_ s71,s72,s79
A_ v123,v31,v123
Maaa 224,70,60
W_ 10
A_ v128,v32,v128
A_ v129,v33,v129
Maaa 228,68,60
S_ s55,s71,491520
A_ v130,v34,v130
A_ v131,v35,v131
Maaa 232,70,64
W_ 8
A_ v132,v52,v132
A_ v133,v53,v133
Maaa 236,68,64
A_ v134,v54,v134
A_ v135,v55,v135
Zvav 56,180,136
W_ 6
Ta 192,v1,36864
Ta 194,v1,38912
Mvav 56,184,156
S_ m0,s51,49152
A_ v124,v48,v124
A_ v125,v49,v125
Mvav 56,188,176
s_nop 0
Ta 196,v1,40960
Ta 198,v1,43008
Zvav 60,188,180
L_ v7,24,s55,0
Mvav 60,184,160
S_ s55,s71,501760
Ta 220,v1,45056
Ta 222,v1,47104
Mvav 60,180,140
S_ m0,s51,50176
A_ v126,v50,v126
A_ v127,v51,v127
Zvav 64,180,144
W_ 10
A_ v116,v44,v116
A_ v117,v45,v117
Mvav 64,184,164
L_ v7,24,s55,0
Mvav 64,188,184
S_ s55,s71,512000
A_ v118,v46,v118
A_ v119,v47,v119
Zvav 68,188,188
W_ 8
A_ v108,v40,v108
A_ v109,v41,v109
Mvav 68,184,168
S_ m0,s51,51200
A_ v110,v42,v110
A_ v111,v43,v111
Mvav 68,180,148
L_ v7,24,s55,0
Zvav 72,180,152
s_waitcnt vmcnt(10)
W_ 7
A_ v100,v36,v100
W_ 6
Mvav 72,184,172
S_ s55,s71,522240
v_cmp_eq_u32 vcc, s62, v11
A_ v101,v37,v101
Mvav 72,188,192
W_ 0
A_ v102,v38,v102
s_mul_i32 s76, s75, 204800
Zvav 92,220,192
S_ m0,s51,52224
Ta 180,v2,36864
Ta 182,v2,38912
Mvav 92,196,172
L_ v7,24,s55,0
Mvav 92,192,152
S_ s55,s71,532480
Ta 184,v2,40960
Ta 186,v2,43008
Zvav 88,192,148
s_bitcmp1_b32 vcc_lo, s75
s_cbranch_scc0 .Lspin1
.Lspun1:
A_ v103,v39,v103
Mvav 88,196,168
S_ s56,s76,s84
Ta 188,v2,45056
Ta 190,v2,47104
Mvav 88,220,188
buffer_load_dwordx4 a[20:23], v6, s[28:31], s56 offen offset:0 sc1
Zvav 84,220,184
S_ m0,s51,53248
C_ 131,130,131
C_ 130,128,129
Mvav 84,196,164
S_ s85,s6,8
C_ 132,132,133
C_ 133,134,135
Mvav 84,192,144
s_and_b32 s85, s85, 7
Zvav 80,192,140
buffer_load_dwordx4 a[24:27], v6, s[28:31], s56 offen offset:1024 sc1
Mvav 80,196,160
s_lshl_b32 s85, s85, 7
Mvav 80,220,180
S_ s85,s85,s87
Zvav 76,220,176
Mvav 76,196,156
buffer_load_dwordx4 a[28:31], v6, s[28:31], s56 offen offset:2048 sc1
Mvav 76,192,136
W_ 0
Zvav 16,180,136
s_nop 0
Ta 192,v3,36864
Ta 194,v3,38912
Mvav 16,184,156
Mvav 16,188,176
s_nop 0
Ta 196,v3,40960
Ta 198,v3,43008
Zvav 20,188,180
buffer_load_dwordx4 a[32:35], v6, s[28:31], s56 offen offset:3072 sc1
Mvav 20,184,160
S_ s56,s56,4096
Ta 220,v3,45056
Ta 222,v3,47104
Mvav 20,180,140
Zvav 24,180,144
Mvav 24,184,164
buffer_load_dwordx4 a[36:39], v6, s[28:31], s56 offen offset:0 sc1
Mvav 24,188,184
S_ s56,s76,s74
Zvav 28,188,188
Mvav 28,184,168
Mvav 28,180,148
buffer_load_dwordx4 a[60:63], v6, s[28:31], s56 offen offset:0 sc1
Zvav 32,180,152
S_ s56,s76,s82
Mvav 32,184,172
Mvav 32,188,192
s_waitcnt vmcnt(25) lgkmcnt(0)
s_barrier
Zvav 52,220,192
s_nop 0
Ta 180,%0,24576
Ta 182,%0,26624
Mvav 52,196,172
buffer_load_dwordx4 a[64:67], v6, s[28:31], s56 offen offset:0 sc1
Mvav 52,192,152
s_nop 0
Ta 184,%0,28672
Ta 186,%0,30720
Zvav 48,192,148
Mvav 48,196,168
s_nop 0
Ta 188,%0,32768
Ta 190,%0,34816
Mvav 48,220,188
buffer_store_dwordx4 v[130:133], v6, s[28:31], s73 offen offset:0 sc1
Zvav 44,220,184
s_sub_u32 s73, s73, 4096
Mvav 44,196,164
C_ 123,122,123
C_ 122,120,121
Mvav 44,192,144
C_ 124,124,125
C_ 125,126,127
Zvav 40,192,140
Mvav 40,196,160
Mvav 40,220,180
Zvav 36,220,176
Mvav 36,196,156
buffer_store_dwordx4 v[122:125], v6, s[28:31], s73 offen offset:3072 sc1
Mvav 36,192,136
W_ 0
C_ 115,114,115
C_ 114,112,113
Mvav 56,180,196
s_nop 0
Ta 192,v1,24576
Ta 194,v1,26624
Mvav 56,184,216
C_ 116,116,117
C_ 117,118,119
Mvav 56,188,236
s_nop 0
Ta 196,v1,28672
Ta 198,v1,30720
Mvav 60,188,240
Mvav 60,184,220
s_nop 0
Ta 220,v1,32768
Ta 222,v1,34816
Mvav 60,180,200
Mvav 64,180,204
buffer_store_dwordx4 v[114:117], v6, s[28:31], s73 offen offset:2048 sc1
Mvav 64,184,224
C_ 107,106,107
C_ 106,104,105
Mvav 64,188,244
C_ 108,108,109
C_ 109,110,111
Mvav 68,188,248
Mvav 68,184,228
Mvav 68,180,208
Mvav 72,180,212
Mvav 72,184,232
Mvav 72,188,252
W_ 0
Mvav 92,220,252
s_nop 0
Ta 180,v2,24576
Ta 182,v2,26624
Mvav 92,196,232
buffer_store_dwordx4 v[106:109], v6, s[28:31], s73 offen offset:1024 sc1
Mvav 92,192,212
s_nop 0
Ta 184,v2,28672
Ta 186,v2,30720
Mvav 88,192,208
C_ 99,98,99
C_ 98,96,97
Mvav 88,196,228
s_nop 0
Ta 188,v2,32768
Ta 190,v2,34816
Mvav 88,220,248
C_ 100,100,101
C_ 101,102,103
Mvav 84,220,244
Mvav 84,196,224
Mvav 84,192,204
Mvav 80,192,200
buffer_store_dwordx4 v[98:101], v6, s[28:31], s73 offen offset:0 sc1
Mvav 80,196,220
Mvav 80,220,240
Mvav 76,220,236
Mvav 76,196,216
Mvav 76,192,196
W_ 0
Mvav 16,180,196
s_nop 0
Ta 192,v3,24576
Ta 194,v3,26624
Mvav 16,184,216
Mvav 16,188,236
s_nop 0
Ta 196,v3,28672
Ta 198,v3,30720
Mvav 20,188,240
L_ v7,24,s55,0
Mvav 20,184,220
S_ s55,s71,542720
Ta 220,v3,32768
Ta 222,v3,34816
Mvav 20,180,200
S_ m0,s51,54272
Mvav 24,180,204
Mvav 24,184,224
L_ v7,24,s55,0
Mvav 24,188,244
S_ s55,s71,614400
Mvav 28,188,248
S_ m0,s51,36864
Mvav 28,184,228
Mvav 28,180,208
L_ v7,24,s55,0
Mvav 32,180,212
S_ s55,s71,624640
Mvav 32,184,232
S_ m0,s51,37888
Mvav 32,188,252
s_waitcnt vmcnt(28) lgkmcnt(0)
s_barrier
Mvav 52,220,252
s_nop 0
Ta 180,%0,0
Ta 182,%0,2048
Mvav 52,196,232
L_ v7,24,s55,0
Mvav 52,192,212
S_ s55,s71,634880
Ta 184,%0,4096
Ta 186,%0,6144
Mvav 48,192,208
S_ m0,s51,38912
Mvav 48,196,228
s_nop 0
Ta 188,%0,8192
Ta 190,%0,10240
Mvav 48,220,248
L_ v7,24,s55,0
Mvav 44,220,244
S_ s55,s71,645120
Mvav 44,196,224
S_ m0,s51,39936
Mvav 44,192,204
Mvav 40,192,200
L_ v7,24,s55,0
Mvav 40,196,220
S_ s55,s71,655360
Mvav 40,220,240
S_ m0,s51,40960
Mvav 36,220,236
Mvav 36,196,216
L_ v7,24,s55,0
Mvav 36,192,196
W_ 0
S_ s55,s71,665600
Mvaa 56,180,0
S_ m0,s51,41984
Ta 192,v1,0
Ta 194,v1,2048
Mvaa 56,184,40
Mvaa 56,188,80
s_nop 0
Ta 196,v1,4096
Ta 198,v1,6144
Mvaa 60,188,84
L_ v7,24,s55,0
Mvaa 60,184,44
S_ s55,s71,0
Ta 220,v1,8192
Ta 222,v1,10240
Mvaa 60,180,4
S_ m0,s51,24576
Mvaa 64,180,8
Mvaa 64,184,48
L_ v7,24,s55,0
Mvaa 64,188,88
S_ s55,s71,10240
Mvaa 68,188,240
S_ m0,s51,25600
Mvaa 68,184,52
Mvaa 68,180,12
L_ v7,24,s55,0
Mvaa 72,180,16
S_ s55,s71,20480
Mvaa 72,184,56
S_ m0,s51,26624
Mvaa 72,188,244
W_ 0
Mvaa 92,220,244
s_nop 0
Ta 180,v2,0
Ta 182,v2,2048
Mvaa 92,196,56
L_ v7,24,s55,0
Mvaa 92,192,16
S_ s55,s71,30720
Ta 184,v2,4096
Ta 186,v2,6144
Mvaa 88,192,12
S_ m0,s51,27648
Mvaa 88,196,52
s_nop 0
Ta 188,v2,8192
Ta 190,v2,10240
Mvaa 88,220,240
L_ v7,24,s55,0
Mvaa 84,220,88
S_ s55,s71,40960
Mvaa 84,196,48
S_ m0,s51,28672
Mvaa 84,192,8
Mvaa 80,192,4
L_ v7,24,s55,0
Mvaa 80,196,44
S_ s55,s71,51200
Mvaa 80,220,84
S_ m0,s51,29696
Mvaa 76,220,80
Mvaa 76,196,40
L_ v7,24,s55,0
Mvaa 76,192,0
W_ 0
Mvaa 16,180,0
s_nop 0
Ta 192,v3,0
Ta 194,v3,2048
Mvaa 16,184,40
Mvaa 16,188,80
s_nop 0
Ta 196,v3,4096
Ta 198,v3,6144
Mvaa 20,188,84
buffer_load_dword a74, v4, s[24:27], s85 offen
Mvaa 20,184,44
s_nop 0
Ta 220,v3,8192
Ta 222,v3,10240
Mvaa 20,180,4
Mvaa 24,180,8
Mvaa 24,184,48
Mvaa 24,188,88
Mvaa 28,188,240
Mvaa 28,184,52
Mvaa 28,180,12
Mvaa 32,180,16
Mvaa 32,184,56
Mvaa 32,188,244
s_waitcnt vmcnt(31) lgkmcnt(0)
s_barrier
Mvaa 52,220,244
s_nop 0
Ta 180,%0,12288
Ta 182,%0,14336
Mvaa 52,196,56
Mvaa 52,192,16
s_nop 0
Ta 184,%0,16384
Ta 186,%0,18432
Mvaa 48,192,12
Mvaa 48,196,52
s_nop 0
Ta 188,%0,20480
Ta 190,%0,22528
Mvaa 48,220,240
Mvaa 44,220,88
Mvaa 44,196,48
Mvaa 44,192,8
Mvaa 40,192,4
Mvaa 40,196,44
Mvaa 40,220,84
Mvaa 36,220,80
Mvaa 36,196,40
Mvaa 36,192,0
W_ 0
Mvaa 56,180,120
S_ s81,s6,5
Ta 192,v1,12288
Ta 194,v1,14336
Mvaa 56,184,160
s_and_b32 s81, s81, 7
Mvaa 56,188,200
s_mul_i32 s81, s81, 20480
Ta 196,v1,16384
Ta 198,v1,18432
Mvaa 60,188,204
s_mov_b32 m0, s52
S_ s73,s81,s78
Mvaa 60,184,164
S_ s73,s73,4096
Ta 220,v1,20480
Ta 222,v1,22528
Mvaa 60,180,124
s_nop 0
Y_ 56,0
Y_ 57,256
Mvaa 64,180,128
s_nop 0
Y_ 58,512
Y_ 59,768
Mvaa 64,184,168
Mvaa 64,188,208
s_nop 0
Y_ 60,1024
Y_ 61,1280
Mvaa 68,188,212
s_nop 0
Y_ 62,1536
Y_ 63,1792
Mvaa 68,184,172
Mvaa 68,180,132
s_nop 0
Y_ 64,2048
Y_ 65,2304
Mvaa 72,180,136
s_nop 0
Y_ 66,2560
Y_ 67,2816
Mvaa 72,184,176
Mvaa 72,188,216
W_ 12
Y_ 68,3072
Y_ 69,3328
Mvaa 92,220,216
s_nop 0
Ta 180,v2,12288
Ta 182,v2,14336
Mvaa 92,196,176
s_nop 0
Y_ 70,3584
Y_ 71,3840
Mvaa 92,192,136
s_nop 0
Ta 184,v2,16384
Ta 186,v2,18432
Mvaa 88,192,132
s_nop 0
Y_ 72,4096
Y_ 73,4352
Mvaa 88,196,172
S_ s83,s6,4
Ta 188,v2,20480
Ta 190,v2,22528
Mvaa 88,220,212
s_and_b32 s83, s83, 7
Y_ 74,4608
Y_ 75,4864
Mvaa 84,220,208
s_lshl_b32 s83, s83, 2
Y_ 92,9216
Y_ 93,9472
Mvaa 84,196,168
s_nop 0
Y_ 94,9728
Y_ 95,9984
Mvaa 84,192,128
s_nop 0
Y_ 88,8192
Y_ 89,8448
Mvaa 80,192,124
s_nop 0
Y_ 90,8704
Y_ 91,8960
Mvaa 80,196,164
v_cmp_eq_u32_e64 s[36:37], s83, v8
Mvaa 80,220,204
s_sub_u32 s75, s6, 3
Y_ 84,7168
Y_ 85,7424
Mvaa 76,220,200
s_and_b32 s75, s75, 7
Y_ 86,7680
Y_ 87,7936
Mvaa 76,196,160
v_mov_b32 v10, 0x7fff0000
Mvaa 76,192,120
W_ 14
Y_ 80,6144
Y_ 81,6400
Mvaa 16,180,120
s_nop 0
Ta 192,v3,12288
Ta 194,v3,14336
Mvaa 16,184,160
s_nop 0
Y_ 82,6656
Y_ 83,6912
Mvaa 16,188,200
s_nop 0
Ta 196,v3,16384
Ta 198,v3,18432
Mvaa 20,188,204
s_nop 0
Y_ 76,5120
Y_ 77,5376
Mvaa 20,184,164
s_nop 0
Ta 220,v3,20480
Ta 222,v3,22528
Mvaa 20,180,124
s_nop 0
Y_ 78,5632
Y_ 79,5888
Mvaa 24,180,128
buffer_load_dword v11, v8, s[92:95], s96 offen offset:384 sc1
Mvaa 24,184,168
.Lpad_o_8:
s_cbranch_execz .Lhop_o_r
v_cndmask_b32_e64 v10, v10, v9, s[36:37]
Mvaa 24,188,208
s_waitcnt vmcnt(14) lgkmcnt(0)
s_barrier
Mvaa 28,188,212
S_ s55,s71,122880
ds_read2st64_b32 v[96:97], v5 offset0:0 offset1:1
Mvaa 28,184,172
buffer_store_dword a255, v10, s[92:95], 0 offen offset:384 sc1
Mvaa 28,180,132
S_ m0,s51,0
ds_read2st64_b32 v[98:99], v5 offset0:2 offset1:3
Mvaa 32,180,136
ds_read2st64_b32 v[104:105], v5 offset0:4 offset1:5
Mvaa 32,184,176
ds_read2st64_b32 v[106:107], v5 offset0:6 offset1:7
Mvaa 32,188,216
L_ v7,24,s55,0
Mvaa 52,220,216
S_ s55,s71,133120
Ta 180,%0,49152
Ta 182,%0,51200
Mvaa 52,196,176
S_ m0,s51,1024
ds_read2st64_b32 v[112:113], v5 offset0:8 offset1:9
Mvaa 52,192,136
W_ 5
Ta 184,%0,53248
Ta 186,%0,55296
Mvaa 48,192,132
L_ v7,24,s55,0
Mvaa 48,196,172
S_ s55,s71,143360
Ta 188,%0,57344
Ta 190,%0,59392
Mvaa 48,220,212
S_ m0,s51,2048
ds_read2st64_b32 v[114:115], v5 offset0:10 offset1:11
A_ v96,v16,v96
Mvaa 44,220,208
ds_read2st64_b32 v[120:121], v5 offset0:12 offset1:13
A_ v97,v17,v97
Mvaa 44,196,168
L_ v7,24,s55,0
Mvaa 44,192,128
S_ s55,s71,153600
ds_read2st64_b32 v[122:123], v5 offset0:14 offset1:15
A_ v98,v18,v98
Mvaa 40,192,124
S_ m0,s51,3072
ds_read2st64_b32 v[128:129], v5 offset0:16 offset1:17
A_ v99,v19,v99
Mvaa 40,196,164
W_ 11
ds_read2st64_b32 v[130:131], v5 offset0:18 offset1:19
A_ v104,v20,v104
Mvaa 40,220,204
L_ v7,24,s55,0
Mvaa 36,220,200
S_ s55,s71,163840
ds_read2st64_b32 v[132:133], v5 offset0:36 offset1:37
A_ v105,v21,v105
Mvaa 36,196,160
S_ m0,s51,4096
ds_read2st64_b32 v[134:135], v5 offset0:38 offset1:39
A_ v106,v22,v106
Mvaa 36,192,120
ds_read2st64_b32 v[124:125], v5 offset0:32 offset1:33
A_ v107,v23,v107
s_waitcnt vmcnt(26)
Maaa 100,70,20
L_ v7,24,s55,0
Maaa 140,68,20
W_ 7
ds_read2st64_b32 v[126:127], v5 offset0:34 offset1:35
A_ v112,v24,v112
Maaa 104,70,24
S_ s55,s71,174080
ds_read2st64_b32 v[116:117], v5 offset0:28 offset1:29
A_ v113,v25,v113
Maaa 144,68,24
S_ m0,s51,5120
ds_read2st64_b32 v[118:119], v5 offset0:30 offset1:31
A_ v114,v26,v114
Maaa 108,70,28
L_ v7,24,s55,0
Maaa 148,68,28
S_ s79,s6,7
ds_read2st64_b32 v[108:109], v5 offset0:24 offset1:25
A_ v115,v27,v115
Maaa 112,70,32
W_ 9
ds_read2st64_b32 v[110:111], v5 offset0:26 offset1:27
A_ v120,v28,v120
Maaa 152,68,32
s_and_b32 s79, s79, 7
ds_read2st64_b32 v[100:101], v5 offset0:20 offset1:21
A_ v121,v29,v121
Maaa 116,70,36
s_lshl_b32 s79, s79, 7
ds_read2st64_b32 v[102:103], v5 offset0:22 offset1:23
A_ v122,v30,v122
Maaa 156,68,36
S_ s71,s72,s79
A_ v123,v31,v123
Maaa 224,70,60
W_ 10
A_ v128,v32,v128
A_ v129,v33,v129
Maaa 228,68,60
S_ s55,s71,491520
A_ v130,v34,v130
A_ v131,v35,v131
Maaa 232,70,64
W_ 8
A_ v132,v52,v132
A_ v133,v53,v133
Maaa 236,68,64
A_ v134,v54,v134
A_ v135,v55,v135
Zvav 56,180,136
W_ 6
Ta 192,v1,49152
Ta 194,v1,51200
Mvav 56,184,156
S_ m0,s51,12288
A_ v124,v48,v124
A_ v125,v49,v125
Mvav 56,188,176
s_nop 0
Ta 196,v1,53248
Ta 198,v1,55296
Zvav 60,188,180
L_ v7,24,s55,0
Mvav 60,184,160
S_ s55,s71,501760
Ta 220,v1,57344
Ta 222,v1,59392
Mvav 60,180,140
S_ m0,s51,13312
A_ v126,v50,v126
A_ v127,v51,v127
Zvav 64,180,144
W_ 10
A_ v116,v44,v116
A_ v117,v45,v117
Mvav 64,184,164
L_ v7,24,s55,0
Mvav 64,188,184
S_ s55,s71,512000
A_ v118,v46,v118
A_ v119,v47,v119
Zvav 68,188,188
W_ 8
A_ v108,v40,v108
A_ v109,v41,v109
Mvav 68,184,168
S_ m0,s51,14336
A_ v110,v42,v110
A_ v111,v43,v111
Mvav 68,180,148
L_ v7,24,s55,0
Zvav 72,180,152
s_waitcnt vmcnt(10)
W_ 7
A_ v100,v36,v100
W_ 6
Mvav 72,184,172
S_ s55,s71,522240
v_cmp_eq_u32 vcc, s62, v11
A_ v101,v37,v101
Mvav 72,188,192
W_ 0
A_ v102,v38,v102
s_mul_i32 s76, s75, 204800
Zvav 92,220,192
S_ m0,s51,15360
Ta 180,v2,49152
Ta 182,v2,51200
Mvav 92,196,172
L_ v7,24,s55,0
Mvav 92,192,152
S_ s55,s71,532480
Ta 184,v2,53248
Ta 186,v2,55296
Zvav 88,192,148
s_bitcmp1_b32 vcc_lo, s75
s_cbranch_scc0 .Lspin2
.Lspun2:
A_ v103,v39,v103
Mvav 88,196,168
S_ s56,s76,s84
Ta 188,v2,57344
Ta 190,v2,59392
Mvav 88,220,188
buffer_load_dwordx4 a[20:23], v6, s[28:31], s56 offen offset:0 sc1
Zvav 84,220,184
S_ m0,s51,16384
C_ 131,130,131
C_ 130,128,129
Mvav 84,196,164
C_ 132,132,133
C_ 133,134,135
Mvav 84,192,144
Zvav 80,192,140
buffer_load_dwordx4 a[24:27], v6, s[28:31], s56 offen offset:1024 sc1
Mvav 80,196,160
Mvav 80,220,180
Zvav 76,220,176
Mvav 76,196,156
buffer_load_dwordx4 a[28:31], v6, s[28:31], s56 offen offset:2048 sc1
Mvav 76,192,136
W_ 0
Zvav 16,180,136
s_nop 0
Ta 192,v3,49152
Ta 194,v3,51200
Mvav 16,184,156
Mvav 16,188,176
s_nop 0
Ta 196,v3,53248
Ta 198,v3,55296
Zvav 20,188,180
buffer_load_dwordx4 a[32:35], v6, s[28:31], s56 offen offset:3072 sc1
Mvav 20,184,160
S_ s56,s56,4096
Ta 220,v3,57344
Ta 222,v3,59392
Mvav 20,180,140
Zvav 24,180,144
Mvav 24,184,164
buffer_load_dwordx4 a[36:39], v6, s[28:31], s56 offen offset:0 sc1
Mvav 24,188,184
S_ s56,s76,s74
Zvav 28,188,188
Mvav 28,184,168
Mvav 28,180,148
buffer_load_dwordx4 a[60:63], v6, s[28:31], s56 offen offset:0 sc1
Zvav 32,180,152
S_ s56,s76,s82
Mvav 32,184,172
Mvav 32,188,192
s_waitcnt vmcnt(25) lgkmcnt(0)
s_barrier
Zvav 52,220,192
s_nop 0
Ta 180,%0,36864
Ta 182,%0,38912
Mvav 52,196,172
buffer_load_dwordx4 a[64:67], v6, s[28:31], s56 offen offset:0 sc1
Mvav 52,192,152
s_nop 0
Ta 184,%0,40960
Ta 186,%0,43008
Zvav 48,192,148
Mvav 48,196,168
s_nop 0
Ta 188,%0,45056
Ta 190,%0,47104
Mvav 48,220,188
buffer_store_dwordx4 v[130:133], v6, s[28:31], s73 offen offset:0 sc1
Zvav 44,220,184
s_sub_u32 s73, s73, 4096
Mvav 44,196,164
C_ 123,122,123
C_ 122,120,121
Mvav 44,192,144
C_ 124,124,125
C_ 125,126,127
Zvav 40,192,140
Mvav 40,196,160
Mvav 40,220,180
Zvav 36,220,176
Mvav 36,196,156
buffer_store_dwordx4 v[122:125], v6, s[28:31], s73 offen offset:3072 sc1
Mvav 36,192,136
W_ 0
C_ 115,114,115
C_ 114,112,113
Mvav 56,180,196
s_nop 0
Ta 192,v1,36864
Ta 194,v1,38912
Mvav 56,184,216
C_ 116,116,117
C_ 117,118,119
Mvav 56,188,236
s_nop 0
Ta 196,v1,40960
Ta 198,v1,43008
Mvav 60,188,240
Mvav 60,184,220
s_nop 0
Ta 220,v1,45056
Ta 222,v1,47104
Mvav 60,180,200
Mvav 64,180,204
buffer_store_dwordx4 v[114:117], v6, s[28:31], s73 offen offset:2048 sc1
Mvav 64,184,224
C_ 107,106,107
C_ 106,104,105
Mvav 64,188,244
C_ 108,108,109
C_ 109,110,111
Mvav 68,188,248
Mvav 68,184,228
Mvav 68,180,208
Mvav 72,180,212
Mvav 72,184,232
Mvav 72,188,252
W_ 0
Mvav 92,220,252
s_nop 0
Ta 180,v2,36864
Ta 182,v2,38912
Mvav 92,196,232
buffer_store_dwordx4 v[106:109], v6, s[28:31], s73 offen offset:1024 sc1
Mvav 92,192,212
s_nop 0
Ta 184,v2,40960
Ta 186,v2,43008
Mvav 88,192,208
C_ 99,98,99
C_ 98,96,97
Mvav 88,196,228
s_nop 0
Ta 188,v2,45056
Ta 190,v2,47104
Mvav 88,220,248
C_ 100,100,101
C_ 101,102,103
Mvav 84,220,244
Mvav 84,196,224
Mvav 84,192,204
Mvav 80,192,200
buffer_store_dwordx4 v[98:101], v6, s[28:31], s73 offen offset:0 sc1
Mvav 80,196,220
Mvav 80,220,240
Mvav 76,220,236
Mvav 76,196,216
Mvav 76,192,196
W_ 0
Mvav 16,180,196
s_nop 0
Ta 192,v3,36864
Ta 194,v3,38912
Mvav 16,184,216
Mvav 16,188,236
s_nop 0
Ta 196,v3,40960
Ta 198,v3,43008
Mvav 20,188,240
L_ v7,24,s55,0
Mvav 20,184,220
S_ s55,s71,542720
Ta 220,v3,45056
Ta 222,v3,47104
Mvav 20,180,200
S_ m0,s51,17408
Mvav 24,180,204
Mvav 24,184,224
L_ v7,24,s55,0
Mvav 24,188,244
S_ s55,s71,614400
Mvav 28,188,248
S_ m0,s51,49152
Mvav 28,184,228
Mvav 28,180,208
L_ v7,24,s55,0
Mvav 32,180,212
S_ s55,s71,624640
Mvav 32,184,232
S_ m0,s51,50176
Mvav 32,188,252
s_waitcnt vmcnt(28) lgkmcnt(0)
s_barrier
Mvav 52,220,252
s_nop 0
Ta 180,%0,24576
Ta 182,%0,26624
Mvav 52,196,232
L_ v7,24,s55,0
Mvav 52,192,212
S_ s55,s71,634880
Ta 184,%0,28672
Ta 186,%0,30720
Mvav 48,192,208
S_ m0,s51,51200
Mvav 48,196,228
s_nop 0
Ta 188,%0,32768
Ta 190,%0,34816
Mvav 48,220,248
L_ v7,24,s55,0
Mvav 44,220,244
S_ s55,s71,645120
Mvav 44,196,224
S_ m0,s51,52224
Mvav 44,192,204
Mvav 40,192,200
L_ v7,24,s55,0
Mvav 40,196,220
S_ s55,s71,655360
Mvav 40,220,240
S_ m0,s51,53248
Mvav 36,220,236
Mvav 36,196,216
L_ v7,24,s55,0
Mvav 36,192,196
W_ 0
S_ s55,s71,665600
Mvaa 56,180,0
S_ m0,s51,54272
Ta 192,v1,24576
Ta 194,v1,26624
Mvaa 56,184,40
Mvaa 56,188,80
s_nop 0
Ta 196,v1,28672
Ta 198,v1,30720
Mvaa 60,188,84
L_ v7,24,s55,0
Mvaa 60,184,44
S_ s55,s71,0
Ta 220,v1,32768
Ta 222,v1,34816
Mvaa 60,180,4
S_ m0,s51,36864
Mvaa 64,180,8
Mvaa 64,184,48
L_ v7,24,s55,0
Mvaa 64,188,88
S_ s55,s71,10240
Mvaa 68,188,240
S_ m0,s51,37888
Mvaa 68,184,52
Mvaa 68,180,12
L_ v7,24,s55,0
Mvaa 72,180,16
S_ s55,s71,20480
Mvaa 72,184,56
S_ m0,s51,38912
Mvaa 72,188,244
W_ 0
Mvaa 92,220,244
s_nop 0
Ta 180,v2,24576
Ta 182,v2,26624
Mvaa 92,196,56
L_ v7,24,s55,0
Mvaa 92,192,16
S_ s55,s71,30720
Ta 184,v2,28672
Ta 186,v2,30720
Mvaa 88,192,12
S_ m0,s51,39936
Mvaa 88,196,52
s_nop 0
Ta 188,v2,32768
Ta 190,v2,34816
Mvaa 88,220,240
L_ v7,24,s55,0
Mvaa 84,220,88
S_ s55,s71,40960
Mvaa 84,196,48
S_ m0,s51,40960
Mvaa 84,192,8
Mvaa 80,192,4
L_ v7,24,s55,0
Mvaa 80,196,44
S_ s55,s71,51200
Mvaa 80,220,84
S_ m0,s51,41984
Mvaa 76,220,80
Mvaa 76,196,40
L_ v7,24,s55,0
Mvaa 76,192,0
W_ 0
Mvaa 16,180,0
s_nop 0
Ta 192,v3,24576
Ta 194,v3,26624
Mvaa 16,184,40
Mvaa 16,188,80
s_nop 0
Ta 196,v3,28672
Ta 198,v3,30720
Mvaa 20,188,84
Mvaa 20,184,44
s_nop 0
Ta 220,v3,32768
Ta 222,v3,34816
Mvaa 20,180,4
Mvaa 24,180,8
Mvaa 24,184,48
Mvaa 24,188,88
Mvaa 28,188,240
Mvaa 28,184,52
Mvaa 28,180,12
Mvaa 32,180,16
Mvaa 32,184,56
Mvaa 32,188,244
s_waitcnt vmcnt(30) lgkmcnt(0)
s_barrier
Mvaa 52,220,244
s_nop 0
Ta 180,%0,0
Ta 182,%0,2048
Mvaa 52,196,56
Mvaa 52,192,16
s_nop 0
Ta 184,%0,4096
Ta 186,%0,6144
Mvaa 48,192,12
Mvaa 48,196,52
s_nop 0
Ta 188,%0,8192
Ta 190,%0,10240
Mvaa 48,220,240
Mvaa 44,220,88
Mvaa 44,196,48
Mvaa 44,192,8
Mvaa 40,192,4
Mvaa 40,196,44
Mvaa 40,220,84
Mvaa 36,220,80
Mvaa 36,196,40
Mvaa 36,192,0
W_ 0
Mvaa 56,180,120
S_ s81,s6,6
Ta 192,v1,0
Ta 194,v1,2048
Mvaa 56,184,160
s_and_b32 s81, s81, 7
Mvaa 56,188,200
s_mul_i32 s81, s81, 20480
Ta 196,v1,4096
Ta 198,v1,6144
Mvaa 60,188,204
s_mov_b32 m0, s52
S_ s73,s81,s78
Mvaa 60,184,164
S_ s73,s73,4096
Ta 220,v1,8192
Ta 222,v1,10240
Mvaa 60,180,124
s_nop 0
Y_ 56,0
Y_ 57,256
Mvaa 64,180,128
s_nop 0
Y_ 58,512
Y_ 59,768
Mvaa 64,184,168
Mvaa 64,188,208
s_nop 0
Y_ 60,1024
Y_ 61,1280
Mvaa 68,188,212
s_nop 0
Y_ 62,1536
Y_ 63,1792
Mvaa 68,184,172
Mvaa 68,180,132
s_nop 0
Y_ 64,2048
Y_ 65,2304
Mvaa 72,180,136
.Lpad_o_9:
s_cbranch_execz .Lhop_o_r
Y_ 66,2560
Y_ 67,2816
Mvaa 72,184,176
Mvaa 72,188,216
W_ 12
Y_ 68,3072
Y_ 69,3328
Mvaa 92,220,216
s_nop 0
Ta 180,v2,0
Ta 182,v2,2048
Mvaa 92,196,176
s_nop 0
Y_ 70,3584
Y_ 71,3840
Mvaa 92,192,136
s_nop 0
Ta 184,v2,4096
Ta 186,v2,6144
Mvaa 88,192,132
s_nop 0
Y_ 72,4096
Y_ 73,4352
Mvaa 88,196,172
S_ s83,s6,5
Ta 188,v2,8192
Ta 190,v2,10240
Mvaa 88,220,212
s_and_b32 s83, s83, 7
Y_ 74,4608
Y_ 75,4864
Mvaa 84,220,208
s_lshl_b32 s83, s83, 2
Y_ 92,9216
Y_ 93,9472
Mvaa 84,196,168
s_nop 0
Y_ 94,9728
Y_ 95,9984
Mvaa 84,192,128
s_nop 0
Y_ 88,8192
Y_ 89,8448
Mvaa 80,192,124
s_nop 0
Y_ 90,8704
Y_ 91,8960
Mvaa 80,196,164
v_cmp_eq_u32_e64 s[36:37], s83, v8
Mvaa 80,220,204
s_sub_u32 s75, s6, 4
Y_ 84,7168
Y_ 85,7424
Mvaa 76,220,200
s_and_b32 s75, s75, 7
Y_ 86,7680
Y_ 87,7936
Mvaa 76,196,160
v_mov_b32 v10, 0x7fff0000
Mvaa 76,192,120
W_ 14
Y_ 80,6144
Y_ 81,6400
Mvaa 16,180,120
s_nop 0
Ta 192,v3,0
Ta 194,v3,2048
Mvaa 16,184,160
s_nop 0
Y_ 82,6656
Y_ 83,6912
Mvaa 16,188,200
s_nop 0
Ta 196,v3,4096
Ta 198,v3,6144
Mvaa 20,188,204
s_nop 0
Y_ 76,5120
Y_ 77,5376
Mvaa 20,184,164
s_nop 0
Ta 220,v3,8192
Ta 222,v3,10240
Mvaa 20,180,124
s_nop 0
Y_ 78,5632
Y_ 79,5888
)A"
R"A(Mvaa 24,180,128
buffer_load_dword v11, v8, s[92:95], s96 offen offset:384 sc1
Mvaa 24,184,168
v_cndmask_b32_e64 v10, v10, v9, s[36:37]
Mvaa 24,188,208
s_waitcnt vmcnt(13) lgkmcnt(0)
s_barrier
Mvaa 28,188,212
S_ s55,s71,122880
ds_read2st64_b32 v[96:97], v5 offset0:0 offset1:1
Mvaa 28,184,172
buffer_store_dword a255, v10, s[92:95], 0 offen offset:384 sc1
Mvaa 28,180,132
S_ m0,s51,24576
ds_read2st64_b32 v[98:99], v5 offset0:2 offset1:3
Mvaa 32,180,136
ds_read2st64_b32 v[104:105], v5 offset0:4 offset1:5
Mvaa 32,184,176
ds_read2st64_b32 v[106:107], v5 offset0:6 offset1:7
Mvaa 32,188,216
L_ v7,24,s55,0
Mvaa 52,220,216
S_ s55,s71,133120
Ta 180,%0,12288
Ta 182,%0,14336
Mvaa 52,196,176
S_ m0,s51,25600
ds_read2st64_b32 v[112:113], v5 offset0:8 offset1:9
Mvaa 52,192,136
W_ 5
Ta 184,%0,16384
Ta 186,%0,18432
Mvaa 48,192,132
L_ v7,24,s55,0
Mvaa 48,196,172
S_ s55,s71,143360
Ta 188,%0,20480
Ta 190,%0,22528
Mvaa 48,220,212
S_ m0,s51,26624
ds_read2st64_b32 v[114:115], v5 offset0:10 offset1:11
A_ v96,v16,v96
Mvaa 44,220,208
ds_read2st64_b32 v[120:121], v5 offset0:12 offset1:13
A_ v97,v17,v97
Mvaa 44,196,168
L_ v7,24,s55,0
Mvaa 44,192,128
S_ s55,s71,153600
ds_read2st64_b32 v[122:123], v5 offset0:14 offset1:15
A_ v98,v18,v98
Mvaa 40,192,124
S_ m0,s51,27648
ds_read2st64_b32 v[128:129], v5 offset0:16 offset1:17
A_ v99,v19,v99
Mvaa 40,196,164
W_ 11
ds_read2st64_b32 v[130:131], v5 offset0:18 offset1:19
A_ v104,v20,v104
Mvaa 40,220,204
L_ v7,24,s55,0
Mvaa 36,220,200
S_ s55,s71,163840
ds_read2st64_b32 v[132:133], v5 offset0:36 offset1:37
A_ v105,v21,v105
Mvaa 36,196,160
S_ m0,s51,28672
ds_read2st64_b32 v[134:135], v5 offset0:38 offset1:39
A_ v106,v22,v106
Mvaa 36,192,120
ds_read2st64_b32 v[124:125], v5 offset0:32 offset1:33
A_ v107,v23,v107
s_waitcnt vmcnt(25)
Maaa 100,70,20
L_ v7,24,s55,0
Maaa 140,68,20
W_ 7
ds_read2st64_b32 v[126:127], v5 offset0:34 offset1:35
A_ v112,v24,v112
Maaa 104,70,24
S_ s55,s71,174080
ds_read2st64_b32 v[116:117], v5 offset0:28 offset1:29
A_ v113,v25,v113
Maaa 144,68,24
S_ m0,s51,29696
ds_read2st64_b32 v[118:119], v5 offset0:30 offset1:31
A_ v114,v26,v114
Maaa 108,70,28
L_ v7,24,s55,0
Maaa 148,68,28
S_ s79,s6,8
ds_read2st64_b32 v[108:109], v5 offset0:24 offset1:25
A_ v115,v27,v115
Maaa 112,70,32
W_ 9
ds_read2st64_b32 v[110:111], v5 offset0:26 offset1:27
A_ v120,v28,v120
Maaa 152,68,32
s_and_b32 s79, s79, 7
ds_read2st64_b32 v[100:101], v5 offset0:20 offset1:21
A_ v121,v29,v121
Maaa 116,70,36
s_lshl_b32 s79, s79, 7
ds_read2st64_b32 v[102:103], v5 offset0:22 offset1:23
A_ v122,v30,v122
Maaa 156,68,36
S_ s71,s72,s79
A_ v123,v31,v123
Maaa 224,70,60
W_ 10
A_ v128,v32,v128
A_ v129,v33,v129
Maaa 228,68,60
S_ s55,s71,491520
A_ v130,v34,v130
A_ v131,v35,v131
Maaa 232,70,64
W_ 8
A_ v132,v52,v132
A_ v133,v53,v133
Maaa 236,68,64
A_ v134,v54,v134
A_ v135,v55,v135
Zvav 56,180,136
W_ 6
Ta 192,v1,12288
Ta 194,v1,14336
Mvav 56,184,156
S_ m0,s51,0
A_ v124,v48,v124
A_ v125,v49,v125
Mvav 56,188,176
s_nop 0
Ta 196,v1,16384
Ta 198,v1,18432
Zvav 60,188,180
L_ v7,24,s55,0
Mvav 60,184,160
S_ s55,s71,501760
Ta 220,v1,20480
Ta 222,v1,22528
Mvav 60,180,140
S_ m0,s51,1024
A_ v126,v50,v126
A_ v127,v51,v127
Zvav 64,180,144
W_ 10
A_ v116,v44,v116
A_ v117,v45,v117
Mvav 64,184,164
L_ v7,24,s55,0
Mvav 64,188,184
S_ s55,s71,512000
A_ v118,v46,v118
A_ v119,v47,v119
Zvav 68,188,188
W_ 8
A_ v108,v40,v108
A_ v109,v41,v109
Mvav 68,184,168
S_ m0,s51,2048
A_ v110,v42,v110
A_ v111,v43,v111
Mvav 68,180,148
L_ v7,24,s55,0
Zvav 72,180,152
s_waitcnt vmcnt(10)
W_ 7
A_ v100,v36,v100
W_ 6
Mvav 72,184,172
S_ s55,s71,522240
v_cmp_eq_u32 vcc, s62, v11
A_ v101,v37,v101
Mvav 72,188,192
W_ 0
A_ v102,v38,v102
s_mul_i32 s76, s75, 204800
Zvav 92,220,192
S_ m0,s51,3072
Ta 180,v2,12288
Ta 182,v2,14336
Mvav 92,196,172
L_ v7,24,s55,0
Mvav 92,192,152
S_ s55,s71,532480
Ta 184,v2,16384
Ta 186,v2,18432
Zvav 88,192,148
s_bitcmp1_b32 vcc_lo, s75
s_cbranch_scc0 .Lspin3
.Lspun3:
A_ v103,v39,v103
Mvav 88,196,168
S_ s56,s76,s84
Ta 188,v2,20480
Ta 190,v2,22528
Mvav 88,220,188
buffer_load_dwordx4 a[20:23], v6, s[28:31], s56 offen offset:0 sc1
Zvav 84,220,184
S_ m0,s51,4096
C_ 131,130,131
C_ 130,128,129
Mvav 84,196,164
C_ 132,132,133
C_ 133,134,135
Mvav 84,192,144
Zvav 80,192,140
buffer_load_dwordx4 a[24:27], v6, s[28:31], s56 offen offset:1024 sc1
Mvav 80,196,160
Mvav 80,220,180
Zvav 76,220,176
Mvav 76,196,156
buffer_load_dwordx4 a[28:31], v6, s[28:31], s56 offen offset:2048 sc1
Mvav 76,192,136
W_ 0
Zvav 16,180,136
s_nop 0
Ta 192,v3,12288
Ta 194,v3,14336
Mvav 16,184,156
Mvav 16,188,176
s_nop 0
Ta 196,v3,16384
Ta 198,v3,18432
Zvav 20,188,180
buffer_load_dwordx4 a[32:35], v6, s[28:31], s56 offen offset:3072 sc1
Mvav 20,184,160
S_ s56,s56,4096
Ta 220,v3,20480
Ta 222,v3,22528
Mvav 20,180,140
Zvav 24,180,144
Mvav 24,184,164
buffer_load_dwordx4 a[36:39], v6, s[28:31], s56 offen offset:0 sc1
Mvav 24,188,184
S_ s56,s76,s74
Zvav 28,188,188
Mvav 28,184,168
Mvav 28,180,148
buffer_load_dwordx4 a[60:63], v6, s[28:31], s56 offen offset:0 sc1
Zvav 32,180,152
S_ s56,s76,s82
Mvav 32,184,172
Mvav 32,188,192
s_waitcnt vmcnt(24) lgkmcnt(0)
s_barrier
Zvav 52,220,192
s_nop 0
Ta 180,%0,49152
Ta 182,%0,51200
Mvav 52,196,172
buffer_load_dwordx4 a[64:67], v6, s[28:31], s56 offen offset:0 sc1
Mvav 52,192,152
s_nop 0
Ta 184,%0,53248
Ta 186,%0,55296
Zvav 48,192,148
Mvav 48,196,168
s_nop 0
Ta 188,%0,57344
Ta 190,%0,59392
Mvav 48,220,188
buffer_store_dwordx4 v[130:133], v6, s[28:31], s73 offen offset:0 sc1
Zvav 44,220,184
s_sub_u32 s73, s73, 4096
Mvav 44,196,164
C_ 123,122,123
C_ 122,120,121
Mvav 44,192,144
C_ 124,124,125
C_ 125,126,127
Zvav 40,192,140
Mvav 40,196,160
Mvav 40,220,180
Zvav 36,220,176
Mvav 36,196,156
buffer_store_dwordx4 v[122:125], v6, s[28:31], s73 offen offset:3072 sc1
Mvav 36,192,136
W_ 0
C_ 115,114,115
C_ 114,112,113
Mvav 56,180,196
s_nop 0
Ta 192,v1,49152
Ta 194,v1,51200
Mvav 56,184,216
C_ 116,116,117
C_ 117,118,119
Mvav 56,188,236
s_nop 0
Ta 196,v1,53248
Ta 198,v1,55296
Mvav 60,188,240
Mvav 60,184,220
s_nop 0
Ta 220,v1,57344
Ta 222,v1,59392
Mvav 60,180,200
Mvav 64,180,204
buffer_store_dwordx4 v[114:117], v6, s[28:31], s73 offen offset:2048 sc1
Mvav 64,184,224
C_ 107,106,107
C_ 106,104,105
Mvav 64,188,244
C_ 108,108,109
C_ 109,110,111
Mvav 68,188,248
Mvav 68,184,228
Mvav 68,180,208
Mvav 72,180,212
Mvav 72,184,232
Mvav 72,188,252
W_ 0
Mvav 92,220,252
s_nop 0
Ta 180,v2,49152
Ta 182,v2,51200
Mvav 92,196,232
buffer_store_dwordx4 v[106:109], v6, s[28:31], s73 offen offset:1024 sc1
Mvav 92,192,212
s_nop 0
Ta 184,v2,53248
Ta 186,v2,55296
Mvav 88,192,208
C_ 99,98,99
C_ 98,96,97
Mvav 88,196,228
s_nop 0
Ta 188,v2,57344
Ta 190,v2,59392
Mvav 88,220,248
C_ 100,100,101
C_ 101,102,103
Mvav 84,220,244
Mvav 84,196,224
Mvav 84,192,204
Mvav 80,192,200
buffer_store_dwordx4 v[98:101], v6, s[28:31], s73 offen offset:0 sc1
Mvav 80,196,220
Mvav 80,220,240
Mvav 76,220,236
Mvav 76,196,216
Mvav 76,192,196
W_ 0
Mvav 16,180,196
s_nop 0
Ta 192,v3,49152
Ta 194,v3,51200
Mvav 16,184,216
Mvav 16,188,236
s_nop 0
Ta 196,v3,53248
Ta 198,v3,55296
Mvav 20,188,240
L_ v7,24,s55,0
Mvav 20,184,220
S_ s55,s71,542720
Ta 220,v3,57344
Ta 222,v3,59392
Mvav 20,180,200
S_ m0,s51,5120
Mvav 24,180,204
Mvav 24,184,224
L_ v7,24,s55,0
Mvav 24,188,244
S_ s55,s71,614400
Mvav 28,188,248
S_ m0,s51,12288
Mvav 28,184,228
Mvav 28,180,208
L_ v7,24,s55,0
Mvav 32,180,212
S_ s55,s71,624640
Mvav 32,184,232
S_ m0,s51,13312
Mvav 32,188,252
s_waitcnt vmcnt(27) lgkmcnt(0)
s_barrier
Mvav 52,220,252
s_nop 0
Ta 180,%0,36864
Ta 182,%0,38912
Mvav 52,196,232
L_ v7,24,s55,0
Mvav 52,192,212
S_ s55,s71,634880
Ta 184,%0,40960
Ta 186,%0,43008
Mvav 48,192,208
S_ m0,s51,14336
Mvav 48,196,228
s_nop 0
Ta 188,%0,45056
Ta 190,%0,47104
Mvav 48,220,248
L_ v7,24,s55,0
Mvav 44,220,244
s_sub_u32 s75, s6, 5
S_ s55,s71,645120
Mvav 44,196,224
s_and_b32 s75, s75, 7
S_ m0,s51,15360
Mvav 44,192,204
Mvav 40,192,200
buffer_load_dword v11, v8, s[92:95], s96 offen offset:384 sc1
Mvav 40,196,220
Mvav 40,220,240
Mvav 36,220,236
Mvav 36,196,216
L_ v7,24,s55,0
Mvav 36,192,196
W_ 0
S_ s55,s71,655360
Mvaa 56,180,0
S_ m0,s51,16384
Ta 192,v1,36864
Ta 194,v1,38912
Mvaa 56,184,40
Mvaa 56,188,80
s_nop 0
Ta 196,v1,40960
Ta 198,v1,43008
Mvaa 60,188,84
L_ v7,24,s55,0
Mvaa 60,184,44
S_ s55,s71,665600
Ta 220,v1,45056
Ta 222,v1,47104
Mvaa 60,180,4
S_ m0,s51,17408
Mvaa 64,180,8
Mvaa 64,184,48
L_ v7,24,s55,0
Mvaa 64,188,88
S_ s55,s71,0
Mvaa 68,188,240
S_ m0,s51,49152
Mvaa 68,184,52
Mvaa 68,180,12
L_ v7,24,s55,0
Mvaa 72,180,16
S_ s55,s71,10240
Mvaa 72,184,56
S_ m0,s51,50176
Mvaa 72,188,244
W_ 0
Mvaa 92,220,244
s_nop 0
Ta 180,v2,36864
Ta 182,v2,38912
Mvaa 92,196,56
L_ v7,24,s55,0
Mvaa 92,192,16
S_ s55,s71,20480
Ta 184,v2,40960
Ta 186,v2,43008
Mvaa 88,192,12
S_ m0,s51,51200
Mvaa 88,196,52
s_nop 0
Ta 188,v2,45056
Ta 190,v2,47104
Mvaa 88,220,240
L_ v7,24,s55,0
Mvaa 84,220,88
S_ s55,s71,30720
Mvaa 84,196,48
S_ m0,s51,52224
Mvaa 84,192,8
Mvaa 80,192,4
L_ v7,24,s55,0
Mvaa 80,196,44
S_ s83,s6,6
S_ s55,s71,40960
Mvaa 80,220,84
s_and_b32 s83, s83, 7
S_ m0,s51,53248
Mvaa 76,220,80
s_lshl_b32 s83, s83, 2
Mvaa 76,196,40
L_ v7,24,s55,0
Mvaa 76,192,0
W_ 0
v_cmp_eq_u32_e64 s[36:37], s83, v8
S_ s55,s71,51200
Mvaa 16,180,0
S_ m0,s51,54272
Ta 192,v3,36864
Ta 194,v3,38912
Mvaa 16,184,40
.Lpad_o_10:
s_cbranch_execz .Lhop_o_r
v_mov_b32 v10, 0x7fff0000
Mvaa 16,188,80
s_nop 0
Ta 196,v3,40960
Ta 198,v3,43008
Mvaa 20,188,84
L_ v7,24,s55,0
Mvaa 20,184,44
s_nop 0
Ta 220,v3,45056
Ta 222,v3,47104
Mvaa 20,180,4
v_cndmask_b32_e64 v10, v10, v9, s[36:37]
Mvaa 24,180,8
Mvaa 24,184,48
Mvaa 24,188,88
Mvaa 28,188,240
Mvaa 28,184,52
Mvaa 28,180,12
Mvaa 32,180,16
Mvaa 32,184,56
Mvaa 32,188,244
s_waitcnt vmcnt(15) lgkmcnt(0)
s_barrier
Mvaa 52,220,244
s_nop 0
Ta 180,%0,24576
Ta 182,%0,26624
Mvaa 52,196,56
buffer_store_dword a255, v10, s[92:95], 0 offen offset:384 sc1
Mvaa 52,192,16
s_nop 0
Ta 184,%0,28672
Ta 186,%0,30720
Mvaa 48,192,12
Mvaa 48,196,52
s_nop 0
Ta 188,%0,32768
Ta 190,%0,34816
Mvaa 48,220,240
Mvaa 44,220,88
Mvaa 44,196,48
Mvaa 44,192,8
Mvaa 40,192,4
Mvaa 40,196,44
Mvaa 40,220,84
Mvaa 36,220,80
Mvaa 36,196,40
Mvaa 36,192,0
W_ 0
Mvaa 56,180,120
S_ s81,s6,7
Ta 192,v1,24576
Ta 194,v1,26624
Mvaa 56,184,160
s_and_b32 s81, s81, 7
Mvaa 56,188,200
s_mul_i32 s81, s81, 20480
Ta 196,v1,28672
Ta 198,v1,30720
Mvaa 60,188,204
s_mov_b32 m0, s52
S_ s73,s81,s78
Mvaa 60,184,164
S_ s73,s73,4096
Ta 220,v1,32768
Ta 222,v1,34816
Mvaa 60,180,124
s_nop 0
Y_ 56,0
Y_ 57,256
Mvaa 64,180,128
s_nop 0
Y_ 58,512
Y_ 59,768
Mvaa 64,184,168
Mvaa 64,188,208
s_nop 0
Y_ 60,1024
Y_ 61,1280
Mvaa 68,188,212
s_nop 0
Y_ 62,1536
Y_ 63,1792
Mvaa 68,184,172
Mvaa 68,180,132
s_nop 0
Y_ 64,2048
Y_ 65,2304
Mvaa 72,180,136
s_nop 0
Y_ 66,2560
Y_ 67,2816
Mvaa 72,184,176
Mvaa 72,188,216
W_ 12
Y_ 68,3072
Y_ 69,3328
Mvaa 92,220,216
s_nop 0
Ta 180,v2,24576
Ta 182,v2,26624
Mvaa 92,196,176
s_nop 0
Y_ 70,3584
Y_ 71,3840
Mvaa 92,192,136
s_nop 0
Ta 184,v2,28672
Ta 186,v2,30720
Mvaa 88,192,132
s_nop 0
Y_ 72,4096
Y_ 73,4352
Mvaa 88,196,172
s_nop 0
Ta 188,v2,32768
Ta 190,v2,34816
Mvaa 88,220,212
s_nop 0
Y_ 74,4608
Y_ 75,4864
Mvaa 84,220,208
s_nop 0
Y_ 92,9216
Y_ 93,9472
Mvaa 84,196,168
s_nop 0
Y_ 94,9728
Y_ 95,9984
Mvaa 84,192,128
s_nop 0
Y_ 88,8192
Y_ 89,8448
Mvaa 80,192,124
s_nop 0
Y_ 90,8704
Y_ 91,8960
Mvaa 80,196,164
Mvaa 80,220,204
s_nop 0
Y_ 84,7168
Y_ 85,7424
Mvaa 76,220,200
s_nop 0
Y_ 86,7680
Y_ 87,7936
Mvaa 76,196,160
Mvaa 76,192,120
W_ 14
Y_ 80,6144
Y_ 81,6400
Mvaa 16,180,120
s_nop 0
Ta 192,v3,24576
Ta 194,v3,26624
Mvaa 16,184,160
s_nop 0
Y_ 82,6656
Y_ 83,6912
Mvaa 16,188,200
s_nop 0
Ta 196,v3,28672
Ta 198,v3,30720
Mvaa 20,188,204
s_nop 0
Y_ 76,5120
Y_ 77,5376
Mvaa 20,184,164
s_nop 0
Ta 220,v3,32768
Ta 222,v3,34816
Mvaa 20,180,124
s_nop 0
Y_ 78,5632
Y_ 79,5888
Mvaa 24,180,128
Mvaa 24,184,168
Mvaa 24,188,208
s_waitcnt vmcnt(14) lgkmcnt(0)
s_barrier
Mvaa 28,188,212
S_ s55,s71,122880
ds_read2st64_b32 v[96:97], v5 offset0:0 offset1:1
Mvaa 28,184,172
S_ m0,s51,36864
ds_read2st64_b32 v[98:99], v5 offset0:2 offset1:3
Mvaa 28,180,132
L_ v7,24,s55,0
Mvaa 32,180,136
S_ s55,s71,133120
ds_read2st64_b32 v[104:105], v5 offset0:4 offset1:5
Mvaa 32,184,176
s_waitcnt vmcnt(11)
ds_read2st64_b32 v[106:107], v5 offset0:6 offset1:7
S_ m0,s51,37888
Mvaa 32,188,216
ds_read2st64_b32 v[112:113], v5 offset0:8 offset1:9
v_cmp_eq_u32 vcc, s62, v11
Mvaa 52,220,216
s_mul_i32 s76, s75, 204800
Ta 180,%0,0
Ta 182,%0,2048
Mvaa 52,196,176
L_ v7,24,s55,0
Mvaa 52,192,136
W_ 5
Ta 184,%0,4096
Ta 186,%0,6144
Mvaa 48,192,132
s_bitcmp1_b32 vcc_lo, s75
s_cbranch_scc0 .Lspin4
.Lspun4:
ds_read2st64_b32 v[114:115], v5 offset0:10 offset1:11
Mvaa 48,196,172
S_ s55,s71,143360
Ta 188,%0,8192
Ta 190,%0,10240
Mvaa 48,220,212
S_ m0,s51,38912
ds_read2st64_b32 v[120:121], v5 offset0:12 offset1:13
A_ v96,v16,v96
Mvaa 44,220,208
L_ v7,24,s55,0
Mvaa 44,196,168
S_ s55,s71,153600
ds_read2st64_b32 v[122:123], v5 offset0:14 offset1:15
A_ v97,v17,v97
Mvaa 44,192,128
S_ m0,s51,39936
ds_read2st64_b32 v[128:129], v5 offset0:16 offset1:17
A_ v98,v18,v98
Mvaa 40,192,124
ds_read2st64_b32 v[130:131], v5 offset0:18 offset1:19
A_ v99,v19,v99
Mvaa 40,196,164
L_ v7,24,s55,0
Mvaa 40,220,204
W_ 12
ds_read2st64_b32 v[132:133], v5 offset0:36 offset1:37
A_ v104,v20,v104
Mvaa 36,220,200
S_ s55,s71,163840
ds_read2st64_b32 v[134:135], v5 offset0:38 offset1:39
A_ v105,v21,v105
Mvaa 36,196,160
S_ m0,s51,40960
ds_read2st64_b32 v[124:125], v5 offset0:32 offset1:33
A_ v106,v22,v106
Mvaa 36,192,120
L_ v7,24,s55,0
s_waitcnt vmcnt(26)
Maaa 100,70,20
S_ s55,s71,174080
ds_read2st64_b32 v[126:127], v5 offset0:34 offset1:35
A_ v107,v23,v107
Maaa 140,68,20
W_ 10
ds_read2st64_b32 v[116:117], v5 offset0:28 offset1:29
A_ v112,v24,v112
Maaa 104,70,24
S_ m0,s51,41984
ds_read2st64_b32 v[118:119], v5 offset0:30 offset1:31
A_ v113,v25,v113
Maaa 144,68,24
L_ v7,24,s55,0
Maaa 108,70,28
ds_read2st64_b32 v[108:109], v5 offset0:24 offset1:25
A_ v114,v26,v114
Maaa 148,68,28
ds_read2st64_b32 v[110:111], v5 offset0:26 offset1:27
A_ v115,v27,v115
Maaa 112,70,32
W_ 10
ds_read2st64_b32 v[100:101], v5 offset0:20 offset1:21
A_ v120,v28,v120
Maaa 152,68,32
ds_read2st64_b32 v[102:103], v5 offset0:22 offset1:23
A_ v121,v29,v121
Maaa 116,70,36
A_ v122,v30,v122
A_ v123,v31,v123
Maaa 156,68,36
W_ 10
A_ v128,v32,v128
A_ v129,v33,v129
Maaa 224,70,60
A_ v130,v34,v130
A_ v131,v35,v131
Maaa 228,68,60
W_ 8
A_ v132,v52,v132
A_ v133,v53,v133
Maaa 232,70,64
A_ v134,v54,v134
A_ v135,v55,v135
Maaa 236,68,64
W_ 7
A_ v124,v48,v124
W_ 6
Zvav 56,180,136
s_nop 0
Ta 192,v1,0
Ta 194,v1,2048
Mvav 56,184,156
A_ v125,v49,v125
A_ v126,v50,v126
Mvav 56,188,176
S_ s56,s76,s84
Ta 196,v1,4096
Ta 198,v1,6144
Zvav 60,188,180
buffer_load_dwordx4 a[20:23], v6, s[28:31], s56 offen offset:0 sc1
Mvav 60,184,160
s_nop 0
Ta 220,v1,8192
Ta 222,v1,10240
Mvav 60,180,140
A_ v127,v51,v127
C_ 131,130,131
Zvav 64,180,144
W_ 10
A_ v116,v44,v116
A_ v117,v45,v117
Mvav 64,184,164
buffer_load_dwordx4 a[24:27], v6, s[28:31], s56 offen offset:1024 sc1
Mvav 64,188,184
A_ v118,v46,v118
A_ v119,v47,v119
Zvav 68,188,188
W_ 8
A_ v108,v40,v108
A_ v109,v41,v109
Mvav 68,184,168
A_ v110,v42,v110
A_ v111,v43,v111
Mvav 68,180,148
buffer_load_dwordx4 a[28:31], v6, s[28:31], s56 offen offset:2048 sc1
Zvav 72,180,152
W_ 6
A_ v100,v36,v100
A_ v101,v37,v101
Mvav 72,184,172
A_ v102,v38,v102
A_ v103,v39,v103
Mvav 72,188,192
W_ 0
C_ 130,128,129
C_ 132,132,133
Zvav 92,220,192
s_nop 0
Ta 180,v2,0
Ta 182,v2,2048
Mvav 92,196,172
buffer_load_dwordx4 a[32:35], v6, s[28:31], s56 offen offset:3072 sc1
Mvav 92,192,152
S_ s56,s56,4096
Ta 184,v2,4096
Ta 186,v2,6144
Zvav 88,192,148
C_ 133,134,135
Mvav 88,196,168
s_nop 0
Ta 188,v2,8192
Ta 190,v2,10240
Mvav 88,220,188
buffer_load_dwordx4 a[36:39], v6, s[28:31], s56 offen offset:0 sc1
Zvav 84,220,184
S_ s56,s76,s74
Mvav 84,196,164
Mvav 84,192,144
Zvav 80,192,140
buffer_load_dwordx4 a[60:63], v6, s[28:31], s56 offen offset:0 sc1
Mvav 80,196,160
S_ s56,s76,s82
Mvav 80,220,180
Zvav 76,220,176
Mvav 76,196,156
buffer_load_dwordx4 a[64:67], v6, s[28:31], s56 offen offset:0 sc1
Mvav 76,192,136
W_ 0
s_sub_u32 s75, s6, 6
Maav 100,180,136
s_and_b32 s75, s75, 7
Ta 192,v3,0
Ta 194,v3,2048
Maav 100,184,156
Maav 100,188,176
s_nop 0
Ta 196,v3,4096
Ta 198,v3,6144
Maav 104,188,180
buffer_load_dword v11, v8, s[92:95], s96 offen offset:384 sc1
Maav 104,184,160
s_nop 0
Ta 220,v3,8192
Ta 222,v3,10240
Maav 104,180,140
Maav 108,180,144
Maav 108,184,164
buffer_store_dwordx4 v[130:133], v6, s[28:31], s73 offen offset:0 sc1
Maav 108,188,184
s_sub_u32 s73, s73, 4096
Maav 112,188,188
C_ 123,122,123
C_ 122,120,121
Maav 112,184,168
C_ 124,124,125
C_ 125,126,127
Maav 112,180,148
buffer_store_dwordx4 v[122:125], v6, s[28:31], s73 offen offset:3072 sc1
Maav 116,180,152
C_ 115,114,115
C_ 114,112,113
Maav 116,184,172
C_ 116,116,117
C_ 117,118,119
Maav 116,188,192
s_waitcnt vmcnt(23) lgkmcnt(0)
s_barrier
Maav 156,220,192
s_nop 0
Ta 180,%0,12288
Ta 182,%0,14336
Maav 156,196,172
buffer_store_dwordx4 v[114:117], v6, s[28:31], s73 offen offset:2048 sc1
Maav 156,192,152
s_nop 0
Ta 184,%0,16384
Ta 186,%0,18432
Maav 152,192,148
C_ 107,106,107
C_ 106,104,105
Maav 152,196,168
s_nop 0
Ta 188,%0,20480
Ta 190,%0,22528
Maav 152,220,188
C_ 108,108,109
C_ 109,110,111
Maav 148,220,184
buffer_store_dwordx4 v[106:109], v6, s[28:31], s73 offen offset:1024 sc1
Maav 148,196,164
C_ 99,98,99
C_ 98,96,97
Maav 148,192,144
C_ 100,100,101
C_ 101,102,103
Maav 144,192,140
Maav 144,196,160
buffer_store_dwordx4 v[98:101], v6, s[28:31], s73 offen offset:0 sc1
Maav 144,220,180
Maav 140,220,176
Maav 140,196,156
Maav 140,192,136
W_ 0
Mvav 56,180,196
s_nop 0
Ta 192,v1,12288
Ta 194,v1,14336
Mvav 56,184,216
Mvav 56,188,236
s_nop 0
Ta 196,v1,16384
Ta 198,v1,18432
Mvav 60,188,240
Mvav 60,184,220
s_nop 0
Ta 220,v1,20480
Ta 222,v1,22528
Mvav 60,180,200
v_bfe_u32 v4, v6, 4, 4
Mvav 64,180,204
v_mul_u32_u24 v4, 2560, v4
Mvav 64,184,224
v_bfe_u32 v10, v6, 8, 1
Mvav 64,188,244
v_lshl_add_u32 v4, v10, 5, v4
Mvav 68,188,248
v_lshrrev_b32 v10, 9, v6
Mvav 68,184,228
v_lshl_add_u32 v4, v10, 4, v4
Mvav 68,180,208
Mvav 72,180,212
Mvav 72,184,232
Mvav 72,188,252
W_ 0
Mvav 92,220,252
s_nop 0
Ta 180,v2,12288
Ta 182,v2,14336
Mvav 92,196,232
Mvav 92,192,212
s_nop 0
Ta 184,v2,16384
Ta 186,v2,18432
Mvav 88,192,208
Mvav 88,196,228
s_nop 0
Ta 188,v2,20480
Ta 190,v2,22528
Mvav 88,220,248
Mvav 84,220,244
Mvav 84,196,224
Mvav 84,192,204
Mvav 80,192,200
Mvav 80,196,220
Mvav 80,220,240
Mvav 76,220,236
Mvav 76,196,216
Mvav 76,192,196
W_ 0
Maav 100,180,196
s_nop 0
Ta 192,v3,12288
Ta 194,v3,14336
Maav 100,184,216
Maav 100,188,236
s_nop 0
Ta 196,v3,16384
Ta 198,v3,18432
Maav 104,188,240
Maav 104,184,220
s_nop 0
Ta 220,v3,20480
Ta 222,v3,22528
Maav 104,180,200
Maav 108,180,204
S_ s86,s6,1
Maav 108,184,224
s_and_b32 s86, s86, 7
Maav 108,188,244
s_waitcnt vmcnt(5)
Maav 112,188,248
v_cmp_eq_u32 vcc, s62, v11
Maav 112,184,228
s_mul_i32 s76, s75, 204800
Maav 112,180,208
s_bitcmp1_b32 vcc_lo, s75
s_cbranch_scc0 .Lspin5
.Lspun5:
Maav 116,180,212
s_bitcmp1_b32 vcc_lo, s86
s_cbranch_scc0 .Lspin6
.Lspun6:
Maav 116,184,232
S_ s56,s76,s84
Maav 116,188,252
s_waitcnt vmcnt(20) lgkmcnt(0)
s_barrier
Maav 156,220,252
s_nop 0
Ta 180,%0,49152
Ta 182,%0,51200
Maav 156,196,232
buffer_load_dwordx4 v[16:19], v6, s[28:31], s56 offen offset:0 sc1
Maav 156,192,212
s_nop 0
Ta 184,%0,53248
Ta 186,%0,55296
Maav 152,192,208
Maav 152,196,228
s_nop 0
Ta 188,%0,57344
Ta 190,%0,59392
Maav 152,220,248
buffer_load_dwordx4 v[20:23], v6, s[28:31], s56 offen offset:1024 sc1
Maav 148,220,244
Maav 148,196,224
Maav 148,192,204
Maav 144,192,200
buffer_load_dwordx4 v[24:27], v6, s[28:31], s56 offen offset:2048 sc1
Maav 144,196,220
Maav 144,220,240
Maav 140,220,236
Maav 140,196,216
buffer_load_dwordx4 v[28:31], v6, s[28:31], s56 offen offset:3072 sc1
Maav 140,192,196
W_ 0
S_ s56,s56,4096
Mvaa 56,180,0
.Lpad_o_11:
s_cbranch_execz .Lhop_o_r
Ta 192,v1,49152
Ta 194,v1,51200
Mvaa 56,184,40
R_ v164,a224
R_ v165,a225
Mvaa 56,188,80
s_nop 0
Ta 196,v1,53248
Ta 198,v1,55296
Mvaa 60,188,84
buffer_load_dwordx4 v[32:35], v6, s[28:31], s56 offen offset:0 sc1
Mvaa 60,184,44
S_ s56,s76,s74
Ta 220,v1,57344
Ta 222,v1,59392
Mvaa 60,180,4
R_ v166,a226
R_ v167,a227
Mvaa 64,180,8
R_ v168,a228
R_ v169,a229
Mvaa 64,184,48
buffer_load_dwordx4 v[36:39], v6, s[28:31], s56 offen offset:0 sc1
Mvaa 64,188,88
S_ s56,s76,s82
R_ v170,a230
R_ v171,a231
Mvaa 68,188,240
R_ v172,a232
R_ v173,a233
Mvaa 68,184,52
R_ v174,a234
R_ v175,a235
Mvaa 68,180,12
buffer_load_dwordx4 v[40:43], v6, s[28:31], s56 offen offset:0 sc1
Mvaa 72,180,16
s_mul_i32 s76, s86, 204800
R_ v176,a236
R_ v177,a237
Mvaa 72,184,56
S_ s80,s76,s84
R_ v178,a238
R_ v179,a239
Mvaa 72,188,244
W_ 0
S_ s56,s76,s74
Mvaa 92,220,244
s_nop 0
Ta 180,v2,49152
Ta 182,v2,51200
Mvaa 92,196,56
buffer_load_dwordx4 v[156:159], v6, s[28:31], s56 offen offset:0 sc1
Mvaa 92,192,16
S_ s56,s76,s82
Ta 184,v2,53248
Ta 186,v2,55296
Mvaa 88,192,12
Mvaa 88,196,52
s_nop 0
Ta 188,v2,57344
Ta 190,v2,59392
Mvaa 88,220,240
buffer_load_dwordx4 v[160:163], v6, s[28:31], s56 offen offset:0 sc1
Mvaa 84,220,88
Mvaa 84,196,48
Mvaa 84,192,8
Mvaa 80,192,4
Mvaa 80,196,44
Mvaa 80,220,84
Mvaa 76,220,80
Mvaa 76,196,40
Mvaa 76,192,0
W_ 0
Maaa 100,180,0
s_nop 0
Ta 192,v3,49152
Ta 194,v3,51200
Maaa 100,184,40
Maaa 100,188,80
s_nop 0
Ta 196,v3,53248
Ta 198,v3,55296
Maaa 104,188,84
Maaa 104,184,44
s_nop 0
Ta 220,v3,57344
Ta 222,v3,59392
Maaa 104,180,4
Maaa 108,180,8
R_ v180,a100
R_ v181,a101
Maaa 108,184,48
R_ v182,a102
R_ v183,a103
Maaa 108,188,88
Maaa 112,188,240
R_ v184,a104
R_ v185,a105
Maaa 112,184,52
R_ v186,a106
R_ v187,a107
Maaa 112,180,12
Maaa 116,180,16
R_ v188,a108
R_ v189,a109
Maaa 116,184,56
R_ v190,a110
R_ v191,a111
Maaa 116,188,244
s_waitcnt vmcnt(22) lgkmcnt(0)
s_barrier
Maaa 156,220,244
s_nop 0
Ta 180,%0,36864
Ta 182,%0,38912
Maaa 156,196,56
R_ v192,a112
R_ v193,a113
Maaa 156,192,16
s_nop 0
Ta 184,%0,40960
Ta 186,%0,43008
Maaa 152,192,12
R_ v194,a114
R_ v195,a115
Maaa 152,196,52
s_nop 0
Ta 188,%0,45056
Ta 190,%0,47104
Maaa 152,220,240
R_ v196,a116
R_ v197,a117
Maaa 148,220,88
R_ v198,a118
R_ v199,a119
Maaa 148,196,48
R_ v216,a156
R_ v217,a157
Maaa 148,192,8
R_ v218,a158
R_ v219,a159
Maaa 144,192,4
R_ v212,a152
R_ v213,a153
Maaa 144,196,44
R_ v214,a154
R_ v215,a155
Maaa 144,220,84
Maaa 140,220,80
R_ v208,a148
R_ v209,a149
Maaa 140,196,40
R_ v210,a150
R_ v211,a151
Maaa 140,192,0
s_waitcnt vmcnt(15)
Mvaa 164,70,60
R_ v204,a144
R_ v205,a145
Mvaa 168,68,60
R_ v206,a146
R_ v207,a147
Mvaa 172,70,64
Mvaa 176,68,64
R_ v200,a140
R_ v201,a141
Mvaa 180,70,20
R_ v202,a142
R_ v203,a143
Mvaa 184,70,24
Mvaa 188,70,28
Mvaa 192,70,32
Mvaa 196,70,36
Mvaa 216,68,36
Mvaa 212,68,32
Mvaa 208,68,28
Mvaa 204,68,24
Mvaa 200,68,20
W_ 0
Mvaa 56,180,120
s_nop 0
Ta 192,v1,36864
Ta 194,v1,38912
Mvaa 56,184,160
Mvaa 56,188,200
s_nop 0
Ta 196,v1,40960
Ta 198,v1,43008
Mvaa 60,188,204
s_mov_b32 m0, s52
Mvaa 60,184,164
s_nop 0
Ta 220,v1,45056
Ta 222,v1,47104
Mvaa 60,180,124
s_nop 0
Y_ 56,0
Y_ 57,256
Mvaa 64,180,128
s_nop 0
Y_ 58,512
Y_ 59,768
Mvaa 64,184,168
Mvaa 64,188,208
s_nop 0
Y_ 60,1024
Y_ 61,1280
Mvaa 68,188,212
s_nop 0
Y_ 62,1536
Y_ 63,1792
Mvaa 68,184,172
Mvaa 68,180,132
s_nop 0
Y_ 64,2048
Y_ 65,2304
Mvaa 72,180,136
s_nop 0
Y_ 66,2560
Y_ 67,2816
Mvaa 72,184,176
Mvaa 72,188,216
W_ 12
Y_ 68,3072
Y_ 69,3328
Mvaa 92,220,216
s_nop 0
Ta 180,v2,36864
Ta 182,v2,38912
Mvaa 92,196,176
s_nop 0
Y_ 70,3584
Y_ 71,3840
Mvaa 92,192,136
s_nop 0
Ta 184,v2,40960
Ta 186,v2,43008
Mvaa 88,192,132
s_nop 0
Y_ 72,4096
Y_ 73,4352
Mvaa 88,196,172
s_nop 0
Ta 188,v2,45056
Ta 190,v2,47104
Mvaa 88,220,212
s_nop 0
Y_ 74,4608
Y_ 75,4864
Mvaa 84,220,208
s_nop 0
Y_ 92,9216
Y_ 93,9472
Mvaa 84,196,168
s_nop 0
Y_ 94,9728
Y_ 95,9984
Mvaa 84,192,128
s_nop 0
Y_ 88,8192
Y_ 89,8448
Mvaa 80,192,124
s_nop 0
Y_ 90,8704
Y_ 91,8960
Mvaa 80,196,164
Mvaa 80,220,204
s_nop 0
Y_ 84,7168
Y_ 85,7424
Mvaa 76,220,200
s_nop 0
Y_ 86,7680
Y_ 87,7936
Mvaa 76,196,160
Mvaa 76,192,120
W_ 14
Y_ 80,6144
Y_ 81,6400
Mvaa 180,180,120
S_ s56,s76,s84
Ta 192,v3,36864
Ta 194,v3,38912
Mvaa 180,184,160
s_nop 0
Y_ 82,6656
Y_ 83,6912
Mvaa 180,188,200
s_nop 0
Ta 196,v3,40960
Ta 198,v3,43008
Mvaa 184,188,204
s_nop 0
Y_ 76,5120
Y_ 77,5376
Mvaa 184,184,164
s_nop 0
Ta 220,v3,45056
Ta 222,v3,47104
Mvaa 184,180,124
s_nop 0
Y_ 78,5632
Y_ 79,5888
Mvaa 188,180,128
buffer_load_dwordx4 v[136:139], v6, s[28:31], s56 offen offset:0 sc1
Mvaa 188,184,168
Mvaa 188,188,208
s_waitcnt vmcnt(63) lgkmcnt(0)
s_barrier
Mvaa 192,188,212
ds_read2st64_b32 v[96:97], v5 offset0:0 offset1:1
Mvaa 192,184,172
buffer_load_dwordx4 v[140:143], v6, s[28:31], s56 offen offset:1024 sc1
Mvaa 192,180,132
ds_read2st64_b32 v[98:99], v5 offset0:2 offset1:3
Mvaa 196,180,136
ds_read2st64_b32 v[104:105], v5 offset0:4 offset1:5
Mvaa 196,184,176
ds_read2st64_b32 v[106:107], v5 offset0:6 offset1:7
Mvaa 196,188,216
buffer_load_dwordx4 v[144:147], v6, s[28:31], s56 offen offset:2048 sc1
Mvaa 216,220,216
ds_read2st64_b32 v[112:113], v5 offset0:8 offset1:9
Mvaa 216,196,176
ds_read2st64_b32 v[114:115], v5 offset0:10 offset1:11
W_ 5
A_ v96,v96,v180
Mvaa 216,192,136
ds_read2st64_b32 v[120:121], v5 offset0:12 offset1:13
A_ v97,v97,v181
Mvaa 212,192,132
buffer_load_dwordx4 v[148:151], v6, s[28:31], s56 offen offset:3072 sc1
Mvaa 212,196,172
S_ s56,s56,4096
ds_read2st64_b32 v[122:123], v5 offset0:14 offset1:15
W_ 6
A_ v98,v98,v182
Mvaa 212,220,212
ds_read2st64_b32 v[128:129], v5 offset0:16 offset1:17
A_ v99,v99,v183
Mvaa 208,220,208
ds_read2st64_b32 v[130:131], v5 offset0:18 offset1:19
W_ 7
A_ v104,v104,v184
Mvaa 208,196,168
buffer_load_dwordx4 v[152:155], v6, s[28:31], s56 offen offset:0 sc1
Mvaa 208,192,128
ds_read2st64_b32 v[132:133], v5 offset0:36 offset1:37
A_ v105,v105,v185
Mvaa 204,192,124
ds_read2st64_b32 v[134:135], v5 offset0:38 offset1:39
W_ 8
A_ v106,v106,v186
Mvaa 204,196,164
ds_read2st64_b32 v[124:125], v5 offset0:32 offset1:33
A_ v107,v107,v187
Mvaa 204,220,204
ds_read2st64_b32 v[126:127], v5 offset0:34 offset1:35
W_ 9
A_ v112,v112,v188
Mvaa 200,220,200
ds_read2st64_b32 v[116:117], v5 offset0:28 offset1:29
A_ v113,v113,v189
Mvaa 200,196,160
ds_read2st64_b32 v[118:119], v5 offset0:30 offset1:31
W_ 10
A_ v114,v114,v190
Mvaa 200,192,120
ds_read2st64_b32 v[108:109], v5 offset0:24 offset1:25
A_ v115,v115,v191
ds_read2st64_b32 v[110:111], v5 offset0:26 offset1:27
ds_read2st64_b32 v[100:101], v5 offset0:20 offset1:21
ds_read2st64_b32 v[102:103], v5 offset0:22 offset1:23
W_ 13
A_ v120,v120,v192
A_ v121,v121,v193
W_ 12
A_ v122,v122,v194
A_ v123,v123,v195
W_ 11
A_ v128,v128,v196
A_ v129,v129,v197
W_ 10
A_ v130,v130,v198
A_ v131,v131,v199
s_waitcnt vmcnt(5)
Mvav 164,70,36
W_ 9
A_ v132,v132,v216
A_ v133,v133,v217
Mvav 168,68,36
W_ 8
A_ v134,v134,v218
A_ v135,v135,v219
Mvav 172,70,40
W_ 7
A_ v124,v124,v212
A_ v125,v125,v213
Mvav 176,68,40
W_ 6
A_ v126,v126,v214
A_ v127,v127,v215
Mvav 164,70,156
W_ 5
A_ v116,v116,v208
A_ v117,v117,v209
Mvav 168,68,156
W_ 4
A_ v118,v118,v210
A_ v119,v119,v211
Mvav 172,70,160
W_ 3
A_ v108,v108,v204
A_ v109,v109,v205
Mvav 176,68,160
W_ 2
A_ v110,v110,v206
A_ v111,v111,v207
Mvav 96,70,16
W_ 1
A_ v100,v100,v200
A_ v101,v101,v201
Mvav 104,70,20
W_ 0
A_ v102,v102,v202
A_ v103,v103,v203
Mvav 112,70,24
C_ 220,164,165
C_ 221,166,167
Mvav 120,70,28
C_ 222,168,169
C_ 223,170,171
Mvav 128,70,32
v_permlane16_swap_b32 v220, v222
v_permlane16_swap_b32 v221, v223
Mvav 132,68,32
buffer_store_dwordx4 v[220:223], v4, s[32:35], s9 offen sc1
Mvav 124,68,28
C_ 228,172,173
C_ 229,174,175
Mvav 116,68,24
C_ 230,176,177
C_ 231,178,179
Mvav 108,68,20
v_permlane16_swap_b32 v228, v230
v_permlane16_swap_b32 v229, v231
Mvav 100,68,16
buffer_store_dwordx4 v[228:231], v4, s[32:35], s10 offen sc1
s_waitcnt vmcnt(2)
.Lsel6:
Nvav 180,70,136,96
Nvav 188,70,140,104
Nvav 196,70,144,112
Nvav 204,70,148,120
Nvav 212,70,152,128
Nvav 184,68,136,100
s_nop 3
A_ v10,v180,v188
Nvav 192,68,140,108
A_ v11,v196,v204
Nvav 200,68,144,116
A_ v10,v10,v212
v_cmp_u_f32 vcc, v10, v11
Nvav 208,68,148,124
s_cbranch_vccnz .Lretry
.Lgood:
Nvav 216,68,152,132
C_ 180,180,181
C_ 181,182,183
C_ 182,184,185
C_ 183,186,187
s_nop 0
v_permlane16_swap_b32 v180, v182
v_permlane16_swap_b32 v181, v183
buffer_store_dwordx4 v[180:183], v4, s[32:35], s8 offen sc1
C_ 188,188,189
C_ 189,190,191
C_ 190,192,193
C_ 191,194,195
S_ s8,s8,40960
v_permlane16_swap_b32 v188, v190
v_permlane16_swap_b32 v189, v191
buffer_store_dwordx4 v[188:191], v4, s[32:35], s8 offen sc1
C_ 196,196,197
C_ 197,198,199
C_ 198,200,201
C_ 199,202,203
S_ s8,s8,40960
v_permlane16_swap_b32 v196, v198
v_permlane16_swap_b32 v197, v199
buffer_store_dwordx4 v[196:199], v4, s[32:35], s8 offen sc1
C_ 204,204,205
C_ 205,206,207
C_ 206,208,209
C_ 207,210,211
S_ s8,s8,40960
v_permlane16_swap_b32 v204, v206
v_permlane16_swap_b32 v205, v207
buffer_store_dwordx4 v[204:207], v4, s[32:35], s8 offen sc1
C_ 212,212,213
C_ 213,214,215
C_ 214,216,217
C_ 215,218,219
S_ s8,s8,40960
v_permlane16_swap_b32 v212, v214
v_permlane16_swap_b32 v213, v215
buffer_store_dwordx4 v[212:215], v4, s[32:35], s8 offen sc1
s_endpgm
.Lspin0:
s_cmp_eq_u32 s57, 0
s_cbranch_scc1 .Lspun0
s_sub_u32 s57, s57, 1
s_sleep 4
buffer_load_dword v11, v8, s[92:95], s96 offen offset:384 sc1
s_waitcnt vmcnt(0)
v_cmp_eq_u32 vcc, s62, v11
s_nop 1
s_bitcmp1_b32 vcc_lo, s75
s_cbranch_scc0 .Lspin0
s_branch .Lspun0
.Lspin1:
s_cmp_eq_u32 s57, 0
s_cbranch_scc1 .Lspun1
s_sub_u32 s57, s57, 1
s_sleep 4
buffer_load_dword v11, v8, s[92:95], s96 offen offset:384 sc1
s_waitcnt vmcnt(0)
v_cmp_eq_u32 vcc, s62, v11
s_nop 1
s_bitcmp1_b32 vcc_lo, s75
s_cbranch_scc0 .Lspin1
s_branch .Lspun1
.Lspin2:
s_cmp_eq_u32 s57, 0
s_cbranch_scc1 .Lspun2
s_sub_u32 s57, s57, 1
s_sleep 4
buffer_load_dword v11, v8, s[92:95], s96 offen offset:384 sc1
s_waitcnt vmcnt(0)
v_cmp_eq_u32 vcc, s62, v11
s_nop 1
s_bitcmp1_b32 vcc_lo, s75
s_cbranch_scc0 .Lspin2
s_branch .Lspun2
.Lspin3:
s_cmp_eq_u32 s57, 0
s_cbranch_scc1 .Lspun3
s_sub_u32 s57, s57, 1
s_sleep 4
buffer_load_dword v11, v8, s[92:95], s96 offen offset:384 sc1
s_waitcnt vmcnt(0)
v_cmp_eq_u32 vcc, s62, v11
s_nop 1
s_bitcmp1_b32 vcc_lo, s75
s_cbranch_scc0 .Lspin3
s_branch .Lspun3
.Lspin4:
s_cmp_eq_u32 s57, 0
s_cbranch_scc1 .Lspun4
s_sub_u32 s57, s57, 1
s_sleep 4
buffer_load_dword v11, v8, s[92:95], s96 offen offset:384 sc1
s_waitcnt vmcnt(0)
v_cmp_eq_u32 vcc, s62, v11
s_nop 1
s_bitcmp1_b32 vcc_lo, s75
s_cbranch_scc0 .Lspin4
s_branch .Lspun4
.Lspin5:
s_cmp_eq_u32 s57, 0
s_cbranch_scc1 .Lspun5
s_sub_u32 s57, s57, 1
s_sleep 4
buffer_load_dword v11, v8, s[92:95], s96 offen offset:384 sc1
s_waitcnt vmcnt(0)
v_cmp_eq_u32 vcc, s62, v11
s_nop 1
s_bitcmp1_b32 vcc_lo, s75
s_cbranch_scc0 .Lspin5
s_branch .Lspun5
.Lspin6:
s_cmp_eq_u32 s57, 0
s_cbranch_scc1 .Lspun6
s_sub_u32 s57, s57, 1
s_sleep 4
buffer_load_dword v11, v8, s[92:95], s96 offen offset:384 sc1
s_waitcnt vmcnt(0)
v_cmp_eq_u32 vcc, s62, v11
s_nop 1
s_bitcmp1_b32 vcc_lo, s86
s_cbranch_scc0 .Lspin6
s_branch .Lspun6
.Lretry:
s_cmp_eq_u32 s57, 0
s_cbranch_scc1 .Lgood
s_sub_u32 s57, s57, 1
v_max_u32 v10, v136, v137
v_max3_u32 v10, v10, v138, v139
v_max3_u32 v10, v10, v140, v141
v_max3_u32 v10, v10, v142, v143
v_max3_u32 v10, v10, v144, v145
v_max3_u32 v10, v10, v146, v147
v_max3_u32 v10, v10, v148, v149
v_max3_u32 v10, v10, v150, v151
v_max3_u32 v10, v10, v152, v153
v_max3_u32 v10, v10, v154, v155
v_cmp_eq_u32 vcc, -1, v10
s_nop 4
s_cbranch_vccz .Lgood
s_nop 7
s_nop 7
S_ s53,s80,4096
buffer_load_dwordx4 v[136:139], v6, s[28:31], s80 offen offset:0 sc1
buffer_load_dwordx4 v[140:143], v6, s[28:31], s80 offen offset:1024 sc1
buffer_load_dwordx4 v[144:147], v6, s[28:31], s80 offen offset:2048 sc1
buffer_load_dwordx4 v[148:151], v6, s[28:31], s80 offen offset:3072 sc1
buffer_load_dwordx4 v[152:155], v6, s[28:31], s53 offen offset:0 sc1
s_waitcnt vmcnt(0)
s_branch .Lsel6
.Ldone:
s_waitcnt vmcnt(0)
.purgem S_
.purgem W_
.purgem L_
.purgem X_
.purgem R_
.purgem Ta
.purgem Da
.purgem Zvaa
.purgem Mvaa
.purgem U_
.purgem C_
.purgem Ln
.purgem Dv
.purgem Zvvv
.purgem Mvvv
.purgem B_
.purgem E_
.purgem Nvvv
.purgem Tv
.purgem Zaav
.purgem Zavv
.purgem A_
.purgem Mavv
.purgem Zvav
.purgem Mvav
.purgem Y_
.purgem Zaaa
.purgem Maaa
.purgem Maav
.purgem Nvav
)A"
    : "+v"(tid)
    : "s"(__builtin_amdgcn_kernarg_segment_ptr()), "s"(blockIdx.x)
    : "v1", "v2", "v3", "v4", "v5", "v6", "v7", "v8", "v9", "v10", "v11", "v12", "v13", "v14", "v15", "v16", "v17", "v18", "v19", "v20", "v21", "v22", "v23", "v24", "v25", "v26", "v27", "v28", "v29", "v30", "v31", "v32", "v33", "v34", "v35", "v36", "v37", "v38", "v39", "v40", "v41", "v42", "v43", "v44", "v45", "v46", "v47", "v48", "v49", "v50", "v51", "v52", "v53", "v54", "v55", "v56", "v57", "v58", "v59", "v60", "v61", "v62", "v63", "v64", "v65", "v66", "v67", "v68", "v69", "v70", "v71", "v72", "v73", "v74", "v75", "v76", "v77", "v78", "v79", "v80", "v81", "v82", "v83", "v84", "v85", "v86", "v87", "v88", "v89", "v90", "v91", "v92", "v93", "v94", "v95", "v96", "v97", "v98", "v99", "v100", "v101", "v102", "v103", "v104", "v105", "v106", "v107", "v108", "v109", "v110", "v111", "v112", "v113", "v114", "v115", "v116", "v117", "v118", "v119", "v120", "v121", "v122", "v123", "v124", "v125", "v126", "v127", "v128", "v129", "v130", "v131", "v132", "v133", "v134", "v135", "v136", "v137", "v138", "v139", "v140", "v141", "v142", "v143", "v144", "v145", "v146", "v147", "v148", "v149", "v150", "v151", "v152", "v153", "v154", "v155", "v156", "v157", "v158", "v159", "v160", "v161", "v162", "v163", "v164", "v165", "v166", "v167", "v168", "v169", "v170", "v171", "v172", "v173", "v174", "v175", "v176", "v177", "v178", "v179", "v180", "v181", "v182", "v183", "v184", "v185", "v186", "v187", "v188", "v189", "v190", "v191", "v192", "v193", "v194", "v195", "v196", "v197", "v198", "v199", "v200", "v201", "v202", "v203", "v204", "v205", "v206", "v207", "v208", "v209", "v210", "v211", "v212", "v213", "v214", "v215", "v216", "v217", "v218", "v219", "v220", "v221", "v222", "v223", "v224", "v225", "v226", "v227", "v228", "v229", "v230", "v231", "v232", "v233", "v234", "v235", "v236", "v237", "v238", "v239", "v240", "v241", "v242", "v243", "v244", "v245", "v246", "v247", "v248", "v249", "v250", "v251", "v252", "v253", "v254", "v255", "a0", "a1", "a2", "a3", "a4", "a5", "a6", "a7", "a8", "a9", "a10", "a11", "a12", "a13", "a14", "a15", "a16", "a17", "a18", "a19", "a20", "a21", "a22", "a23", "a24", "a25", "a26", "a27", "a28", "a29", "a30", "a31", "a32", "a33", "a34", "a35", "a36", "a37", "a38", "a39", "a40", "a41", "a42", "a43", "a44", "a45", "a46", "a47", "a48", "a49", "a50", "a51", "a52", "a53", "a54", "a55", "a56", "a57", "a58", "a59", "a60", "a61", "a62", "a63", "a64", "a65", "a66", "a67", "a68", "a69", "a70", "a71", "a72", "a73", "a74", "a75", "a76", "a77", "a78", "a79", "a80", "a81", "a82", "a83", "a84", "a85", "a86", "a87", "a88", "a89", "a90", "a91", "a92", "a93", "a94", "a95", "a96", "a97", "a98", "a99", "a100", "a101", "a102", "a103", "a104", "a105", "a106", "a107", "a108", "a109", "a110", "a111", "a112", "a113", "a114", "a115", "a116", "a117", "a118", "a119", "a120", "a121", "a122", "a123", "a124", "a125", "a126", "a127", "a128", "a129", "a130", "a131", "a132", "a133", "a134", "a135", "a136", "a137", "a138", "a139", "a140", "a141", "a142", "a143", "a144", "a145", "a146", "a147", "a148", "a149", "a150", "a151", "a152", "a153", "a154", "a155", "a156", "a157", "a158", "a159", "a160", "a161", "a162", "a163", "a164", "a165", "a166", "a167", "a168", "a169", "a170", "a171", "a172", "a173", "a174", "a175", "a176", "a177", "a178", "a179", "a180", "a181", "a182", "a183", "a184", "a185", "a186", "a187", "a188", "a189", "a190", "a191", "a192", "a193", "a194", "a195", "a196", "a197", "a198", "a199", "a200", "a201", "a202", "a203", "a204", "a205", "a206", "a207", "a208", "a209", "a210", "a211", "a212", "a213", "a214", "a215", "a216", "a217", "a218", "a219", "a220", "a221", "a222", "a223", "a224", "a225", "a226", "a227", "a228", "a229", "a230", "a231", "a232", "a233", "a234", "a235", "a236", "a237", "a238", "a239", "a240", "a241", "a242", "a243", "a244", "a245", "a246", "a247", "a248", "a249", "a250", "a251", "a252", "a253", "a254", "a255", "s3", "s4", "s5", "s6", "s7", "s8", "s9", "s10", "s11", "s12", "s13", "s14", "s15", "s16", "s17", "s18", "s19", "s20", "s21", "s22", "s23", "s24", "s25", "s26", "s27", "s28", "s29", "s30", "s31", "s32", "s33", "s34", "s35", "s36", "s37", "s38", "s39", "s40", "s41", "s42", "s43", "s44", "s45", "s46", "s47", "s48", "s49", "s50", "s51", "s52", "s53", "s54", "s55", "s56", "s57", "s58", "s59", "s60", "s61", "s62", "s63", "s64", "s65", "s66", "s67", "s68", "s69", "s70", "s71", "s72", "s73", "s74", "s75", "s76", "s77", "s78", "s79", "s80", "s81", "s82", "s83", "s84", "s85", "s86", "s87", "s88", "s89", "s90", "s91", "s92", "s93", "s94", "s95", "s96", "s97", "s98", "s99", "s100", "s101", "vcc", "scc", "memory");
}

extern "C" void kernel_launch(void* const* d_in, const int* in_sizes, int n_in, void* d_out, int out_size, void* d_ws, size_t ws_size, hipStream_t stream) {
  Args P;
  for (int br = 0; br < 2; ++br) {
    P.p[br][0] = d_in[5 * br]; P.p[br][1] = d_in[5 * br + 3]; P.p[br][2] = d_in[5 * br + 1]; P.p[br][3] = d_in[5 * br + 2]; P.p[br][4] = d_in[5 * br + 4];
    P.p[br][5] = d_ws; P.p[br][6] = (char*)d_out + br * 1280; P.p[br][7] = d_ws;
  }
  (void)hipFuncSetAttribute((const void*)fused_kernel, hipFuncAttributeMaxDynamicSharedMemorySize, LDS1);
  fused_kernel<<<256, 256, LDS1, stream>>>(P);
}
```

```cpp
#include <hip/hip_runtime.h>
#include <hip/hip_bf16.h>

constexpr unsigned LDS1 = 160 * 1024;

struct Args { const void* p[2][8]; };

__global__ __launch_bounds__(256, 1) void fused_kernel(Args P) {
  unsigned tid = threadIdx.x;
  asm volatile(
R"A(.macro S_ a,b,c
s_add_u32 \a, \b, \c
.endm
.macro W_ a
s_waitcnt lgkmcnt(\a)
.endm
.macro L_ x,r,s,o
buffer_load_dwordx4 \x, s[\r:\r+3], \s offen offset:\o lds
.endm
.macro X_ a,b
v_accvgpr_write_b32 \a, \b
.endm
.macro R_ a,b
v_accvgpr_read_b32 \a, \b
.endm
.macro Ta d,x,o
ds_read_b64_tr_b16 a[\d:\d+1], \x offset:\o
.endm
.macro Da d,x,o
ds_read_b128 a[\d:\d+3], \x offset:\o
.endm
.macro Zvaa d,a,b
v_mfma_f32_16x16x32_bf16 v[\d:\d+3], a[\a:\a+3], a[\b:\b+3], 0
.endm
.macro Mvaa d,a,b
v_mfma_f32_16x16x32_bf16 v[\d:\d+3], a[\a:\a+3], a[\b:\b+3], v[\d:\d+3]
.endm
.macro U_ a,b,c
v_mul_f32 \a, \b, \c
.endm
.macro C_ d,a,b
v_cvt_pk_bf16_f32 v\d, v\a, v\b
.endm
.macro Ln x,r,s,o
buffer_load_dwordx4 \x, s[\r:\r+3], \s offen offset:\o nt lds
.endm
.macro Dv d,x,o
ds_read_b128 v[\d:\d+3], \x offset:\o
.endm
.macro Zvvv d,a,b
v_mfma_f32_16x16x32_bf16 v[\d:\d+3], v[\a:\a+3], v[\b:\b+3], 0
.endm
.macro Mvvv d,a,b
v_mfma_f32_16x16x32_bf16 v[\d:\d+3], v[\a:\a+3], v[\b:\b+3], v[\d:\d+3]
.endm
.macro B_ a,b,c
v_sub_f32 \a, \b, \c
.endm
.macro E_ r
v_exp_f32 v\r, v\r
.endm
.macro Nvvv d,a,b,c
v_mfma_f32_16x16x32_bf16 v[\d:\d+3], v[\a:\a+3], v[\b:\b+3], v[\c:\c+3]
.endm
.macro Tv d,x,o
ds_read_b64_tr_b16 v[\d:\d+1], \x offset:\o
.endm
.macro Zaav d,a,b
v_mfma_f32_16x16x32_bf16 a[\d:\d+3], a[\a:\a+3], v[\b:\b+3], 0
.endm
.macro Zavv d,a,b
v_mfma_f32_16x16x32_bf16 a[\d:\d+3], v[\a:\a+3], v[\b:\b+3], 0
.endm
.macro A_ a,b,c
v_add_f32 \a, \b, \c
.endm
.macro Mavv d,a,b
v_mfma_f32_16x16x32_bf16 a[\d:\d+3], v[\a:\a+3], v[\b:\b+3], a[\d:\d+3]
.endm
.macro Zvav d,a,b
v_mfma_f32_16x16x32_bf16 v[\d:\d+3], a[\a:\a+3], v[\b:\b+3], 0
.endm
.macro Mvav d,a,b
v_mfma_f32_16x16x32_bf16 v[\d:\d+3], a[\a:\a+3], v[\b:\b+3], v[\d:\d+3]
.endm
.macro Y_ r,o
ds_write_addtid_b32 v\r offset:\o
.endm
.macro Zaaa d,a,b
v_mfma_f32_16x16x32_bf16 a[\d:\d+3], a[\a:\a+3], a[\b:\b+3], 0
.endm
.macro Maaa d,a,b
v_mfma_f32_16x16x32_bf16 a[\d:\d+3], a[\a:\a+3], a[\b:\b+3], a[\d:\d+3]
.endm
.macro Maav d,a,b
v_mfma_f32_16x16x32_bf16 a[\d:\d+3], a[\a:\a+3], v[\b:\b+3], a[\d:\d+3]
.endm
.macro Nvav d,a,b,c
v_mfma_f32_16x16x32_bf16 v[\d:\d+3], a[\a:\a+3], v[\b:\b+3], v[\c:\c+3]
.endm
.p2align 12
s_and_b32 s3, %2, 4
s_lshl_b32 s3, s3, 4
s_load_dwordx16 s[8:23], %1, s3
s_memrealtime s[100:101]
s_memtime s[90:91]
v_readfirstlane_b32 s4, %0
v_and_b32 v16, 63, %0
v_lshlrev_b32 v10, 4, v16
s_mov_b32 s7, %2
s_lshr_b32 s4, s4, 6
.Lcode0:
s_getpc_b64 s[44:45]
s_mov_b32 s46, .Ldone-.Lcode0
s_mov_b32 s47, 0x00020000
s_lshr_b32 s66, s7, 3
s_lshl_b32 s66, s66, 2
S_ s66,s66,s4
s_lshl_b32 s66, s66, 11
v_min_u32 v40, 15, v16
v_lshlrev_b32 v40, 7, v40
buffer_load_dword v247, v40, s[44:47], s66 offen
s_and_b32 s3, s7, 1
s_lshl_b32 s3, s3, 3
s_lshr_b32 s53, s7, 5
S_ s3,s3,s53
s_and_b32 s6, s7, 2
s_lshl_b32 s6, s6, 1
s_bfe_u32 s53, s7, 0x20003
S_ s6,s6,s53
s_and_b32 s5, s7, 4
s_lshl_b32 s5, s5, 2
S_ s5,s5,s3
s_mul_i32 s50, s4, 5120
s_mul_i32 s60, s4, 51200
s_lshr_b32 s77, s4, 1
s_and_b32 s53, s4, 1
s_mul_i32 s59, s53, 393216
s_lshl_b32 s52, s53, 11
s_mul_i32 s61, s53, 6144
s_mul_i32 s51, s77, 61440
S_ s55,s51,40960
S_ s51,s55,s61
s_sub_u32 s52, s51, s52
s_lshl_b32 s86, s53, 2
s_mul_i32 s56, s53, 10240
v_lshrrev_b32 v17, 3, v16
v_and_b32 v18, 7, v16
v_lshrrev_b32 v23, 4, v16
v_and_b32 v24, 1, v23
v_lshlrev_b32 v24, 2, v24
v_xor_b32 v24, v18, v24
v_mul_u32_u24 v6, 12288, v17
v_lshl_add_u32 v6, v24, 4, v6
v_add_u32 v24, s86, v23
v_xor_b32 v24, v18, v24
v_mul_u32_u24 v4, 1280, v17
v_lshl_add_u32 v4, v24, 4, v4
v_xor_b32 v5, 64, v4
s_getreg_b32 s53, hwreg(HW_REG_XCC_ID, 0, 4)
s_bitcmp1_b32 0xaaaa, s53
s_cselect_b32 s58, 2, 0
s_cselect_b32 s89, 1, 0
s_cselect_b32 s53, 768, 0
S_ s59,s59,s53
s_lshl_b32 s54, s6, 2
S_ s54,s54,s77
s_mul_i32 s86, s54, 384
s_mul_i32 s87, s3, 204800
s_lshl_b32 s53, s3, 5
S_ s53,s53,s54
s_mul_i32 s85, s53, 196608
s_mov_b32 s30, 7853184
s_mov_b32 s31, 0x00020000
s_mov_b32 s26, 204800
s_mov_b32 s27, 0x00020000
W_ 0
s_mov_b32 s98, s100
s_mov_b32 s99, 0
s_mov_b32 s88, s90
S_ s28,s10,s86
s_addc_u32 s29, s11, 0
S_ s24,s8,s87
s_addc_u32 s25, s9, 0
s_buffer_load_dword s67, s[24:27], s60
s_mul_i32 s64, s66, 30
s_buffer_load_dword s67, s[28:31], s64
s_mov_b32 s11, 0
S_ s65,s59,0
S_ m0,s52,8192
s_mov_b64 s[92:93], s[18:19]
s_mov_b32 s53, 251658240
L_ v6,28,s65,0
S_ s65,s59,98304
S_ m0,s52,9216
S_ s92,s92,s53
s_addc_u32 s93, s93, 0
L_ v6,28,s65,0
S_ s65,s59,196608
S_ m0,s52,10240
s_mov_b32 s94, 131072
s_mov_b32 s95, 0x00020000
L_ v6,28,s65,0
S_ s65,s59,294912
S_ m0,s52,11264
s_lshl_b32 s96, s5, 3
S_ s96,s96,s6
L_ v6,28,s65,0
S_ s65,s60,0
S_ m0,s50,0
s_lshl_b32 s96, s96, 9
v_cmp_gt_u32 vcc, 8, v16
L_ v4,24,s65,0
S_ s65,s60,10240
S_ m0,s50,1024
s_lshl_b32 s54, s5, 12
v_lshl_add_u32 v24, v16, 9, s54
L_ v5,24,s65,0
S_ s65,s60,20480
S_ m0,s50,2048
v_mov_b32 v12, 0x7fff0000
v_cndmask_b32 v12, v12, v24, vcc
L_ v4,24,s65,0
S_ s65,s60,30720
S_ m0,s50,3072
s_lshl_b32 s54, s6, 2
v_add_u32 v13, s54, v12
L_ v5,24,s65,0
S_ s65,s60,40960
S_ m0,s50,4096
v_lshrrev_b32 v19, 2, v10
v_mul_u32_u24 v25, 384, v17
L_ v4,24,s65,0
s_load_dword s67, s[12:13], s85
s_load_dword s67, s[14:15], s85
S_ s64,s85,393216
s_load_dword s67, s[12:13], s64
s_load_dword s67, s[14:15], s64
s_lshl_b32 s64, s6, 2
S_ s64,s64,s4
s_mul_i32 s64, s64, 245760
s_load_dword s67, s[16:17], s64
s_cmp_eq_u32 s4, 0
s_cbranch_scc0 .Lhs0a
buffer_load_dword v20, v19, s[92:95], s96 offen offset:128 sc1
buffer_load_dword v21, v19, s[92:95], s96 offen offset:384 sc1
buffer_load_dword a252, v19, s[92:95], s96 offen offset:256 sc1
buffer_load_dword a253, v13, s[92:95], 0 offen offset:256 sc1
buffer_load_dword v22, off, s[92:95], s96 offset:0 sc1
.Lhs0a:
S_ s65,s59,0
S_ m0,s52,16256
v_xor_b32 v24, v18, v23
v_lshl_add_u32 v7, v24, 4, v25
v_lshlrev_b32 v24, 1, v23
v_xor_b32 v24, v18, v24
v_lshl_add_u32 v9, v24, 4, v25
v_and_b32 v26, 15, v16
v_lshrrev_b32 v27, 2, v26
v_and_b32 v28, 3, v16
v_lshrrev_b32 v24, 1, v26
v_xor_b32 v24, v23, v24
L_ v6,28,s65,128
S_ s65,s59,98304
S_ m0,s52,17280
v_lshlrev_b32 v24, 4, v24
v_lshl_add_u32 v24, v18, 7, v24
v_lshrrev_b32 v25, 3, v26
v_lshl_add_u32 v2, v25, 10, v24
v_add_u32 v2, s56, v2
v_mul_u32_u24 v1, 3072, v25
v_add3_u32 v1, v1, v24, s55
v_lshlrev_b32 v24, 2, v25
v_xor_b32 v24, v28, v24
v_lshlrev_b32 v24, 4, v24
L_ v6,28,s65,128
S_ s65,s59,196608
S_ m0,s52,18304
v_lshl_add_u32 v24, v27, 7, v24
v_lshl_add_u32 %0, v23, 10, v24
v_add_u32 %0, s55, %0
v_and_b32 v29, 1, v23
v_lshl_add_u32 v24, v29, 1, v25
v_lshlrev_b32 v24, 1, v24
v_lshrrev_b32 v30, 1, v28
v_xor_b32 v24, v30, v24
v_lshlrev_b32 v24, 4, v24
v_lshl_add_u32 v29, v29, 2, v27
L_ v6,28,s65,128
S_ s65,s59,294912
S_ m0,s52,19328
v_lshl_add_u32 v24, v29, 7, v24
v_and_b32 v29, 1, v28
v_lshl_add_u32 v24, v29, 3, v24
v_lshrrev_b32 v29, 1, v23
v_mul_u32_u24 v3, 3072, v29
v_add3_u32 v3, v3, v24, s55
X_ a242,%0
X_ a244,v1
X_ a243,v2
X_ a245,v3
L_ v6,28,s65,128
S_ s65,s59,0
S_ m0,s52,24320
X_ a240,v4
X_ a248,v10
X_ a241,v12
X_ a251,v13
v_xor_b32 v1, 64, %0
v_xor_b32 v3, 64, v2
v_xor_b32 v8, 64, v7
S_ s32,s12,s85
s_addc_u32 s33, s13, 0
s_mov_b32 s34, 589824
L_ v6,28,s65,256
S_ s65,s59,98304
S_ m0,s52,25344
s_mov_b32 s35, 0x00020000
S_ s36,s14,s85
s_addc_u32 s37, s15, 0
s_mov_b32 s38, 589824
s_mov_b32 s39, 0x00020000
s_lshl_b32 s87, s87, 1
S_ s20,s20,s87
s_addc_u32 s21, s21, 0
s_mov_b32 s57, 20000
s_mov_b32 s48, 1037384597
L_ v6,28,s65,256
S_ s65,s59,196608
S_ m0,s52,26368
s_mov_b32 s49, 1037384597
s_mul_i32 s53, s5, 1638400
S_ s53,s53,134217728
S_ s22,s18,s53
s_addc_u32 s23, s19, 0
s_lshl_b32 s53, s6, 3
s_lshr_b32 s54, s7, 5
S_ s53,s53,s54
s_lshl_b32 s53, s53, 2
S_ s53,s53,s4
L_ v6,28,s65,256
S_ s65,s59,294912
S_ m0,s52,27392
s_mul_i32 s87, s53, 30720
s_lshl_b32 s53, s6, 2
S_ s53,s53,s77
s_mul_i32 s72, s53, 245760
s_and_b32 s54, s4, 1
s_mul_i32 s53, s54, 61440
S_ s72,s72,s53
s_lshl_b32 s53, s77, 6
s_xor_b32 s83, s53, 64
s_xor_b32 s53, s4, 2
L_ v6,28,s65,256
S_ s65,s60,128
S_ m0,s50,20480
s_mul_i32 s75, s53, 10240
s_cmp_eq_u32 s4, 0
s_cselect_b32 s73, 0, 2147418112
s_mul_i32 s53, s4, 5120
s_mul_i32 s78, s6, 204800
S_ s78,s78,s53
s_and_b32 s84, s6, 7
s_mul_i32 s84, s84, 20480
S_ s84,s84,s53
s_mul_i32 s53, s6, 5120
L_ v4,24,s65,0
S_ s65,s60,10368
S_ m0,s50,21504
S_ s53,s53,163840
s_lshl_b32 s54, s4, 10
S_ s74,s53,s54
S_ s53,s53,4096
s_cmp_eq_u32 s4, 0
s_cselect_b32 s82, s53, 4026531840
s_and_b32 s53, s4, 1
s_mul_i32 s53, s53, 204800
s_lshl_b32 s54, s6, 7
S_ s53,s53,s54
L_ v5,24,s65,0
S_ s65,s60,20608
S_ m0,s50,22528
s_lshl_b32 s54, s77, 6
S_ s8,s53,s54
s_and_b32 s53, s6, 1
s_mul_i32 s53, s53, 204800
s_lshr_b32 s54, s6, 1
s_lshl_b32 s54, s54, 6
S_ s53,s53,s54
S_ s53,s53,1024
s_mul_i32 s54, s4, 40960
S_ s9,s53,s54
L_ v4,24,s65,0
S_ s65,s60,30848
S_ m0,s50,23552
S_ s53,s53,163840
s_cmp_eq_u32 s4, 0
s_cselect_b32 s10, s53, 4026531840
R_ v12,a248
v_lshrrev_b32 v12, 4, v12
v_min_u32 v10, 11, v12
s_mul_i32 s56, s4, 12
v_add_u32 v10, s56, v10
v_mul_u32_u24 v11, 43691, v10
v_lshrrev_b32 v11, 18, v11
L_ v5,24,s65,0
S_ s65,s60,41088
S_ m0,s50,24576
v_lshlrev_b32 v10, 7, v10
v_mul_u32_u24 v11, 11520, v11
v_add_u32 v10, v10, v11
v_min_u32 v12, 9, v12
s_and_b32 s56, s6, 3
s_lshl_b32 s56, s56, 2
S_ s56,s56,s4
s_mul_i32 s56, s56, 10
v_add_u32 v12, s56, v12
v_mul_u32_u24 v12, 1280, v12
L_ v4,24,s65,0
S_ s65,s59,786432
S_ m0,s52,0
s_lshr_b32 s56, s7, 5
s_mul_i32 s56, s56, 98304
s_and_b32 s55, s59, 4095
S_ s56,s56,s55
s_mul_i32 s55, s77, 384
s_sub_u32 s56, s56, s55
s_mov_b32 s63, 0
L_ v6,28,s65,0
S_ s65,s59,884736
S_ m0,s52,1024
s_nop 0
L_ v6,28,s65,0
S_ s65,s59,983040
S_ m0,s52,2048
s_nop 0
L_ v6,28,s65,0
S_ s65,s59,1081344
S_ m0,s52,3072
s_nop 0
L_ v6,28,s65,0
S_ s55,s56,786432
buffer_load_dword a254, v10, s[28:31], s55 offen
S_ s55,s56,1572864
buffer_load_dword a254, v10, s[28:31], s55 offen
buffer_load_dword a254, v12, s[24:27], s63 offen offset:256
S_ s55,s56,2359296
buffer_load_dword a254, v10, s[28:31], s55 offen
buffer_load_dword a254, v12, s[24:27], s63 offen offset:384
s_waitcnt vmcnt(22) lgkmcnt(0)
s_barrier
s_memtime s[90:91]
s_memrealtime s[68:69]
Ta 20,%0,8192
Ta 22,%0,8704
Ta 24,%0,12288
Ta 26,%0,12800
Da 60,v2,0
Da 100,v3,0
Da 64,v2,2048
Da 104,v3,2048
Da 68,v2,4096
Da 108,v3,4096
Da 72,v2,6144
Da 112,v3,6144
Da 76,v2,8192
Da 116,v3,8192
Ta 28,%0,8200
Ta 30,%0,8712
Ta 32,%0,12296
Ta 34,%0,12808
s_cmp_eq_u32 s4, 0
s_cbranch_scc0 .Lhs0b
v_readfirstlane_b32 s62, v22
.Lhs0l:
s_nop 3
S_ s62,s62,1
v_cmp_eq_u32 vcc, s62, v20
s_nop 1
s_or_b32 s53, vcc_lo, vcc_hi
v_cmp_eq_u32 vcc, s62, v21
s_nop 1
s_or_b32 s54, vcc_lo, vcc_hi
s_or_b32 s53, s53, s54
s_cbranch_scc1 .Lhs0l
v_mov_b32 v22, s62
s_mov_b64 exec, 1
s_nop 1
buffer_store_dword v22, off, s[92:95], s96 sc1
s_mov_b64 exec, -1
s_nop 1
.Lhs0b:
s_cmp_eq_u32 s58, 0
s_cbranch_scc1 .Lq
.Lqy:
W_ 13
Zvaa 16,20,60
s_nop 0
Ta 36,v1,8192
Ta 38,v1,8704
W_ 14
Mvaa 16,24,100
R_ v12,a248
W_ 13
Zvaa 24,20,64
v_lshrrev_b32 v12, 4, v12
W_ 12
Mvaa 24,24,104
s_nop 0
Ta 224,v1,12288
Ta 226,v1,12800
W_ 13
Zvaa 32,20,68
v_min_u32 v10, 11, v12
W_ 12
Mvaa 32,24,108
s_mul_i32 s56, s4, 12
W_ 11
Zvaa 40,20,72
v_add_u32 v10, s56, v10
W_ 10
Mvaa 40,24,112
v_mul_u32_u24 v11, 43691, v10
W_ 9
Zvaa 48,20,76
v_lshrrev_b32 v11, 18, v11
W_ 8
Mvaa 48,24,116
W_ 4
v_lshlrev_b32 v10, 7, v10
Zvaa 52,32,116
s_nop 0
Ta 20,v1,8200
Ta 22,v1,8712
Mvaa 52,28,76
v_mul_u32_u24 v11, 11520, v11
Zvaa 44,32,112
v_add_u32 v10, v10, v11
Mvaa 44,28,72
s_nop 0
Ta 24,v1,12296
Ta 26,v1,12808
Zvaa 36,32,108
v_min_u32 v12, 9, v12
Mvaa 36,28,68
s_and_b32 s56, s6, 3
Zvaa 28,32,104
s_lshl_b32 s56, s56, 2
Mvaa 28,28,64
S_ s56,s56,s4
Zvaa 20,32,100
s_mul_i32 s56, s56, 10
Mvaa 20,28,60
s_waitcnt vmcnt(18) lgkmcnt(0)
s_barrier
v_add_u32 v12, s56, v12
Zvaa 56,36,60
S_ s65,s59,786432
Ta 28,%0,16384
Ta 30,%0,16896
Mvaa 56,224,100
S_ m0,s52,8064
v_mul_u32_u24 v12, 1280, v12
Zvaa 64,36,64
s_lshr_b32 s56, s7, 5
Mvaa 64,224,104
s_mul_i32 s56, s56, 98304
Ta 32,%0,20480
Ta 34,%0,20992
Zvaa 72,36,68
s_and_b32 s55, s59, 4095
Mvaa 72,224,108
L_ v6,28,s65,128
Zvaa 80,36,72
S_ s65,s59,884736
S_ s56,s56,s55
Mvaa 80,224,112
S_ m0,s52,9088
s_mul_i32 s55, s77, 384
Zvaa 88,36,76
s_sub_u32 s56, s56, s55
Mvaa 88,224,116
s_mov_b32 s63, 0
Zvaa 92,24,116
S_ s55,s56,3145728
Ta 36,%0,16392
Ta 38,%0,16904
Mvaa 92,20,76
L_ v6,28,s65,128
Zvaa 84,24,112
S_ s65,s59,983040
Mvaa 84,20,72
S_ m0,s52,10112
Ta 224,%0,20488
Ta 226,%0,21000
Zvaa 76,24,108
Mvaa 76,20,68
L_ v6,28,s65,128
Zvaa 68,24,104
S_ s65,s59,1081344
Mvaa 68,20,64
S_ m0,s52,11136
Zvaa 60,24,100
Mvaa 60,20,60
W_ 4
Zvaa 96,28,60
s_nop 0
Ta 20,v1,16384
Ta 22,v1,16896
Mvaa 96,32,100
L_ v6,28,s65,128
Zvaa 104,28,64
S_ s65,s60,256
Mvaa 104,32,104
S_ m0,s50,0
Ta 24,v1,20480
Ta 26,v1,20992
Zvaa 112,28,68
Mvaa 112,32,108
L_ v4,24,s65,0
Zvaa 120,28,72
S_ s65,s60,10496
Mvaa 120,32,112
S_ m0,s50,1024
Zvaa 128,28,76
Mvaa 128,32,116
W_ 4
Zvaa 132,224,116
s_nop 0
Ta 28,v1,16392
Ta 30,v1,16904
Mvaa 132,36,76
L_ v5,24,s65,0
Zvaa 124,224,112
S_ s65,s60,20736
Mvaa 124,36,72
S_ m0,s50,2048
Ta 32,v1,20488
Ta 34,v1,21000
Zvaa 116,224,108
Mvaa 116,36,68
L_ v4,24,s65,0
Zvaa 108,224,104
S_ s65,s60,30976
Mvaa 108,36,64
S_ m0,s50,3072
Zvaa 100,224,100
Mvaa 100,36,60
s_mov_b64 exec, 0
s_bitcmp1_b32 s4, 1
s_cbranch_scc1 .Lhop_q1_b
s_bitcmp1_b32 s4, 0
s_cbranch_scc1 .Lpad_q1_1
s_branch .Lpad_q1_0
.Lhop_q1_b:
s_bitcmp1_b32 s4, 0
s_cbranch_scc1 .Lpad_q1_3
s_branch .Lpad_q1_2
.Lhop_q1_r:
s_mov_b64 exec, -1
s_nop 1
.Lqy_early:
s_waitcnt vmcnt(16) lgkmcnt(0)
s_barrier
Zvaa 136,20,60
s_nop 0
Ta 36,%0,24576
Ta 38,%0,25088
Mvaa 136,24,100
Da 140,v2,20480
Zvaa 144,20,64
Mvaa 144,24,104
s_nop 0
Ta 224,%0,28672
Ta 226,%0,29184
Zvaa 152,20,68
Da 180,v3,20480
Mvaa 152,24,108
L_ v5,24,s65,0
Zvaa 160,20,72
S_ s65,s60,41216
Da 144,v2,22528
Mvaa 160,24,112
S_ m0,s50,4096
Zvaa 168,20,76
Mvaa 168,24,116
Da 184,v3,22528
Zvaa 172,32,116
s_nop 0
Ta 20,%0,24584
Ta 22,%0,25096
Mvaa 172,28,76
L_ v4,24,s65,0
Zvaa 164,32,112
S_ s65,s59,786432
Da 148,v2,24576
Mvaa 164,28,72
S_ m0,s52,16128
Ta 24,%0,28680
Ta 26,%0,29192
Zvaa 156,32,108
Mvaa 156,28,68
Da 188,v3,24576
Zvaa 148,32,104
Mvaa 148,28,64
L_ v6,28,s65,256
Zvaa 140,32,100
S_ s65,s59,884736
Da 152,v2,26624
Mvaa 140,28,60
W_ 10
S_ m0,s52,17152
Zvaa 176,36,60
s_nop 0
Ta 28,v1,24576
Ta 30,v1,25088
Mvaa 176,224,100
Da 192,v3,26624
Zvaa 184,36,64
Mvaa 184,224,104
s_nop 0
Ta 32,v1,28672
Ta 34,v1,29184
Zvaa 192,36,68
Da 156,v2,28672
Mvaa 192,224,108
L_ v6,28,s65,256
Zvaa 200,36,72
S_ s65,s59,983040
Mvaa 200,224,112
S_ m0,s52,18176
Da 196,v3,28672
Zvaa 208,36,76
Mvaa 208,224,116
W_ 9
Zvaa 212,24,116
s_nop 0
Ta 36,v1,24584
Ta 38,v1,25096
Mvaa 212,20,76
L_ v6,28,s65,256
Zvaa 204,24,112
S_ s65,s59,1081344
Mvaa 204,20,72
S_ m0,s52,19200
Ta 224,v1,28680
Ta 226,v1,29192
Zvaa 196,24,108
.Lpad_q1_0:
s_cbranch_execz .Lhop_q1_r
Mvaa 196,20,68
L_ v6,28,s65,256
Zvaa 188,24,104
Mvaa 188,20,64
Zvaa 180,24,100
Mvaa 180,20,60
s_waitcnt vmcnt(18) lgkmcnt(0)
s_barrier
Zvaa 216,28,60
S_ s65,s59,1572864
Ta 20,%0,0
Ta 22,%0,512
Mvaa 216,32,100
buffer_load_dword a254, v10, s[28:31], s55 offen
Zvaa 224,28,64
S_ m0,s52,24576
Mvaa 224,32,104
s_nop 0
Ta 24,%0,4096
Ta 26,%0,4608
Zvaa 232,28,68
Mvaa 232,32,108
L_ v6,28,s65,0
Zvaa 240,28,72
S_ s65,s59,1671168
Mvaa 240,32,112
S_ m0,s52,25600
Zvaa 248,28,76
Mvaa 248,32,116
Zvaa 252,224,116
s_nop 0
Ta 28,%0,8
Ta 30,%0,520
Mvaa 252,36,76
L_ v6,28,s65,0
Zvaa 244,224,112
S_ s65,s59,1769472
Mvaa 244,36,72
S_ m0,s52,26624
Ta 32,%0,4104
Ta 34,%0,4616
Zvaa 236,224,108
Mvaa 236,36,68
L_ v6,28,s65,0
Zvaa 228,224,104
S_ s65,s59,1867776
Mvaa 228,36,64
S_ m0,s52,27648
Zvaa 220,224,100
Mvaa 220,36,60
W_ 4
Mvaa 16,20,140
s_nop 0
Ta 36,v1,0
Ta 38,v1,512
Mvaa 16,24,180
L_ v6,28,s65,0
Mvaa 24,20,144
S_ s65,s60,384
Mvaa 24,24,184
S_ m0,s50,20480
Ta 224,v1,4096
Ta 226,v1,4608
Mvaa 32,20,148
Mvaa 32,24,188
L_ v4,24,s65,0
Mvaa 40,20,152
S_ s65,s60,10624
Mvaa 40,24,192
S_ m0,s50,21504
Mvaa 48,20,156
Mvaa 48,24,196
W_ 4
Mvaa 52,32,196
s_nop 0
Ta 20,v1,8
Ta 22,v1,520
Mvaa 52,28,156
L_ v5,24,s65,0
Mvaa 44,32,192
S_ s65,s60,20864
Mvaa 44,28,152
S_ m0,s50,22528
Ta 24,v1,4104
Ta 26,v1,4616
Mvaa 36,32,188
Mvaa 36,28,148
L_ v4,24,s65,0
Mvaa 28,32,184
S_ s65,s60,31104
Mvaa 28,28,144
S_ m0,s50,23552
Mvaa 20,32,180
Mvaa 20,28,140
s_waitcnt vmcnt(17) lgkmcnt(0)
s_barrier
Mvaa 56,36,140
s_nop 0
Ta 28,%0,8192
Ta 30,%0,8704
Mvaa 56,224,180
L_ v5,24,s65,0
Mvaa 64,36,144
S_ s65,s60,41344
Mvaa 64,224,184
S_ m0,s50,24576
Ta 32,%0,12288
Ta 34,%0,12800
Mvaa 72,36,148
Mvaa 72,224,188
L_ v4,24,s65,0
Mvaa 80,36,152
S_ s65,s59,1572864
Mvaa 80,224,192
S_ m0,s52,-128
Mvaa 88,36,156
Mvaa 88,224,196
Mvaa 92,24,196
s_nop 0
Ta 36,%0,8200
Ta 38,%0,8712
Mvaa 92,20,156
L_ v6,28,s65,128
Mvaa 84,24,192
S_ s65,s59,1671168
Mvaa 84,20,152
S_ m0,s52,896
Ta 224,%0,12296
Ta 226,%0,12808
Mvaa 76,24,188
Mvaa 76,20,148
L_ v6,28,s65,128
Mvaa 68,24,184
S_ s65,s59,1769472
Mvaa 68,20,144
S_ m0,s52,1920
Mvaa 60,24,180
Mvaa 60,20,140
W_ 4
Mvaa 96,28,140
s_nop 0
Ta 20,v1,8192
Ta 22,v1,8704
Mvaa 96,32,180
L_ v6,28,s65,128
Mvaa 104,28,144
S_ s65,s59,1867776
Mvaa 104,32,184
S_ m0,s52,2944
Ta 24,v1,12288
Ta 26,v1,12800
Mvaa 112,28,148
Mvaa 112,32,188
L_ v6,28,s65,128
Mvaa 120,28,152
Mvaa 120,32,192
Mvaa 128,28,156
Mvaa 128,32,196
W_ 4
Mvaa 132,224,196
s_nop 0
Ta 28,v1,8200
Ta 30,v1,8712
Mvaa 132,36,156
buffer_load_dword a254, v12, s[24:27], s63 offen offset:512
Mvaa 124,224,192
S_ s55,s56,3932160
Mvaa 124,36,152
s_nop 0
Ta 32,v1,12296
Ta 34,v1,12808
Mvaa 116,224,188
Mvaa 116,36,148
buffer_load_dword a254, v10, s[28:31], s55 offen
Mvaa 108,224,184
Mvaa 108,36,144
Mvaa 100,224,180
Mvaa 100,36,140
s_waitcnt vmcnt(16) lgkmcnt(0)
s_barrier
Mvaa 136,20,140
S_ s65,s59,1572864
Ta 36,%0,16384
Ta 38,%0,16896
Mvaa 136,24,180
S_ m0,s52,7936
Da 60,v2,0
Mvaa 144,20,144
Mvaa 144,24,184
s_nop 0
Ta 224,%0,20480
Ta 226,%0,20992
Mvaa 152,20,148
Da 100,v3,0
Mvaa 152,24,188
L_ v6,28,s65,256
Mvaa 160,20,152
S_ s65,s59,1671168
Da 64,v2,2048
Mvaa 160,24,192
S_ m0,s52,8960
Mvaa 168,20,156
Mvaa 168,24,196
Da 104,v3,2048
Mvaa 172,32,196
s_nop 0
Ta 20,%0,16392
Ta 22,%0,16904
Mvaa 172,28,156
L_ v6,28,s65,256
Mvaa 164,32,192
S_ s65,s59,1769472
Da 68,v2,4096
Mvaa 164,28,152
S_ m0,s52,9984
Ta 24,%0,20488
Ta 26,%0,21000
Mvaa 156,32,188
Mvaa 156,28,148
Da 108,v3,4096
Mvaa 148,32,184
Mvaa 148,28,144
L_ v6,28,s65,256
Mvaa 140,32,180
S_ s65,s59,1867776
Da 72,v2,6144
Mvaa 140,28,140
W_ 10
S_ m0,s52,11008
Mvaa 176,36,140
s_nop 0
Ta 28,v1,16384
Ta 30,v1,16896
Mvaa 176,224,180
Da 112,v3,6144
Mvaa 184,36,144
Mvaa 184,224,184
s_nop 0
Ta 32,v1,20480
Ta 34,v1,20992
Mvaa 192,36,148
Da 76,v2,8192
Mvaa 192,224,188
L_ v6,28,s65,256
Mvaa 200,36,152
Mvaa 200,224,192
Da 116,v3,8192
Mvaa 208,36,156
Mvaa 208,224,196
W_ 9
Mvaa 212,24,196
s_nop 0
Ta 36,v1,16392
Ta 38,v1,16904
Mvaa 212,20,156
buffer_load_dword a254, v12, s[24:27], s63 offen offset:640
Mvaa 204,24,192
Mvaa 204,20,152
s_nop 0
Ta 224,v1,20488
Ta 226,v1,21000
Mvaa 196,24,188
Mvaa 196,20,148
Mvaa 188,24,184
Mvaa 188,20,144
Mvaa 180,24,180
Mvaa 180,20,140
s_waitcnt vmcnt(16) lgkmcnt(0)
s_barrier
Mvaa 216,28,140
S_ s65,s59,2359296
Ta 20,%0,24576
Ta 22,%0,25088
Mvaa 216,32,180
S_ m0,s52,16384
Mvaa 224,28,144
Mvaa 224,32,184
s_nop 0
Ta 24,%0,28672
Ta 26,%0,29184
Mvaa 232,28,148
Mvaa 232,32,188
L_ v6,28,s65,0
Mvaa 240,28,152
S_ s65,s59,2457600
Mvaa 240,32,192
S_ m0,s52,17408
Mvaa 248,28,156
Mvaa 248,32,196
Mvaa 252,224,196
s_nop 0
Ta 28,%0,24584
Ta 30,%0,25096
Mvaa 252,36,156
L_ v6,28,s65,0
Mvaa 244,224,192
S_ s65,s59,2555904
Mvaa 244,36,152
S_ m0,s52,18432
Ta 32,%0,28680
Ta 34,%0,29192
Mvaa 236,224,188
Mvaa 236,36,148
L_ v6,28,s65,0
Mvaa 228,224,184
S_ s65,s59,2654208
Mvaa 228,36,144
S_ m0,s52,19456
Mvaa 220,224,180
Mvaa 220,36,140
W_ 4
Mvaa 16,20,60
s_nop 0
Ta 36,v1,24576
Ta 38,v1,25088
Mvaa 16,24,100
L_ v6,28,s65,0
Mvaa 24,20,64
S_ s65,s60,512
Mvaa 24,24,104
S_ m0,s50,0
Ta 224,v1,28672
Ta 226,v1,29184
Mvaa 32,20,68
Mvaa 32,24,108
L_ v4,24,s65,0
Mvaa 40,20,72
S_ s65,s60,10752
Mvaa 40,24,112
S_ m0,s50,1024
Mvaa 48,20,76
Mvaa 48,24,116
W_ 4
Mvaa 52,32,116
s_nop 0
Ta 20,v1,24584
Ta 22,v1,25096
Mvaa 52,28,76
L_ v5,24,s65,0
Mvaa 44,32,112
S_ s65,s60,20992
Mvaa 44,28,72
S_ m0,s50,2048
Ta 24,v1,28680
Ta 26,v1,29192
Mvaa 36,32,108
Mvaa 36,28,68
L_ v4,24,s65,0
Mvaa 28,32,104
S_ s65,s60,31232
Mvaa 28,28,64
S_ m0,s50,3072
Mvaa 20,32,100
Mvaa 20,28,60
s_waitcnt vmcnt(14) lgkmcnt(0)
s_barrier
Mvaa 56,36,60
s_nop 0
Ta 28,%0,0
Ta 30,%0,512
Mvaa 56,224,100
L_ v5,24,s65,0
Mvaa 64,36,64
S_ s65,s60,41472
Mvaa 64,224,104
S_ m0,s50,4096
Ta 32,%0,4096
Ta 34,%0,4608
Mvaa 72,36,68
Mvaa 72,224,108
L_ v4,24,s65,0
Mvaa 80,36,72
S_ s65,s59,2359296
Mvaa 80,224,112
S_ m0,s52,24448
Mvaa 88,36,76
Mvaa 88,224,116
Mvaa 92,24,116
S_ s55,s56,4718592
Ta 36,%0,8
Ta 38,%0,520
Mvaa 92,20,76
L_ v6,28,s65,128
Mvaa 84,24,112
S_ s65,s59,2457600
Mvaa 84,20,72
S_ m0,s52,25472
Ta 224,%0,4104
Ta 226,%0,4616
Mvaa 76,24,108
Mvaa 76,20,68
L_ v6,28,s65,128
Mvaa 68,24,104
S_ s65,s59,2555904
Mvaa 68,20,64
S_ m0,s52,26496
Mvaa 60,24,100
Mvaa 60,20,60
W_ 4
Mvaa 96,28,60
s_nop 0
Ta 20,v1,0
Ta 22,v1,512
Mvaa 96,32,100
L_ v6,28,s65,128
Mvaa 104,28,64
S_ s65,s59,2654208
Mvaa 104,32,104
S_ m0,s52,27520
Ta 24,v1,4096
Ta 26,v1,4608
Mvaa 112,28,68
Mvaa 112,32,108
L_ v6,28,s65,128
Mvaa 120,28,72
Mvaa 120,32,112
Mvaa 128,28,76
Mvaa 128,32,116
W_ 4
Mvaa 132,224,116
s_nop 0
Ta 28,v1,8
Ta 30,v1,520
Mvaa 132,36,76
buffer_load_dword a254, v10, s[28:31], s55 offen
Mvaa 124,224,112
Mvaa 124,36,72
s_nop 0
Ta 32,v1,4104
Ta 34,v1,4616
Mvaa 116,224,108
Mvaa 116,36,68
buffer_load_dword a254, v12, s[24:27], s63 offen offset:768
Mvaa 108,224,104
Mvaa 108,36,64
Mvaa 100,224,100
Mvaa 100,36,60
s_waitcnt vmcnt(16) lgkmcnt(0)
s_barrier
Mvaa 136,20,60
S_ s65,s59,2359296
Ta 36,%0,8192
Ta 38,%0,8704
Mvaa 136,24,100
S_ m0,s52,-256
Da 140,v2,20480
Mvaa 144,20,64
Mvaa 144,24,104
s_nop 0
Ta 224,%0,12288
Ta 226,%0,12800
Mvaa 152,20,68
Da 180,v3,20480
Mvaa 152,24,108
L_ v6,28,s65,256
Mvaa 160,20,72
S_ s65,s59,2457600
Da 144,v2,22528
Mvaa 160,24,112
S_ m0,s52,768
Mvaa 168,20,76
Mvaa 168,24,116
Da 184,v3,22528
Mvaa 172,32,116
s_nop 0
Ta 20,%0,8200
Ta 22,%0,8712
Mvaa 172,28,76
L_ v6,28,s65,256
Mvaa 164,32,112
S_ s65,s59,2555904
Da 148,v2,24576
Mvaa 164,28,72
S_ m0,s52,1792
Ta 24,%0,12296
Ta 26,%0,12808
Mvaa 156,32,108
Mvaa 156,28,68
Da 188,v3,24576
Mvaa 148,32,104
Mvaa 148,28,64
L_ v6,28,s65,256
Mvaa 140,32,100
S_ s65,s59,2654208
Da 152,v2,26624
Mvaa 140,28,60
W_ 10
S_ m0,s52,2816
Mvaa 176,36,60
s_nop 0
Ta 28,v1,8192
Ta 30,v1,8704
Mvaa 176,224,100
Da 192,v3,26624
Mvaa 184,36,64
Mvaa 184,224,104
s_nop 0
Ta 32,v1,12288
Ta 34,v1,12800
Mvaa 192,36,68
Da 156,v2,28672
Mvaa 192,224,108
L_ v6,28,s65,256
Mvaa 200,36,72
Mvaa 200,224,112
Da 196,v3,28672
Mvaa 208,36,76
Mvaa 208,224,116
W_ 9
Mvaa 212,24,116
s_nop 0
Ta 36,v1,8200
Ta 38,v1,8712
Mvaa 212,20,76
Mvaa 204,24,112
Mvaa 204,20,72
s_nop 0
Ta 224,v1,12296
Ta 226,v1,12808
Mvaa 196,24,108
Mvaa 196,20,68
Mvaa 188,24,104
Mvaa 188,20,64
Mvaa 180,24,100
Mvaa 180,20,60
s_waitcnt vmcnt(15) lgkmcnt(0)
s_barrier
Mvaa 216,28,60
S_ s65,s59,3145728
Ta 20,%0,16384
Ta 22,%0,16896
Mvaa 216,32,100
S_ m0,s52,8192
Mvaa 224,28,64
Mvaa 224,32,104
.Lpad_q1_1:
s_cbranch_execz .Lhop_q1_r
Ta 24,%0,20480
Ta 26,%0,20992
Mvaa 232,28,68
Mvaa 232,32,108
L_ v6,28,s65,0
Mvaa 240,28,72
S_ s65,s59,3244032
Mvaa 240,32,112
S_ m0,s52,9216
Mvaa 248,28,76
Mvaa 248,32,116
Mvaa 252,224,116
s_nop 0
Ta 28,%0,16392
Ta 30,%0,16904
Mvaa 252,36,76
L_ v6,28,s65,0
Mvaa 244,224,112
S_ s65,s59,3342336
Mvaa 244,36,72
S_ m0,s52,10240
Ta 32,%0,20488
Ta 34,%0,21000
Mvaa 236,224,108
Mvaa 236,36,68
L_ v6,28,s65,0
Mvaa 228,224,104
S_ s65,s59,3440640
Mvaa 228,36,64
S_ m0,s52,11264
Mvaa 220,224,100
Mvaa 220,36,60
W_ 4
Mvaa 16,20,140
s_nop 0
Ta 36,v1,16384
Ta 38,v1,16896
Mvaa 16,24,180
L_ v6,28,s65,0
Mvaa 24,20,144
S_ s65,s60,640
Mvaa 24,24,184
S_ m0,s50,20480
Ta 224,v1,20480
Ta 226,v1,20992
Mvaa 32,20,148
Mvaa 32,24,188
L_ v4,24,s65,0
Mvaa 40,20,152
S_ s65,s60,10880
Mvaa 40,24,192
S_ m0,s50,21504
Mvaa 48,20,156
Mvaa 48,24,196
W_ 4
Mvaa 52,32,196
s_nop 0
Ta 20,v1,16392
Ta 22,v1,16904
Mvaa 52,28,156
L_ v5,24,s65,0
Mvaa 44,32,192
S_ s65,s60,21120
Mvaa 44,28,152
S_ m0,s50,22528
Ta 24,v1,20488
Ta 26,v1,21000
Mvaa 36,32,188
Mvaa 36,28,148
L_ v4,24,s65,0
Mvaa 28,32,184
S_ s65,s60,31360
Mvaa 28,28,144
S_ m0,s50,23552
Mvaa 20,32,180
Mvaa 20,28,140
s_waitcnt vmcnt(13) lgkmcnt(0)
s_barrier
Mvaa 56,36,140
s_nop 0
Ta 28,%0,24576
Ta 30,%0,25088
Mvaa 56,224,180
L_ v5,24,s65,0
Mvaa 64,36,144
S_ s65,s60,41600
Mvaa 64,224,184
S_ m0,s50,24576
Ta 32,%0,28672
Ta 34,%0,29184
Mvaa 72,36,148
Mvaa 72,224,188
L_ v4,24,s65,0
Mvaa 80,36,152
S_ s65,s59,3145728
Mvaa 80,224,192
S_ m0,s52,16256
Mvaa 88,36,156
Mvaa 88,224,196
Mvaa 92,24,196
S_ s55,s56,5505024
Ta 36,%0,24584
Ta 38,%0,25096
Mvaa 92,20,156
L_ v6,28,s65,128
Mvaa 84,24,192
S_ s65,s59,3244032
Mvaa 84,20,152
S_ m0,s52,17280
Ta 224,%0,28680
Ta 226,%0,29192
Mvaa 76,24,188
Mvaa 76,20,148
L_ v6,28,s65,128
Mvaa 68,24,184
S_ s65,s59,3342336
Mvaa 68,20,144
S_ m0,s52,18304
Mvaa 60,24,180
Mvaa 60,20,140
W_ 4
Mvaa 96,28,140
s_nop 0
Ta 20,v1,24576
Ta 22,v1,25088
Mvaa 96,32,180
L_ v6,28,s65,128
Mvaa 104,28,144
S_ s65,s59,3440640
Mvaa 104,32,184
S_ m0,s52,19328
Ta 24,v1,28672
Ta 26,v1,29184
Mvaa 112,28,148
Mvaa 112,32,188
L_ v6,28,s65,128
Mvaa 120,28,152
Mvaa 120,32,192
Mvaa 128,28,156
Mvaa 128,32,196
W_ 4
Mvaa 132,224,196
s_nop 0
Ta 28,v1,24584
Ta 30,v1,25096
Mvaa 132,36,156
buffer_load_dword a254, v10, s[28:31], s55 offen
Mvaa 124,224,192
Mvaa 124,36,152
s_nop 0
Ta 32,v1,28680
Ta 34,v1,29192
Mvaa 116,224,188
Mvaa 116,36,148
buffer_load_dword a254, v12, s[24:27], s63 offen offset:896
Mvaa 108,224,184
Mvaa 108,36,144
Mvaa 100,224,180
Mvaa 100,36,140
s_waitcnt vmcnt(15) lgkmcnt(0)
s_barrier
Mvaa 136,20,140
S_ s65,s59,3145728
Ta 36,%0,0
Ta 38,%0,512
Mvaa 136,24,180
S_ m0,s52,24320
Da 60,v2,0
Mvaa 144,20,144
Mvaa 144,24,184
s_nop 0
Ta 224,%0,4096
Ta 226,%0,4608
Mvaa 152,20,148
Da 100,v3,0
Mvaa 152,24,188
L_ v6,28,s65,256
Mvaa 160,20,152
S_ s65,s59,3244032
Da 64,v2,2048
Mvaa 160,24,192
S_ m0,s52,25344
Mvaa 168,20,156
Mvaa 168,24,196
Da 104,v3,2048
Mvaa 172,32,196
s_nop 0
Ta 20,%0,8
Ta 22,%0,520
Mvaa 172,28,156
L_ v6,28,s65,256
Mvaa 164,32,192
S_ s65,s59,3342336
Da 68,v2,4096
Mvaa 164,28,152
S_ m0,s52,26368
Ta 24,%0,4104
Ta 26,%0,4616
Mvaa 156,32,188
Mvaa 156,28,148
Da 108,v3,4096
Mvaa 148,32,184
Mvaa 148,28,144
L_ v6,28,s65,256
Mvaa 140,32,180
S_ s65,s59,3440640
Da 72,v2,6144
Mvaa 140,28,140
W_ 10
S_ m0,s52,27392
Mvaa 176,36,140
s_nop 0
Ta 28,v1,0
Ta 30,v1,512
Mvaa 176,224,180
Da 112,v3,6144
Mvaa 184,36,144
Mvaa 184,224,184
s_nop 0
Ta 32,v1,4096
Ta 34,v1,4608
Mvaa 192,36,148
Da 76,v2,8192
Mvaa 192,224,188
L_ v6,28,s65,256
Mvaa 200,36,152
Mvaa 200,224,192
Da 116,v3,8192
Mvaa 208,36,156
Mvaa 208,224,196
W_ 9
Mvaa 212,24,196
s_nop 0
Ta 36,v1,8
Ta 38,v1,520
Mvaa 212,20,156
Mvaa 204,24,192
Mvaa 204,20,152
s_nop 0
Ta 224,v1,4104
Ta 226,v1,4616
Mvaa 196,24,188
Mvaa 196,20,148
Mvaa 188,24,184
Mvaa 188,20,144
Mvaa 180,24,180
Mvaa 180,20,140
s_waitcnt vmcnt(15) lgkmcnt(0)
s_barrier
Mvaa 216,28,140
S_ s65,s59,3932160
Ta 20,%0,8192
Ta 22,%0,8704
Mvaa 216,32,180
S_ m0,s52,0
Mvaa 224,28,144
Mvaa 224,32,184
s_nop 0
Ta 24,%0,12288
Ta 26,%0,12800
Mvaa 232,28,148
Mvaa 232,32,188
L_ v6,28,s65,0
Mvaa 240,28,152
S_ s65,s59,4030464
Mvaa 240,32,192
S_ m0,s52,1024
Mvaa 248,28,156
Mvaa 248,32,196
Mvaa 252,224,196
s_nop 0
Ta 28,%0,8200
Ta 30,%0,8712
Mvaa 252,36,156
L_ v6,28,s65,0
Mvaa 244,224,192
S_ s65,s59,4128768
Mvaa 244,36,152
S_ m0,s52,2048
Ta 32,%0,12296
Ta 34,%0,12808
Mvaa 236,224,188
Mvaa 236,36,148
L_ v6,28,s65,0
Mvaa 228,224,184
S_ s65,s59,4227072
Mvaa 228,36,144
S_ m0,s52,3072
Mvaa 220,224,180
Mvaa 220,36,140
W_ 4
Mvaa 16,20,60
s_nop 0
Ta 36,v1,8192
Ta 38,v1,8704
Mvaa 16,24,100
L_ v6,28,s65,0
Mvaa 24,20,64
S_ s65,s60,768
R_ v13,a248
Mvaa 24,24,104
S_ m0,s50,0
Ta 224,v1,12288
Ta 226,v1,12800
Mvaa 32,20,68
v_min_u32 v13, 240, v13
Mvaa 32,24,108
L_ v4,24,s65,0
Mvaa 40,20,72
S_ s65,s60,11008
v_lshlrev_b32 v13, 3, v13
Mvaa 40,24,112
S_ m0,s50,1024
Mvaa 48,20,76
Mvaa 48,24,116
W_ 4
Mvaa 52,32,116
s_nop 0
Ta 20,v1,8200
Ta 22,v1,8712
Mvaa 52,28,76
L_ v5,24,s65,0
Mvaa 44,32,112
S_ s65,s60,21248
Mvaa 44,28,72
S_ m0,s50,2048
Ta 24,v1,12296
Ta 26,v1,12808
Mvaa 36,32,108
Mvaa 36,28,68
L_ v4,24,s65,0
Mvaa 28,32,104
S_ s65,s60,31488
Mvaa 28,28,64
S_ m0,s50,3072
Mvaa 20,32,100
Mvaa 20,28,60
s_waitcnt vmcnt(13) lgkmcnt(0)
s_barrier
Mvaa 56,36,60
s_nop 0
Ta 28,%0,16384
Ta 30,%0,16896
Mvaa 56,224,100
L_ v5,24,s65,0
Mvaa 64,36,64
S_ s65,s60,41728
Mvaa 64,224,104
S_ m0,s50,4096
Ta 32,%0,20480
Ta 34,%0,20992
Mvaa 72,36,68
Mvaa 72,224,108
L_ v4,24,s65,0
Mvaa 80,36,72
S_ s65,s59,3932160
Mvaa 80,224,112
S_ m0,s52,8064
Mvaa 88,36,76
Mvaa 88,224,116
Mvaa 92,24,116
S_ s55,s56,6291456
Ta 36,%0,16392
Ta 38,%0,16904
Mvaa 92,20,76
L_ v6,28,s65,128
Mvaa 84,24,112
S_ s65,s59,4030464
Mvaa 84,20,72
S_ m0,s52,9088
Ta 224,%0,20488
Ta 226,%0,21000
Mvaa 76,24,108
Mvaa 76,20,68
L_ v6,28,s65,128
Mvaa 68,24,104
S_ s65,s59,4128768
Mvaa 68,20,64
S_ m0,s52,10112
Mvaa 60,24,100
Mvaa 60,20,60
W_ 4
Mvaa 96,28,60
s_nop 0
Ta 20,v1,16384
Ta 22,v1,16896
Mvaa 96,32,100
L_ v6,28,s65,128
Mvaa 104,28,64
S_ s65,s59,4227072
Mvaa 104,32,104
S_ m0,s52,11136
Ta 24,v1,20480
Ta 26,v1,20992
Mvaa 112,28,68
Mvaa 112,32,108
L_ v6,28,s65,128
Mvaa 120,28,72
Mvaa 120,32,112
Mvaa 128,28,76
Mvaa 128,32,116
W_ 4
Mvaa 132,224,116
s_nop 0
Ta 28,v1,16392
Ta 30,v1,16904
Mvaa 132,36,76
buffer_load_dword a220, v13, s[44:47], s66 offen
Mvaa 124,224,112
Mvaa 124,36,72
s_nop 0
Ta 32,v1,20488
Ta 34,v1,21000
Mvaa 116,224,108
Mvaa 116,36,68
buffer_load_dword a254, v10, s[28:31], s55 offen
Mvaa 108,224,104
Mvaa 108,36,64
Mvaa 100,224,100
Mvaa 100,36,60
s_waitcnt vmcnt(15) lgkmcnt(0)
s_barrier
Mvaa 136,20,60
S_ s65,s59,3932160
Ta 36,%0,24576
Ta 38,%0,25088
Mvaa 136,24,100
S_ m0,s52,16128
Da 140,v2,20480
Mvaa 144,20,64
Mvaa 144,24,104
s_nop 0
Ta 224,%0,28672
Ta 226,%0,29184
Mvaa 152,20,68
Da 180,v3,20480
Mvaa 152,24,108
L_ v6,28,s65,256
Mvaa 160,20,72
S_ s65,s59,4030464
Da 144,v2,22528
Mvaa 160,24,112
S_ m0,s52,17152
Mvaa 168,20,76
Mvaa 168,24,116
Da 184,v3,22528
Mvaa 172,32,116
s_nop 0
Ta 20,%0,24584
Ta 22,%0,25096
Mvaa 172,28,76
L_ v6,28,s65,256
Mvaa 164,32,112
S_ s65,s59,4128768
Da 148,v2,24576
Mvaa 164,28,72
S_ m0,s52,18176
Ta 24,%0,28680
Ta 26,%0,29192
Mvaa 156,32,108
Mvaa 156,28,68
Da 188,v3,24576
Mvaa 148,32,104
Mvaa 148,28,64
L_ v6,28,s65,256
Mvaa 140,32,100
S_ s65,s59,4227072
Da 152,v2,26624
Mvaa 140,28,60
W_ 10
S_ m0,s52,19200
Mvaa 176,36,60
s_nop 0
Ta 28,v1,24576
Ta 30,v1,25088
Mvaa 176,224,100
Da 192,v3,26624
Mvaa 184,36,64
Mvaa 184,224,104
s_nop 0
Ta 32,v1,28672
Ta 34,v1,29184
Mvaa 192,36,68
Da 156,v2,28672
Mvaa 192,224,108
L_ v6,28,s65,256
Mvaa 200,36,72
Mvaa 200,224,112
Da 196,v3,28672
Mvaa 208,36,76
Mvaa 208,224,116
W_ 9
Mvaa 212,24,116
s_nop 0
Ta 36,v1,24584
Ta 38,v1,25096
Mvaa 212,20,76
buffer_load_dword a254, v12, s[24:27], s63 offen offset:1024
Mvaa 204,24,112
Mvaa 204,20,72
s_nop 0
Ta 224,v1,28680
Ta 226,v1,29192
Mvaa 196,24,108
Mvaa 196,20,68
Mvaa 188,24,104
Mvaa 188,20,64
Mvaa 180,24,100
Mvaa 180,20,60
s_waitcnt vmcnt(16) lgkmcnt(0)
s_barrier
Mvaa 216,28,60
S_ s65,s59,4718592
Ta 20,%0,0
Ta 22,%0,512
Mvaa 216,32,100
S_ m0,s52,24576
Mvaa 224,28,64
Mvaa 224,32,104
s_nop 0
Ta 24,%0,4096
Ta 26,%0,4608
Mvaa 232,28,68
Mvaa 232,32,108
L_ v6,28,s65,0
Mvaa 240,28,72
S_ s65,s59,4816896
Mvaa 240,32,112
S_ m0,s52,25600
Mvaa 248,28,76
Mvaa 248,32,116
Mvaa 252,224,116
s_nop 0
Ta 28,%0,8
Ta 30,%0,520
Mvaa 252,36,76
L_ v6,28,s65,0
Mvaa 244,224,112
S_ s65,s59,4915200
Mvaa 244,36,72
S_ m0,s52,26624
Ta 32,%0,4104
Ta 34,%0,4616
Mvaa 236,224,108
.Lpad_q1_2:
s_cbranch_execz .Lhop_q1_r
Mvaa 236,36,68
L_ v6,28,s65,0
Mvaa 228,224,104
S_ s65,s59,5013504
Mvaa 228,36,64
S_ m0,s52,27648
Mvaa 220,224,100
Mvaa 220,36,60
W_ 4
Mvaa 16,20,140
s_nop 0
Ta 36,v1,0
Ta 38,v1,512
Mvaa 16,24,180
L_ v6,28,s65,0
Mvaa 24,20,144
S_ s65,s60,896
Mvaa 24,24,184
S_ m0,s50,20480
Ta 224,v1,4096
Ta 226,v1,4608
Mvaa 32,20,148
Mvaa 32,24,188
L_ v4,24,s65,0
Mvaa 40,20,152
S_ s65,s60,11136
Mvaa 40,24,192
S_ m0,s50,21504
Mvaa 48,20,156
Mvaa 48,24,196
W_ 4
Mvaa 52,32,196
s_nop 0
Ta 20,v1,8
Ta 22,v1,520
Mvaa 52,28,156
L_ v5,24,s65,0
Mvaa 44,32,192
S_ s65,s60,21376
Mvaa 44,28,152
S_ m0,s50,22528
Ta 24,v1,4104
Ta 26,v1,4616
Mvaa 36,32,188
Mvaa 36,28,148
L_ v4,24,s65,0
Mvaa 28,32,184
S_ s65,s60,31616
Mvaa 28,28,144
S_ m0,s50,23552
Mvaa 20,32,180
Mvaa 20,28,140
s_waitcnt vmcnt(14) lgkmcnt(0)
s_barrier
Mvaa 56,36,140
s_nop 0
Ta 28,%0,8192
Ta 30,%0,8704
Mvaa 56,224,180
L_ v5,24,s65,0
Mvaa 64,36,144
S_ s65,s60,41856
Mvaa 64,224,184
S_ m0,s50,24576
Ta 32,%0,12288
Ta 34,%0,12800
Mvaa 72,36,148
Mvaa 72,224,188
L_ v4,24,s65,0
Mvaa 80,36,152
S_ s65,s59,4718592
Mvaa 80,224,192
S_ m0,s52,-128
Mvaa 88,36,156
Mvaa 88,224,196
Mvaa 92,24,196
S_ s55,s56,7077888
Ta 36,%0,8200
Ta 38,%0,8712
Mvaa 92,20,156
L_ v6,28,s65,128
Mvaa 84,24,192
S_ s65,s59,4816896
Mvaa 84,20,152
S_ m0,s52,896
Ta 224,%0,12296
Ta 226,%0,12808
Mvaa 76,24,188
Mvaa 76,20,148
L_ v6,28,s65,128
Mvaa 68,24,184
S_ s65,s59,4915200
Mvaa 68,20,144
S_ m0,s52,1920
Mvaa 60,24,180
Mvaa 60,20,140
W_ 4
Mvaa 96,28,140
s_nop 0
Ta 20,v1,8192
Ta 22,v1,8704
Mvaa 96,32,180
L_ v6,28,s65,128
Mvaa 104,28,144
S_ s65,s59,5013504
Mvaa 104,32,184
S_ m0,s52,2944
Ta 24,v1,12288
Ta 26,v1,12800
Mvaa 112,28,148
Mvaa 112,32,188
L_ v6,28,s65,128
Mvaa 120,28,152
Mvaa 120,32,192
Mvaa 128,28,156
Mvaa 128,32,196
W_ 4
Mvaa 132,224,196
s_nop 0
Ta 28,v1,8200
Ta 30,v1,8712
Mvaa 132,36,156
buffer_load_dword a254, v10, s[28:31], s55 offen
Mvaa 124,224,192
Mvaa 124,36,152
s_nop 0
Ta 32,v1,12296
Ta 34,v1,12808
Mvaa 116,224,188
Mvaa 116,36,148
buffer_load_dword a254, v12, s[24:27], s63 offen offset:1152
Mvaa 108,224,184
Mvaa 108,36,144
Mvaa 100,224,180
Mvaa 100,36,140
s_waitcnt vmcnt(16) lgkmcnt(0)
s_barrier
Mvaa 136,20,140
S_ s65,s59,4718592
Ta 36,%0,16384
Ta 38,%0,16896
Mvaa 136,24,180
S_ m0,s52,7936
Da 60,v2,0
Mvaa 144,20,144
Mvaa 144,24,184
s_nop 0
Ta 224,%0,20480
Ta 226,%0,20992
Mvaa 152,20,148
Da 100,v3,0
Mvaa 152,24,188
L_ v6,28,s65,256
Mvaa 160,20,152
S_ s65,s59,4816896
Da 64,v2,2048
Mvaa 160,24,192
S_ m0,s52,8960
Mvaa 168,20,156
Mvaa 168,24,196
Da 104,v3,2048
Mvaa 172,32,196
s_nop 0
Ta 20,%0,16392
Ta 22,%0,16904
Mvaa 172,28,156
L_ v6,28,s65,256
Mvaa 164,32,192
S_ s65,s59,4915200
Da 68,v2,4096
Mvaa 164,28,152
S_ m0,s52,9984
Ta 24,%0,20488
Ta 26,%0,21000
Mvaa 156,32,188
Mvaa 156,28,148
Da 108,v3,4096
)A"
R"A(Mvaa 148,32,184
Mvaa 148,28,144
L_ v6,28,s65,256
Mvaa 140,32,180
S_ s65,s59,5013504
Da 72,v2,6144
Mvaa 140,28,140
W_ 10
S_ m0,s52,11008
Mvaa 176,36,140
s_nop 0
Ta 28,v1,16384
Ta 30,v1,16896
Mvaa 176,224,180
Da 112,v3,6144
Mvaa 184,36,144
Mvaa 184,224,184
s_nop 0
Ta 32,v1,20480
Ta 34,v1,20992
Mvaa 192,36,148
Da 76,v2,8192
Mvaa 192,224,188
L_ v6,28,s65,256
Mvaa 200,36,152
Mvaa 200,224,192
Da 116,v3,8192
Mvaa 208,36,156
Mvaa 208,224,196
W_ 9
Mvaa 212,24,196
s_nop 0
Ta 36,v1,16392
Ta 38,v1,16904
Mvaa 212,20,156
Mvaa 204,24,192
Mvaa 204,20,152
s_nop 0
Ta 224,v1,20488
Ta 226,v1,21000
Mvaa 196,24,188
Mvaa 196,20,148
Mvaa 188,24,184
Mvaa 188,20,144
Mvaa 180,24,180
Mvaa 180,20,140
s_waitcnt vmcnt(15) lgkmcnt(0)
s_barrier
Mvaa 216,28,140
S_ s65,s59,5505024
Ta 20,%0,24576
Ta 22,%0,25088
Mvaa 216,32,180
S_ m0,s52,16384
Mvaa 224,28,144
Mvaa 224,32,184
s_nop 0
Ta 24,%0,28672
Ta 26,%0,29184
Mvaa 232,28,148
Mvaa 232,32,188
L_ v6,28,s65,0
Mvaa 240,28,152
S_ s65,s59,5603328
Mvaa 240,32,192
S_ m0,s52,17408
Mvaa 248,28,156
Mvaa 248,32,196
Mvaa 252,224,196
s_nop 0
Ta 28,%0,24584
Ta 30,%0,25096
Mvaa 252,36,156
L_ v6,28,s65,0
Mvaa 244,224,192
S_ s65,s59,5701632
Mvaa 244,36,152
S_ m0,s52,18432
Ta 32,%0,28680
Ta 34,%0,29192
Mvaa 236,224,188
Mvaa 236,36,148
L_ v6,28,s65,0
Mvaa 228,224,184
S_ s65,s59,5799936
Mvaa 228,36,144
S_ m0,s52,19456
Mvaa 220,224,180
Mvaa 220,36,140
W_ 4
Mvaa 16,20,60
s_nop 0
Ta 36,v1,24576
Ta 38,v1,25088
Mvaa 16,24,100
L_ v6,28,s65,0
Mvaa 24,20,64
S_ s65,s60,1024
Mvaa 24,24,104
S_ m0,s50,0
Ta 224,v1,28672
Ta 226,v1,29184
Mvaa 32,20,68
Mvaa 32,24,108
L_ v4,24,s65,0
Mvaa 40,20,72
S_ s65,s60,11264
Mvaa 40,24,112
S_ m0,s50,1024
Mvaa 48,20,76
Mvaa 48,24,116
W_ 4
Mvaa 52,32,116
s_nop 0
Ta 20,v1,24584
Ta 22,v1,25096
Mvaa 52,28,76
L_ v5,24,s65,0
Mvaa 44,32,112
S_ s65,s60,21504
Mvaa 44,28,72
S_ m0,s50,2048
Ta 24,v1,28680
Ta 26,v1,29192
Mvaa 36,32,108
Mvaa 36,28,68
L_ v4,24,s65,0
Mvaa 28,32,104
S_ s65,s60,31744
Mvaa 28,28,64
S_ m0,s50,3072
Mvaa 20,32,100
Mvaa 20,28,60
s_waitcnt vmcnt(13) lgkmcnt(0)
s_barrier
Mvaa 56,36,60
s_nop 0
Ta 28,%0,0
Ta 30,%0,512
Mvaa 56,224,100
L_ v5,24,s65,0
Mvaa 64,36,64
S_ s65,s60,41984
Mvaa 64,224,104
S_ m0,s50,4096
Ta 32,%0,4096
Ta 34,%0,4608
Mvaa 72,36,68
Mvaa 72,224,108
L_ v4,24,s65,0
Mvaa 80,36,72
S_ s65,s59,5505024
Mvaa 80,224,112
S_ m0,s52,24448
Mvaa 88,36,76
Mvaa 88,224,116
Mvaa 92,24,116
s_nop 0
Ta 36,%0,8
Ta 38,%0,520
Mvaa 92,20,76
L_ v6,28,s65,128
Mvaa 84,24,112
S_ s65,s59,5603328
Mvaa 84,20,72
S_ m0,s52,25472
Ta 224,%0,4104
Ta 226,%0,4616
Mvaa 76,24,108
Mvaa 76,20,68
L_ v6,28,s65,128
Mvaa 68,24,104
S_ s65,s59,5701632
Mvaa 68,20,64
S_ m0,s52,26496
Mvaa 60,24,100
Mvaa 60,20,60
W_ 4
Mvaa 96,28,60
s_nop 0
Ta 20,v1,0
Ta 22,v1,512
Mvaa 96,32,100
L_ v6,28,s65,128
Mvaa 104,28,64
S_ s65,s59,5799936
Mvaa 104,32,104
S_ m0,s52,27520
Ta 24,v1,4096
Ta 26,v1,4608
Mvaa 112,28,68
Mvaa 112,32,108
L_ v6,28,s65,128
Mvaa 120,28,72
Mvaa 120,32,112
Mvaa 128,28,76
Mvaa 128,32,116
W_ 4
Mvaa 132,224,116
s_nop 0
Ta 28,v1,8
Ta 30,v1,520
Mvaa 132,36,76
Mvaa 124,224,112
Mvaa 124,36,72
s_nop 0
Ta 32,v1,4104
Ta 34,v1,4616
Mvaa 116,224,108
Mvaa 116,36,68
Mvaa 108,224,104
Mvaa 108,36,64
Mvaa 100,224,100
Mvaa 100,36,60
s_waitcnt vmcnt(13) lgkmcnt(0)
s_barrier
Mvaa 136,20,60
S_ s65,s59,5505024
Ta 36,%0,8192
Ta 38,%0,8704
Mvaa 136,24,100
S_ m0,s52,-256
Da 140,v2,20480
Mvaa 144,20,64
Mvaa 144,24,104
s_nop 0
Ta 224,%0,12288
Ta 226,%0,12800
Mvaa 152,20,68
Da 180,v3,20480
Mvaa 152,24,108
L_ v6,28,s65,256
Mvaa 160,20,72
S_ s65,s59,5603328
Da 144,v2,22528
Mvaa 160,24,112
S_ m0,s52,768
Mvaa 168,20,76
Mvaa 168,24,116
Da 184,v3,22528
Mvaa 172,32,116
s_nop 0
Ta 20,%0,8200
Ta 22,%0,8712
Mvaa 172,28,76
L_ v6,28,s65,256
Mvaa 164,32,112
S_ s65,s59,5701632
Da 148,v2,24576
Mvaa 164,28,72
S_ m0,s52,1792
Ta 24,%0,12296
Ta 26,%0,12808
Mvaa 156,32,108
Mvaa 156,28,68
Da 188,v3,24576
Mvaa 148,32,104
Mvaa 148,28,64
L_ v6,28,s65,256
Mvaa 140,32,100
S_ s65,s59,5799936
Da 152,v2,26624
Mvaa 140,28,60
W_ 10
S_ m0,s52,2816
Mvaa 176,36,60
s_nop 0
Ta 28,v1,8192
Ta 30,v1,8704
Mvaa 176,224,100
Da 192,v3,26624
Mvaa 184,36,64
Mvaa 184,224,104
s_nop 0
Ta 32,v1,12288
Ta 34,v1,12800
Mvaa 192,36,68
Da 156,v2,28672
Mvaa 192,224,108
L_ v6,28,s65,256
Mvaa 200,36,72
Mvaa 200,224,112
Da 196,v3,28672
Mvaa 208,36,76
Mvaa 208,224,116
W_ 9
Mvaa 212,24,116
s_nop 0
Ta 36,v1,8200
Ta 38,v1,8712
Mvaa 212,20,76
Mvaa 204,24,112
Mvaa 204,20,72
s_nop 0
Ta 224,v1,12296
Ta 226,v1,12808
Mvaa 196,24,108
Mvaa 196,20,68
Mvaa 188,24,104
Mvaa 188,20,64
Mvaa 180,24,100
Mvaa 180,20,60
s_waitcnt vmcnt(13) lgkmcnt(0)
s_barrier
Mvaa 216,28,60
S_ s65,s59,6291456
Ta 20,%0,16384
Ta 22,%0,16896
Mvaa 216,32,100
S_ m0,s52,7936
Mvaa 224,28,64
Mvaa 224,32,104
s_nop 0
Ta 24,%0,20480
Ta 26,%0,20992
Mvaa 232,28,68
Mvaa 232,32,108
L_ v6,28,s65,256
Mvaa 240,28,72
S_ s65,s59,6389760
Mvaa 240,32,112
S_ m0,s52,8960
Mvaa 248,28,76
Mvaa 248,32,116
Mvaa 252,224,116
s_nop 0
Ta 28,%0,16392
Ta 30,%0,16904
Mvaa 252,36,76
L_ v6,28,s65,256
Mvaa 244,224,112
S_ s65,s59,6488064
Mvaa 244,36,72
S_ m0,s52,9984
Ta 32,%0,20488
Ta 34,%0,21000
Mvaa 236,224,108
Mvaa 236,36,68
L_ v6,28,s65,256
Mvaa 228,224,104
S_ s65,s59,6586368
Mvaa 228,36,64
S_ m0,s52,11008
Mvaa 220,224,100
Mvaa 220,36,60
W_ 4
Mvaa 16,20,140
s_nop 0
Ta 36,v1,16384
Ta 38,v1,16896
Mvaa 16,24,180
L_ v6,28,s65,256
Mvaa 24,20,144
S_ s65,s60,1152
Mvaa 24,24,184
S_ m0,s50,20480
Ta 224,v1,20480
Ta 226,v1,20992
Mvaa 32,20,148
.Lpad_q1_3:
s_cbranch_execz .Lhop_q1_r
Mvaa 32,24,188
L_ v4,24,s65,0
Mvaa 40,20,152
S_ s65,s60,11392
Mvaa 40,24,192
S_ m0,s50,21504
Mvaa 48,20,156
Mvaa 48,24,196
W_ 4
Mvaa 52,32,196
s_nop 0
Ta 20,v1,16392
Ta 22,v1,16904
Mvaa 52,28,156
L_ v5,24,s65,0
Mvaa 44,32,192
S_ s65,s60,21632
Mvaa 44,28,152
S_ m0,s50,22528
Ta 24,v1,20488
Ta 26,v1,21000
Mvaa 36,32,188
Mvaa 36,28,148
L_ v4,24,s65,0
Mvaa 28,32,184
S_ s65,s60,31872
Mvaa 28,28,144
S_ m0,s50,23552
Mvaa 20,32,180
Mvaa 20,28,140
s_waitcnt vmcnt(11) lgkmcnt(0)
s_barrier
Mvaa 56,36,140
s_nop 0
Ta 28,%0,24576
Ta 30,%0,25088
Mvaa 56,224,180
L_ v5,24,s65,0
Mvaa 64,36,144
S_ s65,s60,42112
Mvaa 64,224,184
S_ m0,s50,24576
Ta 32,%0,28672
Ta 34,%0,29184
Mvaa 72,36,148
Mvaa 72,224,188
L_ v4,24,s65,0
Mvaa 80,36,152
S_ s65,s59,7077888
Mvaa 80,224,192
S_ m0,s52,16128
Mvaa 88,36,156
Mvaa 88,224,196
Mvaa 92,24,196
s_nop 0
Ta 36,%0,24584
Ta 38,%0,25096
Mvaa 92,20,156
L_ v6,28,s65,256
Mvaa 84,24,192
S_ s65,s59,7176192
Mvaa 84,20,152
S_ m0,s52,17152
Ta 224,%0,28680
Ta 226,%0,29192
Mvaa 76,24,188
Mvaa 76,20,148
L_ v6,28,s65,256
Mvaa 68,24,184
S_ s65,s59,7274496
Mvaa 68,20,144
S_ m0,s52,18176
Mvaa 60,24,180
Mvaa 60,20,140
W_ 4
Mvaa 96,28,140
s_nop 0
Ta 20,v1,24576
Ta 22,v1,25088
Mvaa 96,32,180
L_ v6,28,s65,256
Mvaa 104,28,144
S_ s65,s59,7372800
Mvaa 104,32,184
S_ m0,s52,19200
Ta 24,v1,28672
Ta 26,v1,29184
Mvaa 112,28,148
Mvaa 112,32,188
L_ v6,28,s65,256
Mvaa 120,28,152
Mvaa 120,32,192
Mvaa 128,28,156
Mvaa 128,32,196
W_ 4
Mvaa 132,224,196
s_nop 0
Ta 28,v1,24584
Ta 30,v1,25096
Mvaa 132,36,156
Mvaa 124,224,192
Mvaa 124,36,152
s_nop 0
Ta 32,v1,28680
Ta 34,v1,29192
Mvaa 116,224,188
Mvaa 116,36,148
Mvaa 108,224,184
Mvaa 108,36,144
Mvaa 100,224,180
Mvaa 100,36,140
s_waitcnt vmcnt(13) lgkmcnt(0)
s_barrier
Mvaa 136,20,140
S_ s65,s59,6291456
Ta 36,%0,0
Ta 38,%0,512
Mvaa 136,24,180
S_ m0,s52,24448
Da 60,v2,0
Mvaa 144,20,144
Mvaa 144,24,184
s_nop 0
Ta 224,%0,4096
Ta 226,%0,4608
Mvaa 152,20,148
Da 100,v3,0
Mvaa 152,24,188
L_ v6,28,s65,128
Mvaa 160,20,152
S_ s65,s59,6389760
Da 64,v2,2048
Mvaa 160,24,192
S_ m0,s52,25472
Mvaa 168,20,156
Mvaa 168,24,196
Da 104,v3,2048
Mvaa 172,32,196
s_nop 0
Ta 20,%0,8
Ta 22,%0,520
Mvaa 172,28,156
L_ v6,28,s65,128
Mvaa 164,32,192
S_ s65,s59,6488064
Da 68,v2,4096
Mvaa 164,28,152
S_ m0,s52,26496
Ta 24,%0,4104
Ta 26,%0,4616
Mvaa 156,32,188
Mvaa 156,28,148
Da 108,v3,4096
Mvaa 148,32,184
Mvaa 148,28,144
L_ v6,28,s65,128
Mvaa 140,32,180
S_ s65,s59,6586368
Da 72,v2,6144
Mvaa 140,28,140
W_ 10
S_ m0,s52,27520
Mvaa 176,36,140
s_nop 0
Ta 28,v1,0
Ta 30,v1,512
Mvaa 176,224,180
Da 112,v3,6144
Mvaa 184,36,144
Mvaa 184,224,184
s_nop 0
Ta 32,v1,4096
Ta 34,v1,4608
Mvaa 192,36,148
Da 76,v2,8192
Mvaa 192,224,188
L_ v6,28,s65,128
Mvaa 200,36,152
Mvaa 200,224,192
Da 116,v3,8192
Mvaa 208,36,156
Mvaa 208,224,196
W_ 9
Mvaa 212,24,196
s_nop 0
Ta 36,v1,8
Ta 38,v1,520
Mvaa 212,20,156
Mvaa 204,24,192
Mvaa 204,20,152
s_nop 0
Ta 224,v1,4104
Ta 226,v1,4616
Mvaa 196,24,188
Mvaa 196,20,148
Mvaa 188,24,184
Mvaa 188,20,144
Mvaa 180,24,180
Mvaa 180,20,140
s_waitcnt vmcnt(8) lgkmcnt(0)
s_barrier
Mvaa 216,28,140
S_ s65,s59,7077888
Ta 20,v1,8200
Ta 22,v1,8712
Mvaa 216,32,180
S_ m0,s52,-128
Mvaa 224,28,144
Mvaa 224,32,184
s_nop 0
Ta 24,v1,12296
Ta 26,v1,12808
Mvaa 232,28,148
Mvaa 232,32,188
L_ v6,28,s65,128
Mvaa 240,28,152
S_ s65,s59,7176192
Mvaa 240,32,192
S_ m0,s52,896
Mvaa 248,28,156
Mvaa 248,32,196
Mvaa 252,224,196
s_nop 0
Ta 28,v1,8192
Ta 30,v1,8704
Mvaa 252,36,156
L_ v6,28,s65,128
Mvaa 244,224,192
S_ s65,s59,7274496
Mvaa 244,36,152
S_ m0,s52,1920
Ta 32,v1,12288
Ta 34,v1,12800
Mvaa 236,224,188
Mvaa 236,36,148
L_ v6,28,s65,128
Mvaa 228,224,184
S_ s65,s59,7372800
Mvaa 228,36,144
S_ m0,s52,2944
Mvaa 220,224,180
Mvaa 220,36,140
W_ 4
Mvaa 220,20,60
s_nop 0
Ta 36,%0,8200
Ta 38,%0,8712
Mvaa 220,24,100
Da 140,v2,20480
Mvaa 228,20,64
Mvaa 228,24,104
s_nop 0
Ta 224,%0,12296
Ta 226,%0,12808
Mvaa 236,20,68
Da 180,v3,20480
Mvaa 236,24,108
L_ v6,28,s65,128
Mvaa 244,20,72
S_ s65,s60,0
Da 144,v2,22528
Mvaa 244,24,112
S_ m0,s50,0
Mvaa 252,20,76
Mvaa 252,24,116
W_ 7
Da 184,v3,22528
Mvaa 248,32,116
s_nop 0
Ta 20,%0,8192
Ta 22,%0,8704
Mvaa 248,28,76
L_ v4,24,s65,0
Mvaa 240,32,112
S_ s65,s60,10240
Da 148,v2,24576
Mvaa 240,28,72
S_ m0,s50,1024
Ta 24,%0,12288
Ta 26,%0,12800
Mvaa 232,32,108
Mvaa 232,28,68
Da 188,v3,24576
Mvaa 224,32,104
Mvaa 224,28,64
L_ v5,24,s65,0
Mvaa 216,32,100
S_ s65,s60,20480
Da 152,v2,26624
Mvaa 216,28,60
s_mov_b64 exec, 0
s_bitcmp1_b32 s4, 1
s_cbranch_scc1 .Lhop_q2_b
s_bitcmp1_b32 s4, 0
s_cbranch_scc1 .Lpad_q2_1
s_branch .Lpad_q2_0
.Lhop_q2_b:
s_bitcmp1_b32 s4, 0
s_cbranch_scc1 .Lpad_q2_3
s_branch .Lpad_q2_2
.Lhop_q2_r:
s_mov_b64 exec, -1
s_nop 1
.Lqy_late:
s_waitcnt vmcnt(10) lgkmcnt(0)
s_barrier
S_ m0,s50,2048
Mvaa 180,36,60
s_nop 0
Ta 28,v1,16392
Ta 30,v1,16904
Mvaa 180,224,100
Da 192,v3,26624
Mvaa 188,36,64
Mvaa 188,224,104
s_nop 0
Ta 32,v1,20488
Ta 34,v1,21000
Mvaa 196,36,68
Da 156,v2,28672
Mvaa 196,224,108
L_ v4,24,s65,0
Mvaa 204,36,72
S_ s65,s60,30720
Mvaa 204,224,112
S_ m0,s50,3072
Da 196,v3,28672
Mvaa 212,36,76
Mvaa 212,224,116
Mvaa 208,24,116
s_nop 0
Ta 36,v1,16384
Ta 38,v1,16896
Mvaa 208,20,76
L_ v5,24,s65,0
Mvaa 200,24,112
S_ s65,s60,40960
Mvaa 200,20,72
S_ m0,s50,4096
Ta 224,v1,20480
Ta 226,v1,20992
Mvaa 192,24,108
Mvaa 192,20,68
L_ v4,24,s65,0
Mvaa 184,24,104
S_ s65,s59,6291456
Mvaa 184,20,64
S_ m0,s52,8192
Mvaa 176,24,100
Mvaa 176,20,60
W_ 6
Mvaa 220,28,140
s_nop 0
Ta 20,%0,16392
Ta 22,%0,16904
Mvaa 220,32,180
L_ v6,28,s65,0
Mvaa 228,28,144
S_ s65,s59,6389760
Mvaa 228,32,184
S_ m0,s52,9216
Ta 24,%0,20488
Ta 26,%0,21000
Mvaa 236,28,148
Mvaa 236,32,188
L_ v6,28,s65,0
Mvaa 244,28,152
S_ s65,s59,6488064
Mvaa 244,32,192
S_ m0,s52,10240
W_ 9
Mvaa 252,28,156
W_ 8
Mvaa 252,32,196
W_ 4
Mvaa 248,224,196
s_nop 0
Ta 28,%0,16384
Ta 30,%0,16896
Mvaa 248,36,156
L_ v6,28,s65,0
Mvaa 240,224,192
S_ s65,s59,6586368
Mvaa 240,36,152
S_ m0,s52,11264
Ta 32,%0,20480
Ta 34,%0,20992
Mvaa 232,224,188
Mvaa 232,36,148
L_ v6,28,s65,0
Mvaa 224,224,184
Mvaa 224,36,144
Mvaa 216,224,180
Mvaa 216,36,140
s_waitcnt vmcnt(13) lgkmcnt(0)
s_barrier
Mvaa 180,20,140
S_ s65,s59,7077888
Ta 36,v1,24584
Ta 38,v1,25096
Mvaa 180,24,180
S_ m0,s52,16384
Mvaa 188,20,144
Mvaa 188,24,184
s_nop 0
Ta 224,v1,28680
Ta 226,v1,29192
Mvaa 196,20,148
U_ v248,s48,v248
U_ v249,s48,v249
Mvaa 196,24,188
L_ v6,28,s65,0
Mvaa 204,20,152
S_ s65,s59,7176192
U_ v250,s48,v250
U_ v251,s48,v251
Mvaa 204,24,192
S_ m0,s52,17408
U_ v252,s48,v252
U_ v253,s48,v253
Mvaa 212,20,156
U_ v254,s48,v254
U_ v255,s48,v255
Mvaa 212,24,196
C_ 255,254,255
C_ 254,252,253
Mvaa 208,32,196
s_nop 0
Ta 20,v1,24576
Ta 22,v1,25088
Mvaa 208,28,156
L_ v6,28,s65,0
Mvaa 200,32,192
S_ s65,s59,7274496
C_ 253,250,251
C_ 252,248,249
Mvaa 200,28,152
S_ m0,s52,18432
Ta 24,v1,28672
Ta 26,v1,29184
Mvaa 192,32,188
X_ a216,v252
X_ a217,v253
Mvaa 192,28,148
L_ v6,28,s65,0
Mvaa 184,32,184
S_ s65,s59,7372800
X_ a218,v254
X_ a219,v255
Mvaa 184,28,144
S_ m0,s52,19456
U_ v240,s48,v240
U_ v241,s48,v241
Mvaa 176,32,180
U_ v242,s48,v242
U_ v243,s48,v243
Mvaa 176,28,140
W_ 4
U_ v244,s48,v244
U_ v245,s48,v245
Mvaa 140,36,60
s_nop 0
Ta 28,%0,24584
Ta 30,%0,25096
Mvaa 140,224,100
L_ v6,28,s65,0
Mvaa 148,36,64
U_ v246,s48,v246
U_ v247,s48,v247
Mvaa 148,224,104
s_nop 0
Ta 32,%0,28680
Ta 34,%0,29192
Mvaa 156,36,68
C_ 251,246,247
C_ 250,244,245
Mvaa 156,224,108
C_ 249,242,243
C_ 248,240,241
Mvaa 164,36,72
X_ a212,v248
X_ a213,v249
Mvaa 164,224,112
X_ a214,v250
X_ a215,v251
Mvaa 172,36,76
U_ v232,s48,v232
U_ v233,s48,v233
Mvaa 172,224,116
W_ 4
U_ v234,s48,v234
U_ v235,s48,v235
Mvaa 168,24,116
s_nop 0
Ta 36,%0,24576
Ta 38,%0,25088
Mvaa 168,20,76
U_ v236,s48,v236
U_ v237,s48,v237
Mvaa 160,24,112
U_ v238,s48,v238
U_ v239,s48,v239
Mvaa 160,20,72
s_nop 0
Ta 224,%0,28672
Ta 226,%0,29184
Mvaa 152,24,108
C_ 247,238,239
C_ 246,236,237
Mvaa 152,20,68
C_ 245,234,235
C_ 244,232,233
Mvaa 144,24,104
X_ a208,v244
X_ a209,v245
Mvaa 144,20,64
X_ a210,v246
X_ a211,v247
Mvaa 136,24,100
U_ v224,s48,v224
U_ v225,s48,v225
Mvaa 136,20,60
s_waitcnt vmcnt(13) lgkmcnt(0)
s_barrier
U_ v226,s48,v226
Mvaa 100,28,60
S_ s65,s59,-768
Ta 20,v1,8
Ta 22,v1,520
Mvaa 100,32,100
S_ m0,s52,24320
U_ v227,s48,v227
Mvaa 108,28,64
U_ v228,s48,v228
U_ v229,s48,v229
Mvaa 108,32,104
s_nop 0
Ta 24,v1,4104
Ta 26,v1,4616
Mvaa 116,28,68
U_ v230,s48,v230
U_ v231,s48,v231
Mvaa 116,32,108
L_ v6,28,s65,256
Mvaa 124,28,72
S_ s65,s59,97536
C_ 243,230,231
C_ 242,228,229
Mvaa 124,32,112
S_ m0,s52,25344
C_ 241,226,227
C_ 240,224,225
Mvaa 132,28,76
X_ a204,v240
X_ a205,v241
Mvaa 132,32,116
X_ a206,v242
X_ a207,v243
Mvaa 128,224,116
s_nop 0
Ta 28,v1,0
Ta 30,v1,512
Mvaa 128,36,76
L_ v6,28,s65,256
Mvaa 120,224,112
S_ s65,s59,195840
U_ v216,s48,v216
U_ v217,s48,v217
Mvaa 120,36,72
S_ m0,s52,26368
Ta 32,v1,4096
Ta 34,v1,4608
.Lpad_q2_0:
s_cbranch_execz .Lpad_q2_4
Mvaa 112,224,108
U_ v218,s48,v218
U_ v219,s48,v219
Mvaa 112,36,68
L_ v6,28,s65,256
Mvaa 104,224,104
S_ s65,s59,294144
U_ v220,s48,v220
U_ v221,s48,v221
Mvaa 104,36,64
S_ m0,s52,27392
U_ v222,s48,v222
U_ v223,s48,v223
Mvaa 96,224,100
C_ 239,222,223
C_ 238,220,221
Mvaa 96,36,60
W_ 4
C_ 237,218,219
C_ 236,216,217
Mvaa 140,20,140
s_nop 0
Ta 36,%0,8
Ta 38,%0,520
Mvaa 140,24,180
L_ v6,28,s65,256
Mvaa 148,20,144
S_ s65,s60,128
X_ a200,v236
X_ a201,v237
Mvaa 148,24,184
S_ m0,s50,20480
Ta 224,%0,4104
Ta 226,%0,4616
Mvaa 156,20,148
X_ a202,v238
X_ a203,v239
Mvaa 156,24,188
L_ v4,24,s65,0
Mvaa 164,20,152
S_ s65,s60,10368
U_ v208,s48,v208
U_ v209,s48,v209
Mvaa 164,24,192
S_ m0,s50,21504
U_ v210,s48,v210
U_ v211,s48,v211
Mvaa 172,20,156
U_ v212,s48,v212
U_ v213,s48,v213
Mvaa 172,24,196
W_ 4
U_ v214,s48,v214
U_ v215,s48,v215
Mvaa 168,32,196
s_nop 0
Ta 20,%0,0
Ta 22,%0,512
Mvaa 168,28,156
L_ v5,24,s65,0
Mvaa 160,32,192
S_ s65,s60,20608
C_ 235,214,215
C_ 234,212,213
Mvaa 160,28,152
S_ m0,s50,22528
Ta 24,%0,4096
Ta 26,%0,4608
Mvaa 152,32,188
C_ 233,210,211
C_ 232,208,209
Mvaa 152,28,148
L_ v4,24,s65,0
Mvaa 144,32,184
S_ s65,s60,30848
X_ a176,v232
X_ a177,v233
Mvaa 144,28,144
S_ m0,s50,23552
X_ a178,v234
X_ a179,v235
Mvaa 136,32,180
U_ v200,s48,v200
U_ v201,s48,v201
Mvaa 136,28,140
s_waitcnt vmcnt(11) lgkmcnt(0)
s_barrier
U_ v202,s48,v202
Mvaa 100,36,140
s_nop 0
Ta 28,v1,8200
Ta 30,v1,8712
Mvaa 100,224,180
L_ v5,24,s65,0
Mvaa 108,36,144
S_ s65,s60,41088
U_ v203,s48,v203
Mvaa 108,224,184
S_ m0,s50,24576
Ta 32,v1,12296
Ta 34,v1,12808
Mvaa 116,36,148
U_ v204,s48,v204
U_ v205,s48,v205
Mvaa 116,224,188
L_ v4,24,s65,0
Mvaa 124,36,152
S_ s65,s59,785664
U_ v206,s48,v206
U_ v207,s48,v207
Mvaa 124,224,192
S_ m0,s52,0
C_ 231,206,207
C_ 230,204,205
Mvaa 132,36,156
C_ 229,202,203
C_ 228,200,201
Mvaa 132,224,196
X_ a172,v228
X_ a173,v229
Mvaa 128,24,196
s_nop 0
Ta 36,v1,8192
Ta 38,v1,8704
Mvaa 128,20,156
L_ v6,28,s65,0
Mvaa 120,24,192
S_ s65,s59,883968
X_ a174,v230
X_ a175,v231
Mvaa 120,20,152
S_ m0,s52,1024
Ta 224,v1,12288
Ta 226,v1,12800
Mvaa 112,24,188
U_ v192,s48,v192
U_ v193,s48,v193
Mvaa 112,20,148
L_ v6,28,s65,0
Mvaa 104,24,184
S_ s65,s59,982272
U_ v194,s48,v194
U_ v195,s48,v195
Mvaa 104,20,144
S_ m0,s52,2048
U_ v196,s48,v196
U_ v197,s48,v197
Mvaa 96,24,180
U_ v198,s48,v198
U_ v199,s48,v199
Mvaa 96,20,140
W_ 4
C_ 227,198,199
C_ 226,196,197
Mvaa 60,28,60
s_nop 0
Ta 20,%0,8200
Ta 22,%0,8712
Mvaa 60,32,100
L_ v6,28,s65,0
Mvaa 68,28,64
S_ s65,s59,1080576
C_ 225,194,195
C_ 224,192,193
Mvaa 68,32,104
S_ m0,s52,3072
Ta 24,%0,12296
Ta 26,%0,12808
Mvaa 76,28,68
X_ a168,v224
X_ a169,v225
Mvaa 76,32,108
L_ v6,28,s65,0
Mvaa 84,28,72
X_ a170,v226
X_ a171,v227
Mvaa 84,32,112
U_ v184,s48,v184
U_ v185,s48,v185
Mvaa 92,28,76
U_ v186,s48,v186
U_ v187,s48,v187
Mvaa 92,32,116
W_ 4
U_ v188,s48,v188
U_ v189,s48,v189
Mvaa 88,224,116
s_nop 0
Ta 28,%0,8192
Ta 30,%0,8704
Mvaa 88,36,76
U_ v190,s48,v190
U_ v191,s48,v191
Mvaa 80,224,112
C_ 223,190,191
C_ 222,188,189
Mvaa 80,36,72
s_nop 0
Ta 32,%0,12288
Ta 34,%0,12800
Mvaa 72,224,108
C_ 221,186,187
C_ 220,184,185
Mvaa 72,36,68
X_ a164,v220
X_ a165,v221
Mvaa 64,224,104
X_ a166,v222
X_ a167,v223
Mvaa 64,36,64
U_ v176,s48,v176
U_ v177,s48,v177
Mvaa 56,224,100
U_ v178,s48,v178
U_ v179,s48,v179
Mvaa 56,36,60
s_waitcnt vmcnt(13) lgkmcnt(0)
s_barrier
U_ v180,s48,v180
Mvaa 20,20,60
S_ s65,s59,-768
Ta 36,v1,16392
Ta 38,v1,16904
Mvaa 20,24,100
S_ m0,s52,8192
U_ v181,s48,v181
Mvaa 28,20,64
U_ v182,s48,v182
U_ v183,s48,v183
Mvaa 28,24,104
s_nop 0
Ta 224,v1,20488
Ta 226,v1,21000
Mvaa 36,20,68
C_ 219,182,183
C_ 218,180,181
Mvaa 36,24,108
L_ v6,28,s65,0
Mvaa 44,20,72
S_ s65,s59,97536
C_ 217,178,179
C_ 216,176,177
Mvaa 44,24,112
S_ m0,s52,9216
X_ a160,v216
X_ a161,v217
Mvaa 52,20,76
X_ a162,v218
X_ a163,v219
Mvaa 52,24,116
U_ v168,s48,v168
U_ v169,s48,v169
Mvaa 48,32,116
s_nop 0
Ta 20,v1,16384
Ta 22,v1,16896
Mvaa 48,28,76
L_ v6,28,s65,0
Mvaa 40,32,112
S_ s65,s59,195840
U_ v170,s48,v170
U_ v171,s48,v171
Mvaa 40,28,72
S_ m0,s52,10240
Ta 24,v1,20480
Ta 26,v1,20992
Mvaa 32,32,108
U_ v172,s48,v172
U_ v173,s48,v173
Mvaa 32,28,68
L_ v6,28,s65,0
Mvaa 24,32,104
S_ s65,s59,294144
U_ v174,s48,v174
U_ v175,s48,v175
Mvaa 24,28,64
S_ m0,s52,11264
C_ 215,174,175
C_ 214,172,173
Mvaa 16,32,100
C_ 213,170,171
C_ 212,168,169
Mvaa 16,28,60
W_ 4
X_ a136,v212
X_ a137,v213
Mvaa 60,36,140
s_nop 0
Ta 28,%0,16392
Ta 30,%0,16904
Mvaa 60,224,180
L_ v6,28,s65,0
Mvaa 68,36,144
X_ a138,v214
X_ a139,v215
Mvaa 68,224,184
s_nop 0
Ta 32,%0,20488
Ta 34,%0,21000
Mvaa 76,36,148
U_ v160,s48,v160
U_ v161,s48,v161
Mvaa 76,224,188
U_ v162,s48,v162
U_ v163,s48,v163
Mvaa 84,36,152
U_ v164,s48,v164
U_ v165,s48,v165
Mvaa 84,224,192
U_ v166,s48,v166
U_ v167,s48,v167
Mvaa 92,36,156
C_ 211,166,167
C_ 210,164,165
Mvaa 92,224,196
W_ 4
C_ 209,162,163
C_ 208,160,161
Mvaa 88,24,196
s_nop 0
Ta 36,%0,16384
Ta 38,%0,16896
Mvaa 88,20,156
X_ a132,v208
X_ a133,v209
Mvaa 80,24,192
X_ a134,v210
X_ a135,v211
Mvaa 80,20,152
s_nop 0
Ta 224,%0,20480
Ta 226,%0,20992
Mvaa 72,24,188
U_ v152,s48,v152
U_ v153,s48,v153
Mvaa 72,20,148
U_ v154,s48,v154
U_ v155,s48,v155
Mvaa 64,24,184
U_ v156,s48,v156
U_ v157,s48,v157
Mvaa 64,20,144
U_ v158,s48,v158
U_ v159,s48,v159
Mvaa 56,24,180
C_ 207,158,159
C_ 206,156,157
Mvaa 56,20,140
s_waitcnt vmcnt(63) lgkmcnt(0)
s_barrier
C_ 205,154,155
Mvaa 20,28,140
S_ s65,s59,-768
C_ 204,152,153
Mvaa 20,32,180
S_ m0,s52,16256
X_ a128,v204
X_ a129,v205
Mvaa 28,28,144
X_ a130,v206
X_ a131,v207
Mvaa 28,32,184
L_ v6,28,s65,128
Mvaa 36,28,148
S_ s65,s59,97536
U_ v144,s48,v144
U_ v145,s48,v145
Mvaa 36,32,188
S_ m0,s52,17280
U_ v146,s48,v146
U_ v147,s48,v147
Mvaa 44,28,152
U_ v148,s48,v148
U_ v149,s48,v149
Mvaa 44,32,192
L_ v6,28,s65,128
Mvaa 52,28,156
S_ s65,s59,195840
U_ v150,s48,v150
U_ v151,s48,v151
Mvaa 52,32,196
C_ 203,150,151
S_ m0,s52,18304
Mvaa 48,224,196
C_ 202,148,149
Mvaa 48,36,156
L_ v6,28,s65,128
Mvaa 40,224,192
S_ s65,s59,294144
C_ 201,146,147
C_ 200,144,145
Mvaa 40,36,152
S_ m0,s52,19328
X_ a124,v200
X_ a125,v201
Mvaa 32,224,188
X_ a126,v202
X_ a127,v203
Mvaa 32,36,148
L_ v6,28,s65,128
Mvaa 24,224,184
U_ v136,s48,v136
U_ v137,s48,v137
Mvaa 24,36,144
U_ v138,s48,v138
U_ v139,s48,v139
Mvaa 16,224,180
U_ v140,s48,v140
U_ v141,s48,v141
Mvaa 16,36,140
U_ v142,s48,v142
U_ v143,s48,v143
C_ 199,142,143
C_ 198,140,141
C_ 197,138,139
C_ 196,136,137
X_ a120,v196
X_ a121,v197
X_ a122,v198
X_ a123,v199
U_ v128,s48,v128
U_ v129,s48,v129
U_ v130,s48,v130
U_ v131,s48,v131
U_ v132,s48,v132
U_ v133,s48,v133
U_ v134,s48,v134
U_ v135,s48,v135
C_ 195,134,135
C_ 194,132,133
C_ 193,130,131
C_ 192,128,129
X_ a96,v192
X_ a97,v193
X_ a98,v194
X_ a99,v195
U_ v120,s48,v120
U_ v121,s48,v121
U_ v122,s48,v122
U_ v123,s48,v123
U_ v124,s48,v124
U_ v125,s48,v125
U_ v126,s48,v126
U_ v127,s48,v127
C_ 191,126,127
C_ 190,124,125
C_ 189,122,123
C_ 188,120,121
X_ a92,v188
X_ a93,v189
X_ a94,v190
X_ a95,v191
U_ v112,s48,v112
U_ v113,s48,v113
U_ v114,s48,v114
U_ v115,s48,v115
U_ v116,s48,v116
U_ v117,s48,v117
U_ v118,s48,v118
U_ v119,s48,v119
C_ 187,118,119
C_ 186,116,117
C_ 185,114,115
C_ 184,112,113
X_ a88,v184
X_ a89,v185
X_ a90,v186
X_ a91,v187
U_ v104,s48,v104
U_ v105,s48,v105
U_ v106,s48,v106
U_ v107,s48,v107
U_ v108,s48,v108
U_ v109,s48,v109
U_ v110,s48,v110
U_ v111,s48,v111
C_ 183,110,111
C_ 182,108,109
C_ 181,106,107
C_ 180,104,105
X_ a84,v180
X_ a85,v181
X_ a86,v182
X_ a87,v183
U_ v96,s48,v96
U_ v97,s48,v97
U_ v98,s48,v98
U_ v99,s48,v99
U_ v100,s48,v100
U_ v101,s48,v101
U_ v102,s48,v102
U_ v103,s48,v103
C_ 179,102,103
C_ 178,100,101
C_ 177,98,99
C_ 176,96,97
X_ a80,v176
X_ a81,v177
X_ a82,v178
X_ a83,v179
v_pk_mul_f32 v[88:89], v[88:89], s[48:49]
v_pk_mul_f32 v[90:91], v[90:91], s[48:49]
v_pk_mul_f32 v[92:93], v[92:93], s[48:49]
v_pk_mul_f32 v[94:95], v[94:95], s[48:49]
C_ 175,94,95
C_ 174,92,93
C_ 173,90,91
C_ 172,88,89
X_ a56,v172
X_ a57,v173
X_ a58,v174
X_ a59,v175
v_pk_mul_f32 v[80:81], v[80:81], s[48:49]
v_pk_mul_f32 v[82:83], v[82:83], s[48:49]
v_pk_mul_f32 v[84:85], v[84:85], s[48:49]
v_pk_mul_f32 v[86:87], v[86:87], s[48:49]
C_ 171,86,87
C_ 170,84,85
C_ 169,82,83
C_ 168,80,81
X_ a52,v168
X_ a53,v169
X_ a54,v170
X_ a55,v171
v_pk_mul_f32 v[72:73], v[72:73], s[48:49]
v_pk_mul_f32 v[74:75], v[74:75], s[48:49]
v_pk_mul_f32 v[76:77], v[76:77], s[48:49]
v_pk_mul_f32 v[78:79], v[78:79], s[48:49]
C_ 167,78,79
C_ 166,76,77
C_ 165,74,75
C_ 164,72,73
X_ a48,v164
X_ a49,v165
X_ a50,v166
X_ a51,v167
v_pk_mul_f32 v[64:65], v[64:65], s[48:49]
v_pk_mul_f32 v[66:67], v[66:67], s[48:49]
v_pk_mul_f32 v[68:69], v[68:69], s[48:49]
v_pk_mul_f32 v[70:71], v[70:71], s[48:49]
C_ 163,70,71
C_ 162,68,69
C_ 161,66,67
C_ 160,64,65
X_ a44,v160
X_ a45,v161
X_ a46,v162
X_ a47,v163
v_pk_mul_f32 v[56:57], v[56:57], s[48:49]
v_pk_mul_f32 v[58:59], v[58:59], s[48:49]
v_pk_mul_f32 v[60:61], v[60:61], s[48:49]
v_pk_mul_f32 v[62:63], v[62:63], s[48:49]
C_ 159,62,63
C_ 158,60,61
C_ 157,58,59
C_ 156,56,57
X_ a40,v156
X_ a41,v157
X_ a42,v158
X_ a43,v159
v_pk_mul_f32 v[48:49], v[48:49], s[48:49]
v_pk_mul_f32 v[50:51], v[50:51], s[48:49]
v_pk_mul_f32 v[52:53], v[52:53], s[48:49]
v_pk_mul_f32 v[54:55], v[54:55], s[48:49]
C_ 155,54,55
C_ 154,52,53
C_ 153,50,51
C_ 152,48,49
X_ a16,v152
X_ a17,v153
X_ a18,v154
X_ a19,v155
v_pk_mul_f32 v[40:41], v[40:41], s[48:49]
v_pk_mul_f32 v[42:43], v[42:43], s[48:49]
v_pk_mul_f32 v[44:45], v[44:45], s[48:49]
v_pk_mul_f32 v[46:47], v[46:47], s[48:49]
C_ 151,46,47
C_ 150,44,45
C_ 149,42,43
C_ 148,40,41
X_ a12,v148
X_ a13,v149
X_ a14,v150
X_ a15,v151
s_sub_u32 s59, s59, 768
s_waitcnt vmcnt(0) lgkmcnt(0)
s_barrier
Ta 20,%0,8192
.Lpad_q2_1:
s_cbranch_execz .Lpad_q2_5
Ta 22,%0,8704
Ta 24,%0,12288
Ta 26,%0,12800
Da 60,v2,0
Da 100,v3,0
Da 64,v2,2048
Da 104,v3,2048
Da 68,v2,4096
Da 108,v3,4096
Da 72,v2,6144
Da 112,v3,6144
Da 76,v2,8192
Da 116,v3,8192
Ta 28,%0,8200
Ta 30,%0,8712
Ta 32,%0,12296
Ta 34,%0,12808
v_pk_mul_f32 v[32:33], v[32:33], s[48:49]
v_pk_mul_f32 v[34:35], v[34:35], s[48:49]
v_pk_mul_f32 v[36:37], v[36:37], s[48:49]
v_pk_mul_f32 v[38:39], v[38:39], s[48:49]
C_ 147,38,39
C_ 146,36,37
C_ 145,34,35
C_ 144,32,33
X_ a8,v144
X_ a9,v145
X_ a10,v146
X_ a11,v147
v_pk_mul_f32 v[24:25], v[24:25], s[48:49]
v_pk_mul_f32 v[26:27], v[26:27], s[48:49]
v_pk_mul_f32 v[28:29], v[28:29], s[48:49]
v_pk_mul_f32 v[30:31], v[30:31], s[48:49]
C_ 143,30,31
C_ 142,28,29
C_ 141,26,27
C_ 140,24,25
X_ a4,v140
X_ a5,v141
X_ a6,v142
X_ a7,v143
v_pk_mul_f32 v[16:17], v[16:17], s[48:49]
v_pk_mul_f32 v[18:19], v[18:19], s[48:49]
v_pk_mul_f32 v[20:21], v[20:21], s[48:49]
v_pk_mul_f32 v[22:23], v[22:23], s[48:49]
C_ 139,22,23
C_ 138,20,21
C_ 137,18,19
C_ 136,16,17
X_ a0,v136
X_ a1,v137
X_ a2,v138
X_ a3,v139
s_sub_u32 s90, s90, s88
s_cmp_gt_u32 s90, 4000
s_cselect_b32 s11, 1, 0
s_sub_u32 s12, s68, s98
S_ s12,s12,1252
s_mov_b32 s13, 110
s_nop 1
.p2align 3
.Lq:
W_ 13
Zvaa 16,20,60
s_nop 0
Ta 36,v1,8192
Ta 38,v1,8704
W_ 14
Mvaa 16,24,100
R_ v12,a248
W_ 13
Zvaa 24,20,64
v_lshrrev_b32 v12, 4, v12
W_ 12
Mvaa 24,24,104
s_nop 0
Ta 224,v1,12288
Ta 226,v1,12800
W_ 13
Zvaa 32,20,68
v_min_u32 v10, 11, v12
W_ 12
Mvaa 32,24,108
s_mul_i32 s56, s4, 12
W_ 11
Zvaa 40,20,72
v_add_u32 v10, s56, v10
W_ 10
Mvaa 40,24,112
v_mul_u32_u24 v11, 43691, v10
W_ 9
Zvaa 48,20,76
v_lshrrev_b32 v11, 18, v11
W_ 8
Mvaa 48,24,116
W_ 4
v_lshlrev_b32 v10, 7, v10
Zvaa 52,32,116
s_nop 0
Ta 20,v1,8200
Ta 22,v1,8712
Mvaa 52,28,76
v_mul_u32_u24 v11, 11520, v11
Zvaa 44,32,112
v_add_u32 v10, v10, v11
Mvaa 44,28,72
s_nop 0
Ta 24,v1,12296
Ta 26,v1,12808
Zvaa 36,32,108
v_min_u32 v12, 9, v12
Mvaa 36,28,68
s_and_b32 s56, s6, 3
Zvaa 28,32,104
s_lshl_b32 s56, s56, 2
Mvaa 28,28,64
S_ s56,s56,s4
Zvaa 20,32,100
s_mul_i32 s56, s56, 10
Mvaa 20,28,60
s_waitcnt vmcnt(18) lgkmcnt(0)
s_barrier
v_add_u32 v12, s56, v12
Zvaa 56,36,60
S_ s65,s59,786432
Ta 28,%0,16384
Ta 30,%0,16896
Mvaa 56,224,100
S_ m0,s52,8064
v_mul_u32_u24 v12, 1280, v12
Zvaa 64,36,64
s_lshr_b32 s56, s7, 5
Mvaa 64,224,104
s_mul_i32 s56, s56, 98304
Ta 32,%0,20480
Ta 34,%0,20992
Zvaa 72,36,68
s_and_b32 s55, s59, 4095
Mvaa 72,224,108
L_ v6,28,s65,128
Zvaa 80,36,72
S_ s65,s59,884736
S_ s56,s56,s55
Mvaa 80,224,112
S_ m0,s52,9088
s_mul_i32 s55, s77, 384
Zvaa 88,36,76
s_sub_u32 s56, s56, s55
Mvaa 88,224,116
s_mov_b32 s63, 0
Zvaa 92,24,116
s_nop 0
Ta 36,%0,16392
Ta 38,%0,16904
Mvaa 92,20,76
L_ v6,28,s65,128
Zvaa 84,24,112
S_ s65,s59,983040
Mvaa 84,20,72
S_ m0,s52,10112
Ta 224,%0,20488
Ta 226,%0,21000
Zvaa 76,24,108
s_bitcmp1_b32 1, s58
s_cselect_b32 s63, 0, 4026531840
Mvaa 76,20,68
L_ v6,28,s65,128
Zvaa 68,24,104
S_ s65,s59,1081344
S_ s56,s56,s63
Mvaa 68,20,64
S_ m0,s52,11136
S_ s55,s56,3145728
Zvaa 60,24,100
Mvaa 60,20,60
W_ 4
Zvaa 96,28,60
s_nop 0
Ta 20,v1,16384
Ta 22,v1,16896
Mvaa 96,32,100
L_ v6,28,s65,128
Zvaa 104,28,64
S_ s65,s60,256
Mvaa 104,32,104
S_ m0,s50,0
Ta 24,v1,20480
Ta 26,v1,20992
Zvaa 112,28,68
Mvaa 112,32,108
L_ v4,24,s65,0
Zvaa 120,28,72
S_ s65,s60,10496
Mvaa 120,32,112
S_ m0,s50,1024
Zvaa 128,28,76
Mvaa 128,32,116
W_ 4
Zvaa 132,224,116
s_nop 0
Ta 28,v1,16392
Ta 30,v1,16904
Mvaa 132,36,76
L_ v5,24,s65,0
Zvaa 124,224,112
S_ s65,s60,20736
Mvaa 124,36,72
S_ m0,s50,2048
Ta 32,v1,20488
Ta 34,v1,21000
Zvaa 116,224,108
Mvaa 116,36,68
L_ v4,24,s65,0
Zvaa 108,224,104
S_ s65,s60,30976
Mvaa 108,36,64
S_ m0,s50,3072
Zvaa 100,224,100
Mvaa 100,36,60
s_cmp_eq_u32 s58, 0
s_cbranch_scc0 .Lq_early
s_mov_b64 exec, 0
s_bitcmp1_b32 s4, 1
s_cbranch_scc1 .Lhop_qx_b
s_bitcmp1_b32 s4, 0
s_cbranch_scc1 .Lpad_qx_1
s_branch .Lpad_qx_0
.Lhop_qx_b:
s_bitcmp1_b32 s4, 0
s_cbranch_scc1 .Lpad_qx_3
s_branch .Lpad_qx_2
.Lhop_qx_r:
s_mov_b64 exec, -1
s_nop 1
.Lq_early:
s_waitcnt vmcnt(16) lgkmcnt(0)
s_barrier
s_cmp_eq_u32 s11, 1
s_cbranch_scc1 .Lpace1
.Lpace1_back:
Zvaa 136,20,60
s_nop 0
Ta 36,%0,24576
Ta 38,%0,25088
Mvaa 136,24,100
Da 140,v2,20480
Zvaa 144,20,64
Mvaa 144,24,104
s_nop 0
Ta 224,%0,28672
Ta 226,%0,29184
Zvaa 152,20,68
Da 180,v3,20480
Mvaa 152,24,108
L_ v5,24,s65,0
Zvaa 160,20,72
S_ s65,s60,41216
Da 144,v2,22528
Mvaa 160,24,112
S_ m0,s50,4096
Zvaa 168,20,76
Mvaa 168,24,116
Da 184,v3,22528
Zvaa 172,32,116
s_nop 0
Ta 20,%0,24584
Ta 22,%0,25096
Mvaa 172,28,76
L_ v4,24,s65,0
Zvaa 164,32,112
S_ s65,s59,786432
Da 148,v2,24576
Mvaa 164,28,72
S_ m0,s52,16128
Ta 24,%0,28680
Ta 26,%0,29192
Zvaa 156,32,108
Mvaa 156,28,68
Da 188,v3,24576
Zvaa 148,32,104
Mvaa 148,28,64
L_ v6,28,s65,256
Zvaa 140,32,100
S_ s65,s59,884736
Da 152,v2,26624
Mvaa 140,28,60
W_ 10
S_ m0,s52,17152
Zvaa 176,36,60
s_nop 0
Ta 28,v1,24576
Ta 30,v1,25088
Mvaa 176,224,100
Da 192,v3,26624
Zvaa 184,36,64
Mvaa 184,224,104
s_nop 0
Ta 32,v1,28672
Ta 34,v1,29184
Zvaa 192,36,68
Da 156,v2,28672
Mvaa 192,224,108
L_ v6,28,s65,256
Zvaa 200,36,72
S_ s65,s59,983040
Mvaa 200,224,112
S_ m0,s52,18176
Da 196,v3,28672
Zvaa 208,36,76
Mvaa 208,224,116
W_ 9
Zvaa 212,24,116
s_nop 0
Ta 36,v1,24584
Ta 38,v1,25096
Mvaa 212,20,76
L_ v6,28,s65,256
Zvaa 204,24,112
S_ s65,s59,1081344
Mvaa 204,20,72
S_ m0,s52,19200
Ta 224,v1,28680
Ta 226,v1,29192
Zvaa 196,24,108
Mvaa 196,20,68
L_ v6,28,s65,256
Zvaa 188,24,104
Mvaa 188,20,64
Zvaa 180,24,100
Mvaa 180,20,60
s_waitcnt vmcnt(18) lgkmcnt(0)
s_barrier
s_cmp_eq_u32 s11, 2
s_cbranch_scc1 .Lqsl2
.Lqsl2_back:
Zvaa 216,28,60
S_ s65,s59,1572864
Ta 20,%0,0
Ta 22,%0,512
Mvaa 216,32,100
buffer_load_dword a254, v10, s[28:31], s55 offen
Zvaa 224,28,64
S_ m0,s52,24576
Mvaa 224,32,104
s_nop 0
Ta 24,%0,4096
Ta 26,%0,4608
Zvaa 232,28,68
Mvaa 232,32,108
L_ v6,28,s65,0
Zvaa 240,28,72
S_ s65,s59,1671168
Mvaa 240,32,112
S_ m0,s52,25600
Zvaa 248,28,76
Mvaa 248,32,116
Zvaa 252,224,116
s_nop 0
Ta 28,%0,8
Ta 30,%0,520
Mvaa 252,36,76
L_ v6,28,s65,0
Zvaa 244,224,112
S_ s65,s59,1769472
Mvaa 244,36,72
S_ m0,s52,26624
Ta 32,%0,4104
Ta 34,%0,4616
Zvaa 236,224,108
Mvaa 236,36,68
L_ v6,28,s65,0
Zvaa 228,224,104
S_ s65,s59,1867776
Mvaa 228,36,64
S_ m0,s52,27648
Zvaa 220,224,100
Mvaa 220,36,60
W_ 4
Mvaa 16,20,140
s_nop 0
Ta 36,v1,0
Ta 38,v1,512
Mvaa 16,24,180
L_ v6,28,s65,0
Mvaa 24,20,144
S_ s65,s60,384
Mvaa 24,24,184
S_ m0,s50,20480
Ta 224,v1,4096
Ta 226,v1,4608
Mvaa 32,20,148
Mvaa 32,24,188
L_ v4,24,s65,0
Mvaa 40,20,152
S_ s65,s60,10624
Mvaa 40,24,192
S_ m0,s50,21504
Mvaa 48,20,156
Mvaa 48,24,196
W_ 4
Mvaa 52,32,196
s_nop 0
Ta 20,v1,8
Ta 22,v1,520
Mvaa 52,28,156
L_ v5,24,s65,0
Mvaa 44,32,192
S_ s65,s60,20864
Mvaa 44,28,152
S_ m0,s50,22528
Ta 24,v1,4104
Ta 26,v1,4616
Mvaa 36,32,188
Mvaa 36,28,148
L_ v4,24,s65,0
Mvaa 28,32,184
S_ s65,s60,31104
Mvaa 28,28,144
S_ m0,s50,23552
Mvaa 20,32,180
Mvaa 20,28,140
s_waitcnt vmcnt(17) lgkmcnt(0)
s_barrier
s_cmp_eq_u32 s11, 1
s_cbranch_scc1 .Lpace3
.Lpace3_back:
Mvaa 56,36,140
s_nop 0
Ta 28,%0,8192
Ta 30,%0,8704
Mvaa 56,224,180
L_ v5,24,s65,0
Mvaa 64,36,144
S_ s65,s60,41344
Mvaa 64,224,184
S_ m0,s50,24576
Ta 32,%0,12288
Ta 34,%0,12800
Mvaa 72,36,148
Mvaa 72,224,188
L_ v4,24,s65,0
Mvaa 80,36,152
S_ s65,s59,1572864
Mvaa 80,224,192
S_ m0,s52,-128
Mvaa 88,36,156
Mvaa 88,224,196
Mvaa 92,24,196
s_nop 0
Ta 36,%0,8200
Ta 38,%0,8712
Mvaa 92,20,156
L_ v6,28,s65,128
Mvaa 84,24,192
S_ s65,s59,1671168
Mvaa 84,20,152
S_ m0,s52,896
Ta 224,%0,12296
Ta 226,%0,12808
Mvaa 76,24,188
Mvaa 76,20,148
L_ v6,28,s65,128
Mvaa 68,24,184
S_ s65,s59,1769472
Mvaa 68,20,144
S_ m0,s52,1920
Mvaa 60,24,180
Mvaa 60,20,140
W_ 4
Mvaa 96,28,140
s_nop 0
Ta 20,v1,8192
Ta 22,v1,8704
Mvaa 96,32,180
L_ v6,28,s65,128
)A"
R"A(Mvaa 104,28,144
S_ s65,s59,1867776
Mvaa 104,32,184
S_ m0,s52,2944
Ta 24,v1,12288
Ta 26,v1,12800
Mvaa 112,28,148
Mvaa 112,32,188
L_ v6,28,s65,128
Mvaa 120,28,152
Mvaa 120,32,192
Mvaa 128,28,156
Mvaa 128,32,196
W_ 4
Mvaa 132,224,196
s_nop 0
Ta 28,v1,8200
Ta 30,v1,8712
Mvaa 132,36,156
buffer_load_dword a254, v12, s[24:27], s63 offen offset:512
Mvaa 124,224,192
S_ s55,s56,3932160
Mvaa 124,36,152
s_nop 0
Ta 32,v1,12296
Ta 34,v1,12808
Mvaa 116,224,188
Mvaa 116,36,148
buffer_load_dword a254, v10, s[28:31], s55 offen
Mvaa 108,224,184
Mvaa 108,36,144
Mvaa 100,224,180
Mvaa 100,36,140
s_waitcnt vmcnt(16) lgkmcnt(0)
s_barrier
s_cmp_eq_u32 s11, 2
s_cbranch_scc1 .Lqsl4
.Lqsl4_back:
Mvaa 136,20,140
S_ s65,s59,1572864
Ta 36,%0,16384
Ta 38,%0,16896
Mvaa 136,24,180
S_ m0,s52,7936
Da 60,v2,0
Mvaa 144,20,144
Mvaa 144,24,184
s_nop 0
Ta 224,%0,20480
Ta 226,%0,20992
Mvaa 152,20,148
Da 100,v3,0
Mvaa 152,24,188
L_ v6,28,s65,256
Mvaa 160,20,152
S_ s65,s59,1671168
Da 64,v2,2048
Mvaa 160,24,192
S_ m0,s52,8960
Mvaa 168,20,156
Mvaa 168,24,196
Da 104,v3,2048
Mvaa 172,32,196
s_nop 0
Ta 20,%0,16392
Ta 22,%0,16904
Mvaa 172,28,156
L_ v6,28,s65,256
Mvaa 164,32,192
S_ s65,s59,1769472
Da 68,v2,4096
Mvaa 164,28,152
S_ m0,s52,9984
Ta 24,%0,20488
Ta 26,%0,21000
Mvaa 156,32,188
Mvaa 156,28,148
Da 108,v3,4096
Mvaa 148,32,184
Mvaa 148,28,144
L_ v6,28,s65,256
Mvaa 140,32,180
S_ s65,s59,1867776
Da 72,v2,6144
Mvaa 140,28,140
W_ 10
S_ m0,s52,11008
Mvaa 176,36,140
.Lpad_q2_2:
s_cbranch_execz .Lpad_q2_6
Ta 28,v1,16384
Ta 30,v1,16896
Mvaa 176,224,180
.Lpad_qx_0:
s_cbranch_execz .Lpad_qx_4
Da 112,v3,6144
Mvaa 184,36,144
Mvaa 184,224,184
s_nop 0
Ta 32,v1,20480
Ta 34,v1,20992
Mvaa 192,36,148
Da 76,v2,8192
Mvaa 192,224,188
L_ v6,28,s65,256
Mvaa 200,36,152
Mvaa 200,224,192
Da 116,v3,8192
Mvaa 208,36,156
Mvaa 208,224,196
W_ 9
Mvaa 212,24,196
s_nop 0
Ta 36,v1,16392
Ta 38,v1,16904
Mvaa 212,20,156
buffer_load_dword a254, v12, s[24:27], s63 offen offset:640
Mvaa 204,24,192
Mvaa 204,20,152
s_nop 0
Ta 224,v1,20488
Ta 226,v1,21000
Mvaa 196,24,188
Mvaa 196,20,148
Mvaa 188,24,184
Mvaa 188,20,144
Mvaa 180,24,180
Mvaa 180,20,140
s_waitcnt vmcnt(16) lgkmcnt(0)
s_barrier
s_cmp_eq_u32 s11, 1
s_cbranch_scc1 .Lpace5
.Lpace5_back:
Mvaa 216,28,140
S_ s65,s59,2359296
Ta 20,%0,24576
Ta 22,%0,25088
Mvaa 216,32,180
S_ m0,s52,16384
Mvaa 224,28,144
Mvaa 224,32,184
s_nop 0
Ta 24,%0,28672
Ta 26,%0,29184
Mvaa 232,28,148
Mvaa 232,32,188
L_ v6,28,s65,0
Mvaa 240,28,152
S_ s65,s59,2457600
Mvaa 240,32,192
S_ m0,s52,17408
Mvaa 248,28,156
Mvaa 248,32,196
Mvaa 252,224,196
s_nop 0
Ta 28,%0,24584
Ta 30,%0,25096
Mvaa 252,36,156
L_ v6,28,s65,0
Mvaa 244,224,192
S_ s65,s59,2555904
Mvaa 244,36,152
S_ m0,s52,18432
Ta 32,%0,28680
Ta 34,%0,29192
Mvaa 236,224,188
Mvaa 236,36,148
L_ v6,28,s65,0
Mvaa 228,224,184
S_ s65,s59,2654208
Mvaa 228,36,144
S_ m0,s52,19456
Mvaa 220,224,180
Mvaa 220,36,140
W_ 4
Mvaa 16,20,60
s_nop 0
Ta 36,v1,24576
Ta 38,v1,25088
Mvaa 16,24,100
L_ v6,28,s65,0
Mvaa 24,20,64
S_ s65,s60,512
Mvaa 24,24,104
S_ m0,s50,0
Ta 224,v1,28672
Ta 226,v1,29184
Mvaa 32,20,68
Mvaa 32,24,108
L_ v4,24,s65,0
Mvaa 40,20,72
S_ s65,s60,10752
Mvaa 40,24,112
S_ m0,s50,1024
Mvaa 48,20,76
Mvaa 48,24,116
W_ 4
Mvaa 52,32,116
s_nop 0
Ta 20,v1,24584
Ta 22,v1,25096
Mvaa 52,28,76
L_ v5,24,s65,0
Mvaa 44,32,112
S_ s65,s60,20992
Mvaa 44,28,72
S_ m0,s50,2048
Ta 24,v1,28680
Ta 26,v1,29192
Mvaa 36,32,108
Mvaa 36,28,68
L_ v4,24,s65,0
Mvaa 28,32,104
S_ s65,s60,31232
Mvaa 28,28,64
S_ m0,s50,3072
Mvaa 20,32,100
Mvaa 20,28,60
s_waitcnt vmcnt(14) lgkmcnt(0)
s_barrier
s_cmp_eq_u32 s11, 2
s_cbranch_scc1 .Lqsl6
.Lqsl6_back:
Mvaa 56,36,60
s_nop 0
Ta 28,%0,0
Ta 30,%0,512
Mvaa 56,224,100
L_ v5,24,s65,0
Mvaa 64,36,64
S_ s65,s60,41472
Mvaa 64,224,104
S_ m0,s50,4096
Ta 32,%0,4096
Ta 34,%0,4608
Mvaa 72,36,68
Mvaa 72,224,108
L_ v4,24,s65,0
Mvaa 80,36,72
S_ s65,s59,2359296
Mvaa 80,224,112
S_ m0,s52,24448
Mvaa 88,36,76
Mvaa 88,224,116
Mvaa 92,24,116
S_ s55,s56,4718592
Ta 36,%0,8
Ta 38,%0,520
Mvaa 92,20,76
L_ v6,28,s65,128
Mvaa 84,24,112
S_ s65,s59,2457600
Mvaa 84,20,72
S_ m0,s52,25472
Ta 224,%0,4104
Ta 226,%0,4616
Mvaa 76,24,108
Mvaa 76,20,68
L_ v6,28,s65,128
Mvaa 68,24,104
S_ s65,s59,2555904
Mvaa 68,20,64
S_ m0,s52,26496
Mvaa 60,24,100
Mvaa 60,20,60
W_ 4
Mvaa 96,28,60
s_nop 0
Ta 20,v1,0
Ta 22,v1,512
Mvaa 96,32,100
L_ v6,28,s65,128
Mvaa 104,28,64
S_ s65,s59,2654208
Mvaa 104,32,104
S_ m0,s52,27520
Ta 24,v1,4096
Ta 26,v1,4608
Mvaa 112,28,68
Mvaa 112,32,108
L_ v6,28,s65,128
Mvaa 120,28,72
Mvaa 120,32,112
Mvaa 128,28,76
Mvaa 128,32,116
W_ 4
Mvaa 132,224,116
s_nop 0
Ta 28,v1,8
Ta 30,v1,520
Mvaa 132,36,76
buffer_load_dword a254, v10, s[28:31], s55 offen
Mvaa 124,224,112
Mvaa 124,36,72
s_nop 0
Ta 32,v1,4104
Ta 34,v1,4616
Mvaa 116,224,108
Mvaa 116,36,68
buffer_load_dword a254, v12, s[24:27], s63 offen offset:768
Mvaa 108,224,104
Mvaa 108,36,64
Mvaa 100,224,100
Mvaa 100,36,60
s_waitcnt vmcnt(16) lgkmcnt(0)
s_barrier
s_cmp_eq_u32 s11, 1
s_cbranch_scc1 .Lpace7
.Lpace7_back:
Mvaa 136,20,60
S_ s65,s59,2359296
Ta 36,%0,8192
Ta 38,%0,8704
Mvaa 136,24,100
S_ m0,s52,-256
Da 140,v2,20480
Mvaa 144,20,64
Mvaa 144,24,104
s_nop 0
Ta 224,%0,12288
Ta 226,%0,12800
Mvaa 152,20,68
Da 180,v3,20480
Mvaa 152,24,108
L_ v6,28,s65,256
Mvaa 160,20,72
S_ s65,s59,2457600
Da 144,v2,22528
Mvaa 160,24,112
S_ m0,s52,768
Mvaa 168,20,76
Mvaa 168,24,116
Da 184,v3,22528
Mvaa 172,32,116
s_nop 0
Ta 20,%0,8200
Ta 22,%0,8712
Mvaa 172,28,76
L_ v6,28,s65,256
Mvaa 164,32,112
S_ s65,s59,2555904
Da 148,v2,24576
Mvaa 164,28,72
S_ m0,s52,1792
Ta 24,%0,12296
Ta 26,%0,12808
Mvaa 156,32,108
Mvaa 156,28,68
Da 188,v3,24576
Mvaa 148,32,104
Mvaa 148,28,64
L_ v6,28,s65,256
Mvaa 140,32,100
S_ s65,s59,2654208
Da 152,v2,26624
Mvaa 140,28,60
W_ 10
S_ m0,s52,2816
Mvaa 176,36,60
s_nop 0
Ta 28,v1,8192
Ta 30,v1,8704
Mvaa 176,224,100
Da 192,v3,26624
Mvaa 184,36,64
Mvaa 184,224,104
s_nop 0
Ta 32,v1,12288
Ta 34,v1,12800
Mvaa 192,36,68
Da 156,v2,28672
Mvaa 192,224,108
L_ v6,28,s65,256
Mvaa 200,36,72
Mvaa 200,224,112
Da 196,v3,28672
Mvaa 208,36,76
Mvaa 208,224,116
W_ 9
Mvaa 212,24,116
s_nop 0
Ta 36,v1,8200
Ta 38,v1,8712
Mvaa 212,20,76
Mvaa 204,24,112
Mvaa 204,20,72
s_nop 0
Ta 224,v1,12296
Ta 226,v1,12808
Mvaa 196,24,108
Mvaa 196,20,68
Mvaa 188,24,104
Mvaa 188,20,64
Mvaa 180,24,100
Mvaa 180,20,60
s_waitcnt vmcnt(15) lgkmcnt(0)
s_barrier
s_cmp_eq_u32 s11, 2
s_cbranch_scc1 .Lqsl8
.Lqsl8_back:
Mvaa 216,28,60
S_ s65,s59,3145728
Ta 20,%0,16384
Ta 22,%0,16896
Mvaa 216,32,100
S_ m0,s52,8192
Mvaa 224,28,64
Mvaa 224,32,104
s_nop 0
Ta 24,%0,20480
Ta 26,%0,20992
Mvaa 232,28,68
Mvaa 232,32,108
L_ v6,28,s65,0
Mvaa 240,28,72
S_ s65,s59,3244032
Mvaa 240,32,112
S_ m0,s52,9216
Mvaa 248,28,76
Mvaa 248,32,116
Mvaa 252,224,116
s_nop 0
Ta 28,%0,16392
Ta 30,%0,16904
Mvaa 252,36,76
L_ v6,28,s65,0
Mvaa 244,224,112
S_ s65,s59,3342336
Mvaa 244,36,72
S_ m0,s52,10240
Ta 32,%0,20488
Ta 34,%0,21000
Mvaa 236,224,108
Mvaa 236,36,68
L_ v6,28,s65,0
Mvaa 228,224,104
S_ s65,s59,3440640
Mvaa 228,36,64
S_ m0,s52,11264
Mvaa 220,224,100
Mvaa 220,36,60
W_ 4
Mvaa 16,20,140
s_nop 0
Ta 36,v1,16384
Ta 38,v1,16896
Mvaa 16,24,180
L_ v6,28,s65,0
Mvaa 24,20,144
S_ s65,s60,640
Mvaa 24,24,184
S_ m0,s50,20480
Ta 224,v1,20480
Ta 226,v1,20992
Mvaa 32,20,148
Mvaa 32,24,188
L_ v4,24,s65,0
Mvaa 40,20,152
S_ s65,s60,10880
Mvaa 40,24,192
S_ m0,s50,21504
Mvaa 48,20,156
Mvaa 48,24,196
W_ 4
Mvaa 52,32,196
s_nop 0
Ta 20,v1,16392
Ta 22,v1,16904
Mvaa 52,28,156
L_ v5,24,s65,0
Mvaa 44,32,192
S_ s65,s60,21120
Mvaa 44,28,152
S_ m0,s50,22528
Ta 24,v1,20488
Ta 26,v1,21000
Mvaa 36,32,188
Mvaa 36,28,148
L_ v4,24,s65,0
Mvaa 28,32,184
S_ s65,s60,31360
Mvaa 28,28,144
S_ m0,s50,23552
Mvaa 20,32,180
Mvaa 20,28,140
s_waitcnt vmcnt(13) lgkmcnt(0)
s_barrier
s_cmp_eq_u32 s11, 1
s_cbranch_scc1 .Lpace9
.Lpace9_back:
Mvaa 56,36,140
s_nop 0
Ta 28,%0,24576
Ta 30,%0,25088
Mvaa 56,224,180
L_ v5,24,s65,0
Mvaa 64,36,144
S_ s65,s60,41600
Mvaa 64,224,184
S_ m0,s50,24576
Ta 32,%0,28672
Ta 34,%0,29184
Mvaa 72,36,148
Mvaa 72,224,188
L_ v4,24,s65,0
Mvaa 80,36,152
S_ s65,s59,3145728
Mvaa 80,224,192
S_ m0,s52,16256
Mvaa 88,36,156
Mvaa 88,224,196
Mvaa 92,24,196
S_ s55,s56,5505024
Ta 36,%0,24584
Ta 38,%0,25096
Mvaa 92,20,156
L_ v6,28,s65,128
Mvaa 84,24,192
S_ s65,s59,3244032
Mvaa 84,20,152
S_ m0,s52,17280
Ta 224,%0,28680
Ta 226,%0,29192
Mvaa 76,24,188
Mvaa 76,20,148
L_ v6,28,s65,128
Mvaa 68,24,184
S_ s65,s59,3342336
Mvaa 68,20,144
S_ m0,s52,18304
Mvaa 60,24,180
Mvaa 60,20,140
W_ 4
Mvaa 96,28,140
s_nop 0
Ta 20,v1,24576
Ta 22,v1,25088
Mvaa 96,32,180
L_ v6,28,s65,128
Mvaa 104,28,144
S_ s65,s59,3440640
Mvaa 104,32,184
S_ m0,s52,19328
Ta 24,v1,28672
Ta 26,v1,29184
Mvaa 112,28,148
Mvaa 112,32,188
L_ v6,28,s65,128
Mvaa 120,28,152
Mvaa 120,32,192
Mvaa 128,28,156
Mvaa 128,32,196
W_ 4
Mvaa 132,224,196
s_nop 0
Ta 28,v1,24584
Ta 30,v1,25096
Mvaa 132,36,156
buffer_load_dword a254, v10, s[28:31], s55 offen
Mvaa 124,224,192
Mvaa 124,36,152
s_nop 0
Ta 32,v1,28680
Ta 34,v1,29192
Mvaa 116,224,188
Mvaa 116,36,148
buffer_load_dword a254, v12, s[24:27], s63 offen offset:896
Mvaa 108,224,184
Mvaa 108,36,144
Mvaa 100,224,180
Mvaa 100,36,140
s_waitcnt vmcnt(15) lgkmcnt(0)
s_barrier
s_cmp_eq_u32 s11, 2
s_cbranch_scc1 .Lqsl10
.Lqsl10_back:
Mvaa 136,20,140
S_ s65,s59,3145728
Ta 36,%0,0
Ta 38,%0,512
Mvaa 136,24,180
S_ m0,s52,24320
Da 60,v2,0
Mvaa 144,20,144
Mvaa 144,24,184
s_nop 0
Ta 224,%0,4096
Ta 226,%0,4608
Mvaa 152,20,148
Da 100,v3,0
Mvaa 152,24,188
L_ v6,28,s65,256
Mvaa 160,20,152
S_ s65,s59,3244032
Da 64,v2,2048
Mvaa 160,24,192
S_ m0,s52,25344
Mvaa 168,20,156
Mvaa 168,24,196
Da 104,v3,2048
Mvaa 172,32,196
s_nop 0
Ta 20,%0,8
Ta 22,%0,520
Mvaa 172,28,156
L_ v6,28,s65,256
Mvaa 164,32,192
S_ s65,s59,3342336
Da 68,v2,4096
Mvaa 164,28,152
S_ m0,s52,26368
Ta 24,%0,4104
Ta 26,%0,4616
Mvaa 156,32,188
Mvaa 156,28,148
Da 108,v3,4096
Mvaa 148,32,184
Mvaa 148,28,144
L_ v6,28,s65,256
Mvaa 140,32,180
S_ s65,s59,3440640
Da 72,v2,6144
Mvaa 140,28,140
W_ 10
S_ m0,s52,27392
Mvaa 176,36,140
s_nop 0
Ta 28,v1,0
Ta 30,v1,512
Mvaa 176,224,180
Da 112,v3,6144
Mvaa 184,36,144
Mvaa 184,224,184
s_nop 0
Ta 32,v1,4096
Ta 34,v1,4608
Mvaa 192,36,148
Da 76,v2,8192
Mvaa 192,224,188
L_ v6,28,s65,256
Mvaa 200,36,152
Mvaa 200,224,192
.Lpad_q2_3:
s_cbranch_execz .Lhop_q2_r
Da 116,v3,8192
Mvaa 208,36,156
.Lpad_qx_1:
s_cbranch_execz .Lhop_qx_r
Mvaa 208,224,196
W_ 9
Mvaa 212,24,196
s_nop 0
Ta 36,v1,8
Ta 38,v1,520
Mvaa 212,20,156
Mvaa 204,24,192
Mvaa 204,20,152
s_nop 0
Ta 224,v1,4104
Ta 226,v1,4616
Mvaa 196,24,188
Mvaa 196,20,148
Mvaa 188,24,184
Mvaa 188,20,144
Mvaa 180,24,180
Mvaa 180,20,140
s_waitcnt vmcnt(15) lgkmcnt(0)
s_barrier
s_cmp_eq_u32 s11, 1
s_cbranch_scc1 .Lpace11
.Lpace11_back:
Mvaa 216,28,140
S_ s65,s59,3932160
Ta 20,%0,8192
Ta 22,%0,8704
Mvaa 216,32,180
S_ m0,s52,0
Mvaa 224,28,144
Mvaa 224,32,184
s_nop 0
Ta 24,%0,12288
Ta 26,%0,12800
Mvaa 232,28,148
Mvaa 232,32,188
L_ v6,28,s65,0
Mvaa 240,28,152
S_ s65,s59,4030464
Mvaa 240,32,192
S_ m0,s52,1024
Mvaa 248,28,156
Mvaa 248,32,196
Mvaa 252,224,196
s_nop 0
Ta 28,%0,8200
Ta 30,%0,8712
Mvaa 252,36,156
L_ v6,28,s65,0
Mvaa 244,224,192
S_ s65,s59,4128768
Mvaa 244,36,152
S_ m0,s52,2048
Ta 32,%0,12296
Ta 34,%0,12808
Mvaa 236,224,188
Mvaa 236,36,148
L_ v6,28,s65,0
Mvaa 228,224,184
S_ s65,s59,4227072
Mvaa 228,36,144
S_ m0,s52,3072
Mvaa 220,224,180
Mvaa 220,36,140
W_ 4
Mvaa 16,20,60
s_nop 0
Ta 36,v1,8192
Ta 38,v1,8704
Mvaa 16,24,100
L_ v6,28,s65,0
Mvaa 24,20,64
S_ s65,s60,768
R_ v13,a248
Mvaa 24,24,104
S_ m0,s50,0
Ta 224,v1,12288
Ta 226,v1,12800
Mvaa 32,20,68
v_min_u32 v13, 240, v13
Mvaa 32,24,108
L_ v4,24,s65,0
Mvaa 40,20,72
S_ s65,s60,11008
v_lshlrev_b32 v13, 3, v13
Mvaa 40,24,112
S_ m0,s50,1024
Mvaa 48,20,76
Mvaa 48,24,116
W_ 4
Mvaa 52,32,116
s_nop 0
Ta 20,v1,8200
Ta 22,v1,8712
Mvaa 52,28,76
L_ v5,24,s65,0
Mvaa 44,32,112
S_ s65,s60,21248
Mvaa 44,28,72
S_ m0,s50,2048
Ta 24,v1,12296
Ta 26,v1,12808
Mvaa 36,32,108
Mvaa 36,28,68
L_ v4,24,s65,0
Mvaa 28,32,104
S_ s65,s60,31488
Mvaa 28,28,64
S_ m0,s50,3072
Mvaa 20,32,100
Mvaa 20,28,60
s_waitcnt vmcnt(13) lgkmcnt(0)
s_barrier
s_cmp_eq_u32 s11, 2
s_cbranch_scc1 .Lqsl12
.Lqsl12_back:
Mvaa 56,36,60
s_nop 0
Ta 28,%0,16384
Ta 30,%0,16896
Mvaa 56,224,100
L_ v5,24,s65,0
Mvaa 64,36,64
S_ s65,s60,41728
Mvaa 64,224,104
S_ m0,s50,4096
Ta 32,%0,20480
Ta 34,%0,20992
Mvaa 72,36,68
Mvaa 72,224,108
L_ v4,24,s65,0
Mvaa 80,36,72
S_ s65,s59,3932160
Mvaa 80,224,112
S_ m0,s52,8064
Mvaa 88,36,76
Mvaa 88,224,116
Mvaa 92,24,116
S_ s55,s56,6291456
Ta 36,%0,16392
Ta 38,%0,16904
Mvaa 92,20,76
L_ v6,28,s65,128
Mvaa 84,24,112
S_ s65,s59,4030464
Mvaa 84,20,72
S_ m0,s52,9088
Ta 224,%0,20488
Ta 226,%0,21000
Mvaa 76,24,108
Mvaa 76,20,68
L_ v6,28,s65,128
Mvaa 68,24,104
S_ s65,s59,4128768
Mvaa 68,20,64
S_ m0,s52,10112
Mvaa 60,24,100
Mvaa 60,20,60
W_ 4
Mvaa 96,28,60
s_nop 0
Ta 20,v1,16384
Ta 22,v1,16896
Mvaa 96,32,100
L_ v6,28,s65,128
Mvaa 104,28,64
S_ s65,s59,4227072
Mvaa 104,32,104
S_ m0,s52,11136
Ta 24,v1,20480
Ta 26,v1,20992
Mvaa 112,28,68
Mvaa 112,32,108
L_ v6,28,s65,128
Mvaa 120,28,72
Mvaa 120,32,112
Mvaa 128,28,76
Mvaa 128,32,116
W_ 4
Mvaa 132,224,116
s_nop 0
Ta 28,v1,16392
Ta 30,v1,16904
Mvaa 132,36,76
buffer_load_dword a220, v13, s[44:47], s66 offen
Mvaa 124,224,112
Mvaa 124,36,72
s_nop 0
Ta 32,v1,20488
Ta 34,v1,21000
Mvaa 116,224,108
Mvaa 116,36,68
buffer_load_dword a254, v10, s[28:31], s55 offen
Mvaa 108,224,104
Mvaa 108,36,64
Mvaa 100,224,100
Mvaa 100,36,60
s_waitcnt vmcnt(15) lgkmcnt(0)
s_barrier
s_cmp_eq_u32 s11, 1
s_cbranch_scc1 .Lpace13
.Lpace13_back:
Mvaa 136,20,60
S_ s65,s59,3932160
Ta 36,%0,24576
Ta 38,%0,25088
Mvaa 136,24,100
S_ m0,s52,16128
Da 140,v2,20480
Mvaa 144,20,64
Mvaa 144,24,104
s_nop 0
Ta 224,%0,28672
Ta 226,%0,29184
Mvaa 152,20,68
Da 180,v3,20480
Mvaa 152,24,108
L_ v6,28,s65,256
Mvaa 160,20,72
S_ s65,s59,4030464
Da 144,v2,22528
Mvaa 160,24,112
S_ m0,s52,17152
Mvaa 168,20,76
Mvaa 168,24,116
Da 184,v3,22528
Mvaa 172,32,116
s_nop 0
Ta 20,%0,24584
Ta 22,%0,25096
Mvaa 172,28,76
L_ v6,28,s65,256
Mvaa 164,32,112
S_ s65,s59,4128768
Da 148,v2,24576
Mvaa 164,28,72
S_ m0,s52,18176
Ta 24,%0,28680
Ta 26,%0,29192
Mvaa 156,32,108
Mvaa 156,28,68
Da 188,v3,24576
Mvaa 148,32,104
Mvaa 148,28,64
L_ v6,28,s65,256
Mvaa 140,32,100
S_ s65,s59,4227072
Da 152,v2,26624
Mvaa 140,28,60
W_ 10
S_ m0,s52,19200
Mvaa 176,36,60
s_nop 0
Ta 28,v1,24576
Ta 30,v1,25088
Mvaa 176,224,100
Da 192,v3,26624
Mvaa 184,36,64
Mvaa 184,224,104
s_nop 0
Ta 32,v1,28672
Ta 34,v1,29184
Mvaa 192,36,68
Da 156,v2,28672
Mvaa 192,224,108
L_ v6,28,s65,256
Mvaa 200,36,72
Mvaa 200,224,112
Da 196,v3,28672
Mvaa 208,36,76
Mvaa 208,224,116
W_ 9
Mvaa 212,24,116
s_nop 0
Ta 36,v1,24584
Ta 38,v1,25096
Mvaa 212,20,76
buffer_load_dword a254, v12, s[24:27], s63 offen offset:1024
Mvaa 204,24,112
Mvaa 204,20,72
s_nop 0
Ta 224,v1,28680
Ta 226,v1,29192
Mvaa 196,24,108
Mvaa 196,20,68
Mvaa 188,24,104
Mvaa 188,20,64
Mvaa 180,24,100
Mvaa 180,20,60
s_waitcnt vmcnt(16) lgkmcnt(0)
s_barrier
s_cmp_eq_u32 s11, 2
s_cbranch_scc1 .Lqsl14
.Lqsl14_back:
Mvaa 216,28,60
S_ s65,s59,4718592
Ta 20,%0,0
Ta 22,%0,512
Mvaa 216,32,100
S_ m0,s52,24576
Mvaa 224,28,64
Mvaa 224,32,104
s_nop 0
Ta 24,%0,4096
Ta 26,%0,4608
Mvaa 232,28,68
Mvaa 232,32,108
L_ v6,28,s65,0
Mvaa 240,28,72
S_ s65,s59,4816896
Mvaa 240,32,112
S_ m0,s52,25600
Mvaa 248,28,76
Mvaa 248,32,116
Mvaa 252,224,116
s_nop 0
Ta 28,%0,8
Ta 30,%0,520
Mvaa 252,36,76
L_ v6,28,s65,0
Mvaa 244,224,112
S_ s65,s59,4915200
Mvaa 244,36,72
S_ m0,s52,26624
Ta 32,%0,4104
Ta 34,%0,4616
Mvaa 236,224,108
Mvaa 236,36,68
L_ v6,28,s65,0
Mvaa 228,224,104
S_ s65,s59,5013504
Mvaa 228,36,64
S_ m0,s52,27648
Mvaa 220,224,100
Mvaa 220,36,60
W_ 4
Mvaa 16,20,140
s_nop 0
Ta 36,v1,0
Ta 38,v1,512
Mvaa 16,24,180
L_ v6,28,s65,0
Mvaa 24,20,144
S_ s65,s60,896
Mvaa 24,24,184
S_ m0,s50,20480
Ta 224,v1,4096
Ta 226,v1,4608
Mvaa 32,20,148
Mvaa 32,24,188
L_ v4,24,s65,0
Mvaa 40,20,152
S_ s65,s60,11136
Mvaa 40,24,192
S_ m0,s50,21504
Mvaa 48,20,156
Mvaa 48,24,196
W_ 4
Mvaa 52,32,196
s_nop 0
Ta 20,v1,8
Ta 22,v1,520
Mvaa 52,28,156
L_ v5,24,s65,0
Mvaa 44,32,192
S_ s65,s60,21376
Mvaa 44,28,152
S_ m0,s50,22528
Ta 24,v1,4104
Ta 26,v1,4616
Mvaa 36,32,188
Mvaa 36,28,148
L_ v4,24,s65,0
Mvaa 28,32,184
S_ s65,s60,31616
Mvaa 28,28,144
S_ m0,s50,23552
Mvaa 20,32,180
Mvaa 20,28,140
s_waitcnt vmcnt(14) lgkmcnt(0)
s_barrier
s_cmp_eq_u32 s11, 1
s_cbranch_scc1 .Lpace15
.Lpace15_back:
Mvaa 56,36,140
s_nop 0
Ta 28,%0,8192
Ta 30,%0,8704
Mvaa 56,224,180
L_ v5,24,s65,0
Mvaa 64,36,144
S_ s65,s60,41856
Mvaa 64,224,184
S_ m0,s50,24576
Ta 32,%0,12288
Ta 34,%0,12800
Mvaa 72,36,148
Mvaa 72,224,188
L_ v4,24,s65,0
Mvaa 80,36,152
S_ s65,s59,4718592
Mvaa 80,224,192
S_ m0,s52,-128
Mvaa 88,36,156
Mvaa 88,224,196
Mvaa 92,24,196
S_ s55,s56,7077888
Ta 36,%0,8200
Ta 38,%0,8712
Mvaa 92,20,156
L_ v6,28,s65,128
Mvaa 84,24,192
S_ s65,s59,4816896
Mvaa 84,20,152
S_ m0,s52,896
Ta 224,%0,12296
Ta 226,%0,12808
Mvaa 76,24,188
Mvaa 76,20,148
L_ v6,28,s65,128
Mvaa 68,24,184
S_ s65,s59,4915200
Mvaa 68,20,144
S_ m0,s52,1920
Mvaa 60,24,180
Mvaa 60,20,140
W_ 4
Mvaa 96,28,140
s_nop 0
Ta 20,v1,8192
Ta 22,v1,8704
Mvaa 96,32,180
L_ v6,28,s65,128
Mvaa 104,28,144
S_ s65,s59,5013504
Mvaa 104,32,184
S_ m0,s52,2944
Ta 24,v1,12288
Ta 26,v1,12800
Mvaa 112,28,148
Mvaa 112,32,188
L_ v6,28,s65,128
Mvaa 120,28,152
Mvaa 120,32,192
Mvaa 128,28,156
Mvaa 128,32,196
W_ 4
Mvaa 132,224,196
s_nop 0
Ta 28,v1,8200
Ta 30,v1,8712
Mvaa 132,36,156
buffer_load_dword a254, v10, s[28:31], s55 offen
Mvaa 124,224,192
Mvaa 124,36,152
s_nop 0
Ta 32,v1,12296
Ta 34,v1,12808
Mvaa 116,224,188
Mvaa 116,36,148
buffer_load_dword a254, v12, s[24:27], s63 offen offset:1152
Mvaa 108,224,184
Mvaa 108,36,144
Mvaa 100,224,180
Mvaa 100,36,140
s_waitcnt vmcnt(16) lgkmcnt(0)
s_barrier
Mvaa 136,20,140
S_ s65,s59,4718592
Ta 36,%0,16384
Ta 38,%0,16896
Mvaa 136,24,180
S_ m0,s52,7936
Da 60,v2,0
Mvaa 144,20,144
Mvaa 144,24,184
s_nop 0
Ta 224,%0,20480
Ta 226,%0,20992
Mvaa 152,20,148
Da 100,v3,0
Mvaa 152,24,188
L_ v6,28,s65,256
Mvaa 160,20,152
S_ s65,s59,4816896
Da 64,v2,2048
Mvaa 160,24,192
S_ m0,s52,8960
Mvaa 168,20,156
Mvaa 168,24,196
Da 104,v3,2048
Mvaa 172,32,196
s_nop 0
Ta 20,%0,16392
Ta 22,%0,16904
Mvaa 172,28,156
L_ v6,28,s65,256
Mvaa 164,32,192
S_ s65,s59,4915200
Da 68,v2,4096
Mvaa 164,28,152
S_ m0,s52,9984
Ta 24,%0,20488
Ta 26,%0,21000
Mvaa 156,32,188
Mvaa 156,28,148
Da 108,v3,4096
Mvaa 148,32,184
Mvaa 148,28,144
L_ v6,28,s65,256
Mvaa 140,32,180
S_ s65,s59,5013504
Da 72,v2,6144
Mvaa 140,28,140
W_ 10
S_ m0,s52,11008
Mvaa 176,36,140
s_nop 0
Ta 28,v1,16384
Ta 30,v1,16896
Mvaa 176,224,180
Da 112,v3,6144
Mvaa 184,36,144
Mvaa 184,224,184
s_nop 0
Ta 32,v1,20480
Ta 34,v1,20992
Mvaa 192,36,148
Da 76,v2,8192
Mvaa 192,224,188
L_ v6,28,s65,256
Mvaa 200,36,152
Mvaa 200,224,192
Da 116,v3,8192
Mvaa 208,36,156
Mvaa 208,224,196
W_ 9
Mvaa 212,24,196
s_nop 0
Ta 36,v1,16392
Ta 38,v1,16904
Mvaa 212,20,156
Mvaa 204,24,192
Mvaa 204,20,152
s_nop 0
Ta 224,v1,20488
Ta 226,v1,21000
Mvaa 196,24,188
.Lpad_q2_4:
s_cbranch_execz .Lhop_q2_r
Mvaa 196,20,148
.Lpad_qx_2:
s_cbranch_execz .Lhop_qx_r
Mvaa 188,24,184
Mvaa 188,20,144
Mvaa 180,24,180
Mvaa 180,20,140
s_waitcnt vmcnt(15) lgkmcnt(0)
s_barrier
s_cmp_eq_u32 s11, 1
s_cbranch_scc1 .Lpace17
.Lpace17_back:
Mvaa 216,28,140
S_ s65,s59,5505024
Ta 20,%0,24576
Ta 22,%0,25088
Mvaa 216,32,180
S_ m0,s52,16384
Mvaa 224,28,144
Mvaa 224,32,184
s_nop 0
Ta 24,%0,28672
Ta 26,%0,29184
Mvaa 232,28,148
Mvaa 232,32,188
L_ v6,28,s65,0
Mvaa 240,28,152
S_ s65,s59,5603328
Mvaa 240,32,192
S_ m0,s52,17408
Mvaa 248,28,156
Mvaa 248,32,196
Mvaa 252,224,196
s_nop 0
Ta 28,%0,24584
Ta 30,%0,25096
Mvaa 252,36,156
L_ v6,28,s65,0
Mvaa 244,224,192
S_ s65,s59,5701632
Mvaa 244,36,152
S_ m0,s52,18432
Ta 32,%0,28680
Ta 34,%0,29192
Mvaa 236,224,188
Mvaa 236,36,148
L_ v6,28,s65,0
Mvaa 228,224,184
S_ s65,s59,5799936
Mvaa 228,36,144
S_ m0,s52,19456
Mvaa 220,224,180
Mvaa 220,36,140
W_ 4
Mvaa 16,20,60
s_nop 0
Ta 36,v1,24576
Ta 38,v1,25088
Mvaa 16,24,100
L_ v6,28,s65,0
Mvaa 24,20,64
S_ s65,s60,1024
Mvaa 24,24,104
S_ m0,s50,0
Ta 224,v1,28672
Ta 226,v1,29184
Mvaa 32,20,68
Mvaa 32,24,108
L_ v4,24,s65,0
Mvaa 40,20,72
S_ s65,s60,11264
Mvaa 40,24,112
S_ m0,s50,1024
Mvaa 48,20,76
Mvaa 48,24,116
W_ 4
Mvaa 52,32,116
s_nop 0
Ta 20,v1,24584
Ta 22,v1,25096
Mvaa 52,28,76
L_ v5,24,s65,0
Mvaa 44,32,112
S_ s65,s60,21504
Mvaa 44,28,72
S_ m0,s50,2048
Ta 24,v1,28680
Ta 26,v1,29192
Mvaa 36,32,108
Mvaa 36,28,68
L_ v4,24,s65,0
Mvaa 28,32,104
S_ s65,s60,31744
Mvaa 28,28,64
S_ m0,s50,3072
Mvaa 20,32,100
Mvaa 20,28,60
s_waitcnt vmcnt(13) lgkmcnt(0)
s_barrier
Mvaa 56,36,60
s_nop 0
Ta 28,%0,0
Ta 30,%0,512
Mvaa 56,224,100
L_ v5,24,s65,0
Mvaa 64,36,64
S_ s65,s60,41984
Mvaa 64,224,104
S_ m0,s50,4096
Ta 32,%0,4096
Ta 34,%0,4608
Mvaa 72,36,68
Mvaa 72,224,108
L_ v4,24,s65,0
Mvaa 80,36,72
S_ s65,s59,5505024
Mvaa 80,224,112
S_ m0,s52,24448
Mvaa 88,36,76
Mvaa 88,224,116
Mvaa 92,24,116
s_nop 0
Ta 36,%0,8
Ta 38,%0,520
Mvaa 92,20,76
L_ v6,28,s65,128
Mvaa 84,24,112
S_ s65,s59,5603328
Mvaa 84,20,72
S_ m0,s52,25472
Ta 224,%0,4104
Ta 226,%0,4616
Mvaa 76,24,108
Mvaa 76,20,68
L_ v6,28,s65,128
Mvaa 68,24,104
S_ s65,s59,5701632
Mvaa 68,20,64
S_ m0,s52,26496
Mvaa 60,24,100
Mvaa 60,20,60
W_ 4
Mvaa 96,28,60
s_nop 0
Ta 20,v1,0
Ta 22,v1,512
Mvaa 96,32,100
L_ v6,28,s65,128
Mvaa 104,28,64
S_ s65,s59,5799936
Mvaa 104,32,104
S_ m0,s52,27520
Ta 24,v1,4096
Ta 26,v1,4608
Mvaa 112,28,68
Mvaa 112,32,108
L_ v6,28,s65,128
Mvaa 120,28,72
S_ s65,s61,0
Mvaa 120,32,112
S_ m0,s51,36864
Mvaa 128,28,76
Mvaa 128,32,116
W_ 4
Mvaa 132,224,116
s_nop 0
Ta 28,v1,8
Ta 30,v1,520
Mvaa 132,36,76
Ln v7,32,s65,0
Mvaa 124,224,112
Mvaa 124,36,72
s_nop 0
Ta 32,v1,4104
Ta 34,v1,4616
Mvaa 116,224,108
Mvaa 116,36,68
Ln v8,32,s65,3072
Mvaa 108,224,104
S_ m0,s51,37760
Mvaa 108,36,64
Mvaa 100,224,100
Mvaa 100,36,60
s_waitcnt vmcnt(15) lgkmcnt(0)
s_barrier
s_cmp_eq_u32 s11, 1
s_cbranch_scc1 .Lpace19
.Lpace19_back:
Mvaa 136,20,60
s_nop 0
Ta 36,%0,8192
Ta 38,%0,8704
Mvaa 136,24,100
Da 140,v2,20480
Mvaa 144,20,64
Mvaa 144,24,104
s_nop 0
Ta 224,%0,12288
Ta 226,%0,12800
Mvaa 152,20,68
Da 180,v3,20480
Mvaa 152,24,108
Ln v7,32,s65,128
Mvaa 160,20,72
Da 144,v2,22528
Mvaa 160,24,112
Mvaa 168,20,76
Mvaa 168,24,116
Da 184,v3,22528
Mvaa 172,32,116
s_nop 0
Ta 20,%0,8200
Ta 22,%0,8712
Mvaa 172,28,76
Ln v8,32,s65,3200
Mvaa 164,32,112
S_ m0,s51,38656
Da 148,v2,24576
Mvaa 164,28,72
s_nop 0
Ta 24,%0,12296
Ta 26,%0,12808
Mvaa 156,32,108
Mvaa 156,28,68
Da 188,v3,24576
Mvaa 148,32,104
Mvaa 148,28,64
Ln v7,32,s65,256
Mvaa 140,32,100
Da 152,v2,26624
Mvaa 140,28,60
W_ 10
Mvaa 176,36,60
s_nop 0
Ta 28,v1,8192
Ta 30,v1,8704
Mvaa 176,224,100
Da 192,v3,26624
Mvaa 184,36,64
Mvaa 184,224,104
s_nop 0
Ta 32,v1,12288
Ta 34,v1,12800
Mvaa 192,36,68
Da 156,v2,28672
Mvaa 192,224,108
Ln v8,32,s65,3328
Mvaa 200,36,72
S_ s65,s59,5505024
Mvaa 200,224,112
S_ m0,s52,-256
Da 196,v3,28672
Mvaa 208,36,76
Mvaa 208,224,116
W_ 9
Mvaa 212,24,116
s_nop 0
Ta 36,v1,8200
Ta 38,v1,8712
Mvaa 212,20,76
L_ v6,28,s65,256
Mvaa 204,24,112
S_ s65,s59,5603328
Mvaa 204,20,72
S_ m0,s52,768
Ta 224,v1,12296
Ta 226,v1,12808
Mvaa 196,24,108
Mvaa 196,20,68
L_ v6,28,s65,256
Mvaa 188,24,104
S_ s65,s59,5701632
Mvaa 188,20,64
S_ m0,s52,1792
Mvaa 180,24,100
Mvaa 180,20,60
s_waitcnt vmcnt(17) lgkmcnt(0)
s_barrier
Mvaa 216,28,60
s_nop 0
Ta 20,%0,16384
Ta 22,%0,16896
Mvaa 216,32,100
L_ v6,28,s65,256
Mvaa 224,28,64
S_ s65,s59,5799936
Mvaa 224,32,104
S_ m0,s52,2816
Ta 24,%0,20480
Ta 26,%0,20992
Mvaa 232,28,68
Mvaa 232,32,108
L_ v6,28,s65,256
Mvaa 240,28,72
S_ s65,s59,6291456
Mvaa 240,32,112
S_ m0,s52,7936
Mvaa 248,28,76
Mvaa 248,32,116
Mvaa 252,224,116
s_nop 0
Ta 28,%0,16392
Ta 30,%0,16904
Mvaa 252,36,76
L_ v6,28,s65,256
Mvaa 244,224,112
S_ s65,s59,6389760
Mvaa 244,36,72
S_ m0,s52,8960
Ta 32,%0,20488
Ta 34,%0,21000
Mvaa 236,224,108
Mvaa 236,36,68
L_ v6,28,s65,256
Mvaa 228,224,104
S_ s65,s59,6488064
Mvaa 228,36,64
S_ m0,s52,9984
Mvaa 220,224,100
Mvaa 220,36,60
W_ 4
Mvaa 16,20,140
s_nop 0
Ta 36,v1,16384
Ta 38,v1,16896
Mvaa 16,24,180
L_ v6,28,s65,256
Mvaa 24,20,144
S_ s65,s59,6586368
Mvaa 24,24,184
S_ m0,s52,11008
Ta 224,v1,20480
Ta 226,v1,20992
Mvaa 32,20,148
Mvaa 32,24,188
L_ v6,28,s65,256
Mvaa 40,20,152
S_ s65,s60,1152
Mvaa 40,24,192
S_ m0,s50,20480
Mvaa 48,20,156
Mvaa 48,24,196
W_ 4
Mvaa 52,32,196
s_nop 0
Ta 20,v1,16392
Ta 22,v1,16904
Mvaa 52,28,156
L_ v4,24,s65,0
Mvaa 44,32,192
S_ s65,s60,11392
Mvaa 44,28,152
S_ m0,s50,21504
Ta 24,v1,20488
Ta 26,v1,21000
Mvaa 36,32,188
Mvaa 36,28,148
L_ v5,24,s65,0
Mvaa 28,32,184
S_ s65,s60,21632
Mvaa 28,28,144
S_ m0,s50,22528
Mvaa 20,32,180
Mvaa 20,28,140
s_waitcnt vmcnt(16) lgkmcnt(0)
s_barrier
s_cmp_eq_u32 s11, 1
s_cbranch_scc1 .Lpace21
.Lpace21_back:
Mvaa 56,36,140
s_nop 0
Ta 28,%0,24576
Ta 30,%0,25088
Mvaa 56,224,180
L_ v4,24,s65,0
Mvaa 64,36,144
S_ s65,s60,31872
Mvaa 64,224,184
S_ m0,s50,23552
Ta 32,%0,28672
Ta 34,%0,29184
Mvaa 72,36,148
Mvaa 72,224,188
L_ v5,24,s65,0
Mvaa 80,36,152
S_ s65,s60,42112
Mvaa 80,224,192
S_ m0,s50,24576
Mvaa 88,36,156
Mvaa 88,224,196
Mvaa 92,24,196
s_nop 0
Ta 36,%0,24584
Ta 38,%0,25096
Mvaa 92,20,156
L_ v4,24,s65,0
Mvaa 84,24,192
S_ s65,s59,7077888
Mvaa 84,20,152
S_ m0,s52,16128
Ta 224,%0,28680
Ta 226,%0,29192
Mvaa 76,24,188
Mvaa 76,20,148
L_ v6,28,s65,256
Mvaa 68,24,184
S_ s65,s59,7176192
Mvaa 68,20,144
S_ m0,s52,17152
Mvaa 60,24,180
Mvaa 60,20,140
W_ 4
Mvaa 96,28,140
s_nop 0
Ta 20,v1,24576
Ta 22,v1,25088
Mvaa 96,32,180
L_ v6,28,s65,256
Mvaa 104,28,144
S_ s65,s59,7274496
Mvaa 104,32,184
S_ m0,s52,18176
Ta 24,v1,28672
Ta 26,v1,29184
Mvaa 112,28,148
Mvaa 112,32,188
L_ v6,28,s65,256
Mvaa 120,28,152
S_ s65,s59,7372800
Mvaa 120,32,192
S_ m0,s52,19200
Mvaa 128,28,156
Mvaa 128,32,196
W_ 4
Mvaa 132,224,196
s_nop 0
Ta 28,v1,24584
Ta 30,v1,25096
Mvaa 132,36,156
L_ v6,28,s65,256
Mvaa 124,224,192
Mvaa 124,36,152
s_nop 0
Ta 32,v1,28680
Ta 34,v1,29192
Mvaa 116,224,188
Mvaa 116,36,148
Mvaa 108,224,184
Mvaa 108,36,144
Mvaa 100,224,180
Mvaa 100,36,140
s_waitcnt vmcnt(13) lgkmcnt(0)
s_barrier
Mvaa 136,20,140
S_ s65,s59,6291456
Ta 36,%0,0
Ta 38,%0,512
Mvaa 136,24,180
S_ m0,s52,24448
Da 60,v2,0
Mvaa 144,20,144
Mvaa 144,24,184
s_nop 0
Ta 224,%0,4096
Ta 226,%0,4608
Mvaa 152,20,148
Da 100,v3,0
Mvaa 152,24,188
L_ v6,28,s65,128
Mvaa 160,20,152
S_ s65,s59,6389760
Da 64,v2,2048
Mvaa 160,24,192
S_ m0,s52,25472
Mvaa 168,20,156
Mvaa 168,24,196
Da 104,v3,2048
Mvaa 172,32,196
s_nop 0
Ta 20,%0,8
Ta 22,%0,520
Mvaa 172,28,156
L_ v6,28,s65,128
Mvaa 164,32,192
S_ s65,s59,6488064
Da 68,v2,4096
Mvaa 164,28,152
S_ m0,s52,26496
Ta 24,%0,4104
Ta 26,%0,4616
Mvaa 156,32,188
Mvaa 156,28,148
Da 108,v3,4096
Mvaa 148,32,184
Mvaa 148,28,144
L_ v6,28,s65,128
Mvaa 140,32,180
S_ s65,s59,6586368
Da 72,v2,6144
Mvaa 140,28,140
W_ 10
S_ m0,s52,27520
Mvaa 176,36,140
s_nop 0
Ta 28,v1,0
Ta 30,v1,512
Mvaa 176,224,180
Da 112,v3,6144
Mvaa 184,36,144
Mvaa 184,224,184
s_nop 0
Ta 32,v1,4096
Ta 34,v1,4608
Mvaa 192,36,148
Da 76,v2,8192
Mvaa 192,224,188
L_ v6,28,s65,128
Mvaa 200,36,152
S_ s65,s61,12288
Mvaa 200,224,192
S_ m0,s51,49152
Da 116,v3,8192
Mvaa 208,36,156
Mvaa 208,224,196
W_ 9
Mvaa 212,24,196
s_nop 0
Ta 36,v1,8
Ta 38,v1,520
Mvaa 212,20,156
Ln v7,32,s65,0
Mvaa 204,24,192
Mvaa 204,20,152
s_nop 0
Ta 224,v1,4104
Ta 226,v1,4616
Mvaa 196,24,188
Mvaa 196,20,148
Ln v8,32,s65,3072
Mvaa 188,24,184
S_ m0,s51,50048
Mvaa 188,20,144
Mvaa 180,24,180
Mvaa 180,20,140
s_waitcnt vmcnt(10) lgkmcnt(0)
s_barrier
s_cmp_eq_u32 s11, 1
s_cbranch_scc1 .Lpace23
.Lpace23_back:
Mvaa 216,28,140
.Lpad_q2_5:
s_cbranch_execz .Lhop_q2_r
Ta 20,v1,8200
Ta 22,v1,8712
Mvaa 216,32,180
Ln v7,32,s65,128
Mvaa 224,28,144
.Lpad_qx_3:
s_cbranch_execz .Lhop_qx_r
Mvaa 224,32,184
s_nop 0
Ta 24,v1,12296
Ta 26,v1,12808
Mvaa 232,28,148
Mvaa 232,32,188
Ln v8,32,s65,3200
Mvaa 240,28,152
S_ m0,s51,50944
Mvaa 240,32,192
Mvaa 248,28,156
Mvaa 248,32,196
Mvaa 252,224,196
s_nop 0
Ta 28,v1,8192
Ta 30,v1,8704
Mvaa 252,36,156
Ln v7,32,s65,256
Mvaa 244,224,192
Mvaa 244,36,152
s_nop 0
Ta 32,v1,12288
Ta 34,v1,12800
Mvaa 236,224,188
Mvaa 236,36,148
Ln v8,32,s65,3328
Mvaa 228,224,184
S_ s65,s59,7077888
Mvaa 228,36,144
S_ m0,s52,-128
Mvaa 220,224,180
Mvaa 220,36,140
W_ 4
Mvaa 220,20,60
s_nop 0
Ta 36,%0,8200
Ta 38,%0,8712
Mvaa 220,24,100
Da 140,v2,20480
Mvaa 228,20,64
Mvaa 228,24,104
s_nop 0
Ta 224,%0,12296
Ta 226,%0,12808
Mvaa 236,20,68
Da 180,v3,20480
Mvaa 236,24,108
L_ v6,28,s65,128
Mvaa 244,20,72
S_ s65,s59,7176192
Da 144,v2,22528
Mvaa 244,24,112
S_ m0,s52,896
Mvaa 252,20,76
Mvaa 252,24,116
W_ 7
Da 184,v3,22528
Mvaa 248,32,116
s_nop 0
Ta 20,%0,8192
Ta 22,%0,8704
Mvaa 248,28,76
L_ v6,28,s65,128
Mvaa 240,32,112
S_ s65,s59,7274496
Da 148,v2,24576
Mvaa 240,28,72
S_ m0,s52,1920
Ta 24,%0,12288
Ta 26,%0,12800
Mvaa 232,32,108
Mvaa 232,28,68
Da 188,v3,24576
Mvaa 224,32,104
Mvaa 224,28,64
L_ v6,28,s65,128
Mvaa 216,32,100
S_ s65,s59,7372800
Da 152,v2,26624
Mvaa 216,28,60
s_waitcnt vmcnt(13) lgkmcnt(0)
s_barrier
S_ m0,s52,2944
Mvaa 180,36,60
s_nop 0
Ta 28,v1,16392
Ta 30,v1,16904
Mvaa 180,224,100
Da 192,v3,26624
Mvaa 188,36,64
Mvaa 188,224,104
s_nop 0
Ta 32,v1,20488
Ta 34,v1,21000
Mvaa 196,36,68
Da 156,v2,28672
Mvaa 196,224,108
L_ v6,28,s65,128
Mvaa 204,36,72
S_ s65,s59,6291456
Mvaa 204,224,112
S_ m0,s52,8192
Da 196,v3,28672
Mvaa 212,36,76
Mvaa 212,224,116
Mvaa 208,24,116
s_nop 0
Ta 36,v1,16384
Ta 38,v1,16896
Mvaa 208,20,76
L_ v6,28,s65,0
Mvaa 200,24,112
S_ s65,s59,6389760
Mvaa 200,20,72
S_ m0,s52,9216
Ta 224,v1,20480
Ta 226,v1,20992
Mvaa 192,24,108
Mvaa 192,20,68
L_ v6,28,s65,0
Mvaa 184,24,104
S_ s65,s59,6488064
Mvaa 184,20,64
S_ m0,s52,10240
Mvaa 176,24,100
Mvaa 176,20,60
W_ 6
Mvaa 220,28,140
s_nop 0
Ta 20,%0,16392
Ta 22,%0,16904
Mvaa 220,32,180
L_ v6,28,s65,0
Mvaa 228,28,144
S_ s65,s59,6586368
Mvaa 228,32,184
S_ m0,s52,11264
Ta 24,%0,20488
Ta 26,%0,21000
Mvaa 236,28,148
Mvaa 236,32,188
L_ v6,28,s65,0
Mvaa 244,28,152
Mvaa 244,32,192
W_ 9
Mvaa 252,28,156
W_ 8
Mvaa 252,32,196
W_ 4
Mvaa 248,224,196
s_nop 0
Ta 28,%0,16384
Ta 30,%0,16896
Mvaa 248,36,156
Mvaa 240,224,192
Mvaa 240,36,152
s_nop 0
Ta 32,%0,20480
Ta 34,%0,20992
Mvaa 232,224,188
Mvaa 232,36,148
Mvaa 224,224,184
Mvaa 224,36,144
Mvaa 216,224,180
Mvaa 216,36,140
s_cmp_eq_u32 s58, 0
s_cbranch_scc0 .Lhq_0
s_mov_b64 exec, 0
s_bitcmp1_b32 s4, 1
s_cbranch_scc1 .Lhop_a0_b
s_bitcmp1_b32 s4, 0
s_cbranch_scc1 .Lpad_a0_1
s_branch .Lpad_a0_0
.Lhop_a0_b:
s_bitcmp1_b32 s4, 0
s_cbranch_scc1 .Lpad_a0_3
s_branch .Lpad_a0_2
.Lhop_a0_r:
s_mov_b64 exec, -1
s_nop 1
.Lhq_0:
s_cmp_eq_u32 s58, 1
s_cbranch_scc0 .Lhq_1
s_mov_b64 exec, 0
s_bitcmp1_b32 s4, 1
s_cbranch_scc1 .Lhop_a1_b
s_bitcmp1_b32 s4, 0
s_cbranch_scc1 .Lpad_a1_1
s_branch .Lpad_a1_0
.Lhop_a1_b:
s_bitcmp1_b32 s4, 0
s_cbranch_scc1 .Lpad_a1_3
s_branch .Lpad_a1_2
.Lhop_a1_r:
s_mov_b64 exec, -1
s_nop 1
.Lhq_1:
s_cmp_eq_u32 s58, 2
s_cbranch_scc0 .Lhq_2
s_mov_b64 exec, 0
s_bitcmp1_b32 s4, 1
s_cbranch_scc1 .Lhop_a2_b
s_bitcmp1_b32 s4, 0
s_cbranch_scc1 .Lpad_a2_1
s_branch .Lpad_a2_0
.Lhop_a2_b:
s_bitcmp1_b32 s4, 0
s_cbranch_scc1 .Lpad_a2_3
s_branch .Lpad_a2_2
.Lhop_a2_r:
s_mov_b64 exec, -1
s_nop 1
.Lhq_2:
s_waitcnt vmcnt(14) lgkmcnt(0)
s_barrier
s_cmp_eq_u32 s11, 1
s_cbranch_scc1 .Lpace25
.Lpace25_back:
Mvaa 180,20,140
S_ s65,s59,7077888
Ta 36,v1,24584
Ta 38,v1,25096
Mvaa 180,24,180
S_ m0,s52,16384
Mvaa 188,20,144
Mvaa 188,24,184
s_nop 0
Ta 224,v1,28680
Ta 226,v1,29192
Mvaa 196,20,148
U_ v248,s48,v248
U_ v249,s48,v249
Mvaa 196,24,188
L_ v6,28,s65,0
Mvaa 204,20,152
S_ s65,s59,7176192
U_ v250,s48,v250
U_ v251,s48,v251
Mvaa 204,24,192
S_ m0,s52,17408
U_ v252,s48,v252
U_ v253,s48,v253
Mvaa 212,20,156
)A"
R"A(U_ v254,s48,v254
U_ v255,s48,v255
Mvaa 212,24,196
C_ 255,254,255
C_ 254,252,253
Mvaa 208,32,196
s_nop 0
Ta 20,v1,24576
Ta 22,v1,25088
Mvaa 208,28,156
L_ v6,28,s65,0
Mvaa 200,32,192
S_ s65,s59,7274496
C_ 253,250,251
C_ 252,248,249
Mvaa 200,28,152
S_ m0,s52,18432
Ta 24,v1,28672
Ta 26,v1,29184
Mvaa 192,32,188
U_ v240,s48,v240
U_ v241,s48,v241
Mvaa 192,28,148
L_ v6,28,s65,0
Mvaa 184,32,184
S_ s65,s59,7372800
U_ v242,s48,v242
U_ v243,s48,v243
Mvaa 184,28,144
S_ m0,s52,19456
U_ v244,s48,v244
U_ v245,s48,v245
Mvaa 176,32,180
U_ v246,s48,v246
U_ v247,s48,v247
Mvaa 176,28,140
W_ 4
C_ 251,246,247
C_ 250,244,245
Mvaa 140,36,60
s_nop 0
Ta 28,%0,24584
Ta 30,%0,25096
Mvaa 140,224,100
L_ v6,28,s65,0
Mvaa 148,36,64
C_ 249,242,243
C_ 248,240,241
Mvaa 148,224,104
s_nop 0
Ta 32,%0,28680
Ta 34,%0,29192
Mvaa 156,36,68
U_ v232,s48,v232
U_ v233,s48,v233
Mvaa 156,224,108
U_ v234,s48,v234
U_ v235,s48,v235
Mvaa 164,36,72
U_ v236,s48,v236
U_ v237,s48,v237
Mvaa 164,224,112
U_ v238,s48,v238
U_ v239,s48,v239
Mvaa 172,36,76
C_ 247,238,239
C_ 246,236,237
Mvaa 172,224,116
W_ 4
C_ 245,234,235
C_ 244,232,233
Mvaa 168,24,116
s_nop 0
Ta 36,%0,24576
Ta 38,%0,25088
Mvaa 168,20,76
U_ v224,s48,v224
U_ v225,s48,v225
Mvaa 160,24,112
U_ v226,s48,v226
U_ v227,s48,v227
Mvaa 160,20,72
s_nop 0
Ta 224,%0,28672
Ta 226,%0,29184
Mvaa 152,24,108
U_ v228,s48,v228
U_ v229,s48,v229
Mvaa 152,20,68
U_ v230,s48,v230
U_ v231,s48,v231
Mvaa 144,24,104
C_ 243,230,231
C_ 242,228,229
Mvaa 144,20,64
C_ 241,226,227
C_ 240,224,225
Mvaa 136,24,100
U_ v216,s48,v216
U_ v217,s48,v217
Mvaa 136,20,60
s_waitcnt vmcnt(8) lgkmcnt(0)
s_barrier
U_ v218,s48,v218
Mvaa 100,28,60
S_ s65,s61,0
Ta 20,v1,8
Ta 22,v1,520
Mvaa 100,32,100
S_ m0,s51,24576
U_ v219,s48,v219
Mvaa 108,28,64
U_ v220,s48,v220
U_ v221,s48,v221
Mvaa 108,32,104
s_nop 0
Ta 24,v1,4104
Ta 26,v1,4616
Mvaa 116,28,68
U_ v222,s48,v222
U_ v223,s48,v223
Mvaa 116,32,108
Ln v9,36,s65,0
Mvaa 124,28,72
C_ 239,222,223
C_ 238,220,221
Mvaa 124,32,112
C_ 237,218,219
C_ 236,216,217
Mvaa 132,28,76
U_ v208,s48,v208
U_ v209,s48,v209
Mvaa 132,32,116
U_ v210,s48,v210
U_ v211,s48,v211
Mvaa 128,224,116
s_nop 0
Ta 28,v1,0
Ta 30,v1,512
Mvaa 128,36,76
Ln v9,36,s65,3072
Mvaa 120,224,112
S_ m0,s51,25472
U_ v212,s48,v212
U_ v213,s48,v213
Mvaa 120,36,72
s_nop 0
Ta 32,v1,4096
Ta 34,v1,4608
Mvaa 112,224,108
U_ v214,s48,v214
U_ v215,s48,v215
Mvaa 112,36,68
Ln v9,36,s65,128
Mvaa 104,224,104
C_ 235,214,215
C_ 234,212,213
Mvaa 104,36,64
C_ 233,210,211
C_ 232,208,209
Mvaa 96,224,100
U_ v200,s48,v200
U_ v201,s48,v201
Mvaa 96,36,60
W_ 4
U_ v202,s48,v202
U_ v203,s48,v203
Mvaa 140,20,140
s_nop 0
Ta 36,%0,8
Ta 38,%0,520
Mvaa 140,24,180
Ln v9,36,s65,3200
Mvaa 148,20,144
S_ m0,s51,26368
U_ v204,s48,v204
U_ v205,s48,v205
Mvaa 148,24,184
s_nop 0
Ta 224,%0,4104
Ta 226,%0,4616
Mvaa 156,20,148
U_ v206,s48,v206
U_ v207,s48,v207
Mvaa 156,24,188
Ln v9,36,s65,256
Mvaa 164,20,152
C_ 231,206,207
C_ 230,204,205
Mvaa 164,24,192
C_ 229,202,203
C_ 228,200,201
Mvaa 172,20,156
U_ v192,s48,v192
U_ v193,s48,v193
Mvaa 172,24,196
W_ 4
U_ v194,s48,v194
U_ v195,s48,v195
Mvaa 168,32,196
s_nop 0
Ta 20,%0,0
Ta 22,%0,512
Mvaa 168,28,156
Ln v9,36,s65,3328
Mvaa 160,32,192
U_ v196,s48,v196
U_ v197,s48,v197
Mvaa 160,28,152
s_nop 0
Ta 24,%0,4096
Ta 26,%0,4608
Mvaa 152,32,188
U_ v198,s48,v198
U_ v199,s48,v199
Mvaa 152,28,148
C_ 227,198,199
C_ 226,196,197
Mvaa 144,32,184
C_ 225,194,195
C_ 224,192,193
Mvaa 144,28,144
U_ v184,s48,v184
U_ v185,s48,v185
Mvaa 136,32,180
U_ v186,s48,v186
U_ v187,s48,v187
Mvaa 136,28,140
s_waitcnt vmcnt(10) lgkmcnt(0)
s_barrier
s_cmp_eq_u32 s11, 1
s_cbranch_scc1 .Lpace27
.Lpace27_back:
U_ v188,s48,v188
Mvaa 100,36,140
s_nop 0
Ta 28,v1,8200
Ta 30,v1,8712
Mvaa 100,224,180
U_ v189,s48,v189
Mvaa 108,36,144
U_ v190,s48,v190
U_ v191,s48,v191
Mvaa 108,224,184
s_nop 0
Ta 32,v1,12296
Ta 34,v1,12808
Mvaa 116,36,148
C_ 223,190,191
C_ 222,188,189
Mvaa 116,224,188
C_ 221,186,187
C_ 220,184,185
Mvaa 124,36,152
U_ v176,s48,v176
U_ v177,s48,v177
Mvaa 124,224,192
U_ v178,s48,v178
U_ v179,s48,v179
Mvaa 132,36,156
U_ v180,s48,v180
U_ v181,s48,v181
Mvaa 132,224,196
U_ v182,s48,v182
U_ v183,s48,v183
Mvaa 128,24,196
s_nop 0
Ta 36,v1,8192
Ta 38,v1,8704
Mvaa 128,20,156
C_ 219,182,183
C_ 218,180,181
Mvaa 120,24,192
C_ 217,178,179
C_ 216,176,177
Mvaa 120,20,152
s_nop 0
Ta 224,v1,12288
Ta 226,v1,12800
Mvaa 112,24,188
U_ v168,s48,v168
U_ v169,s48,v169
Mvaa 112,20,148
U_ v170,s48,v170
U_ v171,s48,v171
Mvaa 104,24,184
U_ v172,s48,v172
U_ v173,s48,v173
Mvaa 104,20,144
U_ v174,s48,v174
U_ v175,s48,v175
Mvaa 96,24,180
C_ 215,174,175
C_ 214,172,173
Mvaa 96,20,140
W_ 4
C_ 213,170,171
C_ 212,168,169
Mvaa 60,28,60
s_nop 0
Ta 20,%0,8200
Ta 22,%0,8712
Mvaa 60,32,100
U_ v160,s48,v160
U_ v161,s48,v161
Mvaa 68,28,64
U_ v162,s48,v162
U_ v163,s48,v163
Mvaa 68,32,104
s_nop 0
Ta 24,%0,12296
Ta 26,%0,12808
Mvaa 76,28,68
U_ v164,s48,v164
U_ v165,s48,v165
Mvaa 76,32,108
U_ v166,s48,v166
U_ v167,s48,v167
Mvaa 84,28,72
C_ 211,166,167
C_ 210,164,165
Mvaa 84,32,112
C_ 209,162,163
C_ 208,160,161
Mvaa 92,28,76
U_ v152,s48,v152
U_ v153,s48,v153
Mvaa 92,32,116
W_ 4
U_ v154,s48,v154
U_ v155,s48,v155
Mvaa 88,224,116
s_nop 0
Ta 28,%0,8192
Ta 30,%0,8704
Mvaa 88,36,76
U_ v156,s48,v156
U_ v157,s48,v157
Mvaa 80,224,112
U_ v158,s48,v158
U_ v159,s48,v159
Mvaa 80,36,72
s_nop 0
Ta 32,%0,12288
Ta 34,%0,12800
Mvaa 72,224,108
C_ 207,158,159
C_ 206,156,157
Mvaa 72,36,68
C_ 205,154,155
C_ 204,152,153
Mvaa 64,224,104
U_ v144,s48,v144
U_ v145,s48,v145
Mvaa 64,36,64
U_ v146,s48,v146
U_ v147,s48,v147
Mvaa 56,224,100
U_ v148,s48,v148
U_ v149,s48,v149
Mvaa 56,36,60
s_waitcnt vmcnt(6) lgkmcnt(0)
s_barrier
U_ v150,s48,v150
Mvaa 20,20,60
S_ s65,s61,24576
Ta 36,v1,16392
Ta 38,v1,16904
Mvaa 20,24,100
S_ m0,s51,0
U_ v151,s48,v151
Mvaa 28,20,64
C_ 203,150,151
C_ 202,148,149
Mvaa 28,24,104
s_nop 0
Ta 224,v1,20488
Ta 226,v1,21000
Mvaa 36,20,68
C_ 201,146,147
C_ 200,144,145
Mvaa 36,24,108
Ln v7,32,s65,0
Mvaa 44,20,72
.Lpad_q2_6:
s_cbranch_execz .Lhop_q2_r
U_ v136,s48,v136
U_ v137,s48,v137
Mvaa 44,24,112
.Lpad_qx_4:
s_cbranch_execz .Lhop_qx_r
U_ v138,s48,v138
U_ v139,s48,v139
Mvaa 52,20,76
U_ v140,s48,v140
U_ v141,s48,v141
Mvaa 52,24,116
U_ v142,s48,v142
U_ v143,s48,v143
Mvaa 48,32,116
s_nop 0
Ta 20,v1,16384
Ta 22,v1,16896
Mvaa 48,28,76
Ln v8,32,s65,3072
Mvaa 40,32,112
S_ m0,s51,896
C_ 199,142,143
C_ 198,140,141
Mvaa 40,28,72
s_nop 0
Ta 24,v1,20480
Ta 26,v1,20992
Mvaa 32,32,108
C_ 197,138,139
C_ 196,136,137
Mvaa 32,28,68
Ln v7,32,s65,128
Mvaa 24,32,104
U_ v128,s48,v128
U_ v129,s48,v129
Mvaa 24,28,64
U_ v130,s48,v130
U_ v131,s48,v131
Mvaa 16,32,100
U_ v132,s48,v132
U_ v133,s48,v133
Mvaa 16,28,60
W_ 4
U_ v134,s48,v134
U_ v135,s48,v135
Mvaa 60,36,140
s_nop 0
Ta 28,%0,16392
Ta 30,%0,16904
Mvaa 60,224,180
Ln v8,32,s65,3200
Mvaa 68,36,144
S_ m0,s51,1792
C_ 195,134,135
C_ 194,132,133
Mvaa 68,224,184
s_nop 0
Ta 32,%0,20488
Ta 34,%0,21000
Mvaa 76,36,148
C_ 193,130,131
C_ 192,128,129
Mvaa 76,224,188
Ln v7,32,s65,256
Mvaa 84,36,152
U_ v120,s48,v120
U_ v121,s48,v121
Mvaa 84,224,192
U_ v122,s48,v122
U_ v123,s48,v123
Mvaa 92,36,156
U_ v124,s48,v124
U_ v125,s48,v125
Mvaa 92,224,196
W_ 4
U_ v126,s48,v126
U_ v127,s48,v127
Mvaa 88,24,196
s_nop 0
Ta 36,%0,16384
Ta 38,%0,16896
Mvaa 88,20,156
Ln v8,32,s65,3328
Mvaa 80,24,192
C_ 191,126,127
C_ 190,124,125
Mvaa 80,20,152
s_nop 0
Ta 224,%0,20480
Ta 226,%0,20992
Mvaa 72,24,188
C_ 189,122,123
C_ 188,120,121
Mvaa 72,20,148
U_ v112,s48,v112
U_ v113,s48,v113
Mvaa 64,24,184
U_ v114,s48,v114
U_ v115,s48,v115
Mvaa 64,20,144
U_ v116,s48,v116
U_ v117,s48,v117
Mvaa 56,24,180
U_ v118,s48,v118
U_ v119,s48,v119
Mvaa 56,20,140
s_waitcnt vmcnt(51) lgkmcnt(0)
s_barrier
s_cmp_eq_u32 s11, 1
s_cbranch_scc1 .Lpace29
.Lpace29_back:
C_ 187,118,119
Mvaa 20,28,140
S_ s65,s61,12288
C_ 186,116,117
Mvaa 20,32,180
S_ m0,s51,12288
C_ 185,114,115
C_ 184,112,113
Mvaa 28,28,144
U_ v104,s48,v104
U_ v105,s48,v105
Mvaa 28,32,184
Ln v9,36,s65,0
Mvaa 36,28,148
U_ v106,s48,v106
U_ v107,s48,v107
Mvaa 36,32,188
U_ v108,s48,v108
U_ v109,s48,v109
Mvaa 44,28,152
U_ v110,s48,v110
U_ v111,s48,v111
Mvaa 44,32,192
Ln v9,36,s65,3072
Mvaa 52,28,156
S_ m0,s51,13184
C_ 183,110,111
C_ 182,108,109
Mvaa 52,32,196
C_ 181,106,107
C_ 180,104,105
Mvaa 48,224,196
U_ v96,s48,v96
U_ v97,s48,v97
Mvaa 48,36,156
Ln v9,36,s65,128
Mvaa 40,224,192
U_ v98,s48,v98
U_ v99,s48,v99
Mvaa 40,36,152
U_ v100,s48,v100
U_ v101,s48,v101
Mvaa 32,224,188
U_ v102,s48,v102
U_ v103,s48,v103
Mvaa 32,36,148
Ln v9,36,s65,3200
Mvaa 24,224,184
S_ m0,s51,14080
C_ 179,102,103
C_ 178,100,101
Mvaa 24,36,144
C_ 177,98,99
C_ 176,96,97
Mvaa 16,224,180
v_pk_mul_f32 v[88:89], v[88:89], s[48:49]
v_pk_mul_f32 v[90:91], v[90:91], s[48:49]
Mvaa 16,36,140
Ln v9,36,s65,256
Ln v9,36,s65,3328
v_pk_mul_f32 v[92:93], v[92:93], s[48:49]
v_pk_mul_f32 v[94:95], v[94:95], s[48:49]
C_ 175,94,95
C_ 174,92,93
C_ 173,90,91
C_ 172,88,89
v_pk_mul_f32 v[80:81], v[80:81], s[48:49]
v_pk_mul_f32 v[82:83], v[82:83], s[48:49]
v_pk_mul_f32 v[84:85], v[84:85], s[48:49]
v_pk_mul_f32 v[86:87], v[86:87], s[48:49]
C_ 171,86,87
C_ 170,84,85
C_ 169,82,83
C_ 168,80,81
v_pk_mul_f32 v[72:73], v[72:73], s[48:49]
v_pk_mul_f32 v[74:75], v[74:75], s[48:49]
v_pk_mul_f32 v[76:77], v[76:77], s[48:49]
v_pk_mul_f32 v[78:79], v[78:79], s[48:49]
C_ 167,78,79
C_ 166,76,77
C_ 165,74,75
C_ 164,72,73
v_pk_mul_f32 v[40:41], v[40:41], s[48:49]
v_pk_mul_f32 v[42:43], v[42:43], s[48:49]
v_pk_mul_f32 v[44:45], v[44:45], s[48:49]
v_pk_mul_f32 v[46:47], v[46:47], s[48:49]
C_ 151,46,47
C_ 150,44,45
C_ 149,42,43
C_ 148,40,41
v_pk_mul_f32 v[32:33], v[32:33], s[48:49]
v_pk_mul_f32 v[34:35], v[34:35], s[48:49]
v_pk_mul_f32 v[36:37], v[36:37], s[48:49]
v_pk_mul_f32 v[38:39], v[38:39], s[48:49]
C_ 147,38,39
C_ 146,36,37
C_ 145,34,35
C_ 144,32,33
v_pk_mul_f32 v[24:25], v[24:25], s[48:49]
v_pk_mul_f32 v[26:27], v[26:27], s[48:49]
v_pk_mul_f32 v[28:29], v[28:29], s[48:49]
v_pk_mul_f32 v[30:31], v[30:31], s[48:49]
C_ 143,30,31
C_ 142,28,29
C_ 141,26,27
C_ 140,24,25
R_ %0,a244
v_xor_b32 v1, 64, %0
Dv 30,%0,43008
Dv 34,v1,43008
Dv 38,%0,44032
v_pk_mul_f32 v[64:65], v[64:65], s[48:49]
v_pk_mul_f32 v[66:67], v[66:67], s[48:49]
v_pk_mul_f32 v[68:69], v[68:69], s[48:49]
v_pk_mul_f32 v[70:71], v[70:71], s[48:49]
C_ 163,70,71
C_ 162,68,69
C_ 161,66,67
C_ 160,64,65
v_pk_mul_f32 v[56:57], v[56:57], s[48:49]
v_pk_mul_f32 v[58:59], v[58:59], s[48:49]
v_pk_mul_f32 v[60:61], v[60:61], s[48:49]
v_pk_mul_f32 v[62:63], v[62:63], s[48:49]
C_ 159,62,63
C_ 158,60,61
C_ 157,58,59
C_ 156,56,57
v_pk_mul_f32 v[48:49], v[48:49], s[48:49]
v_pk_mul_f32 v[50:51], v[50:51], s[48:49]
v_pk_mul_f32 v[52:53], v[52:53], s[48:49]
v_pk_mul_f32 v[54:55], v[54:55], s[48:49]
C_ 155,54,55
C_ 154,52,53
C_ 153,50,51
C_ 152,48,49
v_pk_mul_f32 v[16:17], v[16:17], s[48:49]
v_pk_mul_f32 v[18:19], v[18:19], s[48:49]
v_pk_mul_f32 v[20:21], v[20:21], s[48:49]
v_pk_mul_f32 v[22:23], v[22:23], s[48:49]
C_ 139,22,23
C_ 138,20,21
C_ 137,18,19
C_ 136,16,17
s_cmp_eq_u32 s58, 1
s_cbranch_scc1 .Latt1
s_cmp_eq_u32 s58, 2
s_cbranch_scc1 .Latt2
.Latt0:
s_cmp_eq_u32 s4, 0
s_cbranch_scc0 .Lhs_tok0
R_ v11,a241
buffer_load_dword a255, v11, s[92:95], 0 offen offset:0 sc1
.Lhs_tok0:
S_ s59,s59,768
W_ 2
Zvvv 96,30,136
Dv 42,v1,44032
R_ v2,a245
Zvvv 100,30,140
v_xor_b32 v3, 32, v2
v_xor_b32 v4, 64, v2
Zvvv 104,30,144
v_xor_b32 v5, 96, v2
Zvvv 108,30,148
s_cmp_eq_u32 s4, 0
s_cselect_b32 s67, 13, 0
Zvvv 112,30,152
W_ 2
s_or_b32 s67, s67, 8192
Mvvv 96,34,156
Dv 30,%0,45056
Mvvv 100,34,160
Mvvv 104,34,164
Mvvv 108,34,168
Mvvv 112,34,172
W_ 2
Mvvv 96,38,176
Dv 34,v1,45056
Mvvv 100,38,180
Mvvv 104,38,184
Mvvv 108,38,188
Mvvv 112,38,192
W_ 2
Mvvv 96,42,196
Dv 38,%0,36864
Mvvv 100,42,200
Mvvv 104,42,204
Mvvv 108,42,208
Mvvv 112,42,212
W_ 2
Mvvv 96,30,216
Dv 42,v1,36864
Mvvv 100,30,220
Mvvv 104,30,224
Mvvv 108,30,228
Mvvv 112,30,232
W_ 2
Mvvv 96,34,236
Dv 30,%0,37888
Mvvv 100,34,240
Mvvv 104,34,244
Mvvv 108,34,248
Mvvv 112,34,252
W_ 2
s_nop 1
v_max3_f32 v56, v96, v97, v98
v_max_f32 v56, v56, v99
Zvvv 76,38,136
s_memtime s[12:13]
Dv 34,v1,37888
v_max3_f32 v60, v100, v101, v102
Zvvv 80,38,140
s_memrealtime s[100:101]
v_max_f32 v60, v60, v103
Zvvv 84,38,144
v_max3_f32 v64, v104, v105, v106
v_max_f32 v64, v64, v107
Zvvv 88,38,148
v_max3_f32 v68, v108, v109, v110
v_max_f32 v68, v68, v111
Zvvv 92,38,152
W_ 2
v_max3_f32 v72, v112, v113, v114
v_max_f32 v72, v72, v115
Mvvv 76,42,156
Dv 38,%0,38912
Mvvv 80,42,160
Mvvv 84,42,164
Mvvv 88,42,168
Mvvv 92,42,172
W_ 2
Mvvv 76,30,176
Dv 42,v1,38912
Mvvv 80,30,180
Mvvv 84,30,184
Mvvv 88,30,188
Mvvv 92,30,192
s_waitcnt vmcnt(30) lgkmcnt(0)
s_barrier
Mvvv 76,34,196
S_ s65,s61,184320
Dv 30,%0,55296
Mvvv 76,38,216
S_ m0,s51,36864
s_sub_u32 s12, s12, s90
Mvvv 76,42,236
L_ v9,36,s65,0
Mvvv 80,34,200
s_sub_u32 s100, s100, s68
Mvvv 80,38,220
L_ v9,36,s65,3072
Mvvv 80,42,240
S_ m0,s51,37760
s_sub_u32 s101, s68, s98
Mvvv 84,34,204
L_ v9,36,s65,128
Mvvv 84,38,224
s_mul_i32 s12, s12, s101
v_max3_f32 v56, v56, v76, v77
v_max3_f32 v56, v56, v78, v79
Mvvv 84,42,244
L_ v9,36,s65,3200
Mvvv 88,34,208
S_ m0,s51,38656
v_max3_f32 v60, v60, v80, v81
s_sub_u32 s101, s90, s88
Mvvv 88,38,228
L_ v9,36,s65,256
Mvvv 88,42,248
s_mul_i32 s100, s100, s101
v_max3_f32 v60, v60, v82, v83
Mvvv 92,34,212
L_ v9,36,s65,3328
Mvvv 92,38,232
s_lshr_b32 s100, s100, 6
v_max3_f32 v64, v64, v84, v85
v_max3_f32 v64, v64, v86, v87
Mvvv 92,42,252
s_mul_i32 s100, s100, 47
s_cmp_gt_u32 s101, 4000
s_cselect_b32 s13, 2, 0
s_cmp_gt_u32 s12, s100
s_cselect_b32 s13, s13, 0
Dv 34,v1,55296
.p2align 3
v_mov_b32 v11, 0x3f803f80
v_permlane32_swap_b32 v56, v60
v_max3_f32 v68, v68, v88, v89
v_max3_f32 v68, v68, v90, v91
v_max_f32 v56, v56, v60
v_max3_f32 v72, v72, v92, v93
v_max3_f32 v72, v72, v94, v95
v_mov_b32 v73, v72
v_permlane32_swap_b32 v64, v68
v_max_f32 v64, v64, v68
v_permlane32_swap_b32 v72, v73
v_max_f32 v72, v72, v73
v_mov_b32 v73, v72
v_permlane16_swap_b32 v56, v64
v_max_f32 v56, v56, v64
v_permlane16_swap_b32 v72, v73
B_ v56,0xc0e00000,v56
v_max_f32 v72, v72, v73
v_mov_b32 v64, v56
B_ v72,0xc0e00000,v72
v_mov_b32 v73, v72
v_permlane16_swap_b32 v56, v64
v_mov_b32 v60, v56
v_mov_b32 v68, v64
v_mov_b64 v[74:75], v[72:73]
v_permlane32_swap_b32 v56, v60
v_permlane32_swap_b32 v64, v68
v_mov_b32 v57, v56
v_mov_b32 v61, v60
v_mov_b32 v65, v64
v_mov_b32 v69, v68
v_pk_add_f32 v[96:97], v[96:97], v[56:57]
v_pk_add_f32 v[98:99], v[98:99], v[56:57]
v_pk_add_f32 v[76:77], v[76:77], v[56:57]
v_pk_add_f32 v[78:79], v[78:79], v[56:57]
v_mov_b64 v[58:59], v[56:57]
v_pk_add_f32 v[100:101], v[100:101], v[60:61]
v_pk_add_f32 v[102:103], v[102:103], v[60:61]
v_pk_add_f32 v[80:81], v[80:81], v[60:61]
v_pk_add_f32 v[82:83], v[82:83], v[60:61]
v_mov_b64 v[62:63], v[60:61]
v_pk_add_f32 v[104:105], v[104:105], v[64:65]
v_pk_add_f32 v[106:107], v[106:107], v[64:65]
v_pk_add_f32 v[84:85], v[84:85], v[64:65]
v_pk_add_f32 v[86:87], v[86:87], v[64:65]
v_mov_b64 v[66:67], v[64:65]
v_pk_add_f32 v[108:109], v[108:109], v[68:69]
v_pk_add_f32 v[110:111], v[110:111], v[68:69]
v_pk_add_f32 v[88:89], v[88:89], v[68:69]
v_pk_add_f32 v[90:91], v[90:91], v[68:69]
v_mov_b64 v[70:71], v[68:69]
v_pk_add_f32 v[112:113], v[112:113], v[72:73]
v_pk_add_f32 v[114:115], v[114:115], v[72:73]
v_pk_add_f32 v[92:93], v[92:93], v[72:73]
v_pk_add_f32 v[94:95], v[94:95], v[72:73]
E_ 96
E_ 97
E_ 98
E_ 99
E_ 76
E_ 77
E_ 78
E_ 79
C_ 99,98,99
C_ 98,96,97
C_ 96,76,77
C_ 97,78,79
E_ 100
E_ 101
E_ 102
E_ 103
E_ 80
E_ 81
E_ 82
E_ 83
C_ 103,102,103
C_ 102,100,101
C_ 100,80,81
C_ 101,82,83
.Lstep0_1:
W_ 1
Nvvv 116,30,136,56
s_nop 0
Dv 38,%0,56320
Dv 42,v1,56320
W_ 2
Mvvv 116,34,156
E_ 104
Nvvv 120,30,140,60
E_ 105
Mvvv 120,34,160
E_ 106
Nvvv 124,30,144,64
E_ 107
Mvvv 124,34,164
E_ 84
Nvvv 128,30,148,68
E_ 85
Mvvv 128,34,168
E_ 86
Nvvv 132,30,152,72
E_ 87
Mvvv 132,34,172
W_ 0
C_ 107,106,107
C_ 106,104,105
Mvvv 132,38,192
s_nop 0
Dv 30,%0,57344
Dv 34,v1,57344
Mvvv 132,42,212
C_ 104,84,85
C_ 105,86,87
Mvvv 128,38,188
E_ 108
Mvvv 128,42,208
E_ 109
Mvvv 124,38,184
E_ 110
Mvvv 124,42,204
E_ 111
Mvvv 120,38,180
E_ 88
Mvvv 120,42,200
E_ 89
Mvvv 116,38,176
E_ 90
Mvvv 116,42,196
W_ 0
E_ 91
Mvvv 116,30,216
s_nop 0
Dv 38,%0,49152
Dv 42,v1,49152
Mvvv 116,34,236
C_ 111,110,111
C_ 110,108,109
Mvvv 120,30,220
C_ 108,88,89
C_ 109,90,91
Mvvv 120,34,240
E_ 112
Mvvv 124,30,224
E_ 113
Mvvv 124,34,244
E_ 114
Mvvv 128,30,228
E_ 115
Mvvv 128,34,248
E_ 92
Mvvv 132,30,232
E_ 93
Mvvv 132,34,252
W_ 0
E_ 94
Nvvv 76,38,136,56
s_nop 0
Dv 30,%0,50176
Dv 34,v1,50176
Mvvv 76,42,156
E_ 95
Nvvv 80,38,140,60
C_ 115,114,115
C_ 114,112,113
Mvvv 80,42,160
C_ 112,92,93
C_ 113,94,95
Nvvv 84,38,144,64
E_ 116
Mvvv 84,42,164
E_ 117
Nvvv 88,38,148,68
E_ 118
Mvvv 88,42,168
E_ 119
Nvvv 92,38,152,72
C_ 119,118,119
C_ 118,116,117
Mvvv 92,42,172
W_ 0
E_ 120
Mvvv 92,30,192
s_nop 0
Dv 38,%0,51200
Dv 42,v1,51200
Mvvv 92,34,212
X_ a180,v11
X_ a181,v11
Mvvv 88,30,188
X_ a182,v11
X_ a183,v11
Mvvv 88,34,208
E_ 121
Mvvv 84,30,184
E_ 122
Mvvv 84,34,204
E_ 123
Mvvv 80,30,180
C_ 123,122,123
C_ 122,120,121
Mvvv 80,34,200
E_ 124
Mvvv 76,30,176
E_ 125
Mvvv 76,34,196
S_ s65,s61,36864
E_ 126
W_ 1
Mvvv 76,38,216
S_ m0,s51,49152
E_ 127
W_ 0
Mvvv 76,42,236
s_waitcnt vmcnt(18) lgkmcnt(0)
s_barrier
C_ 127,126,127
Mvvv 80,38,220
Ln v7,32,s65,0
Mvvv 80,42,240
.Lpad_a0_0:
s_cbranch_execz .Lhop_a0_r
C_ 126,124,125
Mvvv 84,38,224
Ln v8,32,s65,3072
S_ m0,s51,50048
Mvvv 84,42,244
s_nop 0
Tv 30,v2,24576
Tv 32,v2,30720
Mvvv 88,38,228
Ln v7,32,s65,128
Mvvv 88,42,248
s_nop 0
Tv 34,v3,24576
Tv 36,v3,30720
Mvvv 92,38,232
Ln v8,32,s65,3200
S_ m0,s51,50944
Mvvv 92,42,252
E_ 128
Zaav 220,180,96
Ln v7,32,s65,256
Zaav 224,180,100
E_ 129
Zaav 228,180,104
Ln v8,32,s65,3328
Zaav 232,180,108
E_ 130
Zaav 236,180,112
W_ 0
E_ 131
Zavv 0,30,96
s_nop 0
Tv 38,v4,24576
Tv 40,v4,30720
Zavv 20,34,96
C_ 131,130,131
C_ 130,128,129
Zavv 24,34,100
s_nop 0
Tv 42,v5,24576
Tv 44,v5,30720
Zavv 4,30,100
E_ 132
Zavv 8,30,104
E_ 133
Zavv 28,34,104
R_ v46,a220
R_ v47,a224
Zavv 32,34,108
R_ v48,a228
R_ v49,a232
Zavv 12,30,108
R_ v50,a236
Zavv 16,30,112
E_ 134
Zavv 36,34,112
W_ 0
E_ 135
Zavv 56,38,112
s_nop 0
Tv 30,v2,25600
Tv 32,v2,31744
Zavv 76,42,112
C_ 135,134,135
C_ 134,132,133
Zavv 72,42,108
s_nop 0
Tv 34,v3,25600
Tv 36,v3,31744
Zavv 52,38,108
E_ 76
Zavv 48,38,104
E_ 77
Zavv 68,42,104
E_ 78
Zavv 64,42,100
E_ 79
Zavv 44,38,100
C_ 116,76,77
C_ 117,78,79
Zavv 40,38,96
E_ 80
Zavv 60,42,96
W_ 0
E_ 81
Zavv 80,30,96
s_nop 0
Tv 38,v4,25600
Tv 40,v4,31744
Zavv 100,34,96
E_ 82
Zavv 104,34,100
s_nop 0
Tv 42,v5,25600
Tv 44,v5,31744
Zavv 84,30,100
E_ 83
Zavv 88,30,104
C_ 120,80,81
C_ 121,82,83
Zavv 108,34,104
E_ 84
Zavv 112,34,108
E_ 85
Zavv 92,30,108
E_ 86
Zavv 96,30,112
E_ 87
Zavv 116,34,112
W_ 0
C_ 124,84,85
C_ 125,86,87
Zavv 136,38,112
s_nop 0
Tv 30,v2,26624
Tv 32,v2,32768
Zavv 156,42,112
E_ 88
Zavv 152,42,108
s_nop 0
Tv 34,v3,26624
Tv 36,v3,32768
Zavv 132,38,108
E_ 89
Zavv 128,38,104
E_ 90
Zavv 148,42,104
E_ 91
Zavv 144,42,100
C_ 128,88,89
C_ 129,90,91
Zavv 124,38,100
E_ 92
Zavv 120,38,96
E_ 93
Zavv 140,42,96
W_ 0
E_ 94
Zavv 160,30,96
s_nop 0
Tv 38,v4,26624
Tv 40,v4,32768
Zavv 180,34,96
E_ 95
Zavv 184,34,100
s_nop 0
Tv 42,v5,26624
Tv 44,v5,32768
Zavv 164,30,100
C_ 132,92,93
C_ 133,94,95
Zavv 168,30,104
v_mov_b32 v92, 0x3f803f80
v_mov_b32 v93, 0x3f803f80
Zavv 188,34,104
S_ s65,s61,24576
v_mov_b32 v94, 0x3f803f80
v_mov_b32 v95, 0x3f803f80
Zavv 192,34,108
S_ m0,s51,24576
Zavv 172,30,108
s_waitcnt vmcnt(18) lgkmcnt(0)
s_barrier
Zavv 176,30,112
L_ v9,36,s65,0
Zavv 196,34,112
Zavv 216,38,112
Dv 30,%0,6144
Zavv 236,42,112
Dv 34,v1,6144
Zavv 232,42,108
L_ v9,36,s65,3072
S_ m0,s51,25472
Zavv 212,38,108
Zavv 208,38,104
L_ v9,36,s65,128
Zavv 228,42,104
Zavv 224,42,100
L_ v9,36,s65,3200
S_ m0,s51,26368
Zavv 204,38,100
Zavv 200,38,96
L_ v9,36,s65,256
Zavv 220,42,96
.Lstep0_2:
Zvvv 76,92,116
Dv 38,%0,7168
Zvvv 80,92,120
Dv 42,v1,7168
Zvvv 84,92,124
Zvvv 88,92,128
Zvvv 92,92,132
W_ 2
Nvvv 96,30,136,56
Mvvv 96,34,156
Nvvv 100,30,140,60
s_nop 1
v_max3_f32 v10, v76, v80, v84
s_nop 0
v_max3_f32 v10, v10, v88, v92
Mvvv 100,34,160
v_cmp_lt_f32 vcc, 0x42000000, v10
s_cbranch_vccnz .Ltramp0_1
Nvvv 104,30,144,64
A_ v46,v46,v76
A_ v47,v47,v80
Mvvv 104,34,164
A_ v48,v48,v84
A_ v49,v49,v88
Nvvv 108,30,148,68
A_ v50,v50,v92
Mvvv 108,34,168
L_ v9,36,s65,3328
Nvvv 112,30,152,72
R_ v11,a248
Mvvv 112,34,172
W_ 0
v_min_u32 v11, 240, v11
Mvvv 112,38,192
Dv 30,%0,8192
v_lshlrev_b32 v11, 3, v11
Mvvv 112,42,212
Dv 34,v1,8192
Mvvv 108,38,188
buffer_load_dword a246, v11, s[44:47], s66 offen
Mvvv 108,42,208
Mvvv 104,38,184
Mvvv 104,42,204
Mvvv 100,38,180
Mvvv 100,42,200
Mvvv 96,38,176
Mvvv 96,42,196
W_ 0
Mvvv 96,30,216
Dv 38,%0,0
Mvvv 96,34,236
Dv 42,v1,0
Mvvv 100,30,220
Mvvv 100,34,240
Mvvv 104,30,224
Mvvv 104,34,244
Mvvv 108,30,228
Mvvv 108,34,248
s_nop 0
E_ 96
Mvvv 112,30,232
E_ 97
Mvvv 112,34,252
W_ 0
E_ 98
Nvvv 76,38,136,56
Dv 30,%0,1024
Mvvv 76,42,156
Dv 34,v1,1024
Nvvv 80,38,140,60
E_ 99
Mvvv 80,42,160
C_ 99,98,99
C_ 98,96,97
Nvvv 84,38,144,64
E_ 100
Mvvv 84,42,164
E_ 101
Nvvv 88,38,148,68
E_ 102
Mvvv 88,42,168
E_ 103
Nvvv 92,38,152,72
C_ 103,102,103
C_ 102,100,101
Mvvv 92,42,172
W_ 0
E_ 104
Mvvv 92,30,192
Dv 38,%0,2048
Mvvv 92,34,212
Dv 42,v1,2048
Mvvv 88,30,188
E_ 105
Mvvv 88,34,208
E_ 106
Mvvv 84,30,184
S_ s65,s61,49152
E_ 107
Mvvv 84,34,204
S_ m0,s51,0
C_ 107,106,107
C_ 106,104,105
Mvvv 80,30,180
s_waitcnt vmcnt(13) lgkmcnt(0)
s_barrier
s_cmp_eq_u32 s11, 2
s_cbranch_scc1 .Lxs2m
.Lxs2m_back:
Mvvv 80,34,200
Ln v7,32,s65,0
Mvvv 76,30,176
E_ 108
Mvvv 76,34,196
Ln v8,32,s65,3072
S_ m0,s51,896
Mvvv 76,38,216
Tv 30,v2,12288
Tv 32,v2,18432
Mvvv 76,42,236
Ln v7,32,s65,128
Mvvv 80,38,220
s_nop 0
Tv 34,v3,12288
Tv 36,v3,18432
Mvvv 80,42,240
Ln v8,32,s65,3200
S_ m0,s51,1792
Mvvv 84,38,224
E_ 109
Mvvv 84,42,244
Ln v7,32,s65,256
Mvvv 88,38,228
E_ 110
Mvvv 88,42,248
Ln v8,32,s65,3328
Mvvv 92,38,232
E_ 111
Mvvv 92,42,252
W_ 0
C_ 111,110,111
C_ 110,108,109
Mavv 0,30,116
s_nop 0
Tv 38,v4,12288
Tv 40,v4,18432
Mavv 20,34,116
E_ 112
Mavv 24,34,120
s_nop 0
Tv 42,v5,12288
Tv 44,v5,18432
Mavv 4,30,120
E_ 113
Mavv 8,30,124
E_ 114
Mavv 28,34,124
E_ 115
Mavv 32,34,128
C_ 115,114,115
C_ 114,112,113
Mavv 12,30,128
E_ 76
Mavv 16,30,132
E_ 77
Mavv 36,34,132
W_ 0
E_ 78
Mavv 56,38,132
s_nop 0
Tv 30,v2,13312
Tv 32,v2,19456
Mavv 76,42,132
E_ 79
Mavv 72,42,128
s_nop 0
Tv 34,v3,13312
Tv 36,v3,19456
Mavv 52,38,128
C_ 96,76,77
C_ 97,78,79
Mavv 48,38,124
E_ 80
Mavv 68,42,124
E_ 81
Mavv 64,42,120
E_ 82
Mavv 44,38,120
E_ 83
Mavv 40,38,116
C_ 100,80,81
C_ 101,82,83
Mavv 60,42,116
W_ 0
E_ 84
Mavv 80,30,116
s_nop 0
Tv 38,v4,13312
Tv 40,v4,19456
Mavv 100,34,116
E_ 85
Mavv 104,34,120
s_nop 0
Tv 42,v5,13312
Tv 44,v5,19456
Mavv 84,30,120
E_ 86
Mavv 88,30,124
E_ 87
Mavv 108,34,124
C_ 104,84,85
C_ 105,86,87
Mavv 112,34,128
E_ 88
Mavv 92,30,128
E_ 89
Mavv 96,30,132
E_ 90
Mavv 116,34,132
W_ 0
E_ 91
Mavv 136,38,132
s_nop 0
Tv 30,v2,14336
Tv 32,v2,20480
Mavv 156,42,132
C_ 108,88,89
C_ 109,90,91
Mavv 152,42,128
s_nop 0
Tv 34,v3,14336
Tv 36,v3,20480
Mavv 132,38,128
E_ 92
Mavv 128,38,124
E_ 93
Mavv 148,42,124
E_ 94
Mavv 144,42,120
E_ 95
Mavv 124,38,120
C_ 112,92,93
C_ 113,94,95
Mavv 120,38,116
v_mov_b32 v92, 0x3f803f80
v_mov_b32 v93, 0x3f803f80
Mavv 140,42,116
W_ 0
v_mov_b32 v94, 0x3f803f80
v_mov_b32 v95, 0x3f803f80
Mavv 160,30,116
s_nop 0
Tv 38,v4,14336
Tv 40,v4,20480
Mavv 180,34,116
Mavv 184,34,120
s_nop 0
Tv 42,v5,14336
Tv 44,v5,20480
Mavv 164,30,120
Mavv 168,30,124
Mavv 188,34,124
S_ s65,s61,36864
Mavv 192,34,128
S_ m0,s51,12288
Mavv 172,30,128
s_waitcnt vmcnt(13) lgkmcnt(0)
s_barrier
s_cmp_eq_u32 s11, 2
s_cbranch_scc1 .Lxs2e
.Lxs2e_back:
Mavv 176,30,132
L_ v9,36,s65,0
Mavv 196,34,132
Mavv 216,38,132
Dv 30,%0,55296
Mavv 236,42,132
Dv 34,v1,55296
Mavv 232,42,128
L_ v9,36,s65,3072
S_ m0,s51,13184
Mavv 212,38,128
Mavv 208,38,124
L_ v9,36,s65,128
Mavv 228,42,124
Mavv 224,42,120
L_ v9,36,s65,3200
S_ m0,s51,14080
Mavv 204,38,120
Mavv 200,38,116
L_ v9,36,s65,256
Mavv 220,42,116
s_bitcmp1_b32 s67, 0
s_cbranch_scc1 .Lhsd0_0
.Lhsd0_0_back:
.Lstep0_3:
Zvvv 76,92,96
Dv 38,%0,56320
Zvvv 80,92,100
Dv 42,v1,56320
Zvvv 84,92,104
Zvvv 88,92,108
Zvvv 92,92,112
W_ 2
Nvvv 116,30,136,56
Mvvv 116,34,156
Nvvv 120,30,140,60
s_nop 1
v_max3_f32 v10, v76, v80, v84
s_nop 0
v_max3_f32 v10, v10, v88, v92
Mvvv 120,34,160
v_cmp_lt_f32 vcc, 0x42000000, v10
s_cbranch_vccnz .Ltramp0_2
Nvvv 124,30,144,64
A_ v46,v46,v76
A_ v47,v47,v80
Mvvv 124,34,164
A_ v48,v48,v84
A_ v49,v49,v88
Nvvv 128,30,148,68
A_ v50,v50,v92
Mvvv 128,34,168
L_ v9,36,s65,3328
Nvvv 132,30,152,72
Mvvv 132,34,172
W_ 0
Mvvv 132,38,192
Dv 30,%0,57344
Mvvv 132,42,212
Dv 34,v1,57344
Mvvv 128,38,188
Mvvv 128,42,208
Mvvv 124,38,184
Mvvv 124,42,204
Mvvv 120,38,180
Mvvv 120,42,200
Mvvv 116,38,176
Mvvv 116,42,196
W_ 0
Mvvv 116,30,216
Dv 38,%0,49152
Mvvv 116,34,236
Dv 42,v1,49152
Mvvv 120,30,220
Mvvv 120,34,240
Mvvv 124,30,224
Mvvv 124,34,244
Mvvv 128,30,228
Mvvv 128,34,248
s_nop 0
E_ 116
Mvvv 132,30,232
E_ 117
Mvvv 132,34,252
W_ 0
E_ 118
Nvvv 76,38,136,56
Dv 30,%0,50176
Mvvv 76,42,156
Dv 34,v1,50176
Nvvv 80,38,140,60
E_ 119
Mvvv 80,42,160
C_ 119,118,119
C_ 118,116,117
Nvvv 84,38,144,64
E_ 120
Mvvv 84,42,164
E_ 121
Nvvv 88,38,148,68
E_ 122
Mvvv 88,42,168
E_ 123
Nvvv 92,38,152,72
C_ 123,122,123
C_ 122,120,121
Mvvv 92,42,172
W_ 0
E_ 124
Mvvv 92,30,192
Dv 38,%0,51200
Mvvv 92,34,212
Dv 42,v1,51200
Mvvv 88,30,188
E_ 125
Mvvv 88,34,208
E_ 126
Mvvv 84,30,184
S_ s65,s61,61440
E_ 127
Mvvv 84,34,204
S_ m0,s51,49152
C_ 127,126,127
C_ 126,124,125
Mvvv 80,30,180
s_waitcnt vmcnt(13) lgkmcnt(0)
s_barrier
s_cmp_eq_u32 s11, 2
s_cbranch_scc1 .Lxs3m
.Lxs3m_back:
Mvvv 80,34,200
Ln v7,32,s65,0
Mvvv 76,30,176
E_ 128
Mvvv 76,34,196
Ln v8,32,s65,3072
S_ m0,s51,50048
Mvvv 76,38,216
.Lpad_a0_1:
s_cbranch_execz .Lhop_a0_r
Tv 30,v2,24576
Tv 32,v2,30720
Mvvv 76,42,236
Ln v7,32,s65,128
Mvvv 80,38,220
s_nop 0
Tv 34,v3,24576
Tv 36,v3,30720
Mvvv 80,42,240
Ln v8,32,s65,3200
S_ m0,s51,50944
Mvvv 84,38,224
E_ 129
Mvvv 84,42,244
Ln v7,32,s65,256
Mvvv 88,38,228
E_ 130
Mvvv 88,42,248
Ln v8,32,s65,3328
Mvvv 92,38,232
E_ 131
Mvvv 92,42,252
W_ 0
C_ 131,130,131
C_ 130,128,129
Mavv 0,30,96
s_nop 0
Tv 38,v4,24576
Tv 40,v4,30720
Mavv 20,34,96
E_ 132
Mavv 24,34,100
s_nop 0
Tv 42,v5,24576
Tv 44,v5,30720
Mavv 4,30,100
E_ 133
Mavv 8,30,104
E_ 134
Mavv 28,34,104
E_ 135
Mavv 32,34,108
C_ 135,134,135
C_ 134,132,133
Mavv 12,30,108
E_ 76
Mavv 16,30,112
E_ 77
Mavv 36,34,112
W_ 0
E_ 78
Mavv 56,38,112
s_nop 0
Tv 30,v2,25600
Tv 32,v2,31744
Mavv 76,42,112
E_ 79
Mavv 72,42,108
s_nop 0
Tv 34,v3,25600
Tv 36,v3,31744
Mavv 52,38,108
C_ 116,76,77
C_ 117,78,79
Mavv 48,38,104
E_ 80
Mavv 68,42,104
E_ 81
Mavv 64,42,100
E_ 82
Mavv 44,38,100
E_ 83
Mavv 40,38,96
C_ 120,80,81
C_ 121,82,83
Mavv 60,42,96
W_ 0
E_ 84
Mavv 80,30,96
s_nop 0
Tv 38,v4,25600
Tv 40,v4,31744
Mavv 100,34,96
E_ 85
Mavv 104,34,100
s_nop 0
Tv 42,v5,25600
Tv 44,v5,31744
Mavv 84,30,100
E_ 86
Mavv 88,30,104
E_ 87
Mavv 108,34,104
C_ 124,84,85
C_ 125,86,87
Mavv 112,34,108
E_ 88
Mavv 92,30,108
E_ 89
Mavv 96,30,112
E_ 90
Mavv 116,34,112
W_ 0
E_ 91
Mavv 136,38,112
s_nop 0
Tv 30,v2,26624
Tv 32,v2,32768
Mavv 156,42,112
C_ 128,88,89
C_ 129,90,91
Mavv 152,42,108
s_nop 0
Tv 34,v3,26624
Tv 36,v3,32768
Mavv 132,38,108
E_ 92
Mavv 128,38,104
E_ 93
Mavv 148,42,104
E_ 94
Mavv 144,42,100
E_ 95
Mavv 124,38,100
C_ 132,92,93
C_ 133,94,95
Mavv 120,38,96
v_mov_b32 v92, 0x3f803f80
v_mov_b32 v93, 0x3f803f80
Mavv 140,42,96
W_ 0
v_mov_b32 v94, 0x3f803f80
v_mov_b32 v95, 0x3f803f80
Mavv 160,30,96
s_nop 0
Tv 38,v4,26624
Tv 40,v4,32768
Mavv 180,34,96
Mavv 184,34,100
s_nop 0
Tv 42,v5,26624
Tv 44,v5,32768
Mavv 164,30,100
Mavv 168,30,104
Mavv 188,34,104
S_ s65,s61,49152
Mavv 192,34,108
S_ m0,s51,24576
Mavv 172,30,108
s_waitcnt vmcnt(12) lgkmcnt(0)
s_barrier
s_cmp_eq_u32 s11, 2
s_cbranch_scc1 .Lxs3e
.Lxs3e_back:
Mavv 176,30,112
L_ v9,36,s65,0
Mavv 196,34,112
S_ s61,s61,24576
Mavv 216,38,112
s_lshr_b32 s67, s67, 1
Dv 30,%0,6144
Mavv 236,42,112
s_bitcmp1_b32 s67, 9
s_cselect_b32 s11, s13, s11
Dv 34,v1,6144
Mavv 232,42,108
L_ v9,36,s65,3072
S_ m0,s51,25472
Mavv 212,38,108
Mavv 208,38,104
L_ v9,36,s65,128
Mavv 228,42,104
Mavv 224,42,100
L_ v9,36,s65,3200
S_ m0,s51,26368
Mavv 204,38,100
Mavv 200,38,96
L_ v9,36,s65,256
Mavv 220,42,96
s_cmp_ge_u32 s67, 256
s_cbranch_scc1 .Lstep0_2
s_sub_u32 s61, s61, 147456
.Lstep0_14:
Zvvv 76,92,116
Dv 38,%0,7168
Zvvv 80,92,120
Dv 42,v1,7168
Zvvv 84,92,124
Zvvv 88,92,128
Zvvv 92,92,132
W_ 2
Nvvv 96,30,136,56
Mvvv 96,34,156
Nvvv 100,30,140,60
s_nop 1
v_max3_f32 v10, v76, v80, v84
s_nop 0
v_max3_f32 v10, v10, v88, v92
Mvvv 100,34,160
v_cmp_lt_f32 vcc, 0x42000000, v10
s_cbranch_vccnz .Ltramp0_13
Nvvv 104,30,144,64
A_ v46,v46,v76
A_ v47,v47,v80
Mvvv 104,34,164
A_ v48,v48,v84
A_ v49,v49,v88
Nvvv 108,30,148,68
A_ v50,v50,v92
Mvvv 108,34,168
L_ v9,36,s65,3328
Nvvv 112,30,152,72
R_ v20,a240
Mvvv 112,34,172
W_ 0
v_xor_b32 v21, 64, v20
Mvvv 112,38,192
S_ s65,s60,0
Dv 30,%0,8192
Mvvv 112,42,212
S_ m0,s50,0
Dv 34,v1,8192
Mvvv 108,38,188
L_ v20,24,s65,0
Mvvv 108,42,208
S_ s65,s60,10240
Mvvv 104,38,184
S_ m0,s50,1024
Mvvv 104,42,204
L_ v21,24,s65,0
Mvvv 100,38,180
S_ s65,s60,20480
Mvvv 100,42,200
S_ m0,s50,2048
Mvvv 96,38,176
L_ v20,24,s65,0
Mvvv 96,42,196
S_ s65,s60,30720
W_ 0
Mvvv 96,30,216
S_ m0,s50,3072
Dv 38,%0,0
Mvvv 96,34,236
Dv 42,v1,0
Mvvv 100,30,220
L_ v21,24,s65,0
Mvvv 100,34,240
S_ s65,s60,40960
Mvvv 104,30,224
S_ m0,s50,4096
Mvvv 104,34,244
L_ v20,24,s65,0
Mvvv 108,30,228
Mvvv 108,34,248
E_ 96
Mvvv 112,30,232
E_ 97
Mvvv 112,34,252
W_ 0
E_ 98
Nvvv 76,38,136,56
Dv 30,%0,1024
Mvvv 76,42,156
Dv 34,v1,1024
Nvvv 80,38,140,60
E_ 99
Mvvv 80,42,160
C_ 99,98,99
C_ 98,96,97
Nvvv 84,38,144,64
E_ 100
Mvvv 84,42,164
E_ 101
Nvvv 88,38,148,68
E_ 102
Mvvv 88,42,168
E_ 103
Nvvv 92,38,152,72
C_ 103,102,103
C_ 102,100,101
Mvvv 92,42,172
W_ 0
E_ 104
Mvvv 92,30,192
Dv 38,%0,2048
Mvvv 92,34,212
Dv 42,v1,2048
Mvvv 88,30,188
E_ 105
Mvvv 88,34,208
E_ 106
Mvvv 84,30,184
E_ 107
Mvvv 84,34,204
C_ 107,106,107
C_ 106,104,105
Mvvv 80,30,180
s_waitcnt vmcnt(17) lgkmcnt(0)
s_barrier
s_cmp_eq_u32 s11, 2
s_cbranch_scc1 .Lxs14m
.Lxs14m_back:
Mvvv 80,34,200
E_ 108
Mvvv 76,30,176
E_ 109
Mvvv 76,34,196
E_ 110
Mvvv 76,38,216
s_nop 0
Tv 30,v2,12288
Tv 32,v2,18432
Mvvv 76,42,236
E_ 111
Mvvv 80,38,220
s_nop 0
Tv 34,v3,12288
Tv 36,v3,18432
Mvvv 80,42,240
C_ 111,110,111
C_ 110,108,109
Mvvv 84,38,224
E_ 112
Mvvv 84,42,244
E_ 113
Mvvv 88,38,228
E_ 114
Mvvv 88,42,248
E_ 115
Mvvv 92,38,232
C_ 115,114,115
C_ 114,112,113
Mvvv 92,42,252
W_ 0
E_ 76
Mavv 0,30,116
s_nop 0
Tv 38,v4,12288
Tv 40,v4,18432
Mavv 20,34,116
E_ 77
Mavv 24,34,120
s_nop 0
Tv 42,v5,12288
Tv 44,v5,18432
Mavv 4,30,120
E_ 78
Mavv 8,30,124
E_ 79
Mavv 28,34,124
C_ 96,76,77
C_ 97,78,79
Mavv 32,34,128
E_ 80
Mavv 12,30,128
E_ 81
Mavv 16,30,132
E_ 82
Mavv 36,34,132
W_ 0
E_ 83
Mavv 56,38,132
s_nop 0
Tv 30,v2,13312
Tv 32,v2,19456
Mavv 76,42,132
C_ 100,80,81
C_ 101,82,83
Mavv 72,42,128
s_nop 0
Tv 34,v3,13312
Tv 36,v3,19456
Mavv 52,38,128
E_ 84
Mavv 48,38,124
E_ 85
Mavv 68,42,124
E_ 86
Mavv 64,42,120
E_ 87
Mavv 44,38,120
C_ 104,84,85
C_ 105,86,87
Mavv 40,38,116
E_ 88
Mavv 60,42,116
W_ 0
E_ 89
Mavv 80,30,116
s_nop 0
Tv 38,v4,13312
Tv 40,v4,19456
Mavv 100,34,116
E_ 90
Mavv 104,34,120
s_nop 0
Tv 42,v5,13312
Tv 44,v5,19456
Mavv 84,30,120
E_ 91
Mavv 88,30,124
)A"
R"A(C_ 108,88,89
C_ 109,90,91
Mavv 108,34,124
E_ 92
Mavv 112,34,128
E_ 93
Mavv 92,30,128
E_ 94
Mavv 96,30,132
E_ 95
Mavv 116,34,132
W_ 0
C_ 112,92,93
C_ 113,94,95
Mavv 136,38,132
s_nop 0
Tv 30,v2,14336
Tv 32,v2,20480
Mavv 156,42,132
v_mov_b32 v92, 0x3f803f80
v_mov_b32 v93, 0x3f803f80
Mavv 152,42,128
s_nop 0
Tv 34,v3,14336
Tv 36,v3,20480
Mavv 132,38,128
v_mov_b32 v94, 0x3f803f80
v_mov_b32 v95, 0x3f803f80
Mavv 128,38,124
Mavv 148,42,124
Mavv 144,42,120
Mavv 124,38,120
Mavv 120,38,116
Mavv 140,42,116
W_ 0
Mavv 160,30,116
s_nop 0
Tv 38,v4,14336
Tv 40,v4,20480
Mavv 180,34,116
Mavv 184,34,120
s_nop 0
Tv 42,v5,14336
Tv 44,v5,20480
Mavv 164,30,120
Mavv 168,30,124
Mavv 188,34,124
Mavv 192,34,128
Mavv 172,30,128
s_waitcnt vmcnt(11) lgkmcnt(0)
s_barrier
s_cmp_eq_u32 s11, 2
s_cbranch_scc1 .Lxs14e
.Lxs14e_back:
Mavv 176,30,132
S_ s65,s59,0
Mavv 196,34,132
S_ m0,s52,8192
Mavv 216,38,132
Dv 30,%0,55296
Mavv 236,42,132
Dv 34,v1,55296
Mavv 232,42,128
L_ v6,28,s65,0
Mavv 212,38,128
S_ s65,s59,98304
Mavv 208,38,124
S_ m0,s52,9216
Mavv 228,42,124
L_ v6,28,s65,0
Mavv 224,42,120
S_ s65,s59,196608
Mavv 204,38,120
S_ m0,s52,10240
Mavv 200,38,116
L_ v6,28,s65,0
Mavv 220,42,116
S_ s65,s59,294912
.Lstep0_15:
Zvvv 76,92,96
Dv 38,%0,56320
Zvvv 80,92,100
Dv 42,v1,56320
Zvvv 84,92,104
Zvvv 88,92,108
Zvvv 92,92,112
W_ 2
Nvvv 116,30,136,56
Mvvv 116,34,156
Nvvv 120,30,140,60
s_nop 1
v_max3_f32 v10, v76, v80, v84
s_nop 0
v_max3_f32 v10, v10, v88, v92
Mvvv 120,34,160
v_cmp_lt_f32 vcc, 0x42000000, v10
s_cbranch_vccnz .Ltramp0_14
Nvvv 124,30,144,64
S_ m0,s52,11264
A_ v46,v46,v76
A_ v47,v47,v80
Mvvv 124,34,164
A_ v48,v48,v84
A_ v49,v49,v88
Nvvv 128,30,148,68
A_ v50,v50,v92
Mvvv 128,34,168
L_ v6,28,s65,0
Nvvv 132,30,152,72
S_ s65,s59,0
Mvvv 132,34,172
S_ m0,s52,16256
W_ 0
Mvvv 132,38,192
Dv 30,%0,57344
Mvvv 132,42,212
Dv 34,v1,57344
Mvvv 128,38,188
L_ v6,28,s65,128
Mvvv 128,42,208
S_ s65,s59,98304
Mvvv 124,38,184
S_ m0,s52,17280
Mvvv 124,42,204
L_ v6,28,s65,128
Mvvv 120,38,180
S_ s65,s59,196608
Mvvv 120,42,200
S_ m0,s52,18304
Mvvv 116,38,176
L_ v6,28,s65,128
Mvvv 116,42,196
S_ s65,s59,294912
W_ 0
Mvvv 116,30,216
S_ m0,s52,19328
Dv 38,%0,49152
Mvvv 116,34,236
Dv 42,v1,49152
Mvvv 120,30,220
L_ v6,28,s65,128
Mvvv 120,34,240
S_ s65,s59,786432
Mvvv 124,30,224
S_ m0,s52,0
Mvvv 124,34,244
L_ v6,28,s65,0
Mvvv 128,30,228
S_ s65,s59,884736
Mvvv 128,34,248
S_ m0,s52,1024
E_ 116
Mvvv 132,30,232
L_ v6,28,s65,0
Mvvv 132,34,252
S_ s65,s59,983040
E_ 117
W_ 1
Nvvv 76,38,136,56
S_ m0,s52,2048
Dv 30,%0,50176
W_ 1
Mvvv 76,42,156
Dv 34,v1,50176
Nvvv 80,38,140,60
L_ v6,28,s65,0
Mvvv 80,42,160
S_ s65,s59,1081344
E_ 118
Nvvv 84,38,144,64
S_ m0,s52,3072
E_ 119
Mvvv 84,42,164
L_ v6,28,s65,0
Nvvv 88,38,148,68
S_ s65,s60,128
C_ 119,118,119
C_ 118,116,117
Mvvv 88,42,168
S_ m0,s50,20480
E_ 120
Nvvv 92,38,152,72
L_ v20,24,s65,0
Mvvv 92,42,172
S_ s65,s60,10368
E_ 121
W_ 1
Mvvv 92,30,192
S_ m0,s50,21504
Dv 38,%0,51200
W_ 1
Mvvv 92,34,212
Dv 42,v1,51200
Mvvv 88,30,188
L_ v21,24,s65,0
Mvvv 88,34,208
S_ s65,s60,20608
E_ 122
Mvvv 84,30,184
S_ m0,s50,22528
E_ 123
Mvvv 84,34,204
.Lpad_a0_2:
s_cbranch_execz .Lhop_a0_r
L_ v20,24,s65,0
Mvvv 80,30,180
s_waitcnt vmcnt(20) lgkmcnt(0)
s_barrier
s_cmp_eq_u32 s11, 2
s_cbranch_scc1 .Lxs15m
.Lxs15m_back:
S_ s65,s60,30848
Mvvv 80,34,200
S_ m0,s50,23552
C_ 123,122,123
C_ 122,120,121
Mvvv 76,30,176
L_ v21,24,s65,0
Mvvv 76,34,196
S_ s65,s60,41088
E_ 124
Mvvv 76,38,216
S_ m0,s50,24576
Tv 30,v2,24576
Tv 32,v2,30720
Mvvv 76,42,236
L_ v20,24,s65,0
Mvvv 80,38,220
Tv 34,v3,24576
Tv 36,v3,30720
Mvvv 80,42,240
E_ 125
Mvvv 84,38,224
E_ 126
Mvvv 84,42,244
E_ 127
Mvvv 88,38,228
C_ 127,126,127
C_ 126,124,125
Mvvv 88,42,248
E_ 128
Mvvv 92,38,232
E_ 129
Mvvv 92,42,252
W_ 0
E_ 130
Mavv 0,30,96
s_nop 0
Tv 38,v4,24576
Tv 40,v4,30720
Mavv 20,34,96
E_ 131
Mavv 24,34,100
s_nop 0
Tv 42,v5,24576
Tv 44,v5,30720
Mavv 4,30,100
C_ 131,130,131
C_ 130,128,129
Mavv 8,30,104
E_ 132
Mavv 28,34,104
E_ 133
Mavv 32,34,108
E_ 134
Mavv 12,30,108
E_ 135
Mavv 16,30,112
C_ 135,134,135
C_ 134,132,133
Mavv 36,34,112
W_ 0
E_ 76
Mavv 56,38,112
s_nop 0
Tv 30,v2,25600
Tv 32,v2,31744
Mavv 76,42,112
E_ 77
Mavv 72,42,108
s_nop 0
Tv 34,v3,25600
Tv 36,v3,31744
Mavv 52,38,108
E_ 78
Mavv 48,38,104
E_ 79
Mavv 68,42,104
C_ 116,76,77
C_ 117,78,79
Mavv 64,42,100
E_ 80
Mavv 44,38,100
E_ 81
Mavv 40,38,96
E_ 82
Mavv 60,42,96
W_ 0
E_ 83
Mavv 80,30,96
s_nop 0
Tv 38,v4,25600
Tv 40,v4,31744
Mavv 100,34,96
C_ 120,80,81
C_ 121,82,83
Mavv 104,34,100
s_nop 0
Tv 42,v5,25600
Tv 44,v5,31744
Mavv 84,30,100
E_ 84
Mavv 88,30,104
E_ 85
Mavv 108,34,104
E_ 86
Mavv 112,34,108
E_ 87
Mavv 92,30,108
C_ 124,84,85
C_ 125,86,87
Mavv 96,30,112
E_ 88
Mavv 116,34,112
W_ 0
E_ 89
Mavv 136,38,112
s_nop 0
Tv 30,v2,26624
Tv 32,v2,32768
Mavv 156,42,112
E_ 90
Mavv 152,42,108
s_nop 0
Tv 34,v3,26624
Tv 36,v3,32768
Mavv 132,38,108
E_ 91
Mavv 128,38,104
C_ 128,88,89
C_ 129,90,91
Mavv 148,42,104
E_ 92
Mavv 144,42,100
E_ 93
Mavv 124,38,100
E_ 94
Mavv 120,38,96
E_ 95
Mavv 140,42,96
W_ 0
C_ 132,92,93
C_ 133,94,95
Mavv 160,30,96
s_nop 0
Tv 38,v4,26624
Tv 40,v4,32768
Mavv 180,34,96
v_mov_b32 v92, 0x3f803f80
v_mov_b32 v93, 0x3f803f80
Mavv 184,34,100
s_nop 0
Tv 42,v5,26624
Tv 44,v5,32768
Mavv 164,30,100
v_mov_b32 v94, 0x3f803f80
v_mov_b32 v95, 0x3f803f80
Mavv 168,30,104
Mavv 188,34,104
Mavv 192,34,108
Mavv 172,30,108
s_waitcnt vmcnt(63) lgkmcnt(0)
s_barrier
s_cmp_eq_u32 s11, 2
s_cbranch_scc1 .Lxs15e
.Lxs15e_back:
Mavv 176,30,112
S_ s65,s59,0
Mavv 196,34,112
S_ m0,s52,24320
Mavv 216,38,112
s_nop 0
Tv 30,v2,36864
Tv 32,v2,43008
Zvvv 76,92,116
L_ v6,28,s65,256
Zvvv 80,92,120
S_ s65,s59,98304
Tv 34,v3,36864
Tv 36,v3,43008
Zvvv 84,92,124
S_ m0,s52,25344
Zvvv 88,92,128
L_ v6,28,s65,256
Zvvv 92,92,132
S_ s65,s59,196608
Mavv 236,42,112
S_ m0,s52,26368
Mavv 232,42,108
L_ v6,28,s65,256
Mavv 212,38,108
S_ s65,s59,294912
Mavv 208,38,104
S_ m0,s52,27392
v_max3_f32 v10, v76, v80, v84
Mavv 228,42,104
v_max3_f32 v10, v10, v88, v92
v_cmp_lt_f32 vcc, 0x42000000, v10
Mavv 224,42,100
L_ v6,28,s65,256
Mavv 204,38,100
Mavv 200,38,96
Mavv 220,42,96
s_cbranch_vccnz .Ltramp0_15
.Ljoin0:
W_ 2
Mavv 0,30,116
s_mov_b32 s58, 1
Tv 38,v4,36864
Tv 40,v4,43008
W_ 2
Mavv 20,34,116
S_ s61,s61,393216
R_ v12,a248
s_mov_b32 s85, 4026531840
Mavv 24,34,120
s_nop 0
Tv 42,v5,36864
Tv 44,v5,43008
Mavv 4,30,120
A_ v46,v46,v76
A_ v47,v47,v80
Mavv 8,30,124
A_ v48,v48,v84
A_ v49,v49,v88
Mavv 28,34,124
A_ v50,v50,v92
Mavv 32,34,128
v_rcp_f32 v46, v46
Mavv 12,30,128
v_rcp_f32 v47, v47
Mavv 16,30,132
v_rcp_f32 v48, v48
Mavv 36,34,132
W_ 0
v_rcp_f32 v49, v49
Mavv 56,38,132
s_nop 0
Tv 30,v2,37888
Tv 32,v2,44032
Mavv 76,42,132
v_rcp_f32 v50, v50
Mavv 72,42,128
s_nop 0
Tv 34,v3,37888
Tv 36,v3,44032
Mavv 52,38,128
R_ v96,a0
R_ v97,a1
Mavv 48,38,124
R_ v98,a2
R_ v99,a3
Mavv 68,42,124
R_ v100,a20
R_ v101,a21
Mavv 64,42,120
R_ v102,a22
R_ v103,a23
Mavv 44,38,120
U_ v96,v96,v46
U_ v97,v97,v46
Mavv 40,38,116
U_ v98,v98,v46
U_ v99,v99,v46
Mavv 60,42,116
W_ 0
U_ v100,v100,v46
U_ v101,v101,v46
Mavv 80,30,116
s_nop 0
Tv 38,v4,37888
Tv 40,v4,44032
Mavv 100,34,116
U_ v102,v102,v46
U_ v103,v103,v46
Mavv 104,34,120
s_nop 0
Tv 42,v5,37888
Tv 44,v5,44032
Mavv 84,30,120
C_ 76,96,97
C_ 77,98,99
Mavv 88,30,124
C_ 78,100,101
C_ 79,102,103
Mavv 108,34,124
X_ a0,v76
X_ a1,v77
Mavv 112,34,128
X_ a2,v78
X_ a3,v79
Mavv 92,30,128
R_ v104,a4
R_ v105,a5
Mavv 96,30,132
R_ v106,a6
R_ v107,a7
Mavv 116,34,132
W_ 0
R_ v108,a24
R_ v109,a25
Mavv 136,38,132
s_nop 0
Tv 30,v2,38912
Tv 32,v2,45056
Mavv 156,42,132
R_ v110,a26
R_ v111,a27
Mavv 152,42,128
s_nop 0
Tv 34,v3,38912
Tv 36,v3,45056
Mavv 132,38,128
U_ v104,v104,v47
U_ v105,v105,v47
Mavv 128,38,124
U_ v106,v106,v47
U_ v107,v107,v47
Mavv 148,42,124
U_ v108,v108,v47
U_ v109,v109,v47
Mavv 144,42,120
U_ v110,v110,v47
U_ v111,v111,v47
Mavv 124,38,120
C_ 80,104,105
C_ 81,106,107
Mavv 120,38,116
C_ 82,108,109
C_ 83,110,111
Mavv 140,42,116
W_ 0
X_ a4,v80
X_ a5,v81
Mavv 160,30,116
s_nop 0
Tv 38,v4,38912
Tv 40,v4,45056
Mavv 180,34,116
X_ a6,v82
X_ a7,v83
Mavv 184,34,120
s_nop 0
Tv 42,v5,38912
Tv 44,v5,45056
Mavv 164,30,120
R_ v96,a8
R_ v97,a9
Mavv 168,30,124
R_ v98,a10
R_ v99,a11
Mavv 188,34,124
R_ v100,a28
R_ v101,a29
Mavv 192,34,128
R_ v102,a30
R_ v103,a31
Mavv 172,30,128
U_ v96,v96,v48
U_ v97,v97,v48
Mavv 176,30,132
U_ v98,v98,v48
U_ v99,v99,v48
Mavv 196,34,132
W_ 0
U_ v100,v100,v48
U_ v101,v101,v48
Mavv 216,38,132
U_ v102,v102,v48
U_ v103,v103,v48
Mavv 236,42,132
C_ 84,96,97
C_ 85,98,99
Mavv 232,42,128
C_ 86,100,101
C_ 87,102,103
Mavv 212,38,128
X_ a8,v84
X_ a9,v85
Mavv 208,38,124
X_ a10,v86
X_ a11,v87
Mavv 228,42,124
Mavv 224,42,120
Mavv 204,38,120
Mavv 200,38,116
Mavv 220,42,116
.p2align 3
R_ v104,a12
R_ v105,a13
R_ v106,a14
R_ v107,a15
R_ v108,a32
R_ v109,a33
R_ v110,a34
R_ v111,a35
v_pk_mul_f32 v[104:105], v[104:105], v[48:49] op_sel:[0,1]
v_pk_mul_f32 v[106:107], v[106:107], v[48:49] op_sel:[0,1]
v_pk_mul_f32 v[108:109], v[108:109], v[48:49] op_sel:[0,1]
v_pk_mul_f32 v[110:111], v[110:111], v[48:49] op_sel:[0,1]
C_ 88,104,105
C_ 89,106,107
C_ 90,108,109
C_ 91,110,111
X_ a12,v88
X_ a13,v89
X_ a14,v90
X_ a15,v91
.p2align 3
R_ v96,a16
R_ v97,a17
R_ v98,a18
R_ v99,a19
R_ v100,a36
R_ v101,a37
R_ v102,a38
R_ v103,a39
v_pk_mul_f32 v[96:97], v[96:97], v[50:51] op_sel_hi:[1,0]
v_pk_mul_f32 v[98:99], v[98:99], v[50:51] op_sel_hi:[1,0]
v_pk_mul_f32 v[100:101], v[100:101], v[50:51] op_sel_hi:[1,0]
v_pk_mul_f32 v[102:103], v[102:103], v[50:51] op_sel_hi:[1,0]
C_ 92,96,97
C_ 93,98,99
C_ 94,100,101
C_ 95,102,103
X_ a16,v92
X_ a17,v93
X_ a18,v94
X_ a19,v95
.p2align 3
R_ v104,a40
R_ v105,a41
R_ v106,a42
R_ v107,a43
R_ v108,a60
R_ v109,a61
R_ v110,a62
R_ v111,a63
v_pk_mul_f32 v[104:105], v[104:105], v[46:47] op_sel_hi:[1,0]
v_pk_mul_f32 v[106:107], v[106:107], v[46:47] op_sel_hi:[1,0]
v_pk_mul_f32 v[108:109], v[108:109], v[46:47] op_sel_hi:[1,0]
v_pk_mul_f32 v[110:111], v[110:111], v[46:47] op_sel_hi:[1,0]
C_ 76,104,105
C_ 77,106,107
C_ 78,108,109
C_ 79,110,111
X_ a40,v76
X_ a41,v77
X_ a42,v78
X_ a43,v79
.p2align 3
R_ v96,a44
R_ v97,a45
R_ v98,a46
R_ v99,a47
R_ v100,a64
R_ v101,a65
R_ v102,a66
R_ v103,a67
v_pk_mul_f32 v[96:97], v[96:97], v[46:47] op_sel:[0,1]
v_pk_mul_f32 v[98:99], v[98:99], v[46:47] op_sel:[0,1]
v_pk_mul_f32 v[100:101], v[100:101], v[46:47] op_sel:[0,1]
v_pk_mul_f32 v[102:103], v[102:103], v[46:47] op_sel:[0,1]
C_ 80,96,97
C_ 81,98,99
C_ 82,100,101
C_ 83,102,103
X_ a44,v80
X_ a45,v81
X_ a46,v82
X_ a47,v83
.p2align 3
R_ v104,a48
R_ v105,a49
R_ v106,a50
R_ v107,a51
R_ v108,a68
R_ v109,a69
R_ v110,a70
R_ v111,a71
v_pk_mul_f32 v[104:105], v[104:105], v[48:49] op_sel_hi:[1,0]
v_pk_mul_f32 v[106:107], v[106:107], v[48:49] op_sel_hi:[1,0]
v_pk_mul_f32 v[108:109], v[108:109], v[48:49] op_sel_hi:[1,0]
v_pk_mul_f32 v[110:111], v[110:111], v[48:49] op_sel_hi:[1,0]
C_ 84,104,105
C_ 85,106,107
C_ 86,108,109
C_ 87,110,111
X_ a48,v84
X_ a49,v85
X_ a50,v86
X_ a51,v87
.p2align 3
R_ v96,a52
R_ v97,a53
R_ v98,a54
R_ v99,a55
R_ v100,a72
R_ v101,a73
R_ v102,a74
R_ v103,a75
v_pk_mul_f32 v[96:97], v[96:97], v[48:49] op_sel:[0,1]
v_pk_mul_f32 v[98:99], v[98:99], v[48:49] op_sel:[0,1]
v_pk_mul_f32 v[100:101], v[100:101], v[48:49] op_sel:[0,1]
v_pk_mul_f32 v[102:103], v[102:103], v[48:49] op_sel:[0,1]
C_ 88,96,97
C_ 89,98,99
C_ 90,100,101
C_ 91,102,103
X_ a52,v88
X_ a53,v89
X_ a54,v90
X_ a55,v91
.p2align 3
R_ v104,a56
R_ v105,a57
R_ v106,a58
R_ v107,a59
R_ v108,a76
R_ v109,a77
R_ v110,a78
R_ v111,a79
v_pk_mul_f32 v[104:105], v[104:105], v[50:51] op_sel_hi:[1,0]
v_pk_mul_f32 v[106:107], v[106:107], v[50:51] op_sel_hi:[1,0]
v_pk_mul_f32 v[108:109], v[108:109], v[50:51] op_sel_hi:[1,0]
v_pk_mul_f32 v[110:111], v[110:111], v[50:51] op_sel_hi:[1,0]
C_ 92,104,105
C_ 93,106,107
C_ 94,108,109
C_ 95,110,111
X_ a56,v92
X_ a57,v93
X_ a58,v94
X_ a59,v95
.p2align 3
R_ v96,a80
R_ v97,a81
R_ v98,a82
R_ v99,a83
R_ v100,a100
R_ v101,a101
R_ v102,a102
R_ v103,a103
v_pk_mul_f32 v[96:97], v[96:97], v[46:47] op_sel_hi:[1,0]
v_pk_mul_f32 v[98:99], v[98:99], v[46:47] op_sel_hi:[1,0]
v_pk_mul_f32 v[100:101], v[100:101], v[46:47] op_sel_hi:[1,0]
v_pk_mul_f32 v[102:103], v[102:103], v[46:47] op_sel_hi:[1,0]
C_ 76,96,97
C_ 77,98,99
C_ 78,100,101
C_ 79,102,103
X_ a80,v76
X_ a81,v77
X_ a82,v78
X_ a83,v79
.p2align 3
R_ v104,a84
R_ v105,a85
R_ v106,a86
R_ v107,a87
R_ v108,a104
R_ v109,a105
R_ v110,a106
R_ v111,a107
v_pk_mul_f32 v[104:105], v[104:105], v[46:47] op_sel:[0,1]
v_pk_mul_f32 v[106:107], v[106:107], v[46:47] op_sel:[0,1]
v_pk_mul_f32 v[108:109], v[108:109], v[46:47] op_sel:[0,1]
v_pk_mul_f32 v[110:111], v[110:111], v[46:47] op_sel:[0,1]
C_ 80,104,105
C_ 81,106,107
C_ 82,108,109
.Lpad_a0_3:
s_cbranch_execz .Lhop_a0_r
C_ 83,110,111
X_ a84,v80
X_ a85,v81
X_ a86,v82
X_ a87,v83
.p2align 3
R_ v96,a88
R_ v97,a89
R_ v98,a90
R_ v99,a91
R_ v100,a108
R_ v101,a109
R_ v102,a110
R_ v103,a111
v_pk_mul_f32 v[96:97], v[96:97], v[48:49] op_sel_hi:[1,0]
v_pk_mul_f32 v[98:99], v[98:99], v[48:49] op_sel_hi:[1,0]
v_pk_mul_f32 v[100:101], v[100:101], v[48:49] op_sel_hi:[1,0]
v_pk_mul_f32 v[102:103], v[102:103], v[48:49] op_sel_hi:[1,0]
C_ 84,96,97
C_ 85,98,99
C_ 86,100,101
C_ 87,102,103
X_ a88,v84
X_ a89,v85
X_ a90,v86
X_ a91,v87
.p2align 3
R_ v104,a92
R_ v105,a93
R_ v106,a94
R_ v107,a95
R_ v108,a112
R_ v109,a113
R_ v110,a114
R_ v111,a115
v_pk_mul_f32 v[104:105], v[104:105], v[48:49] op_sel:[0,1]
v_pk_mul_f32 v[106:107], v[106:107], v[48:49] op_sel:[0,1]
v_pk_mul_f32 v[108:109], v[108:109], v[48:49] op_sel:[0,1]
v_pk_mul_f32 v[110:111], v[110:111], v[48:49] op_sel:[0,1]
C_ 88,104,105
C_ 89,106,107
C_ 90,108,109
C_ 91,110,111
X_ a92,v88
X_ a93,v89
X_ a94,v90
X_ a95,v91
.p2align 3
R_ v96,a96
R_ v97,a97
R_ v98,a98
R_ v99,a99
R_ v100,a116
R_ v101,a117
R_ v102,a118
R_ v103,a119
v_pk_mul_f32 v[96:97], v[96:97], v[50:51] op_sel_hi:[1,0]
v_pk_mul_f32 v[98:99], v[98:99], v[50:51] op_sel_hi:[1,0]
v_pk_mul_f32 v[100:101], v[100:101], v[50:51] op_sel_hi:[1,0]
v_pk_mul_f32 v[102:103], v[102:103], v[50:51] op_sel_hi:[1,0]
C_ 92,96,97
C_ 93,98,99
C_ 94,100,101
C_ 95,102,103
X_ a96,v92
X_ a97,v93
X_ a98,v94
X_ a99,v95
.p2align 3
R_ v104,a120
R_ v105,a121
R_ v106,a122
R_ v107,a123
R_ v108,a140
R_ v109,a141
R_ v110,a142
R_ v111,a143
v_pk_mul_f32 v[104:105], v[104:105], v[46:47] op_sel_hi:[1,0]
v_pk_mul_f32 v[106:107], v[106:107], v[46:47] op_sel_hi:[1,0]
v_pk_mul_f32 v[108:109], v[108:109], v[46:47] op_sel_hi:[1,0]
v_pk_mul_f32 v[110:111], v[110:111], v[46:47] op_sel_hi:[1,0]
C_ 76,104,105
C_ 77,106,107
C_ 78,108,109
C_ 79,110,111
X_ a120,v76
X_ a121,v77
X_ a122,v78
X_ a123,v79
.p2align 3
R_ v96,a124
R_ v97,a125
R_ v98,a126
R_ v99,a127
R_ v100,a144
R_ v101,a145
R_ v102,a146
R_ v103,a147
v_pk_mul_f32 v[96:97], v[96:97], v[46:47] op_sel:[0,1]
v_pk_mul_f32 v[98:99], v[98:99], v[46:47] op_sel:[0,1]
v_pk_mul_f32 v[100:101], v[100:101], v[46:47] op_sel:[0,1]
v_pk_mul_f32 v[102:103], v[102:103], v[46:47] op_sel:[0,1]
C_ 80,96,97
C_ 81,98,99
C_ 82,100,101
C_ 83,102,103
X_ a124,v80
X_ a125,v81
X_ a126,v82
X_ a127,v83
.p2align 3
R_ v104,a128
R_ v105,a129
R_ v106,a130
R_ v107,a131
R_ v108,a148
R_ v109,a149
R_ v110,a150
R_ v111,a151
v_pk_mul_f32 v[104:105], v[104:105], v[48:49] op_sel_hi:[1,0]
v_pk_mul_f32 v[106:107], v[106:107], v[48:49] op_sel_hi:[1,0]
v_pk_mul_f32 v[108:109], v[108:109], v[48:49] op_sel_hi:[1,0]
v_pk_mul_f32 v[110:111], v[110:111], v[48:49] op_sel_hi:[1,0]
C_ 84,104,105
C_ 85,106,107
C_ 86,108,109
C_ 87,110,111
X_ a128,v84
X_ a129,v85
X_ a130,v86
X_ a131,v87
.p2align 3
R_ v96,a132
R_ v97,a133
R_ v98,a134
R_ v99,a135
R_ v100,a152
R_ v101,a153
R_ v102,a154
R_ v103,a155
v_pk_mul_f32 v[96:97], v[96:97], v[48:49] op_sel:[0,1]
v_pk_mul_f32 v[98:99], v[98:99], v[48:49] op_sel:[0,1]
v_pk_mul_f32 v[100:101], v[100:101], v[48:49] op_sel:[0,1]
v_pk_mul_f32 v[102:103], v[102:103], v[48:49] op_sel:[0,1]
C_ 88,96,97
C_ 89,98,99
C_ 90,100,101
C_ 91,102,103
X_ a132,v88
X_ a133,v89
X_ a134,v90
X_ a135,v91
.p2align 3
R_ v104,a136
R_ v105,a137
R_ v106,a138
R_ v107,a139
R_ v108,a156
R_ v109,a157
R_ v110,a158
R_ v111,a159
v_pk_mul_f32 v[104:105], v[104:105], v[50:51] op_sel_hi:[1,0]
v_pk_mul_f32 v[106:107], v[106:107], v[50:51] op_sel_hi:[1,0]
v_pk_mul_f32 v[108:109], v[108:109], v[50:51] op_sel_hi:[1,0]
v_pk_mul_f32 v[110:111], v[110:111], v[50:51] op_sel_hi:[1,0]
C_ 92,104,105
C_ 93,106,107
C_ 94,108,109
C_ 95,110,111
X_ a136,v92
X_ a137,v93
X_ a138,v94
X_ a139,v95
.p2align 3
R_ v96,a160
R_ v97,a161
R_ v98,a162
R_ v99,a163
R_ v100,a180
R_ v101,a181
R_ v102,a182
R_ v103,a183
v_pk_mul_f32 v[96:97], v[96:97], v[46:47] op_sel_hi:[1,0]
v_pk_mul_f32 v[98:99], v[98:99], v[46:47] op_sel_hi:[1,0]
v_pk_mul_f32 v[100:101], v[100:101], v[46:47] op_sel_hi:[1,0]
v_pk_mul_f32 v[102:103], v[102:103], v[46:47] op_sel_hi:[1,0]
C_ 76,96,97
C_ 77,98,99
C_ 78,100,101
C_ 79,102,103
X_ a160,v76
X_ a161,v77
X_ a162,v78
X_ a163,v79
.p2align 3
R_ v104,a164
R_ v105,a165
R_ v106,a166
R_ v107,a167
R_ v108,a184
R_ v109,a185
R_ v110,a186
R_ v111,a187
v_pk_mul_f32 v[104:105], v[104:105], v[46:47] op_sel:[0,1]
v_pk_mul_f32 v[106:107], v[106:107], v[46:47] op_sel:[0,1]
v_pk_mul_f32 v[108:109], v[108:109], v[46:47] op_sel:[0,1]
v_pk_mul_f32 v[110:111], v[110:111], v[46:47] op_sel:[0,1]
C_ 80,104,105
C_ 81,106,107
C_ 82,108,109
C_ 83,110,111
X_ a164,v80
X_ a165,v81
X_ a166,v82
X_ a167,v83
.p2align 3
R_ v96,a168
R_ v97,a169
R_ v98,a170
R_ v99,a171
R_ v100,a188
R_ v101,a189
R_ v102,a190
R_ v103,a191
v_pk_mul_f32 v[96:97], v[96:97], v[48:49] op_sel_hi:[1,0]
v_pk_mul_f32 v[98:99], v[98:99], v[48:49] op_sel_hi:[1,0]
v_pk_mul_f32 v[100:101], v[100:101], v[48:49] op_sel_hi:[1,0]
v_pk_mul_f32 v[102:103], v[102:103], v[48:49] op_sel_hi:[1,0]
C_ 84,96,97
C_ 85,98,99
C_ 86,100,101
C_ 87,102,103
X_ a168,v84
X_ a169,v85
X_ a170,v86
X_ a171,v87
.p2align 3
R_ v104,a172
R_ v105,a173
R_ v106,a174
R_ v107,a175
R_ v108,a192
R_ v109,a193
R_ v110,a194
R_ v111,a195
v_pk_mul_f32 v[104:105], v[104:105], v[48:49] op_sel:[0,1]
v_pk_mul_f32 v[106:107], v[106:107], v[48:49] op_sel:[0,1]
v_pk_mul_f32 v[108:109], v[108:109], v[48:49] op_sel:[0,1]
v_pk_mul_f32 v[110:111], v[110:111], v[48:49] op_sel:[0,1]
C_ 88,104,105
C_ 89,106,107
C_ 90,108,109
C_ 91,110,111
X_ a172,v88
X_ a173,v89
X_ a174,v90
X_ a175,v91
.p2align 3
R_ v96,a176
R_ v97,a177
R_ v98,a178
R_ v99,a179
R_ v100,a196
R_ v101,a197
R_ v102,a198
R_ v103,a199
v_pk_mul_f32 v[96:97], v[96:97], v[50:51] op_sel_hi:[1,0]
v_pk_mul_f32 v[98:99], v[98:99], v[50:51] op_sel_hi:[1,0]
v_pk_mul_f32 v[100:101], v[100:101], v[50:51] op_sel_hi:[1,0]
v_pk_mul_f32 v[102:103], v[102:103], v[50:51] op_sel_hi:[1,0]
C_ 92,96,97
C_ 93,98,99
C_ 94,100,101
C_ 95,102,103
X_ a176,v92
X_ a177,v93
X_ a178,v94
X_ a179,v95
.p2align 3
R_ v104,a200
R_ v105,a201
R_ v106,a202
R_ v107,a203
R_ v108,a220
R_ v109,a221
R_ v110,a222
R_ v111,a223
v_pk_mul_f32 v[104:105], v[104:105], v[46:47] op_sel_hi:[1,0]
v_pk_mul_f32 v[106:107], v[106:107], v[46:47] op_sel_hi:[1,0]
v_pk_mul_f32 v[108:109], v[108:109], v[46:47] op_sel_hi:[1,0]
v_pk_mul_f32 v[110:111], v[110:111], v[46:47] op_sel_hi:[1,0]
C_ 76,104,105
C_ 77,106,107
C_ 78,108,109
C_ 79,110,111
X_ a200,v76
X_ a201,v77
X_ a202,v78
X_ a203,v79
.p2align 3
R_ v96,a204
R_ v97,a205
R_ v98,a206
R_ v99,a207
R_ v100,a224
R_ v101,a225
R_ v102,a226
R_ v103,a227
v_pk_mul_f32 v[96:97], v[96:97], v[46:47] op_sel:[0,1]
v_pk_mul_f32 v[98:99], v[98:99], v[46:47] op_sel:[0,1]
v_pk_mul_f32 v[100:101], v[100:101], v[46:47] op_sel:[0,1]
v_pk_mul_f32 v[102:103], v[102:103], v[46:47] op_sel:[0,1]
C_ 80,96,97
C_ 81,98,99
C_ 82,100,101
C_ 83,102,103
X_ a204,v80
X_ a205,v81
X_ a206,v82
X_ a207,v83
v_mov_b32 v4, v20
v_mov_b32 v5, v21
R_ %0,a242
R_ v2,a243
v_xor_b32 v1, 64, %0
v_xor_b32 v3, 64, v2
s_waitcnt vmcnt(0) lgkmcnt(0)
s_barrier
Ta 20,%0,8192
Ta 22,%0,8704
Ta 24,%0,12288
Ta 26,%0,12800
Da 60,v2,0
Da 100,v3,0
Da 64,v2,2048
Da 104,v3,2048
Da 68,v2,4096
Da 108,v3,4096
Da 72,v2,6144
Da 112,v3,6144
Da 76,v2,8192
Da 116,v3,8192
Ta 28,%0,8200
Ta 30,%0,8712
Ta 32,%0,12296
Ta 34,%0,12808
.p2align 3
R_ v104,a208
R_ v105,a209
R_ v106,a210
R_ v107,a211
R_ v108,a228
R_ v109,a229
R_ v110,a230
R_ v111,a231
v_pk_mul_f32 v[104:105], v[104:105], v[48:49] op_sel_hi:[1,0]
v_pk_mul_f32 v[106:107], v[106:107], v[48:49] op_sel_hi:[1,0]
v_pk_mul_f32 v[108:109], v[108:109], v[48:49] op_sel_hi:[1,0]
v_pk_mul_f32 v[110:111], v[110:111], v[48:49] op_sel_hi:[1,0]
C_ 84,104,105
C_ 85,106,107
C_ 86,108,109
C_ 87,110,111
X_ a208,v84
X_ a209,v85
X_ a210,v86
X_ a211,v87
.p2align 3
R_ v96,a212
R_ v97,a213
R_ v98,a214
R_ v99,a215
R_ v100,a232
R_ v101,a233
R_ v102,a234
R_ v103,a235
v_pk_mul_f32 v[96:97], v[96:97], v[48:49] op_sel:[0,1]
v_pk_mul_f32 v[98:99], v[98:99], v[48:49] op_sel:[0,1]
v_pk_mul_f32 v[100:101], v[100:101], v[48:49] op_sel:[0,1]
v_pk_mul_f32 v[102:103], v[102:103], v[48:49] op_sel:[0,1]
C_ 88,96,97
C_ 89,98,99
C_ 90,100,101
C_ 91,102,103
X_ a212,v88
X_ a213,v89
X_ a214,v90
X_ a215,v91
.p2align 3
R_ v104,a216
R_ v105,a217
R_ v106,a218
R_ v107,a219
R_ v108,a236
R_ v109,a237
R_ v110,a238
R_ v111,a239
v_pk_mul_f32 v[104:105], v[104:105], v[50:51] op_sel_hi:[1,0]
v_pk_mul_f32 v[106:107], v[106:107], v[50:51] op_sel_hi:[1,0]
v_pk_mul_f32 v[108:109], v[108:109], v[50:51] op_sel_hi:[1,0]
v_pk_mul_f32 v[110:111], v[110:111], v[50:51] op_sel_hi:[1,0]
C_ 92,104,105
C_ 93,106,107
C_ 94,108,109
C_ 95,110,111
X_ a216,v92
X_ a217,v93
X_ a218,v94
X_ a219,v95
s_branch .Lq
.Ltramp0_1:
s_mov_b32 s63, 1
s_branch .Lslow1
.Ltramp0_2:
s_mov_b32 s63, 2
s_branch .Lslow0
.Ltramp0_13:
s_mov_b32 s63, 13
s_branch .Lslow1
.Ltramp0_14:
s_mov_b32 s63, 14
s_branch .Lslow0
.Ltramp0_15:
s_mov_b32 s63, 15
s_branch .Lslow1
.Lstep0_16:
Zvvv 76,92,116
Zvvv 80,92,120
Zvvv 84,92,124
Zvvv 88,92,128
Zvvv 92,92,132
s_nop 5
v_max3_f32 v10, v76, v80, v84
s_nop 0
v_max3_f32 v10, v10, v88, v92
v_cmp_lt_f32 vcc, 0x42000000, v10
s_cbranch_vccnz .Ltramp0_15
s_branch .Ljoin0
.Ltramp1_1:
s_mov_b32 s63, 1
s_branch .Lslow1
.Ltramp1_2:
s_mov_b32 s63, 2
s_branch .Lslow0
.Ltramp1_3:
s_mov_b32 s63, 3
s_branch .Lslow1
.Ltramp1_4:
s_mov_b32 s63, 4
s_branch .Lslow0
.Ltramp1_5:
s_mov_b32 s63, 5
s_branch .Lslow1
.Ltramp1_6:
s_mov_b32 s63, 6
s_branch .Lslow0
.Ltramp1_7:
s_mov_b32 s63, 7
s_branch .Lslow1
.Ltramp1_8:
s_mov_b32 s63, 8
s_branch .Lslow0
.Ltramp1_9:
s_mov_b32 s63, 9
s_branch .Lslow1
.Ltramp1_10:
s_mov_b32 s63, 10
s_branch .Lslow0
.Ltramp1_11:
s_mov_b32 s63, 11
s_branch .Lslow1
.Ltramp1_12:
s_mov_b32 s63, 12
s_branch .Lslow0
.Ltramp1_13:
s_mov_b32 s63, 13
s_branch .Lslow1
.Ltramp1_14:
s_mov_b32 s63, 14
s_branch .Lslow0
.Ltramp1_15:
s_mov_b32 s63, 15
s_branch .Lslow1
.Lstep1_16:
Zvvv 76,92,116
Zvvv 80,92,120
Zvvv 84,92,124
Zvvv 88,92,128
Zvvv 92,92,132
s_nop 5
v_max3_f32 v10, v76, v80, v84
s_nop 0
v_max3_f32 v10, v10, v88, v92
v_cmp_lt_f32 vcc, 0x42000000, v10
s_cbranch_vccnz .Ltramp1_15
s_branch .Ljoin1
.Ltramp2_1:
s_mov_b32 s63, 1
s_branch .Lslow1
.Ltramp2_2:
s_mov_b32 s63, 2
s_branch .Lslow0
.Ltramp2_3:
s_mov_b32 s63, 3
s_branch .Lslow1
.Ltramp2_4:
s_mov_b32 s63, 4
s_branch .Lslow0
.Ltramp2_5:
s_mov_b32 s63, 5
s_branch .Lslow1
.Ltramp2_6:
s_mov_b32 s63, 6
s_branch .Lslow0
.Ltramp2_7:
s_mov_b32 s63, 7
s_branch .Lslow1
.Ltramp2_8:
s_mov_b32 s63, 8
s_branch .Lslow0
.Ltramp2_9:
s_mov_b32 s63, 9
s_branch .Lslow1
.Ltramp2_10:
s_mov_b32 s63, 10
s_branch .Lslow0
.Ltramp2_11:
s_mov_b32 s63, 11
s_branch .Lslow1
.Ltramp2_12:
s_mov_b32 s63, 12
s_branch .Lslow0
.Ltramp2_13:
s_mov_b32 s63, 13
s_branch .Lslow1
.Ltramp2_14:
s_mov_b32 s63, 14
s_branch .Lslow0
.Ltramp2_15:
s_mov_b32 s63, 15
s_branch .Lslow1
.Lstep2_16:
Zvvv 76,92,116
Zvvv 80,92,120
Zvvv 84,92,124
Zvvv 88,92,128
Zvvv 92,92,132
s_nop 5
v_max3_f32 v10, v76, v80, v84
s_nop 0
v_max3_f32 v10, v10, v88, v92
v_cmp_lt_f32 vcc, 0x42000000, v10
s_cbranch_vccnz .Ltramp2_15
s_branch .Ljoin2
.Lslow0:
W_ 0
s_nop 7
s_nop 7
s_and_b32 s64, s61, 4294959104
s_mul_i32 s53, s63, 12288
S_ s64,s64,s53
R_ v11,a248
v_and_b32 v10, 0xf0, v11
v_mul_u32_u24 v10, 24, v10
v_lshrrev_b32 v11, 8, v11
v_lshl_add_u32 v10, v11, 4, v10
S_ s53,s64,6144
buffer_load_dwordx4 v[116:119], v10, s[32:35], s53 offen offset:0
S_ s53,s64,6144
buffer_load_dwordx4 v[120:123], v10, s[32:35], s53 offen offset:64
S_ s53,s64,6144
buffer_load_dwordx4 v[124:127], v10, s[32:35], s53 offen offset:128
S_ s53,s64,6144
buffer_load_dwordx4 v[128:131], v10, s[32:35], s53 offen offset:192
s_waitcnt vmcnt(0)
Nvvv 96,116,136,56
Nvvv 100,116,140,60
Nvvv 104,116,144,64
Nvvv 108,116,148,68
Nvvv 112,116,152,72
Mvvv 96,120,156
Mvvv 100,120,160
Mvvv 104,120,164
Mvvv 108,120,168
Mvvv 112,120,172
Mvvv 96,124,176
Mvvv 100,124,180
Mvvv 104,124,184
Mvvv 108,124,188
Mvvv 112,124,192
Mvvv 96,128,196
Mvvv 100,128,200
Mvvv 104,128,204
Mvvv 108,128,208
Mvvv 112,128,212
s_nop 7
S_ s53,s64,6144
buffer_load_dwordx4 v[116:119], v10, s[32:35], s53 offen offset:256
S_ s53,s64,6144
buffer_load_dwordx4 v[120:123], v10, s[32:35], s53 offen offset:320
S_ s53,s64,0
buffer_load_dwordx4 v[124:127], v10, s[32:35], s53 offen offset:0
S_ s53,s64,0
buffer_load_dwordx4 v[128:131], v10, s[32:35], s53 offen offset:64
s_waitcnt vmcnt(0)
Mvvv 96,116,216
Mvvv 100,116,220
Mvvv 104,116,224
Mvvv 108,116,228
Mvvv 112,116,232
Mvvv 96,120,236
Mvvv 100,120,240
Mvvv 104,120,244
Mvvv 108,120,248
Mvvv 112,120,252
Nvvv 76,124,136,56
Nvvv 80,124,140,60
Nvvv 84,124,144,64
Nvvv 88,124,148,68
Nvvv 92,124,152,72
Mvvv 76,128,156
Mvvv 80,128,160
Mvvv 84,128,164
Mvvv 88,128,168
Mvvv 92,128,172
s_nop 7
S_ s53,s64,0
buffer_load_dwordx4 v[116:119], v10, s[32:35], s53 offen offset:128
S_ s53,s64,0
buffer_load_dwordx4 v[120:123], v10, s[32:35], s53 offen offset:192
S_ s53,s64,0
buffer_load_dwordx4 v[124:127], v10, s[32:35], s53 offen offset:256
S_ s53,s64,0
buffer_load_dwordx4 v[128:131], v10, s[32:35], s53 offen offset:320
s_waitcnt vmcnt(0)
Mvvv 76,116,176
Mvvv 80,116,180
Mvvv 84,116,184
Mvvv 88,116,188
Mvvv 92,116,192
Mvvv 76,120,196
Mvvv 80,120,200
Mvvv 84,120,204
Mvvv 88,120,208
Mvvv 92,120,212
Mvvv 76,124,216
Mvvv 80,124,220
Mvvv 84,124,224
Mvvv 88,124,228
Mvvv 92,124,232
Mvvv 76,128,236
Mvvv 80,128,240
Mvvv 84,128,244
Mvvv 88,128,248
Mvvv 92,128,252
s_nop 7
s_nop 2
v_max3_f32 v10, v76, v77, v78
v_max3_f32 v10, v10, v79, v96
v_max3_f32 v10, v10, v97, v98
v_max_f32 v10, v10, v99
v_mov_b32 v11, v10
s_nop 1
v_permlane16_swap_b32 v10, v11
v_max_f32 v10, v10, v11
v_mov_b32 v11, v10
s_nop 1
v_permlane32_swap_b32 v10, v11
v_max_f32 v10, v10, v11
A_ v10,0x40e00000,v10
v_max_f32 v10, 0, v10
v_exp_f32 v11, -v10
s_nop 0
U_ v46,v46,v11
R_ v12,a0
U_ v12,v12,v11
X_ a0,v12
R_ v13,a1
U_ v13,v13,v11
X_ a1,v13
R_ v12,a2
U_ v12,v12,v11
X_ a2,v12
R_ v13,a3
U_ v13,v13,v11
X_ a3,v13
R_ v12,a20
U_ v12,v12,v11
X_ a20,v12
R_ v13,a21
U_ v13,v13,v11
X_ a21,v13
R_ v12,a22
U_ v12,v12,v11
X_ a22,v12
R_ v13,a23
U_ v13,v13,v11
X_ a23,v13
R_ v12,a40
U_ v12,v12,v11
X_ a40,v12
R_ v13,a41
U_ v13,v13,v11
X_ a41,v13
R_ v12,a42
U_ v12,v12,v11
X_ a42,v12
R_ v13,a43
U_ v13,v13,v11
X_ a43,v13
R_ v12,a60
U_ v12,v12,v11
X_ a60,v12
R_ v13,a61
U_ v13,v13,v11
X_ a61,v13
R_ v12,a62
U_ v12,v12,v11
X_ a62,v12
R_ v13,a63
U_ v13,v13,v11
X_ a63,v13
R_ v12,a80
U_ v12,v12,v11
X_ a80,v12
R_ v13,a81
U_ v13,v13,v11
X_ a81,v13
R_ v12,a82
U_ v12,v12,v11
X_ a82,v12
R_ v13,a83
U_ v13,v13,v11
X_ a83,v13
R_ v12,a100
U_ v12,v12,v11
X_ a100,v12
R_ v13,a101
U_ v13,v13,v11
X_ a101,v13
R_ v12,a102
U_ v12,v12,v11
X_ a102,v12
R_ v13,a103
U_ v13,v13,v11
X_ a103,v13
R_ v12,a120
U_ v12,v12,v11
X_ a120,v12
R_ v13,a121
U_ v13,v13,v11
X_ a121,v13
R_ v12,a122
U_ v12,v12,v11
X_ a122,v12
R_ v13,a123
U_ v13,v13,v11
X_ a123,v13
R_ v12,a140
U_ v12,v12,v11
X_ a140,v12
R_ v13,a141
U_ v13,v13,v11
X_ a141,v13
R_ v12,a142
U_ v12,v12,v11
X_ a142,v12
R_ v13,a143
U_ v13,v13,v11
X_ a143,v13
R_ v12,a160
U_ v12,v12,v11
X_ a160,v12
R_ v13,a161
U_ v13,v13,v11
X_ a161,v13
R_ v12,a162
U_ v12,v12,v11
X_ a162,v12
R_ v13,a163
U_ v13,v13,v11
X_ a163,v13
R_ v12,a180
U_ v12,v12,v11
X_ a180,v12
R_ v13,a181
U_ v13,v13,v11
X_ a181,v13
R_ v12,a182
U_ v12,v12,v11
X_ a182,v12
R_ v13,a183
U_ v13,v13,v11
X_ a183,v13
R_ v12,a200
U_ v12,v12,v11
X_ a200,v12
R_ v13,a201
U_ v13,v13,v11
X_ a201,v13
R_ v12,a202
U_ v12,v12,v11
X_ a202,v12
R_ v13,a203
U_ v13,v13,v11
X_ a203,v13
R_ v12,a220
U_ v12,v12,v11
X_ a220,v12
R_ v13,a221
U_ v13,v13,v11
X_ a221,v13
R_ v12,a222
U_ v12,v12,v11
X_ a222,v12
R_ v13,a223
U_ v13,v13,v11
X_ a223,v13
B_ v56,v56,v10
B_ v57,v57,v10
B_ v58,v58,v10
B_ v59,v59,v10
B_ v76,v76,v10
B_ v77,v77,v10
B_ v78,v78,v10
B_ v79,v79,v10
B_ v96,v96,v10
B_ v97,v97,v10
B_ v98,v98,v10
B_ v99,v99,v10
E_ 76
E_ 77
E_ 78
E_ 79
E_ 96
E_ 97
E_ 98
E_ 99
s_nop 0
C_ 99,98,99
C_ 98,96,97
C_ 96,76,77
C_ 97,78,79
v_max3_f32 v10, v80, v81, v82
v_max3_f32 v10, v10, v83, v100
v_max3_f32 v10, v10, v101, v102
v_max_f32 v10, v10, v103
v_mov_b32 v11, v10
s_nop 1
v_permlane16_swap_b32 v10, v11
v_max_f32 v10, v10, v11
v_mov_b32 v11, v10
s_nop 1
v_permlane32_swap_b32 v10, v11
v_max_f32 v10, v10, v11
A_ v10,0x40e00000,v10
v_max_f32 v10, 0, v10
v_exp_f32 v11, -v10
s_nop 0
U_ v47,v47,v11
R_ v12,a4
U_ v12,v12,v11
X_ a4,v12
R_ v13,a5
U_ v13,v13,v11
X_ a5,v13
R_ v12,a6
U_ v12,v12,v11
X_ a6,v12
R_ v13,a7
U_ v13,v13,v11
X_ a7,v13
R_ v12,a24
U_ v12,v12,v11
X_ a24,v12
R_ v13,a25
U_ v13,v13,v11
X_ a25,v13
R_ v12,a26
U_ v12,v12,v11
X_ a26,v12
R_ v13,a27
U_ v13,v13,v11
X_ a27,v13
R_ v12,a44
U_ v12,v12,v11
X_ a44,v12
R_ v13,a45
U_ v13,v13,v11
X_ a45,v13
R_ v12,a46
U_ v12,v12,v11
X_ a46,v12
R_ v13,a47
U_ v13,v13,v11
X_ a47,v13
R_ v12,a64
U_ v12,v12,v11
X_ a64,v12
R_ v13,a65
U_ v13,v13,v11
X_ a65,v13
R_ v12,a66
U_ v12,v12,v11
X_ a66,v12
R_ v13,a67
U_ v13,v13,v11
X_ a67,v13
R_ v12,a84
U_ v12,v12,v11
X_ a84,v12
R_ v13,a85
U_ v13,v13,v11
X_ a85,v13
R_ v12,a86
U_ v12,v12,v11
X_ a86,v12
R_ v13,a87
U_ v13,v13,v11
X_ a87,v13
R_ v12,a104
U_ v12,v12,v11
X_ a104,v12
R_ v13,a105
U_ v13,v13,v11
X_ a105,v13
R_ v12,a106
U_ v12,v12,v11
X_ a106,v12
R_ v13,a107
U_ v13,v13,v11
X_ a107,v13
R_ v12,a124
U_ v12,v12,v11
X_ a124,v12
R_ v13,a125
U_ v13,v13,v11
X_ a125,v13
R_ v12,a126
U_ v12,v12,v11
X_ a126,v12
R_ v13,a127
U_ v13,v13,v11
X_ a127,v13
R_ v12,a144
U_ v12,v12,v11
X_ a144,v12
R_ v13,a145
U_ v13,v13,v11
X_ a145,v13
R_ v12,a146
U_ v12,v12,v11
X_ a146,v12
R_ v13,a147
U_ v13,v13,v11
X_ a147,v13
R_ v12,a164
U_ v12,v12,v11
X_ a164,v12
R_ v13,a165
U_ v13,v13,v11
X_ a165,v13
R_ v12,a166
U_ v12,v12,v11
X_ a166,v12
R_ v13,a167
U_ v13,v13,v11
X_ a167,v13
R_ v12,a184
U_ v12,v12,v11
X_ a184,v12
R_ v13,a185
U_ v13,v13,v11
X_ a185,v13
R_ v12,a186
U_ v12,v12,v11
X_ a186,v12
R_ v13,a187
U_ v13,v13,v11
X_ a187,v13
R_ v12,a204
U_ v12,v12,v11
X_ a204,v12
R_ v13,a205
U_ v13,v13,v11
X_ a205,v13
R_ v12,a206
U_ v12,v12,v11
X_ a206,v12
R_ v13,a207
U_ v13,v13,v11
X_ a207,v13
R_ v12,a224
U_ v12,v12,v11
X_ a224,v12
R_ v13,a225
U_ v13,v13,v11
X_ a225,v13
R_ v12,a226
U_ v12,v12,v11
X_ a226,v12
R_ v13,a227
U_ v13,v13,v11
X_ a227,v13
B_ v60,v60,v10
B_ v61,v61,v10
B_ v62,v62,v10
B_ v63,v63,v10
B_ v80,v80,v10
B_ v81,v81,v10
B_ v82,v82,v10
B_ v83,v83,v10
B_ v100,v100,v10
B_ v101,v101,v10
B_ v102,v102,v10
B_ v103,v103,v10
E_ 80
E_ 81
E_ 82
E_ 83
E_ 100
E_ 101
E_ 102
E_ 103
s_nop 0
C_ 103,102,103
C_ 102,100,101
C_ 100,80,81
C_ 101,82,83
v_max3_f32 v10, v84, v85, v86
v_max3_f32 v10, v10, v87, v104
v_max3_f32 v10, v10, v105, v106
v_max_f32 v10, v10, v107
v_mov_b32 v11, v10
s_nop 1
v_permlane16_swap_b32 v10, v11
v_max_f32 v10, v10, v11
v_mov_b32 v11, v10
s_nop 1
v_permlane32_swap_b32 v10, v11
v_max_f32 v10, v10, v11
A_ v10,0x40e00000,v10
v_max_f32 v10, 0, v10
v_exp_f32 v11, -v10
s_nop 0
U_ v48,v48,v11
R_ v12,a8
U_ v12,v12,v11
X_ a8,v12
R_ v13,a9
U_ v13,v13,v11
X_ a9,v13
R_ v12,a10
U_ v12,v12,v11
X_ a10,v12
R_ v13,a11
U_ v13,v13,v11
X_ a11,v13
R_ v12,a28
U_ v12,v12,v11
X_ a28,v12
R_ v13,a29
U_ v13,v13,v11
X_ a29,v13
R_ v12,a30
U_ v12,v12,v11
X_ a30,v12
R_ v13,a31
U_ v13,v13,v11
X_ a31,v13
R_ v12,a48
U_ v12,v12,v11
X_ a48,v12
R_ v13,a49
U_ v13,v13,v11
X_ a49,v13
R_ v12,a50
U_ v12,v12,v11
X_ a50,v12
R_ v13,a51
U_ v13,v13,v11
X_ a51,v13
R_ v12,a68
U_ v12,v12,v11
X_ a68,v12
R_ v13,a69
U_ v13,v13,v11
X_ a69,v13
R_ v12,a70
U_ v12,v12,v11
X_ a70,v12
R_ v13,a71
U_ v13,v13,v11
X_ a71,v13
R_ v12,a88
U_ v12,v12,v11
X_ a88,v12
R_ v13,a89
U_ v13,v13,v11
X_ a89,v13
R_ v12,a90
U_ v12,v12,v11
X_ a90,v12
R_ v13,a91
U_ v13,v13,v11
X_ a91,v13
R_ v12,a108
U_ v12,v12,v11
X_ a108,v12
R_ v13,a109
U_ v13,v13,v11
X_ a109,v13
R_ v12,a110
U_ v12,v12,v11
X_ a110,v12
R_ v13,a111
U_ v13,v13,v11
X_ a111,v13
R_ v12,a128
U_ v12,v12,v11
X_ a128,v12
R_ v13,a129
U_ v13,v13,v11
X_ a129,v13
R_ v12,a130
U_ v12,v12,v11
X_ a130,v12
R_ v13,a131
U_ v13,v13,v11
X_ a131,v13
R_ v12,a148
U_ v12,v12,v11
X_ a148,v12
R_ v13,a149
U_ v13,v13,v11
X_ a149,v13
R_ v12,a150
U_ v12,v12,v11
X_ a150,v12
R_ v13,a151
U_ v13,v13,v11
X_ a151,v13
R_ v12,a168
U_ v12,v12,v11
X_ a168,v12
R_ v13,a169
U_ v13,v13,v11
X_ a169,v13
R_ v12,a170
U_ v12,v12,v11
X_ a170,v12
R_ v13,a171
U_ v13,v13,v11
X_ a171,v13
R_ v12,a188
U_ v12,v12,v11
X_ a188,v12
R_ v13,a189
U_ v13,v13,v11
X_ a189,v13
R_ v12,a190
U_ v12,v12,v11
X_ a190,v12
R_ v13,a191
U_ v13,v13,v11
X_ a191,v13
R_ v12,a208
U_ v12,v12,v11
X_ a208,v12
R_ v13,a209
U_ v13,v13,v11
X_ a209,v13
R_ v12,a210
U_ v12,v12,v11
X_ a210,v12
R_ v13,a211
U_ v13,v13,v11
X_ a211,v13
R_ v12,a228
U_ v12,v12,v11
X_ a228,v12
R_ v13,a229
U_ v13,v13,v11
X_ a229,v13
R_ v12,a230
U_ v12,v12,v11
X_ a230,v12
R_ v13,a231
U_ v13,v13,v11
X_ a231,v13
B_ v64,v64,v10
B_ v65,v65,v10
B_ v66,v66,v10
B_ v67,v67,v10
B_ v84,v84,v10
B_ v85,v85,v10
B_ v86,v86,v10
B_ v87,v87,v10
B_ v104,v104,v10
B_ v105,v105,v10
B_ v106,v106,v10
B_ v107,v107,v10
E_ 84
E_ 85
E_ 86
E_ 87
E_ 104
E_ 105
E_ 106
E_ 107
s_nop 0
C_ 107,106,107
C_ 106,104,105
C_ 104,84,85
C_ 105,86,87
v_max3_f32 v10, v88, v89, v90
v_max3_f32 v10, v10, v91, v108
)A"
R"A(v_max3_f32 v10, v10, v109, v110
v_max_f32 v10, v10, v111
v_mov_b32 v11, v10
s_nop 1
v_permlane16_swap_b32 v10, v11
v_max_f32 v10, v10, v11
v_mov_b32 v11, v10
s_nop 1
v_permlane32_swap_b32 v10, v11
v_max_f32 v10, v10, v11
A_ v10,0x40e00000,v10
v_max_f32 v10, 0, v10
v_exp_f32 v11, -v10
s_nop 0
U_ v49,v49,v11
R_ v12,a12
U_ v12,v12,v11
X_ a12,v12
R_ v13,a13
U_ v13,v13,v11
X_ a13,v13
R_ v12,a14
U_ v12,v12,v11
X_ a14,v12
R_ v13,a15
U_ v13,v13,v11
X_ a15,v13
R_ v12,a32
U_ v12,v12,v11
X_ a32,v12
R_ v13,a33
U_ v13,v13,v11
X_ a33,v13
R_ v12,a34
U_ v12,v12,v11
X_ a34,v12
R_ v13,a35
U_ v13,v13,v11
X_ a35,v13
R_ v12,a52
U_ v12,v12,v11
X_ a52,v12
R_ v13,a53
U_ v13,v13,v11
X_ a53,v13
R_ v12,a54
U_ v12,v12,v11
X_ a54,v12
R_ v13,a55
U_ v13,v13,v11
X_ a55,v13
R_ v12,a72
U_ v12,v12,v11
X_ a72,v12
R_ v13,a73
U_ v13,v13,v11
X_ a73,v13
R_ v12,a74
U_ v12,v12,v11
X_ a74,v12
R_ v13,a75
U_ v13,v13,v11
X_ a75,v13
R_ v12,a92
U_ v12,v12,v11
X_ a92,v12
R_ v13,a93
U_ v13,v13,v11
X_ a93,v13
R_ v12,a94
U_ v12,v12,v11
X_ a94,v12
R_ v13,a95
U_ v13,v13,v11
X_ a95,v13
R_ v12,a112
U_ v12,v12,v11
X_ a112,v12
R_ v13,a113
U_ v13,v13,v11
X_ a113,v13
R_ v12,a114
U_ v12,v12,v11
X_ a114,v12
R_ v13,a115
U_ v13,v13,v11
X_ a115,v13
R_ v12,a132
U_ v12,v12,v11
X_ a132,v12
R_ v13,a133
U_ v13,v13,v11
X_ a133,v13
R_ v12,a134
U_ v12,v12,v11
X_ a134,v12
R_ v13,a135
U_ v13,v13,v11
X_ a135,v13
R_ v12,a152
U_ v12,v12,v11
X_ a152,v12
R_ v13,a153
U_ v13,v13,v11
X_ a153,v13
R_ v12,a154
U_ v12,v12,v11
X_ a154,v12
R_ v13,a155
U_ v13,v13,v11
X_ a155,v13
R_ v12,a172
U_ v12,v12,v11
X_ a172,v12
R_ v13,a173
U_ v13,v13,v11
X_ a173,v13
R_ v12,a174
U_ v12,v12,v11
X_ a174,v12
R_ v13,a175
U_ v13,v13,v11
X_ a175,v13
R_ v12,a192
U_ v12,v12,v11
X_ a192,v12
R_ v13,a193
U_ v13,v13,v11
X_ a193,v13
R_ v12,a194
U_ v12,v12,v11
X_ a194,v12
R_ v13,a195
U_ v13,v13,v11
X_ a195,v13
R_ v12,a212
U_ v12,v12,v11
X_ a212,v12
R_ v13,a213
U_ v13,v13,v11
X_ a213,v13
R_ v12,a214
U_ v12,v12,v11
X_ a214,v12
R_ v13,a215
U_ v13,v13,v11
X_ a215,v13
R_ v12,a232
U_ v12,v12,v11
X_ a232,v12
R_ v13,a233
U_ v13,v13,v11
X_ a233,v13
R_ v12,a234
U_ v12,v12,v11
X_ a234,v12
R_ v13,a235
U_ v13,v13,v11
X_ a235,v13
B_ v68,v68,v10
B_ v69,v69,v10
B_ v70,v70,v10
B_ v71,v71,v10
B_ v88,v88,v10
B_ v89,v89,v10
B_ v90,v90,v10
B_ v91,v91,v10
B_ v108,v108,v10
B_ v109,v109,v10
B_ v110,v110,v10
B_ v111,v111,v10
E_ 88
E_ 89
E_ 90
E_ 91
E_ 108
E_ 109
E_ 110
E_ 111
s_nop 0
C_ 111,110,111
C_ 110,108,109
C_ 108,88,89
C_ 109,90,91
v_max3_f32 v10, v92, v93, v94
v_max3_f32 v10, v10, v95, v112
v_max3_f32 v10, v10, v113, v114
v_max_f32 v10, v10, v115
v_mov_b32 v11, v10
s_nop 1
v_permlane16_swap_b32 v10, v11
v_max_f32 v10, v10, v11
v_mov_b32 v11, v10
s_nop 1
v_permlane32_swap_b32 v10, v11
v_max_f32 v10, v10, v11
A_ v10,0x40e00000,v10
v_max_f32 v10, 0, v10
v_exp_f32 v11, -v10
s_nop 0
U_ v50,v50,v11
R_ v12,a16
U_ v12,v12,v11
X_ a16,v12
R_ v13,a17
U_ v13,v13,v11
X_ a17,v13
R_ v12,a18
U_ v12,v12,v11
X_ a18,v12
R_ v13,a19
U_ v13,v13,v11
X_ a19,v13
R_ v12,a36
U_ v12,v12,v11
X_ a36,v12
R_ v13,a37
U_ v13,v13,v11
X_ a37,v13
R_ v12,a38
U_ v12,v12,v11
X_ a38,v12
R_ v13,a39
U_ v13,v13,v11
X_ a39,v13
R_ v12,a56
U_ v12,v12,v11
X_ a56,v12
R_ v13,a57
U_ v13,v13,v11
X_ a57,v13
R_ v12,a58
U_ v12,v12,v11
X_ a58,v12
R_ v13,a59
U_ v13,v13,v11
X_ a59,v13
R_ v12,a76
U_ v12,v12,v11
X_ a76,v12
R_ v13,a77
U_ v13,v13,v11
X_ a77,v13
R_ v12,a78
U_ v12,v12,v11
X_ a78,v12
R_ v13,a79
U_ v13,v13,v11
X_ a79,v13
R_ v12,a96
U_ v12,v12,v11
X_ a96,v12
R_ v13,a97
U_ v13,v13,v11
X_ a97,v13
R_ v12,a98
U_ v12,v12,v11
X_ a98,v12
R_ v13,a99
U_ v13,v13,v11
X_ a99,v13
R_ v12,a116
U_ v12,v12,v11
X_ a116,v12
R_ v13,a117
U_ v13,v13,v11
X_ a117,v13
R_ v12,a118
U_ v12,v12,v11
X_ a118,v12
R_ v13,a119
U_ v13,v13,v11
X_ a119,v13
R_ v12,a136
U_ v12,v12,v11
X_ a136,v12
R_ v13,a137
U_ v13,v13,v11
X_ a137,v13
R_ v12,a138
U_ v12,v12,v11
X_ a138,v12
R_ v13,a139
U_ v13,v13,v11
X_ a139,v13
R_ v12,a156
U_ v12,v12,v11
X_ a156,v12
R_ v13,a157
U_ v13,v13,v11
X_ a157,v13
R_ v12,a158
U_ v12,v12,v11
X_ a158,v12
R_ v13,a159
U_ v13,v13,v11
X_ a159,v13
R_ v12,a176
U_ v12,v12,v11
X_ a176,v12
R_ v13,a177
U_ v13,v13,v11
X_ a177,v13
R_ v12,a178
U_ v12,v12,v11
X_ a178,v12
R_ v13,a179
U_ v13,v13,v11
X_ a179,v13
R_ v12,a196
U_ v12,v12,v11
X_ a196,v12
R_ v13,a197
U_ v13,v13,v11
X_ a197,v13
R_ v12,a198
U_ v12,v12,v11
X_ a198,v12
R_ v13,a199
U_ v13,v13,v11
X_ a199,v13
R_ v12,a216
U_ v12,v12,v11
X_ a216,v12
R_ v13,a217
U_ v13,v13,v11
X_ a217,v13
R_ v12,a218
U_ v12,v12,v11
X_ a218,v12
R_ v13,a219
U_ v13,v13,v11
X_ a219,v13
R_ v12,a236
U_ v12,v12,v11
X_ a236,v12
R_ v13,a237
U_ v13,v13,v11
X_ a237,v13
R_ v12,a238
U_ v12,v12,v11
X_ a238,v12
R_ v13,a239
U_ v13,v13,v11
X_ a239,v13
B_ v72,v72,v10
B_ v73,v73,v10
B_ v74,v74,v10
B_ v75,v75,v10
B_ v92,v92,v10
B_ v93,v93,v10
B_ v94,v94,v10
B_ v95,v95,v10
B_ v112,v112,v10
B_ v113,v113,v10
B_ v114,v114,v10
B_ v115,v115,v10
E_ 92
E_ 93
E_ 94
E_ 95
E_ 112
E_ 113
E_ 114
E_ 115
s_nop 0
C_ 115,114,115
C_ 114,112,113
C_ 112,92,93
C_ 113,94,95
v_mov_b32 v92, 0x3f803f80
v_mov_b32 v93, 0x3f803f80
v_mov_b32 v94, 0x3f803f80
v_mov_b32 v95, 0x3f803f80
s_nop 7
s_lshl_b32 s53, s58, 4
S_ s53,s53,s63
s_cmp_eq_u32 s53, 2
s_cbranch_scc1 .Lstep0_3
s_cmp_eq_u32 s53, 14
s_cbranch_scc1 .Lstep0_15
s_cmp_eq_u32 s53, 18
s_cbranch_scc1 .Lstep1_3
s_cmp_eq_u32 s53, 20
s_cbranch_scc1 .Lstep1_5
s_cmp_eq_u32 s53, 22
s_cbranch_scc1 .Lstep1_7
s_cmp_eq_u32 s53, 24
s_cbranch_scc1 .Lstep1_9
s_cmp_eq_u32 s53, 26
s_cbranch_scc1 .Lstep1_11
s_cmp_eq_u32 s53, 28
s_cbranch_scc1 .Lstep1_13
s_cmp_eq_u32 s53, 30
s_cbranch_scc1 .Lstep1_15
s_cmp_eq_u32 s53, 34
s_cbranch_scc1 .Lstep2_3
s_cmp_eq_u32 s53, 36
s_cbranch_scc1 .Lstep2_5
s_cmp_eq_u32 s53, 38
s_cbranch_scc1 .Lstep2_7
s_cmp_eq_u32 s53, 40
s_cbranch_scc1 .Lstep2_9
s_cmp_eq_u32 s53, 42
s_cbranch_scc1 .Lstep2_11
s_cmp_eq_u32 s53, 44
s_cbranch_scc1 .Lstep2_13
s_cmp_eq_u32 s53, 46
s_cbranch_scc1 .Lstep2_15
s_endpgm
.Lslow1:
W_ 0
s_nop 7
s_nop 7
s_and_b32 s64, s61, 4294959104
s_mul_i32 s53, s63, 12288
S_ s64,s64,s53
R_ v11,a248
v_and_b32 v10, 0xf0, v11
v_mul_u32_u24 v10, 24, v10
v_lshrrev_b32 v11, 8, v11
v_lshl_add_u32 v10, v11, 4, v10
S_ s53,s64,6144
buffer_load_dwordx4 v[96:99], v10, s[32:35], s53 offen offset:0
S_ s53,s64,6144
buffer_load_dwordx4 v[100:103], v10, s[32:35], s53 offen offset:64
S_ s53,s64,6144
buffer_load_dwordx4 v[104:107], v10, s[32:35], s53 offen offset:128
S_ s53,s64,6144
buffer_load_dwordx4 v[108:111], v10, s[32:35], s53 offen offset:192
s_waitcnt vmcnt(0)
Nvvv 116,96,136,56
Nvvv 120,96,140,60
Nvvv 124,96,144,64
Nvvv 128,96,148,68
Nvvv 132,96,152,72
Mvvv 116,100,156
Mvvv 120,100,160
Mvvv 124,100,164
Mvvv 128,100,168
Mvvv 132,100,172
Mvvv 116,104,176
Mvvv 120,104,180
Mvvv 124,104,184
Mvvv 128,104,188
Mvvv 132,104,192
Mvvv 116,108,196
Mvvv 120,108,200
Mvvv 124,108,204
Mvvv 128,108,208
Mvvv 132,108,212
s_nop 7
S_ s53,s64,6144
buffer_load_dwordx4 v[96:99], v10, s[32:35], s53 offen offset:256
S_ s53,s64,6144
buffer_load_dwordx4 v[100:103], v10, s[32:35], s53 offen offset:320
S_ s53,s64,0
buffer_load_dwordx4 v[104:107], v10, s[32:35], s53 offen offset:0
S_ s53,s64,0
buffer_load_dwordx4 v[108:111], v10, s[32:35], s53 offen offset:64
s_waitcnt vmcnt(0)
Mvvv 116,96,216
Mvvv 120,96,220
Mvvv 124,96,224
Mvvv 128,96,228
Mvvv 132,96,232
Mvvv 116,100,236
Mvvv 120,100,240
Mvvv 124,100,244
Mvvv 128,100,248
Mvvv 132,100,252
Nvvv 76,104,136,56
Nvvv 80,104,140,60
Nvvv 84,104,144,64
Nvvv 88,104,148,68
Nvvv 92,104,152,72
Mvvv 76,108,156
Mvvv 80,108,160
Mvvv 84,108,164
Mvvv 88,108,168
Mvvv 92,108,172
s_nop 7
S_ s53,s64,0
buffer_load_dwordx4 v[96:99], v10, s[32:35], s53 offen offset:128
S_ s53,s64,0
buffer_load_dwordx4 v[100:103], v10, s[32:35], s53 offen offset:192
S_ s53,s64,0
buffer_load_dwordx4 v[104:107], v10, s[32:35], s53 offen offset:256
S_ s53,s64,0
buffer_load_dwordx4 v[108:111], v10, s[32:35], s53 offen offset:320
s_waitcnt vmcnt(0)
Mvvv 76,96,176
Mvvv 80,96,180
Mvvv 84,96,184
Mvvv 88,96,188
Mvvv 92,96,192
Mvvv 76,100,196
Mvvv 80,100,200
Mvvv 84,100,204
Mvvv 88,100,208
Mvvv 92,100,212
Mvvv 76,104,216
Mvvv 80,104,220
Mvvv 84,104,224
Mvvv 88,104,228
Mvvv 92,104,232
Mvvv 76,108,236
Mvvv 80,108,240
Mvvv 84,108,244
Mvvv 88,108,248
Mvvv 92,108,252
s_nop 7
s_nop 2
v_max3_f32 v10, v76, v77, v78
v_max3_f32 v10, v10, v79, v116
v_max3_f32 v10, v10, v117, v118
v_max_f32 v10, v10, v119
v_mov_b32 v11, v10
s_nop 1
v_permlane16_swap_b32 v10, v11
v_max_f32 v10, v10, v11
v_mov_b32 v11, v10
s_nop 1
v_permlane32_swap_b32 v10, v11
v_max_f32 v10, v10, v11
A_ v10,0x40e00000,v10
v_max_f32 v10, 0, v10
v_exp_f32 v11, -v10
s_nop 0
U_ v46,v46,v11
R_ v12,a0
U_ v12,v12,v11
X_ a0,v12
R_ v13,a1
U_ v13,v13,v11
X_ a1,v13
R_ v12,a2
U_ v12,v12,v11
X_ a2,v12
R_ v13,a3
U_ v13,v13,v11
X_ a3,v13
R_ v12,a20
U_ v12,v12,v11
X_ a20,v12
R_ v13,a21
U_ v13,v13,v11
X_ a21,v13
R_ v12,a22
U_ v12,v12,v11
X_ a22,v12
R_ v13,a23
U_ v13,v13,v11
X_ a23,v13
R_ v12,a40
U_ v12,v12,v11
X_ a40,v12
R_ v13,a41
U_ v13,v13,v11
X_ a41,v13
R_ v12,a42
U_ v12,v12,v11
X_ a42,v12
R_ v13,a43
U_ v13,v13,v11
X_ a43,v13
R_ v12,a60
U_ v12,v12,v11
X_ a60,v12
R_ v13,a61
U_ v13,v13,v11
X_ a61,v13
R_ v12,a62
U_ v12,v12,v11
X_ a62,v12
R_ v13,a63
U_ v13,v13,v11
X_ a63,v13
R_ v12,a80
U_ v12,v12,v11
X_ a80,v12
R_ v13,a81
U_ v13,v13,v11
X_ a81,v13
R_ v12,a82
U_ v12,v12,v11
X_ a82,v12
R_ v13,a83
U_ v13,v13,v11
X_ a83,v13
R_ v12,a100
U_ v12,v12,v11
X_ a100,v12
R_ v13,a101
U_ v13,v13,v11
X_ a101,v13
R_ v12,a102
U_ v12,v12,v11
X_ a102,v12
R_ v13,a103
U_ v13,v13,v11
X_ a103,v13
R_ v12,a120
U_ v12,v12,v11
X_ a120,v12
R_ v13,a121
U_ v13,v13,v11
X_ a121,v13
R_ v12,a122
U_ v12,v12,v11
X_ a122,v12
R_ v13,a123
U_ v13,v13,v11
X_ a123,v13
R_ v12,a140
U_ v12,v12,v11
X_ a140,v12
R_ v13,a141
U_ v13,v13,v11
X_ a141,v13
R_ v12,a142
U_ v12,v12,v11
X_ a142,v12
R_ v13,a143
U_ v13,v13,v11
X_ a143,v13
R_ v12,a160
U_ v12,v12,v11
X_ a160,v12
R_ v13,a161
U_ v13,v13,v11
X_ a161,v13
R_ v12,a162
U_ v12,v12,v11
X_ a162,v12
R_ v13,a163
U_ v13,v13,v11
X_ a163,v13
R_ v12,a180
U_ v12,v12,v11
X_ a180,v12
R_ v13,a181
U_ v13,v13,v11
X_ a181,v13
R_ v12,a182
U_ v12,v12,v11
X_ a182,v12
R_ v13,a183
U_ v13,v13,v11
X_ a183,v13
R_ v12,a200
U_ v12,v12,v11
X_ a200,v12
R_ v13,a201
U_ v13,v13,v11
X_ a201,v13
R_ v12,a202
U_ v12,v12,v11
X_ a202,v12
R_ v13,a203
U_ v13,v13,v11
X_ a203,v13
R_ v12,a220
U_ v12,v12,v11
X_ a220,v12
R_ v13,a221
U_ v13,v13,v11
X_ a221,v13
R_ v12,a222
U_ v12,v12,v11
X_ a222,v12
R_ v13,a223
U_ v13,v13,v11
X_ a223,v13
B_ v56,v56,v10
B_ v57,v57,v10
B_ v58,v58,v10
B_ v59,v59,v10
B_ v76,v76,v10
B_ v77,v77,v10
B_ v78,v78,v10
B_ v79,v79,v10
B_ v116,v116,v10
B_ v117,v117,v10
B_ v118,v118,v10
B_ v119,v119,v10
E_ 76
E_ 77
E_ 78
E_ 79
E_ 116
E_ 117
E_ 118
E_ 119
s_nop 0
C_ 119,118,119
C_ 118,116,117
C_ 116,76,77
C_ 117,78,79
v_max3_f32 v10, v80, v81, v82
v_max3_f32 v10, v10, v83, v120
v_max3_f32 v10, v10, v121, v122
v_max_f32 v10, v10, v123
v_mov_b32 v11, v10
s_nop 1
v_permlane16_swap_b32 v10, v11
v_max_f32 v10, v10, v11
v_mov_b32 v11, v10
s_nop 1
v_permlane32_swap_b32 v10, v11
v_max_f32 v10, v10, v11
A_ v10,0x40e00000,v10
v_max_f32 v10, 0, v10
v_exp_f32 v11, -v10
s_nop 0
U_ v47,v47,v11
R_ v12,a4
U_ v12,v12,v11
X_ a4,v12
R_ v13,a5
U_ v13,v13,v11
X_ a5,v13
R_ v12,a6
U_ v12,v12,v11
X_ a6,v12
R_ v13,a7
U_ v13,v13,v11
X_ a7,v13
R_ v12,a24
U_ v12,v12,v11
X_ a24,v12
R_ v13,a25
U_ v13,v13,v11
X_ a25,v13
R_ v12,a26
U_ v12,v12,v11
X_ a26,v12
R_ v13,a27
U_ v13,v13,v11
X_ a27,v13
R_ v12,a44
U_ v12,v12,v11
X_ a44,v12
R_ v13,a45
U_ v13,v13,v11
X_ a45,v13
R_ v12,a46
U_ v12,v12,v11
X_ a46,v12
R_ v13,a47
U_ v13,v13,v11
X_ a47,v13
R_ v12,a64
U_ v12,v12,v11
X_ a64,v12
R_ v13,a65
U_ v13,v13,v11
X_ a65,v13
R_ v12,a66
U_ v12,v12,v11
X_ a66,v12
R_ v13,a67
U_ v13,v13,v11
X_ a67,v13
R_ v12,a84
U_ v12,v12,v11
X_ a84,v12
R_ v13,a85
U_ v13,v13,v11
X_ a85,v13
R_ v12,a86
U_ v12,v12,v11
X_ a86,v12
R_ v13,a87
U_ v13,v13,v11
X_ a87,v13
R_ v12,a104
U_ v12,v12,v11
X_ a104,v12
R_ v13,a105
U_ v13,v13,v11
X_ a105,v13
R_ v12,a106
U_ v12,v12,v11
X_ a106,v12
R_ v13,a107
U_ v13,v13,v11
X_ a107,v13
R_ v12,a124
U_ v12,v12,v11
X_ a124,v12
R_ v13,a125
U_ v13,v13,v11
X_ a125,v13
R_ v12,a126
U_ v12,v12,v11
X_ a126,v12
R_ v13,a127
U_ v13,v13,v11
X_ a127,v13
R_ v12,a144
U_ v12,v12,v11
X_ a144,v12
R_ v13,a145
U_ v13,v13,v11
X_ a145,v13
R_ v12,a146
U_ v12,v12,v11
X_ a146,v12
R_ v13,a147
U_ v13,v13,v11
X_ a147,v13
R_ v12,a164
U_ v12,v12,v11
X_ a164,v12
R_ v13,a165
U_ v13,v13,v11
X_ a165,v13
R_ v12,a166
U_ v12,v12,v11
X_ a166,v12
R_ v13,a167
U_ v13,v13,v11
X_ a167,v13
R_ v12,a184
U_ v12,v12,v11
X_ a184,v12
R_ v13,a185
U_ v13,v13,v11
X_ a185,v13
R_ v12,a186
U_ v12,v12,v11
X_ a186,v12
R_ v13,a187
U_ v13,v13,v11
X_ a187,v13
R_ v12,a204
U_ v12,v12,v11
X_ a204,v12
R_ v13,a205
U_ v13,v13,v11
X_ a205,v13
R_ v12,a206
U_ v12,v12,v11
X_ a206,v12
R_ v13,a207
U_ v13,v13,v11
X_ a207,v13
R_ v12,a224
U_ v12,v12,v11
X_ a224,v12
R_ v13,a225
U_ v13,v13,v11
X_ a225,v13
R_ v12,a226
U_ v12,v12,v11
X_ a226,v12
R_ v13,a227
U_ v13,v13,v11
X_ a227,v13
B_ v60,v60,v10
B_ v61,v61,v10
B_ v62,v62,v10
B_ v63,v63,v10
B_ v80,v80,v10
B_ v81,v81,v10
B_ v82,v82,v10
B_ v83,v83,v10
B_ v120,v120,v10
B_ v121,v121,v10
B_ v122,v122,v10
B_ v123,v123,v10
E_ 80
E_ 81
E_ 82
E_ 83
E_ 120
E_ 121
E_ 122
E_ 123
s_nop 0
C_ 123,122,123
C_ 122,120,121
C_ 120,80,81
C_ 121,82,83
v_max3_f32 v10, v84, v85, v86
v_max3_f32 v10, v10, v87, v124
v_max3_f32 v10, v10, v125, v126
v_max_f32 v10, v10, v127
v_mov_b32 v11, v10
s_nop 1
v_permlane16_swap_b32 v10, v11
v_max_f32 v10, v10, v11
v_mov_b32 v11, v10
s_nop 1
v_permlane32_swap_b32 v10, v11
v_max_f32 v10, v10, v11
A_ v10,0x40e00000,v10
v_max_f32 v10, 0, v10
v_exp_f32 v11, -v10
s_nop 0
U_ v48,v48,v11
R_ v12,a8
U_ v12,v12,v11
X_ a8,v12
R_ v13,a9
U_ v13,v13,v11
X_ a9,v13
R_ v12,a10
U_ v12,v12,v11
X_ a10,v12
R_ v13,a11
U_ v13,v13,v11
X_ a11,v13
R_ v12,a28
U_ v12,v12,v11
X_ a28,v12
R_ v13,a29
U_ v13,v13,v11
X_ a29,v13
R_ v12,a30
U_ v12,v12,v11
X_ a30,v12
R_ v13,a31
U_ v13,v13,v11
X_ a31,v13
R_ v12,a48
U_ v12,v12,v11
X_ a48,v12
R_ v13,a49
U_ v13,v13,v11
X_ a49,v13
R_ v12,a50
U_ v12,v12,v11
X_ a50,v12
R_ v13,a51
U_ v13,v13,v11
X_ a51,v13
R_ v12,a68
U_ v12,v12,v11
X_ a68,v12
R_ v13,a69
U_ v13,v13,v11
X_ a69,v13
R_ v12,a70
U_ v12,v12,v11
X_ a70,v12
R_ v13,a71
U_ v13,v13,v11
X_ a71,v13
R_ v12,a88
U_ v12,v12,v11
X_ a88,v12
R_ v13,a89
U_ v13,v13,v11
X_ a89,v13
R_ v12,a90
U_ v12,v12,v11
X_ a90,v12
R_ v13,a91
U_ v13,v13,v11
X_ a91,v13
R_ v12,a108
U_ v12,v12,v11
X_ a108,v12
R_ v13,a109
U_ v13,v13,v11
X_ a109,v13
R_ v12,a110
U_ v12,v12,v11
X_ a110,v12
R_ v13,a111
U_ v13,v13,v11
X_ a111,v13
R_ v12,a128
U_ v12,v12,v11
X_ a128,v12
R_ v13,a129
U_ v13,v13,v11
X_ a129,v13
R_ v12,a130
U_ v12,v12,v11
X_ a130,v12
R_ v13,a131
U_ v13,v13,v11
X_ a131,v13
R_ v12,a148
U_ v12,v12,v11
X_ a148,v12
R_ v13,a149
U_ v13,v13,v11
X_ a149,v13
R_ v12,a150
U_ v12,v12,v11
X_ a150,v12
R_ v13,a151
U_ v13,v13,v11
X_ a151,v13
R_ v12,a168
U_ v12,v12,v11
X_ a168,v12
R_ v13,a169
U_ v13,v13,v11
X_ a169,v13
R_ v12,a170
U_ v12,v12,v11
X_ a170,v12
R_ v13,a171
U_ v13,v13,v11
X_ a171,v13
R_ v12,a188
U_ v12,v12,v11
X_ a188,v12
R_ v13,a189
U_ v13,v13,v11
X_ a189,v13
R_ v12,a190
U_ v12,v12,v11
X_ a190,v12
R_ v13,a191
U_ v13,v13,v11
X_ a191,v13
R_ v12,a208
U_ v12,v12,v11
X_ a208,v12
R_ v13,a209
U_ v13,v13,v11
X_ a209,v13
R_ v12,a210
U_ v12,v12,v11
X_ a210,v12
R_ v13,a211
U_ v13,v13,v11
X_ a211,v13
R_ v12,a228
U_ v12,v12,v11
X_ a228,v12
R_ v13,a229
U_ v13,v13,v11
X_ a229,v13
R_ v12,a230
U_ v12,v12,v11
X_ a230,v12
R_ v13,a231
U_ v13,v13,v11
X_ a231,v13
B_ v64,v64,v10
B_ v65,v65,v10
B_ v66,v66,v10
B_ v67,v67,v10
B_ v84,v84,v10
B_ v85,v85,v10
B_ v86,v86,v10
B_ v87,v87,v10
B_ v124,v124,v10
B_ v125,v125,v10
B_ v126,v126,v10
B_ v127,v127,v10
E_ 84
E_ 85
E_ 86
E_ 87
E_ 124
E_ 125
E_ 126
E_ 127
s_nop 0
C_ 127,126,127
C_ 126,124,125
C_ 124,84,85
C_ 125,86,87
v_max3_f32 v10, v88, v89, v90
v_max3_f32 v10, v10, v91, v128
v_max3_f32 v10, v10, v129, v130
v_max_f32 v10, v10, v131
v_mov_b32 v11, v10
s_nop 1
v_permlane16_swap_b32 v10, v11
v_max_f32 v10, v10, v11
v_mov_b32 v11, v10
s_nop 1
v_permlane32_swap_b32 v10, v11
v_max_f32 v10, v10, v11
A_ v10,0x40e00000,v10
v_max_f32 v10, 0, v10
v_exp_f32 v11, -v10
s_nop 0
U_ v49,v49,v11
R_ v12,a12
U_ v12,v12,v11
X_ a12,v12
R_ v13,a13
U_ v13,v13,v11
X_ a13,v13
R_ v12,a14
U_ v12,v12,v11
X_ a14,v12
R_ v13,a15
U_ v13,v13,v11
X_ a15,v13
R_ v12,a32
U_ v12,v12,v11
X_ a32,v12
R_ v13,a33
U_ v13,v13,v11
X_ a33,v13
R_ v12,a34
U_ v12,v12,v11
X_ a34,v12
R_ v13,a35
U_ v13,v13,v11
X_ a35,v13
R_ v12,a52
U_ v12,v12,v11
X_ a52,v12
R_ v13,a53
U_ v13,v13,v11
X_ a53,v13
R_ v12,a54
U_ v12,v12,v11
X_ a54,v12
R_ v13,a55
U_ v13,v13,v11
X_ a55,v13
R_ v12,a72
U_ v12,v12,v11
X_ a72,v12
R_ v13,a73
U_ v13,v13,v11
X_ a73,v13
R_ v12,a74
U_ v12,v12,v11
X_ a74,v12
R_ v13,a75
U_ v13,v13,v11
X_ a75,v13
R_ v12,a92
U_ v12,v12,v11
X_ a92,v12
R_ v13,a93
U_ v13,v13,v11
X_ a93,v13
R_ v12,a94
U_ v12,v12,v11
X_ a94,v12
R_ v13,a95
U_ v13,v13,v11
X_ a95,v13
R_ v12,a112
U_ v12,v12,v11
X_ a112,v12
R_ v13,a113
U_ v13,v13,v11
X_ a113,v13
R_ v12,a114
U_ v12,v12,v11
X_ a114,v12
R_ v13,a115
U_ v13,v13,v11
X_ a115,v13
R_ v12,a132
U_ v12,v12,v11
X_ a132,v12
R_ v13,a133
U_ v13,v13,v11
X_ a133,v13
R_ v12,a134
U_ v12,v12,v11
X_ a134,v12
R_ v13,a135
U_ v13,v13,v11
X_ a135,v13
R_ v12,a152
U_ v12,v12,v11
X_ a152,v12
R_ v13,a153
U_ v13,v13,v11
X_ a153,v13
R_ v12,a154
U_ v12,v12,v11
X_ a154,v12
R_ v13,a155
U_ v13,v13,v11
X_ a155,v13
R_ v12,a172
U_ v12,v12,v11
X_ a172,v12
R_ v13,a173
U_ v13,v13,v11
X_ a173,v13
R_ v12,a174
U_ v12,v12,v11
X_ a174,v12
R_ v13,a175
U_ v13,v13,v11
X_ a175,v13
R_ v12,a192
U_ v12,v12,v11
X_ a192,v12
R_ v13,a193
U_ v13,v13,v11
X_ a193,v13
R_ v12,a194
U_ v12,v12,v11
X_ a194,v12
R_ v13,a195
U_ v13,v13,v11
X_ a195,v13
R_ v12,a212
U_ v12,v12,v11
X_ a212,v12
R_ v13,a213
U_ v13,v13,v11
X_ a213,v13
R_ v12,a214
U_ v12,v12,v11
X_ a214,v12
R_ v13,a215
U_ v13,v13,v11
X_ a215,v13
R_ v12,a232
U_ v12,v12,v11
X_ a232,v12
R_ v13,a233
U_ v13,v13,v11
X_ a233,v13
R_ v12,a234
U_ v12,v12,v11
X_ a234,v12
R_ v13,a235
U_ v13,v13,v11
X_ a235,v13
B_ v68,v68,v10
B_ v69,v69,v10
B_ v70,v70,v10
B_ v71,v71,v10
B_ v88,v88,v10
B_ v89,v89,v10
B_ v90,v90,v10
B_ v91,v91,v10
B_ v128,v128,v10
B_ v129,v129,v10
B_ v130,v130,v10
B_ v131,v131,v10
E_ 88
E_ 89
E_ 90
E_ 91
E_ 128
E_ 129
E_ 130
E_ 131
s_nop 0
C_ 131,130,131
C_ 130,128,129
C_ 128,88,89
C_ 129,90,91
v_max3_f32 v10, v92, v93, v94
v_max3_f32 v10, v10, v95, v132
v_max3_f32 v10, v10, v133, v134
v_max_f32 v10, v10, v135
v_mov_b32 v11, v10
s_nop 1
v_permlane16_swap_b32 v10, v11
v_max_f32 v10, v10, v11
v_mov_b32 v11, v10
s_nop 1
v_permlane32_swap_b32 v10, v11
v_max_f32 v10, v10, v11
A_ v10,0x40e00000,v10
v_max_f32 v10, 0, v10
v_exp_f32 v11, -v10
s_nop 0
U_ v50,v50,v11
R_ v12,a16
U_ v12,v12,v11
X_ a16,v12
R_ v13,a17
U_ v13,v13,v11
X_ a17,v13
R_ v12,a18
U_ v12,v12,v11
X_ a18,v12
R_ v13,a19
U_ v13,v13,v11
X_ a19,v13
R_ v12,a36
U_ v12,v12,v11
X_ a36,v12
R_ v13,a37
U_ v13,v13,v11
X_ a37,v13
R_ v12,a38
U_ v12,v12,v11
X_ a38,v12
R_ v13,a39
U_ v13,v13,v11
X_ a39,v13
R_ v12,a56
U_ v12,v12,v11
X_ a56,v12
R_ v13,a57
U_ v13,v13,v11
X_ a57,v13
R_ v12,a58
U_ v12,v12,v11
X_ a58,v12
R_ v13,a59
U_ v13,v13,v11
X_ a59,v13
R_ v12,a76
U_ v12,v12,v11
X_ a76,v12
R_ v13,a77
U_ v13,v13,v11
X_ a77,v13
R_ v12,a78
U_ v12,v12,v11
X_ a78,v12
R_ v13,a79
U_ v13,v13,v11
X_ a79,v13
R_ v12,a96
U_ v12,v12,v11
X_ a96,v12
R_ v13,a97
U_ v13,v13,v11
X_ a97,v13
R_ v12,a98
U_ v12,v12,v11
X_ a98,v12
R_ v13,a99
U_ v13,v13,v11
X_ a99,v13
R_ v12,a116
U_ v12,v12,v11
X_ a116,v12
R_ v13,a117
U_ v13,v13,v11
X_ a117,v13
R_ v12,a118
U_ v12,v12,v11
X_ a118,v12
R_ v13,a119
U_ v13,v13,v11
X_ a119,v13
R_ v12,a136
U_ v12,v12,v11
X_ a136,v12
R_ v13,a137
U_ v13,v13,v11
X_ a137,v13
R_ v12,a138
U_ v12,v12,v11
X_ a138,v12
R_ v13,a139
U_ v13,v13,v11
X_ a139,v13
R_ v12,a156
U_ v12,v12,v11
X_ a156,v12
R_ v13,a157
U_ v13,v13,v11
X_ a157,v13
R_ v12,a158
U_ v12,v12,v11
X_ a158,v12
R_ v13,a159
U_ v13,v13,v11
X_ a159,v13
R_ v12,a176
U_ v12,v12,v11
X_ a176,v12
R_ v13,a177
U_ v13,v13,v11
X_ a177,v13
R_ v12,a178
U_ v12,v12,v11
X_ a178,v12
R_ v13,a179
U_ v13,v13,v11
X_ a179,v13
R_ v12,a196
U_ v12,v12,v11
X_ a196,v12
R_ v13,a197
U_ v13,v13,v11
X_ a197,v13
R_ v12,a198
U_ v12,v12,v11
X_ a198,v12
R_ v13,a199
U_ v13,v13,v11
X_ a199,v13
R_ v12,a216
U_ v12,v12,v11
X_ a216,v12
R_ v13,a217
U_ v13,v13,v11
X_ a217,v13
R_ v12,a218
U_ v12,v12,v11
X_ a218,v12
R_ v13,a219
U_ v13,v13,v11
X_ a219,v13
R_ v12,a236
U_ v12,v12,v11
X_ a236,v12
R_ v13,a237
U_ v13,v13,v11
X_ a237,v13
R_ v12,a238
U_ v12,v12,v11
X_ a238,v12
R_ v13,a239
U_ v13,v13,v11
X_ a239,v13
B_ v72,v72,v10
B_ v73,v73,v10
B_ v74,v74,v10
B_ v75,v75,v10
B_ v92,v92,v10
B_ v93,v93,v10
B_ v94,v94,v10
B_ v95,v95,v10
B_ v132,v132,v10
B_ v133,v133,v10
B_ v134,v134,v10
B_ v135,v135,v10
E_ 92
E_ 93
E_ 94
E_ 95
E_ 132
E_ 133
E_ 134
E_ 135
s_nop 0
C_ 135,134,135
C_ 134,132,133
C_ 132,92,93
C_ 133,94,95
v_mov_b32 v92, 0x3f803f80
v_mov_b32 v93, 0x3f803f80
v_mov_b32 v94, 0x3f803f80
v_mov_b32 v95, 0x3f803f80
s_nop 7
s_lshl_b32 s53, s58, 4
S_ s53,s53,s63
s_cmp_eq_u32 s53, 1
s_cbranch_scc1 .Lstep0_2
s_cmp_eq_u32 s53, 13
s_cbranch_scc1 .Lstep0_14
s_cmp_eq_u32 s53, 15
s_cbranch_scc1 .Lstep0_16
s_cmp_eq_u32 s53, 17
s_cbranch_scc1 .Lstep1_2
s_cmp_eq_u32 s53, 19
s_cbranch_scc1 .Lstep1_4
s_cmp_eq_u32 s53, 21
s_cbranch_scc1 .Lstep1_6
s_cmp_eq_u32 s53, 23
s_cbranch_scc1 .Lstep1_8
s_cmp_eq_u32 s53, 25
s_cbranch_scc1 .Lstep1_10
s_cmp_eq_u32 s53, 27
s_cbranch_scc1 .Lstep1_12
s_cmp_eq_u32 s53, 29
s_cbranch_scc1 .Lstep1_14
s_cmp_eq_u32 s53, 31
s_cbranch_scc1 .Lstep1_16
s_cmp_eq_u32 s53, 33
s_cbranch_scc1 .Lstep2_2
s_cmp_eq_u32 s53, 35
s_cbranch_scc1 .Lstep2_4
s_cmp_eq_u32 s53, 37
s_cbranch_scc1 .Lstep2_6
s_cmp_eq_u32 s53, 39
s_cbranch_scc1 .Lstep2_8
s_cmp_eq_u32 s53, 41
s_cbranch_scc1 .Lstep2_10
s_cmp_eq_u32 s53, 43
s_cbranch_scc1 .Lstep2_12
s_cmp_eq_u32 s53, 45
s_cbranch_scc1 .Lstep2_14
s_cmp_eq_u32 s53, 47
s_cbranch_scc1 .Lstep2_16
s_endpgm
.Lxs2m:
s_sleep 5
s_branch .Lxs2m_back
.Lxs2e:
s_sleep 5
s_branch .Lxs2e_back
.Lxs3m:
s_sleep 5
s_branch .Lxs3m_back
.Lxs3e:
s_sleep 5
s_branch .Lxs3e_back
.Lxs14m:
s_sleep 7
s_branch .Lxs14m_back
.Lxs14e:
s_sleep 7
s_branch .Lxs14e_back
.Lxs15m:
s_sleep 7
s_branch .Lxs15m_back
.Lxs15e:
s_sleep 7
s_branch .Lxs15e_back
.Lqsl2:
s_sleep 2
s_branch .Lqsl2_back
.Lqsl4:
s_sleep 2
s_branch .Lqsl4_back
.Lqsl6:
s_sleep 2
s_branch .Lqsl6_back
.Lqsl8:
s_sleep 2
s_branch .Lqsl8_back
.Lqsl10:
s_sleep 2
s_branch .Lqsl10_back
.Lqsl12:
s_sleep 2
s_branch .Lqsl12_back
.Lqsl14:
s_sleep 2
s_branch .Lqsl14_back
.Lpace1:
S_ s12,s12,s13
.Lpace1_l:
s_memrealtime s[100:101]
W_ 0
s_sub_u32 s100, s100, s98
s_cmp_ge_u32 s100, s12
s_cbranch_scc1 .Lpace1_back
s_sleep 2
s_branch .Lpace1_l
.Lpace3:
S_ s12,s12,s13
.Lpace3_l:
s_memrealtime s[100:101]
W_ 0
s_sub_u32 s100, s100, s98
s_cmp_ge_u32 s100, s12
s_cbranch_scc1 .Lpace3_back
s_sleep 2
s_branch .Lpace3_l
.Lpace5:
S_ s12,s12,s13
.Lpace5_l:
s_memrealtime s[100:101]
W_ 0
s_sub_u32 s100, s100, s98
s_cmp_ge_u32 s100, s12
s_cbranch_scc1 .Lpace5_back
s_sleep 2
s_branch .Lpace5_l
.Lpace7:
S_ s12,s12,s13
.Lpace7_l:
s_memrealtime s[100:101]
W_ 0
s_sub_u32 s100, s100, s98
s_cmp_ge_u32 s100, s12
s_cbranch_scc1 .Lpace7_back
s_sleep 2
s_branch .Lpace7_l
.Lpace9:
S_ s12,s12,s13
.Lpace9_l:
s_memrealtime s[100:101]
W_ 0
s_sub_u32 s100, s100, s98
s_cmp_ge_u32 s100, s12
s_cbranch_scc1 .Lpace9_back
s_sleep 2
s_branch .Lpace9_l
.Lpace11:
S_ s12,s12,s13
.Lpace11_l:
s_memrealtime s[100:101]
W_ 0
s_sub_u32 s100, s100, s98
s_cmp_ge_u32 s100, s12
s_cbranch_scc1 .Lpace11_back
s_sleep 2
s_branch .Lpace11_l
.Lpace13:
S_ s12,s12,s13
.Lpace13_l:
s_memrealtime s[100:101]
W_ 0
s_sub_u32 s100, s100, s98
s_cmp_ge_u32 s100, s12
s_cbranch_scc1 .Lpace13_back
s_sleep 2
s_branch .Lpace13_l
.Lpace15:
S_ s12,s12,s13
.Lpace15_l:
s_memrealtime s[100:101]
W_ 0
s_sub_u32 s100, s100, s98
s_cmp_ge_u32 s100, s12
s_cbranch_scc1 .Lpace15_back
s_sleep 2
s_branch .Lpace15_l
.Lpace17:
S_ s12,s12,s13
.Lpace17_l:
s_memrealtime s[100:101]
W_ 0
s_sub_u32 s100, s100, s98
s_cmp_ge_u32 s100, s12
s_cbranch_scc1 .Lpace17_back
s_sleep 2
s_branch .Lpace17_l
.Lpace19:
S_ s12,s12,s13
.Lpace19_l:
s_memrealtime s[100:101]
W_ 0
s_sub_u32 s100, s100, s98
s_cmp_ge_u32 s100, s12
s_cbranch_scc1 .Lpace19_back
s_sleep 2
s_branch .Lpace19_l
.Lpace21:
S_ s12,s12,s13
.Lpace21_l:
s_memrealtime s[100:101]
W_ 0
s_sub_u32 s100, s100, s98
s_cmp_ge_u32 s100, s12
s_cbranch_scc1 .Lpace21_back
s_sleep 2
s_branch .Lpace21_l
.Lpace23:
S_ s12,s12,s13
.Lpace23_l:
s_memrealtime s[100:101]
W_ 0
s_sub_u32 s100, s100, s98
s_cmp_ge_u32 s100, s12
s_cbranch_scc1 .Lpace23_back
s_sleep 2
s_branch .Lpace23_l
.Lpace25:
S_ s12,s12,s13
.Lpace25_l:
s_memrealtime s[100:101]
W_ 0
s_sub_u32 s100, s100, s98
s_cmp_ge_u32 s100, s12
s_cbranch_scc1 .Lpace25_back
s_sleep 2
s_branch .Lpace25_l
.Lpace27:
S_ s12,s12,s13
.Lpace27_l:
s_memrealtime s[100:101]
W_ 0
s_sub_u32 s100, s100, s98
s_cmp_ge_u32 s100, s12
s_cbranch_scc1 .Lpace27_back
s_sleep 2
s_branch .Lpace27_l
.Lpace29:
S_ s12,s12,s13
.Lpace29_l:
s_memrealtime s[100:101]
W_ 0
s_sub_u32 s100, s100, s98
s_cmp_ge_u32 s100, s12
s_cbranch_scc1 .Lpace29_back
s_sleep 2
s_branch .Lpace29_l
.Lhsd0_0:
s_bitcmp1_b32 s67, 13
s_cbranch_scc1 .Lhs_a0
s_bitcmp1_b32 s67, 11
s_cbranch_scc1 .Lhs_c0
s_branch .Lhs_d0
.Lhs_a0:
s_nop 4
s_waitcnt vmcnt(12)
R_ v10,a251
buffer_store_dword a255, v10, s[92:95], 0 offen offset:128 sc1
s_branch .Lhsd0_0_back
.Lhs_a1:
s_nop 4
R_ v11,a248
v_lshrrev_b32 v11, 2, v11
buffer_load_dword a254, v11, s[92:95], s96 offen offset:128 sc1
s_branch .Lhs_a1_back
.Lhs_a2:
s_nop 4
s_waitcnt vmcnt(12)
R_ v10,a251
buffer_store_dword a255, v10, s[92:95], 0 offen offset:128 sc1
s_branch .Lhs_a2_back
.Lhs_b1:
s_nop 4
s_waitcnt vmcnt(12)
R_ v11,a254
v_cmp_eq_u32 vcc, s62, v11
R_ v10,a251
v_mov_b32 v11, 0x7fff0000
v_cndmask_b32 v10, v11, v10, vcc
R_ v11,a253
v_add_u32 v11, 1, v11
buffer_store_dword v11, v10, s[92:95], 0 offen offset:256 sc1
s_and_b32 s53, vcc_lo, 255
s_cmp_eq_u32 s53, 0xFF
s_cselect_b32 s97, 1, 0
s_branch .Lhs_b1_back
.Lhs_c0:
s_nop 4
R_ v11,a248
v_lshrrev_b32 v11, 2, v11
buffer_load_dword a254, v11, s[92:95], s96 offen offset:128 sc1
s_branch .Lhsd0_0_back
.Lhs_c1:
s_nop 4
R_ v11,a248
v_lshrrev_b32 v11, 2, v11
buffer_load_dword a254, v11, s[92:95], s96 offen offset:256 sc1
s_branch .Lhs_c1_back
.Lhs_c2:
s_nop 4
R_ v11,a248
v_lshrrev_b32 v11, 2, v11
buffer_load_dword a254, v11, s[92:95], s96 offen offset:128 sc1
s_branch .Lhs_c2_back
.Lhs_d0:
s_nop 4
s_waitcnt vmcnt(12)
R_ v11,a254
v_cmp_eq_u32 vcc, s62, v11
R_ v10,a251
v_mov_b32 v11, 0x7fff0000
v_cndmask_b32 v10, v11, v10, vcc
R_ v11,a253
v_add_u32 v11, 1, v11
buffer_store_dword v11, v10, s[92:95], 0 offen offset:256 sc1
s_and_b32 s53, vcc_lo, 255
s_cmp_eq_u32 s53, 0xFF
s_cselect_b32 s97, 1, 0
s_branch .Lhsd0_0_back
.Lhs_d2:
s_nop 4
s_waitcnt vmcnt(12)
R_ v11,a254
v_cmp_eq_u32 vcc, s62, v11
R_ v10,a251
v_mov_b32 v11, 0x7fff0000
v_cndmask_b32 v10, v11, v10, vcc
R_ v11,a253
v_add_u32 v11, 1, v11
buffer_store_dword v11, v10, s[92:95], 0 offen offset:256 sc1
s_and_b32 s53, vcc_lo, 255
s_cmp_eq_u32 s53, 0xFF
s_cselect_b32 s97, 1, 0
s_branch .Lhs_d2_back
.Lhs_d1:
s_nop 4
s_waitcnt vmcnt(12)
R_ v11,a254
R_ v10,a252
v_cmp_ne_u32 vcc, v11, v10
s_nop 1
s_and_b32 s53, vcc_lo, 255
s_cmp_eq_u32 s53, 0xFF
s_cselect_b32 s53, s97, 0
s_cmp_eq_u32 s53, 1
s_cbranch_scc1 .Lhs_ok
.Lhs_spin:
s_cmp_eq_u32 s57, 0
s_cbranch_scc1 .Lhs_ok
s_sub_u32 s57, s57, 1
s_sleep 8
R_ v10,a251
s_lshl_b32 s53, s6, 2
v_subrev_u32 v10, s53, v10
buffer_load_dword v11, v10, s[92:95], 0 offen offset:0 sc1
R_ v10,a248
v_lshrrev_b32 v10, 2, v10
buffer_load_dword v12, v10, s[92:95], s96 offen offset:128 sc1
buffer_load_dword v13, v10, s[92:95], s96 offen offset:256 sc1
s_waitcnt vmcnt(0)
X_ a255,v11
R_ v10,a251
buffer_store_dword v11, v10, s[92:95], 0 offen offset:128 sc1
s_nop 1
v_cmp_eq_u32 vcc, s62, v12
R_ v10,a251
v_mov_b32 v12, 0x7fff0000
v_cndmask_b32 v10, v12, v10, vcc
R_ v12,a253
v_add_u32 v12, 1, v12
buffer_store_dword v12, v10, s[92:95], 0 offen offset:256 sc1
s_and_b32 s53, vcc_lo, 255
s_cmp_eq_u32 s53, 0xFF
s_cselect_b32 s97, 1, 0
s_nop 1
R_ v10,a252
v_cmp_ne_u32 vcc, v13, v10
s_nop 1
s_and_b32 s53, vcc_lo, 255
s_cmp_eq_u32 s53, 0xFF
s_cselect_b32 s53, s97, 0
s_cmp_eq_u32 s53, 1
s_cbranch_scc0 .Lhs_spin
.Lhs_ok:
s_branch .Lhs_d1_back
.Latt2:
R_ v13,a248
S_ s53,s51,36864
v_add_u32 v12, s53, v13
s_mul_i32 s53, s4, 10240
S_ s53,s53,0
v_add_u32 v13, s53, v13
s_cmp_eq_u32 s4, 0
s_cbranch_scc0 .Lhs_tok2
R_ v11,a241
.Lpad_a2_0:
s_cbranch_execz .Lpad_a2_4
buffer_load_dword a255, v11, s[92:95], 0 offen offset:0 sc1
.Lhs_tok2:
W_ 2
Zvvv 96,30,136
Dv 42,v1,44032
R_ v2,a245
Zvvv 100,30,140
ds_write_b128 v13, a[0:3] offset:0
Zvvv 104,30,144
v_xor_b32 v3, 32, v2
v_xor_b32 v4, 64, v2
Zvvv 108,30,148
ds_write_b128 v13, a[4:7] offset:1024
Zvvv 112,30,152
W_ 4
v_xor_b32 v5, 96, v2
s_mov_b64 s[24:25], s[16:17]
Mvvv 96,34,156
s_mov_b32 s26, 7864320
Dv 30,%0,45056
s_mov_b64 s[28:29], s[22:23]
Mvvv 100,34,160
ds_write_b128 v13, a[8:11] offset:2048
Mvvv 104,34,164
s_mov_b32 s30, 1638400
R_ v20,a80
s_mul_i32 s52, s4, 10240
Mvvv 108,34,168
ds_write_b128 v13, a[12:15] offset:3072
Mvvv 112,34,172
W_ 6
R_ v21,a81
R_ v22,a82
Mvvv 96,38,176
Dv 34,v1,45056
R_ v23,a83
Mvvv 100,38,180
ds_write_b128 v13, a[16:19] offset:4096
Mvvv 104,38,184
R_ v24,a84
R_ v25,a85
Mvvv 108,38,188
ds_write_b128 v13, a[40:43] offset:5120
Mvvv 112,38,192
W_ 8
R_ v26,a86
R_ v27,a87
Mvvv 96,42,196
Dv 38,%0,36864
R_ v52,a88
Mvvv 100,42,200
ds_write_b128 v13, a[44:47] offset:6144
Mvvv 104,42,204
R_ v53,a89
R_ v54,a90
Mvvv 108,42,208
ds_write_b128 v13, a[48:51] offset:7168
Mvvv 112,42,212
W_ 8
R_ v55,a91
v_accvgpr_mov_b32 a240, a92
Mvvv 96,30,216
Dv 42,v1,36864
v_accvgpr_mov_b32 a241, a93
Mvvv 100,30,220
ds_write_b128 v13, a[52:55] offset:8192
Mvvv 104,30,224
v_accvgpr_mov_b32 a242, a94
v_accvgpr_mov_b32 a243, a95
Mvvv 108,30,228
ds_write_b128 v13, a[56:59] offset:9216
Mvvv 112,30,232
W_ 8
v_accvgpr_mov_b32 a244, a96
v_accvgpr_mov_b32 a245, a97
Mvvv 96,34,236
Dv 30,%0,37888
v_accvgpr_mov_b32 a246, a98
Mvvv 100,34,240
v_accvgpr_mov_b32 a247, a99
R_ v16,a120
Mvvv 104,34,244
R_ v17,a121
R_ v18,a122
Mvvv 108,34,248
R_ v19,a123
R_ v28,a124
Mvvv 112,34,252
W_ 6
R_ v29,a125
R_ v51,a126
Zvvv 76,38,136
Dv 34,v1,37888
R_ v6,a127
Zvvv 80,38,140
R_ v14,a128
R_ v15,a129
Zvvv 84,38,144
v_accvgpr_mov_b32 a249, a130
v_accvgpr_mov_b32 a250, a131
Zvvv 88,38,148
v_max3_f32 v56, v96, v97, v98
v_max_f32 v56, v56, v99
Zvvv 92,38,152
W_ 4
v_max3_f32 v60, v100, v101, v102
v_max_f32 v60, v60, v103
Mvvv 76,42,156
Dv 38,%0,38912
v_max3_f32 v64, v104, v105, v106
Mvvv 80,42,160
v_max_f32 v64, v64, v107
Mvvv 84,42,164
v_max3_f32 v68, v108, v109, v110
v_max_f32 v68, v68, v111
Mvvv 88,42,168
v_max3_f32 v72, v112, v113, v114
v_max_f32 v72, v72, v115
Mvvv 92,42,172
W_ 2
Mvvv 76,30,176
Dv 42,v1,38912
Mvvv 80,30,180
Mvvv 84,30,184
Mvvv 88,30,188
Mvvv 92,30,192
s_waitcnt vmcnt(30) lgkmcnt(0)
s_barrier
Mvvv 76,34,196
Dv 30,%0,55296
Mvvv 76,38,216
Mvvv 76,42,236
Mvvv 80,34,200
Mvvv 80,38,220
Mvvv 80,42,240
Mvvv 84,34,204
s_nop 3
v_max3_f32 v56, v56, v76, v77
v_max3_f32 v56, v56, v78, v79
Mvvv 84,38,224
Mvvv 84,42,244
Mvvv 88,34,208
v_max3_f32 v60, v60, v80, v81
v_max3_f32 v60, v60, v82, v83
Mvvv 88,38,228
Mvvv 88,42,248
Mvvv 92,34,212
s_nop 1
v_max3_f32 v64, v64, v84, v85
v_max3_f32 v64, v64, v86, v87
Mvvv 92,38,232
Mvvv 92,42,252
Dv 34,v1,55296
.p2align 3
v_mov_b32 v11, 0x3f803f80
v_permlane32_swap_b32 v56, v60
v_max3_f32 v68, v68, v88, v89
v_max3_f32 v68, v68, v90, v91
v_max_f32 v56, v56, v60
s_nop 1
v_max3_f32 v72, v72, v92, v93
v_max3_f32 v72, v72, v94, v95
v_mov_b32 v73, v72
v_permlane32_swap_b32 v64, v68
v_max_f32 v64, v64, v68
v_permlane32_swap_b32 v72, v73
)A"
R"A(v_max_f32 v72, v72, v73
v_mov_b32 v73, v72
v_permlane16_swap_b32 v56, v64
v_max_f32 v56, v56, v64
v_permlane16_swap_b32 v72, v73
B_ v56,0xc0e00000,v56
v_max_f32 v72, v72, v73
v_mov_b32 v64, v56
B_ v72,0xc0e00000,v72
v_mov_b32 v73, v72
v_permlane16_swap_b32 v56, v64
v_mov_b32 v60, v56
v_mov_b32 v68, v64
v_mov_b64 v[74:75], v[72:73]
v_permlane32_swap_b32 v56, v60
v_permlane32_swap_b32 v64, v68
v_mov_b32 v57, v56
v_mov_b32 v61, v60
v_mov_b32 v65, v64
v_mov_b32 v69, v68
v_pk_add_f32 v[96:97], v[96:97], v[56:57]
v_pk_add_f32 v[98:99], v[98:99], v[56:57]
v_pk_add_f32 v[76:77], v[76:77], v[56:57]
v_pk_add_f32 v[78:79], v[78:79], v[56:57]
v_mov_b64 v[58:59], v[56:57]
v_pk_add_f32 v[100:101], v[100:101], v[60:61]
v_pk_add_f32 v[102:103], v[102:103], v[60:61]
v_pk_add_f32 v[80:81], v[80:81], v[60:61]
v_pk_add_f32 v[82:83], v[82:83], v[60:61]
v_mov_b64 v[62:63], v[60:61]
v_pk_add_f32 v[104:105], v[104:105], v[64:65]
v_pk_add_f32 v[106:107], v[106:107], v[64:65]
v_pk_add_f32 v[84:85], v[84:85], v[64:65]
v_pk_add_f32 v[86:87], v[86:87], v[64:65]
v_mov_b64 v[66:67], v[64:65]
v_pk_add_f32 v[108:109], v[108:109], v[68:69]
v_pk_add_f32 v[110:111], v[110:111], v[68:69]
v_pk_add_f32 v[88:89], v[88:89], v[68:69]
v_pk_add_f32 v[90:91], v[90:91], v[68:69]
v_mov_b64 v[70:71], v[68:69]
v_pk_add_f32 v[112:113], v[112:113], v[72:73]
v_pk_add_f32 v[114:115], v[114:115], v[72:73]
v_pk_add_f32 v[92:93], v[92:93], v[72:73]
v_pk_add_f32 v[94:95], v[94:95], v[72:73]
E_ 96
E_ 97
E_ 98
E_ 99
E_ 76
E_ 77
E_ 78
E_ 79
C_ 99,98,99
C_ 98,96,97
C_ 96,76,77
C_ 97,78,79
E_ 100
E_ 101
E_ 102
E_ 103
E_ 80
E_ 81
E_ 82
E_ 83
C_ 103,102,103
C_ 102,100,101
C_ 100,80,81
C_ 101,82,83
.Lstep2_1:
W_ 1
Nvvv 116,30,136,56
s_nop 0
Dv 38,%0,56320
Dv 42,v1,56320
W_ 2
Mvvv 116,34,156
E_ 104
Nvvv 120,30,140,60
E_ 105
Mvvv 120,34,160
E_ 106
Nvvv 124,30,144,64
E_ 107
Mvvv 124,34,164
E_ 84
Nvvv 128,30,148,68
E_ 85
Mvvv 128,34,168
E_ 86
Nvvv 132,30,152,72
E_ 87
Mvvv 132,34,172
W_ 0
C_ 107,106,107
C_ 106,104,105
Mvvv 132,38,192
s_nop 0
Dv 30,%0,57344
Dv 34,v1,57344
Mvvv 132,42,212
C_ 104,84,85
C_ 105,86,87
Mvvv 128,38,188
E_ 108
Mvvv 128,42,208
E_ 109
Mvvv 124,38,184
E_ 110
Mvvv 124,42,204
E_ 111
Mvvv 120,38,180
E_ 88
Mvvv 120,42,200
E_ 89
Mvvv 116,38,176
E_ 90
Mvvv 116,42,196
W_ 0
E_ 91
Mvvv 116,30,216
.Lpad_a2_1:
s_cbranch_execz .Lpad_a2_5
Dv 38,%0,49152
Dv 42,v1,49152
Mvvv 116,34,236
C_ 111,110,111
C_ 110,108,109
Mvvv 120,30,220
C_ 108,88,89
C_ 109,90,91
Mvvv 120,34,240
E_ 112
Mvvv 124,30,224
E_ 113
Mvvv 124,34,244
E_ 114
Mvvv 128,30,228
E_ 115
Mvvv 128,34,248
E_ 92
Mvvv 132,30,232
E_ 93
Mvvv 132,34,252
W_ 0
E_ 94
Nvvv 76,38,136,56
s_nop 0
Dv 30,%0,50176
Dv 34,v1,50176
Mvvv 76,42,156
E_ 95
Nvvv 80,38,140,60
C_ 115,114,115
C_ 114,112,113
Mvvv 80,42,160
C_ 112,92,93
C_ 113,94,95
Nvvv 84,38,144,64
E_ 116
Mvvv 84,42,164
E_ 117
Nvvv 88,38,148,68
E_ 118
Mvvv 88,42,168
E_ 119
Nvvv 92,38,152,72
C_ 119,118,119
C_ 118,116,117
Mvvv 92,42,172
W_ 0
E_ 120
Mvvv 92,30,192
s_nop 0
Dv 38,%0,51200
Dv 42,v1,51200
Mvvv 92,34,212
X_ a180,v11
X_ a181,v11
Mvvv 88,30,188
X_ a182,v11
X_ a183,v11
Mvvv 88,34,208
ds_write_b128 v12, a[132:135] offset:0
Mvvv 84,30,184
E_ 121
Mvvv 84,34,204
E_ 122
Mvvv 80,30,180
ds_write_b128 v12, a[136:139] offset:1024
Mvvv 80,34,200
E_ 123
Mvvv 76,30,176
C_ 123,122,123
C_ 122,120,121
Mvvv 76,34,196
ds_write_b128 v12, a[160:163] offset:2048
W_ 4
Mvvv 76,38,216
W_ 3
E_ 124
Mvvv 76,42,236
s_waitcnt vmcnt(12) lgkmcnt(0)
s_barrier
Mvvv 80,38,220
ds_write_b128 v12, a[164:167] offset:3072
Mvvv 80,42,240
E_ 125
Mvvv 84,38,224
E_ 126
Mvvv 84,42,244
s_nop 0
Tv 30,v2,24576
Tv 32,v2,30720
Mvvv 88,38,228
ds_write_b128 v12, a[168:171] offset:4096
Mvvv 88,42,248
s_nop 0
Tv 34,v3,24576
Tv 36,v3,30720
Mvvv 92,38,232
E_ 127
Mvvv 92,42,252
ds_write_b128 v12, a[172:175] offset:5120
Zaav 220,180,96
C_ 127,126,127
C_ 126,124,125
Zaav 224,180,100
E_ 128
Zaav 228,180,104
ds_write_b128 v12, a[176:179] offset:12288
Zaav 232,180,108
E_ 129
Zaav 236,180,112
W_ 2
E_ 130
Zavv 0,30,96
s_nop 0
Tv 38,v4,24576
Tv 40,v4,30720
Zavv 20,34,96
ds_write_b128 v12, a[200:203] offset:13312
Zavv 24,34,100
s_nop 0
Tv 42,v5,24576
Tv 44,v5,30720
Zavv 4,30,100
E_ 131
Zavv 8,30,104
ds_write_b128 v12, a[204:207] offset:14336
Zavv 28,34,104
R_ v46,a220
R_ v47,a224
Zavv 32,34,108
R_ v48,a228
R_ v49,a232
Zavv 12,30,108
R_ v50,a236
C_ 131,130,131
Zavv 16,30,112
ds_write_b128 v12, a[208:211] offset:15360
Zavv 36,34,112
W_ 2
C_ 130,128,129
Zavv 56,38,112
s_nop 0
Tv 30,v2,25600
Tv 32,v2,31744
Zavv 76,42,112
ds_write_b128 v12, a[212:215] offset:16384
Zavv 72,42,108
s_nop 0
Tv 34,v3,25600
Tv 36,v3,31744
Zavv 52,38,108
E_ 132
Zavv 48,38,104
ds_write_b128 v12, a[216:219] offset:17408
Zavv 68,42,104
E_ 133
Zavv 64,42,100
E_ 134
Zavv 44,38,100
E_ 135
Zavv 40,38,96
C_ 135,134,135
C_ 134,132,133
Zavv 60,42,96
W_ 1
E_ 76
Zavv 80,30,96
s_nop 0
Tv 38,v4,25600
Tv 40,v4,31744
Zavv 100,34,96
E_ 77
Zavv 104,34,100
s_nop 0
Tv 42,v5,25600
Tv 44,v5,31744
Zavv 84,30,100
E_ 78
Zavv 88,30,104
E_ 79
Zavv 108,34,104
C_ 116,76,77
C_ 117,78,79
Zavv 112,34,108
E_ 80
Zavv 92,30,108
E_ 81
Zavv 96,30,112
E_ 82
Zavv 116,34,112
W_ 0
E_ 83
Zavv 136,38,112
s_nop 0
Tv 30,v2,26624
Tv 32,v2,32768
Zavv 156,42,112
C_ 120,80,81
C_ 121,82,83
Zavv 152,42,108
s_nop 0
Tv 34,v3,26624
Tv 36,v3,32768
Zavv 132,38,108
E_ 84
Zavv 128,38,104
E_ 85
Zavv 148,42,104
E_ 86
Zavv 144,42,100
E_ 87
Zavv 124,38,100
C_ 124,84,85
C_ 125,86,87
Zavv 120,38,96
E_ 88
Zavv 140,42,96
W_ 0
E_ 89
Zavv 160,30,96
s_nop 0
Tv 38,v4,26624
Tv 40,v4,32768
Zavv 180,34,96
E_ 90
Zavv 184,34,100
s_nop 0
Tv 42,v5,26624
Tv 44,v5,32768
Zavv 164,30,100
E_ 91
Zavv 168,30,104
C_ 128,88,89
C_ 129,90,91
Zavv 188,34,104
S_ s65,s61,36864
E_ 92
Zavv 192,34,108
S_ m0,s51,24576
E_ 93
Zavv 172,30,108
s_waitcnt vmcnt(6) lgkmcnt(0)
s_barrier
Zavv 176,30,112
Ln v7,32,s65,0
Zavv 196,34,112
E_ 94
Zavv 216,38,112
Dv 30,%0,6144
Zavv 236,42,112
Dv 34,v1,6144
Zavv 232,42,108
Ln v8,32,s65,3072
S_ m0,s51,25472
Zavv 212,38,108
E_ 95
Zavv 208,38,104
Ln v7,32,s65,128
Zavv 228,42,104
C_ 132,92,93
C_ 133,94,95
Zavv 224,42,100
Ln v8,32,s65,3200
S_ m0,s51,26368
Zavv 204,38,100
v_mov_b32 v92, 0x3f803f80
v_mov_b32 v93, 0x3f803f80
Zavv 200,38,96
Ln v7,32,s65,256
Zavv 220,42,96
v_mov_b32 v94, 0x3f803f80
v_mov_b32 v95, 0x3f803f80
Ln v8,32,s65,3328
.Lstep2_2:
s_nop 0
Zvvv 76,92,116
Dv 38,%0,7168
Zvvv 80,92,120
Dv 42,v1,7168
Zvvv 84,92,124
Zvvv 88,92,128
Zvvv 92,92,132
W_ 2
Nvvv 96,30,136,56
Mvvv 96,34,156
Nvvv 100,30,140,60
s_nop 1
v_max3_f32 v10, v76, v80, v84
s_nop 0
v_max3_f32 v10, v10, v88, v92
Mvvv 100,34,160
v_cmp_lt_f32 vcc, 0x42000000, v10
s_cbranch_vccnz .Ltramp2_1
Nvvv 104,30,144,64
A_ v46,v46,v76
A_ v47,v47,v80
Mvvv 104,34,164
A_ v48,v48,v84
A_ v49,v49,v88
Nvvv 108,30,148,68
A_ v50,v50,v92
Mvvv 108,34,168
Nvvv 112,30,152,72
Mvvv 112,34,172
W_ 0
Mvvv 112,38,192
Dv 30,%0,8192
Mvvv 112,42,212
Dv 34,v1,8192
Mvvv 108,38,188
Mvvv 108,42,208
Mvvv 104,38,184
Mvvv 104,42,204
Mvvv 100,38,180
Mvvv 100,42,200
Mvvv 96,38,176
Mvvv 96,42,196
W_ 0
Mvvv 96,30,216
Dv 38,%0,0
Mvvv 96,34,236
Dv 42,v1,0
Mvvv 100,30,220
Mvvv 100,34,240
Mvvv 104,30,224
Mvvv 104,34,244
Mvvv 108,30,228
Mvvv 108,34,248
s_nop 0
E_ 96
Mvvv 112,30,232
E_ 97
Mvvv 112,34,252
W_ 0
E_ 98
Nvvv 76,38,136,56
Dv 30,%0,1024
Mvvv 76,42,156
Dv 34,v1,1024
Nvvv 80,38,140,60
E_ 99
Mvvv 80,42,160
C_ 99,98,99
C_ 98,96,97
Nvvv 84,38,144,64
E_ 100
Mvvv 84,42,164
E_ 101
Nvvv 88,38,148,68
E_ 102
Mvvv 88,42,168
E_ 103
Nvvv 92,38,152,72
C_ 103,102,103
C_ 102,100,101
Mvvv 92,42,172
W_ 0
E_ 104
Mvvv 92,30,192
Dv 38,%0,2048
Mvvv 92,34,212
Dv 42,v1,2048
Mvvv 88,30,188
E_ 105
Mvvv 88,34,208
E_ 106
Mvvv 84,30,184
S_ s65,s61,24576
E_ 107
Mvvv 84,34,204
S_ m0,s51,0
C_ 107,106,107
C_ 106,104,105
Mvvv 80,30,180
s_waitcnt vmcnt(6) lgkmcnt(0)
s_barrier
Mvvv 80,34,200
L_ v9,36,s65,0
Mvvv 76,30,176
E_ 108
Mvvv 76,34,196
L_ v9,36,s65,3072
S_ m0,s51,896
Mvvv 76,38,216
Tv 30,v2,12288
Tv 32,v2,18432
Mvvv 76,42,236
L_ v9,36,s65,128
Mvvv 80,38,220
s_nop 0
Tv 34,v3,12288
Tv 36,v3,18432
Mvvv 80,42,240
L_ v9,36,s65,3200
S_ m0,s51,1792
Mvvv 84,38,224
E_ 109
Mvvv 84,42,244
L_ v9,36,s65,256
Mvvv 88,38,228
E_ 110
Mvvv 88,42,248
L_ v9,36,s65,3328
Mvvv 92,38,232
E_ 111
Mvvv 92,42,252
W_ 0
C_ 111,110,111
C_ 110,108,109
Mavv 0,30,116
s_nop 0
Tv 38,v4,12288
Tv 40,v4,18432
Mavv 20,34,116
E_ 112
Mavv 24,34,120
s_nop 0
Tv 42,v5,12288
Tv 44,v5,18432
Mavv 4,30,120
E_ 113
Mavv 8,30,124
E_ 114
Mavv 28,34,124
E_ 115
Mavv 32,34,128
C_ 115,114,115
C_ 114,112,113
Mavv 12,30,128
E_ 76
Mavv 16,30,132
E_ 77
Mavv 36,34,132
W_ 0
E_ 78
Mavv 56,38,132
s_nop 0
Tv 30,v2,13312
Tv 32,v2,19456
Mavv 76,42,132
E_ 79
Mavv 72,42,128
s_nop 0
Tv 34,v3,13312
Tv 36,v3,19456
Mavv 52,38,128
C_ 96,76,77
C_ 97,78,79
Mavv 48,38,124
E_ 80
Mavv 68,42,124
E_ 81
Mavv 64,42,120
E_ 82
Mavv 44,38,120
E_ 83
Mavv 40,38,116
C_ 100,80,81
C_ 101,82,83
Mavv 60,42,116
W_ 0
E_ 84
Mavv 80,30,116
s_nop 0
Tv 38,v4,13312
Tv 40,v4,19456
Mavv 100,34,116
E_ 85
Mavv 104,34,120
s_nop 0
Tv 42,v5,13312
Tv 44,v5,19456
Mavv 84,30,120
E_ 86
Mavv 88,30,124
E_ 87
Mavv 108,34,124
C_ 104,84,85
C_ 105,86,87
Mavv 112,34,128
E_ 88
Mavv 92,30,128
E_ 89
Mavv 96,30,132
E_ 90
Mavv 116,34,132
W_ 0
E_ 91
Mavv 136,38,132
s_nop 0
Tv 30,v2,14336
Tv 32,v2,20480
Mavv 156,42,132
C_ 108,88,89
C_ 109,90,91
Mavv 152,42,128
s_nop 0
Tv 34,v3,14336
Tv 36,v3,20480
Mavv 132,38,128
E_ 92
Mavv 128,38,124
E_ 93
Mavv 148,42,124
E_ 94
Mavv 144,42,120
E_ 95
Mavv 124,38,120
C_ 112,92,93
C_ 113,94,95
Mavv 120,38,116
v_mov_b32 v92, 0x3f803f80
v_mov_b32 v93, 0x3f803f80
Mavv 140,42,116
W_ 0
v_mov_b32 v94, 0x3f803f80
v_mov_b32 v95, 0x3f803f80
Mavv 160,30,116
s_nop 0
Tv 38,v4,14336
Tv 40,v4,20480
Mavv 180,34,116
Mavv 184,34,120
s_nop 0
Tv 42,v5,14336
Tv 44,v5,20480
Mavv 164,30,120
Mavv 168,30,124
Mavv 188,34,124
S_ s65,s61,49152
Mavv 192,34,128
S_ m0,s51,12288
Mavv 172,30,128
s_waitcnt vmcnt(6) lgkmcnt(0)
s_barrier
Mavv 176,30,132
Ln v7,32,s65,0
Mavv 196,34,132
Mavv 216,38,132
Dv 30,%0,30720
Mavv 236,42,132
Dv 34,v1,30720
Mavv 232,42,128
Ln v8,32,s65,3072
S_ m0,s51,13184
Mavv 212,38,128
Mavv 208,38,124
Ln v7,32,s65,128
Mavv 228,42,124
Mavv 224,42,120
Ln v8,32,s65,3200
S_ m0,s51,14080
Mavv 204,38,120
Mavv 200,38,116
Ln v7,32,s65,256
Mavv 220,42,116
s_cmp_eq_u32 s4, 0
s_cbranch_scc1 .Lhs_a2
.Lhs_a2_back:
.Lstep2_3:
Zvvv 76,92,96
Dv 38,%0,31744
Zvvv 80,92,100
Dv 42,v1,31744
Zvvv 84,92,104
Zvvv 88,92,108
Zvvv 92,92,112
W_ 2
Nvvv 116,30,136,56
Mvvv 116,34,156
Nvvv 120,30,140,60
s_nop 1
v_max3_f32 v10, v76, v80, v84
s_nop 0
v_max3_f32 v10, v10, v88, v92
Mvvv 120,34,160
v_cmp_lt_f32 vcc, 0x42000000, v10
s_cbranch_vccnz .Ltramp2_2
Nvvv 124,30,144,64
A_ v46,v46,v76
A_ v47,v47,v80
Mvvv 124,34,164
A_ v48,v48,v84
A_ v49,v49,v88
Nvvv 128,30,148,68
A_ v50,v50,v92
Mvvv 128,34,168
Ln v8,32,s65,3328
Nvvv 132,30,152,72
Mvvv 132,34,172
W_ 0
Mvvv 132,38,192
Dv 30,%0,32768
Mvvv 132,42,212
.Lpad_a2_2:
s_cbranch_execz .Lpad_a2_6
Dv 34,v1,32768
Mvvv 128,38,188
Mvvv 128,42,208
Mvvv 124,38,184
Mvvv 124,42,204
Mvvv 120,38,180
Mvvv 120,42,200
Mvvv 116,38,176
Mvvv 116,42,196
W_ 0
Mvvv 116,30,216
Dv 38,%0,24576
Mvvv 116,34,236
Dv 42,v1,24576
Mvvv 120,30,220
Mvvv 120,34,240
Mvvv 124,30,224
Mvvv 124,34,244
Mvvv 128,30,228
Mvvv 128,34,248
s_nop 0
E_ 116
Mvvv 132,30,232
E_ 117
Mvvv 132,34,252
W_ 0
E_ 118
Nvvv 76,38,136,56
Dv 30,%0,25600
Mvvv 76,42,156
Dv 34,v1,25600
Nvvv 80,38,140,60
E_ 119
Mvvv 80,42,160
C_ 119,118,119
C_ 118,116,117
Nvvv 84,38,144,64
E_ 120
Mvvv 84,42,164
E_ 121
Nvvv 88,38,148,68
E_ 122
Mvvv 88,42,168
E_ 123
Nvvv 92,38,152,72
C_ 123,122,123
C_ 122,120,121
Mvvv 92,42,172
W_ 0
E_ 124
Mvvv 92,30,192
Dv 38,%0,26624
Mvvv 92,34,212
Dv 42,v1,26624
Mvvv 88,30,188
E_ 125
Mvvv 88,34,208
E_ 126
Mvvv 84,30,184
S_ s65,s61,36864
E_ 127
Mvvv 84,34,204
S_ m0,s51,24576
C_ 127,126,127
C_ 126,124,125
Mvvv 80,30,180
s_waitcnt vmcnt(6) lgkmcnt(0)
s_barrier
Mvvv 80,34,200
L_ v9,36,s65,0
Mvvv 76,30,176
E_ 128
Mvvv 76,34,196
L_ v9,36,s65,3072
S_ m0,s51,25472
Mvvv 76,38,216
Tv 30,v2,0
Tv 32,v2,6144
Mvvv 76,42,236
L_ v9,36,s65,128
Mvvv 80,38,220
s_nop 0
Tv 34,v3,0
Tv 36,v3,6144
Mvvv 80,42,240
L_ v9,36,s65,3200
S_ m0,s51,26368
Mvvv 84,38,224
E_ 129
Mvvv 84,42,244
L_ v9,36,s65,256
Mvvv 88,38,228
E_ 130
Mvvv 88,42,248
L_ v9,36,s65,3328
Mvvv 92,38,232
E_ 131
Mvvv 92,42,252
W_ 0
C_ 131,130,131
C_ 130,128,129
Mavv 0,30,96
s_nop 0
Tv 38,v4,0
Tv 40,v4,6144
Mavv 20,34,96
E_ 132
Mavv 24,34,100
s_nop 0
Tv 42,v5,0
Tv 44,v5,6144
Mavv 4,30,100
E_ 133
Mavv 8,30,104
E_ 134
Mavv 28,34,104
E_ 135
Mavv 32,34,108
C_ 135,134,135
C_ 134,132,133
Mavv 12,30,108
E_ 76
Mavv 16,30,112
E_ 77
Mavv 36,34,112
W_ 0
E_ 78
Mavv 56,38,112
s_nop 0
Tv 30,v2,1024
Tv 32,v2,7168
Mavv 76,42,112
E_ 79
Mavv 72,42,108
s_nop 0
Tv 34,v3,1024
Tv 36,v3,7168
Mavv 52,38,108
C_ 116,76,77
C_ 117,78,79
Mavv 48,38,104
E_ 80
Mavv 68,42,104
E_ 81
Mavv 64,42,100
E_ 82
Mavv 44,38,100
E_ 83
Mavv 40,38,96
C_ 120,80,81
C_ 121,82,83
Mavv 60,42,96
W_ 0
E_ 84
Mavv 80,30,96
s_nop 0
Tv 38,v4,1024
Tv 40,v4,7168
Mavv 100,34,96
E_ 85
Mavv 104,34,100
s_nop 0
Tv 42,v5,1024
Tv 44,v5,7168
Mavv 84,30,100
E_ 86
Mavv 88,30,104
E_ 87
Mavv 108,34,104
C_ 124,84,85
C_ 125,86,87
Mavv 112,34,108
E_ 88
Mavv 92,30,108
E_ 89
Mavv 96,30,112
E_ 90
Mavv 116,34,112
W_ 0
E_ 91
Mavv 136,38,112
s_nop 0
Tv 30,v2,2048
Tv 32,v2,8192
Mavv 156,42,112
C_ 128,88,89
C_ 129,90,91
Mavv 152,42,108
s_nop 0
Tv 34,v3,2048
Tv 36,v3,8192
Mavv 132,38,108
E_ 92
Mavv 128,38,104
E_ 93
Mavv 148,42,104
E_ 94
Mavv 144,42,100
E_ 95
Mavv 124,38,100
C_ 132,92,93
C_ 133,94,95
Mavv 120,38,96
v_mov_b32 v92, 0x3f803f80
v_mov_b32 v93, 0x3f803f80
Mavv 140,42,96
W_ 0
v_mov_b32 v94, 0x3f803f80
v_mov_b32 v95, 0x3f803f80
Mavv 160,30,96
s_nop 0
Tv 38,v4,2048
Tv 40,v4,8192
Mavv 180,34,96
Mavv 184,34,100
s_nop 0
Tv 42,v5,2048
Tv 44,v5,8192
Mavv 164,30,100
Mavv 168,30,104
Mavv 188,34,104
S_ s65,s61,61440
Mavv 192,34,108
S_ m0,s51,0
Mavv 172,30,108
s_waitcnt vmcnt(6) lgkmcnt(0)
s_barrier
Mavv 176,30,112
Ln v7,32,s65,0
Mavv 196,34,112
Mavv 216,38,112
Dv 30,%0,18432
Mavv 236,42,112
Dv 34,v1,18432
Mavv 232,42,108
Ln v8,32,s65,3072
S_ m0,s51,896
Mavv 212,38,108
Mavv 208,38,104
Ln v7,32,s65,128
Mavv 228,42,104
Mavv 224,42,100
Ln v8,32,s65,3200
S_ m0,s51,1792
Mavv 204,38,100
Mavv 200,38,96
Ln v7,32,s65,256
Mavv 220,42,96
.Lstep2_4:
Zvvv 76,92,116
Dv 38,%0,19456
Zvvv 80,92,120
Dv 42,v1,19456
Zvvv 84,92,124
Zvvv 88,92,128
Zvvv 92,92,132
W_ 2
Nvvv 96,30,136,56
Mvvv 96,34,156
Nvvv 100,30,140,60
s_nop 1
v_max3_f32 v10, v76, v80, v84
s_nop 0
v_max3_f32 v10, v10, v88, v92
Mvvv 100,34,160
v_cmp_lt_f32 vcc, 0x42000000, v10
s_cbranch_vccnz .Ltramp2_3
Nvvv 104,30,144,64
A_ v46,v46,v76
A_ v47,v47,v80
Mvvv 104,34,164
A_ v48,v48,v84
A_ v49,v49,v88
Nvvv 108,30,148,68
A_ v50,v50,v92
Mvvv 108,34,168
Ln v8,32,s65,3328
Nvvv 112,30,152,72
Mvvv 112,34,172
W_ 0
Mvvv 112,38,192
Dv 30,%0,20480
Mvvv 112,42,212
Dv 34,v1,20480
Mvvv 108,38,188
Mvvv 108,42,208
Mvvv 104,38,184
Mvvv 104,42,204
Mvvv 100,38,180
Mvvv 100,42,200
Mvvv 96,38,176
Mvvv 96,42,196
W_ 0
Mvvv 96,30,216
Dv 38,%0,12288
Mvvv 96,34,236
Dv 42,v1,12288
Mvvv 100,30,220
Mvvv 100,34,240
Mvvv 104,30,224
Mvvv 104,34,244
Mvvv 108,30,228
Mvvv 108,34,248
s_nop 0
E_ 96
Mvvv 112,30,232
E_ 97
Mvvv 112,34,252
W_ 0
E_ 98
Nvvv 76,38,136,56
Dv 30,%0,13312
Mvvv 76,42,156
Dv 34,v1,13312
Nvvv 80,38,140,60
E_ 99
Mvvv 80,42,160
C_ 99,98,99
C_ 98,96,97
Nvvv 84,38,144,64
E_ 100
Mvvv 84,42,164
E_ 101
Nvvv 88,38,148,68
E_ 102
Mvvv 88,42,168
E_ 103
Nvvv 92,38,152,72
C_ 103,102,103
C_ 102,100,101
Mvvv 92,42,172
W_ 0
E_ 104
Mvvv 92,30,192
Dv 38,%0,14336
Mvvv 92,34,212
Dv 42,v1,14336
Mvvv 88,30,188
E_ 105
Mvvv 88,34,208
E_ 106
Mvvv 84,30,184
S_ s65,s61,49152
E_ 107
Mvvv 84,34,204
S_ m0,s51,12288
C_ 107,106,107
C_ 106,104,105
Mvvv 80,30,180
s_waitcnt vmcnt(6) lgkmcnt(0)
s_barrier
Mvvv 80,34,200
L_ v9,36,s65,0
Mvvv 76,30,176
E_ 108
Mvvv 76,34,196
L_ v9,36,s65,3072
S_ m0,s51,13184
Mvvv 76,38,216
Tv 30,v2,24576
Tv 32,v2,30720
Mvvv 76,42,236
L_ v9,36,s65,128
Mvvv 80,38,220
s_nop 0
Tv 34,v3,24576
Tv 36,v3,30720
Mvvv 80,42,240
L_ v9,36,s65,3200
S_ m0,s51,14080
Mvvv 84,38,224
E_ 109
Mvvv 84,42,244
L_ v9,36,s65,256
Mvvv 88,38,228
E_ 110
Mvvv 88,42,248
L_ v9,36,s65,3328
Mvvv 92,38,232
E_ 111
Mvvv 92,42,252
W_ 0
C_ 111,110,111
C_ 110,108,109
Mavv 0,30,116
s_nop 0
Tv 38,v4,24576
Tv 40,v4,30720
Mavv 20,34,116
E_ 112
Mavv 24,34,120
s_nop 0
Tv 42,v5,24576
Tv 44,v5,30720
Mavv 4,30,120
E_ 113
Mavv 8,30,124
E_ 114
Mavv 28,34,124
E_ 115
Mavv 32,34,128
C_ 115,114,115
C_ 114,112,113
Mavv 12,30,128
E_ 76
Mavv 16,30,132
E_ 77
Mavv 36,34,132
W_ 0
E_ 78
Mavv 56,38,132
s_nop 0
Tv 30,v2,25600
Tv 32,v2,31744
Mavv 76,42,132
E_ 79
Mavv 72,42,128
s_nop 0
Tv 34,v3,25600
Tv 36,v3,31744
Mavv 52,38,128
C_ 96,76,77
C_ 97,78,79
Mavv 48,38,124
E_ 80
Mavv 68,42,124
E_ 81
Mavv 64,42,120
E_ 82
Mavv 44,38,120
E_ 83
Mavv 40,38,116
C_ 100,80,81
C_ 101,82,83
Mavv 60,42,116
W_ 0
E_ 84
Mavv 80,30,116
s_nop 0
Tv 38,v4,25600
Tv 40,v4,31744
Mavv 100,34,116
E_ 85
Mavv 104,34,120
s_nop 0
Tv 42,v5,25600
Tv 44,v5,31744
Mavv 84,30,120
E_ 86
Mavv 88,30,124
E_ 87
Mavv 108,34,124
C_ 104,84,85
C_ 105,86,87
Mavv 112,34,128
E_ 88
Mavv 92,30,128
E_ 89
Mavv 96,30,132
E_ 90
Mavv 116,34,132
W_ 0
E_ 91
Mavv 136,38,132
s_nop 0
Tv 30,v2,26624
Tv 32,v2,32768
Mavv 156,42,132
C_ 108,88,89
C_ 109,90,91
Mavv 152,42,128
s_nop 0
Tv 34,v3,26624
Tv 36,v3,32768
Mavv 132,38,128
E_ 92
Mavv 128,38,124
E_ 93
Mavv 148,42,124
E_ 94
Mavv 144,42,120
E_ 95
Mavv 124,38,120
C_ 112,92,93
C_ 113,94,95
Mavv 120,38,116
v_mov_b32 v92, 0x3f803f80
v_mov_b32 v93, 0x3f803f80
Mavv 140,42,116
W_ 0
v_mov_b32 v94, 0x3f803f80
v_mov_b32 v95, 0x3f803f80
Mavv 160,30,116
s_nop 0
Tv 38,v4,26624
Tv 40,v4,32768
Mavv 180,34,116
Mavv 184,34,120
s_nop 0
Tv 42,v5,26624
Tv 44,v5,32768
Mavv 164,30,120
Mavv 168,30,124
Mavv 188,34,124
S_ s65,s61,73728
Mavv 192,34,128
S_ m0,s51,24576
Mavv 172,30,128
s_waitcnt vmcnt(6) lgkmcnt(0)
s_barrier
Mavv 176,30,132
Ln v7,32,s65,0
Mavv 196,34,132
Mavv 216,38,132
Dv 30,%0,6144
Mavv 236,42,132
Dv 34,v1,6144
Mavv 232,42,128
Ln v8,32,s65,3072
S_ m0,s51,25472
Mavv 212,38,128
Mavv 208,38,124
Ln v7,32,s65,128
Mavv 228,42,124
Mavv 224,42,120
Ln v8,32,s65,3200
S_ m0,s51,26368
Mavv 204,38,120
Mavv 200,38,116
Ln v7,32,s65,256
Mavv 220,42,116
.Lstep2_5:
Zvvv 76,92,96
Dv 38,%0,7168
Zvvv 80,92,100
Dv 42,v1,7168
Zvvv 84,92,104
Zvvv 88,92,108
Zvvv 92,92,112
W_ 2
Nvvv 116,30,136,56
Mvvv 116,34,156
Nvvv 120,30,140,60
s_nop 1
v_max3_f32 v10, v76, v80, v84
s_nop 0
v_max3_f32 v10, v10, v88, v92
Mvvv 120,34,160
v_cmp_lt_f32 vcc, 0x42000000, v10
s_cbranch_vccnz .Ltramp2_4
Nvvv 124,30,144,64
A_ v46,v46,v76
A_ v47,v47,v80
Mvvv 124,34,164
A_ v48,v48,v84
A_ v49,v49,v88
Nvvv 128,30,148,68
A_ v50,v50,v92
Mvvv 128,34,168
Ln v8,32,s65,3328
Nvvv 132,30,152,72
Mvvv 132,34,172
W_ 0
Mvvv 132,38,192
Dv 30,%0,8192
Mvvv 132,42,212
Dv 34,v1,8192
Mvvv 128,38,188
Mvvv 128,42,208
Mvvv 124,38,184
Mvvv 124,42,204
Mvvv 120,38,180
Mvvv 120,42,200
Mvvv 116,38,176
Mvvv 116,42,196
W_ 0
Mvvv 116,30,216
Dv 38,%0,0
Mvvv 116,34,236
Dv 42,v1,0
Mvvv 120,30,220
Mvvv 120,34,240
Mvvv 124,30,224
Mvvv 124,34,244
Mvvv 128,30,228
Mvvv 128,34,248
s_nop 0
E_ 116
Mvvv 132,30,232
.Lpad_a2_3:
s_cbranch_execz .Lpad_a2_7
E_ 117
Mvvv 132,34,252
W_ 0
E_ 118
Nvvv 76,38,136,56
Dv 30,%0,1024
Mvvv 76,42,156
Dv 34,v1,1024
Nvvv 80,38,140,60
E_ 119
Mvvv 80,42,160
C_ 119,118,119
C_ 118,116,117
Nvvv 84,38,144,64
E_ 120
Mvvv 84,42,164
E_ 121
Nvvv 88,38,148,68
E_ 122
Mvvv 88,42,168
E_ 123
Nvvv 92,38,152,72
C_ 123,122,123
C_ 122,120,121
Mvvv 92,42,172
W_ 0
E_ 124
Mvvv 92,30,192
Dv 38,%0,2048
Mvvv 92,34,212
Dv 42,v1,2048
Mvvv 88,30,188
E_ 125
Mvvv 88,34,208
E_ 126
Mvvv 84,30,184
S_ s65,s61,61440
E_ 127
Mvvv 84,34,204
S_ m0,s51,0
C_ 127,126,127
C_ 126,124,125
Mvvv 80,30,180
s_waitcnt vmcnt(6) lgkmcnt(0)
s_barrier
Mvvv 80,34,200
L_ v9,36,s65,0
Mvvv 76,30,176
E_ 128
Mvvv 76,34,196
L_ v9,36,s65,3072
S_ m0,s51,896
Mvvv 76,38,216
Tv 30,v2,12288
Tv 32,v2,18432
Mvvv 76,42,236
L_ v9,36,s65,128
Mvvv 80,38,220
s_nop 0
Tv 34,v3,12288
Tv 36,v3,18432
Mvvv 80,42,240
L_ v9,36,s65,3200
S_ m0,s51,1792
Mvvv 84,38,224
E_ 129
Mvvv 84,42,244
L_ v9,36,s65,256
Mvvv 88,38,228
E_ 130
Mvvv 88,42,248
L_ v9,36,s65,3328
Mvvv 92,38,232
E_ 131
Mvvv 92,42,252
W_ 0
C_ 131,130,131
C_ 130,128,129
Mavv 0,30,96
s_nop 0
Tv 38,v4,12288
Tv 40,v4,18432
Mavv 20,34,96
E_ 132
Mavv 24,34,100
s_nop 0
Tv 42,v5,12288
Tv 44,v5,18432
Mavv 4,30,100
E_ 133
Mavv 8,30,104
E_ 134
Mavv 28,34,104
E_ 135
Mavv 32,34,108
C_ 135,134,135
C_ 134,132,133
Mavv 12,30,108
E_ 76
Mavv 16,30,112
E_ 77
Mavv 36,34,112
W_ 0
E_ 78
Mavv 56,38,112
s_nop 0
Tv 30,v2,13312
Tv 32,v2,19456
Mavv 76,42,112
E_ 79
Mavv 72,42,108
s_nop 0
Tv 34,v3,13312
Tv 36,v3,19456
Mavv 52,38,108
C_ 116,76,77
C_ 117,78,79
Mavv 48,38,104
E_ 80
Mavv 68,42,104
E_ 81
Mavv 64,42,100
E_ 82
Mavv 44,38,100
E_ 83
Mavv 40,38,96
C_ 120,80,81
C_ 121,82,83
Mavv 60,42,96
W_ 0
E_ 84
Mavv 80,30,96
s_nop 0
Tv 38,v4,13312
Tv 40,v4,19456
Mavv 100,34,96
E_ 85
Mavv 104,34,100
s_nop 0
Tv 42,v5,13312
Tv 44,v5,19456
Mavv 84,30,100
E_ 86
Mavv 88,30,104
E_ 87
Mavv 108,34,104
C_ 124,84,85
C_ 125,86,87
Mavv 112,34,108
E_ 88
Mavv 92,30,108
E_ 89
Mavv 96,30,112
E_ 90
Mavv 116,34,112
W_ 0
E_ 91
Mavv 136,38,112
s_nop 0
Tv 30,v2,14336
Tv 32,v2,20480
Mavv 156,42,112
C_ 128,88,89
C_ 129,90,91
Mavv 152,42,108
s_nop 0
Tv 34,v3,14336
Tv 36,v3,20480
Mavv 132,38,108
E_ 92
Mavv 128,38,104
E_ 93
Mavv 148,42,104
E_ 94
Mavv 144,42,100
E_ 95
Mavv 124,38,100
C_ 132,92,93
C_ 133,94,95
Mavv 120,38,96
v_mov_b32 v92, 0x3f803f80
v_mov_b32 v93, 0x3f803f80
Mavv 140,42,96
W_ 0
v_mov_b32 v94, 0x3f803f80
v_mov_b32 v95, 0x3f803f80
Mavv 160,30,96
s_nop 0
Tv 38,v4,14336
Tv 40,v4,20480
Mavv 180,34,96
Mavv 184,34,100
s_nop 0
Tv 42,v5,14336
Tv 44,v5,20480
Mavv 164,30,100
Mavv 168,30,104
Mavv 188,34,104
S_ s65,s61,86016
Mavv 192,34,108
S_ m0,s51,12288
Mavv 172,30,108
s_waitcnt vmcnt(6) lgkmcnt(0)
s_barrier
Mavv 176,30,112
Ln v7,32,s65,0
Mavv 196,34,112
Mavv 216,38,112
Dv 30,%0,30720
Mavv 236,42,112
Dv 34,v1,30720
Mavv 232,42,108
Ln v8,32,s65,3072
S_ m0,s51,13184
Mavv 212,38,108
Mavv 208,38,104
Ln v7,32,s65,128
Mavv 228,42,104
Mavv 224,42,100
Ln v8,32,s65,3200
S_ m0,s51,14080
Mavv 204,38,100
Mavv 200,38,96
Ln v7,32,s65,256
Mavv 220,42,96
.Lstep2_6:
Zvvv 76,92,116
Dv 38,%0,31744
Zvvv 80,92,120
Dv 42,v1,31744
Zvvv 84,92,124
Zvvv 88,92,128
Zvvv 92,92,132
W_ 2
Nvvv 96,30,136,56
Mvvv 96,34,156
Nvvv 100,30,140,60
s_nop 1
v_max3_f32 v10, v76, v80, v84
s_nop 0
v_max3_f32 v10, v10, v88, v92
Mvvv 100,34,160
v_cmp_lt_f32 vcc, 0x42000000, v10
s_cbranch_vccnz .Ltramp2_5
Nvvv 104,30,144,64
A_ v46,v46,v76
A_ v47,v47,v80
Mvvv 104,34,164
A_ v48,v48,v84
A_ v49,v49,v88
Nvvv 108,30,148,68
A_ v50,v50,v92
Mvvv 108,34,168
Ln v8,32,s65,3328
Nvvv 112,30,152,72
R_ v11,a248
Mvvv 112,34,172
W_ 0
v_min_u32 v11, 240, v11
Mvvv 112,38,192
Dv 30,%0,32768
v_lshlrev_b32 v11, 3, v11
Mvvv 112,42,212
Dv 34,v1,32768
Mvvv 108,38,188
buffer_load_dword a254, v11, s[44:47], s66 offen
Mvvv 108,42,208
Mvvv 104,38,184
Mvvv 104,42,204
Mvvv 100,38,180
Mvvv 100,42,200
Mvvv 96,38,176
Mvvv 96,42,196
W_ 0
Mvvv 96,30,216
Dv 38,%0,24576
Mvvv 96,34,236
Dv 42,v1,24576
Mvvv 100,30,220
Mvvv 100,34,240
Mvvv 104,30,224
Mvvv 104,34,244
Mvvv 108,30,228
Mvvv 108,34,248
s_nop 0
E_ 96
Mvvv 112,30,232
E_ 97
Mvvv 112,34,252
W_ 0
E_ 98
Nvvv 76,38,136,56
Dv 30,%0,25600
Mvvv 76,42,156
Dv 34,v1,25600
Nvvv 80,38,140,60
E_ 99
Mvvv 80,42,160
C_ 99,98,99
C_ 98,96,97
Nvvv 84,38,144,64
E_ 100
Mvvv 84,42,164
E_ 101
Nvvv 88,38,148,68
E_ 102
Mvvv 88,42,168
E_ 103
Nvvv 92,38,152,72
C_ 103,102,103
C_ 102,100,101
Mvvv 92,42,172
W_ 0
E_ 104
Mvvv 92,30,192
Dv 38,%0,26624
Mvvv 92,34,212
Dv 42,v1,26624
Mvvv 88,30,188
E_ 105
Mvvv 88,34,208
E_ 106
Mvvv 84,30,184
S_ s65,s61,73728
E_ 107
Mvvv 84,34,204
S_ m0,s51,24576
C_ 107,106,107
C_ 106,104,105
Mvvv 80,30,180
s_waitcnt vmcnt(7) lgkmcnt(0)
s_barrier
Mvvv 80,34,200
L_ v9,36,s65,0
Mvvv 76,30,176
E_ 108
Mvvv 76,34,196
L_ v9,36,s65,3072
S_ m0,s51,25472
Mvvv 76,38,216
Tv 30,v2,0
Tv 32,v2,6144
Mvvv 76,42,236
L_ v9,36,s65,128
Mvvv 80,38,220
s_nop 0
Tv 34,v3,0
Tv 36,v3,6144
Mvvv 80,42,240
L_ v9,36,s65,3200
S_ m0,s51,26368
Mvvv 84,38,224
E_ 109
Mvvv 84,42,244
L_ v9,36,s65,256
Mvvv 88,38,228
E_ 110
Mvvv 88,42,248
L_ v9,36,s65,3328
Mvvv 92,38,232
E_ 111
Mvvv 92,42,252
W_ 0
C_ 111,110,111
C_ 110,108,109
Mavv 0,30,116
s_nop 0
Tv 38,v4,0
Tv 40,v4,6144
Mavv 20,34,116
E_ 112
Mavv 24,34,120
s_nop 0
Tv 42,v5,0
Tv 44,v5,6144
Mavv 4,30,120
E_ 113
Mavv 8,30,124
E_ 114
Mavv 28,34,124
E_ 115
Mavv 32,34,128
C_ 115,114,115
C_ 114,112,113
Mavv 12,30,128
E_ 76
Mavv 16,30,132
E_ 77
Mavv 36,34,132
W_ 0
E_ 78
Mavv 56,38,132
s_nop 0
Tv 30,v2,1024
Tv 32,v2,7168
Mavv 76,42,132
E_ 79
Mavv 72,42,128
s_nop 0
Tv 34,v3,1024
Tv 36,v3,7168
Mavv 52,38,128
C_ 96,76,77
C_ 97,78,79
Mavv 48,38,124
E_ 80
Mavv 68,42,124
E_ 81
Mavv 64,42,120
E_ 82
Mavv 44,38,120
E_ 83
Mavv 40,38,116
C_ 100,80,81
C_ 101,82,83
Mavv 60,42,116
W_ 0
E_ 84
Mavv 80,30,116
s_nop 0
Tv 38,v4,1024
Tv 40,v4,7168
Mavv 100,34,116
E_ 85
Mavv 104,34,120
s_nop 0
Tv 42,v5,1024
Tv 44,v5,7168
Mavv 84,30,120
E_ 86
Mavv 88,30,124
E_ 87
Mavv 108,34,124
C_ 104,84,85
C_ 105,86,87
Mavv 112,34,128
E_ 88
Mavv 92,30,128
E_ 89
Mavv 96,30,132
E_ 90
Mavv 116,34,132
W_ 0
E_ 91
Mavv 136,38,132
s_nop 0
Tv 30,v2,2048
Tv 32,v2,8192
Mavv 156,42,132
C_ 108,88,89
C_ 109,90,91
Mavv 152,42,128
s_nop 0
Tv 34,v3,2048
Tv 36,v3,8192
Mavv 132,38,128
E_ 92
Mavv 128,38,124
E_ 93
Mavv 148,42,124
E_ 94
Mavv 144,42,120
E_ 95
Mavv 124,38,120
C_ 112,92,93
C_ 113,94,95
Mavv 120,38,116
v_mov_b32 v92, 0x3f803f80
v_mov_b32 v93, 0x3f803f80
Mavv 140,42,116
W_ 0
v_mov_b32 v94, 0x3f803f80
v_mov_b32 v95, 0x3f803f80
Mavv 160,30,116
s_nop 0
Tv 38,v4,2048
Tv 40,v4,8192
Mavv 180,34,116
Mavv 184,34,120
s_nop 0
Tv 42,v5,2048
Tv 44,v5,8192
Mavv 164,30,120
Mavv 168,30,124
Mavv 188,34,124
S_ s65,s61,98304
Mavv 192,34,128
S_ m0,s51,0
Mavv 172,30,128
s_waitcnt vmcnt(7) lgkmcnt(0)
s_barrier
Mavv 176,30,132
Ln v7,32,s65,0
Mavv 196,34,132
Mavv 216,38,132
Dv 30,%0,18432
Mavv 236,42,132
Dv 34,v1,18432
Mavv 232,42,128
Ln v8,32,s65,3072
S_ m0,s51,896
Mavv 212,38,128
Mavv 208,38,124
Ln v7,32,s65,128
Mavv 228,42,124
Mavv 224,42,120
Ln v8,32,s65,3200
S_ m0,s51,1792
Mavv 204,38,120
Mavv 200,38,116
Ln v7,32,s65,256
Mavv 220,42,116
s_cmp_eq_u32 s4, 0
s_cbranch_scc1 .Lhs_c2
.Lhs_c2_back:
.Lstep2_7:
Zvvv 76,92,96
Dv 38,%0,19456
Zvvv 80,92,100
Dv 42,v1,19456
Zvvv 84,92,104
Zvvv 88,92,108
Zvvv 92,92,112
W_ 2
Nvvv 116,30,136,56
Mvvv 116,34,156
Nvvv 120,30,140,60
s_nop 1
v_max3_f32 v10, v76, v80, v84
s_nop 0
v_max3_f32 v10, v10, v88, v92
Mvvv 120,34,160
v_cmp_lt_f32 vcc, 0x42000000, v10
s_cbranch_vccnz .Ltramp2_6
Nvvv 124,30,144,64
A_ v46,v46,v76
A_ v47,v47,v80
Mvvv 124,34,164
A_ v48,v48,v84
A_ v49,v49,v88
Nvvv 128,30,148,68
A_ v50,v50,v92
Mvvv 128,34,168
Ln v8,32,s65,3328
Nvvv 132,30,152,72
Mvvv 132,34,172
W_ 0
Mvvv 132,38,192
Dv 30,%0,20480
Mvvv 132,42,212
Dv 34,v1,20480
Mvvv 128,38,188
Mvvv 128,42,208
Mvvv 124,38,184
Mvvv 124,42,204
Mvvv 120,38,180
Mvvv 120,42,200
Mvvv 116,38,176
Mvvv 116,42,196
W_ 0
Mvvv 116,30,216
Dv 38,%0,12288
Mvvv 116,34,236
Dv 42,v1,12288
Mvvv 120,30,220
Mvvv 120,34,240
Mvvv 124,30,224
Mvvv 124,34,244
Mvvv 128,30,228
Mvvv 128,34,248
s_nop 0
E_ 116
Mvvv 132,30,232
E_ 117
Mvvv 132,34,252
W_ 0
E_ 118
Nvvv 76,38,136,56
Dv 30,%0,13312
Mvvv 76,42,156
Dv 34,v1,13312
Nvvv 80,38,140,60
E_ 119
Mvvv 80,42,160
C_ 119,118,119
C_ 118,116,117
Nvvv 84,38,144,64
E_ 120
Mvvv 84,42,164
E_ 121
Nvvv 88,38,148,68
E_ 122
Mvvv 88,42,168
E_ 123
Nvvv 92,38,152,72
.Lpad_a2_4:
s_cbranch_execz .Lhop_a2_r
C_ 123,122,123
C_ 122,120,121
Mvvv 92,42,172
W_ 0
E_ 124
Mvvv 92,30,192
Dv 38,%0,14336
Mvvv 92,34,212
Dv 42,v1,14336
Mvvv 88,30,188
E_ 125
Mvvv 88,34,208
E_ 126
Mvvv 84,30,184
S_ s65,s61,86016
E_ 127
Mvvv 84,34,204
S_ m0,s51,12288
C_ 127,126,127
C_ 126,124,125
Mvvv 80,30,180
s_waitcnt vmcnt(6) lgkmcnt(0)
s_barrier
Mvvv 80,34,200
L_ v9,36,s65,0
Mvvv 76,30,176
E_ 128
Mvvv 76,34,196
L_ v9,36,s65,3072
S_ m0,s51,13184
Mvvv 76,38,216
Tv 30,v2,24576
Tv 32,v2,30720
Mvvv 76,42,236
L_ v9,36,s65,128
Mvvv 80,38,220
s_nop 0
Tv 34,v3,24576
Tv 36,v3,30720
Mvvv 80,42,240
L_ v9,36,s65,3200
S_ m0,s51,14080
Mvvv 84,38,224
E_ 129
Mvvv 84,42,244
L_ v9,36,s65,256
Mvvv 88,38,228
E_ 130
Mvvv 88,42,248
L_ v9,36,s65,3328
Mvvv 92,38,232
E_ 131
Mvvv 92,42,252
W_ 0
C_ 131,130,131
C_ 130,128,129
Mavv 0,30,96
s_nop 0
Tv 38,v4,24576
Tv 40,v4,30720
Mavv 20,34,96
E_ 132
Mavv 24,34,100
s_nop 0
Tv 42,v5,24576
Tv 44,v5,30720
Mavv 4,30,100
E_ 133
Mavv 8,30,104
E_ 134
Mavv 28,34,104
E_ 135
Mavv 32,34,108
C_ 135,134,135
C_ 134,132,133
Mavv 12,30,108
E_ 76
Mavv 16,30,112
E_ 77
Mavv 36,34,112
W_ 0
E_ 78
Mavv 56,38,112
s_nop 0
Tv 30,v2,25600
Tv 32,v2,31744
Mavv 76,42,112
E_ 79
Mavv 72,42,108
s_nop 0
Tv 34,v3,25600
Tv 36,v3,31744
Mavv 52,38,108
)A"
R"A(C_ 116,76,77
C_ 117,78,79
Mavv 48,38,104
E_ 80
Mavv 68,42,104
E_ 81
Mavv 64,42,100
E_ 82
Mavv 44,38,100
E_ 83
Mavv 40,38,96
C_ 120,80,81
C_ 121,82,83
Mavv 60,42,96
W_ 0
E_ 84
Mavv 80,30,96
s_nop 0
Tv 38,v4,25600
Tv 40,v4,31744
Mavv 100,34,96
E_ 85
Mavv 104,34,100
s_nop 0
Tv 42,v5,25600
Tv 44,v5,31744
Mavv 84,30,100
E_ 86
Mavv 88,30,104
E_ 87
Mavv 108,34,104
C_ 124,84,85
C_ 125,86,87
Mavv 112,34,108
E_ 88
Mavv 92,30,108
E_ 89
Mavv 96,30,112
E_ 90
Mavv 116,34,112
W_ 0
E_ 91
Mavv 136,38,112
s_nop 0
Tv 30,v2,26624
Tv 32,v2,32768
Mavv 156,42,112
C_ 128,88,89
C_ 129,90,91
Mavv 152,42,108
s_nop 0
Tv 34,v3,26624
Tv 36,v3,32768
Mavv 132,38,108
E_ 92
Mavv 128,38,104
E_ 93
Mavv 148,42,104
E_ 94
Mavv 144,42,100
E_ 95
Mavv 124,38,100
C_ 132,92,93
C_ 133,94,95
Mavv 120,38,96
v_mov_b32 v92, 0x3f803f80
v_mov_b32 v93, 0x3f803f80
Mavv 140,42,96
W_ 0
v_mov_b32 v94, 0x3f803f80
v_mov_b32 v95, 0x3f803f80
Mavv 160,30,96
s_nop 0
Tv 38,v4,26624
Tv 40,v4,32768
Mavv 180,34,96
Mavv 184,34,100
s_nop 0
Tv 42,v5,26624
Tv 44,v5,32768
Mavv 164,30,100
Mavv 168,30,104
Mavv 188,34,104
S_ s65,s61,110592
Mavv 192,34,108
S_ m0,s51,24576
Mavv 172,30,108
s_waitcnt vmcnt(6) lgkmcnt(0)
s_barrier
Mavv 176,30,112
Ln v7,32,s65,0
Mavv 196,34,112
Mavv 216,38,112
Dv 30,%0,6144
Mavv 236,42,112
Dv 34,v1,6144
Mavv 232,42,108
Ln v8,32,s65,3072
S_ m0,s51,25472
Mavv 212,38,108
Mavv 208,38,104
Ln v7,32,s65,128
Mavv 228,42,104
Mavv 224,42,100
Ln v8,32,s65,3200
S_ m0,s51,26368
Mavv 204,38,100
Mavv 200,38,96
Ln v7,32,s65,256
Mavv 220,42,96
.Lstep2_8:
Zvvv 76,92,116
Dv 38,%0,7168
Zvvv 80,92,120
Dv 42,v1,7168
Zvvv 84,92,124
Zvvv 88,92,128
Zvvv 92,92,132
W_ 2
Nvvv 96,30,136,56
Mvvv 96,34,156
Nvvv 100,30,140,60
s_nop 1
v_max3_f32 v10, v76, v80, v84
s_nop 0
v_max3_f32 v10, v10, v88, v92
Mvvv 100,34,160
v_cmp_lt_f32 vcc, 0x42000000, v10
s_cbranch_vccnz .Ltramp2_7
Nvvv 104,30,144,64
A_ v46,v46,v76
A_ v47,v47,v80
Mvvv 104,34,164
A_ v48,v48,v84
A_ v49,v49,v88
Nvvv 108,30,148,68
A_ v50,v50,v92
Mvvv 108,34,168
Ln v8,32,s65,3328
Nvvv 112,30,152,72
Mvvv 112,34,172
W_ 0
Mvvv 112,38,192
Dv 30,%0,8192
Mvvv 112,42,212
Dv 34,v1,8192
Mvvv 108,38,188
Mvvv 108,42,208
Mvvv 104,38,184
Mvvv 104,42,204
Mvvv 100,38,180
Mvvv 100,42,200
Mvvv 96,38,176
Mvvv 96,42,196
W_ 0
Mvvv 96,30,216
Dv 38,%0,0
Mvvv 96,34,236
Dv 42,v1,0
Mvvv 100,30,220
Mvvv 100,34,240
Mvvv 104,30,224
Mvvv 104,34,244
Mvvv 108,30,228
Mvvv 108,34,248
s_nop 0
E_ 96
Mvvv 112,30,232
E_ 97
Mvvv 112,34,252
W_ 0
E_ 98
Nvvv 76,38,136,56
Dv 30,%0,1024
Mvvv 76,42,156
Dv 34,v1,1024
Nvvv 80,38,140,60
E_ 99
Mvvv 80,42,160
C_ 99,98,99
C_ 98,96,97
Nvvv 84,38,144,64
E_ 100
Mvvv 84,42,164
E_ 101
Nvvv 88,38,148,68
E_ 102
Mvvv 88,42,168
E_ 103
Nvvv 92,38,152,72
C_ 103,102,103
C_ 102,100,101
Mvvv 92,42,172
W_ 0
E_ 104
Mvvv 92,30,192
Dv 38,%0,2048
Mvvv 92,34,212
Dv 42,v1,2048
Mvvv 88,30,188
E_ 105
Mvvv 88,34,208
E_ 106
Mvvv 84,30,184
S_ s65,s61,98304
E_ 107
Mvvv 84,34,204
S_ m0,s51,0
C_ 107,106,107
C_ 106,104,105
Mvvv 80,30,180
s_waitcnt vmcnt(6) lgkmcnt(0)
s_barrier
Mvvv 80,34,200
L_ v9,36,s65,0
Mvvv 76,30,176
E_ 108
Mvvv 76,34,196
L_ v9,36,s65,3072
S_ m0,s51,896
Mvvv 76,38,216
Tv 30,v2,12288
Tv 32,v2,18432
Mvvv 76,42,236
L_ v9,36,s65,128
Mvvv 80,38,220
s_nop 0
Tv 34,v3,12288
Tv 36,v3,18432
Mvvv 80,42,240
L_ v9,36,s65,3200
S_ m0,s51,1792
Mvvv 84,38,224
E_ 109
Mvvv 84,42,244
L_ v9,36,s65,256
Mvvv 88,38,228
E_ 110
Mvvv 88,42,248
L_ v9,36,s65,3328
Mvvv 92,38,232
E_ 111
Mvvv 92,42,252
W_ 0
C_ 111,110,111
C_ 110,108,109
Mavv 0,30,116
s_nop 0
Tv 38,v4,12288
Tv 40,v4,18432
Mavv 20,34,116
E_ 112
Mavv 24,34,120
s_nop 0
Tv 42,v5,12288
Tv 44,v5,18432
Mavv 4,30,120
E_ 113
Mavv 8,30,124
E_ 114
Mavv 28,34,124
E_ 115
Mavv 32,34,128
C_ 115,114,115
C_ 114,112,113
Mavv 12,30,128
E_ 76
Mavv 16,30,132
E_ 77
Mavv 36,34,132
W_ 0
E_ 78
Mavv 56,38,132
s_nop 0
Tv 30,v2,13312
Tv 32,v2,19456
Mavv 76,42,132
E_ 79
Mavv 72,42,128
s_nop 0
Tv 34,v3,13312
Tv 36,v3,19456
Mavv 52,38,128
C_ 96,76,77
C_ 97,78,79
Mavv 48,38,124
E_ 80
Mavv 68,42,124
E_ 81
Mavv 64,42,120
E_ 82
Mavv 44,38,120
E_ 83
Mavv 40,38,116
C_ 100,80,81
C_ 101,82,83
Mavv 60,42,116
W_ 0
E_ 84
Mavv 80,30,116
s_nop 0
Tv 38,v4,13312
Tv 40,v4,19456
Mavv 100,34,116
E_ 85
Mavv 104,34,120
s_nop 0
Tv 42,v5,13312
Tv 44,v5,19456
Mavv 84,30,120
E_ 86
Mavv 88,30,124
E_ 87
Mavv 108,34,124
C_ 104,84,85
C_ 105,86,87
Mavv 112,34,128
E_ 88
Mavv 92,30,128
E_ 89
Mavv 96,30,132
E_ 90
Mavv 116,34,132
W_ 0
E_ 91
Mavv 136,38,132
s_nop 0
Tv 30,v2,14336
Tv 32,v2,20480
Mavv 156,42,132
C_ 108,88,89
C_ 109,90,91
Mavv 152,42,128
s_nop 0
Tv 34,v3,14336
Tv 36,v3,20480
Mavv 132,38,128
E_ 92
Mavv 128,38,124
E_ 93
Mavv 148,42,124
E_ 94
Mavv 144,42,120
E_ 95
Mavv 124,38,120
C_ 112,92,93
C_ 113,94,95
Mavv 120,38,116
v_mov_b32 v92, 0x3f803f80
v_mov_b32 v93, 0x3f803f80
Mavv 140,42,116
W_ 0
v_mov_b32 v94, 0x3f803f80
v_mov_b32 v95, 0x3f803f80
Mavv 160,30,116
s_nop 0
Tv 38,v4,14336
Tv 40,v4,20480
Mavv 180,34,116
Mavv 184,34,120
s_nop 0
Tv 42,v5,14336
Tv 44,v5,20480
Mavv 164,30,120
Mavv 168,30,124
Mavv 188,34,124
S_ s65,s61,122880
Mavv 192,34,128
S_ m0,s51,12288
Mavv 172,30,128
s_waitcnt vmcnt(6) lgkmcnt(0)
s_barrier
Mavv 176,30,132
Ln v7,32,s65,0
Mavv 196,34,132
Mavv 216,38,132
Dv 30,%0,30720
Mavv 236,42,132
Dv 34,v1,30720
Mavv 232,42,128
Ln v8,32,s65,3072
S_ m0,s51,13184
Mavv 212,38,128
Mavv 208,38,124
Ln v7,32,s65,128
Mavv 228,42,124
Mavv 224,42,120
Ln v8,32,s65,3200
S_ m0,s51,14080
Mavv 204,38,120
Mavv 200,38,116
Ln v7,32,s65,256
Mavv 220,42,116
s_cmp_eq_u32 s4, 0
s_cbranch_scc1 .Lhs_d2
.Lhs_d2_back:
.Lstep2_9:
Zvvv 76,92,96
Dv 38,%0,31744
Zvvv 80,92,100
Dv 42,v1,31744
Zvvv 84,92,104
Zvvv 88,92,108
Zvvv 92,92,112
W_ 2
Nvvv 116,30,136,56
Mvvv 116,34,156
Nvvv 120,30,140,60
s_nop 1
v_max3_f32 v10, v76, v80, v84
s_nop 0
v_max3_f32 v10, v10, v88, v92
Mvvv 120,34,160
v_cmp_lt_f32 vcc, 0x42000000, v10
s_cbranch_vccnz .Ltramp2_8
Nvvv 124,30,144,64
A_ v46,v46,v76
A_ v47,v47,v80
Mvvv 124,34,164
A_ v48,v48,v84
A_ v49,v49,v88
Nvvv 128,30,148,68
A_ v50,v50,v92
Mvvv 128,34,168
Ln v8,32,s65,3328
Nvvv 132,30,152,72
Mvvv 132,34,172
W_ 0
Mvvv 132,38,192
Dv 30,%0,32768
Mvvv 132,42,212
Dv 34,v1,32768
Mvvv 128,38,188
Mvvv 128,42,208
Mvvv 124,38,184
Mvvv 124,42,204
Mvvv 120,38,180
Mvvv 120,42,200
Mvvv 116,38,176
Mvvv 116,42,196
W_ 0
Mvvv 116,30,216
Dv 38,%0,24576
Mvvv 116,34,236
Dv 42,v1,24576
Mvvv 120,30,220
Mvvv 120,34,240
Mvvv 124,30,224
Mvvv 124,34,244
Mvvv 128,30,228
Mvvv 128,34,248
s_nop 0
E_ 116
Mvvv 132,30,232
E_ 117
Mvvv 132,34,252
W_ 0
E_ 118
Nvvv 76,38,136,56
Dv 30,%0,25600
Mvvv 76,42,156
Dv 34,v1,25600
Nvvv 80,38,140,60
E_ 119
Mvvv 80,42,160
C_ 119,118,119
C_ 118,116,117
Nvvv 84,38,144,64
E_ 120
Mvvv 84,42,164
E_ 121
Nvvv 88,38,148,68
E_ 122
Mvvv 88,42,168
E_ 123
Nvvv 92,38,152,72
C_ 123,122,123
C_ 122,120,121
Mvvv 92,42,172
W_ 0
E_ 124
Mvvv 92,30,192
Dv 38,%0,26624
Mvvv 92,34,212
Dv 42,v1,26624
Mvvv 88,30,188
E_ 125
Mvvv 88,34,208
E_ 126
Mvvv 84,30,184
S_ s65,s61,110592
E_ 127
Mvvv 84,34,204
S_ m0,s51,24576
C_ 127,126,127
C_ 126,124,125
Mvvv 80,30,180
s_waitcnt vmcnt(6) lgkmcnt(0)
s_barrier
Mvvv 80,34,200
L_ v9,36,s65,0
Mvvv 76,30,176
.Lpad_a2_5:
s_cbranch_execz .Lhop_a2_r
E_ 128
Mvvv 76,34,196
L_ v9,36,s65,3072
S_ m0,s51,25472
Mvvv 76,38,216
Tv 30,v2,0
Tv 32,v2,6144
Mvvv 76,42,236
L_ v9,36,s65,128
Mvvv 80,38,220
s_nop 0
Tv 34,v3,0
Tv 36,v3,6144
Mvvv 80,42,240
L_ v9,36,s65,3200
S_ m0,s51,26368
Mvvv 84,38,224
E_ 129
Mvvv 84,42,244
L_ v9,36,s65,256
Mvvv 88,38,228
E_ 130
Mvvv 88,42,248
L_ v9,36,s65,3328
Mvvv 92,38,232
E_ 131
Mvvv 92,42,252
W_ 0
C_ 131,130,131
C_ 130,128,129
Mavv 0,30,96
s_nop 0
Tv 38,v4,0
Tv 40,v4,6144
Mavv 20,34,96
E_ 132
Mavv 24,34,100
s_nop 0
Tv 42,v5,0
Tv 44,v5,6144
Mavv 4,30,100
E_ 133
Mavv 8,30,104
E_ 134
Mavv 28,34,104
E_ 135
Mavv 32,34,108
C_ 135,134,135
C_ 134,132,133
Mavv 12,30,108
E_ 76
Mavv 16,30,112
E_ 77
Mavv 36,34,112
W_ 0
E_ 78
Mavv 56,38,112
s_nop 0
Tv 30,v2,1024
Tv 32,v2,7168
Mavv 76,42,112
E_ 79
Mavv 72,42,108
s_nop 0
Tv 34,v3,1024
Tv 36,v3,7168
Mavv 52,38,108
C_ 116,76,77
C_ 117,78,79
Mavv 48,38,104
E_ 80
Mavv 68,42,104
E_ 81
Mavv 64,42,100
E_ 82
Mavv 44,38,100
E_ 83
Mavv 40,38,96
C_ 120,80,81
C_ 121,82,83
Mavv 60,42,96
W_ 0
E_ 84
Mavv 80,30,96
s_nop 0
Tv 38,v4,1024
Tv 40,v4,7168
Mavv 100,34,96
E_ 85
Mavv 104,34,100
s_nop 0
Tv 42,v5,1024
Tv 44,v5,7168
Mavv 84,30,100
E_ 86
Mavv 88,30,104
E_ 87
Mavv 108,34,104
C_ 124,84,85
C_ 125,86,87
Mavv 112,34,108
E_ 88
Mavv 92,30,108
E_ 89
Mavv 96,30,112
E_ 90
Mavv 116,34,112
W_ 0
E_ 91
Mavv 136,38,112
s_nop 0
Tv 30,v2,2048
Tv 32,v2,8192
Mavv 156,42,112
C_ 128,88,89
C_ 129,90,91
Mavv 152,42,108
s_nop 0
Tv 34,v3,2048
Tv 36,v3,8192
Mavv 132,38,108
E_ 92
Mavv 128,38,104
E_ 93
Mavv 148,42,104
E_ 94
Mavv 144,42,100
E_ 95
Mavv 124,38,100
C_ 132,92,93
C_ 133,94,95
Mavv 120,38,96
v_mov_b32 v92, 0x3f803f80
v_mov_b32 v93, 0x3f803f80
Mavv 140,42,96
W_ 0
v_mov_b32 v94, 0x3f803f80
v_mov_b32 v95, 0x3f803f80
Mavv 160,30,96
s_nop 0
Tv 38,v4,2048
Tv 40,v4,8192
Mavv 180,34,96
Mavv 184,34,100
s_nop 0
Tv 42,v5,2048
Tv 44,v5,8192
Mavv 164,30,100
Mavv 168,30,104
Mavv 188,34,104
S_ s65,s61,135168
Mavv 192,34,108
S_ m0,s51,0
Mavv 172,30,108
s_waitcnt vmcnt(6) lgkmcnt(0)
s_barrier
Mavv 176,30,112
Ln v7,32,s65,0
Mavv 196,34,112
Mavv 216,38,112
Dv 30,%0,18432
Mavv 236,42,112
Dv 34,v1,18432
Mavv 232,42,108
Ln v8,32,s65,3072
S_ m0,s51,896
Mavv 212,38,108
Mavv 208,38,104
Ln v7,32,s65,128
Mavv 228,42,104
Mavv 224,42,100
Ln v8,32,s65,3200
S_ m0,s51,1792
Mavv 204,38,100
Mavv 200,38,96
Ln v7,32,s65,256
Mavv 220,42,96
.Lstep2_10:
Zvvv 76,92,116
Dv 38,%0,19456
Zvvv 80,92,120
Dv 42,v1,19456
Zvvv 84,92,124
Zvvv 88,92,128
Zvvv 92,92,132
W_ 2
Nvvv 96,30,136,56
Mvvv 96,34,156
Nvvv 100,30,140,60
s_nop 1
v_max3_f32 v10, v76, v80, v84
s_nop 0
v_max3_f32 v10, v10, v88, v92
Mvvv 100,34,160
v_cmp_lt_f32 vcc, 0x42000000, v10
s_cbranch_vccnz .Ltramp2_9
Nvvv 104,30,144,64
A_ v46,v46,v76
A_ v47,v47,v80
Mvvv 104,34,164
A_ v48,v48,v84
A_ v49,v49,v88
Nvvv 108,30,148,68
A_ v50,v50,v92
Mvvv 108,34,168
Ln v8,32,s65,3328
Nvvv 112,30,152,72
Mvvv 112,34,172
W_ 0
Mvvv 112,38,192
Dv 30,%0,20480
Mvvv 112,42,212
Dv 34,v1,20480
Mvvv 108,38,188
Mvvv 108,42,208
Mvvv 104,38,184
Mvvv 104,42,204
Mvvv 100,38,180
Mvvv 100,42,200
Mvvv 96,38,176
Mvvv 96,42,196
W_ 0
Mvvv 96,30,216
Dv 38,%0,12288
Mvvv 96,34,236
Dv 42,v1,12288
Mvvv 100,30,220
Mvvv 100,34,240
Mvvv 104,30,224
Mvvv 104,34,244
Mvvv 108,30,228
Mvvv 108,34,248
s_nop 0
E_ 96
Mvvv 112,30,232
E_ 97
Mvvv 112,34,252
W_ 0
E_ 98
Nvvv 76,38,136,56
Dv 30,%0,13312
Mvvv 76,42,156
Dv 34,v1,13312
Nvvv 80,38,140,60
E_ 99
Mvvv 80,42,160
C_ 99,98,99
C_ 98,96,97
Nvvv 84,38,144,64
E_ 100
Mvvv 84,42,164
E_ 101
Nvvv 88,38,148,68
E_ 102
Mvvv 88,42,168
E_ 103
Nvvv 92,38,152,72
C_ 103,102,103
C_ 102,100,101
Mvvv 92,42,172
W_ 0
E_ 104
Mvvv 92,30,192
Dv 38,%0,14336
Mvvv 92,34,212
Dv 42,v1,14336
Mvvv 88,30,188
E_ 105
Mvvv 88,34,208
E_ 106
Mvvv 84,30,184
S_ s65,s61,122880
E_ 107
Mvvv 84,34,204
S_ m0,s51,12288
C_ 107,106,107
C_ 106,104,105
Mvvv 80,30,180
s_waitcnt vmcnt(6) lgkmcnt(0)
s_barrier
Mvvv 80,34,200
L_ v9,36,s65,0
Mvvv 76,30,176
E_ 108
Mvvv 76,34,196
L_ v9,36,s65,3072
S_ m0,s51,13184
Mvvv 76,38,216
Tv 30,v2,24576
Tv 32,v2,30720
Mvvv 76,42,236
L_ v9,36,s65,128
Mvvv 80,38,220
s_nop 0
Tv 34,v3,24576
Tv 36,v3,30720
Mvvv 80,42,240
L_ v9,36,s65,3200
S_ m0,s51,14080
Mvvv 84,38,224
E_ 109
Mvvv 84,42,244
L_ v9,36,s65,256
Mvvv 88,38,228
E_ 110
Mvvv 88,42,248
L_ v9,36,s65,3328
Mvvv 92,38,232
E_ 111
Mvvv 92,42,252
W_ 0
C_ 111,110,111
C_ 110,108,109
Mavv 0,30,116
s_nop 0
Tv 38,v4,24576
Tv 40,v4,30720
Mavv 20,34,116
E_ 112
Mavv 24,34,120
s_nop 0
Tv 42,v5,24576
Tv 44,v5,30720
Mavv 4,30,120
E_ 113
Mavv 8,30,124
E_ 114
Mavv 28,34,124
E_ 115
Mavv 32,34,128
C_ 115,114,115
C_ 114,112,113
Mavv 12,30,128
E_ 76
Mavv 16,30,132
E_ 77
Mavv 36,34,132
W_ 0
E_ 78
Mavv 56,38,132
s_nop 0
Tv 30,v2,25600
Tv 32,v2,31744
Mavv 76,42,132
E_ 79
Mavv 72,42,128
s_nop 0
Tv 34,v3,25600
Tv 36,v3,31744
Mavv 52,38,128
C_ 96,76,77
C_ 97,78,79
Mavv 48,38,124
E_ 80
Mavv 68,42,124
E_ 81
Mavv 64,42,120
E_ 82
Mavv 44,38,120
E_ 83
Mavv 40,38,116
C_ 100,80,81
C_ 101,82,83
Mavv 60,42,116
W_ 0
E_ 84
Mavv 80,30,116
s_nop 0
Tv 38,v4,25600
Tv 40,v4,31744
Mavv 100,34,116
E_ 85
Mavv 104,34,120
s_nop 0
Tv 42,v5,25600
Tv 44,v5,31744
Mavv 84,30,120
E_ 86
Mavv 88,30,124
E_ 87
Mavv 108,34,124
C_ 104,84,85
C_ 105,86,87
Mavv 112,34,128
E_ 88
Mavv 92,30,128
E_ 89
Mavv 96,30,132
E_ 90
Mavv 116,34,132
W_ 0
E_ 91
Mavv 136,38,132
s_nop 0
Tv 30,v2,26624
Tv 32,v2,32768
Mavv 156,42,132
C_ 108,88,89
C_ 109,90,91
Mavv 152,42,128
s_nop 0
Tv 34,v3,26624
Tv 36,v3,32768
Mavv 132,38,128
E_ 92
Mavv 128,38,124
E_ 93
Mavv 148,42,124
E_ 94
Mavv 144,42,120
E_ 95
Mavv 124,38,120
C_ 112,92,93
C_ 113,94,95
Mavv 120,38,116
v_mov_b32 v92, 0x3f803f80
v_mov_b32 v93, 0x3f803f80
Mavv 140,42,116
W_ 0
v_mov_b32 v94, 0x3f803f80
v_mov_b32 v95, 0x3f803f80
Mavv 160,30,116
s_nop 0
Tv 38,v4,26624
Tv 40,v4,32768
Mavv 180,34,116
Mavv 184,34,120
s_nop 0
Tv 42,v5,26624
Tv 44,v5,32768
Mavv 164,30,120
Mavv 168,30,124
Mavv 188,34,124
S_ s65,s61,147456
Mavv 192,34,128
S_ m0,s51,24576
Mavv 172,30,128
s_waitcnt vmcnt(6) lgkmcnt(0)
s_barrier
Mavv 176,30,132
Ln v7,32,s65,0
Mavv 196,34,132
Mavv 216,38,132
Dv 30,%0,6144
Mavv 236,42,132
Dv 34,v1,6144
Mavv 232,42,128
Ln v8,32,s65,3072
S_ m0,s51,25472
Mavv 212,38,128
Mavv 208,38,124
Ln v7,32,s65,128
Mavv 228,42,124
Mavv 224,42,120
Ln v8,32,s65,3200
S_ m0,s51,26368
Mavv 204,38,120
Mavv 200,38,116
Ln v7,32,s65,256
Mavv 220,42,116
.Lstep2_11:
Zvvv 76,92,96
Dv 38,%0,7168
Zvvv 80,92,100
Dv 42,v1,7168
Zvvv 84,92,104
Zvvv 88,92,108
Zvvv 92,92,112
W_ 2
Nvvv 116,30,136,56
Mvvv 116,34,156
Nvvv 120,30,140,60
s_nop 1
v_max3_f32 v10, v76, v80, v84
s_nop 0
v_max3_f32 v10, v10, v88, v92
Mvvv 120,34,160
v_cmp_lt_f32 vcc, 0x42000000, v10
s_cbranch_vccnz .Ltramp2_10
Nvvv 124,30,144,64
A_ v46,v46,v76
A_ v47,v47,v80
Mvvv 124,34,164
A_ v48,v48,v84
A_ v49,v49,v88
Nvvv 128,30,148,68
A_ v50,v50,v92
Mvvv 128,34,168
Ln v8,32,s65,3328
Nvvv 132,30,152,72
Mvvv 132,34,172
W_ 0
Mvvv 132,38,192
Dv 30,%0,8192
Mvvv 132,42,212
Dv 34,v1,8192
Mvvv 128,38,188
Mvvv 128,42,208
Mvvv 124,38,184
Mvvv 124,42,204
Mvvv 120,38,180
Mvvv 120,42,200
Mvvv 116,38,176
Mvvv 116,42,196
W_ 0
Mvvv 116,30,216
Dv 38,%0,0
Mvvv 116,34,236
Dv 42,v1,0
Mvvv 120,30,220
Mvvv 120,34,240
Mvvv 124,30,224
Mvvv 124,34,244
Mvvv 128,30,228
Mvvv 128,34,248
s_nop 0
E_ 116
Mvvv 132,30,232
E_ 117
Mvvv 132,34,252
W_ 0
E_ 118
Nvvv 76,38,136,56
Dv 30,%0,1024
Mvvv 76,42,156
Dv 34,v1,1024
Nvvv 80,38,140,60
E_ 119
Mvvv 80,42,160
C_ 119,118,119
C_ 118,116,117
Nvvv 84,38,144,64
E_ 120
Mvvv 84,42,164
E_ 121
Nvvv 88,38,148,68
E_ 122
Mvvv 88,42,168
E_ 123
Nvvv 92,38,152,72
C_ 123,122,123
C_ 122,120,121
Mvvv 92,42,172
W_ 0
E_ 124
Mvvv 92,30,192
Dv 38,%0,2048
Mvvv 92,34,212
Dv 42,v1,2048
Mvvv 88,30,188
E_ 125
Mvvv 88,34,208
E_ 126
Mvvv 84,30,184
S_ s65,s61,135168
E_ 127
Mvvv 84,34,204
S_ m0,s51,0
C_ 127,126,127
C_ 126,124,125
Mvvv 80,30,180
s_waitcnt vmcnt(6) lgkmcnt(0)
s_barrier
Mvvv 80,34,200
L_ v9,36,s65,0
Mvvv 76,30,176
E_ 128
Mvvv 76,34,196
L_ v9,36,s65,3072
S_ m0,s51,896
Mvvv 76,38,216
Tv 30,v2,12288
Tv 32,v2,18432
Mvvv 76,42,236
L_ v9,36,s65,128
Mvvv 80,38,220
s_nop 0
Tv 34,v3,12288
Tv 36,v3,18432
Mvvv 80,42,240
L_ v9,36,s65,3200
S_ m0,s51,1792
Mvvv 84,38,224
E_ 129
Mvvv 84,42,244
L_ v9,36,s65,256
Mvvv 88,38,228
E_ 130
Mvvv 88,42,248
L_ v9,36,s65,3328
Mvvv 92,38,232
.Lpad_a2_6:
s_cbranch_execz .Lhop_a2_r
E_ 131
Mvvv 92,42,252
W_ 0
C_ 131,130,131
C_ 130,128,129
Mavv 0,30,96
s_nop 0
Tv 38,v4,12288
Tv 40,v4,18432
Mavv 20,34,96
E_ 132
Mavv 24,34,100
s_nop 0
Tv 42,v5,12288
Tv 44,v5,18432
Mavv 4,30,100
E_ 133
Mavv 8,30,104
E_ 134
Mavv 28,34,104
E_ 135
Mavv 32,34,108
C_ 135,134,135
C_ 134,132,133
Mavv 12,30,108
E_ 76
Mavv 16,30,112
E_ 77
Mavv 36,34,112
W_ 0
E_ 78
Mavv 56,38,112
s_nop 0
Tv 30,v2,13312
Tv 32,v2,19456
Mavv 76,42,112
E_ 79
Mavv 72,42,108
s_nop 0
Tv 34,v3,13312
Tv 36,v3,19456
Mavv 52,38,108
C_ 116,76,77
C_ 117,78,79
Mavv 48,38,104
E_ 80
Mavv 68,42,104
E_ 81
Mavv 64,42,100
E_ 82
Mavv 44,38,100
E_ 83
Mavv 40,38,96
C_ 120,80,81
C_ 121,82,83
Mavv 60,42,96
W_ 0
E_ 84
Mavv 80,30,96
s_nop 0
Tv 38,v4,13312
Tv 40,v4,19456
Mavv 100,34,96
E_ 85
Mavv 104,34,100
s_nop 0
Tv 42,v5,13312
Tv 44,v5,19456
Mavv 84,30,100
E_ 86
Mavv 88,30,104
E_ 87
Mavv 108,34,104
C_ 124,84,85
C_ 125,86,87
Mavv 112,34,108
E_ 88
Mavv 92,30,108
E_ 89
Mavv 96,30,112
E_ 90
Mavv 116,34,112
W_ 0
E_ 91
Mavv 136,38,112
s_nop 0
Tv 30,v2,14336
Tv 32,v2,20480
Mavv 156,42,112
C_ 128,88,89
C_ 129,90,91
Mavv 152,42,108
s_nop 0
Tv 34,v3,14336
Tv 36,v3,20480
Mavv 132,38,108
E_ 92
Mavv 128,38,104
E_ 93
Mavv 148,42,104
E_ 94
Mavv 144,42,100
E_ 95
Mavv 124,38,100
C_ 132,92,93
C_ 133,94,95
Mavv 120,38,96
v_mov_b32 v92, 0x3f803f80
v_mov_b32 v93, 0x3f803f80
Mavv 140,42,96
W_ 0
v_mov_b32 v94, 0x3f803f80
v_mov_b32 v95, 0x3f803f80
Mavv 160,30,96
s_nop 0
Tv 38,v4,14336
Tv 40,v4,20480
Mavv 180,34,96
Mavv 184,34,100
s_nop 0
Tv 42,v5,14336
Tv 44,v5,20480
Mavv 164,30,100
Mavv 168,30,104
Mavv 188,34,104
S_ s65,s61,159744
Mavv 192,34,108
S_ m0,s51,12288
Mavv 172,30,108
s_waitcnt vmcnt(6) lgkmcnt(0)
s_barrier
Mavv 176,30,112
Ln v7,32,s65,0
Mavv 196,34,112
Mavv 216,38,112
Dv 30,%0,30720
Mavv 236,42,112
Dv 34,v1,30720
Mavv 232,42,108
Ln v8,32,s65,3072
S_ m0,s51,13184
Mavv 212,38,108
Mavv 208,38,104
Ln v7,32,s65,128
Mavv 228,42,104
Mavv 224,42,100
Ln v8,32,s65,3200
S_ m0,s51,14080
Mavv 204,38,100
Mavv 200,38,96
Ln v7,32,s65,256
Mavv 220,42,96
.Lstep2_12:
Zvvv 76,92,116
Dv 38,%0,31744
Zvvv 80,92,120
Dv 42,v1,31744
Zvvv 84,92,124
Zvvv 88,92,128
Zvvv 92,92,132
W_ 2
Nvvv 96,30,136,56
Mvvv 96,34,156
Nvvv 100,30,140,60
s_nop 1
v_max3_f32 v10, v76, v80, v84
s_nop 0
v_max3_f32 v10, v10, v88, v92
Mvvv 100,34,160
v_cmp_lt_f32 vcc, 0x42000000, v10
s_cbranch_vccnz .Ltramp2_11
Nvvv 104,30,144,64
A_ v46,v46,v76
A_ v47,v47,v80
Mvvv 104,34,164
A_ v48,v48,v84
A_ v49,v49,v88
Nvvv 108,30,148,68
A_ v50,v50,v92
Mvvv 108,34,168
Ln v8,32,s65,3328
Nvvv 112,30,152,72
R_ v11,a248
Mvvv 112,34,172
W_ 0
v_min_u32 v11, 240, v11
Mvvv 112,38,192
Dv 30,%0,32768
v_lshlrev_b32 v11, 3, v11
Mvvv 112,42,212
Dv 34,v1,32768
Mvvv 108,38,188
buffer_load_dword a254, v11, s[44:47], s66 offen
Mvvv 108,42,208
Mvvv 104,38,184
Mvvv 104,42,204
Mvvv 100,38,180
Mvvv 100,42,200
Mvvv 96,38,176
Mvvv 96,42,196
W_ 0
Mvvv 96,30,216
Dv 38,%0,24576
Mvvv 96,34,236
Dv 42,v1,24576
Mvvv 100,30,220
Mvvv 100,34,240
Mvvv 104,30,224
Mvvv 104,34,244
Mvvv 108,30,228
Mvvv 108,34,248
s_nop 0
E_ 96
Mvvv 112,30,232
E_ 97
Mvvv 112,34,252
W_ 0
E_ 98
Nvvv 76,38,136,56
Dv 30,%0,25600
Mvvv 76,42,156
Dv 34,v1,25600
Nvvv 80,38,140,60
E_ 99
Mvvv 80,42,160
C_ 99,98,99
C_ 98,96,97
Nvvv 84,38,144,64
E_ 100
Mvvv 84,42,164
E_ 101
Nvvv 88,38,148,68
E_ 102
Mvvv 88,42,168
E_ 103
Nvvv 92,38,152,72
C_ 103,102,103
C_ 102,100,101
Mvvv 92,42,172
W_ 0
E_ 104
Mvvv 92,30,192
Dv 38,%0,26624
Mvvv 92,34,212
Dv 42,v1,26624
Mvvv 88,30,188
E_ 105
Mvvv 88,34,208
E_ 106
Mvvv 84,30,184
S_ s65,s61,147456
E_ 107
Mvvv 84,34,204
S_ m0,s51,24576
C_ 107,106,107
C_ 106,104,105
Mvvv 80,30,180
s_waitcnt vmcnt(7) lgkmcnt(0)
s_barrier
Mvvv 80,34,200
Ln v9,36,s65,0
Mvvv 76,30,176
E_ 108
Mvvv 76,34,196
Ln v9,36,s65,3072
S_ m0,s51,25472
Mvvv 76,38,216
Tv 30,v2,0
Tv 32,v2,6144
Mvvv 76,42,236
Ln v9,36,s65,128
Mvvv 80,38,220
s_nop 0
Tv 34,v3,0
Tv 36,v3,6144
Mvvv 80,42,240
Ln v9,36,s65,3200
S_ m0,s51,26368
Mvvv 84,38,224
E_ 109
Mvvv 84,42,244
Ln v9,36,s65,256
Mvvv 88,38,228
E_ 110
Mvvv 88,42,248
Ln v9,36,s65,3328
Mvvv 92,38,232
E_ 111
Mvvv 92,42,252
W_ 0
C_ 111,110,111
C_ 110,108,109
Mavv 0,30,116
s_nop 0
Tv 38,v4,0
Tv 40,v4,6144
Mavv 20,34,116
E_ 112
Mavv 24,34,120
s_nop 0
Tv 42,v5,0
Tv 44,v5,6144
Mavv 4,30,120
E_ 113
Mavv 8,30,124
E_ 114
Mavv 28,34,124
E_ 115
Mavv 32,34,128
C_ 115,114,115
C_ 114,112,113
Mavv 12,30,128
E_ 76
Mavv 16,30,132
E_ 77
Mavv 36,34,132
W_ 0
E_ 78
Mavv 56,38,132
s_nop 0
Tv 30,v2,1024
Tv 32,v2,7168
Mavv 76,42,132
E_ 79
Mavv 72,42,128
s_nop 0
Tv 34,v3,1024
Tv 36,v3,7168
Mavv 52,38,128
C_ 96,76,77
C_ 97,78,79
Mavv 48,38,124
E_ 80
Mavv 68,42,124
E_ 81
Mavv 64,42,120
E_ 82
Mavv 44,38,120
E_ 83
Mavv 40,38,116
C_ 100,80,81
C_ 101,82,83
Mavv 60,42,116
W_ 0
E_ 84
Mavv 80,30,116
s_nop 0
Tv 38,v4,1024
Tv 40,v4,7168
Mavv 100,34,116
E_ 85
Mavv 104,34,120
s_nop 0
Tv 42,v5,1024
Tv 44,v5,7168
Mavv 84,30,120
E_ 86
Mavv 88,30,124
E_ 87
Mavv 108,34,124
C_ 104,84,85
C_ 105,86,87
Mavv 112,34,128
E_ 88
Mavv 92,30,128
E_ 89
Mavv 96,30,132
E_ 90
Mavv 116,34,132
W_ 0
E_ 91
Mavv 136,38,132
s_nop 0
Tv 30,v2,2048
Tv 32,v2,8192
Mavv 156,42,132
C_ 108,88,89
C_ 109,90,91
Mavv 152,42,128
s_nop 0
Tv 34,v3,2048
Tv 36,v3,8192
Mavv 132,38,128
E_ 92
Mavv 128,38,124
E_ 93
Mavv 148,42,124
E_ 94
Mavv 144,42,120
E_ 95
Mavv 124,38,120
C_ 112,92,93
C_ 113,94,95
Mavv 120,38,116
v_mov_b32 v92, 0x3f803f80
v_mov_b32 v93, 0x3f803f80
Mavv 140,42,116
W_ 0
v_mov_b32 v94, 0x3f803f80
v_mov_b32 v95, 0x3f803f80
Mavv 160,30,116
s_nop 0
Tv 38,v4,2048
Tv 40,v4,8192
Mavv 180,34,116
Mavv 184,34,120
s_nop 0
Tv 42,v5,2048
Tv 44,v5,8192
Mavv 164,30,120
Mavv 168,30,124
Mavv 188,34,124
S_ s65,s61,172032
Mavv 192,34,128
S_ m0,s51,0
Mavv 172,30,128
s_waitcnt vmcnt(7) lgkmcnt(0)
s_barrier
Mavv 176,30,132
Ln v7,32,s65,0
Mavv 196,34,132
Mavv 216,38,132
Dv 30,%0,18432
Mavv 236,42,132
Dv 34,v1,18432
Mavv 232,42,128
Ln v8,32,s65,3072
S_ m0,s51,896
Mavv 212,38,128
Mavv 208,38,124
Ln v7,32,s65,128
Mavv 228,42,124
Mavv 224,42,120
Ln v8,32,s65,3200
S_ m0,s51,1792
Mavv 204,38,120
Mavv 200,38,116
Ln v7,32,s65,256
Mavv 220,42,116
.Lstep2_13:
Zvvv 76,92,96
Dv 38,%0,19456
Zvvv 80,92,100
Dv 42,v1,19456
Zvvv 84,92,104
Zvvv 88,92,108
Zvvv 92,92,112
W_ 2
Nvvv 116,30,136,56
Mvvv 116,34,156
Nvvv 120,30,140,60
s_nop 1
v_max3_f32 v10, v76, v80, v84
s_nop 0
v_max3_f32 v10, v10, v88, v92
Mvvv 120,34,160
v_cmp_lt_f32 vcc, 0x42000000, v10
s_cbranch_vccnz .Ltramp2_12
Nvvv 124,30,144,64
A_ v46,v46,v76
A_ v47,v47,v80
Mvvv 124,34,164
A_ v48,v48,v84
A_ v49,v49,v88
Nvvv 128,30,148,68
A_ v50,v50,v92
Mvvv 128,34,168
Ln v8,32,s65,3328
Nvvv 132,30,152,72
Mvvv 132,34,172
W_ 0
Mvvv 132,38,192
Dv 30,%0,20480
Mvvv 132,42,212
Dv 34,v1,20480
Mvvv 128,38,188
Mvvv 128,42,208
Mvvv 124,38,184
Mvvv 124,42,204
Mvvv 120,38,180
Mvvv 120,42,200
Mvvv 116,38,176
Mvvv 116,42,196
W_ 0
Mvvv 116,30,216
Dv 38,%0,12288
Mvvv 116,34,236
Dv 42,v1,12288
Mvvv 120,30,220
Mvvv 120,34,240
Mvvv 124,30,224
Mvvv 124,34,244
Mvvv 128,30,228
Mvvv 128,34,248
s_nop 0
E_ 116
Mvvv 132,30,232
E_ 117
Mvvv 132,34,252
W_ 0
E_ 118
Nvvv 76,38,136,56
Dv 30,%0,13312
Mvvv 76,42,156
Dv 34,v1,13312
Nvvv 80,38,140,60
E_ 119
Mvvv 80,42,160
C_ 119,118,119
C_ 118,116,117
Nvvv 84,38,144,64
E_ 120
Mvvv 84,42,164
E_ 121
Nvvv 88,38,148,68
E_ 122
Mvvv 88,42,168
E_ 123
Nvvv 92,38,152,72
C_ 123,122,123
C_ 122,120,121
Mvvv 92,42,172
W_ 0
E_ 124
Mvvv 92,30,192
Dv 38,%0,14336
Mvvv 92,34,212
Dv 42,v1,14336
Mvvv 88,30,188
E_ 125
Mvvv 88,34,208
E_ 126
Mvvv 84,30,184
S_ s65,s61,159744
E_ 127
Mvvv 84,34,204
S_ m0,s51,12288
C_ 127,126,127
C_ 126,124,125
Mvvv 80,30,180
s_waitcnt vmcnt(6) lgkmcnt(0)
s_barrier
Mvvv 80,34,200
Ln v9,36,s65,0
Mvvv 76,30,176
E_ 128
Mvvv 76,34,196
Ln v9,36,s65,3072
S_ m0,s51,13184
Mvvv 76,38,216
Tv 30,v2,24576
Tv 32,v2,30720
Mvvv 76,42,236
Ln v9,36,s65,128
Mvvv 80,38,220
s_nop 0
Tv 34,v3,24576
Tv 36,v3,30720
Mvvv 80,42,240
Ln v9,36,s65,3200
S_ m0,s51,14080
Mvvv 84,38,224
E_ 129
Mvvv 84,42,244
Ln v9,36,s65,256
Mvvv 88,38,228
E_ 130
Mvvv 88,42,248
Ln v9,36,s65,3328
Mvvv 92,38,232
E_ 131
Mvvv 92,42,252
W_ 0
C_ 131,130,131
C_ 130,128,129
Mavv 0,30,96
s_nop 0
Tv 38,v4,24576
Tv 40,v4,30720
Mavv 20,34,96
E_ 132
Mavv 24,34,100
s_nop 0
Tv 42,v5,24576
Tv 44,v5,30720
Mavv 4,30,100
E_ 133
Mavv 8,30,104
E_ 134
Mavv 28,34,104
.Lpad_a2_7:
s_cbranch_execz .Lhop_a2_r
E_ 135
Mavv 32,34,108
C_ 135,134,135
C_ 134,132,133
Mavv 12,30,108
E_ 76
Mavv 16,30,112
E_ 77
Mavv 36,34,112
W_ 0
E_ 78
Mavv 56,38,112
s_nop 0
Tv 30,v2,25600
Tv 32,v2,31744
Mavv 76,42,112
E_ 79
Mavv 72,42,108
s_nop 0
Tv 34,v3,25600
Tv 36,v3,31744
Mavv 52,38,108
C_ 116,76,77
C_ 117,78,79
Mavv 48,38,104
E_ 80
Mavv 68,42,104
E_ 81
Mavv 64,42,100
E_ 82
Mavv 44,38,100
E_ 83
Mavv 40,38,96
C_ 120,80,81
C_ 121,82,83
Mavv 60,42,96
W_ 0
E_ 84
Mavv 80,30,96
s_nop 0
Tv 38,v4,25600
Tv 40,v4,31744
Mavv 100,34,96
E_ 85
Mavv 104,34,100
s_nop 0
Tv 42,v5,25600
Tv 44,v5,31744
Mavv 84,30,100
E_ 86
Mavv 88,30,104
E_ 87
Mavv 108,34,104
C_ 124,84,85
C_ 125,86,87
Mavv 112,34,108
E_ 88
Mavv 92,30,108
E_ 89
Mavv 96,30,112
E_ 90
Mavv 116,34,112
W_ 0
E_ 91
Mavv 136,38,112
s_nop 0
Tv 30,v2,26624
Tv 32,v2,32768
Mavv 156,42,112
C_ 128,88,89
C_ 129,90,91
Mavv 152,42,108
s_nop 0
Tv 34,v3,26624
Tv 36,v3,32768
Mavv 132,38,108
E_ 92
Mavv 128,38,104
E_ 93
Mavv 148,42,104
E_ 94
Mavv 144,42,100
E_ 95
Mavv 124,38,100
C_ 132,92,93
C_ 133,94,95
Mavv 120,38,96
v_mov_b32 v92, 0x3f803f80
v_mov_b32 v93, 0x3f803f80
Mavv 140,42,96
W_ 0
v_mov_b32 v94, 0x3f803f80
v_mov_b32 v95, 0x3f803f80
Mavv 160,30,96
s_nop 0
Tv 38,v4,26624
Tv 40,v4,32768
Mavv 180,34,96
Mavv 184,34,100
s_nop 0
Tv 42,v5,26624
Tv 44,v5,32768
Mavv 164,30,100
Mavv 168,30,104
Mavv 188,34,104
S_ s65,s61,184320
Mavv 192,34,108
S_ m0,s51,24576
Mavv 172,30,108
s_waitcnt vmcnt(6) lgkmcnt(0)
s_barrier
Mavv 176,30,112
Ln v7,32,s65,0
Mavv 196,34,112
Mavv 216,38,112
Dv 30,%0,6144
Mavv 236,42,112
Dv 34,v1,6144
Mavv 232,42,108
Ln v8,32,s65,3072
S_ m0,s51,25472
Mavv 212,38,108
Mavv 208,38,104
Ln v7,32,s65,128
Mavv 228,42,104
Mavv 224,42,100
Ln v8,32,s65,3200
S_ m0,s51,26368
Mavv 204,38,100
Mavv 200,38,96
Ln v7,32,s65,256
Mavv 220,42,96
.Lstep2_14:
Zvvv 76,92,116
Dv 38,%0,7168
Zvvv 80,92,120
Dv 42,v1,7168
Zvvv 84,92,124
Zvvv 88,92,128
Zvvv 92,92,132
W_ 2
Nvvv 96,30,136,56
Mvvv 96,34,156
Nvvv 100,30,140,60
s_nop 1
v_max3_f32 v10, v76, v80, v84
s_nop 0
v_max3_f32 v10, v10, v88, v92
Mvvv 100,34,160
v_cmp_lt_f32 vcc, 0x42000000, v10
s_cbranch_vccnz .Ltramp2_13
Nvvv 104,30,144,64
A_ v46,v46,v76
A_ v47,v47,v80
Mvvv 104,34,164
A_ v48,v48,v84
A_ v49,v49,v88
Nvvv 108,30,148,68
A_ v50,v50,v92
Mvvv 108,34,168
Ln v8,32,s65,3328
Nvvv 112,30,152,72
Mvvv 112,34,172
W_ 0
Mvvv 112,38,192
Dv 30,%0,8192
Mvvv 112,42,212
Dv 34,v1,8192
Mvvv 108,38,188
Mvvv 108,42,208
Mvvv 104,38,184
Mvvv 104,42,204
Mvvv 100,38,180
Mvvv 100,42,200
Mvvv 96,38,176
Mvvv 96,42,196
W_ 0
Mvvv 96,30,216
Dv 38,%0,0
Mvvv 96,34,236
Dv 42,v1,0
Mvvv 100,30,220
Mvvv 100,34,240
Mvvv 104,30,224
Mvvv 104,34,244
Mvvv 108,30,228
Mvvv 108,34,248
s_nop 0
E_ 96
Mvvv 112,30,232
E_ 97
Mvvv 112,34,252
W_ 0
E_ 98
Nvvv 76,38,136,56
Dv 30,%0,1024
Mvvv 76,42,156
Dv 34,v1,1024
Nvvv 80,38,140,60
E_ 99
Mvvv 80,42,160
C_ 99,98,99
C_ 98,96,97
Nvvv 84,38,144,64
E_ 100
Mvvv 84,42,164
E_ 101
Nvvv 88,38,148,68
E_ 102
Mvvv 88,42,168
E_ 103
Nvvv 92,38,152,72
C_ 103,102,103
C_ 102,100,101
Mvvv 92,42,172
W_ 0
E_ 104
Mvvv 92,30,192
Dv 38,%0,2048
Mvvv 92,34,212
Dv 42,v1,2048
Mvvv 88,30,188
E_ 105
Mvvv 88,34,208
E_ 106
Mvvv 84,30,184
S_ s65,s61,172032
E_ 107
Mvvv 84,34,204
S_ m0,s51,0
C_ 107,106,107
C_ 106,104,105
Mvvv 80,30,180
s_waitcnt vmcnt(6) lgkmcnt(0)
s_barrier
Mvvv 80,34,200
Ln v9,36,s65,0
Mvvv 76,30,176
E_ 108
Mvvv 76,34,196
Ln v9,36,s65,3072
S_ m0,s51,896
Mvvv 76,38,216
Tv 30,v2,12288
Tv 32,v2,18432
Mvvv 76,42,236
Ln v9,36,s65,128
Mvvv 80,38,220
s_nop 0
Tv 34,v3,12288
Tv 36,v3,18432
Mvvv 80,42,240
Ln v9,36,s65,3200
S_ m0,s51,1792
Mvvv 84,38,224
E_ 109
Mvvv 84,42,244
Ln v9,36,s65,256
Mvvv 88,38,228
E_ 110
Mvvv 88,42,248
Ln v9,36,s65,3328
Mvvv 92,38,232
E_ 111
Mvvv 92,42,252
W_ 0
C_ 111,110,111
C_ 110,108,109
Mavv 0,30,116
s_nop 0
Tv 38,v4,12288
Tv 40,v4,18432
Mavv 20,34,116
E_ 112
Mavv 24,34,120
s_nop 0
Tv 42,v5,12288
Tv 44,v5,18432
Mavv 4,30,120
E_ 113
Mavv 8,30,124
E_ 114
Mavv 28,34,124
E_ 115
Mavv 32,34,128
C_ 115,114,115
C_ 114,112,113
Mavv 12,30,128
E_ 76
Mavv 16,30,132
E_ 77
Mavv 36,34,132
W_ 0
E_ 78
Mavv 56,38,132
s_nop 0
Tv 30,v2,13312
Tv 32,v2,19456
Mavv 76,42,132
E_ 79
Mavv 72,42,128
s_nop 0
Tv 34,v3,13312
Tv 36,v3,19456
Mavv 52,38,128
C_ 96,76,77
C_ 97,78,79
Mavv 48,38,124
E_ 80
Mavv 68,42,124
E_ 81
)A"
R"A(Mavv 64,42,120
E_ 82
Mavv 44,38,120
E_ 83
Mavv 40,38,116
C_ 100,80,81
C_ 101,82,83
Mavv 60,42,116
W_ 0
E_ 84
Mavv 80,30,116
s_nop 0
Tv 38,v4,13312
Tv 40,v4,19456
Mavv 100,34,116
E_ 85
Mavv 104,34,120
s_nop 0
Tv 42,v5,13312
Tv 44,v5,19456
Mavv 84,30,120
E_ 86
Mavv 88,30,124
E_ 87
Mavv 108,34,124
C_ 104,84,85
C_ 105,86,87
Mavv 112,34,128
E_ 88
Mavv 92,30,128
E_ 89
Mavv 96,30,132
E_ 90
Mavv 116,34,132
W_ 0
E_ 91
Mavv 136,38,132
s_nop 0
Tv 30,v2,14336
Tv 32,v2,20480
Mavv 156,42,132
C_ 108,88,89
C_ 109,90,91
Mavv 152,42,128
s_nop 0
Tv 34,v3,14336
Tv 36,v3,20480
Mavv 132,38,128
E_ 92
Mavv 128,38,124
E_ 93
Mavv 148,42,124
E_ 94
Mavv 144,42,120
E_ 95
Mavv 124,38,120
C_ 112,92,93
C_ 113,94,95
Mavv 120,38,116
v_mov_b32 v92, 0x3f803f80
v_mov_b32 v93, 0x3f803f80
Mavv 140,42,116
W_ 0
v_mov_b32 v94, 0x3f803f80
v_mov_b32 v95, 0x3f803f80
Mavv 160,30,116
s_nop 0
Tv 38,v4,14336
Tv 40,v4,20480
Mavv 180,34,116
Mavv 184,34,120
s_nop 0
Tv 42,v5,14336
Tv 44,v5,20480
Mavv 164,30,120
Mavv 168,30,124
Mavv 188,34,124
S_ s65,s61,184320
Mavv 192,34,128
S_ m0,s51,12288
Mavv 172,30,128
s_waitcnt vmcnt(6) lgkmcnt(0)
s_barrier
Mavv 176,30,132
Ln v9,36,s65,0
Mavv 196,34,132
Mavv 216,38,132
Dv 30,%0,30720
Mavv 236,42,132
Dv 34,v1,30720
Mavv 232,42,128
Ln v9,36,s65,3072
S_ m0,s51,13184
Mavv 212,38,128
Mavv 208,38,124
Ln v9,36,s65,128
Mavv 228,42,124
Mavv 224,42,120
Ln v9,36,s65,3200
S_ m0,s51,14080
Mavv 204,38,120
Mavv 200,38,116
Ln v9,36,s65,256
Mavv 220,42,116
.Lstep2_15:
Zvvv 76,92,96
Dv 38,%0,31744
Zvvv 80,92,100
Dv 42,v1,31744
Zvvv 84,92,104
Zvvv 88,92,108
Zvvv 92,92,112
W_ 2
Nvvv 116,30,136,56
Mvvv 116,34,156
Nvvv 120,30,140,60
s_nop 1
v_max3_f32 v10, v76, v80, v84
s_nop 0
v_max3_f32 v10, v10, v88, v92
Mvvv 120,34,160
v_cmp_lt_f32 vcc, 0x42000000, v10
s_cbranch_vccnz .Ltramp2_14
Nvvv 124,30,144,64
A_ v46,v46,v76
A_ v47,v47,v80
Mvvv 124,34,164
A_ v48,v48,v84
A_ v49,v49,v88
Nvvv 128,30,148,68
A_ v50,v50,v92
Mvvv 128,34,168
Ln v9,36,s65,3328
Nvvv 132,30,152,72
Mvvv 132,34,172
W_ 0
Mvvv 132,38,192
Dv 30,%0,32768
Mvvv 132,42,212
Dv 34,v1,32768
Mvvv 128,38,188
Mvvv 128,42,208
Mvvv 124,38,184
Mvvv 124,42,204
Mvvv 120,38,180
Mvvv 120,42,200
Mvvv 116,38,176
Mvvv 116,42,196
W_ 0
Mvvv 116,30,216
Dv 38,%0,24576
Mvvv 116,34,236
Dv 42,v1,24576
Mvvv 120,30,220
Mvvv 120,34,240
Mvvv 124,30,224
Mvvv 124,34,244
Mvvv 128,30,228
Mvvv 128,34,248
s_nop 0
E_ 116
Mvvv 132,30,232
E_ 117
Mvvv 132,34,252
W_ 0
E_ 118
Nvvv 76,38,136,56
Dv 30,%0,25600
Mvvv 76,42,156
Dv 34,v1,25600
Nvvv 80,38,140,60
E_ 119
Mvvv 80,42,160
C_ 119,118,119
C_ 118,116,117
Nvvv 84,38,144,64
E_ 120
Mvvv 84,42,164
E_ 121
Nvvv 88,38,148,68
E_ 122
Mvvv 88,42,168
E_ 123
Nvvv 92,38,152,72
C_ 123,122,123
C_ 122,120,121
Mvvv 92,42,172
W_ 0
E_ 124
Mvvv 92,30,192
Dv 38,%0,26624
Mvvv 92,34,212
Dv 42,v1,26624
Mvvv 88,30,188
E_ 125
Mvvv 88,34,208
E_ 126
Mvvv 84,30,184
S_ s65,s61,393216
E_ 127
Mvvv 84,34,204
S_ m0,s51,24576
C_ 127,126,127
C_ 126,124,125
Mvvv 80,30,180
s_waitcnt vmcnt(6) lgkmcnt(0)
s_barrier
Mvvv 80,34,200
Ln v9,36,s65,0
Mvvv 76,30,176
E_ 128
Mvvv 76,34,196
Ln v9,36,s65,3072
S_ m0,s51,25472
Mvvv 76,38,216
Tv 30,v2,0
Tv 32,v2,6144
Mvvv 76,42,236
Ln v9,36,s65,128
Mvvv 80,38,220
s_nop 0
Tv 34,v3,0
Tv 36,v3,6144
Mvvv 80,42,240
Ln v9,36,s65,3200
S_ m0,s51,26368
Mvvv 84,38,224
E_ 129
Mvvv 84,42,244
Ln v9,36,s65,256
Mvvv 88,38,228
E_ 130
Mvvv 88,42,248
Ln v9,36,s65,3328
Mvvv 92,38,232
E_ 131
Mvvv 92,42,252
W_ 0
C_ 131,130,131
C_ 130,128,129
Mavv 0,30,96
s_nop 0
Tv 38,v4,0
Tv 40,v4,6144
Mavv 20,34,96
E_ 132
Mavv 24,34,100
s_nop 0
Tv 42,v5,0
Tv 44,v5,6144
Mavv 4,30,100
E_ 133
Mavv 8,30,104
E_ 134
Mavv 28,34,104
E_ 135
Mavv 32,34,108
C_ 135,134,135
C_ 134,132,133
Mavv 12,30,108
E_ 76
Mavv 16,30,112
E_ 77
Mavv 36,34,112
W_ 0
E_ 78
Mavv 56,38,112
s_nop 0
Tv 30,v2,1024
Tv 32,v2,7168
Mavv 76,42,112
E_ 79
Mavv 72,42,108
s_nop 0
Tv 34,v3,1024
Tv 36,v3,7168
Mavv 52,38,108
C_ 116,76,77
C_ 117,78,79
Mavv 48,38,104
E_ 80
Mavv 68,42,104
E_ 81
Mavv 64,42,100
E_ 82
Mavv 44,38,100
E_ 83
Mavv 40,38,96
C_ 120,80,81
C_ 121,82,83
Mavv 60,42,96
W_ 0
E_ 84
Mavv 80,30,96
s_nop 0
Tv 38,v4,1024
Tv 40,v4,7168
Mavv 100,34,96
E_ 85
Mavv 104,34,100
s_nop 0
Tv 42,v5,1024
Tv 44,v5,7168
Mavv 84,30,100
E_ 86
Mavv 88,30,104
E_ 87
Mavv 108,34,104
C_ 124,84,85
C_ 125,86,87
Mavv 112,34,108
E_ 88
Mavv 92,30,108
E_ 89
Mavv 96,30,112
E_ 90
Mavv 116,34,112
W_ 0
E_ 91
Mavv 136,38,112
s_nop 0
Tv 30,v2,2048
Tv 32,v2,8192
Mavv 156,42,112
C_ 128,88,89
C_ 129,90,91
Mavv 152,42,108
s_nop 0
Tv 34,v3,2048
Tv 36,v3,8192
Mavv 132,38,108
E_ 92
Mavv 128,38,104
E_ 93
Mavv 148,42,104
E_ 94
Mavv 144,42,100
E_ 95
Mavv 124,38,100
C_ 132,92,93
C_ 133,94,95
Mavv 120,38,96
v_mov_b32 v92, 0x3f803f80
v_mov_b32 v93, 0x3f803f80
Mavv 140,42,96
W_ 0
v_mov_b32 v94, 0x3f803f80
v_mov_b32 v95, 0x3f803f80
Mavv 160,30,96
s_nop 0
Tv 38,v4,2048
Tv 40,v4,8192
Mavv 180,34,96
Mavv 184,34,100
s_nop 0
Tv 42,v5,2048
Tv 44,v5,8192
Mavv 164,30,100
Mavv 168,30,104
Mavv 188,34,104
S_ s65,s61,393216
Mavv 192,34,108
S_ m0,s51,0
Mavv 172,30,108
s_waitcnt vmcnt(6) lgkmcnt(0)
s_barrier
Mavv 176,30,112
Ln v7,32,s65,0
Mavv 196,34,112
Mavv 216,38,112
s_nop 0
Tv 30,v2,12288
Tv 32,v2,18432
Zvvv 76,92,116
Ln v8,32,s65,3072
S_ m0,s51,896
Zvvv 80,92,120
s_nop 0
Tv 34,v3,12288
Tv 36,v3,18432
Zvvv 84,92,124
Ln v7,32,s65,128
Zvvv 88,92,128
Zvvv 92,92,132
Ln v8,32,s65,3200
S_ m0,s51,1792
Mavv 236,42,112
Mavv 232,42,108
Ln v7,32,s65,256
Mavv 212,38,108
Mavv 208,38,104
v_max3_f32 v10, v76, v80, v84
Mavv 228,42,104
v_max3_f32 v10, v10, v88, v92
v_cmp_lt_f32 vcc, 0x42000000, v10
Mavv 224,42,100
Ln v8,32,s65,3328
Mavv 204,38,100
Mavv 200,38,96
Mavv 220,42,96
s_cbranch_vccnz .Ltramp2_15
.Ljoin2:
W_ 2
Mavv 0,30,116
s_mov_b32 s58, 1
Tv 38,v4,12288
Tv 40,v4,18432
W_ 2
Mavv 20,34,116
R_ v11,a248
R_ v13,a248
Mavv 24,34,120
S_ s53,s51,36864
Tv 42,v5,12288
Tv 44,v5,18432
Mavv 4,30,120
v_add_u32 v12, s53, v13
s_mul_i32 s53, s4, 10240
Mavv 8,30,124
v_add_u32 v13, s53, v13
A_ v46,v46,v76
Mavv 28,34,124
A_ v47,v47,v80
Mavv 32,34,128
A_ v48,v48,v84
A_ v49,v49,v88
Mavv 12,30,128
A_ v50,v50,v92
Mavv 16,30,132
v_rcp_f32 v46, v46
Mavv 36,34,132
W_ 0
v_rcp_f32 v47, v47
Mavv 56,38,132
s_nop 0
Tv 30,v2,13312
Tv 32,v2,19456
Mavv 76,42,132
v_rcp_f32 v48, v48
Mavv 72,42,128
s_nop 0
Tv 34,v3,13312
Tv 36,v3,19456
Mavv 52,38,128
v_rcp_f32 v49, v49
Mavv 48,38,124
v_rcp_f32 v50, v50
Mavv 68,42,124
s_mov_b32 s56, 12288
v_mov_b32 v176, v20
v_mov_b32 v177, v21
Mavv 64,42,120
Dv 136,v13,0
v_mov_b32 v178, v22
Mavv 44,38,120
Dv 140,v13,1024
v_mov_b32 v179, v23
Mavv 40,38,116
Dv 144,v13,2048
v_mov_b32 v180, v24
Mavv 60,42,116
W_ 3
Dv 148,v13,3072
v_mov_b32 v181, v25
Mavv 80,30,116
s_nop 0
Tv 38,v4,13312
Tv 40,v4,19456
Mavv 100,34,116
Dv 152,v13,4096
v_mov_b32 v182, v26
Mavv 104,34,120
s_nop 0
Tv 42,v5,13312
Tv 44,v5,19456
Mavv 84,30,120
Dv 156,v13,5120
v_mov_b32 v183, v27
Mavv 88,30,124
Dv 160,v13,6144
v_mov_b32 v184, v52
Mavv 108,34,124
Dv 164,v13,7168
v_mov_b32 v185, v53
Mavv 112,34,128
Dv 168,v13,8192
v_mov_b32 v186, v54
Mavv 92,30,128
Dv 172,v13,9216
v_mov_b32 v187, v55
Mavv 96,30,132
Dv 208,v12,0
R_ v188,a240
Mavv 116,34,132
W_ 6
Dv 212,v12,1024
R_ v189,a241
Mavv 136,38,132
s_nop 0
Tv 30,v2,14336
Tv 32,v2,20480
Mavv 156,42,132
Dv 216,v12,2048
R_ v190,a242
Mavv 152,42,128
s_nop 0
Tv 34,v3,14336
Tv 36,v3,20480
Mavv 132,38,128
Dv 220,v12,3072
R_ v191,a243
Mavv 128,38,124
Dv 224,v12,4096
R_ v192,a244
Mavv 148,42,124
Dv 228,v12,5120
R_ v193,a245
Mavv 144,42,120
Dv 232,v12,12288
R_ v194,a246
Mavv 124,38,120
Dv 236,v12,13312
R_ v195,a247
Mavv 120,38,116
Dv 240,v12,14336
v_mov_b32 v196, v16
Mavv 140,42,116
W_ 6
Dv 244,v12,15360
v_mov_b32 v197, v17
Mavv 160,30,116
s_nop 0
Tv 38,v4,14336
Tv 40,v4,20480
Mavv 180,34,116
Dv 248,v12,16384
v_mov_b32 v198, v18
Mavv 184,34,120
s_nop 0
Tv 42,v5,14336
Tv 44,v5,20480
Mavv 164,30,120
Dv 252,v12,17408
v_mov_b32 v199, v19
Mavv 168,30,124
v_mov_b32 v200, v28
v_mov_b32 v201, v29
Mavv 188,34,124
v_mov_b32 v202, v51
v_mov_b32 v203, v6
Mavv 192,34,128
v_mov_b32 v204, v14
v_mov_b32 v205, v15
Mavv 172,30,128
S_ s65,s61,405504
R_ v206,a249
R_ v207,a250
Mavv 176,30,132
S_ m0,s51,12288
R_ v96,a0
R_ v97,a1
Mavv 196,34,132
s_waitcnt vmcnt(63) lgkmcnt(0)
s_barrier
R_ v98,a2
Mavv 216,38,132
Ln v7,32,s65,0
Mavv 236,42,132
R_ v99,a3
Mavv 232,42,128
Ln v8,32,s65,3072
S_ m0,s51,13184
Mavv 212,38,128
R_ v100,a20
R_ v101,a21
Mavv 208,38,124
Ln v7,32,s65,128
Mavv 228,42,124
R_ v102,a22
R_ v103,a23
Mavv 224,42,120
Ln v8,32,s65,3200
S_ m0,s51,14080
Mavv 204,38,120
U_ v96,v96,v46
U_ v97,v97,v46
Mavv 200,38,116
Ln v7,32,s65,256
Mavv 220,42,116
U_ v98,v98,v46
U_ v99,v99,v46
U_ v100,v100,v46
U_ v101,v101,v46
U_ v102,v102,v46
U_ v103,v103,v46
C_ 76,96,97
C_ 77,98,99
C_ 78,100,101
C_ 79,102,103
ds_write_b128 v13, v[76:79] offset:0
Ln v8,32,s65,3328
S_ s61,s61,393216
.p2align 3
R_ v104,a4
R_ v105,a5
R_ v106,a6
R_ v107,a7
R_ v108,a24
R_ v109,a25
R_ v110,a26
R_ v111,a27
v_pk_mul_f32 v[104:105], v[104:105], v[46:47] op_sel:[0,1]
v_pk_mul_f32 v[106:107], v[106:107], v[46:47] op_sel:[0,1]
v_pk_mul_f32 v[108:109], v[108:109], v[46:47] op_sel:[0,1]
v_pk_mul_f32 v[110:111], v[110:111], v[46:47] op_sel:[0,1]
C_ 80,104,105
C_ 81,106,107
C_ 82,108,109
C_ 83,110,111
ds_write_b128 v13, v[80:83] offset:1024
.p2align 3
R_ v96,a8
R_ v97,a9
R_ v98,a10
R_ v99,a11
R_ v100,a28
R_ v101,a29
R_ v102,a30
R_ v103,a31
v_pk_mul_f32 v[96:97], v[96:97], v[48:49] op_sel_hi:[1,0]
v_pk_mul_f32 v[98:99], v[98:99], v[48:49] op_sel_hi:[1,0]
v_pk_mul_f32 v[100:101], v[100:101], v[48:49] op_sel_hi:[1,0]
v_pk_mul_f32 v[102:103], v[102:103], v[48:49] op_sel_hi:[1,0]
C_ 84,96,97
C_ 85,98,99
C_ 86,100,101
C_ 87,102,103
ds_write_b128 v13, v[84:87] offset:2048
.p2align 3
R_ v104,a12
R_ v105,a13
R_ v106,a14
R_ v107,a15
R_ v108,a32
R_ v109,a33
R_ v110,a34
R_ v111,a35
v_pk_mul_f32 v[104:105], v[104:105], v[48:49] op_sel:[0,1]
v_pk_mul_f32 v[106:107], v[106:107], v[48:49] op_sel:[0,1]
v_pk_mul_f32 v[108:109], v[108:109], v[48:49] op_sel:[0,1]
v_pk_mul_f32 v[110:111], v[110:111], v[48:49] op_sel:[0,1]
C_ 88,104,105
C_ 89,106,107
C_ 90,108,109
C_ 91,110,111
ds_write_b128 v13, v[88:91] offset:3072
.p2align 3
R_ v96,a16
R_ v97,a17
R_ v98,a18
R_ v99,a19
R_ v100,a36
R_ v101,a37
R_ v102,a38
R_ v103,a39
v_pk_mul_f32 v[96:97], v[96:97], v[50:51] op_sel_hi:[1,0]
v_pk_mul_f32 v[98:99], v[98:99], v[50:51] op_sel_hi:[1,0]
v_pk_mul_f32 v[100:101], v[100:101], v[50:51] op_sel_hi:[1,0]
v_pk_mul_f32 v[102:103], v[102:103], v[50:51] op_sel_hi:[1,0]
C_ 92,96,97
C_ 93,98,99
C_ 94,100,101
C_ 95,102,103
ds_write_b128 v13, v[92:95] offset:4096
.p2align 3
R_ v104,a40
R_ v105,a41
R_ v106,a42
R_ v107,a43
R_ v108,a60
R_ v109,a61
R_ v110,a62
R_ v111,a63
v_pk_mul_f32 v[104:105], v[104:105], v[46:47] op_sel_hi:[1,0]
v_pk_mul_f32 v[106:107], v[106:107], v[46:47] op_sel_hi:[1,0]
v_pk_mul_f32 v[108:109], v[108:109], v[46:47] op_sel_hi:[1,0]
v_pk_mul_f32 v[110:111], v[110:111], v[46:47] op_sel_hi:[1,0]
C_ 76,104,105
C_ 77,106,107
C_ 78,108,109
C_ 79,110,111
ds_write_b128 v13, v[76:79] offset:5120
.p2align 3
R_ v96,a44
R_ v97,a45
R_ v98,a46
R_ v99,a47
R_ v100,a64
R_ v101,a65
R_ v102,a66
R_ v103,a67
v_pk_mul_f32 v[96:97], v[96:97], v[46:47] op_sel:[0,1]
v_pk_mul_f32 v[98:99], v[98:99], v[46:47] op_sel:[0,1]
v_pk_mul_f32 v[100:101], v[100:101], v[46:47] op_sel:[0,1]
v_pk_mul_f32 v[102:103], v[102:103], v[46:47] op_sel:[0,1]
C_ 80,96,97
C_ 81,98,99
C_ 82,100,101
C_ 83,102,103
ds_write_b128 v13, v[80:83] offset:6144
.p2align 3
R_ v104,a48
R_ v105,a49
R_ v106,a50
R_ v107,a51
R_ v108,a68
R_ v109,a69
R_ v110,a70
R_ v111,a71
v_pk_mul_f32 v[104:105], v[104:105], v[48:49] op_sel_hi:[1,0]
v_pk_mul_f32 v[106:107], v[106:107], v[48:49] op_sel_hi:[1,0]
v_pk_mul_f32 v[108:109], v[108:109], v[48:49] op_sel_hi:[1,0]
v_pk_mul_f32 v[110:111], v[110:111], v[48:49] op_sel_hi:[1,0]
C_ 84,104,105
C_ 85,106,107
C_ 86,108,109
C_ 87,110,111
ds_write_b128 v13, v[84:87] offset:7168
.p2align 3
R_ v96,a52
R_ v97,a53
R_ v98,a54
R_ v99,a55
R_ v100,a72
R_ v101,a73
R_ v102,a74
R_ v103,a75
v_pk_mul_f32 v[96:97], v[96:97], v[48:49] op_sel:[0,1]
v_pk_mul_f32 v[98:99], v[98:99], v[48:49] op_sel:[0,1]
v_pk_mul_f32 v[100:101], v[100:101], v[48:49] op_sel:[0,1]
v_pk_mul_f32 v[102:103], v[102:103], v[48:49] op_sel:[0,1]
C_ 88,96,97
C_ 89,98,99
C_ 90,100,101
C_ 91,102,103
ds_write_b128 v13, v[88:91] offset:8192
.p2align 3
R_ v104,a56
R_ v105,a57
R_ v106,a58
R_ v107,a59
R_ v108,a76
R_ v109,a77
R_ v110,a78
R_ v111,a79
v_pk_mul_f32 v[104:105], v[104:105], v[50:51] op_sel_hi:[1,0]
v_pk_mul_f32 v[106:107], v[106:107], v[50:51] op_sel_hi:[1,0]
v_pk_mul_f32 v[108:109], v[108:109], v[50:51] op_sel_hi:[1,0]
v_pk_mul_f32 v[110:111], v[110:111], v[50:51] op_sel_hi:[1,0]
C_ 92,104,105
C_ 93,106,107
C_ 94,108,109
C_ 95,110,111
ds_write_b128 v13, v[92:95] offset:9216
.p2align 3
R_ v104,a92
R_ v105,a93
R_ v106,a94
R_ v107,a95
R_ v108,a112
R_ v109,a113
R_ v110,a114
R_ v111,a115
v_pk_mul_f32 v[104:105], v[104:105], v[48:49] op_sel:[0,1]
v_pk_mul_f32 v[106:107], v[106:107], v[48:49] op_sel:[0,1]
v_pk_mul_f32 v[108:109], v[108:109], v[48:49] op_sel:[0,1]
v_pk_mul_f32 v[110:111], v[110:111], v[48:49] op_sel:[0,1]
C_ 88,104,105
C_ 89,106,107
C_ 90,108,109
C_ 91,110,111
X_ a240,v88
X_ a241,v89
X_ a242,v90
X_ a243,v91
.p2align 3
R_ v96,a96
R_ v97,a97
R_ v98,a98
R_ v99,a99
R_ v100,a116
R_ v101,a117
R_ v102,a118
R_ v103,a119
v_pk_mul_f32 v[96:97], v[96:97], v[50:51] op_sel_hi:[1,0]
v_pk_mul_f32 v[98:99], v[98:99], v[50:51] op_sel_hi:[1,0]
v_pk_mul_f32 v[100:101], v[100:101], v[50:51] op_sel_hi:[1,0]
v_pk_mul_f32 v[102:103], v[102:103], v[50:51] op_sel_hi:[1,0]
C_ 92,96,97
C_ 93,98,99
C_ 94,100,101
C_ 95,102,103
X_ a244,v92
X_ a245,v93
X_ a246,v94
X_ a247,v95
.p2align 3
R_ v96,a124
R_ v97,a125
R_ v98,a126
R_ v99,a127
R_ v100,a144
R_ v101,a145
R_ v102,a146
R_ v103,a147
v_pk_mul_f32 v[96:97], v[96:97], v[46:47] op_sel:[0,1]
v_pk_mul_f32 v[98:99], v[98:99], v[46:47] op_sel:[0,1]
v_pk_mul_f32 v[100:101], v[100:101], v[46:47] op_sel:[0,1]
v_pk_mul_f32 v[102:103], v[102:103], v[46:47] op_sel:[0,1]
C_ 80,96,97
C_ 81,98,99
C_ 82,100,101
C_ 83,102,103
v_mov_b32 v28, v80
v_mov_b32 v29, v81
v_mov_b32 v51, v82
v_mov_b32 v6, v83
.p2align 3
R_ v104,a128
R_ v105,a129
R_ v106,a130
R_ v107,a131
R_ v108,a148
R_ v109,a149
R_ v110,a150
R_ v111,a151
v_pk_mul_f32 v[104:105], v[104:105], v[48:49] op_sel_hi:[1,0]
v_pk_mul_f32 v[106:107], v[106:107], v[48:49] op_sel_hi:[1,0]
v_pk_mul_f32 v[108:109], v[108:109], v[48:49] op_sel_hi:[1,0]
v_pk_mul_f32 v[110:111], v[110:111], v[48:49] op_sel_hi:[1,0]
C_ 84,104,105
C_ 85,106,107
C_ 86,108,109
C_ 87,110,111
v_mov_b32 v14, v84
v_mov_b32 v15, v85
X_ a249,v86
X_ a250,v87
.p2align 3
R_ v96,a132
R_ v97,a133
R_ v98,a134
R_ v99,a135
R_ v100,a152
R_ v101,a153
R_ v102,a154
R_ v103,a155
v_pk_mul_f32 v[96:97], v[96:97], v[48:49] op_sel:[0,1]
v_pk_mul_f32 v[98:99], v[98:99], v[48:49] op_sel:[0,1]
v_pk_mul_f32 v[100:101], v[100:101], v[48:49] op_sel:[0,1]
v_pk_mul_f32 v[102:103], v[102:103], v[48:49] op_sel:[0,1]
C_ 88,96,97
C_ 89,98,99
C_ 90,100,101
C_ 91,102,103
ds_write_b128 v12, v[88:91] offset:0
.p2align 3
R_ v104,a136
R_ v105,a137
R_ v106,a138
R_ v107,a139
R_ v108,a156
R_ v109,a157
R_ v110,a158
R_ v111,a159
v_pk_mul_f32 v[104:105], v[104:105], v[50:51] op_sel_hi:[1,0]
v_pk_mul_f32 v[106:107], v[106:107], v[50:51] op_sel_hi:[1,0]
v_pk_mul_f32 v[108:109], v[108:109], v[50:51] op_sel_hi:[1,0]
v_pk_mul_f32 v[110:111], v[110:111], v[50:51] op_sel_hi:[1,0]
C_ 92,104,105
C_ 93,106,107
C_ 94,108,109
C_ 95,110,111
ds_write_b128 v12, v[92:95] offset:1024
.p2align 3
R_ v96,a160
R_ v97,a161
R_ v98,a162
R_ v99,a163
R_ v100,a180
R_ v101,a181
R_ v102,a182
R_ v103,a183
v_pk_mul_f32 v[96:97], v[96:97], v[46:47] op_sel_hi:[1,0]
v_pk_mul_f32 v[98:99], v[98:99], v[46:47] op_sel_hi:[1,0]
v_pk_mul_f32 v[100:101], v[100:101], v[46:47] op_sel_hi:[1,0]
v_pk_mul_f32 v[102:103], v[102:103], v[46:47] op_sel_hi:[1,0]
C_ 76,96,97
C_ 77,98,99
C_ 78,100,101
C_ 79,102,103
ds_write_b128 v12, v[76:79] offset:2048
.p2align 3
R_ v104,a164
R_ v105,a165
R_ v106,a166
R_ v107,a167
R_ v108,a184
R_ v109,a185
R_ v110,a186
R_ v111,a187
v_pk_mul_f32 v[104:105], v[104:105], v[46:47] op_sel:[0,1]
v_pk_mul_f32 v[106:107], v[106:107], v[46:47] op_sel:[0,1]
v_pk_mul_f32 v[108:109], v[108:109], v[46:47] op_sel:[0,1]
v_pk_mul_f32 v[110:111], v[110:111], v[46:47] op_sel:[0,1]
C_ 80,104,105
C_ 81,106,107
C_ 82,108,109
C_ 83,110,111
ds_write_b128 v12, v[80:83] offset:3072
.p2align 3
R_ v96,a168
R_ v97,a169
R_ v98,a170
R_ v99,a171
R_ v100,a188
R_ v101,a189
R_ v102,a190
R_ v103,a191
v_pk_mul_f32 v[96:97], v[96:97], v[48:49] op_sel_hi:[1,0]
v_pk_mul_f32 v[98:99], v[98:99], v[48:49] op_sel_hi:[1,0]
v_pk_mul_f32 v[100:101], v[100:101], v[48:49] op_sel_hi:[1,0]
v_pk_mul_f32 v[102:103], v[102:103], v[48:49] op_sel_hi:[1,0]
C_ 84,96,97
C_ 85,98,99
C_ 86,100,101
C_ 87,102,103
ds_write_b128 v12, v[84:87] offset:4096
.p2align 3
R_ v104,a172
R_ v105,a173
R_ v106,a174
R_ v107,a175
R_ v108,a192
R_ v109,a193
R_ v110,a194
R_ v111,a195
v_pk_mul_f32 v[104:105], v[104:105], v[48:49] op_sel:[0,1]
v_pk_mul_f32 v[106:107], v[106:107], v[48:49] op_sel:[0,1]
v_pk_mul_f32 v[108:109], v[108:109], v[48:49] op_sel:[0,1]
v_pk_mul_f32 v[110:111], v[110:111], v[48:49] op_sel:[0,1]
C_ 88,104,105
C_ 89,106,107
C_ 90,108,109
C_ 91,110,111
ds_write_b128 v12, v[88:91] offset:5120
.p2align 3
R_ v96,a176
R_ v97,a177
R_ v98,a178
R_ v99,a179
R_ v100,a196
R_ v101,a197
R_ v102,a198
R_ v103,a199
v_pk_mul_f32 v[96:97], v[96:97], v[50:51] op_sel_hi:[1,0]
v_pk_mul_f32 v[98:99], v[98:99], v[50:51] op_sel_hi:[1,0]
v_pk_mul_f32 v[100:101], v[100:101], v[50:51] op_sel_hi:[1,0]
v_pk_mul_f32 v[102:103], v[102:103], v[50:51] op_sel_hi:[1,0]
C_ 92,96,97
C_ 93,98,99
C_ 94,100,101
C_ 95,102,103
ds_write_b128 v12, v[92:95] offset:12288
.p2align 3
R_ v104,a200
R_ v105,a201
R_ v106,a202
R_ v107,a203
R_ v108,a220
R_ v109,a221
R_ v110,a222
R_ v111,a223
v_pk_mul_f32 v[104:105], v[104:105], v[46:47] op_sel_hi:[1,0]
v_pk_mul_f32 v[106:107], v[106:107], v[46:47] op_sel_hi:[1,0]
v_pk_mul_f32 v[108:109], v[108:109], v[46:47] op_sel_hi:[1,0]
v_pk_mul_f32 v[110:111], v[110:111], v[46:47] op_sel_hi:[1,0]
C_ 76,104,105
C_ 77,106,107
C_ 78,108,109
C_ 79,110,111
ds_write_b128 v12, v[76:79] offset:13312
.p2align 3
R_ v96,a204
R_ v97,a205
R_ v98,a206
R_ v99,a207
R_ v100,a224
R_ v101,a225
R_ v102,a226
R_ v103,a227
v_pk_mul_f32 v[96:97], v[96:97], v[46:47] op_sel:[0,1]
v_pk_mul_f32 v[98:99], v[98:99], v[46:47] op_sel:[0,1]
v_pk_mul_f32 v[100:101], v[100:101], v[46:47] op_sel:[0,1]
v_pk_mul_f32 v[102:103], v[102:103], v[46:47] op_sel:[0,1]
C_ 80,96,97
C_ 81,98,99
C_ 82,100,101
C_ 83,102,103
ds_write_b128 v12, v[80:83] offset:14336
.p2align 3
R_ v104,a208
R_ v105,a209
R_ v106,a210
R_ v107,a211
R_ v108,a228
R_ v109,a229
R_ v110,a230
R_ v111,a231
v_pk_mul_f32 v[104:105], v[104:105], v[48:49] op_sel_hi:[1,0]
v_pk_mul_f32 v[106:107], v[106:107], v[48:49] op_sel_hi:[1,0]
v_pk_mul_f32 v[108:109], v[108:109], v[48:49] op_sel_hi:[1,0]
v_pk_mul_f32 v[110:111], v[110:111], v[48:49] op_sel_hi:[1,0]
C_ 84,104,105
C_ 85,106,107
C_ 86,108,109
C_ 87,110,111
ds_write_b128 v12, v[84:87] offset:15360
s_waitcnt vmcnt(6) lgkmcnt(0)
s_barrier
Dv 30,%0,6144
Dv 34,v1,6144
Dv 38,%0,7168
.p2align 3
R_ v96,a212
R_ v97,a213
R_ v98,a214
R_ v99,a215
R_ v100,a232
R_ v101,a233
R_ v102,a234
R_ v103,a235
v_pk_mul_f32 v[96:97], v[96:97], v[48:49] op_sel:[0,1]
v_pk_mul_f32 v[98:99], v[98:99], v[48:49] op_sel:[0,1]
v_pk_mul_f32 v[100:101], v[100:101], v[48:49] op_sel:[0,1]
v_pk_mul_f32 v[102:103], v[102:103], v[48:49] op_sel:[0,1]
C_ 88,96,97
C_ 89,98,99
C_ 90,100,101
C_ 91,102,103
ds_write_b128 v12, v[88:91] offset:16384
.p2align 3
R_ v104,a216
R_ v105,a217
R_ v106,a218
R_ v107,a219
R_ v108,a236
R_ v109,a237
R_ v110,a238
R_ v111,a239
v_pk_mul_f32 v[104:105], v[104:105], v[50:51] op_sel_hi:[1,0]
v_pk_mul_f32 v[106:107], v[106:107], v[50:51] op_sel_hi:[1,0]
v_pk_mul_f32 v[108:109], v[108:109], v[50:51] op_sel_hi:[1,0]
v_pk_mul_f32 v[110:111], v[110:111], v[50:51] op_sel_hi:[1,0]
C_ 92,104,105
C_ 93,106,107
C_ 94,108,109
C_ 95,110,111
ds_write_b128 v12, v[92:95] offset:17408
.Latt3:
W_ 4
Zvvv 96,30,136
Dv 42,v1,7168
R_ v116,a80
Zvvv 100,30,140
R_ v117,a81
R_ v118,a82
Zvvv 104,30,144
R_ v119,a83
R_ v120,a100
Zvvv 108,30,148
R_ v121,a101
R_ v122,a102
Zvvv 112,30,152
W_ 4
R_ v123,a103
U_ v116,v116,v46
Mvvv 96,34,156
Dv 30,%0,8192
U_ v117,v117,v46
Mvvv 100,34,160
U_ v118,v118,v46
U_ v119,v119,v46
Mvvv 104,34,164
U_ v120,v120,v46
U_ v121,v121,v46
Mvvv 108,34,168
U_ v122,v122,v46
U_ v123,v123,v46
Mvvv 112,34,172
W_ 4
C_ 20,116,117
C_ 21,118,119
Mvvv 96,38,176
Dv 34,v1,8192
C_ 22,120,121
Mvvv 100,38,180
C_ 23,122,123
R_ v124,a84
Mvvv 104,38,184
R_ v125,a85
R_ v126,a86
Mvvv 108,38,188
R_ v127,a87
R_ v128,a104
Mvvv 112,38,192
W_ 2
R_ v129,a105
R_ v130,a106
Mvvv 96,42,196
Dv 38,%0,0
R_ v131,a107
Mvvv 100,42,200
U_ v124,v124,v47
U_ v125,v125,v47
Mvvv 104,42,204
U_ v126,v126,v47
U_ v127,v127,v47
Mvvv 108,42,208
U_ v128,v128,v47
U_ v129,v129,v47
Mvvv 112,42,212
W_ 2
U_ v130,v130,v47
U_ v131,v131,v47
Mvvv 96,30,216
Dv 42,v1,0
C_ 24,124,125
Mvvv 100,30,220
C_ 25,126,127
C_ 26,128,129
Mvvv 104,30,224
C_ 27,130,131
R_ v116,a88
Mvvv 108,30,228
R_ v117,a89
R_ v118,a90
Mvvv 112,30,232
W_ 2
R_ v119,a91
R_ v120,a108
Mvvv 96,34,236
Dv 30,%0,1024
R_ v121,a109
Mvvv 100,34,240
R_ v122,a110
R_ v123,a111
Mvvv 104,34,244
U_ v116,v116,v48
U_ v117,v117,v48
Mvvv 108,34,248
U_ v118,v118,v48
U_ v119,v119,v48
Mvvv 112,34,252
W_ 2
U_ v120,v120,v48
U_ v121,v121,v48
Zvvv 76,38,136
Dv 34,v1,1024
U_ v122,v122,v48
Zvvv 80,38,140
U_ v123,v123,v48
C_ 52,116,117
Zvvv 84,38,144
C_ 53,118,119
C_ 54,120,121
Zvvv 88,38,148
C_ 55,122,123
R_ v124,a120
Zvvv 92,38,152
W_ 2
R_ v125,a121
R_ v126,a122
Mvvv 76,42,156
Dv 38,%0,2048
R_ v127,a123
Mvvv 80,42,160
R_ v128,a140
R_ v129,a141
Mvvv 84,42,164
R_ v130,a142
R_ v131,a143
Mvvv 88,42,168
U_ v124,v124,v46
U_ v125,v125,v46
Mvvv 92,42,172
W_ 2
U_ v126,v126,v46
U_ v127,v127,v46
Mvvv 76,30,176
Dv 42,v1,2048
U_ v128,v128,v46
Mvvv 80,30,180
U_ v129,v129,v46
U_ v130,v130,v46
Mvvv 84,30,184
U_ v131,v131,v46
C_ 16,124,125
Mvvv 88,30,188
C_ 17,126,127
C_ 18,128,129
Mvvv 92,30,192
s_waitcnt vmcnt(0) lgkmcnt(0)
s_barrier
Mvvv 76,34,196
S_ s65,s61,24576
Dv 30,%0,18432
C_ 19,130,131
Mvvv 76,38,216
S_ m0,s51,0
v_max3_f32 v56, v96, v97, v98
v_max_f32 v56, v56, v99
Mvvv 76,42,236
Ln v7,32,s65,0
Mvvv 80,34,200
v_max3_f32 v60, v100, v101, v102
v_max_f32 v60, v60, v103
Mvvv 80,38,220
Ln v8,32,s65,3072
Mvvv 80,42,240
S_ m0,s51,896
v_max3_f32 v64, v104, v105, v106
v_max_f32 v64, v64, v107
Mvvv 84,34,204
Ln v7,32,s65,128
Mvvv 84,38,224
v_max3_f32 v68, v108, v109, v110
v_max_f32 v68, v68, v111
Mvvv 84,42,244
Ln v8,32,s65,3200
Mvvv 88,34,208
S_ m0,s51,1792
v_max3_f32 v72, v112, v113, v114
v_max_f32 v72, v72, v115
Mvvv 88,38,228
Ln v7,32,s65,256
Mvvv 88,42,248
v_max3_f32 v56, v56, v76, v77
v_max3_f32 v56, v56, v78, v79
Mvvv 92,34,212
Ln v8,32,s65,3328
Mvvv 92,38,232
v_max3_f32 v60, v60, v80, v81
v_max3_f32 v60, v60, v82, v83
Mvvv 92,42,252
v_max3_f32 v64, v64, v84, v85
v_max3_f32 v64, v64, v86, v87
Dv 34,v1,18432
.p2align 3
v_mov_b32 v11, 0x3f803f80
v_permlane32_swap_b32 v56, v60
v_max3_f32 v68, v68, v88, v89
v_max3_f32 v68, v68, v90, v91
v_max_f32 v56, v56, v60
v_max3_f32 v72, v72, v92, v93
v_max3_f32 v72, v72, v94, v95
v_mov_b32 v73, v72
v_permlane32_swap_b32 v64, v68
v_max_f32 v64, v64, v68
v_permlane32_swap_b32 v72, v73
v_max_f32 v72, v72, v73
v_mov_b32 v73, v72
v_permlane16_swap_b32 v56, v64
v_max_f32 v56, v56, v64
v_permlane16_swap_b32 v72, v73
B_ v56,0xc0e00000,v56
v_max_f32 v72, v72, v73
v_mov_b32 v64, v56
B_ v72,0xc0e00000,v72
v_mov_b32 v73, v72
v_permlane16_swap_b32 v56, v64
v_mov_b32 v60, v56
v_mov_b32 v68, v64
v_mov_b64 v[74:75], v[72:73]
v_permlane32_swap_b32 v56, v60
v_permlane32_swap_b32 v64, v68
v_mov_b32 v57, v56
v_mov_b32 v61, v60
v_mov_b32 v65, v64
v_mov_b32 v69, v68
v_pk_add_f32 v[96:97], v[96:97], v[56:57]
v_pk_add_f32 v[98:99], v[98:99], v[56:57]
v_pk_add_f32 v[76:77], v[76:77], v[56:57]
v_pk_add_f32 v[78:79], v[78:79], v[56:57]
v_mov_b64 v[58:59], v[56:57]
v_pk_add_f32 v[100:101], v[100:101], v[60:61]
v_pk_add_f32 v[102:103], v[102:103], v[60:61]
v_pk_add_f32 v[80:81], v[80:81], v[60:61]
v_pk_add_f32 v[82:83], v[82:83], v[60:61]
v_mov_b64 v[62:63], v[60:61]
v_pk_add_f32 v[104:105], v[104:105], v[64:65]
v_pk_add_f32 v[106:107], v[106:107], v[64:65]
v_pk_add_f32 v[84:85], v[84:85], v[64:65]
v_pk_add_f32 v[86:87], v[86:87], v[64:65]
v_mov_b64 v[66:67], v[64:65]
v_pk_add_f32 v[108:109], v[108:109], v[68:69]
v_pk_add_f32 v[110:111], v[110:111], v[68:69]
v_pk_add_f32 v[88:89], v[88:89], v[68:69]
v_pk_add_f32 v[90:91], v[90:91], v[68:69]
v_mov_b64 v[70:71], v[68:69]
v_pk_add_f32 v[112:113], v[112:113], v[72:73]
v_pk_add_f32 v[114:115], v[114:115], v[72:73]
v_pk_add_f32 v[92:93], v[92:93], v[72:73]
v_pk_add_f32 v[94:95], v[94:95], v[72:73]
E_ 96
E_ 97
E_ 98
E_ 99
E_ 76
E_ 77
E_ 78
E_ 79
C_ 99,98,99
C_ 98,96,97
C_ 96,76,77
C_ 97,78,79
E_ 100
E_ 101
E_ 102
E_ 103
E_ 80
E_ 81
E_ 82
E_ 83
C_ 103,102,103
C_ 102,100,101
C_ 100,80,81
C_ 101,82,83
.Lstep3_1:
W_ 1
Nvvv 116,30,136,56
s_nop 0
Dv 38,%0,19456
Dv 42,v1,19456
W_ 2
Mvvv 116,34,156
E_ 104
Nvvv 120,30,140,60
E_ 105
Mvvv 120,34,160
E_ 106
Nvvv 124,30,144,64
E_ 107
Mvvv 124,34,164
E_ 84
Nvvv 128,30,148,68
E_ 85
Mvvv 128,34,168
E_ 86
Nvvv 132,30,152,72
E_ 87
Mvvv 132,34,172
W_ 0
C_ 107,106,107
C_ 106,104,105
Mvvv 132,38,192
s_nop 0
Dv 30,%0,20480
Dv 34,v1,20480
Mvvv 132,42,212
C_ 104,84,85
C_ 105,86,87
Mvvv 128,38,188
E_ 108
Mvvv 128,42,208
E_ 109
Mvvv 124,38,184
E_ 110
Mvvv 124,42,204
E_ 111
Mvvv 120,38,180
E_ 88
Mvvv 120,42,200
E_ 89
Mvvv 116,38,176
E_ 90
Mvvv 116,42,196
W_ 0
E_ 91
Mvvv 116,30,216
s_nop 0
Dv 38,%0,12288
Dv 42,v1,12288
Mvvv 116,34,236
C_ 111,110,111
C_ 110,108,109
Mvvv 120,30,220
C_ 108,88,89
C_ 109,90,91
Mvvv 120,34,240
E_ 112
Mvvv 124,30,224
E_ 113
Mvvv 124,34,244
E_ 114
Mvvv 128,30,228
E_ 115
Mvvv 128,34,248
E_ 92
Mvvv 132,30,232
E_ 93
Mvvv 132,34,252
W_ 0
E_ 94
Nvvv 76,38,136,56
s_nop 0
Dv 30,%0,13312
Dv 34,v1,13312
Mvvv 76,42,156
E_ 95
Nvvv 80,38,140,60
C_ 115,114,115
C_ 114,112,113
Mvvv 80,42,160
C_ 112,92,93
C_ 113,94,95
Nvvv 84,38,144,64
E_ 116
Mvvv 84,42,164
E_ 117
Nvvv 88,38,148,68
E_ 118
Mvvv 88,42,168
E_ 119
Nvvv 92,38,152,72
C_ 119,118,119
C_ 118,116,117
Mvvv 92,42,172
W_ 0
E_ 120
Mvvv 92,30,192
s_nop 0
Dv 38,%0,14336
Dv 42,v1,14336
Mvvv 92,34,212
X_ a180,v11
X_ a181,v11
Mvvv 88,30,188
X_ a182,v11
X_ a183,v11
Mvvv 88,34,208
E_ 121
Mvvv 84,30,184
E_ 122
Mvvv 84,34,204
E_ 123
Mvvv 80,30,180
C_ 123,122,123
C_ 122,120,121
Mvvv 80,34,200
E_ 124
Mvvv 76,30,176
E_ 125
Mvvv 76,34,196
S_ s65,s61,12288
E_ 126
W_ 1
Mvvv 76,38,216
S_ m0,s51,12288
E_ 127
W_ 0
Mvvv 76,42,236
s_waitcnt vmcnt(18) lgkmcnt(0)
s_barrier
C_ 127,126,127
Mvvv 80,38,220
Ln v9,36,s65,0
Mvvv 80,42,240
C_ 126,124,125
Mvvv 84,38,224
Ln v9,36,s65,3072
S_ m0,s51,13184
Mvvv 84,42,244
s_nop 0
Tv 30,v2,24576
Tv 32,v2,30720
Mvvv 88,38,228
Ln v9,36,s65,128
Mvvv 88,42,248
s_nop 0
Tv 34,v3,24576
Tv 36,v3,30720
Mvvv 92,38,232
Ln v9,36,s65,3200
S_ m0,s51,14080
Mvvv 92,42,252
E_ 128
Zaav 220,180,96
Ln v9,36,s65,256
Zaav 224,180,100
E_ 129
Zaav 228,180,104
Ln v9,36,s65,3328
Zaav 232,180,108
E_ 130
Zaav 236,180,112
W_ 0
E_ 131
Zavv 0,30,96
s_nop 0
Tv 38,v4,24576
Tv 40,v4,30720
Zavv 20,34,96
C_ 131,130,131
C_ 130,128,129
Zavv 24,34,100
s_nop 0
Tv 42,v5,24576
Tv 44,v5,30720
Zavv 4,30,100
E_ 132
Zavv 8,30,104
E_ 133
Zavv 28,34,104
R_ v46,a220
R_ v47,a224
Zavv 32,34,108
R_ v48,a228
R_ v49,a232
Zavv 12,30,108
R_ v50,a236
Zavv 16,30,112
E_ 134
Zavv 36,34,112
W_ 0
E_ 135
Zavv 56,38,112
s_nop 0
Tv 30,v2,25600
Tv 32,v2,31744
Zavv 76,42,112
C_ 135,134,135
C_ 134,132,133
Zavv 72,42,108
s_nop 0
Tv 34,v3,25600
Tv 36,v3,31744
Zavv 52,38,108
E_ 76
Zavv 48,38,104
E_ 77
Zavv 68,42,104
E_ 78
Zavv 64,42,100
E_ 79
Zavv 44,38,100
C_ 116,76,77
C_ 117,78,79
Zavv 40,38,96
E_ 80
Zavv 60,42,96
W_ 0
E_ 81
Zavv 80,30,96
s_nop 0
Tv 38,v4,25600
Tv 40,v4,31744
Zavv 100,34,96
E_ 82
Zavv 104,34,100
s_nop 0
Tv 42,v5,25600
Tv 44,v5,31744
Zavv 84,30,100
E_ 83
Zavv 88,30,104
C_ 120,80,81
C_ 121,82,83
Zavv 108,34,104
E_ 84
Zavv 112,34,108
E_ 85
Zavv 92,30,108
E_ 86
Zavv 96,30,112
E_ 87
Zavv 116,34,112
W_ 0
C_ 124,84,85
C_ 125,86,87
Zavv 136,38,112
s_nop 0
Tv 30,v2,26624
Tv 32,v2,32768
Zavv 156,42,112
E_ 88
Zavv 152,42,108
s_nop 0
Tv 34,v3,26624
Tv 36,v3,32768
Zavv 132,38,108
E_ 89
Zavv 128,38,104
E_ 90
Zavv 148,42,104
E_ 91
Zavv 144,42,100
C_ 128,88,89
C_ 129,90,91
Zavv 124,38,100
E_ 92
Zavv 120,38,96
E_ 93
Zavv 140,42,96
W_ 0
E_ 94
Zavv 160,30,96
s_nop 0
Tv 38,v4,26624
Tv 40,v4,32768
Zavv 180,34,96
E_ 95
Zavv 184,34,100
s_nop 0
Tv 42,v5,26624
Tv 44,v5,32768
Zavv 164,30,100
C_ 132,92,93
C_ 133,94,95
Zavv 168,30,104
v_mov_b32 v92, 0x3f803f80
v_mov_b32 v93, 0x3f803f80
Zavv 188,34,104
S_ s65,s61,36864
v_mov_b32 v94, 0x3f803f80
v_mov_b32 v95, 0x3f803f80
Zavv 192,34,108
S_ m0,s51,24576
Zavv 172,30,108
s_waitcnt vmcnt(6) lgkmcnt(0)
s_barrier
Zavv 176,30,112
Ln v7,32,s65,0
Zavv 196,34,112
Zavv 216,38,112
Dv 30,%0,6144
Zavv 236,42,112
Dv 34,v1,6144
Zavv 232,42,108
Ln v8,32,s65,3072
S_ m0,s51,25472
Zavv 212,38,108
Zavv 208,38,104
Ln v7,32,s65,128
Zavv 228,42,104
Zavv 224,42,100
Ln v8,32,s65,3200
S_ m0,s51,26368
Zavv 204,38,100
Zavv 200,38,96
Ln v7,32,s65,256
Zavv 220,42,96
Ln v8,32,s65,3328
W_ 0
s_nop 7
s_branch .Lstep1_2
.Latt1:
R_ v13,a248
S_ s53,s51,36864
v_add_u32 v12, s53, v13
s_mul_i32 s53, s4, 10240
S_ s53,s53,0
v_add_u32 v13, s53, v13
W_ 2
.Lpad_a1_0:
s_cbranch_execz .Lhop_a1_r
Zvvv 96,30,136
Dv 42,v1,44032
R_ v2,a245
Zvvv 100,30,140
ds_write_b128 v13, a[0:3] offset:0
Zvvv 104,30,144
v_xor_b32 v3, 32, v2
v_xor_b32 v4, 64, v2
Zvvv 108,30,148
ds_write_b128 v13, a[4:7] offset:1024
Zvvv 112,30,152
W_ 4
v_xor_b32 v5, 96, v2
s_mov_b64 s[24:25], s[16:17]
Mvvv 96,34,156
s_mov_b32 s26, 7864320
Dv 30,%0,45056
s_mov_b64 s[28:29], s[22:23]
Mvvv 100,34,160
ds_write_b128 v13, a[8:11] offset:2048
Mvvv 104,34,164
s_mov_b32 s30, 1638400
R_ v20,a80
s_mul_i32 s52, s4, 10240
Mvvv 108,34,168
ds_write_b128 v13, a[12:15] offset:3072
Mvvv 112,34,172
W_ 6
R_ v21,a81
R_ v22,a82
Mvvv 96,38,176
Dv 34,v1,45056
R_ v23,a83
Mvvv 100,38,180
ds_write_b128 v13, a[16:19] offset:4096
Mvvv 104,38,184
R_ v24,a84
R_ v25,a85
Mvvv 108,38,188
ds_write_b128 v13, a[40:43] offset:5120
Mvvv 112,38,192
W_ 8
R_ v26,a86
R_ v27,a87
Mvvv 96,42,196
Dv 38,%0,36864
R_ v52,a88
Mvvv 100,42,200
ds_write_b128 v13, a[44:47] offset:6144
Mvvv 104,42,204
R_ v53,a89
R_ v54,a90
Mvvv 108,42,208
ds_write_b128 v13, a[48:51] offset:7168
Mvvv 112,42,212
W_ 8
R_ v55,a91
v_accvgpr_mov_b32 a240, a92
Mvvv 96,30,216
Dv 42,v1,36864
v_accvgpr_mov_b32 a241, a93
Mvvv 100,30,220
ds_write_b128 v13, a[52:55] offset:8192
Mvvv 104,30,224
v_accvgpr_mov_b32 a242, a94
v_accvgpr_mov_b32 a243, a95
Mvvv 108,30,228
ds_write_b128 v13, a[56:59] offset:9216
Mvvv 112,30,232
W_ 8
v_accvgpr_mov_b32 a244, a96
v_accvgpr_mov_b32 a245, a97
Mvvv 96,34,236
Dv 30,%0,37888
v_accvgpr_mov_b32 a246, a98
Mvvv 100,34,240
v_accvgpr_mov_b32 a247, a99
R_ v16,a120
Mvvv 104,34,244
R_ v17,a121
R_ v18,a122
Mvvv 108,34,248
R_ v19,a123
R_ v28,a124
Mvvv 112,34,252
W_ 6
R_ v29,a125
R_ v51,a126
Zvvv 76,38,136
Dv 34,v1,37888
R_ v6,a127
Zvvv 80,38,140
R_ v14,a128
R_ v15,a129
Zvvv 84,38,144
v_accvgpr_mov_b32 a249, a130
v_accvgpr_mov_b32 a250, a131
Zvvv 88,38,148
v_max3_f32 v56, v96, v97, v98
v_max_f32 v56, v56, v99
Zvvv 92,38,152
W_ 4
v_max3_f32 v60, v100, v101, v102
v_max_f32 v60, v60, v103
Mvvv 76,42,156
Dv 38,%0,38912
v_max3_f32 v64, v104, v105, v106
Mvvv 80,42,160
v_max_f32 v64, v64, v107
Mvvv 84,42,164
v_max3_f32 v68, v108, v109, v110
v_max_f32 v68, v68, v111
Mvvv 88,42,168
v_max3_f32 v72, v112, v113, v114
v_max_f32 v72, v72, v115
Mvvv 92,42,172
W_ 2
Mvvv 76,30,176
Dv 42,v1,38912
Mvvv 80,30,180
Mvvv 84,30,184
Mvvv 88,30,188
Mvvv 92,30,192
s_waitcnt vmcnt(30) lgkmcnt(0)
s_barrier
Mvvv 76,34,196
Dv 30,%0,55296
Mvvv 76,38,216
Mvvv 76,42,236
Mvvv 80,34,200
Mvvv 80,38,220
Mvvv 80,42,240
Mvvv 84,34,204
s_nop 3
v_max3_f32 v56, v56, v76, v77
v_max3_f32 v56, v56, v78, v79
Mvvv 84,38,224
Mvvv 84,42,244
Mvvv 88,34,208
v_max3_f32 v60, v60, v80, v81
v_max3_f32 v60, v60, v82, v83
Mvvv 88,38,228
Mvvv 88,42,248
Mvvv 92,34,212
s_nop 1
v_max3_f32 v64, v64, v84, v85
v_max3_f32 v64, v64, v86, v87
Mvvv 92,38,232
Mvvv 92,42,252
Dv 34,v1,55296
.p2align 3
v_mov_b32 v11, 0x3f803f80
v_permlane32_swap_b32 v56, v60
v_max3_f32 v68, v68, v88, v89
v_max3_f32 v68, v68, v90, v91
v_max_f32 v56, v56, v60
s_nop 1
v_max3_f32 v72, v72, v92, v93
v_max3_f32 v72, v72, v94, v95
v_mov_b32 v73, v72
v_permlane32_swap_b32 v64, v68
v_max_f32 v64, v64, v68
v_permlane32_swap_b32 v72, v73
v_max_f32 v72, v72, v73
v_mov_b32 v73, v72
v_permlane16_swap_b32 v56, v64
v_max_f32 v56, v56, v64
v_permlane16_swap_b32 v72, v73
B_ v56,0xc0e00000,v56
v_max_f32 v72, v72, v73
v_mov_b32 v64, v56
B_ v72,0xc0e00000,v72
v_mov_b32 v73, v72
v_permlane16_swap_b32 v56, v64
v_mov_b32 v60, v56
v_mov_b32 v68, v64
v_mov_b64 v[74:75], v[72:73]
v_permlane32_swap_b32 v56, v60
v_permlane32_swap_b32 v64, v68
v_mov_b32 v57, v56
v_mov_b32 v61, v60
v_mov_b32 v65, v64
v_mov_b32 v69, v68
v_pk_add_f32 v[96:97], v[96:97], v[56:57]
v_pk_add_f32 v[98:99], v[98:99], v[56:57]
v_pk_add_f32 v[76:77], v[76:77], v[56:57]
v_pk_add_f32 v[78:79], v[78:79], v[56:57]
v_mov_b64 v[58:59], v[56:57]
v_pk_add_f32 v[100:101], v[100:101], v[60:61]
v_pk_add_f32 v[102:103], v[102:103], v[60:61]
v_pk_add_f32 v[80:81], v[80:81], v[60:61]
v_pk_add_f32 v[82:83], v[82:83], v[60:61]
v_mov_b64 v[62:63], v[60:61]
v_pk_add_f32 v[104:105], v[104:105], v[64:65]
v_pk_add_f32 v[106:107], v[106:107], v[64:65]
v_pk_add_f32 v[84:85], v[84:85], v[64:65]
v_pk_add_f32 v[86:87], v[86:87], v[64:65]
v_mov_b64 v[66:67], v[64:65]
v_pk_add_f32 v[108:109], v[108:109], v[68:69]
v_pk_add_f32 v[110:111], v[110:111], v[68:69]
v_pk_add_f32 v[88:89], v[88:89], v[68:69]
v_pk_add_f32 v[90:91], v[90:91], v[68:69]
v_mov_b64 v[70:71], v[68:69]
v_pk_add_f32 v[112:113], v[112:113], v[72:73]
v_pk_add_f32 v[114:115], v[114:115], v[72:73]
v_pk_add_f32 v[92:93], v[92:93], v[72:73]
v_pk_add_f32 v[94:95], v[94:95], v[72:73]
E_ 96
E_ 97
E_ 98
E_ 99
E_ 76
E_ 77
E_ 78
E_ 79
C_ 99,98,99
C_ 98,96,97
C_ 96,76,77
C_ 97,78,79
E_ 100
E_ 101
E_ 102
E_ 103
E_ 80
E_ 81
E_ 82
E_ 83
C_ 103,102,103
C_ 102,100,101
C_ 100,80,81
C_ 101,82,83
.Lstep1_1:
W_ 1
Nvvv 116,30,136,56
s_nop 0
Dv 38,%0,56320
Dv 42,v1,56320
W_ 2
Mvvv 116,34,156
E_ 104
Nvvv 120,30,140,60
E_ 105
Mvvv 120,34,160
E_ 106
Nvvv 124,30,144,64
E_ 107
Mvvv 124,34,164
E_ 84
Nvvv 128,30,148,68
E_ 85
Mvvv 128,34,168
E_ 86
Nvvv 132,30,152,72
E_ 87
Mvvv 132,34,172
W_ 0
C_ 107,106,107
C_ 106,104,105
Mvvv 132,38,192
s_nop 0
Dv 30,%0,57344
Dv 34,v1,57344
Mvvv 132,42,212
C_ 104,84,85
C_ 105,86,87
Mvvv 128,38,188
E_ 108
Mvvv 128,42,208
E_ 109
Mvvv 124,38,184
E_ 110
Mvvv 124,42,204
E_ 111
Mvvv 120,38,180
E_ 88
Mvvv 120,42,200
E_ 89
Mvvv 116,38,176
E_ 90
Mvvv 116,42,196
W_ 0
E_ 91
Mvvv 116,30,216
s_nop 0
Dv 38,%0,49152
Dv 42,v1,49152
Mvvv 116,34,236
C_ 111,110,111
C_ 110,108,109
Mvvv 120,30,220
C_ 108,88,89
C_ 109,90,91
Mvvv 120,34,240
E_ 112
Mvvv 124,30,224
E_ 113
Mvvv 124,34,244
E_ 114
Mvvv 128,30,228
E_ 115
Mvvv 128,34,248
E_ 92
Mvvv 132,30,232
E_ 93
Mvvv 132,34,252
W_ 0
E_ 94
Nvvv 76,38,136,56
s_nop 0
Dv 30,%0,50176
Dv 34,v1,50176
Mvvv 76,42,156
E_ 95
Nvvv 80,38,140,60
C_ 115,114,115
C_ 114,112,113
Mvvv 80,42,160
C_ 112,92,93
C_ 113,94,95
Nvvv 84,38,144,64
E_ 116
Mvvv 84,42,164
E_ 117
Nvvv 88,38,148,68
)A"
R"A(E_ 118
Mvvv 88,42,168
E_ 119
Nvvv 92,38,152,72
C_ 119,118,119
C_ 118,116,117
Mvvv 92,42,172
W_ 0
E_ 120
Mvvv 92,30,192
s_nop 0
Dv 38,%0,51200
Dv 42,v1,51200
Mvvv 92,34,212
X_ a180,v11
X_ a181,v11
Mvvv 88,30,188
X_ a182,v11
X_ a183,v11
Mvvv 88,34,208
ds_write_b128 v12, a[132:135] offset:0
Mvvv 84,30,184
E_ 121
Mvvv 84,34,204
E_ 122
Mvvv 80,30,180
ds_write_b128 v12, a[136:139] offset:1024
Mvvv 80,34,200
E_ 123
Mvvv 76,30,176
C_ 123,122,123
C_ 122,120,121
Mvvv 76,34,196
ds_write_b128 v12, a[160:163] offset:2048
W_ 4
Mvvv 76,38,216
W_ 3
E_ 124
Mvvv 76,42,236
s_waitcnt vmcnt(12) lgkmcnt(0)
s_barrier
Mvvv 80,38,220
ds_write_b128 v12, a[164:167] offset:3072
Mvvv 80,42,240
E_ 125
Mvvv 84,38,224
E_ 126
Mvvv 84,42,244
s_nop 0
Tv 30,v2,24576
Tv 32,v2,30720
Mvvv 88,38,228
ds_write_b128 v12, a[168:171] offset:4096
Mvvv 88,42,248
s_nop 0
Tv 34,v3,24576
Tv 36,v3,30720
Mvvv 92,38,232
E_ 127
Mvvv 92,42,252
ds_write_b128 v12, a[172:175] offset:5120
Zaav 220,180,96
.Lpad_a1_1:
s_cbranch_execz .Lhop_a1_r
C_ 127,126,127
C_ 126,124,125
Zaav 224,180,100
E_ 128
Zaav 228,180,104
ds_write_b128 v12, a[176:179] offset:12288
Zaav 232,180,108
E_ 129
Zaav 236,180,112
W_ 2
E_ 130
Zavv 0,30,96
s_nop 0
Tv 38,v4,24576
Tv 40,v4,30720
Zavv 20,34,96
ds_write_b128 v12, a[200:203] offset:13312
Zavv 24,34,100
s_nop 0
Tv 42,v5,24576
Tv 44,v5,30720
Zavv 4,30,100
E_ 131
Zavv 8,30,104
ds_write_b128 v12, a[204:207] offset:14336
Zavv 28,34,104
R_ v46,a220
R_ v47,a224
Zavv 32,34,108
R_ v48,a228
R_ v49,a232
Zavv 12,30,108
R_ v50,a236
C_ 131,130,131
Zavv 16,30,112
ds_write_b128 v12, a[208:211] offset:15360
Zavv 36,34,112
W_ 2
C_ 130,128,129
Zavv 56,38,112
s_nop 0
Tv 30,v2,25600
Tv 32,v2,31744
Zavv 76,42,112
ds_write_b128 v12, a[212:215] offset:16384
Zavv 72,42,108
s_nop 0
Tv 34,v3,25600
Tv 36,v3,31744
Zavv 52,38,108
E_ 132
Zavv 48,38,104
ds_write_b128 v12, a[216:219] offset:17408
Zavv 68,42,104
E_ 133
Zavv 64,42,100
E_ 134
Zavv 44,38,100
E_ 135
Zavv 40,38,96
C_ 135,134,135
C_ 134,132,133
Zavv 60,42,96
W_ 1
E_ 76
Zavv 80,30,96
s_nop 0
Tv 38,v4,25600
Tv 40,v4,31744
Zavv 100,34,96
E_ 77
Zavv 104,34,100
s_nop 0
Tv 42,v5,25600
Tv 44,v5,31744
Zavv 84,30,100
E_ 78
Zavv 88,30,104
E_ 79
Zavv 108,34,104
C_ 116,76,77
C_ 117,78,79
Zavv 112,34,108
E_ 80
Zavv 92,30,108
E_ 81
Zavv 96,30,112
E_ 82
Zavv 116,34,112
W_ 0
E_ 83
Zavv 136,38,112
s_nop 0
Tv 30,v2,26624
Tv 32,v2,32768
Zavv 156,42,112
C_ 120,80,81
C_ 121,82,83
Zavv 152,42,108
s_nop 0
Tv 34,v3,26624
Tv 36,v3,32768
Zavv 132,38,108
E_ 84
Zavv 128,38,104
E_ 85
Zavv 148,42,104
E_ 86
Zavv 144,42,100
E_ 87
Zavv 124,38,100
C_ 124,84,85
C_ 125,86,87
Zavv 120,38,96
E_ 88
Zavv 140,42,96
W_ 0
E_ 89
Zavv 160,30,96
s_nop 0
Tv 38,v4,26624
Tv 40,v4,32768
Zavv 180,34,96
E_ 90
Zavv 184,34,100
s_nop 0
Tv 42,v5,26624
Tv 44,v5,32768
Zavv 164,30,100
E_ 91
Zavv 168,30,104
C_ 128,88,89
C_ 129,90,91
Zavv 188,34,104
S_ s65,s61,36864
E_ 92
Zavv 192,34,108
S_ m0,s51,24576
E_ 93
Zavv 172,30,108
s_waitcnt vmcnt(6) lgkmcnt(0)
s_barrier
Zavv 176,30,112
Ln v7,32,s65,0
Zavv 196,34,112
E_ 94
Zavv 216,38,112
Dv 30,%0,6144
Zavv 236,42,112
Dv 34,v1,6144
Zavv 232,42,108
Ln v8,32,s65,3072
S_ m0,s51,25472
Zavv 212,38,108
E_ 95
Zavv 208,38,104
Ln v7,32,s65,128
Zavv 228,42,104
C_ 132,92,93
C_ 133,94,95
Zavv 224,42,100
Ln v8,32,s65,3200
S_ m0,s51,26368
Zavv 204,38,100
v_mov_b32 v92, 0x3f803f80
v_mov_b32 v93, 0x3f803f80
Zavv 200,38,96
Ln v7,32,s65,256
Zavv 220,42,96
v_mov_b32 v94, 0x3f803f80
v_mov_b32 v95, 0x3f803f80
Ln v8,32,s65,3328
.Lstep1_2:
s_nop 0
Zvvv 76,92,116
Dv 38,%0,7168
Zvvv 80,92,120
Dv 42,v1,7168
Zvvv 84,92,124
Zvvv 88,92,128
Zvvv 92,92,132
W_ 2
Nvvv 96,30,136,56
Mvvv 96,34,156
Nvvv 100,30,140,60
s_nop 1
v_max3_f32 v10, v76, v80, v84
s_nop 0
v_max3_f32 v10, v10, v88, v92
Mvvv 100,34,160
v_cmp_lt_f32 vcc, 0x42000000, v10
s_cbranch_vccnz .Ltramp1_1
Nvvv 104,30,144,64
A_ v46,v46,v76
A_ v47,v47,v80
Mvvv 104,34,164
A_ v48,v48,v84
A_ v49,v49,v88
Nvvv 108,30,148,68
A_ v50,v50,v92
Mvvv 108,34,168
Nvvv 112,30,152,72
Mvvv 112,34,172
W_ 0
Mvvv 112,38,192
Dv 30,%0,8192
Mvvv 112,42,212
Dv 34,v1,8192
Mvvv 108,38,188
Mvvv 108,42,208
Mvvv 104,38,184
Mvvv 104,42,204
Mvvv 100,38,180
Mvvv 100,42,200
Mvvv 96,38,176
Mvvv 96,42,196
W_ 0
Mvvv 96,30,216
Dv 38,%0,0
Mvvv 96,34,236
Dv 42,v1,0
Mvvv 100,30,220
Mvvv 100,34,240
Mvvv 104,30,224
Mvvv 104,34,244
Mvvv 108,30,228
Mvvv 108,34,248
s_nop 0
E_ 96
Mvvv 112,30,232
E_ 97
Mvvv 112,34,252
W_ 0
E_ 98
Nvvv 76,38,136,56
Dv 30,%0,1024
Mvvv 76,42,156
Dv 34,v1,1024
Nvvv 80,38,140,60
E_ 99
Mvvv 80,42,160
C_ 99,98,99
C_ 98,96,97
Nvvv 84,38,144,64
E_ 100
Mvvv 84,42,164
E_ 101
Nvvv 88,38,148,68
E_ 102
Mvvv 88,42,168
E_ 103
Nvvv 92,38,152,72
C_ 103,102,103
C_ 102,100,101
Mvvv 92,42,172
W_ 0
E_ 104
Mvvv 92,30,192
Dv 38,%0,2048
Mvvv 92,34,212
Dv 42,v1,2048
Mvvv 88,30,188
E_ 105
Mvvv 88,34,208
E_ 106
Mvvv 84,30,184
S_ s65,s61,24576
E_ 107
Mvvv 84,34,204
S_ m0,s51,0
C_ 107,106,107
C_ 106,104,105
Mvvv 80,30,180
s_waitcnt vmcnt(6) lgkmcnt(0)
s_barrier
Mvvv 80,34,200
Ln v9,36,s65,0
Mvvv 76,30,176
E_ 108
Mvvv 76,34,196
Ln v9,36,s65,3072
S_ m0,s51,896
Mvvv 76,38,216
Tv 30,v2,12288
Tv 32,v2,18432
Mvvv 76,42,236
Ln v9,36,s65,128
Mvvv 80,38,220
s_nop 0
Tv 34,v3,12288
Tv 36,v3,18432
Mvvv 80,42,240
Ln v9,36,s65,3200
S_ m0,s51,1792
Mvvv 84,38,224
E_ 109
Mvvv 84,42,244
Ln v9,36,s65,256
Mvvv 88,38,228
E_ 110
Mvvv 88,42,248
Ln v9,36,s65,3328
Mvvv 92,38,232
E_ 111
Mvvv 92,42,252
W_ 0
C_ 111,110,111
C_ 110,108,109
Mavv 0,30,116
s_nop 0
Tv 38,v4,12288
Tv 40,v4,18432
Mavv 20,34,116
E_ 112
Mavv 24,34,120
s_nop 0
Tv 42,v5,12288
Tv 44,v5,18432
Mavv 4,30,120
E_ 113
Mavv 8,30,124
E_ 114
Mavv 28,34,124
E_ 115
Mavv 32,34,128
C_ 115,114,115
C_ 114,112,113
Mavv 12,30,128
E_ 76
Mavv 16,30,132
E_ 77
Mavv 36,34,132
W_ 0
E_ 78
Mavv 56,38,132
s_nop 0
Tv 30,v2,13312
Tv 32,v2,19456
Mavv 76,42,132
E_ 79
Mavv 72,42,128
s_nop 0
Tv 34,v3,13312
Tv 36,v3,19456
Mavv 52,38,128
C_ 96,76,77
C_ 97,78,79
Mavv 48,38,124
E_ 80
Mavv 68,42,124
E_ 81
Mavv 64,42,120
E_ 82
Mavv 44,38,120
E_ 83
Mavv 40,38,116
C_ 100,80,81
C_ 101,82,83
Mavv 60,42,116
W_ 0
E_ 84
Mavv 80,30,116
s_nop 0
Tv 38,v4,13312
Tv 40,v4,19456
Mavv 100,34,116
E_ 85
Mavv 104,34,120
s_nop 0
Tv 42,v5,13312
Tv 44,v5,19456
Mavv 84,30,120
E_ 86
Mavv 88,30,124
E_ 87
Mavv 108,34,124
C_ 104,84,85
C_ 105,86,87
Mavv 112,34,128
E_ 88
Mavv 92,30,128
E_ 89
Mavv 96,30,132
E_ 90
Mavv 116,34,132
W_ 0
E_ 91
Mavv 136,38,132
s_nop 0
Tv 30,v2,14336
Tv 32,v2,20480
Mavv 156,42,132
C_ 108,88,89
C_ 109,90,91
Mavv 152,42,128
s_nop 0
Tv 34,v3,14336
Tv 36,v3,20480
Mavv 132,38,128
E_ 92
Mavv 128,38,124
E_ 93
Mavv 148,42,124
E_ 94
Mavv 144,42,120
E_ 95
Mavv 124,38,120
C_ 112,92,93
C_ 113,94,95
Mavv 120,38,116
v_mov_b32 v92, 0x3f803f80
v_mov_b32 v93, 0x3f803f80
Mavv 140,42,116
W_ 0
v_mov_b32 v94, 0x3f803f80
v_mov_b32 v95, 0x3f803f80
Mavv 160,30,116
s_nop 0
Tv 38,v4,14336
Tv 40,v4,20480
Mavv 180,34,116
Mavv 184,34,120
s_nop 0
Tv 42,v5,14336
Tv 44,v5,20480
Mavv 164,30,120
Mavv 168,30,124
Mavv 188,34,124
S_ s65,s61,49152
Mavv 192,34,128
S_ m0,s51,12288
Mavv 172,30,128
s_waitcnt vmcnt(6) lgkmcnt(0)
s_barrier
Mavv 176,30,132
Ln v7,32,s65,0
Mavv 196,34,132
Mavv 216,38,132
Dv 30,%0,30720
Mavv 236,42,132
Dv 34,v1,30720
Mavv 232,42,128
Ln v8,32,s65,3072
S_ m0,s51,13184
Mavv 212,38,128
Mavv 208,38,124
Ln v7,32,s65,128
Mavv 228,42,124
Mavv 224,42,120
Ln v8,32,s65,3200
S_ m0,s51,14080
Mavv 204,38,120
Mavv 200,38,116
Ln v7,32,s65,256
Mavv 220,42,116
s_cmp_eq_u32 s4, 0
s_cbranch_scc1 .Lhs_a1
.Lhs_a1_back:
.Lstep1_3:
Zvvv 76,92,96
Dv 38,%0,31744
Zvvv 80,92,100
Dv 42,v1,31744
Zvvv 84,92,104
Zvvv 88,92,108
Zvvv 92,92,112
W_ 2
Nvvv 116,30,136,56
Mvvv 116,34,156
Nvvv 120,30,140,60
s_nop 1
v_max3_f32 v10, v76, v80, v84
s_nop 0
v_max3_f32 v10, v10, v88, v92
Mvvv 120,34,160
v_cmp_lt_f32 vcc, 0x42000000, v10
s_cbranch_vccnz .Ltramp1_2
Nvvv 124,30,144,64
A_ v46,v46,v76
A_ v47,v47,v80
Mvvv 124,34,164
A_ v48,v48,v84
A_ v49,v49,v88
Nvvv 128,30,148,68
A_ v50,v50,v92
Mvvv 128,34,168
Ln v8,32,s65,3328
Nvvv 132,30,152,72
Mvvv 132,34,172
W_ 0
Mvvv 132,38,192
Dv 30,%0,32768
Mvvv 132,42,212
Dv 34,v1,32768
Mvvv 128,38,188
Mvvv 128,42,208
Mvvv 124,38,184
Mvvv 124,42,204
Mvvv 120,38,180
Mvvv 120,42,200
Mvvv 116,38,176
Mvvv 116,42,196
W_ 0
Mvvv 116,30,216
Dv 38,%0,24576
Mvvv 116,34,236
Dv 42,v1,24576
Mvvv 120,30,220
Mvvv 120,34,240
Mvvv 124,30,224
Mvvv 124,34,244
Mvvv 128,30,228
Mvvv 128,34,248
s_nop 0
E_ 116
Mvvv 132,30,232
E_ 117
Mvvv 132,34,252
W_ 0
E_ 118
Nvvv 76,38,136,56
Dv 30,%0,25600
Mvvv 76,42,156
Dv 34,v1,25600
Nvvv 80,38,140,60
E_ 119
Mvvv 80,42,160
C_ 119,118,119
C_ 118,116,117
Nvvv 84,38,144,64
E_ 120
Mvvv 84,42,164
E_ 121
Nvvv 88,38,148,68
E_ 122
Mvvv 88,42,168
E_ 123
Nvvv 92,38,152,72
C_ 123,122,123
C_ 122,120,121
Mvvv 92,42,172
W_ 0
E_ 124
Mvvv 92,30,192
Dv 38,%0,26624
Mvvv 92,34,212
Dv 42,v1,26624
Mvvv 88,30,188
E_ 125
Mvvv 88,34,208
E_ 126
Mvvv 84,30,184
S_ s65,s61,36864
E_ 127
Mvvv 84,34,204
S_ m0,s51,24576
C_ 127,126,127
C_ 126,124,125
Mvvv 80,30,180
s_waitcnt vmcnt(6) lgkmcnt(0)
s_barrier
Mvvv 80,34,200
Ln v9,36,s65,0
Mvvv 76,30,176
E_ 128
Mvvv 76,34,196
Ln v9,36,s65,3072
S_ m0,s51,25472
Mvvv 76,38,216
Tv 30,v2,0
Tv 32,v2,6144
Mvvv 76,42,236
Ln v9,36,s65,128
Mvvv 80,38,220
s_nop 0
Tv 34,v3,0
Tv 36,v3,6144
Mvvv 80,42,240
Ln v9,36,s65,3200
S_ m0,s51,26368
Mvvv 84,38,224
E_ 129
Mvvv 84,42,244
Ln v9,36,s65,256
Mvvv 88,38,228
E_ 130
Mvvv 88,42,248
Ln v9,36,s65,3328
Mvvv 92,38,232
E_ 131
Mvvv 92,42,252
W_ 0
C_ 131,130,131
C_ 130,128,129
Mavv 0,30,96
.Lpad_a1_2:
s_cbranch_execz .Lhop_a1_r
Tv 38,v4,0
Tv 40,v4,6144
Mavv 20,34,96
E_ 132
Mavv 24,34,100
s_nop 0
Tv 42,v5,0
Tv 44,v5,6144
Mavv 4,30,100
E_ 133
Mavv 8,30,104
E_ 134
Mavv 28,34,104
E_ 135
Mavv 32,34,108
C_ 135,134,135
C_ 134,132,133
Mavv 12,30,108
E_ 76
Mavv 16,30,112
E_ 77
Mavv 36,34,112
W_ 0
E_ 78
Mavv 56,38,112
s_nop 0
Tv 30,v2,1024
Tv 32,v2,7168
Mavv 76,42,112
E_ 79
Mavv 72,42,108
s_nop 0
Tv 34,v3,1024
Tv 36,v3,7168
Mavv 52,38,108
C_ 116,76,77
C_ 117,78,79
Mavv 48,38,104
E_ 80
Mavv 68,42,104
E_ 81
Mavv 64,42,100
E_ 82
Mavv 44,38,100
E_ 83
Mavv 40,38,96
C_ 120,80,81
C_ 121,82,83
Mavv 60,42,96
W_ 0
E_ 84
Mavv 80,30,96
s_nop 0
Tv 38,v4,1024
Tv 40,v4,7168
Mavv 100,34,96
E_ 85
Mavv 104,34,100
s_nop 0
Tv 42,v5,1024
Tv 44,v5,7168
Mavv 84,30,100
E_ 86
Mavv 88,30,104
E_ 87
Mavv 108,34,104
C_ 124,84,85
C_ 125,86,87
Mavv 112,34,108
E_ 88
Mavv 92,30,108
E_ 89
Mavv 96,30,112
E_ 90
Mavv 116,34,112
W_ 0
E_ 91
Mavv 136,38,112
s_nop 0
Tv 30,v2,2048
Tv 32,v2,8192
Mavv 156,42,112
C_ 128,88,89
C_ 129,90,91
Mavv 152,42,108
s_nop 0
Tv 34,v3,2048
Tv 36,v3,8192
Mavv 132,38,108
E_ 92
Mavv 128,38,104
E_ 93
Mavv 148,42,104
E_ 94
Mavv 144,42,100
E_ 95
Mavv 124,38,100
C_ 132,92,93
C_ 133,94,95
Mavv 120,38,96
v_mov_b32 v92, 0x3f803f80
v_mov_b32 v93, 0x3f803f80
Mavv 140,42,96
W_ 0
v_mov_b32 v94, 0x3f803f80
v_mov_b32 v95, 0x3f803f80
Mavv 160,30,96
s_nop 0
Tv 38,v4,2048
Tv 40,v4,8192
Mavv 180,34,96
Mavv 184,34,100
s_nop 0
Tv 42,v5,2048
Tv 44,v5,8192
Mavv 164,30,100
Mavv 168,30,104
Mavv 188,34,104
S_ s65,s61,61440
Mavv 192,34,108
S_ m0,s51,0
Mavv 172,30,108
s_waitcnt vmcnt(6) lgkmcnt(0)
s_barrier
Mavv 176,30,112
Ln v7,32,s65,0
Mavv 196,34,112
Mavv 216,38,112
Dv 30,%0,18432
Mavv 236,42,112
Dv 34,v1,18432
Mavv 232,42,108
Ln v8,32,s65,3072
S_ m0,s51,896
Mavv 212,38,108
Mavv 208,38,104
Ln v7,32,s65,128
Mavv 228,42,104
Mavv 224,42,100
Ln v8,32,s65,3200
S_ m0,s51,1792
Mavv 204,38,100
Mavv 200,38,96
Ln v7,32,s65,256
Mavv 220,42,96
.Lstep1_4:
Zvvv 76,92,116
Dv 38,%0,19456
Zvvv 80,92,120
Dv 42,v1,19456
Zvvv 84,92,124
Zvvv 88,92,128
Zvvv 92,92,132
W_ 2
Nvvv 96,30,136,56
Mvvv 96,34,156
Nvvv 100,30,140,60
s_nop 1
v_max3_f32 v10, v76, v80, v84
s_nop 0
v_max3_f32 v10, v10, v88, v92
Mvvv 100,34,160
v_cmp_lt_f32 vcc, 0x42000000, v10
s_cbranch_vccnz .Ltramp1_3
Nvvv 104,30,144,64
A_ v46,v46,v76
A_ v47,v47,v80
Mvvv 104,34,164
A_ v48,v48,v84
A_ v49,v49,v88
Nvvv 108,30,148,68
A_ v50,v50,v92
Mvvv 108,34,168
Ln v8,32,s65,3328
Nvvv 112,30,152,72
Mvvv 112,34,172
W_ 0
Mvvv 112,38,192
Dv 30,%0,20480
Mvvv 112,42,212
Dv 34,v1,20480
Mvvv 108,38,188
Mvvv 108,42,208
Mvvv 104,38,184
Mvvv 104,42,204
Mvvv 100,38,180
Mvvv 100,42,200
Mvvv 96,38,176
Mvvv 96,42,196
W_ 0
Mvvv 96,30,216
Dv 38,%0,12288
Mvvv 96,34,236
Dv 42,v1,12288
Mvvv 100,30,220
Mvvv 100,34,240
Mvvv 104,30,224
Mvvv 104,34,244
Mvvv 108,30,228
Mvvv 108,34,248
s_nop 0
E_ 96
Mvvv 112,30,232
E_ 97
Mvvv 112,34,252
W_ 0
E_ 98
Nvvv 76,38,136,56
Dv 30,%0,13312
Mvvv 76,42,156
Dv 34,v1,13312
Nvvv 80,38,140,60
E_ 99
Mvvv 80,42,160
C_ 99,98,99
C_ 98,96,97
Nvvv 84,38,144,64
E_ 100
Mvvv 84,42,164
E_ 101
Nvvv 88,38,148,68
E_ 102
Mvvv 88,42,168
E_ 103
Nvvv 92,38,152,72
C_ 103,102,103
C_ 102,100,101
Mvvv 92,42,172
W_ 0
E_ 104
Mvvv 92,30,192
Dv 38,%0,14336
Mvvv 92,34,212
Dv 42,v1,14336
Mvvv 88,30,188
E_ 105
Mvvv 88,34,208
E_ 106
Mvvv 84,30,184
S_ s65,s61,49152
E_ 107
Mvvv 84,34,204
S_ m0,s51,12288
C_ 107,106,107
C_ 106,104,105
Mvvv 80,30,180
s_waitcnt vmcnt(6) lgkmcnt(0)
s_barrier
Mvvv 80,34,200
L_ v9,36,s65,0
Mvvv 76,30,176
E_ 108
Mvvv 76,34,196
L_ v9,36,s65,3072
S_ m0,s51,13184
Mvvv 76,38,216
Tv 30,v2,24576
Tv 32,v2,30720
Mvvv 76,42,236
L_ v9,36,s65,128
Mvvv 80,38,220
s_nop 0
Tv 34,v3,24576
Tv 36,v3,30720
Mvvv 80,42,240
L_ v9,36,s65,3200
S_ m0,s51,14080
Mvvv 84,38,224
E_ 109
Mvvv 84,42,244
L_ v9,36,s65,256
Mvvv 88,38,228
E_ 110
Mvvv 88,42,248
L_ v9,36,s65,3328
Mvvv 92,38,232
E_ 111
Mvvv 92,42,252
W_ 0
C_ 111,110,111
C_ 110,108,109
Mavv 0,30,116
s_nop 0
Tv 38,v4,24576
Tv 40,v4,30720
Mavv 20,34,116
E_ 112
Mavv 24,34,120
s_nop 0
Tv 42,v5,24576
Tv 44,v5,30720
Mavv 4,30,120
E_ 113
Mavv 8,30,124
E_ 114
Mavv 28,34,124
E_ 115
Mavv 32,34,128
C_ 115,114,115
C_ 114,112,113
Mavv 12,30,128
E_ 76
Mavv 16,30,132
E_ 77
Mavv 36,34,132
W_ 0
E_ 78
Mavv 56,38,132
s_nop 0
Tv 30,v2,25600
Tv 32,v2,31744
Mavv 76,42,132
E_ 79
Mavv 72,42,128
s_nop 0
Tv 34,v3,25600
Tv 36,v3,31744
Mavv 52,38,128
C_ 96,76,77
C_ 97,78,79
Mavv 48,38,124
E_ 80
Mavv 68,42,124
E_ 81
Mavv 64,42,120
E_ 82
Mavv 44,38,120
E_ 83
Mavv 40,38,116
C_ 100,80,81
C_ 101,82,83
Mavv 60,42,116
W_ 0
E_ 84
Mavv 80,30,116
s_nop 0
Tv 38,v4,25600
Tv 40,v4,31744
Mavv 100,34,116
E_ 85
Mavv 104,34,120
s_nop 0
Tv 42,v5,25600
Tv 44,v5,31744
Mavv 84,30,120
E_ 86
Mavv 88,30,124
E_ 87
Mavv 108,34,124
C_ 104,84,85
C_ 105,86,87
Mavv 112,34,128
E_ 88
Mavv 92,30,128
E_ 89
Mavv 96,30,132
E_ 90
Mavv 116,34,132
W_ 0
E_ 91
Mavv 136,38,132
s_nop 0
Tv 30,v2,26624
Tv 32,v2,32768
Mavv 156,42,132
C_ 108,88,89
C_ 109,90,91
Mavv 152,42,128
s_nop 0
Tv 34,v3,26624
Tv 36,v3,32768
Mavv 132,38,128
E_ 92
Mavv 128,38,124
E_ 93
Mavv 148,42,124
E_ 94
Mavv 144,42,120
E_ 95
Mavv 124,38,120
C_ 112,92,93
C_ 113,94,95
Mavv 120,38,116
v_mov_b32 v92, 0x3f803f80
v_mov_b32 v93, 0x3f803f80
Mavv 140,42,116
W_ 0
v_mov_b32 v94, 0x3f803f80
v_mov_b32 v95, 0x3f803f80
Mavv 160,30,116
s_nop 0
Tv 38,v4,26624
Tv 40,v4,32768
Mavv 180,34,116
Mavv 184,34,120
s_nop 0
Tv 42,v5,26624
Tv 44,v5,32768
Mavv 164,30,120
Mavv 168,30,124
Mavv 188,34,124
S_ s65,s61,73728
Mavv 192,34,128
S_ m0,s51,24576
Mavv 172,30,128
s_waitcnt vmcnt(6) lgkmcnt(0)
s_barrier
Mavv 176,30,132
Ln v7,32,s65,0
Mavv 196,34,132
Mavv 216,38,132
Dv 30,%0,6144
Mavv 236,42,132
Dv 34,v1,6144
Mavv 232,42,128
Ln v8,32,s65,3072
S_ m0,s51,25472
Mavv 212,38,128
Mavv 208,38,124
Ln v7,32,s65,128
Mavv 228,42,124
Mavv 224,42,120
Ln v8,32,s65,3200
S_ m0,s51,26368
Mavv 204,38,120
Mavv 200,38,116
Ln v7,32,s65,256
Mavv 220,42,116
s_cmp_eq_u32 s4, 0
s_cbranch_scc1 .Lhs_b1
.Lhs_b1_back:
.Lstep1_5:
Zvvv 76,92,96
Dv 38,%0,7168
Zvvv 80,92,100
Dv 42,v1,7168
Zvvv 84,92,104
Zvvv 88,92,108
Zvvv 92,92,112
W_ 2
Nvvv 116,30,136,56
Mvvv 116,34,156
Nvvv 120,30,140,60
s_nop 1
v_max3_f32 v10, v76, v80, v84
s_nop 0
v_max3_f32 v10, v10, v88, v92
Mvvv 120,34,160
v_cmp_lt_f32 vcc, 0x42000000, v10
s_cbranch_vccnz .Ltramp1_4
Nvvv 124,30,144,64
A_ v46,v46,v76
A_ v47,v47,v80
Mvvv 124,34,164
A_ v48,v48,v84
A_ v49,v49,v88
Nvvv 128,30,148,68
A_ v50,v50,v92
Mvvv 128,34,168
Ln v8,32,s65,3328
Nvvv 132,30,152,72
Mvvv 132,34,172
W_ 0
Mvvv 132,38,192
Dv 30,%0,8192
Mvvv 132,42,212
Dv 34,v1,8192
Mvvv 128,38,188
Mvvv 128,42,208
Mvvv 124,38,184
Mvvv 124,42,204
Mvvv 120,38,180
Mvvv 120,42,200
Mvvv 116,38,176
Mvvv 116,42,196
W_ 0
Mvvv 116,30,216
Dv 38,%0,0
Mvvv 116,34,236
Dv 42,v1,0
Mvvv 120,30,220
Mvvv 120,34,240
Mvvv 124,30,224
Mvvv 124,34,244
Mvvv 128,30,228
Mvvv 128,34,248
s_nop 0
E_ 116
Mvvv 132,30,232
E_ 117
Mvvv 132,34,252
W_ 0
E_ 118
Nvvv 76,38,136,56
Dv 30,%0,1024
Mvvv 76,42,156
Dv 34,v1,1024
Nvvv 80,38,140,60
E_ 119
Mvvv 80,42,160
C_ 119,118,119
C_ 118,116,117
Nvvv 84,38,144,64
E_ 120
Mvvv 84,42,164
E_ 121
Nvvv 88,38,148,68
E_ 122
Mvvv 88,42,168
E_ 123
Nvvv 92,38,152,72
C_ 123,122,123
C_ 122,120,121
Mvvv 92,42,172
W_ 0
E_ 124
Mvvv 92,30,192
Dv 38,%0,2048
Mvvv 92,34,212
Dv 42,v1,2048
Mvvv 88,30,188
E_ 125
Mvvv 88,34,208
E_ 126
Mvvv 84,30,184
S_ s65,s61,61440
E_ 127
Mvvv 84,34,204
S_ m0,s51,0
C_ 127,126,127
C_ 126,124,125
Mvvv 80,30,180
s_waitcnt vmcnt(6) lgkmcnt(0)
s_barrier
Mvvv 80,34,200
L_ v9,36,s65,0
Mvvv 76,30,176
E_ 128
Mvvv 76,34,196
L_ v9,36,s65,3072
S_ m0,s51,896
Mvvv 76,38,216
Tv 30,v2,12288
Tv 32,v2,18432
Mvvv 76,42,236
L_ v9,36,s65,128
Mvvv 80,38,220
s_nop 0
Tv 34,v3,12288
Tv 36,v3,18432
Mvvv 80,42,240
L_ v9,36,s65,3200
S_ m0,s51,1792
Mvvv 84,38,224
E_ 129
Mvvv 84,42,244
L_ v9,36,s65,256
Mvvv 88,38,228
E_ 130
Mvvv 88,42,248
L_ v9,36,s65,3328
Mvvv 92,38,232
E_ 131
Mvvv 92,42,252
W_ 0
C_ 131,130,131
C_ 130,128,129
Mavv 0,30,96
s_nop 0
Tv 38,v4,12288
Tv 40,v4,18432
Mavv 20,34,96
E_ 132
Mavv 24,34,100
s_nop 0
Tv 42,v5,12288
Tv 44,v5,18432
Mavv 4,30,100
E_ 133
Mavv 8,30,104
E_ 134
Mavv 28,34,104
E_ 135
Mavv 32,34,108
C_ 135,134,135
C_ 134,132,133
Mavv 12,30,108
E_ 76
Mavv 16,30,112
E_ 77
Mavv 36,34,112
W_ 0
E_ 78
Mavv 56,38,112
.Lpad_a1_3:
s_cbranch_execz .Lhop_a1_r
Tv 30,v2,13312
Tv 32,v2,19456
Mavv 76,42,112
E_ 79
Mavv 72,42,108
s_nop 0
Tv 34,v3,13312
Tv 36,v3,19456
Mavv 52,38,108
C_ 116,76,77
C_ 117,78,79
Mavv 48,38,104
E_ 80
Mavv 68,42,104
E_ 81
Mavv 64,42,100
E_ 82
Mavv 44,38,100
E_ 83
Mavv 40,38,96
C_ 120,80,81
C_ 121,82,83
Mavv 60,42,96
W_ 0
E_ 84
Mavv 80,30,96
s_nop 0
Tv 38,v4,13312
Tv 40,v4,19456
Mavv 100,34,96
E_ 85
Mavv 104,34,100
s_nop 0
Tv 42,v5,13312
Tv 44,v5,19456
Mavv 84,30,100
E_ 86
Mavv 88,30,104
E_ 87
Mavv 108,34,104
C_ 124,84,85
C_ 125,86,87
Mavv 112,34,108
E_ 88
Mavv 92,30,108
E_ 89
Mavv 96,30,112
E_ 90
Mavv 116,34,112
W_ 0
E_ 91
Mavv 136,38,112
s_nop 0
Tv 30,v2,14336
Tv 32,v2,20480
Mavv 156,42,112
C_ 128,88,89
C_ 129,90,91
Mavv 152,42,108
s_nop 0
Tv 34,v3,14336
Tv 36,v3,20480
Mavv 132,38,108
E_ 92
Mavv 128,38,104
E_ 93
Mavv 148,42,104
E_ 94
Mavv 144,42,100
E_ 95
Mavv 124,38,100
C_ 132,92,93
C_ 133,94,95
Mavv 120,38,96
v_mov_b32 v92, 0x3f803f80
v_mov_b32 v93, 0x3f803f80
Mavv 140,42,96
W_ 0
v_mov_b32 v94, 0x3f803f80
v_mov_b32 v95, 0x3f803f80
Mavv 160,30,96
s_nop 0
Tv 38,v4,14336
Tv 40,v4,20480
Mavv 180,34,96
Mavv 184,34,100
s_nop 0
Tv 42,v5,14336
Tv 44,v5,20480
Mavv 164,30,100
Mavv 168,30,104
Mavv 188,34,104
S_ s65,s61,86016
Mavv 192,34,108
S_ m0,s51,12288
Mavv 172,30,108
s_waitcnt vmcnt(6) lgkmcnt(0)
s_barrier
Mavv 176,30,112
Ln v7,32,s65,0
Mavv 196,34,112
Mavv 216,38,112
Dv 30,%0,30720
Mavv 236,42,112
Dv 34,v1,30720
Mavv 232,42,108
Ln v8,32,s65,3072
S_ m0,s51,13184
Mavv 212,38,108
Mavv 208,38,104
Ln v7,32,s65,128
Mavv 228,42,104
Mavv 224,42,100
Ln v8,32,s65,3200
S_ m0,s51,14080
Mavv 204,38,100
Mavv 200,38,96
Ln v7,32,s65,256
Mavv 220,42,96
.Lstep1_6:
Zvvv 76,92,116
Dv 38,%0,31744
Zvvv 80,92,120
Dv 42,v1,31744
Zvvv 84,92,124
Zvvv 88,92,128
Zvvv 92,92,132
W_ 2
Nvvv 96,30,136,56
Mvvv 96,34,156
Nvvv 100,30,140,60
s_nop 1
v_max3_f32 v10, v76, v80, v84
s_nop 0
v_max3_f32 v10, v10, v88, v92
Mvvv 100,34,160
v_cmp_lt_f32 vcc, 0x42000000, v10
s_cbranch_vccnz .Ltramp1_5
Nvvv 104,30,144,64
A_ v46,v46,v76
A_ v47,v47,v80
Mvvv 104,34,164
A_ v48,v48,v84
A_ v49,v49,v88
Nvvv 108,30,148,68
A_ v50,v50,v92
Mvvv 108,34,168
Ln v8,32,s65,3328
Nvvv 112,30,152,72
R_ v11,a248
Mvvv 112,34,172
W_ 0
v_min_u32 v11, 240, v11
Mvvv 112,38,192
Dv 30,%0,32768
v_lshlrev_b32 v11, 3, v11
Mvvv 112,42,212
Dv 34,v1,32768
Mvvv 108,38,188
buffer_load_dword a254, v11, s[44:47], s66 offen
Mvvv 108,42,208
Mvvv 104,38,184
Mvvv 104,42,204
Mvvv 100,38,180
Mvvv 100,42,200
Mvvv 96,38,176
Mvvv 96,42,196
W_ 0
Mvvv 96,30,216
Dv 38,%0,24576
Mvvv 96,34,236
Dv 42,v1,24576
Mvvv 100,30,220
Mvvv 100,34,240
Mvvv 104,30,224
Mvvv 104,34,244
Mvvv 108,30,228
Mvvv 108,34,248
s_nop 0
E_ 96
Mvvv 112,30,232
E_ 97
Mvvv 112,34,252
W_ 0
E_ 98
Nvvv 76,38,136,56
Dv 30,%0,25600
Mvvv 76,42,156
Dv 34,v1,25600
Nvvv 80,38,140,60
E_ 99
Mvvv 80,42,160
C_ 99,98,99
C_ 98,96,97
Nvvv 84,38,144,64
E_ 100
Mvvv 84,42,164
E_ 101
Nvvv 88,38,148,68
E_ 102
Mvvv 88,42,168
E_ 103
Nvvv 92,38,152,72
C_ 103,102,103
C_ 102,100,101
Mvvv 92,42,172
W_ 0
E_ 104
Mvvv 92,30,192
Dv 38,%0,26624
Mvvv 92,34,212
Dv 42,v1,26624
Mvvv 88,30,188
E_ 105
Mvvv 88,34,208
E_ 106
Mvvv 84,30,184
S_ s65,s61,73728
E_ 107
Mvvv 84,34,204
S_ m0,s51,24576
C_ 107,106,107
C_ 106,104,105
Mvvv 80,30,180
s_waitcnt vmcnt(7) lgkmcnt(0)
s_barrier
Mvvv 80,34,200
L_ v9,36,s65,0
Mvvv 76,30,176
E_ 108
Mvvv 76,34,196
L_ v9,36,s65,3072
S_ m0,s51,25472
Mvvv 76,38,216
Tv 30,v2,0
Tv 32,v2,6144
Mvvv 76,42,236
L_ v9,36,s65,128
Mvvv 80,38,220
s_nop 0
Tv 34,v3,0
Tv 36,v3,6144
Mvvv 80,42,240
L_ v9,36,s65,3200
S_ m0,s51,26368
Mvvv 84,38,224
E_ 109
Mvvv 84,42,244
L_ v9,36,s65,256
Mvvv 88,38,228
E_ 110
Mvvv 88,42,248
L_ v9,36,s65,3328
Mvvv 92,38,232
E_ 111
Mvvv 92,42,252
W_ 0
C_ 111,110,111
C_ 110,108,109
Mavv 0,30,116
s_nop 0
Tv 38,v4,0
Tv 40,v4,6144
Mavv 20,34,116
E_ 112
Mavv 24,34,120
s_nop 0
Tv 42,v5,0
Tv 44,v5,6144
Mavv 4,30,120
E_ 113
Mavv 8,30,124
E_ 114
Mavv 28,34,124
E_ 115
Mavv 32,34,128
C_ 115,114,115
C_ 114,112,113
Mavv 12,30,128
E_ 76
Mavv 16,30,132
E_ 77
Mavv 36,34,132
W_ 0
E_ 78
Mavv 56,38,132
s_nop 0
Tv 30,v2,1024
Tv 32,v2,7168
Mavv 76,42,132
E_ 79
Mavv 72,42,128
s_nop 0
Tv 34,v3,1024
Tv 36,v3,7168
Mavv 52,38,128
C_ 96,76,77
C_ 97,78,79
Mavv 48,38,124
E_ 80
Mavv 68,42,124
E_ 81
Mavv 64,42,120
E_ 82
Mavv 44,38,120
E_ 83
Mavv 40,38,116
C_ 100,80,81
C_ 101,82,83
Mavv 60,42,116
W_ 0
E_ 84
Mavv 80,30,116
s_nop 0
Tv 38,v4,1024
Tv 40,v4,7168
Mavv 100,34,116
E_ 85
Mavv 104,34,120
s_nop 0
Tv 42,v5,1024
Tv 44,v5,7168
Mavv 84,30,120
E_ 86
Mavv 88,30,124
E_ 87
Mavv 108,34,124
C_ 104,84,85
C_ 105,86,87
Mavv 112,34,128
E_ 88
Mavv 92,30,128
E_ 89
Mavv 96,30,132
E_ 90
Mavv 116,34,132
W_ 0
E_ 91
Mavv 136,38,132
s_nop 0
Tv 30,v2,2048
Tv 32,v2,8192
Mavv 156,42,132
C_ 108,88,89
C_ 109,90,91
Mavv 152,42,128
s_nop 0
Tv 34,v3,2048
Tv 36,v3,8192
Mavv 132,38,128
E_ 92
Mavv 128,38,124
E_ 93
Mavv 148,42,124
E_ 94
Mavv 144,42,120
E_ 95
Mavv 124,38,120
C_ 112,92,93
C_ 113,94,95
Mavv 120,38,116
v_mov_b32 v92, 0x3f803f80
v_mov_b32 v93, 0x3f803f80
Mavv 140,42,116
W_ 0
v_mov_b32 v94, 0x3f803f80
v_mov_b32 v95, 0x3f803f80
Mavv 160,30,116
s_nop 0
Tv 38,v4,2048
Tv 40,v4,8192
Mavv 180,34,116
Mavv 184,34,120
s_nop 0
Tv 42,v5,2048
Tv 44,v5,8192
Mavv 164,30,120
Mavv 168,30,124
Mavv 188,34,124
S_ s65,s61,98304
Mavv 192,34,128
S_ m0,s51,0
Mavv 172,30,128
s_waitcnt vmcnt(7) lgkmcnt(0)
s_barrier
Mavv 176,30,132
Ln v7,32,s65,0
Mavv 196,34,132
Mavv 216,38,132
Dv 30,%0,18432
Mavv 236,42,132
Dv 34,v1,18432
Mavv 232,42,128
Ln v8,32,s65,3072
S_ m0,s51,896
Mavv 212,38,128
Mavv 208,38,124
Ln v7,32,s65,128
Mavv 228,42,124
Mavv 224,42,120
Ln v8,32,s65,3200
S_ m0,s51,1792
Mavv 204,38,120
Mavv 200,38,116
Ln v7,32,s65,256
Mavv 220,42,116
s_cmp_eq_u32 s4, 0
s_cbranch_scc1 .Lhs_c1
.Lhs_c1_back:
.Lstep1_7:
Zvvv 76,92,96
Dv 38,%0,19456
Zvvv 80,92,100
Dv 42,v1,19456
Zvvv 84,92,104
Zvvv 88,92,108
Zvvv 92,92,112
W_ 2
Nvvv 116,30,136,56
Mvvv 116,34,156
Nvvv 120,30,140,60
s_nop 1
v_max3_f32 v10, v76, v80, v84
s_nop 0
v_max3_f32 v10, v10, v88, v92
Mvvv 120,34,160
v_cmp_lt_f32 vcc, 0x42000000, v10
s_cbranch_vccnz .Ltramp1_6
Nvvv 124,30,144,64
A_ v46,v46,v76
A_ v47,v47,v80
Mvvv 124,34,164
A_ v48,v48,v84
A_ v49,v49,v88
Nvvv 128,30,148,68
A_ v50,v50,v92
Mvvv 128,34,168
Ln v8,32,s65,3328
Nvvv 132,30,152,72
Mvvv 132,34,172
W_ 0
Mvvv 132,38,192
Dv 30,%0,20480
Mvvv 132,42,212
Dv 34,v1,20480
Mvvv 128,38,188
Mvvv 128,42,208
Mvvv 124,38,184
Mvvv 124,42,204
Mvvv 120,38,180
Mvvv 120,42,200
Mvvv 116,38,176
Mvvv 116,42,196
W_ 0
Mvvv 116,30,216
Dv 38,%0,12288
Mvvv 116,34,236
Dv 42,v1,12288
Mvvv 120,30,220
Mvvv 120,34,240
Mvvv 124,30,224
Mvvv 124,34,244
Mvvv 128,30,228
Mvvv 128,34,248
s_nop 0
E_ 116
Mvvv 132,30,232
E_ 117
Mvvv 132,34,252
W_ 0
E_ 118
Nvvv 76,38,136,56
Dv 30,%0,13312
Mvvv 76,42,156
Dv 34,v1,13312
Nvvv 80,38,140,60
E_ 119
Mvvv 80,42,160
C_ 119,118,119
C_ 118,116,117
Nvvv 84,38,144,64
E_ 120
Mvvv 84,42,164
E_ 121
Nvvv 88,38,148,68
E_ 122
Mvvv 88,42,168
E_ 123
Nvvv 92,38,152,72
C_ 123,122,123
C_ 122,120,121
Mvvv 92,42,172
W_ 0
E_ 124
Mvvv 92,30,192
Dv 38,%0,14336
Mvvv 92,34,212
Dv 42,v1,14336
Mvvv 88,30,188
E_ 125
Mvvv 88,34,208
E_ 126
Mvvv 84,30,184
S_ s65,s61,86016
E_ 127
Mvvv 84,34,204
S_ m0,s51,12288
C_ 127,126,127
C_ 126,124,125
Mvvv 80,30,180
s_waitcnt vmcnt(6) lgkmcnt(0)
s_barrier
Mvvv 80,34,200
L_ v9,36,s65,0
Mvvv 76,30,176
E_ 128
Mvvv 76,34,196
L_ v9,36,s65,3072
S_ m0,s51,13184
Mvvv 76,38,216
Tv 30,v2,24576
Tv 32,v2,30720
Mvvv 76,42,236
L_ v9,36,s65,128
Mvvv 80,38,220
s_nop 0
Tv 34,v3,24576
Tv 36,v3,30720
Mvvv 80,42,240
L_ v9,36,s65,3200
S_ m0,s51,14080
Mvvv 84,38,224
E_ 129
Mvvv 84,42,244
L_ v9,36,s65,256
Mvvv 88,38,228
E_ 130
Mvvv 88,42,248
L_ v9,36,s65,3328
Mvvv 92,38,232
E_ 131
Mvvv 92,42,252
W_ 0
C_ 131,130,131
C_ 130,128,129
Mavv 0,30,96
s_nop 0
Tv 38,v4,24576
Tv 40,v4,30720
Mavv 20,34,96
E_ 132
Mavv 24,34,100
s_nop 0
Tv 42,v5,24576
Tv 44,v5,30720
Mavv 4,30,100
E_ 133
Mavv 8,30,104
E_ 134
Mavv 28,34,104
E_ 135
Mavv 32,34,108
C_ 135,134,135
C_ 134,132,133
Mavv 12,30,108
E_ 76
Mavv 16,30,112
E_ 77
Mavv 36,34,112
W_ 0
E_ 78
Mavv 56,38,112
s_nop 0
Tv 30,v2,25600
Tv 32,v2,31744
Mavv 76,42,112
E_ 79
Mavv 72,42,108
s_nop 0
Tv 34,v3,25600
Tv 36,v3,31744
Mavv 52,38,108
C_ 116,76,77
C_ 117,78,79
Mavv 48,38,104
E_ 80
Mavv 68,42,104
E_ 81
Mavv 64,42,100
E_ 82
Mavv 44,38,100
E_ 83
Mavv 40,38,96
C_ 120,80,81
C_ 121,82,83
Mavv 60,42,96
W_ 0
E_ 84
Mavv 80,30,96
s_nop 0
Tv 38,v4,25600
Tv 40,v4,31744
Mavv 100,34,96
E_ 85
Mavv 104,34,100
s_nop 0
Tv 42,v5,25600
Tv 44,v5,31744
Mavv 84,30,100
E_ 86
Mavv 88,30,104
E_ 87
Mavv 108,34,104
C_ 124,84,85
C_ 125,86,87
Mavv 112,34,108
E_ 88
Mavv 92,30,108
E_ 89
Mavv 96,30,112
E_ 90
Mavv 116,34,112
W_ 0
E_ 91
Mavv 136,38,112
s_nop 0
Tv 30,v2,26624
Tv 32,v2,32768
Mavv 156,42,112
C_ 128,88,89
C_ 129,90,91
Mavv 152,42,108
s_nop 0
Tv 34,v3,26624
Tv 36,v3,32768
Mavv 132,38,108
E_ 92
Mavv 128,38,104
E_ 93
Mavv 148,42,104
E_ 94
Mavv 144,42,100
E_ 95
Mavv 124,38,100
C_ 132,92,93
C_ 133,94,95
Mavv 120,38,96
v_mov_b32 v92, 0x3f803f80
v_mov_b32 v93, 0x3f803f80
Mavv 140,42,96
W_ 0
v_mov_b32 v94, 0x3f803f80
v_mov_b32 v95, 0x3f803f80
Mavv 160,30,96
s_nop 0
Tv 38,v4,26624
Tv 40,v4,32768
Mavv 180,34,96
Mavv 184,34,100
s_nop 0
Tv 42,v5,26624
Tv 44,v5,32768
Mavv 164,30,100
Mavv 168,30,104
Mavv 188,34,104
S_ s65,s61,110592
Mavv 192,34,108
S_ m0,s51,24576
Mavv 172,30,108
s_waitcnt vmcnt(6) lgkmcnt(0)
s_barrier
Mavv 176,30,112
Ln v7,32,s65,0
Mavv 196,34,112
Mavv 216,38,112
Dv 30,%0,6144
Mavv 236,42,112
Dv 34,v1,6144
Mavv 232,42,108
Ln v8,32,s65,3072
S_ m0,s51,25472
Mavv 212,38,108
Mavv 208,38,104
Ln v7,32,s65,128
Mavv 228,42,104
Mavv 224,42,100
Ln v8,32,s65,3200
S_ m0,s51,26368
Mavv 204,38,100
Mavv 200,38,96
Ln v7,32,s65,256
Mavv 220,42,96
.Lstep1_8:
Zvvv 76,92,116
Dv 38,%0,7168
Zvvv 80,92,120
Dv 42,v1,7168
Zvvv 84,92,124
Zvvv 88,92,128
Zvvv 92,92,132
W_ 2
Nvvv 96,30,136,56
Mvvv 96,34,156
Nvvv 100,30,140,60
s_nop 1
v_max3_f32 v10, v76, v80, v84
s_nop 0
v_max3_f32 v10, v10, v88, v92
Mvvv 100,34,160
v_cmp_lt_f32 vcc, 0x42000000, v10
s_cbranch_vccnz .Ltramp1_7
Nvvv 104,30,144,64
A_ v46,v46,v76
A_ v47,v47,v80
Mvvv 104,34,164
A_ v48,v48,v84
A_ v49,v49,v88
Nvvv 108,30,148,68
A_ v50,v50,v92
Mvvv 108,34,168
Ln v8,32,s65,3328
Nvvv 112,30,152,72
Mvvv 112,34,172
W_ 0
Mvvv 112,38,192
Dv 30,%0,8192
Mvvv 112,42,212
Dv 34,v1,8192
Mvvv 108,38,188
Mvvv 108,42,208
Mvvv 104,38,184
Mvvv 104,42,204
Mvvv 100,38,180
Mvvv 100,42,200
Mvvv 96,38,176
Mvvv 96,42,196
W_ 0
Mvvv 96,30,216
Dv 38,%0,0
Mvvv 96,34,236
Dv 42,v1,0
Mvvv 100,30,220
Mvvv 100,34,240
)A"
R"A(Mvvv 104,30,224
Mvvv 104,34,244
Mvvv 108,30,228
Mvvv 108,34,248
s_nop 0
E_ 96
Mvvv 112,30,232
E_ 97
Mvvv 112,34,252
W_ 0
E_ 98
Nvvv 76,38,136,56
Dv 30,%0,1024
Mvvv 76,42,156
Dv 34,v1,1024
Nvvv 80,38,140,60
E_ 99
Mvvv 80,42,160
C_ 99,98,99
C_ 98,96,97
Nvvv 84,38,144,64
E_ 100
Mvvv 84,42,164
E_ 101
Nvvv 88,38,148,68
E_ 102
Mvvv 88,42,168
E_ 103
Nvvv 92,38,152,72
C_ 103,102,103
C_ 102,100,101
Mvvv 92,42,172
W_ 0
E_ 104
Mvvv 92,30,192
Dv 38,%0,2048
Mvvv 92,34,212
Dv 42,v1,2048
Mvvv 88,30,188
E_ 105
Mvvv 88,34,208
E_ 106
Mvvv 84,30,184
S_ s65,s61,98304
E_ 107
Mvvv 84,34,204
S_ m0,s51,0
C_ 107,106,107
C_ 106,104,105
Mvvv 80,30,180
s_waitcnt vmcnt(6) lgkmcnt(0)
s_barrier
Mvvv 80,34,200
L_ v9,36,s65,0
Mvvv 76,30,176
E_ 108
Mvvv 76,34,196
L_ v9,36,s65,3072
S_ m0,s51,896
Mvvv 76,38,216
Tv 30,v2,12288
Tv 32,v2,18432
Mvvv 76,42,236
L_ v9,36,s65,128
Mvvv 80,38,220
s_nop 0
Tv 34,v3,12288
Tv 36,v3,18432
Mvvv 80,42,240
L_ v9,36,s65,3200
S_ m0,s51,1792
Mvvv 84,38,224
E_ 109
Mvvv 84,42,244
L_ v9,36,s65,256
Mvvv 88,38,228
E_ 110
Mvvv 88,42,248
L_ v9,36,s65,3328
Mvvv 92,38,232
E_ 111
Mvvv 92,42,252
W_ 0
C_ 111,110,111
C_ 110,108,109
Mavv 0,30,116
s_nop 0
Tv 38,v4,12288
Tv 40,v4,18432
Mavv 20,34,116
E_ 112
Mavv 24,34,120
s_nop 0
Tv 42,v5,12288
Tv 44,v5,18432
Mavv 4,30,120
E_ 113
Mavv 8,30,124
E_ 114
Mavv 28,34,124
E_ 115
Mavv 32,34,128
C_ 115,114,115
C_ 114,112,113
Mavv 12,30,128
E_ 76
Mavv 16,30,132
E_ 77
Mavv 36,34,132
W_ 0
E_ 78
Mavv 56,38,132
s_nop 0
Tv 30,v2,13312
Tv 32,v2,19456
Mavv 76,42,132
E_ 79
Mavv 72,42,128
s_nop 0
Tv 34,v3,13312
Tv 36,v3,19456
Mavv 52,38,128
C_ 96,76,77
C_ 97,78,79
Mavv 48,38,124
E_ 80
Mavv 68,42,124
E_ 81
Mavv 64,42,120
E_ 82
Mavv 44,38,120
E_ 83
Mavv 40,38,116
C_ 100,80,81
C_ 101,82,83
Mavv 60,42,116
W_ 0
E_ 84
Mavv 80,30,116
s_nop 0
Tv 38,v4,13312
Tv 40,v4,19456
Mavv 100,34,116
E_ 85
Mavv 104,34,120
s_nop 0
Tv 42,v5,13312
Tv 44,v5,19456
Mavv 84,30,120
E_ 86
Mavv 88,30,124
E_ 87
Mavv 108,34,124
C_ 104,84,85
C_ 105,86,87
Mavv 112,34,128
E_ 88
Mavv 92,30,128
E_ 89
Mavv 96,30,132
E_ 90
Mavv 116,34,132
W_ 0
E_ 91
Mavv 136,38,132
s_nop 0
Tv 30,v2,14336
Tv 32,v2,20480
Mavv 156,42,132
C_ 108,88,89
C_ 109,90,91
Mavv 152,42,128
s_nop 0
Tv 34,v3,14336
Tv 36,v3,20480
Mavv 132,38,128
E_ 92
Mavv 128,38,124
E_ 93
Mavv 148,42,124
E_ 94
Mavv 144,42,120
E_ 95
Mavv 124,38,120
C_ 112,92,93
C_ 113,94,95
Mavv 120,38,116
v_mov_b32 v92, 0x3f803f80
v_mov_b32 v93, 0x3f803f80
Mavv 140,42,116
W_ 0
v_mov_b32 v94, 0x3f803f80
v_mov_b32 v95, 0x3f803f80
Mavv 160,30,116
s_nop 0
Tv 38,v4,14336
Tv 40,v4,20480
Mavv 180,34,116
Mavv 184,34,120
s_nop 0
Tv 42,v5,14336
Tv 44,v5,20480
Mavv 164,30,120
Mavv 168,30,124
Mavv 188,34,124
S_ s65,s61,122880
Mavv 192,34,128
S_ m0,s51,12288
Mavv 172,30,128
s_waitcnt vmcnt(6) lgkmcnt(0)
s_barrier
Mavv 176,30,132
Ln v7,32,s65,0
Mavv 196,34,132
Mavv 216,38,132
Dv 30,%0,30720
Mavv 236,42,132
Dv 34,v1,30720
Mavv 232,42,128
Ln v8,32,s65,3072
S_ m0,s51,13184
Mavv 212,38,128
Mavv 208,38,124
Ln v7,32,s65,128
Mavv 228,42,124
Mavv 224,42,120
Ln v8,32,s65,3200
S_ m0,s51,14080
Mavv 204,38,120
Mavv 200,38,116
Ln v7,32,s65,256
Mavv 220,42,116
s_cmp_eq_u32 s4, 0
s_cbranch_scc1 .Lhs_d1
.Lhs_d1_back:
.Lstep1_9:
Zvvv 76,92,96
Dv 38,%0,31744
Zvvv 80,92,100
Dv 42,v1,31744
Zvvv 84,92,104
Zvvv 88,92,108
Zvvv 92,92,112
W_ 2
Nvvv 116,30,136,56
Mvvv 116,34,156
Nvvv 120,30,140,60
s_nop 1
v_max3_f32 v10, v76, v80, v84
s_nop 0
v_max3_f32 v10, v10, v88, v92
Mvvv 120,34,160
v_cmp_lt_f32 vcc, 0x42000000, v10
s_cbranch_vccnz .Ltramp1_8
Nvvv 124,30,144,64
A_ v46,v46,v76
A_ v47,v47,v80
Mvvv 124,34,164
A_ v48,v48,v84
A_ v49,v49,v88
Nvvv 128,30,148,68
A_ v50,v50,v92
Mvvv 128,34,168
Ln v8,32,s65,3328
Nvvv 132,30,152,72
Mvvv 132,34,172
W_ 0
Mvvv 132,38,192
Dv 30,%0,32768
Mvvv 132,42,212
Dv 34,v1,32768
Mvvv 128,38,188
Mvvv 128,42,208
Mvvv 124,38,184
Mvvv 124,42,204
Mvvv 120,38,180
Mvvv 120,42,200
Mvvv 116,38,176
Mvvv 116,42,196
W_ 0
Mvvv 116,30,216
Dv 38,%0,24576
Mvvv 116,34,236
Dv 42,v1,24576
Mvvv 120,30,220
Mvvv 120,34,240
Mvvv 124,30,224
Mvvv 124,34,244
Mvvv 128,30,228
Mvvv 128,34,248
s_nop 0
E_ 116
Mvvv 132,30,232
E_ 117
Mvvv 132,34,252
W_ 0
E_ 118
Nvvv 76,38,136,56
Dv 30,%0,25600
Mvvv 76,42,156
Dv 34,v1,25600
Nvvv 80,38,140,60
E_ 119
Mvvv 80,42,160
C_ 119,118,119
C_ 118,116,117
Nvvv 84,38,144,64
E_ 120
Mvvv 84,42,164
E_ 121
Nvvv 88,38,148,68
E_ 122
Mvvv 88,42,168
E_ 123
Nvvv 92,38,152,72
C_ 123,122,123
C_ 122,120,121
Mvvv 92,42,172
W_ 0
E_ 124
Mvvv 92,30,192
Dv 38,%0,26624
Mvvv 92,34,212
Dv 42,v1,26624
Mvvv 88,30,188
E_ 125
Mvvv 88,34,208
E_ 126
Mvvv 84,30,184
S_ s65,s61,110592
E_ 127
Mvvv 84,34,204
S_ m0,s51,24576
C_ 127,126,127
C_ 126,124,125
Mvvv 80,30,180
s_waitcnt vmcnt(6) lgkmcnt(0)
s_barrier
Mvvv 80,34,200
L_ v9,36,s65,0
Mvvv 76,30,176
E_ 128
Mvvv 76,34,196
L_ v9,36,s65,3072
S_ m0,s51,25472
Mvvv 76,38,216
Tv 30,v2,0
Tv 32,v2,6144
Mvvv 76,42,236
L_ v9,36,s65,128
Mvvv 80,38,220
s_nop 0
Tv 34,v3,0
Tv 36,v3,6144
Mvvv 80,42,240
L_ v9,36,s65,3200
S_ m0,s51,26368
Mvvv 84,38,224
E_ 129
Mvvv 84,42,244
L_ v9,36,s65,256
Mvvv 88,38,228
E_ 130
Mvvv 88,42,248
L_ v9,36,s65,3328
Mvvv 92,38,232
E_ 131
Mvvv 92,42,252
W_ 0
C_ 131,130,131
C_ 130,128,129
Mavv 0,30,96
s_nop 0
Tv 38,v4,0
Tv 40,v4,6144
Mavv 20,34,96
E_ 132
Mavv 24,34,100
s_nop 0
Tv 42,v5,0
Tv 44,v5,6144
Mavv 4,30,100
E_ 133
Mavv 8,30,104
E_ 134
Mavv 28,34,104
E_ 135
Mavv 32,34,108
C_ 135,134,135
C_ 134,132,133
Mavv 12,30,108
E_ 76
Mavv 16,30,112
E_ 77
Mavv 36,34,112
W_ 0
E_ 78
Mavv 56,38,112
s_nop 0
Tv 30,v2,1024
Tv 32,v2,7168
Mavv 76,42,112
E_ 79
Mavv 72,42,108
s_nop 0
Tv 34,v3,1024
Tv 36,v3,7168
Mavv 52,38,108
C_ 116,76,77
C_ 117,78,79
Mavv 48,38,104
E_ 80
Mavv 68,42,104
E_ 81
Mavv 64,42,100
E_ 82
Mavv 44,38,100
E_ 83
Mavv 40,38,96
C_ 120,80,81
C_ 121,82,83
Mavv 60,42,96
W_ 0
E_ 84
Mavv 80,30,96
s_nop 0
Tv 38,v4,1024
Tv 40,v4,7168
Mavv 100,34,96
E_ 85
Mavv 104,34,100
s_nop 0
Tv 42,v5,1024
Tv 44,v5,7168
Mavv 84,30,100
E_ 86
Mavv 88,30,104
E_ 87
Mavv 108,34,104
C_ 124,84,85
C_ 125,86,87
Mavv 112,34,108
E_ 88
Mavv 92,30,108
E_ 89
Mavv 96,30,112
E_ 90
Mavv 116,34,112
W_ 0
E_ 91
Mavv 136,38,112
s_nop 0
Tv 30,v2,2048
Tv 32,v2,8192
Mavv 156,42,112
C_ 128,88,89
C_ 129,90,91
Mavv 152,42,108
s_nop 0
Tv 34,v3,2048
Tv 36,v3,8192
Mavv 132,38,108
E_ 92
Mavv 128,38,104
E_ 93
Mavv 148,42,104
E_ 94
Mavv 144,42,100
E_ 95
Mavv 124,38,100
C_ 132,92,93
C_ 133,94,95
Mavv 120,38,96
v_mov_b32 v92, 0x3f803f80
v_mov_b32 v93, 0x3f803f80
Mavv 140,42,96
W_ 0
v_mov_b32 v94, 0x3f803f80
v_mov_b32 v95, 0x3f803f80
Mavv 160,30,96
s_nop 0
Tv 38,v4,2048
Tv 40,v4,8192
Mavv 180,34,96
Mavv 184,34,100
s_nop 0
Tv 42,v5,2048
Tv 44,v5,8192
Mavv 164,30,100
Mavv 168,30,104
Mavv 188,34,104
S_ s65,s61,135168
Mavv 192,34,108
S_ m0,s51,0
Mavv 172,30,108
s_waitcnt vmcnt(6) lgkmcnt(0)
s_barrier
Mavv 176,30,112
Ln v7,32,s65,0
Mavv 196,34,112
Mavv 216,38,112
Dv 30,%0,18432
Mavv 236,42,112
Dv 34,v1,18432
Mavv 232,42,108
Ln v8,32,s65,3072
S_ m0,s51,896
Mavv 212,38,108
Mavv 208,38,104
Ln v7,32,s65,128
Mavv 228,42,104
Mavv 224,42,100
Ln v8,32,s65,3200
S_ m0,s51,1792
Mavv 204,38,100
Mavv 200,38,96
Ln v7,32,s65,256
Mavv 220,42,96
.Lstep1_10:
Zvvv 76,92,116
Dv 38,%0,19456
Zvvv 80,92,120
Dv 42,v1,19456
Zvvv 84,92,124
Zvvv 88,92,128
Zvvv 92,92,132
W_ 2
Nvvv 96,30,136,56
Mvvv 96,34,156
Nvvv 100,30,140,60
s_nop 1
v_max3_f32 v10, v76, v80, v84
s_nop 0
v_max3_f32 v10, v10, v88, v92
Mvvv 100,34,160
v_cmp_lt_f32 vcc, 0x42000000, v10
s_cbranch_vccnz .Ltramp1_9
Nvvv 104,30,144,64
A_ v46,v46,v76
A_ v47,v47,v80
Mvvv 104,34,164
A_ v48,v48,v84
A_ v49,v49,v88
Nvvv 108,30,148,68
A_ v50,v50,v92
Mvvv 108,34,168
Ln v8,32,s65,3328
Nvvv 112,30,152,72
Mvvv 112,34,172
W_ 0
Mvvv 112,38,192
Dv 30,%0,20480
Mvvv 112,42,212
Dv 34,v1,20480
Mvvv 108,38,188
Mvvv 108,42,208
Mvvv 104,38,184
Mvvv 104,42,204
Mvvv 100,38,180
Mvvv 100,42,200
Mvvv 96,38,176
Mvvv 96,42,196
W_ 0
Mvvv 96,30,216
Dv 38,%0,12288
Mvvv 96,34,236
Dv 42,v1,12288
Mvvv 100,30,220
Mvvv 100,34,240
Mvvv 104,30,224
Mvvv 104,34,244
Mvvv 108,30,228
Mvvv 108,34,248
s_nop 0
E_ 96
Mvvv 112,30,232
E_ 97
Mvvv 112,34,252
W_ 0
E_ 98
Nvvv 76,38,136,56
Dv 30,%0,13312
Mvvv 76,42,156
Dv 34,v1,13312
Nvvv 80,38,140,60
E_ 99
Mvvv 80,42,160
C_ 99,98,99
C_ 98,96,97
Nvvv 84,38,144,64
E_ 100
Mvvv 84,42,164
E_ 101
Nvvv 88,38,148,68
E_ 102
Mvvv 88,42,168
E_ 103
Nvvv 92,38,152,72
C_ 103,102,103
C_ 102,100,101
Mvvv 92,42,172
W_ 0
E_ 104
Mvvv 92,30,192
Dv 38,%0,14336
Mvvv 92,34,212
Dv 42,v1,14336
Mvvv 88,30,188
E_ 105
Mvvv 88,34,208
E_ 106
Mvvv 84,30,184
S_ s65,s61,122880
E_ 107
Mvvv 84,34,204
S_ m0,s51,12288
C_ 107,106,107
C_ 106,104,105
Mvvv 80,30,180
s_waitcnt vmcnt(6) lgkmcnt(0)
s_barrier
Mvvv 80,34,200
L_ v9,36,s65,0
Mvvv 76,30,176
E_ 108
Mvvv 76,34,196
L_ v9,36,s65,3072
S_ m0,s51,13184
Mvvv 76,38,216
Tv 30,v2,24576
Tv 32,v2,30720
Mvvv 76,42,236
L_ v9,36,s65,128
Mvvv 80,38,220
s_nop 0
Tv 34,v3,24576
Tv 36,v3,30720
Mvvv 80,42,240
L_ v9,36,s65,3200
S_ m0,s51,14080
Mvvv 84,38,224
E_ 109
Mvvv 84,42,244
L_ v9,36,s65,256
Mvvv 88,38,228
E_ 110
Mvvv 88,42,248
L_ v9,36,s65,3328
Mvvv 92,38,232
E_ 111
Mvvv 92,42,252
W_ 0
C_ 111,110,111
C_ 110,108,109
Mavv 0,30,116
s_nop 0
Tv 38,v4,24576
Tv 40,v4,30720
Mavv 20,34,116
E_ 112
Mavv 24,34,120
s_nop 0
Tv 42,v5,24576
Tv 44,v5,30720
Mavv 4,30,120
E_ 113
Mavv 8,30,124
E_ 114
Mavv 28,34,124
E_ 115
Mavv 32,34,128
C_ 115,114,115
C_ 114,112,113
Mavv 12,30,128
E_ 76
Mavv 16,30,132
E_ 77
Mavv 36,34,132
W_ 0
E_ 78
Mavv 56,38,132
s_nop 0
Tv 30,v2,25600
Tv 32,v2,31744
Mavv 76,42,132
E_ 79
Mavv 72,42,128
s_nop 0
Tv 34,v3,25600
Tv 36,v3,31744
Mavv 52,38,128
C_ 96,76,77
C_ 97,78,79
Mavv 48,38,124
E_ 80
Mavv 68,42,124
E_ 81
Mavv 64,42,120
E_ 82
Mavv 44,38,120
E_ 83
Mavv 40,38,116
C_ 100,80,81
C_ 101,82,83
Mavv 60,42,116
W_ 0
E_ 84
Mavv 80,30,116
s_nop 0
Tv 38,v4,25600
Tv 40,v4,31744
Mavv 100,34,116
E_ 85
Mavv 104,34,120
s_nop 0
Tv 42,v5,25600
Tv 44,v5,31744
Mavv 84,30,120
E_ 86
Mavv 88,30,124
E_ 87
Mavv 108,34,124
C_ 104,84,85
C_ 105,86,87
Mavv 112,34,128
E_ 88
Mavv 92,30,128
E_ 89
Mavv 96,30,132
E_ 90
Mavv 116,34,132
W_ 0
E_ 91
Mavv 136,38,132
s_nop 0
Tv 30,v2,26624
Tv 32,v2,32768
Mavv 156,42,132
C_ 108,88,89
C_ 109,90,91
Mavv 152,42,128
s_nop 0
Tv 34,v3,26624
Tv 36,v3,32768
Mavv 132,38,128
E_ 92
Mavv 128,38,124
E_ 93
Mavv 148,42,124
E_ 94
Mavv 144,42,120
E_ 95
Mavv 124,38,120
C_ 112,92,93
C_ 113,94,95
Mavv 120,38,116
v_mov_b32 v92, 0x3f803f80
v_mov_b32 v93, 0x3f803f80
Mavv 140,42,116
W_ 0
v_mov_b32 v94, 0x3f803f80
v_mov_b32 v95, 0x3f803f80
Mavv 160,30,116
s_nop 0
Tv 38,v4,26624
Tv 40,v4,32768
Mavv 180,34,116
Mavv 184,34,120
s_nop 0
Tv 42,v5,26624
Tv 44,v5,32768
Mavv 164,30,120
Mavv 168,30,124
Mavv 188,34,124
S_ s65,s61,147456
Mavv 192,34,128
S_ m0,s51,24576
Mavv 172,30,128
s_waitcnt vmcnt(6) lgkmcnt(0)
s_barrier
Mavv 176,30,132
Ln v7,32,s65,0
Mavv 196,34,132
Mavv 216,38,132
Dv 30,%0,6144
Mavv 236,42,132
Dv 34,v1,6144
Mavv 232,42,128
Ln v8,32,s65,3072
S_ m0,s51,25472
Mavv 212,38,128
Mavv 208,38,124
Ln v7,32,s65,128
Mavv 228,42,124
Mavv 224,42,120
Ln v8,32,s65,3200
S_ m0,s51,26368
Mavv 204,38,120
Mavv 200,38,116
Ln v7,32,s65,256
Mavv 220,42,116
.Lstep1_11:
Zvvv 76,92,96
Dv 38,%0,7168
Zvvv 80,92,100
Dv 42,v1,7168
Zvvv 84,92,104
Zvvv 88,92,108
Zvvv 92,92,112
W_ 2
Nvvv 116,30,136,56
Mvvv 116,34,156
Nvvv 120,30,140,60
s_nop 1
v_max3_f32 v10, v76, v80, v84
s_nop 0
v_max3_f32 v10, v10, v88, v92
Mvvv 120,34,160
v_cmp_lt_f32 vcc, 0x42000000, v10
s_cbranch_vccnz .Ltramp1_10
Nvvv 124,30,144,64
A_ v46,v46,v76
A_ v47,v47,v80
Mvvv 124,34,164
A_ v48,v48,v84
A_ v49,v49,v88
Nvvv 128,30,148,68
A_ v50,v50,v92
Mvvv 128,34,168
Ln v8,32,s65,3328
Nvvv 132,30,152,72
Mvvv 132,34,172
W_ 0
Mvvv 132,38,192
Dv 30,%0,8192
Mvvv 132,42,212
Dv 34,v1,8192
Mvvv 128,38,188
Mvvv 128,42,208
Mvvv 124,38,184
Mvvv 124,42,204
Mvvv 120,38,180
Mvvv 120,42,200
Mvvv 116,38,176
Mvvv 116,42,196
W_ 0
Mvvv 116,30,216
Dv 38,%0,0
Mvvv 116,34,236
Dv 42,v1,0
Mvvv 120,30,220
Mvvv 120,34,240
Mvvv 124,30,224
Mvvv 124,34,244
Mvvv 128,30,228
Mvvv 128,34,248
s_nop 0
E_ 116
Mvvv 132,30,232
E_ 117
Mvvv 132,34,252
W_ 0
E_ 118
Nvvv 76,38,136,56
Dv 30,%0,1024
Mvvv 76,42,156
Dv 34,v1,1024
Nvvv 80,38,140,60
E_ 119
Mvvv 80,42,160
C_ 119,118,119
C_ 118,116,117
Nvvv 84,38,144,64
E_ 120
Mvvv 84,42,164
E_ 121
Nvvv 88,38,148,68
E_ 122
Mvvv 88,42,168
E_ 123
Nvvv 92,38,152,72
C_ 123,122,123
C_ 122,120,121
Mvvv 92,42,172
W_ 0
E_ 124
Mvvv 92,30,192
Dv 38,%0,2048
Mvvv 92,34,212
Dv 42,v1,2048
Mvvv 88,30,188
E_ 125
Mvvv 88,34,208
E_ 126
Mvvv 84,30,184
S_ s65,s61,135168
E_ 127
Mvvv 84,34,204
S_ m0,s51,0
C_ 127,126,127
C_ 126,124,125
Mvvv 80,30,180
s_waitcnt vmcnt(6) lgkmcnt(0)
s_barrier
Mvvv 80,34,200
L_ v9,36,s65,0
Mvvv 76,30,176
E_ 128
Mvvv 76,34,196
L_ v9,36,s65,3072
S_ m0,s51,896
Mvvv 76,38,216
Tv 30,v2,12288
Tv 32,v2,18432
Mvvv 76,42,236
L_ v9,36,s65,128
Mvvv 80,38,220
s_nop 0
Tv 34,v3,12288
Tv 36,v3,18432
Mvvv 80,42,240
L_ v9,36,s65,3200
S_ m0,s51,1792
Mvvv 84,38,224
E_ 129
Mvvv 84,42,244
L_ v9,36,s65,256
Mvvv 88,38,228
E_ 130
Mvvv 88,42,248
L_ v9,36,s65,3328
Mvvv 92,38,232
E_ 131
Mvvv 92,42,252
W_ 0
C_ 131,130,131
C_ 130,128,129
Mavv 0,30,96
s_nop 0
Tv 38,v4,12288
Tv 40,v4,18432
Mavv 20,34,96
E_ 132
Mavv 24,34,100
s_nop 0
Tv 42,v5,12288
Tv 44,v5,18432
Mavv 4,30,100
E_ 133
Mavv 8,30,104
E_ 134
Mavv 28,34,104
E_ 135
Mavv 32,34,108
C_ 135,134,135
C_ 134,132,133
Mavv 12,30,108
E_ 76
Mavv 16,30,112
E_ 77
Mavv 36,34,112
W_ 0
E_ 78
Mavv 56,38,112
s_nop 0
Tv 30,v2,13312
Tv 32,v2,19456
Mavv 76,42,112
E_ 79
Mavv 72,42,108
s_nop 0
Tv 34,v3,13312
Tv 36,v3,19456
Mavv 52,38,108
C_ 116,76,77
C_ 117,78,79
Mavv 48,38,104
E_ 80
Mavv 68,42,104
E_ 81
Mavv 64,42,100
E_ 82
Mavv 44,38,100
E_ 83
Mavv 40,38,96
C_ 120,80,81
C_ 121,82,83
Mavv 60,42,96
W_ 0
E_ 84
Mavv 80,30,96
s_nop 0
Tv 38,v4,13312
Tv 40,v4,19456
Mavv 100,34,96
E_ 85
Mavv 104,34,100
s_nop 0
Tv 42,v5,13312
Tv 44,v5,19456
Mavv 84,30,100
E_ 86
Mavv 88,30,104
E_ 87
Mavv 108,34,104
C_ 124,84,85
C_ 125,86,87
Mavv 112,34,108
E_ 88
Mavv 92,30,108
E_ 89
Mavv 96,30,112
E_ 90
Mavv 116,34,112
W_ 0
E_ 91
Mavv 136,38,112
s_nop 0
Tv 30,v2,14336
Tv 32,v2,20480
Mavv 156,42,112
C_ 128,88,89
C_ 129,90,91
Mavv 152,42,108
s_nop 0
Tv 34,v3,14336
Tv 36,v3,20480
Mavv 132,38,108
E_ 92
Mavv 128,38,104
E_ 93
Mavv 148,42,104
E_ 94
Mavv 144,42,100
E_ 95
Mavv 124,38,100
C_ 132,92,93
C_ 133,94,95
Mavv 120,38,96
v_mov_b32 v92, 0x3f803f80
v_mov_b32 v93, 0x3f803f80
Mavv 140,42,96
W_ 0
v_mov_b32 v94, 0x3f803f80
v_mov_b32 v95, 0x3f803f80
Mavv 160,30,96
s_nop 0
Tv 38,v4,14336
Tv 40,v4,20480
Mavv 180,34,96
Mavv 184,34,100
s_nop 0
Tv 42,v5,14336
Tv 44,v5,20480
Mavv 164,30,100
Mavv 168,30,104
Mavv 188,34,104
S_ s65,s61,159744
Mavv 192,34,108
S_ m0,s51,12288
Mavv 172,30,108
s_waitcnt vmcnt(6) lgkmcnt(0)
s_barrier
Mavv 176,30,112
Ln v7,32,s65,0
Mavv 196,34,112
Mavv 216,38,112
Dv 30,%0,30720
Mavv 236,42,112
Dv 34,v1,30720
Mavv 232,42,108
Ln v8,32,s65,3072
S_ m0,s51,13184
Mavv 212,38,108
Mavv 208,38,104
Ln v7,32,s65,128
Mavv 228,42,104
Mavv 224,42,100
Ln v8,32,s65,3200
S_ m0,s51,14080
Mavv 204,38,100
Mavv 200,38,96
Ln v7,32,s65,256
Mavv 220,42,96
.Lstep1_12:
Zvvv 76,92,116
Dv 38,%0,31744
Zvvv 80,92,120
Dv 42,v1,31744
Zvvv 84,92,124
Zvvv 88,92,128
Zvvv 92,92,132
W_ 2
Nvvv 96,30,136,56
Mvvv 96,34,156
Nvvv 100,30,140,60
s_nop 1
v_max3_f32 v10, v76, v80, v84
s_nop 0
v_max3_f32 v10, v10, v88, v92
Mvvv 100,34,160
v_cmp_lt_f32 vcc, 0x42000000, v10
s_cbranch_vccnz .Ltramp1_11
Nvvv 104,30,144,64
A_ v46,v46,v76
A_ v47,v47,v80
Mvvv 104,34,164
A_ v48,v48,v84
A_ v49,v49,v88
Nvvv 108,30,148,68
A_ v50,v50,v92
Mvvv 108,34,168
Ln v8,32,s65,3328
Nvvv 112,30,152,72
R_ v11,a248
Mvvv 112,34,172
W_ 0
v_min_u32 v11, 240, v11
Mvvv 112,38,192
Dv 30,%0,32768
v_lshlrev_b32 v11, 3, v11
Mvvv 112,42,212
Dv 34,v1,32768
Mvvv 108,38,188
buffer_load_dword a254, v11, s[44:47], s66 offen
Mvvv 108,42,208
Mvvv 104,38,184
Mvvv 104,42,204
Mvvv 100,38,180
Mvvv 100,42,200
Mvvv 96,38,176
Mvvv 96,42,196
W_ 0
Mvvv 96,30,216
Dv 38,%0,24576
Mvvv 96,34,236
Dv 42,v1,24576
Mvvv 100,30,220
Mvvv 100,34,240
Mvvv 104,30,224
Mvvv 104,34,244
Mvvv 108,30,228
Mvvv 108,34,248
s_nop 0
E_ 96
Mvvv 112,30,232
E_ 97
Mvvv 112,34,252
W_ 0
E_ 98
Nvvv 76,38,136,56
Dv 30,%0,25600
Mvvv 76,42,156
Dv 34,v1,25600
Nvvv 80,38,140,60
E_ 99
Mvvv 80,42,160
C_ 99,98,99
C_ 98,96,97
Nvvv 84,38,144,64
E_ 100
Mvvv 84,42,164
E_ 101
Nvvv 88,38,148,68
E_ 102
Mvvv 88,42,168
E_ 103
Nvvv 92,38,152,72
C_ 103,102,103
C_ 102,100,101
Mvvv 92,42,172
W_ 0
E_ 104
Mvvv 92,30,192
Dv 38,%0,26624
Mvvv 92,34,212
Dv 42,v1,26624
Mvvv 88,30,188
E_ 105
Mvvv 88,34,208
E_ 106
Mvvv 84,30,184
S_ s65,s61,147456
E_ 107
Mvvv 84,34,204
S_ m0,s51,24576
C_ 107,106,107
C_ 106,104,105
Mvvv 80,30,180
s_waitcnt vmcnt(7) lgkmcnt(0)
s_barrier
Mvvv 80,34,200
L_ v9,36,s65,0
Mvvv 76,30,176
E_ 108
Mvvv 76,34,196
L_ v9,36,s65,3072
S_ m0,s51,25472
Mvvv 76,38,216
Tv 30,v2,0
Tv 32,v2,6144
Mvvv 76,42,236
L_ v9,36,s65,128
Mvvv 80,38,220
s_nop 0
Tv 34,v3,0
Tv 36,v3,6144
Mvvv 80,42,240
L_ v9,36,s65,3200
S_ m0,s51,26368
Mvvv 84,38,224
E_ 109
Mvvv 84,42,244
L_ v9,36,s65,256
Mvvv 88,38,228
E_ 110
Mvvv 88,42,248
L_ v9,36,s65,3328
Mvvv 92,38,232
E_ 111
Mvvv 92,42,252
W_ 0
C_ 111,110,111
C_ 110,108,109
Mavv 0,30,116
s_nop 0
Tv 38,v4,0
Tv 40,v4,6144
Mavv 20,34,116
E_ 112
Mavv 24,34,120
s_nop 0
Tv 42,v5,0
Tv 44,v5,6144
Mavv 4,30,120
E_ 113
Mavv 8,30,124
E_ 114
Mavv 28,34,124
E_ 115
Mavv 32,34,128
C_ 115,114,115
C_ 114,112,113
Mavv 12,30,128
E_ 76
Mavv 16,30,132
E_ 77
Mavv 36,34,132
W_ 0
E_ 78
Mavv 56,38,132
s_nop 0
Tv 30,v2,1024
Tv 32,v2,7168
Mavv 76,42,132
E_ 79
Mavv 72,42,128
s_nop 0
Tv 34,v3,1024
Tv 36,v3,7168
Mavv 52,38,128
C_ 96,76,77
C_ 97,78,79
Mavv 48,38,124
E_ 80
Mavv 68,42,124
E_ 81
Mavv 64,42,120
E_ 82
Mavv 44,38,120
E_ 83
Mavv 40,38,116
C_ 100,80,81
C_ 101,82,83
Mavv 60,42,116
W_ 0
E_ 84
Mavv 80,30,116
s_nop 0
Tv 38,v4,1024
Tv 40,v4,7168
Mavv 100,34,116
E_ 85
Mavv 104,34,120
s_nop 0
Tv 42,v5,1024
Tv 44,v5,7168
Mavv 84,30,120
E_ 86
Mavv 88,30,124
E_ 87
Mavv 108,34,124
C_ 104,84,85
C_ 105,86,87
Mavv 112,34,128
E_ 88
Mavv 92,30,128
E_ 89
Mavv 96,30,132
E_ 90
Mavv 116,34,132
W_ 0
E_ 91
Mavv 136,38,132
s_nop 0
Tv 30,v2,2048
Tv 32,v2,8192
Mavv 156,42,132
C_ 108,88,89
C_ 109,90,91
Mavv 152,42,128
s_nop 0
Tv 34,v3,2048
Tv 36,v3,8192
Mavv 132,38,128
E_ 92
Mavv 128,38,124
E_ 93
Mavv 148,42,124
E_ 94
Mavv 144,42,120
E_ 95
Mavv 124,38,120
C_ 112,92,93
C_ 113,94,95
Mavv 120,38,116
v_mov_b32 v92, 0x3f803f80
v_mov_b32 v93, 0x3f803f80
Mavv 140,42,116
W_ 0
v_mov_b32 v94, 0x3f803f80
v_mov_b32 v95, 0x3f803f80
Mavv 160,30,116
s_nop 0
Tv 38,v4,2048
Tv 40,v4,8192
Mavv 180,34,116
Mavv 184,34,120
s_nop 0
Tv 42,v5,2048
Tv 44,v5,8192
Mavv 164,30,120
Mavv 168,30,124
Mavv 188,34,124
S_ s65,s61,172032
Mavv 192,34,128
S_ m0,s51,0
Mavv 172,30,128
s_waitcnt vmcnt(7) lgkmcnt(0)
s_barrier
Mavv 176,30,132
Ln v7,32,s65,0
Mavv 196,34,132
Mavv 216,38,132
Dv 30,%0,18432
Mavv 236,42,132
Dv 34,v1,18432
Mavv 232,42,128
Ln v8,32,s65,3072
S_ m0,s51,896
Mavv 212,38,128
Mavv 208,38,124
Ln v7,32,s65,128
Mavv 228,42,124
Mavv 224,42,120
Ln v8,32,s65,3200
S_ m0,s51,1792
Mavv 204,38,120
Mavv 200,38,116
Ln v7,32,s65,256
Mavv 220,42,116
.Lstep1_13:
Zvvv 76,92,96
Dv 38,%0,19456
Zvvv 80,92,100
Dv 42,v1,19456
Zvvv 84,92,104
Zvvv 88,92,108
Zvvv 92,92,112
W_ 2
Nvvv 116,30,136,56
Mvvv 116,34,156
Nvvv 120,30,140,60
s_nop 1
v_max3_f32 v10, v76, v80, v84
s_nop 0
v_max3_f32 v10, v10, v88, v92
Mvvv 120,34,160
v_cmp_lt_f32 vcc, 0x42000000, v10
s_cbranch_vccnz .Ltramp1_12
Nvvv 124,30,144,64
A_ v46,v46,v76
A_ v47,v47,v80
Mvvv 124,34,164
A_ v48,v48,v84
A_ v49,v49,v88
Nvvv 128,30,148,68
A_ v50,v50,v92
Mvvv 128,34,168
Ln v8,32,s65,3328
Nvvv 132,30,152,72
Mvvv 132,34,172
W_ 0
Mvvv 132,38,192
Dv 30,%0,20480
Mvvv 132,42,212
Dv 34,v1,20480
Mvvv 128,38,188
Mvvv 128,42,208
Mvvv 124,38,184
Mvvv 124,42,204
Mvvv 120,38,180
Mvvv 120,42,200
Mvvv 116,38,176
Mvvv 116,42,196
W_ 0
Mvvv 116,30,216
Dv 38,%0,12288
Mvvv 116,34,236
Dv 42,v1,12288
Mvvv 120,30,220
Mvvv 120,34,240
Mvvv 124,30,224
Mvvv 124,34,244
Mvvv 128,30,228
Mvvv 128,34,248
s_nop 0
E_ 116
Mvvv 132,30,232
E_ 117
Mvvv 132,34,252
W_ 0
E_ 118
Nvvv 76,38,136,56
Dv 30,%0,13312
Mvvv 76,42,156
Dv 34,v1,13312
Nvvv 80,38,140,60
E_ 119
Mvvv 80,42,160
C_ 119,118,119
C_ 118,116,117
Nvvv 84,38,144,64
E_ 120
Mvvv 84,42,164
E_ 121
Nvvv 88,38,148,68
E_ 122
Mvvv 88,42,168
E_ 123
Nvvv 92,38,152,72
C_ 123,122,123
C_ 122,120,121
Mvvv 92,42,172
W_ 0
E_ 124
Mvvv 92,30,192
Dv 38,%0,14336
Mvvv 92,34,212
Dv 42,v1,14336
Mvvv 88,30,188
E_ 125
Mvvv 88,34,208
E_ 126
Mvvv 84,30,184
S_ s65,s61,159744
E_ 127
Mvvv 84,34,204
S_ m0,s51,12288
C_ 127,126,127
C_ 126,124,125
Mvvv 80,30,180
s_waitcnt vmcnt(6) lgkmcnt(0)
s_barrier
Mvvv 80,34,200
L_ v9,36,s65,0
Mvvv 76,30,176
E_ 128
Mvvv 76,34,196
L_ v9,36,s65,3072
S_ m0,s51,13184
Mvvv 76,38,216
Tv 30,v2,24576
Tv 32,v2,30720
Mvvv 76,42,236
L_ v9,36,s65,128
Mvvv 80,38,220
s_nop 0
Tv 34,v3,24576
Tv 36,v3,30720
Mvvv 80,42,240
L_ v9,36,s65,3200
S_ m0,s51,14080
Mvvv 84,38,224
E_ 129
Mvvv 84,42,244
L_ v9,36,s65,256
Mvvv 88,38,228
E_ 130
Mvvv 88,42,248
L_ v9,36,s65,3328
Mvvv 92,38,232
E_ 131
Mvvv 92,42,252
W_ 0
C_ 131,130,131
C_ 130,128,129
Mavv 0,30,96
s_nop 0
Tv 38,v4,24576
Tv 40,v4,30720
Mavv 20,34,96
E_ 132
Mavv 24,34,100
s_nop 0
Tv 42,v5,24576
Tv 44,v5,30720
Mavv 4,30,100
E_ 133
Mavv 8,30,104
E_ 134
Mavv 28,34,104
E_ 135
Mavv 32,34,108
C_ 135,134,135
C_ 134,132,133
Mavv 12,30,108
E_ 76
Mavv 16,30,112
E_ 77
Mavv 36,34,112
W_ 0
E_ 78
Mavv 56,38,112
s_nop 0
Tv 30,v2,25600
Tv 32,v2,31744
Mavv 76,42,112
E_ 79
Mavv 72,42,108
s_nop 0
Tv 34,v3,25600
Tv 36,v3,31744
Mavv 52,38,108
C_ 116,76,77
C_ 117,78,79
Mavv 48,38,104
E_ 80
Mavv 68,42,104
E_ 81
Mavv 64,42,100
E_ 82
Mavv 44,38,100
E_ 83
Mavv 40,38,96
C_ 120,80,81
C_ 121,82,83
Mavv 60,42,96
W_ 0
E_ 84
Mavv 80,30,96
s_nop 0
Tv 38,v4,25600
Tv 40,v4,31744
Mavv 100,34,96
E_ 85
Mavv 104,34,100
s_nop 0
Tv 42,v5,25600
Tv 44,v5,31744
Mavv 84,30,100
E_ 86
Mavv 88,30,104
E_ 87
Mavv 108,34,104
C_ 124,84,85
C_ 125,86,87
Mavv 112,34,108
E_ 88
Mavv 92,30,108
E_ 89
Mavv 96,30,112
E_ 90
Mavv 116,34,112
W_ 0
E_ 91
Mavv 136,38,112
s_nop 0
Tv 30,v2,26624
Tv 32,v2,32768
Mavv 156,42,112
C_ 128,88,89
C_ 129,90,91
Mavv 152,42,108
s_nop 0
Tv 34,v3,26624
Tv 36,v3,32768
Mavv 132,38,108
E_ 92
Mavv 128,38,104
E_ 93
Mavv 148,42,104
E_ 94
Mavv 144,42,100
E_ 95
Mavv 124,38,100
C_ 132,92,93
C_ 133,94,95
Mavv 120,38,96
v_mov_b32 v92, 0x3f803f80
v_mov_b32 v93, 0x3f803f80
Mavv 140,42,96
W_ 0
v_mov_b32 v94, 0x3f803f80
v_mov_b32 v95, 0x3f803f80
Mavv 160,30,96
s_nop 0
Tv 38,v4,26624
Tv 40,v4,32768
Mavv 180,34,96
Mavv 184,34,100
s_nop 0
Tv 42,v5,26624
Tv 44,v5,32768
Mavv 164,30,100
Mavv 168,30,104
Mavv 188,34,104
S_ s65,s61,184320
Mavv 192,34,108
S_ m0,s51,24576
Mavv 172,30,108
s_waitcnt vmcnt(6) lgkmcnt(0)
s_barrier
Mavv 176,30,112
Ln v7,32,s65,0
Mavv 196,34,112
Mavv 216,38,112
Dv 30,%0,6144
Mavv 236,42,112
Dv 34,v1,6144
Mavv 232,42,108
Ln v8,32,s65,3072
S_ m0,s51,25472
Mavv 212,38,108
Mavv 208,38,104
Ln v7,32,s65,128
Mavv 228,42,104
Mavv 224,42,100
Ln v8,32,s65,3200
S_ m0,s51,26368
Mavv 204,38,100
Mavv 200,38,96
Ln v7,32,s65,256
Mavv 220,42,96
.Lstep1_14:
Zvvv 76,92,116
Dv 38,%0,7168
Zvvv 80,92,120
Dv 42,v1,7168
Zvvv 84,92,124
Zvvv 88,92,128
Zvvv 92,92,132
W_ 2
Nvvv 96,30,136,56
Mvvv 96,34,156
Nvvv 100,30,140,60
s_nop 1
v_max3_f32 v10, v76, v80, v84
s_nop 0
v_max3_f32 v10, v10, v88, v92
Mvvv 100,34,160
v_cmp_lt_f32 vcc, 0x42000000, v10
s_cbranch_vccnz .Ltramp1_13
Nvvv 104,30,144,64
A_ v46,v46,v76
A_ v47,v47,v80
Mvvv 104,34,164
A_ v48,v48,v84
A_ v49,v49,v88
Nvvv 108,30,148,68
A_ v50,v50,v92
Mvvv 108,34,168
Ln v8,32,s65,3328
Nvvv 112,30,152,72
Mvvv 112,34,172
W_ 0
Mvvv 112,38,192
Dv 30,%0,8192
Mvvv 112,42,212
Dv 34,v1,8192
Mvvv 108,38,188
Mvvv 108,42,208
Mvvv 104,38,184
Mvvv 104,42,204
Mvvv 100,38,180
Mvvv 100,42,200
Mvvv 96,38,176
Mvvv 96,42,196
W_ 0
Mvvv 96,30,216
Dv 38,%0,0
Mvvv 96,34,236
Dv 42,v1,0
Mvvv 100,30,220
Mvvv 100,34,240
Mvvv 104,30,224
Mvvv 104,34,244
Mvvv 108,30,228
Mvvv 108,34,248
s_nop 0
E_ 96
Mvvv 112,30,232
E_ 97
Mvvv 112,34,252
W_ 0
E_ 98
Nvvv 76,38,136,56
Dv 30,%0,1024
Mvvv 76,42,156
Dv 34,v1,1024
Nvvv 80,38,140,60
E_ 99
Mvvv 80,42,160
C_ 99,98,99
C_ 98,96,97
Nvvv 84,38,144,64
E_ 100
Mvvv 84,42,164
E_ 101
Nvvv 88,38,148,68
E_ 102
Mvvv 88,42,168
E_ 103
Nvvv 92,38,152,72
C_ 103,102,103
C_ 102,100,101
Mvvv 92,42,172
W_ 0
E_ 104
Mvvv 92,30,192
Dv 38,%0,2048
Mvvv 92,34,212
Dv 42,v1,2048
Mvvv 88,30,188
E_ 105
Mvvv 88,34,208
E_ 106
Mvvv 84,30,184
S_ s65,s61,172032
E_ 107
Mvvv 84,34,204
S_ m0,s51,0
C_ 107,106,107
C_ 106,104,105
Mvvv 80,30,180
s_mov_b64 exec, 0
s_bitcmp1_b32 s4, 1
s_cbranch_scc1 .Lhop_o_b
s_bitcmp1_b32 s4, 0
s_cbranch_scc1 .Lpad_o_1
s_branch .Lpad_o_0
.Lhop_o_b:
s_bitcmp1_b32 s4, 0
s_cbranch_scc1 .Lpad_o_3
s_branch .Lpad_o_2
.Lhop_o_r:
s_mov_b64 exec, -1
s_nop 1
s_waitcnt vmcnt(6) lgkmcnt(0)
s_barrier
Mvvv 80,34,200
L_ v9,36,s65,0
Mvvv 76,30,176
E_ 108
Mvvv 76,34,196
L_ v9,36,s65,3072
S_ m0,s51,896
Mvvv 76,38,216
Tv 30,v2,12288
Tv 32,v2,18432
Mvvv 76,42,236
L_ v9,36,s65,128
Mvvv 80,38,220
s_nop 0
Tv 34,v3,12288
Tv 36,v3,18432
Mvvv 80,42,240
L_ v9,36,s65,3200
S_ m0,s51,1792
Mvvv 84,38,224
E_ 109
Mvvv 84,42,244
L_ v9,36,s65,256
Mvvv 88,38,228
E_ 110
Mvvv 88,42,248
L_ v9,36,s65,3328
Mvvv 92,38,232
E_ 111
Mvvv 92,42,252
W_ 0
C_ 111,110,111
C_ 110,108,109
Mavv 0,30,116
s_nop 0
Tv 38,v4,12288
Tv 40,v4,18432
Mavv 20,34,116
E_ 112
Mavv 24,34,120
s_nop 0
Tv 42,v5,12288
Tv 44,v5,18432
Mavv 4,30,120
E_ 113
Mavv 8,30,124
E_ 114
Mavv 28,34,124
E_ 115
Mavv 32,34,128
C_ 115,114,115
C_ 114,112,113
Mavv 12,30,128
E_ 76
Mavv 16,30,132
E_ 77
Mavv 36,34,132
W_ 0
E_ 78
Mavv 56,38,132
s_nop 0
Tv 30,v2,13312
Tv 32,v2,19456
Mavv 76,42,132
E_ 79
Mavv 72,42,128
s_nop 0
Tv 34,v3,13312
Tv 36,v3,19456
Mavv 52,38,128
C_ 96,76,77
C_ 97,78,79
Mavv 48,38,124
E_ 80
Mavv 68,42,124
E_ 81
Mavv 64,42,120
E_ 82
Mavv 44,38,120
E_ 83
Mavv 40,38,116
C_ 100,80,81
C_ 101,82,83
Mavv 60,42,116
W_ 0
E_ 84
Mavv 80,30,116
s_nop 0
Tv 38,v4,13312
Tv 40,v4,19456
Mavv 100,34,116
E_ 85
Mavv 104,34,120
s_nop 0
Tv 42,v5,13312
Tv 44,v5,19456
Mavv 84,30,120
E_ 86
Mavv 88,30,124
E_ 87
Mavv 108,34,124
C_ 104,84,85
C_ 105,86,87
Mavv 112,34,128
E_ 88
Mavv 92,30,128
E_ 89
Mavv 96,30,132
E_ 90
Mavv 116,34,132
W_ 0
E_ 91
Mavv 136,38,132
s_nop 0
Tv 30,v2,14336
Tv 32,v2,20480
Mavv 156,42,132
C_ 108,88,89
C_ 109,90,91
Mavv 152,42,128
s_nop 0
Tv 34,v3,14336
Tv 36,v3,20480
Mavv 132,38,128
E_ 92
Mavv 128,38,124
E_ 93
Mavv 148,42,124
E_ 94
Mavv 144,42,120
E_ 95
Mavv 124,38,120
C_ 112,92,93
C_ 113,94,95
Mavv 120,38,116
v_mov_b32 v92, 0x3f803f80
v_mov_b32 v93, 0x3f803f80
Mavv 140,42,116
W_ 0
v_mov_b32 v94, 0x3f803f80
v_mov_b32 v95, 0x3f803f80
Mavv 160,30,116
s_nop 0
Tv 38,v4,14336
Tv 40,v4,20480
Mavv 180,34,116
Mavv 184,34,120
s_nop 0
Tv 42,v5,14336
Tv 44,v5,20480
Mavv 164,30,120
Mavv 168,30,124
Mavv 188,34,124
S_ s65,s61,184320
Mavv 192,34,128
S_ m0,s51,12288
Mavv 172,30,128
s_waitcnt vmcnt(6) lgkmcnt(0)
s_barrier
Mavv 176,30,132
L_ v9,36,s65,0
Mavv 196,34,132
Mavv 216,38,132
Dv 30,%0,30720
Mavv 236,42,132
Dv 34,v1,30720
Mavv 232,42,128
L_ v9,36,s65,3072
S_ m0,s51,13184
Mavv 212,38,128
Mavv 208,38,124
L_ v9,36,s65,128
Mavv 228,42,124
Mavv 224,42,120
L_ v9,36,s65,3200
S_ m0,s51,14080
Mavv 204,38,120
Mavv 200,38,116
L_ v9,36,s65,256
Mavv 220,42,116
.Lstep1_15:
Zvvv 76,92,96
Dv 38,%0,31744
Zvvv 80,92,100
Dv 42,v1,31744
Zvvv 84,92,104
Zvvv 88,92,108
Zvvv 92,92,112
W_ 2
Nvvv 116,30,136,56
Mvvv 116,34,156
Nvvv 120,30,140,60
s_nop 1
v_max3_f32 v10, v76, v80, v84
s_nop 0
v_max3_f32 v10, v10, v88, v92
Mvvv 120,34,160
v_cmp_lt_f32 vcc, 0x42000000, v10
s_cbranch_vccnz .Ltramp1_14
Nvvv 124,30,144,64
A_ v46,v46,v76
A_ v47,v47,v80
Mvvv 124,34,164
A_ v48,v48,v84
A_ v49,v49,v88
Nvvv 128,30,148,68
A_ v50,v50,v92
Mvvv 128,34,168
L_ v9,36,s65,3328
Nvvv 132,30,152,72
Mvvv 132,34,172
W_ 0
Mvvv 132,38,192
Dv 30,%0,32768
Mvvv 132,42,212
Dv 34,v1,32768
Mvvv 128,38,188
Mvvv 128,42,208
Mvvv 124,38,184
Mvvv 124,42,204
Mvvv 120,38,180
Mvvv 120,42,200
Mvvv 116,38,176
Mvvv 116,42,196
W_ 0
Mvvv 116,30,216
Dv 38,%0,24576
Mvvv 116,34,236
Dv 42,v1,24576
Mvvv 120,30,220
)A"
R"A(Mvvv 120,34,240
Mvvv 124,30,224
Mvvv 124,34,244
Mvvv 128,30,228
Mvvv 128,34,248
s_nop 0
E_ 116
Mvvv 132,30,232
E_ 117
Mvvv 132,34,252
W_ 0
E_ 118
Nvvv 76,38,136,56
Dv 30,%0,25600
Mvvv 76,42,156
Dv 34,v1,25600
Nvvv 80,38,140,60
E_ 119
Mvvv 80,42,160
C_ 119,118,119
C_ 118,116,117
Nvvv 84,38,144,64
E_ 120
Mvvv 84,42,164
E_ 121
Nvvv 88,38,148,68
E_ 122
Mvvv 88,42,168
E_ 123
Nvvv 92,38,152,72
C_ 123,122,123
C_ 122,120,121
Mvvv 92,42,172
W_ 0
E_ 124
Mvvv 92,30,192
Dv 38,%0,26624
Mvvv 92,34,212
Dv 42,v1,26624
Mvvv 88,30,188
E_ 125
Mvvv 88,34,208
E_ 126
Mvvv 84,30,184
E_ 127
Mvvv 84,34,204
C_ 127,126,127
C_ 126,124,125
Mvvv 80,30,180
s_waitcnt vmcnt(6) lgkmcnt(0)
s_barrier
Mvvv 80,34,200
R_ v7,a248
Mvvv 76,30,176
v_lshrrev_b32 v7, 7, v7
Mvvv 76,34,196
v_mul_u32_u24 v7, 896, v7
Mvvv 76,38,216
s_nop 0
Tv 30,v2,0
Tv 32,v2,6144
Mvvv 76,42,236
v_add_u32 v7, v7, v9
Mvvv 80,38,220
S_ s71,s72,1024
Tv 34,v3,0
Tv 36,v3,6144
Mvvv 80,42,240
S_ s55,s71,491520
E_ 128
Mvvv 84,38,224
S_ m0,s51,24576
E_ 129
Mvvv 84,42,244
L_ v7,24,s55,0
Mvvv 88,38,228
S_ s55,s71,501760
E_ 130
Mvvv 88,42,248
S_ m0,s51,25600
E_ 131
Mvvv 92,38,232
L_ v7,24,s55,0
Mvvv 92,42,252
S_ s55,s71,512000
C_ 131,130,131
C_ 130,128,129
W_ 2
Mavv 0,30,96
S_ m0,s51,26624
Tv 38,v4,0
Tv 40,v4,6144
W_ 2
Mavv 20,34,96
L_ v7,24,s55,0
Mavv 24,34,100
S_ s55,s71,522240
Tv 42,v5,0
Tv 44,v5,6144
Mavv 4,30,100
S_ m0,s51,27648
E_ 132
Mavv 8,30,104
L_ v7,24,s55,0
Mavv 28,34,104
S_ s55,s71,532480
E_ 133
Mavv 32,34,108
S_ m0,s51,28672
E_ 134
Mavv 12,30,108
L_ v7,24,s55,0
Mavv 16,30,112
S_ s55,s71,542720
E_ 135
Mavv 36,34,112
S_ m0,s51,29696
C_ 135,134,135
C_ 134,132,133
W_ 2
Mavv 56,38,112
Tv 30,v2,1024
Tv 32,v2,7168
W_ 2
Mavv 76,42,112
L_ v7,24,s55,0
Mavv 72,42,108
Tv 34,v3,1024
Tv 36,v3,7168
Mavv 52,38,108
E_ 76
Mavv 48,38,104
E_ 77
Mavv 68,42,104
E_ 78
Mavv 64,42,100
E_ 79
Mavv 44,38,100
C_ 116,76,77
C_ 117,78,79
Mavv 40,38,96
E_ 80
Mavv 60,42,96
W_ 0
E_ 81
Mavv 80,30,96
s_nop 0
Tv 38,v4,1024
Tv 40,v4,7168
Mavv 100,34,96
E_ 82
Mavv 104,34,100
s_nop 0
Tv 42,v5,1024
Tv 44,v5,7168
Mavv 84,30,100
E_ 83
Mavv 88,30,104
C_ 120,80,81
C_ 121,82,83
Mavv 108,34,104
E_ 84
Mavv 112,34,108
E_ 85
Mavv 92,30,108
E_ 86
Mavv 96,30,112
E_ 87
Mavv 116,34,112
W_ 0
C_ 124,84,85
C_ 125,86,87
Mavv 136,38,112
s_nop 0
Tv 30,v2,2048
Tv 32,v2,8192
Mavv 156,42,112
E_ 88
Mavv 152,42,108
s_nop 0
Tv 34,v3,2048
Tv 36,v3,8192
Mavv 132,38,108
E_ 89
Mavv 128,38,104
E_ 90
Mavv 148,42,104
E_ 91
Mavv 144,42,100
C_ 128,88,89
C_ 129,90,91
Mavv 124,38,100
E_ 92
Mavv 120,38,96
E_ 93
Mavv 140,42,96
W_ 0
E_ 94
Mavv 160,30,96
s_nop 0
Tv 38,v4,2048
Tv 40,v4,8192
Mavv 180,34,96
E_ 95
Mavv 184,34,100
s_nop 0
Tv 42,v5,2048
Tv 44,v5,8192
Mavv 164,30,100
C_ 132,92,93
C_ 133,94,95
Mavv 168,30,104
v_mov_b32 v92, 0x3f803f80
v_mov_b32 v93, 0x3f803f80
Mavv 188,34,104
v_mov_b32 v94, 0x3f803f80
v_mov_b32 v95, 0x3f803f80
Mavv 192,34,108
Mavv 172,30,108
s_waitcnt vmcnt(6) lgkmcnt(0)
s_barrier
Mavv 176,30,112
S_ s55,s71,614400
Mavv 196,34,112
S_ m0,s51,0
Mavv 216,38,112
s_nop 0
Tv 30,v2,12288
Tv 32,v2,18432
Zvvv 76,92,116
L_ v7,24,s55,0
Zvvv 80,92,120
S_ s55,s71,624640
Tv 34,v3,12288
Tv 36,v3,18432
Zvvv 84,92,124
S_ m0,s51,1024
R_ v11,a248
Zvvv 88,92,128
L_ v7,24,s55,0
Zvvv 92,92,132
S_ s55,s71,634880
v_mul_u32_u24 v11, 80, v11
Mavv 236,42,112
S_ m0,s51,2048
v_min_u32 v11, 29440, v11
Mavv 232,42,108
L_ v7,24,s55,0
Mavv 212,38,108
S_ s55,s71,645120
S_ s85,s87,1024
Mavv 208,38,104
S_ m0,s51,3072
v_max3_f32 v10, v76, v80, v84
Mavv 228,42,104
v_max3_f32 v10, v10, v88, v92
v_cmp_lt_f32 vcc, 0x42000000, v10
Mavv 224,42,100
L_ v7,24,s55,0
Mavv 204,38,100
S_ s55,s71,655360
Mavv 200,38,96
buffer_load_dword a254, v11, s[24:27], s85 offen
Mavv 220,42,96
S_ m0,s51,4096
S_ s85,s87,1152
buffer_load_dword a254, v11, s[24:27], s85 offen
s_cbranch_vccnz .Ltramp1_15
.Ljoin1:
W_ 2
Mavv 0,30,116
s_nop 0
Tv 38,v4,12288
Tv 40,v4,18432
W_ 2
Mavv 20,34,116
L_ v7,24,s55,0
Mavv 24,34,120
S_ s55,s71,665600
Tv 42,v5,12288
Tv 44,v5,18432
Mavv 4,30,120
S_ m0,s51,5120
R_ v13,a248
S_ s53,s51,36864
Mavv 8,30,124
L_ v7,24,s55,0
Mavv 28,34,124
v_add_u32 v12, s53, v13
s_mul_i32 s53, s4, 10240
Mavv 32,34,128
v_add_u32 v13, s53, v13
R_ v10,a248
Mavv 12,30,128
buffer_load_dword v11, off, s[92:95], s96 offset:0 sc1
Mavv 16,30,132
A_ v46,v46,v76
A_ v47,v47,v80
Mavv 36,34,132
W_ 0
A_ v48,v48,v84
A_ v49,v49,v88
Mavv 56,38,132
s_nop 0
Tv 30,v2,13312
Tv 32,v2,19456
Mavv 76,42,132
A_ v50,v50,v92
Mavv 72,42,128
s_nop 0
Tv 34,v3,13312
Tv 36,v3,19456
Mavv 52,38,128
v_rcp_f32 v46, v46
Mavv 48,38,124
v_rcp_f32 v47, v47
Mavv 68,42,124
v_rcp_f32 v48, v48
Mavv 64,42,120
v_rcp_f32 v49, v49
Mavv 44,38,120
v_rcp_f32 v50, v50
Mavv 40,38,116
R_ v96,a0
R_ v97,a1
Mavv 60,42,116
W_ 0
R_ v98,a2
R_ v99,a3
Mavv 80,30,116
s_nop 0
Tv 38,v4,13312
Tv 40,v4,19456
Mavv 100,34,116
R_ v100,a20
R_ v101,a21
Mavv 104,34,120
s_nop 0
Tv 42,v5,13312
Tv 44,v5,19456
Mavv 84,30,120
R_ v102,a22
R_ v103,a23
Mavv 88,30,124
U_ v96,v96,v46
U_ v97,v97,v46
Mavv 108,34,124
U_ v98,v98,v46
U_ v99,v99,v46
Mavv 112,34,128
U_ v100,v100,v46
U_ v101,v101,v46
Mavv 92,30,128
U_ v102,v102,v46
U_ v103,v103,v46
Mavv 96,30,132
C_ 136,96,97
C_ 137,98,99
Mavv 116,34,132
W_ 0
C_ 138,100,101
C_ 139,102,103
Mavv 136,38,132
s_nop 0
Tv 30,v2,14336
Tv 32,v2,20480
Mavv 156,42,132
Da 0,v13,0
Mavv 152,42,128
s_nop 0
Tv 34,v3,14336
Tv 36,v3,20480
Mavv 132,38,128
R_ v104,a4
R_ v105,a5
Mavv 128,38,124
R_ v106,a6
R_ v107,a7
Mavv 148,42,124
R_ v108,a24
R_ v109,a25
Mavv 144,42,120
R_ v110,a26
R_ v111,a27
Mavv 124,38,120
U_ v104,v104,v47
U_ v105,v105,v47
Mavv 120,38,116
U_ v106,v106,v47
U_ v107,v107,v47
Mavv 140,42,116
W_ 0
U_ v108,v108,v47
U_ v109,v109,v47
Mavv 160,30,116
s_nop 0
Tv 38,v4,14336
Tv 40,v4,20480
Mavv 180,34,116
U_ v110,v110,v47
U_ v111,v111,v47
Mavv 184,34,120
s_nop 0
Tv 42,v5,14336
Tv 44,v5,20480
Mavv 164,30,120
C_ 140,104,105
C_ 141,106,107
Mavv 168,30,124
C_ 142,108,109
C_ 143,110,111
Mavv 188,34,124
Da 4,v13,1024
Mavv 192,34,128
R_ v96,a8
R_ v97,a9
Mavv 172,30,128
S_ s55,s71,0
R_ v98,a10
R_ v99,a11
Mavv 176,30,132
S_ m0,s51,12288
R_ v100,a28
R_ v101,a29
Mavv 196,34,132
s_waitcnt vmcnt(63) lgkmcnt(0)
s_barrier
R_ v102,a30
Mavv 216,38,132
L_ v7,24,s55,0
S_ s55,s71,10240
S_ m0,s51,13312
Mavv 236,42,132
R_ v103,a31
Mavv 232,42,128
L_ v7,24,s55,0
S_ s55,s71,20480
S_ m0,s51,14336
Mavv 212,38,128
U_ v96,v96,v48
U_ v97,v97,v48
Mavv 208,38,124
L_ v7,24,s55,0
S_ s55,s71,30720
S_ m0,s51,15360
Mavv 228,42,124
U_ v98,v98,v48
U_ v99,v99,v48
Mavv 224,42,120
L_ v7,24,s55,0
S_ s55,s71,40960
S_ m0,s51,16384
Mavv 204,38,120
U_ v100,v100,v48
U_ v101,v101,v48
Mavv 200,38,116
L_ v7,24,s55,0
S_ s55,s71,51200
S_ m0,s51,17408
Mavv 220,42,116
U_ v102,v102,v48
U_ v103,v103,v48
C_ 144,96,97
C_ 145,98,99
C_ 146,100,101
C_ 147,102,103
Da 8,v13,2048
L_ v7,24,s55,0
.p2align 3
R_ v104,a12
R_ v105,a13
R_ v106,a14
R_ v107,a15
R_ v108,a32
R_ v109,a33
R_ v110,a34
R_ v111,a35
v_pk_mul_f32 v[104:105], v[104:105], v[48:49] op_sel:[0,1]
v_pk_mul_f32 v[106:107], v[106:107], v[48:49] op_sel:[0,1]
v_pk_mul_f32 v[108:109], v[108:109], v[48:49] op_sel:[0,1]
v_pk_mul_f32 v[110:111], v[110:111], v[48:49] op_sel:[0,1]
C_ 148,104,105
C_ 149,106,107
C_ 150,108,109
C_ 151,110,111
Da 12,v13,3072
.p2align 3
R_ v96,a16
R_ v97,a17
R_ v98,a18
R_ v99,a19
R_ v100,a36
R_ v101,a37
R_ v102,a38
R_ v103,a39
v_pk_mul_f32 v[96:97], v[96:97], v[50:51] op_sel_hi:[1,0]
v_pk_mul_f32 v[98:99], v[98:99], v[50:51] op_sel_hi:[1,0]
v_pk_mul_f32 v[100:101], v[100:101], v[50:51] op_sel_hi:[1,0]
v_pk_mul_f32 v[102:103], v[102:103], v[50:51] op_sel_hi:[1,0]
C_ 152,96,97
C_ 153,98,99
C_ 154,100,101
C_ 155,102,103
Da 16,v13,4096
.p2align 3
R_ v104,a40
R_ v105,a41
R_ v106,a42
R_ v107,a43
R_ v108,a60
R_ v109,a61
R_ v110,a62
R_ v111,a63
v_pk_mul_f32 v[104:105], v[104:105], v[46:47] op_sel_hi:[1,0]
v_pk_mul_f32 v[106:107], v[106:107], v[46:47] op_sel_hi:[1,0]
v_pk_mul_f32 v[108:109], v[108:109], v[46:47] op_sel_hi:[1,0]
v_pk_mul_f32 v[110:111], v[110:111], v[46:47] op_sel_hi:[1,0]
C_ 156,104,105
C_ 157,106,107
C_ 158,108,109
C_ 159,110,111
Da 40,v13,5120
.p2align 3
R_ v96,a44
R_ v97,a45
R_ v98,a46
R_ v99,a47
R_ v100,a64
R_ v101,a65
R_ v102,a66
R_ v103,a67
v_pk_mul_f32 v[96:97], v[96:97], v[46:47] op_sel:[0,1]
v_pk_mul_f32 v[98:99], v[98:99], v[46:47] op_sel:[0,1]
v_pk_mul_f32 v[100:101], v[100:101], v[46:47] op_sel:[0,1]
v_pk_mul_f32 v[102:103], v[102:103], v[46:47] op_sel:[0,1]
C_ 160,96,97
C_ 161,98,99
C_ 162,100,101
C_ 163,102,103
Da 44,v13,6144
.p2align 3
R_ v104,a48
R_ v105,a49
R_ v106,a50
R_ v107,a51
R_ v108,a68
R_ v109,a69
R_ v110,a70
R_ v111,a71
v_pk_mul_f32 v[104:105], v[104:105], v[48:49] op_sel_hi:[1,0]
v_pk_mul_f32 v[106:107], v[106:107], v[48:49] op_sel_hi:[1,0]
v_pk_mul_f32 v[108:109], v[108:109], v[48:49] op_sel_hi:[1,0]
v_pk_mul_f32 v[110:111], v[110:111], v[48:49] op_sel_hi:[1,0]
C_ 164,104,105
C_ 165,106,107
C_ 166,108,109
C_ 167,110,111
Da 48,v13,7168
.p2align 3
R_ v96,a52
R_ v97,a53
R_ v98,a54
R_ v99,a55
R_ v100,a72
R_ v101,a73
R_ v102,a74
R_ v103,a75
v_pk_mul_f32 v[96:97], v[96:97], v[48:49] op_sel:[0,1]
v_pk_mul_f32 v[98:99], v[98:99], v[48:49] op_sel:[0,1]
v_pk_mul_f32 v[100:101], v[100:101], v[48:49] op_sel:[0,1]
v_pk_mul_f32 v[102:103], v[102:103], v[48:49] op_sel:[0,1]
C_ 168,96,97
C_ 169,98,99
C_ 170,100,101
C_ 171,102,103
Da 52,v13,8192
.p2align 3
R_ v104,a56
R_ v105,a57
R_ v106,a58
R_ v107,a59
R_ v108,a76
R_ v109,a77
R_ v110,a78
R_ v111,a79
v_pk_mul_f32 v[104:105], v[104:105], v[50:51] op_sel_hi:[1,0]
v_pk_mul_f32 v[106:107], v[106:107], v[50:51] op_sel_hi:[1,0]
v_pk_mul_f32 v[108:109], v[108:109], v[50:51] op_sel_hi:[1,0]
v_pk_mul_f32 v[110:111], v[110:111], v[50:51] op_sel_hi:[1,0]
C_ 172,104,105
C_ 173,106,107
C_ 174,108,109
C_ 175,110,111
Da 56,v13,9216
.p2align 3
R_ v96,a80
R_ v97,a81
R_ v98,a82
R_ v99,a83
R_ v100,a100
R_ v101,a101
R_ v102,a102
R_ v103,a103
v_pk_mul_f32 v[96:97], v[96:97], v[46:47] op_sel_hi:[1,0]
v_pk_mul_f32 v[98:99], v[98:99], v[46:47] op_sel_hi:[1,0]
v_pk_mul_f32 v[100:101], v[100:101], v[46:47] op_sel_hi:[1,0]
v_pk_mul_f32 v[102:103], v[102:103], v[46:47] op_sel_hi:[1,0]
C_ 176,96,97
C_ 177,98,99
C_ 178,100,101
C_ 179,102,103
X_ a80,v20
X_ a81,v21
X_ a82,v22
X_ a83,v23
.p2align 3
R_ v104,a84
R_ v105,a85
R_ v106,a86
R_ v107,a87
R_ v108,a104
R_ v109,a105
R_ v110,a106
R_ v111,a107
v_pk_mul_f32 v[104:105], v[104:105], v[46:47] op_sel:[0,1]
v_pk_mul_f32 v[106:107], v[106:107], v[46:47] op_sel:[0,1]
v_pk_mul_f32 v[108:109], v[108:109], v[46:47] op_sel:[0,1]
v_pk_mul_f32 v[110:111], v[110:111], v[46:47] op_sel:[0,1]
C_ 180,104,105
C_ 181,106,107
C_ 182,108,109
C_ 183,110,111
X_ a84,v24
X_ a85,v25
X_ a86,v26
X_ a87,v27
.p2align 3
R_ v96,a88
R_ v97,a89
R_ v98,a90
R_ v99,a91
R_ v100,a108
R_ v101,a109
R_ v102,a110
R_ v103,a111
v_pk_mul_f32 v[96:97], v[96:97], v[48:49] op_sel_hi:[1,0]
v_pk_mul_f32 v[98:99], v[98:99], v[48:49] op_sel_hi:[1,0]
v_pk_mul_f32 v[100:101], v[100:101], v[48:49] op_sel_hi:[1,0]
v_pk_mul_f32 v[102:103], v[102:103], v[48:49] op_sel_hi:[1,0]
C_ 184,96,97
C_ 185,98,99
C_ 186,100,101
C_ 187,102,103
X_ a88,v52
X_ a89,v53
X_ a90,v54
X_ a91,v55
.p2align 3
R_ v104,a92
R_ v105,a93
R_ v106,a94
R_ v107,a95
R_ v108,a112
R_ v109,a113
R_ v110,a114
R_ v111,a115
v_pk_mul_f32 v[104:105], v[104:105], v[48:49] op_sel:[0,1]
v_pk_mul_f32 v[106:107], v[106:107], v[48:49] op_sel:[0,1]
v_pk_mul_f32 v[108:109], v[108:109], v[48:49] op_sel:[0,1]
v_pk_mul_f32 v[110:111], v[110:111], v[48:49] op_sel:[0,1]
C_ 188,104,105
C_ 189,106,107
C_ 190,108,109
C_ 191,110,111
.p2align 3
R_ v96,a96
R_ v97,a97
R_ v98,a98
R_ v99,a99
R_ v100,a116
R_ v101,a117
R_ v102,a118
R_ v103,a119
v_pk_mul_f32 v[96:97], v[96:97], v[50:51] op_sel_hi:[1,0]
v_pk_mul_f32 v[98:99], v[98:99], v[50:51] op_sel_hi:[1,0]
v_pk_mul_f32 v[100:101], v[100:101], v[50:51] op_sel_hi:[1,0]
v_pk_mul_f32 v[102:103], v[102:103], v[50:51] op_sel_hi:[1,0]
C_ 192,96,97
C_ 193,98,99
C_ 194,100,101
C_ 195,102,103
.p2align 3
R_ v104,a120
R_ v105,a121
R_ v106,a122
R_ v107,a123
R_ v108,a140
R_ v109,a141
R_ v110,a142
R_ v111,a143
v_pk_mul_f32 v[104:105], v[104:105], v[46:47] op_sel_hi:[1,0]
v_pk_mul_f32 v[106:107], v[106:107], v[46:47] op_sel_hi:[1,0]
v_pk_mul_f32 v[108:109], v[108:109], v[46:47] op_sel_hi:[1,0]
v_pk_mul_f32 v[110:111], v[110:111], v[46:47] op_sel_hi:[1,0]
C_ 196,104,105
C_ 197,106,107
C_ 198,108,109
C_ 199,110,111
X_ a120,v16
X_ a121,v17
X_ a122,v18
X_ a123,v19
.p2align 3
R_ v96,a124
R_ v97,a125
R_ v98,a126
R_ v99,a127
R_ v100,a144
R_ v101,a145
R_ v102,a146
R_ v103,a147
v_pk_mul_f32 v[96:97], v[96:97], v[46:47] op_sel:[0,1]
v_pk_mul_f32 v[98:99], v[98:99], v[46:47] op_sel:[0,1]
v_pk_mul_f32 v[100:101], v[100:101], v[46:47] op_sel:[0,1]
v_pk_mul_f32 v[102:103], v[102:103], v[46:47] op_sel:[0,1]
C_ 200,96,97
C_ 201,98,99
C_ 202,100,101
C_ 203,102,103
X_ a124,v28
X_ a125,v29
X_ a126,v51
X_ a127,v6
.p2align 3
R_ v104,a128
R_ v105,a129
R_ v106,a130
R_ v107,a131
R_ v108,a148
R_ v109,a149
R_ v110,a150
R_ v111,a151
v_pk_mul_f32 v[104:105], v[104:105], v[48:49] op_sel_hi:[1,0]
v_pk_mul_f32 v[106:107], v[106:107], v[48:49] op_sel_hi:[1,0]
v_pk_mul_f32 v[108:109], v[108:109], v[48:49] op_sel_hi:[1,0]
v_pk_mul_f32 v[110:111], v[110:111], v[48:49] op_sel_hi:[1,0]
C_ 204,104,105
C_ 205,106,107
C_ 206,108,109
C_ 207,110,111
X_ a128,v14
X_ a129,v15
v_accvgpr_mov_b32 a130, a249
v_accvgpr_mov_b32 a131, a250
.p2align 3
R_ v96,a132
R_ v97,a133
R_ v98,a134
R_ v99,a135
R_ v100,a152
R_ v101,a153
R_ v102,a154
R_ v103,a155
v_pk_mul_f32 v[96:97], v[96:97], v[48:49] op_sel:[0,1]
v_pk_mul_f32 v[98:99], v[98:99], v[48:49] op_sel:[0,1]
v_pk_mul_f32 v[100:101], v[100:101], v[48:49] op_sel:[0,1]
v_pk_mul_f32 v[102:103], v[102:103], v[48:49] op_sel:[0,1]
C_ 208,96,97
C_ 209,98,99
C_ 210,100,101
C_ 211,102,103
Da 132,v12,0
.p2align 3
R_ v104,a136
R_ v105,a137
R_ v106,a138
R_ v107,a139
R_ v108,a156
R_ v109,a157
R_ v110,a158
R_ v111,a159
v_pk_mul_f32 v[104:105], v[104:105], v[50:51] op_sel_hi:[1,0]
v_pk_mul_f32 v[106:107], v[106:107], v[50:51] op_sel_hi:[1,0]
v_pk_mul_f32 v[108:109], v[108:109], v[50:51] op_sel_hi:[1,0]
v_pk_mul_f32 v[110:111], v[110:111], v[50:51] op_sel_hi:[1,0]
C_ 212,104,105
C_ 213,106,107
C_ 214,108,109
C_ 215,110,111
Da 136,v12,1024
.p2align 3
R_ v96,a160
R_ v97,a161
.Lpad_o_0:
s_cbranch_execz .Lpad_o_4
R_ v98,a162
R_ v99,a163
R_ v100,a180
R_ v101,a181
R_ v102,a182
R_ v103,a183
v_pk_mul_f32 v[96:97], v[96:97], v[46:47] op_sel_hi:[1,0]
v_pk_mul_f32 v[98:99], v[98:99], v[46:47] op_sel_hi:[1,0]
v_pk_mul_f32 v[100:101], v[100:101], v[46:47] op_sel_hi:[1,0]
v_pk_mul_f32 v[102:103], v[102:103], v[46:47] op_sel_hi:[1,0]
C_ 216,96,97
C_ 217,98,99
C_ 218,100,101
C_ 219,102,103
Da 160,v12,2048
.p2align 3
R_ v104,a164
R_ v105,a165
R_ v106,a166
R_ v107,a167
R_ v108,a184
R_ v109,a185
R_ v110,a186
R_ v111,a187
v_pk_mul_f32 v[104:105], v[104:105], v[46:47] op_sel:[0,1]
v_pk_mul_f32 v[106:107], v[106:107], v[46:47] op_sel:[0,1]
v_pk_mul_f32 v[108:109], v[108:109], v[46:47] op_sel:[0,1]
v_pk_mul_f32 v[110:111], v[110:111], v[46:47] op_sel:[0,1]
C_ 220,104,105
C_ 221,106,107
C_ 222,108,109
C_ 223,110,111
Da 164,v12,3072
.p2align 3
R_ v96,a168
R_ v97,a169
R_ v98,a170
R_ v99,a171
R_ v100,a188
R_ v101,a189
R_ v102,a190
R_ v103,a191
v_pk_mul_f32 v[96:97], v[96:97], v[48:49] op_sel_hi:[1,0]
v_pk_mul_f32 v[98:99], v[98:99], v[48:49] op_sel_hi:[1,0]
v_pk_mul_f32 v[100:101], v[100:101], v[48:49] op_sel_hi:[1,0]
v_pk_mul_f32 v[102:103], v[102:103], v[48:49] op_sel_hi:[1,0]
C_ 224,96,97
C_ 225,98,99
C_ 226,100,101
C_ 227,102,103
Da 168,v12,4096
.p2align 3
R_ v104,a172
R_ v105,a173
R_ v106,a174
R_ v107,a175
R_ v108,a192
R_ v109,a193
R_ v110,a194
R_ v111,a195
v_pk_mul_f32 v[104:105], v[104:105], v[48:49] op_sel:[0,1]
v_pk_mul_f32 v[106:107], v[106:107], v[48:49] op_sel:[0,1]
v_pk_mul_f32 v[108:109], v[108:109], v[48:49] op_sel:[0,1]
v_pk_mul_f32 v[110:111], v[110:111], v[48:49] op_sel:[0,1]
C_ 228,104,105
C_ 229,106,107
C_ 230,108,109
C_ 231,110,111
Da 172,v12,5120
.p2align 3
R_ v96,a176
R_ v97,a177
R_ v98,a178
R_ v99,a179
R_ v100,a196
R_ v101,a197
R_ v102,a198
R_ v103,a199
v_pk_mul_f32 v[96:97], v[96:97], v[50:51] op_sel_hi:[1,0]
v_pk_mul_f32 v[98:99], v[98:99], v[50:51] op_sel_hi:[1,0]
v_pk_mul_f32 v[100:101], v[100:101], v[50:51] op_sel_hi:[1,0]
v_pk_mul_f32 v[102:103], v[102:103], v[50:51] op_sel_hi:[1,0]
C_ 232,96,97
C_ 233,98,99
C_ 234,100,101
C_ 235,102,103
Da 176,v12,12288
.p2align 3
R_ v104,a200
R_ v105,a201
R_ v106,a202
R_ v107,a203
R_ v108,a220
R_ v109,a221
R_ v110,a222
R_ v111,a223
v_pk_mul_f32 v[104:105], v[104:105], v[46:47] op_sel_hi:[1,0]
v_pk_mul_f32 v[106:107], v[106:107], v[46:47] op_sel_hi:[1,0]
v_pk_mul_f32 v[108:109], v[108:109], v[46:47] op_sel_hi:[1,0]
v_pk_mul_f32 v[110:111], v[110:111], v[46:47] op_sel_hi:[1,0]
C_ 236,104,105
C_ 237,106,107
C_ 238,108,109
C_ 239,110,111
Da 200,v12,13312
.p2align 3
R_ v96,a204
R_ v97,a205
R_ v98,a206
R_ v99,a207
R_ v100,a224
R_ v101,a225
R_ v102,a226
R_ v103,a227
v_pk_mul_f32 v[96:97], v[96:97], v[46:47] op_sel:[0,1]
v_pk_mul_f32 v[98:99], v[98:99], v[46:47] op_sel:[0,1]
v_pk_mul_f32 v[100:101], v[100:101], v[46:47] op_sel:[0,1]
v_pk_mul_f32 v[102:103], v[102:103], v[46:47] op_sel:[0,1]
C_ 240,96,97
C_ 241,98,99
C_ 242,100,101
C_ 243,102,103
Da 204,v12,14336
v_mov_b32 v6, v10
v_lshrrev_b32 v10, 9, v6
v_lshlrev_b32 v10, 11, v10
v_sub_u32 %0, v2, v10
v_xor_b32 %0, s83, %0
s_waitcnt vmcnt(15) lgkmcnt(0)
s_barrier
Ta 180,%0,24576
Ta 182,%0,26624
Ta 184,%0,28672
Ta 186,%0,30720
Ta 188,%0,32768
Ta 190,%0,34816
.p2align 3
R_ v104,a208
R_ v105,a209
R_ v106,a210
R_ v107,a211
R_ v108,a228
R_ v109,a229
R_ v110,a230
R_ v111,a231
v_pk_mul_f32 v[104:105], v[104:105], v[48:49] op_sel_hi:[1,0]
v_pk_mul_f32 v[106:107], v[106:107], v[48:49] op_sel_hi:[1,0]
v_pk_mul_f32 v[108:109], v[108:109], v[48:49] op_sel_hi:[1,0]
v_pk_mul_f32 v[110:111], v[110:111], v[48:49] op_sel_hi:[1,0]
C_ 244,104,105
C_ 245,106,107
C_ 246,108,109
C_ 247,110,111
Da 208,v12,15360
.p2align 3
R_ v96,a212
R_ v97,a213
R_ v98,a214
R_ v99,a215
R_ v100,a232
R_ v101,a233
R_ v102,a234
R_ v103,a235
v_pk_mul_f32 v[96:97], v[96:97], v[48:49] op_sel:[0,1]
v_pk_mul_f32 v[98:99], v[98:99], v[48:49] op_sel:[0,1]
v_pk_mul_f32 v[100:101], v[100:101], v[48:49] op_sel:[0,1]
v_pk_mul_f32 v[102:103], v[102:103], v[48:49] op_sel:[0,1]
C_ 248,96,97
C_ 249,98,99
C_ 250,100,101
C_ 251,102,103
Da 212,v12,16384
.p2align 3
R_ v104,a216
R_ v105,a217
R_ v106,a218
R_ v107,a219
R_ v108,a236
R_ v109,a237
R_ v110,a238
R_ v111,a239
v_pk_mul_f32 v[104:105], v[104:105], v[50:51] op_sel_hi:[1,0]
v_pk_mul_f32 v[106:107], v[106:107], v[50:51] op_sel_hi:[1,0]
v_pk_mul_f32 v[108:109], v[108:109], v[50:51] op_sel_hi:[1,0]
v_pk_mul_f32 v[110:111], v[110:111], v[50:51] op_sel_hi:[1,0]
C_ 252,104,105
C_ 253,106,107
C_ 254,108,109
C_ 255,110,111
Da 216,v12,17408
v_xor_b32 v1, 32, %0
W_ 7
Zvav 56,180,136
s_mul_i32 s76, s6, 204800
Ta 192,v1,24576
Ta 194,v1,26624
W_ 7
Mvav 56,184,156
v_xor_b32 v2, 64, %0
v_xor_b32 v3, 96, %0
W_ 5
Mvav 56,188,176
s_nop 0
Ta 196,v1,28672
Ta 198,v1,30720
Zvav 60,188,180
W_ 4
R_ v4,a248
v_mul_u32_u24 v4, 80, v4
Mvav 60,184,160
S_ s55,s71,122880
Ta 220,v1,32768
Ta 222,v1,34816
Mvav 60,180,140
S_ m0,s51,49152
v_min_u32 v4, 29440, v4
s_waitcnt vmcnt(6)
Zvav 64,180,144
v_readfirstlane_b32 s62, v11
v_lshrrev_b32 v8, 2, v6
Mvav 64,184,164
v_add_u32 v5, s75, v8
R_ v9,a251
Mvav 64,188,184
v_or_b32 v9, s73, v9
v_lshrrev_b32 v10, 8, v6
Zvav 68,188,188
v_bfe_u32 v12, v6, 6, 2
v_cmp_eq_u32 vcc, v10, v12
Mvav 68,184,168
v_and_b32 v10, 16, v6
v_mov_b32 v12, 0x3f80
Mvav 68,180,148
v_lshlrev_b32 v12, v10, v12
v_cndmask_b32 v12, 0, v12, vcc
Zvav 72,180,152
v_and_b32 v10, 32, v6
v_cmp_eq_u32 vcc, 0, v10
Mvav 72,184,172
X_ a68,0
X_ a69,0
Mvav 72,188,192
W_ 0
X_ a72,0
X_ a73,0
Zvav 92,220,192
s_nop 0
Ta 180,v2,24576
Ta 182,v2,26624
Mvav 92,196,172
v_cndmask_b32 v10, 0, v12, vcc
v_sub_u32 v12, v12, v10
Mvav 92,192,152
s_nop 0
Ta 184,v2,28672
Ta 186,v2,30720
Zvav 88,192,148
X_ a70,v10
X_ a71,v12
Mvav 88,196,168
s_mov_b64 s[32:33], s[20:21]
Ta 188,v2,32768
Ta 190,v2,34816
Mvav 88,220,188
s_mov_b32 s34, 409600
S_ s85,s6,1
Zvav 84,220,184
L_ v7,24,s55,0
Mvav 84,196,164
S_ s55,s71,133120
s_and_b32 s85, s85, 7
Mvav 84,192,144
S_ m0,s51,50176
s_lshl_b32 s85, s85, 7
Zvav 80,192,140
S_ s85,s85,s87
Mvav 80,196,160
L_ v7,24,s55,0
Mvav 80,220,180
S_ s55,s71,143360
Zvav 76,220,176
S_ m0,s51,51200
Mvav 76,196,156
Mvav 76,192,136
W_ 0
Zvav 16,180,136
s_nop 0
Ta 192,v3,24576
Ta 194,v3,26624
Mvav 16,184,156
L_ v7,24,s55,0
Mvav 16,188,176
S_ s55,s71,153600
Ta 196,v3,28672
Ta 198,v3,30720
Zvav 20,188,180
S_ m0,s51,52224
Mvav 20,184,160
s_nop 0
Ta 220,v3,32768
Ta 222,v3,34816
Mvav 20,180,140
L_ v7,24,s55,0
Zvav 24,180,144
S_ s55,s71,163840
Mvav 24,184,164
S_ m0,s51,53248
Mvav 24,188,184
Zvav 28,188,188
L_ v7,24,s55,0
Mvav 28,184,168
S_ s55,s71,174080
Mvav 28,180,148
S_ m0,s51,54272
Zvav 32,180,152
Mvav 32,184,172
L_ v7,24,s55,0
Mvav 32,188,192
s_waitcnt vmcnt(13) lgkmcnt(0)
s_barrier
S_ s71,s72,1152
Zvav 52,220,192
S_ s55,s71,491520
Ta 180,%0,0
Ta 182,%0,2048
Mvav 52,196,172
S_ m0,s51,36864
Mvav 52,192,152
s_nop 0
Ta 184,%0,4096
Ta 186,%0,6144
Zvav 48,192,148
L_ v7,24,s55,0
Mvav 48,196,168
S_ s55,s71,501760
Ta 188,%0,8192
Ta 190,%0,10240
Mvav 48,220,188
S_ m0,s51,37888
Zvav 44,220,184
Mvav 44,196,164
L_ v7,24,s55,0
Mvav 44,192,144
S_ s55,s71,512000
Zvav 40,192,140
S_ m0,s51,38912
Mvav 40,196,160
Mvav 40,220,180
L_ v7,24,s55,0
Zvav 36,220,176
S_ s55,s71,522240
Mvav 36,196,156
S_ m0,s51,39936
Mvav 36,192,136
W_ 0
Mvav 56,180,196
s_nop 0
Ta 192,v1,0
Ta 194,v1,2048
Mvav 56,184,216
L_ v7,24,s55,0
Mvav 56,188,236
S_ s55,s71,532480
Ta 196,v1,4096
Ta 198,v1,6144
Mvav 60,188,240
S_ m0,s51,40960
Mvav 60,184,220
s_nop 0
Ta 220,v1,8192
Ta 222,v1,10240
Mvav 60,180,200
L_ v7,24,s55,0
Mvav 64,180,204
S_ s55,s71,542720
Mvav 64,184,224
S_ m0,s51,41984
Mvav 64,188,244
Mvav 68,188,248
L_ v7,24,s55,0
Mvav 68,184,228
S_ s55,s71,614400
Mvav 68,180,208
S_ m0,s51,24576
Mvav 72,180,212
Mvav 72,184,232
L_ v7,24,s55,0
Mvav 72,188,252
W_ 0
S_ s55,s71,624640
Mvav 92,220,252
S_ m0,s51,25600
Ta 180,v2,0
Ta 182,v2,2048
Mvav 92,196,232
Mvav 92,192,212
s_nop 0
Ta 184,v2,4096
Ta 186,v2,6144
Mvav 88,192,208
L_ v7,24,s55,0
Mvav 88,196,228
S_ s55,s71,634880
Ta 188,v2,8192
Ta 190,v2,10240
Mvav 88,220,248
S_ m0,s51,26624
Mvav 84,220,244
Mvav 84,196,224
L_ v7,24,s55,0
Mvav 84,192,204
S_ s55,s71,645120
Mvav 80,192,200
S_ m0,s51,27648
Mvav 80,196,220
Mvav 80,220,240
L_ v7,24,s55,0
Mvav 76,220,236
S_ s55,s71,655360
Mvav 76,196,216
S_ m0,s51,28672
Mvav 76,192,196
W_ 0
Mvav 16,180,196
s_nop 0
Ta 192,v3,0
Ta 194,v3,2048
Mvav 16,184,216
L_ v7,24,s55,0
Mvav 16,188,236
S_ s55,s71,665600
Ta 196,v3,4096
Ta 198,v3,6144
Mvav 20,188,240
S_ m0,s51,29696
Mvav 20,184,220
s_nop 0
Ta 220,v3,8192
Ta 222,v3,10240
Mvav 20,180,200
L_ v7,24,s55,0
Mvav 24,180,204
Mvav 24,184,224
Mvav 24,188,244
Mvav 28,188,248
buffer_load_dword a74, v4, s[24:27], s85 offen
Mvav 28,184,228
S_ s85,s6,2
Mvav 28,180,208
s_and_b32 s85, s85, 7
Mvav 32,180,212
s_lshl_b32 s85, s85, 7
Mvav 32,184,232
S_ s85,s85,s87
Mvav 32,188,252
s_waitcnt vmcnt(19) lgkmcnt(0)
s_barrier
Mvav 52,220,252
S_ s55,s71,0
Ta 180,%0,12288
Ta 182,%0,14336
Mvav 52,196,232
buffer_load_dword a74, v4, s[24:27], s85 offen
Mvav 52,192,212
S_ m0,s51,0
Ta 184,%0,16384
Ta 186,%0,18432
Mvav 48,192,208
Mvav 48,196,228
s_nop 0
Ta 188,%0,20480
Ta 190,%0,22528
Mvav 48,220,248
L_ v7,24,s55,0
Mvav 44,220,244
S_ s55,s71,10240
Mvav 44,196,224
S_ m0,s51,1024
Mvav 44,192,204
Mvav 40,192,200
L_ v7,24,s55,0
Mvav 40,196,220
S_ s55,s71,20480
Mvav 40,220,240
S_ m0,s51,2048
Mvav 36,220,236
Mvav 36,196,216
L_ v7,24,s55,0
Mvav 36,192,196
W_ 0
S_ s55,s71,30720
Mvaa 56,180,0
S_ m0,s51,3072
Ta 192,v1,12288
Ta 194,v1,14336
Mvaa 56,184,40
Mvaa 56,188,80
s_nop 0
Ta 196,v1,16384
Ta 198,v1,18432
Mvaa 60,188,84
L_ v7,24,s55,0
Mvaa 60,184,44
S_ s55,s71,40960
Ta 220,v1,20480
Ta 222,v1,22528
Mvaa 60,180,4
S_ m0,s51,4096
Mvaa 64,180,8
Mvaa 64,184,48
L_ v7,24,s55,0
Mvaa 64,188,88
S_ s55,s71,51200
Mvaa 68,188,240
S_ m0,s51,5120
Mvaa 68,184,52
Mvaa 68,180,12
L_ v7,24,s55,0
Mvaa 72,180,16
Mvaa 72,184,56
Mvaa 72,188,244
W_ 0
Mvaa 92,220,244
s_nop 0
Ta 180,v2,12288
Ta 182,v2,14336
Mvaa 92,196,56
Mvaa 92,192,16
s_nop 0
Ta 184,v2,16384
Ta 186,v2,18432
Mvaa 88,192,12
Mvaa 88,196,52
s_nop 0
Ta 188,v2,20480
Ta 190,v2,22528
Mvaa 88,220,240
Mvaa 84,220,88
Mvaa 84,196,48
Mvaa 84,192,8
Mvaa 80,192,4
Mvaa 80,196,44
Mvaa 80,220,84
Mvaa 76,220,80
Mvaa 76,196,40
.Lpad_o_1:
s_cbranch_execz .Lpad_o_5
Mvaa 76,192,0
W_ 0
Mvaa 16,180,0
s_nop 0
Ta 192,v3,12288
Ta 194,v3,14336
Mvaa 16,184,40
Mvaa 16,188,80
s_nop 0
Ta 196,v3,16384
Ta 198,v3,18432
Mvaa 20,188,84
Mvaa 20,184,44
s_nop 0
Ta 220,v3,20480
Ta 222,v3,22528
Mvaa 20,180,4
Mvaa 24,180,8
Mvaa 24,184,48
Mvaa 24,188,88
Mvaa 28,188,240
Mvaa 28,184,52
Mvaa 28,180,12
Mvaa 32,180,16
Mvaa 32,184,56
Mvaa 32,188,244
s_waitcnt vmcnt(20) lgkmcnt(0)
s_barrier
Mvaa 52,220,244
s_nop 0
Ta 180,%0,49152
Ta 182,%0,51200
Mvaa 52,196,56
Mvaa 52,192,16
s_nop 0
Ta 184,%0,53248
Ta 186,%0,55296
Mvaa 48,192,12
Mvaa 48,196,52
s_nop 0
Ta 188,%0,57344
Ta 190,%0,59392
Mvaa 48,220,240
Mvaa 44,220,88
Mvaa 44,196,48
Mvaa 44,192,8
Mvaa 40,192,4
Mvaa 40,196,44
Mvaa 40,220,84
Mvaa 36,220,80
Mvaa 36,196,40
Mvaa 36,192,0
W_ 0
Mvaa 56,180,120
S_ s73,s78,163840
Ta 192,v1,49152
Ta 194,v1,51200
Mvaa 56,184,160
S_ s73,s73,4096
Mvaa 56,188,200
s_nop 0
Ta 196,v1,53248
Ta 198,v1,55296
Mvaa 60,188,204
s_mov_b32 m0, s52
Mvaa 60,184,164
s_nop 0
Ta 220,v1,57344
Ta 222,v1,59392
Mvaa 60,180,124
s_nop 0
Y_ 56,0
Y_ 57,256
Mvaa 64,180,128
s_nop 0
Y_ 58,512
Y_ 59,768
Mvaa 64,184,168
Mvaa 64,188,208
s_nop 0
Y_ 60,1024
Y_ 61,1280
Mvaa 68,188,212
s_nop 0
Y_ 62,1536
Y_ 63,1792
Mvaa 68,184,172
Mvaa 68,180,132
s_nop 0
Y_ 64,2048
Y_ 65,2304
Mvaa 72,180,136
s_nop 0
Y_ 66,2560
Y_ 67,2816
Mvaa 72,184,176
Mvaa 72,188,216
W_ 12
Y_ 68,3072
Y_ 69,3328
Mvaa 92,220,216
s_nop 0
Ta 180,v2,49152
Ta 182,v2,51200
Mvaa 92,196,176
s_nop 0
Y_ 70,3584
Y_ 71,3840
Mvaa 92,192,136
s_nop 0
Ta 184,v2,53248
Ta 186,v2,55296
Mvaa 88,192,132
s_nop 0
Y_ 72,4096
Y_ 73,4352
Mvaa 88,196,172
s_nop 0
Ta 188,v2,57344
Ta 190,v2,59392
Mvaa 88,220,212
s_nop 0
Y_ 74,4608
Y_ 75,4864
Mvaa 84,220,208
s_nop 0
Y_ 92,9216
Y_ 93,9472
Mvaa 84,196,168
s_nop 0
Y_ 94,9728
Y_ 95,9984
Mvaa 84,192,128
s_nop 0
Y_ 88,8192
Y_ 89,8448
Mvaa 80,192,124
s_nop 0
Y_ 90,8704
Y_ 91,8960
Mvaa 80,196,164
Mvaa 80,220,204
s_nop 0
Y_ 84,7168
Y_ 85,7424
Mvaa 76,220,200
s_nop 0
Y_ 86,7680
Y_ 87,7936
Mvaa 76,196,160
Mvaa 76,192,120
W_ 14
Y_ 80,6144
Y_ 81,6400
Mvaa 16,180,120
s_nop 0
Ta 192,v3,49152
Ta 194,v3,51200
Mvaa 16,184,160
s_nop 0
Y_ 82,6656
Y_ 83,6912
Mvaa 16,188,200
s_nop 0
Ta 196,v3,53248
Ta 198,v3,55296
Mvaa 20,188,204
s_nop 0
Y_ 76,5120
Y_ 77,5376
Mvaa 20,184,164
s_nop 0
Ta 220,v3,57344
Ta 222,v3,59392
Mvaa 20,180,124
s_nop 0
Y_ 78,5632
Y_ 79,5888
Mvaa 24,180,128
Mvaa 24,184,168
Mvaa 24,188,208
s_waitcnt vmcnt(14) lgkmcnt(0)
s_barrier
Mvaa 28,188,212
S_ s55,s71,122880
ds_read2st64_b32 v[96:97], v5 offset0:0 offset1:1
Mvaa 28,184,172
S_ m0,s51,12288
ds_read2st64_b32 v[98:99], v5 offset0:2 offset1:3
Mvaa 28,180,132
L_ v7,24,s55,0
Mvaa 32,180,136
S_ s55,s71,133120
ds_read2st64_b32 v[104:105], v5 offset0:4 offset1:5
Mvaa 32,184,176
S_ m0,s51,13312
ds_read2st64_b32 v[106:107], v5 offset0:6 offset1:7
Mvaa 32,188,216
ds_read2st64_b32 v[112:113], v5 offset0:8 offset1:9
Mvaa 52,220,216
s_nop 0
Ta 180,%0,36864
Ta 182,%0,38912
Mvaa 52,196,176
L_ v7,24,s55,0
Mvaa 52,192,136
W_ 5
Ta 184,%0,40960
Ta 186,%0,43008
Mvaa 48,192,132
S_ s55,s71,143360
ds_read2st64_b32 v[114:115], v5 offset0:10 offset1:11
A_ v96,v16,v96
Mvaa 48,196,172
S_ m0,s51,14336
Ta 188,%0,45056
Ta 190,%0,47104
Mvaa 48,220,212
L_ v7,24,s55,0
Mvaa 44,220,208
S_ s55,s71,153600
ds_read2st64_b32 v[120:121], v5 offset0:12 offset1:13
A_ v97,v17,v97
Mvaa 44,196,168
S_ m0,s51,15360
ds_read2st64_b32 v[122:123], v5 offset0:14 offset1:15
A_ v98,v18,v98
Mvaa 44,192,128
ds_read2st64_b32 v[128:129], v5 offset0:16 offset1:17
A_ v99,v19,v99
Mvaa 40,192,124
L_ v7,24,s55,0
Mvaa 40,196,164
W_ 11
ds_read2st64_b32 v[130:131], v5 offset0:18 offset1:19
A_ v104,v20,v104
Mvaa 40,220,204
S_ s55,s71,163840
ds_read2st64_b32 v[132:133], v5 offset0:36 offset1:37
A_ v105,v21,v105
Mvaa 36,220,200
S_ m0,s51,16384
ds_read2st64_b32 v[134:135], v5 offset0:38 offset1:39
A_ v106,v22,v106
Mvaa 36,196,160
L_ v7,24,s55,0
Mvaa 36,192,120
W_ 6
ds_read2st64_b32 v[124:125], v5 offset0:32 offset1:33
A_ v107,v23,v107
Zvav 56,180,136
Ta 192,v1,36864
Ta 194,v1,38912
Mvav 56,184,156
S_ s55,s71,174080
ds_read2st64_b32 v[126:127], v5 offset0:34 offset1:35
A_ v112,v24,v112
Mvav 56,188,176
S_ m0,s51,17408
Ta 196,v1,40960
Ta 198,v1,43008
Zvav 60,188,180
L_ v7,24,s55,0
Mvav 60,184,160
S_ s79,s6,1
Ta 220,v1,45056
Ta 222,v1,47104
Mvav 60,180,140
s_and_b32 s79, s79, 7
ds_read2st64_b32 v[116:117], v5 offset0:28 offset1:29
A_ v113,v25,v113
Zvav 64,180,144
s_lshl_b32 s79, s79, 7
ds_read2st64_b32 v[118:119], v5 offset0:30 offset1:31
A_ v114,v26,v114
Mvav 64,184,164
S_ s71,s72,s79
ds_read2st64_b32 v[108:109], v5 offset0:24 offset1:25
A_ v115,v27,v115
Mvav 64,188,184
W_ 14
ds_read2st64_b32 v[110:111], v5 offset0:26 offset1:27
A_ v120,v28,v120
Zvav 68,188,188
S_ s55,s71,491520
ds_read2st64_b32 v[100:101], v5 offset0:20 offset1:21
A_ v121,v29,v121
Mvav 68,184,168
S_ m0,s51,49152
ds_read2st64_b32 v[102:103], v5 offset0:22 offset1:23
A_ v122,v30,v122
Mvav 68,180,148
L_ v7,24,s55,0
Zvav 72,180,152
S_ s55,s71,501760
A_ v123,v31,v123
Mvav 72,184,172
W_ 14
A_ v128,v32,v128
A_ v129,v33,v129
Mvav 72,188,192
W_ 6
A_ v130,v34,v130
A_ v131,v35,v131
Zvav 92,220,192
Ta 180,v2,36864
Ta 182,v2,38912
Mvav 92,196,172
S_ m0,s51,50176
A_ v132,v52,v132
A_ v133,v53,v133
Mvav 92,192,152
s_nop 0
Ta 184,v2,40960
Ta 186,v2,43008
Zvav 88,192,148
L_ v7,24,s55,0
Mvav 88,196,168
S_ s55,s71,512000
Ta 188,v2,45056
Ta 190,v2,47104
Mvav 88,220,188
S_ m0,s51,51200
A_ v134,v54,v134
A_ v135,v55,v135
Zvav 84,220,184
A_ v124,v48,v124
A_ v125,v49,v125
Mvav 84,196,164
L_ v7,24,s55,0
Mvav 84,192,144
S_ s55,s71,522240
A_ v126,v50,v126
A_ v127,v51,v127
Zvav 80,192,140
W_ 10
A_ v116,v44,v116
A_ v117,v45,v117
Mvav 80,196,160
S_ m0,s51,52224
A_ v118,v46,v118
A_ v119,v47,v119
Mvav 80,220,180
L_ v7,24,s55,0
Zvav 76,220,176
W_ 8
A_ v108,v40,v108
A_ v109,v41,v109
Mvav 76,196,156
S_ s55,s71,532480
A_ v110,v42,v110
A_ v111,v43,v111
Mvav 76,192,136
W_ 0
A_ v100,v36,v100
Zvav 16,180,136
S_ m0,s51,53248
Ta 192,v3,36864
Ta 194,v3,38912
Mvav 16,184,156
L_ v7,24,s55,0
Mvav 16,188,176
S_ s55,s71,542720
Ta 196,v3,40960
Ta 198,v3,43008
Zvav 20,188,180
S_ m0,s51,54272
A_ v101,v37,v101
A_ v102,v38,v102
Mvav 20,184,160
S_ s85,s6,3
Ta 220,v3,45056
Ta 222,v3,47104
Mvav 20,180,140
L_ v7,24,s55,0
Zvav 24,180,144
s_and_b32 s85, s85, 7
A_ v103,v39,v103
C_ 131,130,131
Mvav 24,184,164
s_lshl_b32 s85, s85, 7
C_ 130,128,129
C_ 132,132,133
Mvav 24,188,184
S_ s85,s85,s87
C_ 133,134,135
Zvav 28,188,188
buffer_store_dwordx4 v[130:133], v6, s[28:31], s73 offen offset:0 sc1
Mvav 28,184,168
s_sub_u32 s73, s73, 4096
Mvav 28,180,148
C_ 123,122,123
C_ 122,120,121
Zvav 32,180,152
C_ 124,124,125
C_ 125,126,127
Mvav 32,184,172
Mvav 32,188,192
s_waitcnt vmcnt(21) lgkmcnt(0)
s_barrier
Zvav 52,220,192
S_ s55,s71,614400
Ta 180,%0,24576
Ta 182,%0,26624
Mvav 52,196,172
S_ m0,s51,36864
Mvav 52,192,152
s_nop 0
Ta 184,%0,28672
Ta 186,%0,30720
Zvav 48,192,148
buffer_store_dwordx4 v[122:125], v6, s[28:31], s73 offen offset:3072 sc1
Mvav 48,196,168
s_nop 0
Ta 188,%0,32768
Ta 190,%0,34816
Mvav 48,220,188
C_ 115,114,115
C_ 114,112,113
Zvav 44,220,184
C_ 116,116,117
C_ 117,118,119
Mvav 44,196,164
Mvav 44,192,144
Zvav 40,192,140
Mvav 40,196,160
Mvav 40,220,180
buffer_store_dwordx4 v[114:117], v6, s[28:31], s73 offen offset:2048 sc1
Zvav 36,220,176
C_ 107,106,107
C_ 106,104,105
Mvav 36,196,156
C_ 108,108,109
C_ 109,110,111
Mvav 36,192,136
W_ 0
Mvav 56,180,196
s_nop 0
Ta 192,v1,24576
Ta 194,v1,26624
Mvav 56,184,216
Mvav 56,188,236
s_nop 0
Ta 196,v1,28672
Ta 198,v1,30720
Mvav 60,188,240
Mvav 60,184,220
s_nop 0
Ta 220,v1,32768
Ta 222,v1,34816
Mvav 60,180,200
buffer_store_dwordx4 v[106:109], v6, s[28:31], s73 offen offset:1024 sc1
Mvav 64,180,204
C_ 99,98,99
C_ 98,96,97
Mvav 64,184,224
C_ 100,100,101
C_ 101,102,103
Mvav 64,188,244
Mvav 68,188,248
Mvav 68,184,228
Mvav 68,180,208
Mvav 72,180,212
Mvav 72,184,232
buffer_store_dwordx4 v[98:101], v6, s[28:31], s73 offen offset:0 sc1
Mvav 72,188,252
W_ 0
Mvav 92,220,252
s_nop 0
Ta 180,v2,24576
Ta 182,v2,26624
Mvav 92,196,232
Mvav 92,192,212
s_nop 0
Ta 184,v2,28672
Ta 186,v2,30720
Mvav 88,192,208
Mvav 88,196,228
s_nop 0
Ta 188,v2,32768
Ta 190,v2,34816
Mvav 88,220,248
Mvav 84,220,244
L_ v7,24,s55,0
Mvav 84,196,224
S_ s55,s71,624640
Mvav 84,192,204
S_ m0,s51,37888
Mvav 80,192,200
Mvav 80,196,220
L_ v7,24,s55,0
Mvav 80,220,240
S_ s55,s71,634880
Mvav 76,220,236
S_ m0,s51,38912
Mvav 76,196,216
Mvav 76,192,196
W_ 0
Mvav 16,180,196
s_nop 0
Ta 192,v3,24576
Ta 194,v3,26624
Mvav 16,184,216
L_ v7,24,s55,0
Mvav 16,188,236
S_ s55,s71,645120
Ta 196,v3,28672
Ta 198,v3,30720
Mvav 20,188,240
S_ m0,s51,39936
Mvav 20,184,220
s_nop 0
Ta 220,v3,32768
Ta 222,v3,34816
Mvav 20,180,200
L_ v7,24,s55,0
Mvav 24,180,204
S_ s55,s71,655360
Mvav 24,184,224
S_ m0,s51,40960
Mvav 24,188,244
Mvav 28,188,248
L_ v7,24,s55,0
Mvav 28,184,228
S_ s55,s71,665600
Mvav 28,180,208
S_ m0,s51,41984
Mvav 32,180,212
Mvav 32,184,232
L_ v7,24,s55,0
Mvav 32,188,252
s_waitcnt vmcnt(23) lgkmcnt(0)
s_barrier
Mvav 52,220,252
S_ s55,s71,0
Ta 180,%0,0
Ta 182,%0,2048
Mvav 52,196,232
S_ m0,s51,24576
Mvav 52,192,212
s_nop 0
Ta 184,%0,4096
Ta 186,%0,6144
Mvav 48,192,208
L_ v7,24,s55,0
Mvav 48,196,228
S_ s55,s71,10240
Ta 188,%0,8192
Ta 190,%0,10240
Mvav 48,220,248
S_ m0,s51,25600
Mvav 44,220,244
Mvav 44,196,224
L_ v7,24,s55,0
Mvav 44,192,204
S_ s55,s71,20480
Mvav 40,192,200
S_ m0,s51,26624
Mvav 40,196,220
Mvav 40,220,240
L_ v7,24,s55,0
Mvav 36,220,236
S_ s55,s71,30720
Mvav 36,196,216
S_ m0,s51,27648
Mvav 36,192,196
W_ 0
Mvaa 56,180,0
s_nop 0
Ta 192,v1,0
Ta 194,v1,2048
Mvaa 56,184,40
L_ v7,24,s55,0
Mvaa 56,188,80
S_ s55,s71,40960
Ta 196,v1,4096
Ta 198,v1,6144
Mvaa 60,188,84
S_ m0,s51,28672
.Lpad_o_2:
s_cbranch_execz .Lpad_o_6
Mvaa 60,184,44
s_nop 0
Ta 220,v1,8192
Ta 222,v1,10240
Mvaa 60,180,4
L_ v7,24,s55,0
Mvaa 64,180,8
S_ s55,s71,51200
Mvaa 64,184,48
S_ m0,s51,29696
Mvaa 64,188,88
Mvaa 68,188,240
L_ v7,24,s55,0
Mvaa 68,184,52
Mvaa 68,180,12
Mvaa 72,180,16
Mvaa 72,184,56
buffer_load_dword a74, v4, s[24:27], s85 offen
Mvaa 72,188,244
W_ 0
Mvaa 92,220,244
s_nop 0
Ta 180,v2,0
Ta 182,v2,2048
Mvaa 92,196,56
Mvaa 92,192,16
s_nop 0
Ta 184,v2,4096
Ta 186,v2,6144
Mvaa 88,192,12
Mvaa 88,196,52
s_nop 0
Ta 188,v2,8192
Ta 190,v2,10240
Mvaa 88,220,240
Mvaa 84,220,88
Mvaa 84,196,48
Mvaa 84,192,8
Mvaa 80,192,4
Mvaa 80,196,44
Mvaa 80,220,84
Mvaa 76,220,80
Mvaa 76,196,40
X_ a20,-1
X_ a21,-1
Mvaa 76,192,0
W_ 0
X_ a22,-1
X_ a23,-1
Mvaa 16,180,0
S_ s81,s6,7
Ta 192,v3,0
Ta 194,v3,2048
Mvaa 16,184,40
s_and_b32 s81, s81, 7
Mvaa 16,188,80
s_mul_i32 s81, s81, 20480
Ta 196,v3,4096
Ta 198,v3,6144
Mvaa 20,188,84
S_ s81,s81,s78
Mvaa 20,184,44
s_nop 0
Ta 220,v3,8192
Ta 222,v3,10240
Mvaa 20,180,4
buffer_store_dwordx4 a[20:23], v6, s[28:31], s81 offen offset:0 sc1
Mvaa 24,180,8
Mvaa 24,184,48
Mvaa 24,188,88
Mvaa 28,188,240
buffer_store_dwordx4 a[20:23], v6, s[28:31], s81 offen offset:1024 sc1
Mvaa 28,184,52
Mvaa 28,180,12
Mvaa 32,180,16
Mvaa 32,184,56
Mvaa 32,188,244
)A"
R"A(s_waitcnt vmcnt(26) lgkmcnt(0)
s_barrier
Mvaa 52,220,244
s_nop 0
Ta 180,%0,12288
Ta 182,%0,14336
Mvaa 52,196,56
buffer_store_dwordx4 a[20:23], v6, s[28:31], s81 offen offset:2048 sc1
Mvaa 52,192,16
s_nop 0
Ta 184,%0,16384
Ta 186,%0,18432
Mvaa 48,192,12
Mvaa 48,196,52
s_nop 0
Ta 188,%0,20480
Ta 190,%0,22528
Mvaa 48,220,240
Mvaa 44,220,88
Mvaa 44,196,48
buffer_store_dwordx4 a[20:23], v6, s[28:31], s81 offen offset:3072 sc1
Mvaa 44,192,8
S_ s81,s81,4096
Mvaa 40,192,4
Mvaa 40,196,44
Mvaa 40,220,84
Mvaa 36,220,80
Mvaa 36,196,40
Mvaa 36,192,0
W_ 0
Mvaa 56,180,120
s_nop 0
Ta 192,v1,12288
Ta 194,v1,14336
Mvaa 56,184,160
buffer_store_dwordx4 a[20:23], v6, s[28:31], s81 offen offset:0 sc1
Mvaa 56,188,200
S_ s73,s78,184320
Ta 196,v1,16384
Ta 198,v1,18432
Mvaa 60,188,204
s_mov_b32 m0, s52
S_ s73,s73,4096
Mvaa 60,184,164
s_nop 0
Ta 220,v1,20480
Ta 222,v1,22528
Mvaa 60,180,124
s_nop 0
Y_ 56,0
Y_ 57,256
Mvaa 64,180,128
s_nop 0
Y_ 58,512
Y_ 59,768
Mvaa 64,184,168
Mvaa 64,188,208
s_nop 0
Y_ 60,1024
Y_ 61,1280
Mvaa 68,188,212
s_nop 0
Y_ 62,1536
Y_ 63,1792
Mvaa 68,184,172
Mvaa 68,180,132
s_nop 0
Y_ 64,2048
Y_ 65,2304
Mvaa 72,180,136
s_nop 0
Y_ 66,2560
Y_ 67,2816
Mvaa 72,184,176
Mvaa 72,188,216
W_ 12
Y_ 68,3072
Y_ 69,3328
Mvaa 92,220,216
s_nop 0
Ta 180,v2,12288
Ta 182,v2,14336
Mvaa 92,196,176
s_nop 0
Y_ 70,3584
Y_ 71,3840
Mvaa 92,192,136
s_nop 0
Ta 184,v2,16384
Ta 186,v2,18432
Mvaa 88,192,132
s_nop 0
Y_ 72,4096
Y_ 73,4352
Mvaa 88,196,172
s_nop 0
Ta 188,v2,20480
Ta 190,v2,22528
Mvaa 88,220,212
s_nop 0
Y_ 74,4608
Y_ 75,4864
Mvaa 84,220,208
s_nop 0
Y_ 92,9216
Y_ 93,9472
Mvaa 84,196,168
s_nop 0
Y_ 94,9728
Y_ 95,9984
Mvaa 84,192,128
s_nop 0
Y_ 88,8192
Y_ 89,8448
Mvaa 80,192,124
s_nop 0
Y_ 90,8704
Y_ 91,8960
Mvaa 80,196,164
Mvaa 80,220,204
s_nop 0
Y_ 84,7168
Y_ 85,7424
Mvaa 76,220,200
s_nop 0
Y_ 86,7680
Y_ 87,7936
Mvaa 76,196,160
Mvaa 76,192,120
W_ 14
Y_ 80,6144
Y_ 81,6400
Mvaa 16,180,120
s_nop 0
Ta 192,v3,12288
Ta 194,v3,14336
Mvaa 16,184,160
s_nop 0
Y_ 82,6656
Y_ 83,6912
Mvaa 16,188,200
s_nop 0
Ta 196,v3,16384
Ta 198,v3,18432
Mvaa 20,188,204
s_nop 0
Y_ 76,5120
Y_ 77,5376
Mvaa 20,184,164
s_nop 0
Ta 220,v3,20480
Ta 222,v3,22528
Mvaa 20,180,124
s_nop 0
Y_ 78,5632
Y_ 79,5888
Mvaa 24,180,128
Mvaa 24,184,168
Mvaa 24,188,208
s_waitcnt vmcnt(23) lgkmcnt(0)
s_barrier
Mvaa 28,188,212
S_ s55,s71,122880
ds_read2st64_b32 v[96:97], v5 offset0:0 offset1:1
Mvaa 28,184,172
S_ m0,s51,0
ds_read2st64_b32 v[98:99], v5 offset0:2 offset1:3
Mvaa 28,180,132
L_ v7,24,s55,0
Mvaa 32,180,136
S_ s55,s71,133120
ds_read2st64_b32 v[104:105], v5 offset0:4 offset1:5
Mvaa 32,184,176
S_ m0,s51,1024
ds_read2st64_b32 v[106:107], v5 offset0:6 offset1:7
Mvaa 32,188,216
ds_read2st64_b32 v[112:113], v5 offset0:8 offset1:9
Mvaa 52,220,216
s_nop 0
Ta 180,%0,49152
Ta 182,%0,51200
Mvaa 52,196,176
L_ v7,24,s55,0
Mvaa 52,192,136
W_ 5
Ta 184,%0,53248
Ta 186,%0,55296
Mvaa 48,192,132
S_ s55,s71,143360
ds_read2st64_b32 v[114:115], v5 offset0:10 offset1:11
A_ v96,v16,v96
Mvaa 48,196,172
S_ m0,s51,2048
Ta 188,%0,57344
Ta 190,%0,59392
Mvaa 48,220,212
L_ v7,24,s55,0
Mvaa 44,220,208
S_ s55,s71,153600
ds_read2st64_b32 v[120:121], v5 offset0:12 offset1:13
A_ v97,v17,v97
Mvaa 44,196,168
S_ m0,s51,3072
ds_read2st64_b32 v[122:123], v5 offset0:14 offset1:15
A_ v98,v18,v98
Mvaa 44,192,128
ds_read2st64_b32 v[128:129], v5 offset0:16 offset1:17
A_ v99,v19,v99
Mvaa 40,192,124
L_ v7,24,s55,0
Mvaa 40,196,164
W_ 11
ds_read2st64_b32 v[130:131], v5 offset0:18 offset1:19
A_ v104,v20,v104
Mvaa 40,220,204
S_ s55,s71,163840
ds_read2st64_b32 v[132:133], v5 offset0:36 offset1:37
A_ v105,v21,v105
Mvaa 36,220,200
S_ m0,s51,4096
ds_read2st64_b32 v[134:135], v5 offset0:38 offset1:39
A_ v106,v22,v106
Mvaa 36,196,160
L_ v7,24,s55,0
Mvaa 36,192,120
W_ 6
ds_read2st64_b32 v[124:125], v5 offset0:32 offset1:33
A_ v107,v23,v107
Zvav 56,180,136
Ta 192,v1,49152
Ta 194,v1,51200
Mvav 56,184,156
S_ s55,s71,174080
ds_read2st64_b32 v[126:127], v5 offset0:34 offset1:35
A_ v112,v24,v112
Mvav 56,188,176
S_ m0,s51,5120
Ta 196,v1,53248
Ta 198,v1,55296
Zvav 60,188,180
L_ v7,24,s55,0
Mvav 60,184,160
S_ s79,s6,2
Ta 220,v1,57344
Ta 222,v1,59392
Mvav 60,180,140
s_and_b32 s79, s79, 7
ds_read2st64_b32 v[116:117], v5 offset0:28 offset1:29
A_ v113,v25,v113
Zvav 64,180,144
s_lshl_b32 s79, s79, 7
ds_read2st64_b32 v[118:119], v5 offset0:30 offset1:31
A_ v114,v26,v114
Mvav 64,184,164
S_ s71,s72,s79
ds_read2st64_b32 v[108:109], v5 offset0:24 offset1:25
A_ v115,v27,v115
Mvav 64,188,184
W_ 14
ds_read2st64_b32 v[110:111], v5 offset0:26 offset1:27
A_ v120,v28,v120
Zvav 68,188,188
S_ s55,s71,491520
ds_read2st64_b32 v[100:101], v5 offset0:20 offset1:21
A_ v121,v29,v121
Mvav 68,184,168
S_ m0,s51,12288
ds_read2st64_b32 v[102:103], v5 offset0:22 offset1:23
A_ v122,v30,v122
Mvav 68,180,148
L_ v7,24,s55,0
Zvav 72,180,152
S_ s55,s71,501760
A_ v123,v31,v123
R_ v12,a248
Mvav 72,184,172
W_ 14
A_ v128,v32,v128
A_ v129,v33,v129
Mvav 72,188,192
W_ 6
A_ v130,v34,v130
A_ v131,v35,v131
Zvav 92,220,192
Ta 180,v2,49152
Ta 182,v2,51200
Mvav 92,196,172
S_ m0,s51,13312
A_ v132,v52,v132
A_ v133,v53,v133
Mvav 92,192,152
s_nop 0
Ta 184,v2,53248
Ta 186,v2,55296
Zvav 88,192,148
L_ v7,24,s55,0
Mvav 88,196,168
S_ s55,s71,512000
Ta 188,v2,57344
Ta 190,v2,59392
Mvav 88,220,188
S_ m0,s51,14336
A_ v134,v54,v134
A_ v135,v55,v135
Zvav 84,220,184
A_ v124,v48,v124
A_ v125,v49,v125
Mvav 84,196,164
L_ v7,24,s55,0
Mvav 84,192,144
S_ s55,s71,522240
A_ v126,v50,v126
A_ v127,v51,v127
Zvav 80,192,140
W_ 10
A_ v116,v44,v116
A_ v117,v45,v117
Mvav 80,196,160
S_ m0,s51,15360
A_ v118,v46,v118
A_ v119,v47,v119
Mvav 80,220,180
L_ v7,24,s55,0
Zvav 76,220,176
W_ 8
A_ v108,v40,v108
A_ v109,v41,v109
Mvav 76,196,156
S_ s55,s71,532480
A_ v110,v42,v110
A_ v111,v43,v111
Mvav 76,192,136
W_ 0
A_ v100,v36,v100
Zvav 16,180,136
S_ m0,s51,16384
Ta 192,v3,49152
Ta 194,v3,51200
Mvav 16,184,156
L_ v7,24,s55,0
Mvav 16,188,176
S_ s55,s71,542720
Ta 196,v3,53248
Ta 198,v3,55296
Zvav 20,188,180
S_ m0,s51,17408
A_ v101,v37,v101
A_ v102,v38,v102
Mvav 20,184,160
S_ s85,s6,4
Ta 220,v3,57344
Ta 222,v3,59392
Mvav 20,180,140
L_ v7,24,s55,0
Zvav 24,180,144
s_and_b32 s85, s85, 7
A_ v103,v39,v103
C_ 131,130,131
Mvav 24,184,164
s_lshl_b32 s85, s85, 7
C_ 130,128,129
C_ 132,132,133
Mvav 24,188,184
S_ s85,s85,s87
C_ 133,134,135
v_min_u32 v12, 240, v12
Zvav 28,188,188
buffer_store_dwordx4 v[130:133], v6, s[28:31], s73 offen offset:0 sc1
Mvav 28,184,168
s_sub_u32 s73, s73, 4096
v_lshlrev_b32 v12, 3, v12
Mvav 28,180,148
C_ 123,122,123
C_ 122,120,121
Zvav 32,180,152
C_ 124,124,125
C_ 125,126,127
Mvav 32,184,172
Mvav 32,188,192
s_waitcnt vmcnt(25) lgkmcnt(0)
s_barrier
Zvav 52,220,192
S_ s55,s71,614400
Ta 180,%0,36864
Ta 182,%0,38912
Mvav 52,196,172
S_ m0,s51,49152
Mvav 52,192,152
s_nop 0
Ta 184,%0,40960
Ta 186,%0,43008
Zvav 48,192,148
buffer_store_dwordx4 v[122:125], v6, s[28:31], s73 offen offset:3072 sc1
Mvav 48,196,168
s_nop 0
Ta 188,%0,45056
Ta 190,%0,47104
Mvav 48,220,188
C_ 115,114,115
C_ 114,112,113
Zvav 44,220,184
C_ 116,116,117
C_ 117,118,119
Mvav 44,196,164
Mvav 44,192,144
Zvav 40,192,140
Mvav 40,196,160
Mvav 40,220,180
buffer_store_dwordx4 v[114:117], v6, s[28:31], s73 offen offset:2048 sc1
Zvav 36,220,176
C_ 107,106,107
C_ 106,104,105
Mvav 36,196,156
C_ 108,108,109
C_ 109,110,111
Mvav 36,192,136
W_ 0
Mvav 56,180,196
s_nop 0
Ta 192,v1,36864
Ta 194,v1,38912
Mvav 56,184,216
Mvav 56,188,236
s_nop 0
Ta 196,v1,40960
Ta 198,v1,43008
Mvav 60,188,240
Mvav 60,184,220
s_nop 0
Ta 220,v1,45056
Ta 222,v1,47104
Mvav 60,180,200
buffer_store_dwordx4 v[106:109], v6, s[28:31], s73 offen offset:1024 sc1
Mvav 64,180,204
C_ 99,98,99
C_ 98,96,97
Mvav 64,184,224
C_ 100,100,101
C_ 101,102,103
Mvav 64,188,244
Mvav 68,188,248
Mvav 68,184,228
Mvav 68,180,208
Mvav 72,180,212
Mvav 72,184,232
buffer_store_dwordx4 v[98:101], v6, s[28:31], s73 offen offset:0 sc1
Mvav 72,188,252
W_ 0
Mvav 92,220,252
s_nop 0
Ta 180,v2,36864
Ta 182,v2,38912
Mvav 92,196,232
Mvav 92,192,212
s_nop 0
Ta 184,v2,40960
Ta 186,v2,43008
Mvav 88,192,208
Mvav 88,196,228
s_nop 0
Ta 188,v2,45056
Ta 190,v2,47104
Mvav 88,220,248
Mvav 84,220,244
L_ v7,24,s55,0
Mvav 84,196,224
S_ s55,s71,624640
Mvav 84,192,204
S_ m0,s51,50176
Mvav 80,192,200
Mvav 80,196,220
L_ v7,24,s55,0
Mvav 80,220,240
S_ s55,s71,634880
Mvav 76,220,236
S_ m0,s51,51200
Mvav 76,196,216
Mvav 76,192,196
W_ 0
Mvav 16,180,196
s_nop 0
Ta 192,v3,36864
Ta 194,v3,38912
Mvav 16,184,216
L_ v7,24,s55,0
Mvav 16,188,236
S_ s55,s71,645120
Ta 196,v3,40960
Ta 198,v3,43008
Mvav 20,188,240
S_ m0,s51,52224
Mvav 20,184,220
.Lpad_o_3:
s_cbranch_execz .Lpad_o_7
Ta 220,v3,45056
Ta 222,v3,47104
Mvav 20,180,200
L_ v7,24,s55,0
Mvav 24,180,204
S_ s55,s71,655360
Mvav 24,184,224
S_ m0,s51,53248
Mvav 24,188,244
Mvav 28,188,248
L_ v7,24,s55,0
Mvav 28,184,228
S_ s55,s71,665600
Mvav 28,180,208
S_ m0,s51,54272
Mvav 32,180,212
Mvav 32,184,232
L_ v7,24,s55,0
Mvav 32,188,252
s_waitcnt vmcnt(29) lgkmcnt(0)
s_barrier
Mvav 52,220,252
S_ s55,s71,0
Ta 180,%0,24576
Ta 182,%0,26624
Mvav 52,196,232
S_ m0,s51,36864
Mvav 52,192,212
s_nop 0
Ta 184,%0,28672
Ta 186,%0,30720
Mvav 48,192,208
L_ v7,24,s55,0
Mvav 48,196,228
S_ s55,s71,10240
Ta 188,%0,32768
Ta 190,%0,34816
Mvav 48,220,248
S_ m0,s51,37888
Mvav 44,220,244
Mvav 44,196,224
L_ v7,24,s55,0
Mvav 44,192,204
S_ s55,s71,20480
Mvav 40,192,200
S_ m0,s51,38912
Mvav 40,196,220
Mvav 40,220,240
L_ v7,24,s55,0
Mvav 36,220,236
S_ s55,s71,30720
Mvav 36,196,216
S_ m0,s51,39936
Mvav 36,192,196
W_ 0
Mvaa 56,180,0
s_nop 0
Ta 192,v1,24576
Ta 194,v1,26624
Mvaa 56,184,40
L_ v7,24,s55,0
Mvaa 56,188,80
S_ s55,s71,40960
Ta 196,v1,28672
Ta 198,v1,30720
Mvaa 60,188,84
S_ m0,s51,40960
Mvaa 60,184,44
s_nop 0
Ta 220,v1,32768
Ta 222,v1,34816
Mvaa 60,180,4
L_ v7,24,s55,0
Mvaa 64,180,8
S_ s55,s71,51200
Mvaa 64,184,48
S_ m0,s51,41984
Mvaa 64,188,88
Mvaa 68,188,240
L_ v7,24,s55,0
Mvaa 68,184,52
Mvaa 68,180,12
Mvaa 72,180,16
Mvaa 72,184,56
buffer_load_dword a74, v12, s[44:47], s66 offen
Mvaa 72,188,244
W_ 0
Mvaa 92,220,244
s_nop 0
Ta 180,v2,24576
Ta 182,v2,26624
Mvaa 92,196,56
Mvaa 92,192,16
s_nop 0
Ta 184,v2,28672
Ta 186,v2,30720
Mvaa 88,192,12
buffer_load_dword a74, v4, s[24:27], s85 offen
Mvaa 88,196,52
s_nop 0
Ta 188,v2,32768
Ta 190,v2,34816
Mvaa 88,220,240
Mvaa 84,220,88
Mvaa 84,196,48
Mvaa 84,192,8
Mvaa 80,192,4
Mvaa 80,196,44
Mvaa 80,220,84
Mvaa 76,220,80
Mvaa 76,196,40
Mvaa 76,192,0
W_ 0
Mvaa 16,180,0
s_nop 0
Ta 192,v3,24576
Ta 194,v3,26624
Mvaa 16,184,40
Mvaa 16,188,80
s_nop 0
Ta 196,v3,28672
Ta 198,v3,30720
Mvaa 20,188,84
Mvaa 20,184,44
s_nop 0
Ta 220,v3,32768
Ta 222,v3,34816
Mvaa 20,180,4
Mvaa 24,180,8
Mvaa 24,184,48
Mvaa 24,188,88
Mvaa 28,188,240
Mvaa 28,184,52
Mvaa 28,180,12
Mvaa 32,180,16
Mvaa 32,184,56
Mvaa 32,188,244
s_waitcnt vmcnt(25) lgkmcnt(0)
s_barrier
Mvaa 52,220,244
s_nop 0
Ta 180,%0,0
Ta 182,%0,2048
Mvaa 52,196,56
Mvaa 52,192,16
s_nop 0
Ta 184,%0,4096
Ta 186,%0,6144
Mvaa 48,192,12
Mvaa 48,196,52
s_nop 0
Ta 188,%0,8192
Ta 190,%0,10240
Mvaa 48,220,240
Mvaa 44,220,88
Mvaa 44,196,48
Mvaa 44,192,8
Mvaa 40,192,4
Mvaa 40,196,44
Mvaa 40,220,84
Mvaa 36,220,80
Mvaa 36,196,40
Mvaa 36,192,0
W_ 0
Mvaa 56,180,120
S_ s81,s6,1
Ta 192,v1,0
Ta 194,v1,2048
Mvaa 56,184,160
s_and_b32 s81, s81, 7
Mvaa 56,188,200
s_mul_i32 s81, s81, 20480
Ta 196,v1,4096
Ta 198,v1,6144
Mvaa 60,188,204
s_mov_b32 m0, s52
S_ s73,s81,s78
Mvaa 60,184,164
S_ s73,s73,4096
Ta 220,v1,8192
Ta 222,v1,10240
Mvaa 60,180,124
s_nop 0
Y_ 56,0
Y_ 57,256
Mvaa 64,180,128
s_nop 0
Y_ 58,512
Y_ 59,768
Mvaa 64,184,168
Mvaa 64,188,208
s_nop 0
Y_ 60,1024
Y_ 61,1280
Mvaa 68,188,212
s_nop 0
Y_ 62,1536
Y_ 63,1792
Mvaa 68,184,172
Mvaa 68,180,132
s_nop 0
Y_ 64,2048
Y_ 65,2304
Mvaa 72,180,136
s_nop 0
Y_ 66,2560
Y_ 67,2816
Mvaa 72,184,176
Mvaa 72,188,216
W_ 12
Y_ 68,3072
Y_ 69,3328
Mvaa 92,220,216
s_nop 0
Ta 180,v2,0
Ta 182,v2,2048
Mvaa 92,196,176
s_nop 0
Y_ 70,3584
Y_ 71,3840
Mvaa 92,192,136
s_nop 0
Ta 184,v2,4096
Ta 186,v2,6144
Mvaa 88,192,132
s_nop 0
Y_ 72,4096
Y_ 73,4352
Mvaa 88,196,172
s_nop 0
Ta 188,v2,8192
Ta 190,v2,10240
Mvaa 88,220,212
s_nop 0
Y_ 74,4608
Y_ 75,4864
Mvaa 84,220,208
s_nop 0
Y_ 92,9216
Y_ 93,9472
Mvaa 84,196,168
s_nop 0
Y_ 94,9728
Y_ 95,9984
Mvaa 84,192,128
s_nop 0
Y_ 88,8192
Y_ 89,8448
Mvaa 80,192,124
s_nop 0
Y_ 90,8704
Y_ 91,8960
Mvaa 80,196,164
Mvaa 80,220,204
s_nop 0
Y_ 84,7168
Y_ 85,7424
Mvaa 76,220,200
s_nop 0
Y_ 86,7680
Y_ 87,7936
Mvaa 76,196,160
Mvaa 76,192,120
W_ 14
Y_ 80,6144
Y_ 81,6400
Mvaa 16,180,120
s_nop 0
Ta 192,v3,0
Ta 194,v3,2048
Mvaa 16,184,160
s_nop 0
Y_ 82,6656
Y_ 83,6912
Mvaa 16,188,200
s_nop 0
Ta 196,v3,4096
Ta 198,v3,6144
Mvaa 20,188,204
s_nop 0
Y_ 76,5120
Y_ 77,5376
Mvaa 20,184,164
s_nop 0
Ta 220,v3,8192
Ta 222,v3,10240
Mvaa 20,180,124
s_nop 0
Y_ 78,5632
Y_ 79,5888
Mvaa 24,180,128
Mvaa 24,184,168
Mvaa 24,188,208
s_waitcnt vmcnt(19) lgkmcnt(0)
s_barrier
Mvaa 28,188,212
S_ s55,s71,122880
ds_read2st64_b32 v[96:97], v5 offset0:0 offset1:1
Mvaa 28,184,172
S_ m0,s51,24576
ds_read2st64_b32 v[98:99], v5 offset0:2 offset1:3
Mvaa 28,180,132
L_ v7,24,s55,0
Mvaa 32,180,136
S_ s55,s71,133120
ds_read2st64_b32 v[104:105], v5 offset0:4 offset1:5
Mvaa 32,184,176
S_ m0,s51,25600
ds_read2st64_b32 v[106:107], v5 offset0:6 offset1:7
Mvaa 32,188,216
ds_read2st64_b32 v[112:113], v5 offset0:8 offset1:9
Mvaa 52,220,216
s_nop 0
Ta 180,%0,12288
Ta 182,%0,14336
Mvaa 52,196,176
L_ v7,24,s55,0
Mvaa 52,192,136
W_ 5
Ta 184,%0,16384
Ta 186,%0,18432
Mvaa 48,192,132
S_ s55,s71,143360
ds_read2st64_b32 v[114:115], v5 offset0:10 offset1:11
A_ v96,v16,v96
Mvaa 48,196,172
S_ m0,s51,26624
Ta 188,%0,20480
Ta 190,%0,22528
Mvaa 48,220,212
L_ v7,24,s55,0
Mvaa 44,220,208
S_ s55,s71,153600
ds_read2st64_b32 v[120:121], v5 offset0:12 offset1:13
A_ v97,v17,v97
Mvaa 44,196,168
S_ m0,s51,27648
ds_read2st64_b32 v[122:123], v5 offset0:14 offset1:15
A_ v98,v18,v98
Mvaa 44,192,128
ds_read2st64_b32 v[128:129], v5 offset0:16 offset1:17
A_ v99,v19,v99
Mvaa 40,192,124
L_ v7,24,s55,0
Mvaa 40,196,164
W_ 11
ds_read2st64_b32 v[130:131], v5 offset0:18 offset1:19
A_ v104,v20,v104
Mvaa 40,220,204
S_ s55,s71,163840
ds_read2st64_b32 v[132:133], v5 offset0:36 offset1:37
A_ v105,v21,v105
Mvaa 36,220,200
S_ m0,s51,28672
ds_read2st64_b32 v[134:135], v5 offset0:38 offset1:39
A_ v106,v22,v106
Mvaa 36,196,160
L_ v7,24,s55,0
Mvaa 36,192,120
W_ 6
ds_read2st64_b32 v[124:125], v5 offset0:32 offset1:33
A_ v107,v23,v107
Zvav 56,180,136
Ta 192,v1,12288
Ta 194,v1,14336
Mvav 56,184,156
S_ s55,s71,174080
ds_read2st64_b32 v[126:127], v5 offset0:34 offset1:35
A_ v112,v24,v112
Mvav 56,188,176
S_ m0,s51,29696
Ta 196,v1,16384
Ta 198,v1,18432
Zvav 60,188,180
L_ v7,24,s55,0
Mvav 60,184,160
S_ s79,s6,3
Ta 220,v1,20480
Ta 222,v1,22528
Mvav 60,180,140
s_and_b32 s79, s79, 7
ds_read2st64_b32 v[116:117], v5 offset0:28 offset1:29
A_ v113,v25,v113
Zvav 64,180,144
s_lshl_b32 s79, s79, 7
ds_read2st64_b32 v[118:119], v5 offset0:30 offset1:31
A_ v114,v26,v114
Mvav 64,184,164
S_ s71,s72,s79
ds_read2st64_b32 v[108:109], v5 offset0:24 offset1:25
A_ v115,v27,v115
Mvav 64,188,184
W_ 14
ds_read2st64_b32 v[110:111], v5 offset0:26 offset1:27
A_ v120,v28,v120
Zvav 68,188,188
S_ s55,s71,491520
ds_read2st64_b32 v[100:101], v5 offset0:20 offset1:21
A_ v121,v29,v121
Mvav 68,184,168
S_ m0,s51,0
ds_read2st64_b32 v[102:103], v5 offset0:22 offset1:23
A_ v122,v30,v122
Mvav 68,180,148
L_ v7,24,s55,0
Zvav 72,180,152
S_ s55,s71,501760
A_ v123,v31,v123
Mvav 72,184,172
W_ 14
A_ v128,v32,v128
A_ v129,v33,v129
Mvav 72,188,192
W_ 6
A_ v130,v34,v130
A_ v131,v35,v131
Zvav 92,220,192
Ta 180,v2,12288
Ta 182,v2,14336
Mvav 92,196,172
S_ m0,s51,1024
A_ v132,v52,v132
A_ v133,v53,v133
Mvav 92,192,152
s_nop 0
Ta 184,v2,16384
Ta 186,v2,18432
Zvav 88,192,148
L_ v7,24,s55,0
Mvav 88,196,168
S_ s55,s71,512000
Ta 188,v2,20480
Ta 190,v2,22528
Mvav 88,220,188
S_ m0,s51,2048
A_ v134,v54,v134
A_ v135,v55,v135
Zvav 84,220,184
A_ v124,v48,v124
A_ v125,v49,v125
Mvav 84,196,164
L_ v7,24,s55,0
Mvav 84,192,144
S_ s55,s71,522240
A_ v126,v50,v126
A_ v127,v51,v127
Zvav 80,192,140
W_ 10
A_ v116,v44,v116
A_ v117,v45,v117
Mvav 80,196,160
S_ m0,s51,3072
A_ v118,v46,v118
A_ v119,v47,v119
Mvav 80,220,180
L_ v7,24,s55,0
Zvav 76,220,176
W_ 8
A_ v108,v40,v108
A_ v109,v41,v109
Mvav 76,196,156
S_ s55,s71,532480
A_ v110,v42,v110
A_ v111,v43,v111
Mvav 76,192,136
W_ 0
A_ v100,v36,v100
Zvav 16,180,136
S_ m0,s51,4096
Ta 192,v3,12288
Ta 194,v3,14336
Mvav 16,184,156
L_ v7,24,s55,0
Mvav 16,188,176
S_ s55,s71,542720
Ta 196,v3,16384
Ta 198,v3,18432
Zvav 20,188,180
S_ m0,s51,5120
A_ v101,v37,v101
A_ v102,v38,v102
Mvav 20,184,160
S_ s85,s6,5
Ta 220,v3,20480
Ta 222,v3,22528
Mvav 20,180,140
L_ v7,24,s55,0
Zvav 24,180,144
s_and_b32 s85, s85, 7
A_ v103,v39,v103
C_ 131,130,131
Mvav 24,184,164
s_lshl_b32 s85, s85, 7
C_ 130,128,129
C_ 132,132,133
Mvav 24,188,184
S_ s85,s85,s87
C_ 133,134,135
Zvav 28,188,188
buffer_store_dwordx4 v[130:133], v6, s[28:31], s73 offen offset:0 sc1
Mvav 28,184,168
s_sub_u32 s73, s73, 4096
Mvav 28,180,148
C_ 123,122,123
C_ 122,120,121
Zvav 32,180,152
C_ 124,124,125
C_ 125,126,127
Mvav 32,184,172
Mvav 32,188,192
s_waitcnt vmcnt(21) lgkmcnt(0)
s_barrier
Zvav 52,220,192
S_ s56,s76,s74
Ta 180,%0,49152
Ta 182,%0,51200
Mvav 52,196,172
S_ s55,s71,614400
Mvav 52,192,152
S_ m0,s51,12288
Ta 184,%0,53248
Ta 186,%0,55296
Zvav 48,192,148
buffer_load_dwordx4 a[60:63], v6, s[28:31], s56 offen offset:0 sc1
Mvav 48,196,168
S_ s56,s76,s82
Ta 188,%0,57344
Ta 190,%0,59392
Mvav 48,220,188
Zvav 44,220,184
Mvav 44,196,164
buffer_load_dwordx4 a[64:67], v6, s[28:31], s56 offen offset:0 sc1
Mvav 44,192,144
Zvav 40,192,140
Mvav 40,196,160
Mvav 40,220,180
buffer_store_dwordx4 v[122:125], v6, s[28:31], s73 offen offset:3072 sc1
Zvav 36,220,176
C_ 115,114,115
C_ 114,112,113
Mvav 36,196,156
C_ 116,116,117
C_ 117,118,119
Mvav 36,192,136
W_ 0
Mvav 56,180,196
s_nop 0
Ta 192,v1,49152
Ta 194,v1,51200
Mvav 56,184,216
Mvav 56,188,236
s_nop 0
Ta 196,v1,53248
Ta 198,v1,55296
Mvav 60,188,240
Mvav 60,184,220
s_nop 0
Ta 220,v1,57344
Ta 222,v1,59392
Mvav 60,180,200
buffer_store_dwordx4 v[114:117], v6, s[28:31], s73 offen offset:2048 sc1
Mvav 64,180,204
C_ 107,106,107
C_ 106,104,105
Mvav 64,184,224
C_ 108,108,109
C_ 109,110,111
Mvav 64,188,244
Mvav 68,188,248
.Lpad_o_4:
s_cbranch_execz .Lpad_o_8
Mvav 68,184,228
Mvav 68,180,208
Mvav 72,180,212
Mvav 72,184,232
buffer_store_dwordx4 v[106:109], v6, s[28:31], s73 offen offset:1024 sc1
Mvav 72,188,252
W_ 0
C_ 99,98,99
C_ 98,96,97
Mvav 92,220,252
s_nop 0
Ta 180,v2,49152
Ta 182,v2,51200
Mvav 92,196,232
C_ 100,100,101
C_ 101,102,103
Mvav 92,192,212
s_nop 0
Ta 184,v2,53248
Ta 186,v2,55296
Mvav 88,192,208
Mvav 88,196,228
s_nop 0
Ta 188,v2,57344
Ta 190,v2,59392
Mvav 88,220,248
Mvav 84,220,244
buffer_store_dwordx4 v[98:101], v6, s[28:31], s73 offen offset:0 sc1
Mvav 84,196,224
Mvav 84,192,204
Mvav 80,192,200
Mvav 80,196,220
S_ s83,s6,7
Mvav 80,220,240
s_and_b32 s83, s83, 7
Mvav 76,220,236
s_lshl_b32 s83, s83, 2
Mvav 76,196,216
v_cmp_eq_u32_e64 s[36:37], s83, v8
Mvav 76,192,196
W_ 0
v_mov_b32 v10, 0x7fff0000
Mvav 16,180,196
s_nop 0
Ta 192,v3,49152
Ta 194,v3,51200
Mvav 16,184,216
L_ v7,24,s55,0
Mvav 16,188,236
S_ s55,s71,624640
Ta 196,v3,53248
Ta 198,v3,55296
Mvav 20,188,240
S_ m0,s51,13312
v_cndmask_b32_e64 v10, v10, v9, s[36:37]
Mvav 20,184,220
s_nop 0
Ta 220,v3,57344
Ta 222,v3,59392
Mvav 20,180,200
L_ v7,24,s55,0
Mvav 24,180,204
S_ s55,s71,634880
Mvav 24,184,224
S_ m0,s51,14336
Mvav 24,188,244
Mvav 28,188,248
L_ v7,24,s55,0
Mvav 28,184,228
S_ s55,s71,645120
Mvav 28,180,208
S_ m0,s51,15360
Mvav 32,180,212
Mvav 32,184,232
L_ v7,24,s55,0
Mvav 32,188,252
s_waitcnt vmcnt(25) lgkmcnt(0)
s_barrier
S_ s55,s71,655360
Mvav 52,220,252
S_ m0,s51,16384
Ta 180,%0,36864
Ta 182,%0,38912
Mvav 52,196,232
Mvav 52,192,212
s_nop 0
Ta 184,%0,40960
Ta 186,%0,43008
Mvav 48,192,208
buffer_store_dword a255, v10, s[92:95], 0 offen offset:384 sc1
Mvav 48,196,228
s_nop 0
Ta 188,%0,45056
Ta 190,%0,47104
Mvav 48,220,248
Mvav 44,220,244
Mvav 44,196,224
L_ v7,24,s55,0
Mvav 44,192,204
S_ s55,s71,665600
Mvav 40,192,200
S_ m0,s51,17408
Mvav 40,196,220
Mvav 40,220,240
L_ v7,24,s55,0
Mvav 36,220,236
S_ s55,s71,0
Mvav 36,196,216
S_ m0,s51,49152
Mvav 36,192,196
W_ 0
Mvaa 56,180,0
s_nop 0
Ta 192,v1,36864
Ta 194,v1,38912
Mvaa 56,184,40
L_ v7,24,s55,0
Mvaa 56,188,80
S_ s55,s71,10240
Ta 196,v1,40960
Ta 198,v1,43008
Mvaa 60,188,84
S_ m0,s51,50176
Mvaa 60,184,44
s_nop 0
Ta 220,v1,45056
Ta 222,v1,47104
Mvaa 60,180,4
L_ v7,24,s55,0
Mvaa 64,180,8
S_ s55,s71,20480
Mvaa 64,184,48
S_ m0,s51,51200
Mvaa 64,188,88
Mvaa 68,188,240
L_ v7,24,s55,0
Mvaa 68,184,52
S_ s55,s71,30720
Mvaa 68,180,12
S_ m0,s51,52224
Mvaa 72,180,16
Mvaa 72,184,56
L_ v7,24,s55,0
Mvaa 72,188,244
W_ 0
S_ s55,s71,40960
Mvaa 92,220,244
S_ m0,s51,53248
Ta 180,v2,36864
Ta 182,v2,38912
Mvaa 92,196,56
Mvaa 92,192,16
s_nop 0
Ta 184,v2,40960
Ta 186,v2,43008
Mvaa 88,192,12
L_ v7,24,s55,0
Mvaa 88,196,52
S_ s55,s71,51200
Ta 188,v2,45056
Ta 190,v2,47104
Mvaa 88,220,240
S_ m0,s51,54272
Mvaa 84,220,88
Mvaa 84,196,48
L_ v7,24,s55,0
Mvaa 84,192,8
Mvaa 80,192,4
Mvaa 80,196,44
Mvaa 80,220,84
buffer_load_dword a74, v4, s[24:27], s85 offen
Mvaa 76,220,80
Mvaa 76,196,40
Mvaa 76,192,0
W_ 0
Mvaa 16,180,0
s_nop 0
Ta 192,v3,36864
Ta 194,v3,38912
Mvaa 16,184,40
Mvaa 16,188,80
s_nop 0
Ta 196,v3,40960
Ta 198,v3,43008
Mvaa 20,188,84
Mvaa 20,184,44
s_nop 0
Ta 220,v3,45056
Ta 222,v3,47104
Mvaa 20,180,4
Mvaa 24,180,8
Mvaa 24,184,48
Mvaa 24,188,88
Mvaa 28,188,240
Mvaa 28,184,52
Mvaa 28,180,12
Mvaa 32,180,16
Mvaa 32,184,56
Mvaa 32,188,244
s_waitcnt vmcnt(27) lgkmcnt(0)
s_barrier
Mvaa 52,220,244
s_nop 0
Ta 180,%0,24576
Ta 182,%0,26624
Mvaa 52,196,56
Mvaa 52,192,16
s_nop 0
Ta 184,%0,28672
Ta 186,%0,30720
Mvaa 48,192,12
Mvaa 48,196,52
s_nop 0
Ta 188,%0,32768
Ta 190,%0,34816
Mvaa 48,220,240
Mvaa 44,220,88
Mvaa 44,196,48
Mvaa 44,192,8
Mvaa 40,192,4
Mvaa 40,196,44
Mvaa 40,220,84
Mvaa 36,220,80
Mvaa 36,196,40
Mvaa 36,192,0
W_ 0
Mvaa 56,180,120
S_ s81,s6,2
Ta 192,v1,24576
Ta 194,v1,26624
Mvaa 56,184,160
s_and_b32 s81, s81, 7
Mvaa 56,188,200
s_mul_i32 s81, s81, 20480
Ta 196,v1,28672
Ta 198,v1,30720
Mvaa 60,188,204
s_mov_b32 m0, s52
S_ s73,s81,s78
Mvaa 60,184,164
S_ s73,s73,4096
Ta 220,v1,32768
Ta 222,v1,34816
Mvaa 60,180,124
s_nop 0
Y_ 56,0
Y_ 57,256
Mvaa 64,180,128
s_nop 0
Y_ 58,512
Y_ 59,768
Mvaa 64,184,168
Mvaa 64,188,208
s_nop 0
Y_ 60,1024
Y_ 61,1280
Mvaa 68,188,212
s_nop 0
Y_ 62,1536
Y_ 63,1792
Mvaa 68,184,172
Mvaa 68,180,132
s_nop 0
Y_ 64,2048
Y_ 65,2304
Mvaa 72,180,136
s_nop 0
Y_ 66,2560
Y_ 67,2816
Mvaa 72,184,176
Mvaa 72,188,216
W_ 12
Y_ 68,3072
Y_ 69,3328
Mvaa 92,220,216
s_nop 0
Ta 180,v2,24576
Ta 182,v2,26624
Mvaa 92,196,176
s_nop 0
Y_ 70,3584
Y_ 71,3840
Mvaa 92,192,136
s_nop 0
Ta 184,v2,28672
Ta 186,v2,30720
Mvaa 88,192,132
s_nop 0
Y_ 72,4096
Y_ 73,4352
Mvaa 88,196,172
S_ s83,s6,1
Ta 188,v2,32768
Ta 190,v2,34816
Mvaa 88,220,212
s_and_b32 s83, s83, 7
Y_ 74,4608
Y_ 75,4864
Mvaa 84,220,208
s_lshl_b32 s83, s83, 2
Y_ 92,9216
Y_ 93,9472
Mvaa 84,196,168
s_nop 0
Y_ 94,9728
Y_ 95,9984
Mvaa 84,192,128
s_nop 0
Y_ 88,8192
Y_ 89,8448
Mvaa 80,192,124
s_nop 0
Y_ 90,8704
Y_ 91,8960
Mvaa 80,196,164
v_cmp_eq_u32_e64 s[36:37], s83, v8
Mvaa 80,220,204
s_nop 0
Y_ 84,7168
Y_ 85,7424
Mvaa 76,220,200
s_nop 0
Y_ 86,7680
Y_ 87,7936
Mvaa 76,196,160
v_mov_b32 v10, 0x7fff0000
Mvaa 76,192,120
W_ 14
Y_ 80,6144
Y_ 81,6400
Mvaa 16,180,120
s_nop 0
Ta 192,v3,24576
Ta 194,v3,26624
Mvaa 16,184,160
s_nop 0
Y_ 82,6656
Y_ 83,6912
Mvaa 16,188,200
s_nop 0
Ta 196,v3,28672
Ta 198,v3,30720
Mvaa 20,188,204
s_nop 0
Y_ 76,5120
Y_ 77,5376
Mvaa 20,184,164
s_nop 0
Ta 220,v3,32768
Ta 222,v3,34816
Mvaa 20,180,124
s_nop 0
Y_ 78,5632
Y_ 79,5888
Mvaa 24,180,128
v_cndmask_b32_e64 v10, v10, v9, s[36:37]
Mvaa 24,184,168
Mvaa 24,188,208
s_waitcnt vmcnt(14) lgkmcnt(0)
s_barrier
Mvaa 28,188,212
buffer_store_dword a255, v10, s[92:95], 0 offen offset:384 sc1
Mvaa 28,184,172
S_ s55,s71,122880
ds_read2st64_b32 v[96:97], v5 offset0:0 offset1:1
Mvaa 28,180,132
S_ m0,s51,36864
ds_read2st64_b32 v[98:99], v5 offset0:2 offset1:3
Mvaa 32,180,136
ds_read2st64_b32 v[104:105], v5 offset0:4 offset1:5
Mvaa 32,184,176
L_ v7,24,s55,0
Mvaa 32,188,216
S_ s55,s71,133120
ds_read2st64_b32 v[106:107], v5 offset0:6 offset1:7
Mvaa 52,220,216
S_ m0,s51,37888
Ta 180,%0,0
Ta 182,%0,2048
Mvaa 52,196,176
ds_read2st64_b32 v[112:113], v5 offset0:8 offset1:9
Mvaa 52,192,136
W_ 5
Ta 184,%0,4096
Ta 186,%0,6144
Mvaa 48,192,132
L_ v7,24,s55,0
Mvaa 48,196,172
S_ s55,s71,143360
Ta 188,%0,8192
Ta 190,%0,10240
Mvaa 48,220,212
S_ m0,s51,38912
ds_read2st64_b32 v[114:115], v5 offset0:10 offset1:11
A_ v96,v16,v96
Mvaa 44,220,208
ds_read2st64_b32 v[120:121], v5 offset0:12 offset1:13
A_ v97,v17,v97
Mvaa 44,196,168
L_ v7,24,s55,0
Mvaa 44,192,128
S_ s55,s71,153600
ds_read2st64_b32 v[122:123], v5 offset0:14 offset1:15
A_ v98,v18,v98
Mvaa 40,192,124
S_ m0,s51,39936
ds_read2st64_b32 v[128:129], v5 offset0:16 offset1:17
A_ v99,v19,v99
Mvaa 40,196,164
W_ 11
ds_read2st64_b32 v[130:131], v5 offset0:18 offset1:19
A_ v104,v20,v104
Mvaa 40,220,204
L_ v7,24,s55,0
Mvaa 36,220,200
S_ s55,s71,163840
ds_read2st64_b32 v[132:133], v5 offset0:36 offset1:37
A_ v105,v21,v105
Mvaa 36,196,160
S_ m0,s51,40960
ds_read2st64_b32 v[134:135], v5 offset0:38 offset1:39
A_ v106,v22,v106
Mvaa 36,192,120
ds_read2st64_b32 v[124:125], v5 offset0:32 offset1:33
A_ v107,v23,v107
s_waitcnt vmcnt(23)
Zaaa 224,70,60
L_ v7,24,s55,0
Zaaa 228,68,60
W_ 7
ds_read2st64_b32 v[126:127], v5 offset0:34 offset1:35
A_ v112,v24,v112
Zaaa 232,70,64
S_ s55,s71,174080
ds_read2st64_b32 v[116:117], v5 offset0:28 offset1:29
A_ v113,v25,v113
Zaaa 236,68,64
ds_read2st64_b32 v[118:119], v5 offset0:30 offset1:31
A_ v114,v26,v114
Zvav 56,180,136
S_ m0,s51,41984
Ta 192,v1,0
Ta 194,v1,2048
Mvav 56,184,156
L_ v7,24,s55,0
Mvav 56,188,176
S_ s79,s6,4
Ta 196,v1,4096
Ta 198,v1,6144
Zvav 60,188,180
s_and_b32 s79, s79, 7
ds_read2st64_b32 v[108:109], v5 offset0:24 offset1:25
A_ v115,v27,v115
Mvav 60,184,160
W_ 13
Ta 220,v1,8192
Ta 222,v1,10240
Mvav 60,180,140
s_lshl_b32 s79, s79, 7
ds_read2st64_b32 v[110:111], v5 offset0:26 offset1:27
A_ v120,v28,v120
Zvav 64,180,144
S_ s71,s72,s79
ds_read2st64_b32 v[100:101], v5 offset0:20 offset1:21
A_ v121,v29,v121
Mvav 64,184,164
S_ s55,s71,491520
ds_read2st64_b32 v[102:103], v5 offset0:22 offset1:23
A_ v122,v30,v122
Mvav 64,188,184
S_ m0,s51,24576
A_ v123,v31,v123
Zvav 68,188,188
L_ v7,24,s55,0
Mvav 68,184,168
W_ 14
A_ v128,v32,v128
A_ v129,v33,v129
Mvav 68,180,148
S_ s55,s71,501760
A_ v130,v34,v130
A_ v131,v35,v131
Zvav 72,180,152
A_ v132,v52,v132
A_ v133,v53,v133
Mvav 72,184,172
S_ m0,s51,25600
A_ v134,v54,v134
A_ v135,v55,v135
Mvav 72,188,192
W_ 3
A_ v124,v48,v124
Zvav 92,220,192
s_nop 0
Ta 180,v2,0
Ta 182,v2,2048
Mvav 92,196,172
L_ v7,24,s55,0
Mvav 92,192,152
S_ s55,s71,512000
Ta 184,v2,4096
Ta 186,v2,6144
Zvav 88,192,148
S_ m0,s51,26624
A_ v125,v49,v125
A_ v126,v50,v126
Mvav 88,196,168
s_nop 0
Ta 188,v2,8192
Ta 190,v2,10240
Mvav 88,220,188
L_ v7,24,s55,0
Zvav 84,220,184
S_ s55,s71,522240
A_ v127,v51,v127
C_ 131,130,131
Mvav 84,196,164
A_ v116,v44,v116
A_ v117,v45,v117
Mvav 84,192,144
S_ m0,s51,27648
A_ v118,v46,v118
A_ v119,v47,v119
Zvav 80,192,140
L_ v7,24,s55,0
Mvav 80,196,160
W_ 8
A_ v108,v40,v108
A_ v109,v41,v109
Mvav 80,220,180
S_ s55,s71,532480
A_ v110,v42,v110
A_ v111,v43,v111
Zvav 76,220,176
W_ 6
A_ v100,v36,v100
A_ v101,v37,v101
Mvav 76,196,156
S_ m0,s51,28672
A_ v102,v38,v102
A_ v103,v39,v103
Mvav 76,192,136
W_ 0
C_ 130,128,129
C_ 132,132,133
Zvav 16,180,136
S_ s85,s6,6
Ta 192,v3,0
Ta 194,v3,2048
Mvav 16,184,156
L_ v7,24,s55,0
Mvav 16,188,176
S_ s55,s71,542720
Ta 196,v3,4096
Ta 198,v3,6144
Zvav 20,188,180
S_ m0,s51,29696
C_ 133,134,135
s_and_b32 s85, s85, 7
Mvav 20,184,160
s_lshl_b32 s85, s85, 7
Ta 220,v3,8192
Ta 222,v3,10240
Mvav 20,180,140
buffer_store_dwordx4 v[130:133], v6, s[28:31], s73 offen offset:0 sc1
Zvav 24,180,144
s_sub_u32 s73, s73, 4096
S_ s85,s85,s87
Mvav 24,184,164
C_ 123,122,123
C_ 122,120,121
Mvav 24,188,184
.Lpad_o_5:
s_cbranch_execz .Lpad_o_9
C_ 124,124,125
C_ 125,126,127
Zvav 28,188,188
Mvav 28,184,168
Mvav 28,180,148
Zvav 32,180,152
Mvav 32,184,172
buffer_store_dwordx4 v[122:125], v6, s[28:31], s73 offen offset:3072 sc1
Mvav 32,188,192
s_waitcnt vmcnt(21) lgkmcnt(0)
s_barrier
C_ 115,114,115
Zvav 52,220,192
s_nop 0
Ta 180,%0,12288
Ta 182,%0,14336
Mvav 52,196,172
C_ 114,112,113
C_ 116,116,117
Mvav 52,192,152
s_nop 0
Ta 184,%0,16384
Ta 186,%0,18432
Zvav 48,192,148
C_ 117,118,119
Mvav 48,196,168
s_nop 0
Ta 188,%0,20480
Ta 190,%0,22528
Mvav 48,220,188
Zvav 44,220,184
buffer_store_dwordx4 v[114:117], v6, s[28:31], s73 offen offset:2048 sc1
Mvav 44,196,164
C_ 107,106,107
C_ 106,104,105
Mvav 44,192,144
C_ 108,108,109
C_ 109,110,111
Zvav 40,192,140
Mvav 40,196,160
Mvav 40,220,180
Zvav 36,220,176
Mvav 36,196,156
Mvav 36,192,136
W_ 0
Mvav 56,180,196
s_nop 0
Ta 192,v1,12288
Ta 194,v1,14336
Mvav 56,184,216
buffer_store_dwordx4 v[106:109], v6, s[28:31], s73 offen offset:1024 sc1
Mvav 56,188,236
s_nop 0
Ta 196,v1,16384
Ta 198,v1,18432
Mvav 60,188,240
C_ 99,98,99
C_ 98,96,97
Mvav 60,184,220
s_nop 0
Ta 220,v1,20480
Ta 222,v1,22528
Mvav 60,180,200
C_ 100,100,101
C_ 101,102,103
Mvav 64,180,204
Mvav 64,184,224
Mvav 64,188,244
Mvav 68,188,248
buffer_store_dwordx4 v[98:101], v6, s[28:31], s73 offen offset:0 sc1
Mvav 68,184,228
Mvav 68,180,208
Mvav 72,180,212
Mvav 72,184,232
Mvav 72,188,252
W_ 0
Mvav 92,220,252
s_nop 0
Ta 180,v2,12288
Ta 182,v2,14336
Mvav 92,196,232
Mvav 92,192,212
s_nop 0
Ta 184,v2,16384
Ta 186,v2,18432
Mvav 88,192,208
L_ v7,24,s55,0
Mvav 88,196,228
S_ s55,s71,614400
Ta 188,v2,20480
Ta 190,v2,22528
Mvav 88,220,248
S_ m0,s51,0
Mvav 84,220,244
Mvav 84,196,224
L_ v7,24,s55,0
Mvav 84,192,204
S_ s55,s71,624640
Mvav 80,192,200
S_ m0,s51,1024
Mvav 80,196,220
Mvav 80,220,240
L_ v7,24,s55,0
Mvav 76,220,236
S_ s55,s71,634880
Mvav 76,196,216
S_ m0,s51,2048
Mvav 76,192,196
W_ 0
Mvav 16,180,196
s_nop 0
Ta 192,v3,12288
Ta 194,v3,14336
Mvav 16,184,216
L_ v7,24,s55,0
Mvav 16,188,236
S_ s55,s71,645120
Ta 196,v3,16384
Ta 198,v3,18432
Mvav 20,188,240
S_ m0,s51,3072
Mvav 20,184,220
s_nop 0
Ta 220,v3,20480
Ta 222,v3,22528
Mvav 20,180,200
L_ v7,24,s55,0
Mvav 24,180,204
S_ s55,s71,655360
Mvav 24,184,224
S_ m0,s51,4096
Mvav 24,188,244
Mvav 28,188,248
L_ v7,24,s55,0
Mvav 28,184,228
S_ s55,s71,665600
Mvav 28,180,208
S_ m0,s51,5120
Mvav 32,180,212
Mvav 32,184,232
L_ v7,24,s55,0
Mvav 32,188,252
s_waitcnt vmcnt(25) lgkmcnt(0)
s_barrier
Mvav 52,220,252
S_ s55,s71,0
Ta 180,%0,49152
Ta 182,%0,51200
Mvav 52,196,232
S_ m0,s51,12288
Mvav 52,192,212
s_nop 0
Ta 184,%0,53248
Ta 186,%0,55296
Mvav 48,192,208
L_ v7,24,s55,0
Mvav 48,196,228
S_ s55,s71,10240
Ta 188,%0,57344
Ta 190,%0,59392
Mvav 48,220,248
S_ m0,s51,13312
Mvav 44,220,244
Mvav 44,196,224
L_ v7,24,s55,0
Mvav 44,192,204
S_ s55,s71,20480
Mvav 40,192,200
S_ m0,s51,14336
Mvav 40,196,220
Mvav 40,220,240
L_ v7,24,s55,0
Mvav 36,220,236
S_ s55,s71,30720
Mvav 36,196,216
S_ m0,s51,15360
Mvav 36,192,196
W_ 0
Mvaa 56,180,0
s_nop 0
Ta 192,v1,49152
Ta 194,v1,51200
Mvaa 56,184,40
L_ v7,24,s55,0
Mvaa 56,188,80
S_ s55,s71,40960
Ta 196,v1,53248
Ta 198,v1,55296
Mvaa 60,188,84
S_ m0,s51,16384
Mvaa 60,184,44
s_nop 0
Ta 220,v1,57344
Ta 222,v1,59392
Mvaa 60,180,4
L_ v7,24,s55,0
Mvaa 64,180,8
S_ s55,s71,51200
Mvaa 64,184,48
S_ m0,s51,17408
Mvaa 64,188,88
Mvaa 68,188,240
L_ v7,24,s55,0
Mvaa 68,184,52
Mvaa 68,180,12
Mvaa 72,180,16
Mvaa 72,184,56
buffer_load_dword a74, v4, s[24:27], s85 offen
Mvaa 72,188,244
W_ 0
Mvaa 92,220,244
s_nop 0
Ta 180,v2,49152
Ta 182,v2,51200
Mvaa 92,196,56
Mvaa 92,192,16
s_nop 0
Ta 184,v2,53248
Ta 186,v2,55296
Mvaa 88,192,12
Mvaa 88,196,52
s_nop 0
Ta 188,v2,57344
Ta 190,v2,59392
Mvaa 88,220,240
Mvaa 84,220,88
Mvaa 84,196,48
Mvaa 84,192,8
Mvaa 80,192,4
Mvaa 80,196,44
Mvaa 80,220,84
Mvaa 76,220,80
Mvaa 76,196,40
Mvaa 76,192,0
W_ 0
Mvaa 16,180,0
s_nop 0
Ta 192,v3,49152
Ta 194,v3,51200
Mvaa 16,184,40
Mvaa 16,188,80
s_nop 0
Ta 196,v3,53248
Ta 198,v3,55296
Mvaa 20,188,84
Mvaa 20,184,44
s_nop 0
Ta 220,v3,57344
Ta 222,v3,59392
Mvaa 20,180,4
Mvaa 24,180,8
Mvaa 24,184,48
Mvaa 24,188,88
Mvaa 28,188,240
Mvaa 28,184,52
Mvaa 28,180,12
Mvaa 32,180,16
Mvaa 32,184,56
Mvaa 32,188,244
s_waitcnt vmcnt(24) lgkmcnt(0)
s_barrier
Mvaa 52,220,244
s_nop 0
Ta 180,%0,36864
Ta 182,%0,38912
Mvaa 52,196,56
Mvaa 52,192,16
s_nop 0
Ta 184,%0,40960
Ta 186,%0,43008
Mvaa 48,192,12
Mvaa 48,196,52
s_nop 0
Ta 188,%0,45056
Ta 190,%0,47104
Mvaa 48,220,240
Mvaa 44,220,88
Mvaa 44,196,48
Mvaa 44,192,8
Mvaa 40,192,4
Mvaa 40,196,44
Mvaa 40,220,84
Mvaa 36,220,80
Mvaa 36,196,40
Mvaa 36,192,0
W_ 0
Mvaa 56,180,120
S_ s81,s6,3
Ta 192,v1,36864
Ta 194,v1,38912
Mvaa 56,184,160
s_and_b32 s81, s81, 7
Mvaa 56,188,200
s_mul_i32 s81, s81, 20480
Ta 196,v1,40960
Ta 198,v1,43008
Mvaa 60,188,204
s_mov_b32 m0, s52
S_ s73,s81,s78
Mvaa 60,184,164
S_ s73,s73,4096
Ta 220,v1,45056
Ta 222,v1,47104
Mvaa 60,180,124
s_nop 0
Y_ 56,0
Y_ 57,256
Mvaa 64,180,128
s_nop 0
Y_ 58,512
Y_ 59,768
Mvaa 64,184,168
Mvaa 64,188,208
s_nop 0
Y_ 60,1024
Y_ 61,1280
Mvaa 68,188,212
s_nop 0
Y_ 62,1536
Y_ 63,1792
Mvaa 68,184,172
Mvaa 68,180,132
s_nop 0
Y_ 64,2048
Y_ 65,2304
Mvaa 72,180,136
s_nop 0
Y_ 66,2560
Y_ 67,2816
Mvaa 72,184,176
Mvaa 72,188,216
W_ 12
Y_ 68,3072
Y_ 69,3328
Mvaa 92,220,216
s_nop 0
Ta 180,v2,36864
Ta 182,v2,38912
Mvaa 92,196,176
s_nop 0
Y_ 70,3584
Y_ 71,3840
Mvaa 92,192,136
s_nop 0
Ta 184,v2,40960
Ta 186,v2,43008
Mvaa 88,192,132
s_nop 0
Y_ 72,4096
Y_ 73,4352
Mvaa 88,196,172
S_ s83,s6,2
Ta 188,v2,45056
Ta 190,v2,47104
Mvaa 88,220,212
s_and_b32 s83, s83, 7
Y_ 74,4608
Y_ 75,4864
Mvaa 84,220,208
s_lshl_b32 s83, s83, 2
Y_ 92,9216
)A"
R"A(Y_ 93,9472
Mvaa 84,196,168
s_nop 0
Y_ 94,9728
Y_ 95,9984
Mvaa 84,192,128
s_nop 0
Y_ 88,8192
Y_ 89,8448
Mvaa 80,192,124
s_nop 0
Y_ 90,8704
Y_ 91,8960
Mvaa 80,196,164
v_cmp_eq_u32_e64 s[36:37], s83, v8
Mvaa 80,220,204
s_sub_u32 s75, s6, 1
Y_ 84,7168
Y_ 85,7424
Mvaa 76,220,200
s_and_b32 s75, s75, 7
Y_ 86,7680
Y_ 87,7936
Mvaa 76,196,160
v_mov_b32 v10, 0x7fff0000
Mvaa 76,192,120
W_ 14
Y_ 80,6144
Y_ 81,6400
Mvaa 16,180,120
s_nop 0
Ta 192,v3,36864
Ta 194,v3,38912
Mvaa 16,184,160
s_nop 0
Y_ 82,6656
Y_ 83,6912
Mvaa 16,188,200
s_nop 0
Ta 196,v3,40960
Ta 198,v3,43008
Mvaa 20,188,204
s_nop 0
Y_ 76,5120
Y_ 77,5376
Mvaa 20,184,164
s_nop 0
Ta 220,v3,45056
Ta 222,v3,47104
Mvaa 20,180,124
s_nop 0
Y_ 78,5632
Y_ 79,5888
Mvaa 24,180,128
buffer_load_dword v11, v8, s[92:95], s96 offen offset:384 sc1
Mvaa 24,184,168
v_cndmask_b32_e64 v10, v10, v9, s[36:37]
Mvaa 24,188,208
s_waitcnt vmcnt(14) lgkmcnt(0)
s_barrier
Mvaa 28,188,212
S_ s55,s71,122880
ds_read2st64_b32 v[96:97], v5 offset0:0 offset1:1
Mvaa 28,184,172
buffer_store_dword a255, v10, s[92:95], 0 offen offset:384 sc1
Mvaa 28,180,132
S_ m0,s51,49152
ds_read2st64_b32 v[98:99], v5 offset0:2 offset1:3
Mvaa 32,180,136
ds_read2st64_b32 v[104:105], v5 offset0:4 offset1:5
Mvaa 32,184,176
ds_read2st64_b32 v[106:107], v5 offset0:6 offset1:7
Mvaa 32,188,216
L_ v7,24,s55,0
Mvaa 52,220,216
S_ s55,s71,133120
Ta 180,%0,24576
Ta 182,%0,26624
Mvaa 52,196,176
S_ m0,s51,50176
ds_read2st64_b32 v[112:113], v5 offset0:8 offset1:9
Mvaa 52,192,136
W_ 5
Ta 184,%0,28672
Ta 186,%0,30720
Mvaa 48,192,132
L_ v7,24,s55,0
Mvaa 48,196,172
S_ s55,s71,143360
Ta 188,%0,32768
Ta 190,%0,34816
Mvaa 48,220,212
S_ m0,s51,51200
ds_read2st64_b32 v[114:115], v5 offset0:10 offset1:11
A_ v96,v16,v96
Mvaa 44,220,208
ds_read2st64_b32 v[120:121], v5 offset0:12 offset1:13
A_ v97,v17,v97
Mvaa 44,196,168
L_ v7,24,s55,0
Mvaa 44,192,128
S_ s55,s71,153600
ds_read2st64_b32 v[122:123], v5 offset0:14 offset1:15
A_ v98,v18,v98
Mvaa 40,192,124
S_ m0,s51,52224
ds_read2st64_b32 v[128:129], v5 offset0:16 offset1:17
A_ v99,v19,v99
Mvaa 40,196,164
W_ 11
ds_read2st64_b32 v[130:131], v5 offset0:18 offset1:19
A_ v104,v20,v104
Mvaa 40,220,204
L_ v7,24,s55,0
Mvaa 36,220,200
S_ s55,s71,163840
ds_read2st64_b32 v[132:133], v5 offset0:36 offset1:37
A_ v105,v21,v105
Mvaa 36,196,160
S_ m0,s51,53248
ds_read2st64_b32 v[134:135], v5 offset0:38 offset1:39
A_ v106,v22,v106
Mvaa 36,192,120
W_ 7
ds_read2st64_b32 v[124:125], v5 offset0:32 offset1:33
A_ v107,v23,v107
Zvav 56,180,136
W_ 7
Ta 192,v1,24576
Ta 194,v1,26624
Mvav 56,184,156
L_ v7,24,s55,0
Mvav 56,188,176
S_ s55,s71,174080
Ta 196,v1,28672
Ta 198,v1,30720
Zvav 60,188,180
S_ m0,s51,54272
ds_read2st64_b32 v[126:127], v5 offset0:34 offset1:35
A_ v112,v24,v112
Mvav 60,184,160
s_nop 0
Ta 220,v1,32768
Ta 222,v1,34816
Mvav 60,180,140
L_ v7,24,s55,0
Zvav 64,180,144
S_ s79,s6,5
ds_read2st64_b32 v[116:117], v5 offset0:28 offset1:29
A_ v113,v25,v113
Mvav 64,184,164
s_and_b32 s79, s79, 7
ds_read2st64_b32 v[118:119], v5 offset0:30 offset1:31
A_ v114,v26,v114
Mvav 64,188,184
s_lshl_b32 s79, s79, 7
ds_read2st64_b32 v[108:109], v5 offset0:24 offset1:25
A_ v115,v27,v115
Zvav 68,188,188
W_ 14
ds_read2st64_b32 v[110:111], v5 offset0:26 offset1:27
A_ v120,v28,v120
Mvav 68,184,168
S_ s71,s72,s79
ds_read2st64_b32 v[100:101], v5 offset0:20 offset1:21
A_ v121,v29,v121
Mvav 68,180,148
S_ s55,s71,491520
ds_read2st64_b32 v[102:103], v5 offset0:22 offset1:23
A_ v122,v30,v122
Zvav 72,180,152
s_waitcnt vmcnt(7)
A_ v123,v31,v123
S_ m0,s51,36864
Mvav 72,184,172
L_ v7,24,s55,0
Mvav 72,188,192
W_ 6
v_cmp_eq_u32 vcc, s62, v11
Zvav 92,220,192
s_mul_i32 s76, s75, 204800
Ta 180,v2,24576
Ta 182,v2,26624
Mvav 92,196,172
s_bitcmp1_b32 vcc_lo, s75
s_cbranch_scc0 .Lspin0
.Lspun0:
A_ v128,v32,v128
Mvav 92,192,152
S_ s56,s76,s84
Ta 184,v2,28672
Ta 186,v2,30720
Zvav 88,192,148
buffer_load_dwordx4 a[20:23], v6, s[28:31], s56 offen offset:0 sc1
Mvav 88,196,168
S_ s55,s71,501760
Ta 188,v2,32768
Ta 190,v2,34816
Mvav 88,220,188
S_ m0,s51,37888
A_ v129,v33,v129
A_ v130,v34,v130
Zvav 84,220,184
S_ s85,s6,7
A_ v131,v35,v131
Mvav 84,196,164
buffer_load_dwordx4 a[24:27], v6, s[28:31], s56 offen offset:1024 sc1
Mvav 84,192,144
.Lpad_o_6:
s_cbranch_execz .Lpad_o_10
A_ v132,v52,v132
A_ v133,v53,v133
Zvav 80,192,140
s_and_b32 s85, s85, 7
A_ v134,v54,v134
A_ v135,v55,v135
Mvav 80,196,160
A_ v124,v48,v124
A_ v125,v49,v125
Mvav 80,220,180
buffer_load_dwordx4 a[28:31], v6, s[28:31], s56 offen offset:2048 sc1
Zvav 76,220,176
s_lshl_b32 s85, s85, 7
A_ v126,v50,v126
A_ v127,v51,v127
Mvav 76,196,156
W_ 10
A_ v116,v44,v116
A_ v117,v45,v117
Mvav 76,192,136
W_ 0
A_ v118,v46,v118
A_ v119,v47,v119
Zvav 16,180,136
Ta 192,v3,24576
Ta 194,v3,26624
Mvav 16,184,156
buffer_load_dwordx4 a[32:35], v6, s[28:31], s56 offen offset:3072 sc1
Mvav 16,188,176
S_ s56,s56,4096
Ta 196,v3,28672
Ta 198,v3,30720
Zvav 20,188,180
S_ s85,s85,s87
A_ v108,v40,v108
A_ v109,v41,v109
Mvav 20,184,160
s_nop 0
Ta 220,v3,32768
Ta 222,v3,34816
Mvav 20,180,140
buffer_load_dwordx4 a[36:39], v6, s[28:31], s56 offen offset:0 sc1
Zvav 24,180,144
S_ s56,s76,s74
A_ v110,v42,v110
A_ v111,v43,v111
Mvav 24,184,164
A_ v100,v36,v100
A_ v101,v37,v101
Mvav 24,188,184
A_ v102,v38,v102
A_ v103,v39,v103
Zvav 28,188,188
buffer_load_dwordx4 a[60:63], v6, s[28:31], s56 offen offset:0 sc1
Mvav 28,184,168
S_ s56,s76,s82
C_ 131,130,131
C_ 130,128,129
Mvav 28,180,148
C_ 132,132,133
C_ 133,134,135
Zvav 32,180,152
Mvav 32,184,172
buffer_load_dwordx4 a[64:67], v6, s[28:31], s56 offen offset:0 sc1
Mvav 32,188,192
s_waitcnt vmcnt(23) lgkmcnt(0)
s_barrier
Zvav 52,220,192
s_nop 0
Ta 180,%0,0
Ta 182,%0,2048
Mvav 52,196,172
Mvav 52,192,152
s_nop 0
Ta 184,%0,4096
Ta 186,%0,6144
Zvav 48,192,148
buffer_store_dwordx4 v[130:133], v6, s[28:31], s73 offen offset:0 sc1
Mvav 48,196,168
s_sub_u32 s73, s73, 4096
Ta 188,%0,8192
Ta 190,%0,10240
Mvav 48,220,188
C_ 123,122,123
C_ 122,120,121
Zvav 44,220,184
C_ 124,124,125
C_ 125,126,127
Mvav 44,196,164
Mvav 44,192,144
Zvav 40,192,140
Mvav 40,196,160
Mvav 40,220,180
buffer_store_dwordx4 v[122:125], v6, s[28:31], s73 offen offset:3072 sc1
Zvav 36,220,176
C_ 115,114,115
C_ 114,112,113
Mvav 36,196,156
C_ 116,116,117
C_ 117,118,119
Mvav 36,192,136
W_ 0
Mvav 56,180,196
s_nop 0
Ta 192,v1,0
Ta 194,v1,2048
Mvav 56,184,216
Mvav 56,188,236
s_nop 0
Ta 196,v1,4096
Ta 198,v1,6144
Mvav 60,188,240
Mvav 60,184,220
s_nop 0
Ta 220,v1,8192
Ta 222,v1,10240
Mvav 60,180,200
buffer_store_dwordx4 v[114:117], v6, s[28:31], s73 offen offset:2048 sc1
Mvav 64,180,204
C_ 107,106,107
C_ 106,104,105
Mvav 64,184,224
C_ 108,108,109
C_ 109,110,111
Mvav 64,188,244
Mvav 68,188,248
Mvav 68,184,228
Mvav 68,180,208
Mvav 72,180,212
Mvav 72,184,232
buffer_store_dwordx4 v[106:109], v6, s[28:31], s73 offen offset:1024 sc1
Mvav 72,188,252
W_ 0
C_ 99,98,99
C_ 98,96,97
Mvav 92,220,252
s_nop 0
Ta 180,v2,0
Ta 182,v2,2048
Mvav 92,196,232
C_ 100,100,101
C_ 101,102,103
Mvav 92,192,212
s_nop 0
Ta 184,v2,4096
Ta 186,v2,6144
Mvav 88,192,208
Mvav 88,196,228
s_nop 0
Ta 188,v2,8192
Ta 190,v2,10240
Mvav 88,220,248
Mvav 84,220,244
buffer_store_dwordx4 v[98:101], v6, s[28:31], s73 offen offset:0 sc1
Mvav 84,196,224
Mvav 84,192,204
Mvav 80,192,200
Mvav 80,196,220
Mvav 80,220,240
Mvav 76,220,236
Mvav 76,196,216
Mvav 76,192,196
W_ 0
Mvav 16,180,196
s_nop 0
Ta 192,v3,0
Ta 194,v3,2048
Mvav 16,184,216
L_ v7,24,s55,0
Mvav 16,188,236
S_ s55,s71,512000
Ta 196,v3,4096
Ta 198,v3,6144
Mvav 20,188,240
S_ m0,s51,38912
Mvav 20,184,220
s_nop 0
Ta 220,v3,8192
Ta 222,v3,10240
Mvav 20,180,200
L_ v7,24,s55,0
Mvav 24,180,204
S_ s55,s71,522240
Mvav 24,184,224
S_ m0,s51,39936
Mvav 24,188,244
Mvav 28,188,248
L_ v7,24,s55,0
Mvav 28,184,228
S_ s55,s71,532480
Mvav 28,180,208
S_ m0,s51,40960
Mvav 32,180,212
Mvav 32,184,232
L_ v7,24,s55,0
Mvav 32,188,252
s_waitcnt vmcnt(26) lgkmcnt(0)
s_barrier
S_ s55,s71,542720
Mvav 52,220,252
S_ m0,s51,41984
Ta 180,%0,12288
Ta 182,%0,14336
Mvav 52,196,232
Mvav 52,192,212
s_nop 0
Ta 184,%0,16384
Ta 186,%0,18432
Mvav 48,192,208
L_ v7,24,s55,0
Mvav 48,196,228
S_ s55,s71,614400
Ta 188,%0,20480
Ta 190,%0,22528
Mvav 48,220,248
S_ m0,s51,24576
Mvav 44,220,244
Mvav 44,196,224
L_ v7,24,s55,0
Mvav 44,192,204
S_ s55,s71,624640
Mvav 40,192,200
S_ m0,s51,25600
Mvav 40,196,220
Mvav 40,220,240
L_ v7,24,s55,0
Mvav 36,220,236
S_ s55,s71,634880
Mvav 36,196,216
S_ m0,s51,26624
Mvav 36,192,196
W_ 0
Mvaa 56,180,0
s_nop 0
Ta 192,v1,12288
Ta 194,v1,14336
Mvaa 56,184,40
L_ v7,24,s55,0
Mvaa 56,188,80
S_ s55,s71,645120
Ta 196,v1,16384
Ta 198,v1,18432
Mvaa 60,188,84
S_ m0,s51,27648
Mvaa 60,184,44
s_nop 0
Ta 220,v1,20480
Ta 222,v1,22528
Mvaa 60,180,4
L_ v7,24,s55,0
Mvaa 64,180,8
S_ s55,s71,655360
Mvaa 64,184,48
S_ m0,s51,28672
Mvaa 64,188,88
Mvaa 68,188,240
L_ v7,24,s55,0
Mvaa 68,184,52
S_ s55,s71,665600
Mvaa 68,180,12
S_ m0,s51,29696
Mvaa 72,180,16
Mvaa 72,184,56
L_ v7,24,s55,0
Mvaa 72,188,244
W_ 0
S_ s55,s71,0
Mvaa 92,220,244
S_ m0,s51,0
Ta 180,v2,12288
Ta 182,v2,14336
Mvaa 92,196,56
Mvaa 92,192,16
s_nop 0
Ta 184,v2,16384
Ta 186,v2,18432
Mvaa 88,192,12
L_ v7,24,s55,0
Mvaa 88,196,52
S_ s55,s71,10240
Ta 188,v2,20480
Ta 190,v2,22528
Mvaa 88,220,240
S_ m0,s51,1024
Mvaa 84,220,88
Mvaa 84,196,48
L_ v7,24,s55,0
Mvaa 84,192,8
S_ s55,s71,20480
Mvaa 80,192,4
S_ m0,s51,2048
Mvaa 80,196,44
Mvaa 80,220,84
L_ v7,24,s55,0
Mvaa 76,220,80
S_ s55,s71,30720
Mvaa 76,196,40
S_ m0,s51,3072
Mvaa 76,192,0
W_ 0
Mvaa 16,180,0
s_nop 0
Ta 192,v3,12288
Ta 194,v3,14336
Mvaa 16,184,40
L_ v7,24,s55,0
Mvaa 16,188,80
S_ s55,s71,40960
Ta 196,v3,16384
Ta 198,v3,18432
Mvaa 20,188,84
S_ m0,s51,4096
Mvaa 20,184,44
s_nop 0
Ta 220,v3,20480
Ta 222,v3,22528
Mvaa 20,180,4
L_ v7,24,s55,0
Mvaa 24,180,8
S_ s55,s71,51200
Mvaa 24,184,48
S_ m0,s51,5120
Mvaa 24,188,88
Mvaa 28,188,240
L_ v7,24,s55,0
Mvaa 28,184,52
Mvaa 28,180,12
Mvaa 32,180,16
Mvaa 32,184,56
buffer_load_dword a74, v4, s[24:27], s85 offen
Mvaa 32,188,244
s_waitcnt vmcnt(31) lgkmcnt(0)
s_barrier
Mvaa 52,220,244
s_nop 0
Ta 180,%0,49152
Ta 182,%0,51200
Mvaa 52,196,56
Mvaa 52,192,16
s_nop 0
Ta 184,%0,53248
Ta 186,%0,55296
Mvaa 48,192,12
Mvaa 48,196,52
s_nop 0
Ta 188,%0,57344
Ta 190,%0,59392
Mvaa 48,220,240
Mvaa 44,220,88
Mvaa 44,196,48
Mvaa 44,192,8
Mvaa 40,192,4
Mvaa 40,196,44
Mvaa 40,220,84
Mvaa 36,220,80
Mvaa 36,196,40
Mvaa 36,192,0
W_ 0
Mvaa 56,180,120
S_ s81,s6,4
Ta 192,v1,49152
Ta 194,v1,51200
Mvaa 56,184,160
s_and_b32 s81, s81, 7
Mvaa 56,188,200
s_mul_i32 s81, s81, 20480
Ta 196,v1,53248
Ta 198,v1,55296
Mvaa 60,188,204
s_mov_b32 m0, s52
S_ s73,s81,s78
Mvaa 60,184,164
S_ s73,s73,4096
Ta 220,v1,57344
Ta 222,v1,59392
Mvaa 60,180,124
s_nop 0
Y_ 56,0
Y_ 57,256
Mvaa 64,180,128
s_nop 0
Y_ 58,512
Y_ 59,768
Mvaa 64,184,168
Mvaa 64,188,208
s_nop 0
Y_ 60,1024
Y_ 61,1280
Mvaa 68,188,212
s_nop 0
Y_ 62,1536
Y_ 63,1792
Mvaa 68,184,172
Mvaa 68,180,132
s_nop 0
Y_ 64,2048
Y_ 65,2304
Mvaa 72,180,136
s_nop 0
Y_ 66,2560
Y_ 67,2816
Mvaa 72,184,176
Mvaa 72,188,216
W_ 12
Y_ 68,3072
Y_ 69,3328
Mvaa 92,220,216
s_nop 0
Ta 180,v2,49152
Ta 182,v2,51200
Mvaa 92,196,176
s_nop 0
Y_ 70,3584
Y_ 71,3840
Mvaa 92,192,136
s_nop 0
Ta 184,v2,53248
Ta 186,v2,55296
Mvaa 88,192,132
s_nop 0
Y_ 72,4096
Y_ 73,4352
Mvaa 88,196,172
S_ s83,s6,3
Ta 188,v2,57344
Ta 190,v2,59392
Mvaa 88,220,212
s_and_b32 s83, s83, 7
Y_ 74,4608
Y_ 75,4864
Mvaa 84,220,208
s_lshl_b32 s83, s83, 2
Y_ 92,9216
Y_ 93,9472
Mvaa 84,196,168
s_nop 0
Y_ 94,9728
Y_ 95,9984
Mvaa 84,192,128
s_nop 0
Y_ 88,8192
Y_ 89,8448
Mvaa 80,192,124
s_nop 0
Y_ 90,8704
Y_ 91,8960
Mvaa 80,196,164
v_cmp_eq_u32_e64 s[36:37], s83, v8
Mvaa 80,220,204
s_sub_u32 s75, s6, 2
Y_ 84,7168
Y_ 85,7424
Mvaa 76,220,200
s_and_b32 s75, s75, 7
Y_ 86,7680
Y_ 87,7936
Mvaa 76,196,160
v_mov_b32 v10, 0x7fff0000
Mvaa 76,192,120
W_ 14
Y_ 80,6144
Y_ 81,6400
Mvaa 16,180,120
s_nop 0
Ta 192,v3,49152
Ta 194,v3,51200
Mvaa 16,184,160
s_nop 0
Y_ 82,6656
Y_ 83,6912
Mvaa 16,188,200
s_nop 0
Ta 196,v3,53248
Ta 198,v3,55296
Mvaa 20,188,204
s_nop 0
Y_ 76,5120
Y_ 77,5376
Mvaa 20,184,164
s_nop 0
Ta 220,v3,57344
Ta 222,v3,59392
Mvaa 20,180,124
s_nop 0
Y_ 78,5632
Y_ 79,5888
Mvaa 24,180,128
buffer_load_dword v11, v8, s[92:95], s96 offen offset:384 sc1
Mvaa 24,184,168
v_cndmask_b32_e64 v10, v10, v9, s[36:37]
Mvaa 24,188,208
s_waitcnt vmcnt(14) lgkmcnt(0)
s_barrier
Mvaa 28,188,212
S_ s55,s71,122880
ds_read2st64_b32 v[96:97], v5 offset0:0 offset1:1
Mvaa 28,184,172
buffer_store_dword a255, v10, s[92:95], 0 offen offset:384 sc1
Mvaa 28,180,132
S_ m0,s51,12288
ds_read2st64_b32 v[98:99], v5 offset0:2 offset1:3
Mvaa 32,180,136
ds_read2st64_b32 v[104:105], v5 offset0:4 offset1:5
Mvaa 32,184,176
ds_read2st64_b32 v[106:107], v5 offset0:6 offset1:7
Mvaa 32,188,216
L_ v7,24,s55,0
Mvaa 52,220,216
S_ s55,s71,133120
Ta 180,%0,36864
Ta 182,%0,38912
Mvaa 52,196,176
S_ m0,s51,13312
ds_read2st64_b32 v[112:113], v5 offset0:8 offset1:9
Mvaa 52,192,136
W_ 5
Ta 184,%0,40960
Ta 186,%0,43008
Mvaa 48,192,132
L_ v7,24,s55,0
Mvaa 48,196,172
S_ s55,s71,143360
Ta 188,%0,45056
Ta 190,%0,47104
Mvaa 48,220,212
S_ m0,s51,14336
ds_read2st64_b32 v[114:115], v5 offset0:10 offset1:11
A_ v96,v16,v96
Mvaa 44,220,208
ds_read2st64_b32 v[120:121], v5 offset0:12 offset1:13
A_ v97,v17,v97
Mvaa 44,196,168
L_ v7,24,s55,0
Mvaa 44,192,128
S_ s55,s71,153600
ds_read2st64_b32 v[122:123], v5 offset0:14 offset1:15
A_ v98,v18,v98
Mvaa 40,192,124
S_ m0,s51,15360
ds_read2st64_b32 v[128:129], v5 offset0:16 offset1:17
A_ v99,v19,v99
Mvaa 40,196,164
W_ 11
ds_read2st64_b32 v[130:131], v5 offset0:18 offset1:19
A_ v104,v20,v104
Mvaa 40,220,204
L_ v7,24,s55,0
Mvaa 36,220,200
S_ s55,s71,163840
ds_read2st64_b32 v[132:133], v5 offset0:36 offset1:37
A_ v105,v21,v105
Mvaa 36,196,160
S_ m0,s51,16384
ds_read2st64_b32 v[134:135], v5 offset0:38 offset1:39
A_ v106,v22,v106
Mvaa 36,192,120
ds_read2st64_b32 v[124:125], v5 offset0:32 offset1:33
A_ v107,v23,v107
s_waitcnt vmcnt(29)
Zaaa 100,70,20
L_ v7,24,s55,0
Zaaa 140,68,20
W_ 7
ds_read2st64_b32 v[126:127], v5 offset0:34 offset1:35
A_ v112,v24,v112
Zaaa 104,70,24
S_ s55,s71,174080
ds_read2st64_b32 v[116:117], v5 offset0:28 offset1:29
A_ v113,v25,v113
.Lpad_o_7:
s_cbranch_execz .Lpad_o_11
Zaaa 144,68,24
S_ m0,s51,17408
ds_read2st64_b32 v[118:119], v5 offset0:30 offset1:31
A_ v114,v26,v114
Zaaa 108,70,28
L_ v7,24,s55,0
Zaaa 148,68,28
S_ s79,s6,6
ds_read2st64_b32 v[108:109], v5 offset0:24 offset1:25
A_ v115,v27,v115
Zaaa 112,70,32
W_ 9
ds_read2st64_b32 v[110:111], v5 offset0:26 offset1:27
A_ v120,v28,v120
Zaaa 152,68,32
s_and_b32 s79, s79, 7
ds_read2st64_b32 v[100:101], v5 offset0:20 offset1:21
A_ v121,v29,v121
Zaaa 116,70,36
s_lshl_b32 s79, s79, 7
ds_read2st64_b32 v[102:103], v5 offset0:22 offset1:23
A_ v122,v30,v122
Zaaa 156,68,36
S_ s71,s72,s79
A_ v123,v31,v123
Maaa 224,70,60
W_ 10
A_ v128,v32,v128
A_ v129,v33,v129
Maaa 228,68,60
S_ s55,s71,491520
A_ v130,v34,v130
A_ v131,v35,v131
Maaa 232,70,64
W_ 8
A_ v132,v52,v132
A_ v133,v53,v133
Maaa 236,68,64
A_ v134,v54,v134
A_ v135,v55,v135
Zvav 56,180,136
W_ 6
Ta 192,v1,36864
Ta 194,v1,38912
Mvav 56,184,156
S_ m0,s51,49152
A_ v124,v48,v124
A_ v125,v49,v125
Mvav 56,188,176
s_nop 0
Ta 196,v1,40960
Ta 198,v1,43008
Zvav 60,188,180
L_ v7,24,s55,0
Mvav 60,184,160
S_ s55,s71,501760
Ta 220,v1,45056
Ta 222,v1,47104
Mvav 60,180,140
S_ m0,s51,50176
A_ v126,v50,v126
A_ v127,v51,v127
Zvav 64,180,144
W_ 10
A_ v116,v44,v116
A_ v117,v45,v117
Mvav 64,184,164
L_ v7,24,s55,0
Mvav 64,188,184
S_ s55,s71,512000
A_ v118,v46,v118
A_ v119,v47,v119
Zvav 68,188,188
W_ 8
A_ v108,v40,v108
A_ v109,v41,v109
Mvav 68,184,168
S_ m0,s51,51200
A_ v110,v42,v110
A_ v111,v43,v111
Mvav 68,180,148
L_ v7,24,s55,0
Zvav 72,180,152
s_waitcnt vmcnt(10)
W_ 7
A_ v100,v36,v100
W_ 6
Mvav 72,184,172
S_ s55,s71,522240
v_cmp_eq_u32 vcc, s62, v11
A_ v101,v37,v101
Mvav 72,188,192
W_ 0
A_ v102,v38,v102
s_mul_i32 s76, s75, 204800
Zvav 92,220,192
S_ m0,s51,52224
Ta 180,v2,36864
Ta 182,v2,38912
Mvav 92,196,172
L_ v7,24,s55,0
Mvav 92,192,152
S_ s55,s71,532480
Ta 184,v2,40960
Ta 186,v2,43008
Zvav 88,192,148
s_bitcmp1_b32 vcc_lo, s75
s_cbranch_scc0 .Lspin1
.Lspun1:
A_ v103,v39,v103
Mvav 88,196,168
S_ s56,s76,s84
Ta 188,v2,45056
Ta 190,v2,47104
Mvav 88,220,188
buffer_load_dwordx4 a[20:23], v6, s[28:31], s56 offen offset:0 sc1
Zvav 84,220,184
S_ m0,s51,53248
C_ 131,130,131
C_ 130,128,129
Mvav 84,196,164
S_ s85,s6,8
C_ 132,132,133
C_ 133,134,135
Mvav 84,192,144
s_and_b32 s85, s85, 7
Zvav 80,192,140
buffer_load_dwordx4 a[24:27], v6, s[28:31], s56 offen offset:1024 sc1
Mvav 80,196,160
s_lshl_b32 s85, s85, 7
Mvav 80,220,180
S_ s85,s85,s87
Zvav 76,220,176
Mvav 76,196,156
buffer_load_dwordx4 a[28:31], v6, s[28:31], s56 offen offset:2048 sc1
Mvav 76,192,136
W_ 0
Zvav 16,180,136
s_nop 0
Ta 192,v3,36864
Ta 194,v3,38912
Mvav 16,184,156
Mvav 16,188,176
s_nop 0
Ta 196,v3,40960
Ta 198,v3,43008
Zvav 20,188,180
buffer_load_dwordx4 a[32:35], v6, s[28:31], s56 offen offset:3072 sc1
Mvav 20,184,160
S_ s56,s56,4096
Ta 220,v3,45056
Ta 222,v3,47104
Mvav 20,180,140
Zvav 24,180,144
Mvav 24,184,164
buffer_load_dwordx4 a[36:39], v6, s[28:31], s56 offen offset:0 sc1
Mvav 24,188,184
S_ s56,s76,s74
Zvav 28,188,188
Mvav 28,184,168
Mvav 28,180,148
buffer_load_dwordx4 a[60:63], v6, s[28:31], s56 offen offset:0 sc1
Zvav 32,180,152
S_ s56,s76,s82
Mvav 32,184,172
Mvav 32,188,192
s_waitcnt vmcnt(25) lgkmcnt(0)
s_barrier
Zvav 52,220,192
s_nop 0
Ta 180,%0,24576
Ta 182,%0,26624
Mvav 52,196,172
buffer_load_dwordx4 a[64:67], v6, s[28:31], s56 offen offset:0 sc1
Mvav 52,192,152
s_nop 0
Ta 184,%0,28672
Ta 186,%0,30720
Zvav 48,192,148
Mvav 48,196,168
s_nop 0
Ta 188,%0,32768
Ta 190,%0,34816
Mvav 48,220,188
buffer_store_dwordx4 v[130:133], v6, s[28:31], s73 offen offset:0 sc1
Zvav 44,220,184
s_sub_u32 s73, s73, 4096
Mvav 44,196,164
C_ 123,122,123
C_ 122,120,121
Mvav 44,192,144
C_ 124,124,125
C_ 125,126,127
Zvav 40,192,140
Mvav 40,196,160
Mvav 40,220,180
Zvav 36,220,176
Mvav 36,196,156
buffer_store_dwordx4 v[122:125], v6, s[28:31], s73 offen offset:3072 sc1
Mvav 36,192,136
W_ 0
C_ 115,114,115
C_ 114,112,113
Mvav 56,180,196
s_nop 0
Ta 192,v1,24576
Ta 194,v1,26624
Mvav 56,184,216
C_ 116,116,117
C_ 117,118,119
Mvav 56,188,236
s_nop 0
Ta 196,v1,28672
Ta 198,v1,30720
Mvav 60,188,240
Mvav 60,184,220
s_nop 0
Ta 220,v1,32768
Ta 222,v1,34816
Mvav 60,180,200
Mvav 64,180,204
buffer_store_dwordx4 v[114:117], v6, s[28:31], s73 offen offset:2048 sc1
Mvav 64,184,224
C_ 107,106,107
C_ 106,104,105
Mvav 64,188,244
C_ 108,108,109
C_ 109,110,111
Mvav 68,188,248
Mvav 68,184,228
Mvav 68,180,208
Mvav 72,180,212
Mvav 72,184,232
Mvav 72,188,252
W_ 0
Mvav 92,220,252
s_nop 0
Ta 180,v2,24576
Ta 182,v2,26624
Mvav 92,196,232
buffer_store_dwordx4 v[106:109], v6, s[28:31], s73 offen offset:1024 sc1
Mvav 92,192,212
s_nop 0
Ta 184,v2,28672
Ta 186,v2,30720
Mvav 88,192,208
C_ 99,98,99
C_ 98,96,97
Mvav 88,196,228
s_nop 0
Ta 188,v2,32768
Ta 190,v2,34816
Mvav 88,220,248
C_ 100,100,101
C_ 101,102,103
Mvav 84,220,244
Mvav 84,196,224
Mvav 84,192,204
Mvav 80,192,200
buffer_store_dwordx4 v[98:101], v6, s[28:31], s73 offen offset:0 sc1
Mvav 80,196,220
Mvav 80,220,240
Mvav 76,220,236
Mvav 76,196,216
Mvav 76,192,196
W_ 0
Mvav 16,180,196
s_nop 0
Ta 192,v3,24576
Ta 194,v3,26624
Mvav 16,184,216
Mvav 16,188,236
s_nop 0
Ta 196,v3,28672
Ta 198,v3,30720
Mvav 20,188,240
L_ v7,24,s55,0
Mvav 20,184,220
S_ s55,s71,542720
Ta 220,v3,32768
Ta 222,v3,34816
Mvav 20,180,200
S_ m0,s51,54272
Mvav 24,180,204
Mvav 24,184,224
L_ v7,24,s55,0
Mvav 24,188,244
S_ s55,s71,614400
Mvav 28,188,248
S_ m0,s51,36864
Mvav 28,184,228
Mvav 28,180,208
L_ v7,24,s55,0
Mvav 32,180,212
S_ s55,s71,624640
Mvav 32,184,232
S_ m0,s51,37888
Mvav 32,188,252
s_waitcnt vmcnt(28) lgkmcnt(0)
s_barrier
Mvav 52,220,252
s_nop 0
Ta 180,%0,0
Ta 182,%0,2048
Mvav 52,196,232
L_ v7,24,s55,0
Mvav 52,192,212
S_ s55,s71,634880
Ta 184,%0,4096
Ta 186,%0,6144
Mvav 48,192,208
S_ m0,s51,38912
Mvav 48,196,228
s_nop 0
Ta 188,%0,8192
Ta 190,%0,10240
Mvav 48,220,248
L_ v7,24,s55,0
Mvav 44,220,244
S_ s55,s71,645120
Mvav 44,196,224
S_ m0,s51,39936
Mvav 44,192,204
Mvav 40,192,200
L_ v7,24,s55,0
Mvav 40,196,220
S_ s55,s71,655360
Mvav 40,220,240
S_ m0,s51,40960
Mvav 36,220,236
Mvav 36,196,216
L_ v7,24,s55,0
Mvav 36,192,196
W_ 0
S_ s55,s71,665600
Mvaa 56,180,0
S_ m0,s51,41984
Ta 192,v1,0
Ta 194,v1,2048
Mvaa 56,184,40
Mvaa 56,188,80
s_nop 0
Ta 196,v1,4096
Ta 198,v1,6144
Mvaa 60,188,84
L_ v7,24,s55,0
Mvaa 60,184,44
S_ s55,s71,0
Ta 220,v1,8192
Ta 222,v1,10240
Mvaa 60,180,4
S_ m0,s51,24576
Mvaa 64,180,8
Mvaa 64,184,48
L_ v7,24,s55,0
Mvaa 64,188,88
S_ s55,s71,10240
Mvaa 68,188,240
S_ m0,s51,25600
Mvaa 68,184,52
Mvaa 68,180,12
L_ v7,24,s55,0
Mvaa 72,180,16
S_ s55,s71,20480
Mvaa 72,184,56
S_ m0,s51,26624
Mvaa 72,188,244
W_ 0
Mvaa 92,220,244
s_nop 0
Ta 180,v2,0
Ta 182,v2,2048
Mvaa 92,196,56
L_ v7,24,s55,0
Mvaa 92,192,16
S_ s55,s71,30720
Ta 184,v2,4096
Ta 186,v2,6144
Mvaa 88,192,12
S_ m0,s51,27648
Mvaa 88,196,52
s_nop 0
Ta 188,v2,8192
Ta 190,v2,10240
Mvaa 88,220,240
L_ v7,24,s55,0
Mvaa 84,220,88
S_ s55,s71,40960
Mvaa 84,196,48
S_ m0,s51,28672
Mvaa 84,192,8
Mvaa 80,192,4
L_ v7,24,s55,0
Mvaa 80,196,44
S_ s55,s71,51200
Mvaa 80,220,84
S_ m0,s51,29696
Mvaa 76,220,80
Mvaa 76,196,40
L_ v7,24,s55,0
Mvaa 76,192,0
W_ 0
Mvaa 16,180,0
s_nop 0
Ta 192,v3,0
Ta 194,v3,2048
Mvaa 16,184,40
Mvaa 16,188,80
s_nop 0
Ta 196,v3,4096
Ta 198,v3,6144
Mvaa 20,188,84
buffer_load_dword a74, v4, s[24:27], s85 offen
Mvaa 20,184,44
s_nop 0
Ta 220,v3,8192
Ta 222,v3,10240
Mvaa 20,180,4
Mvaa 24,180,8
Mvaa 24,184,48
Mvaa 24,188,88
Mvaa 28,188,240
Mvaa 28,184,52
Mvaa 28,180,12
Mvaa 32,180,16
Mvaa 32,184,56
Mvaa 32,188,244
s_waitcnt vmcnt(31) lgkmcnt(0)
s_barrier
Mvaa 52,220,244
s_nop 0
Ta 180,%0,12288
Ta 182,%0,14336
Mvaa 52,196,56
Mvaa 52,192,16
s_nop 0
Ta 184,%0,16384
Ta 186,%0,18432
Mvaa 48,192,12
Mvaa 48,196,52
s_nop 0
Ta 188,%0,20480
Ta 190,%0,22528
Mvaa 48,220,240
Mvaa 44,220,88
Mvaa 44,196,48
Mvaa 44,192,8
Mvaa 40,192,4
Mvaa 40,196,44
Mvaa 40,220,84
Mvaa 36,220,80
Mvaa 36,196,40
Mvaa 36,192,0
W_ 0
Mvaa 56,180,120
S_ s81,s6,5
Ta 192,v1,12288
Ta 194,v1,14336
Mvaa 56,184,160
s_and_b32 s81, s81, 7
Mvaa 56,188,200
s_mul_i32 s81, s81, 20480
Ta 196,v1,16384
Ta 198,v1,18432
Mvaa 60,188,204
s_mov_b32 m0, s52
S_ s73,s81,s78
Mvaa 60,184,164
S_ s73,s73,4096
Ta 220,v1,20480
Ta 222,v1,22528
Mvaa 60,180,124
s_nop 0
Y_ 56,0
Y_ 57,256
Mvaa 64,180,128
s_nop 0
Y_ 58,512
Y_ 59,768
Mvaa 64,184,168
Mvaa 64,188,208
s_nop 0
Y_ 60,1024
Y_ 61,1280
Mvaa 68,188,212
s_nop 0
Y_ 62,1536
Y_ 63,1792
Mvaa 68,184,172
Mvaa 68,180,132
s_nop 0
Y_ 64,2048
Y_ 65,2304
Mvaa 72,180,136
s_nop 0
Y_ 66,2560
Y_ 67,2816
Mvaa 72,184,176
Mvaa 72,188,216
W_ 12
Y_ 68,3072
Y_ 69,3328
Mvaa 92,220,216
s_nop 0
Ta 180,v2,12288
Ta 182,v2,14336
Mvaa 92,196,176
s_nop 0
Y_ 70,3584
Y_ 71,3840
Mvaa 92,192,136
s_nop 0
Ta 184,v2,16384
Ta 186,v2,18432
Mvaa 88,192,132
s_nop 0
Y_ 72,4096
Y_ 73,4352
Mvaa 88,196,172
S_ s83,s6,4
Ta 188,v2,20480
Ta 190,v2,22528
Mvaa 88,220,212
s_and_b32 s83, s83, 7
Y_ 74,4608
Y_ 75,4864
Mvaa 84,220,208
s_lshl_b32 s83, s83, 2
Y_ 92,9216
Y_ 93,9472
Mvaa 84,196,168
s_nop 0
Y_ 94,9728
Y_ 95,9984
Mvaa 84,192,128
s_nop 0
Y_ 88,8192
Y_ 89,8448
Mvaa 80,192,124
s_nop 0
Y_ 90,8704
Y_ 91,8960
Mvaa 80,196,164
v_cmp_eq_u32_e64 s[36:37], s83, v8
Mvaa 80,220,204
s_sub_u32 s75, s6, 3
Y_ 84,7168
Y_ 85,7424
Mvaa 76,220,200
s_and_b32 s75, s75, 7
Y_ 86,7680
Y_ 87,7936
Mvaa 76,196,160
v_mov_b32 v10, 0x7fff0000
Mvaa 76,192,120
W_ 14
Y_ 80,6144
Y_ 81,6400
Mvaa 16,180,120
s_nop 0
Ta 192,v3,12288
Ta 194,v3,14336
Mvaa 16,184,160
s_nop 0
Y_ 82,6656
Y_ 83,6912
Mvaa 16,188,200
.Lpad_o_8:
s_cbranch_execz .Lhop_o_r
Ta 196,v3,16384
Ta 198,v3,18432
Mvaa 20,188,204
s_nop 0
Y_ 76,5120
Y_ 77,5376
Mvaa 20,184,164
s_nop 0
Ta 220,v3,20480
Ta 222,v3,22528
Mvaa 20,180,124
s_nop 0
Y_ 78,5632
Y_ 79,5888
Mvaa 24,180,128
buffer_load_dword v11, v8, s[92:95], s96 offen offset:384 sc1
Mvaa 24,184,168
v_cndmask_b32_e64 v10, v10, v9, s[36:37]
Mvaa 24,188,208
s_waitcnt vmcnt(14) lgkmcnt(0)
s_barrier
Mvaa 28,188,212
S_ s55,s71,122880
ds_read2st64_b32 v[96:97], v5 offset0:0 offset1:1
Mvaa 28,184,172
buffer_store_dword a255, v10, s[92:95], 0 offen offset:384 sc1
Mvaa 28,180,132
S_ m0,s51,0
ds_read2st64_b32 v[98:99], v5 offset0:2 offset1:3
Mvaa 32,180,136
ds_read2st64_b32 v[104:105], v5 offset0:4 offset1:5
Mvaa 32,184,176
ds_read2st64_b32 v[106:107], v5 offset0:6 offset1:7
Mvaa 32,188,216
L_ v7,24,s55,0
Mvaa 52,220,216
S_ s55,s71,133120
Ta 180,%0,49152
Ta 182,%0,51200
Mvaa 52,196,176
S_ m0,s51,1024
ds_read2st64_b32 v[112:113], v5 offset0:8 offset1:9
Mvaa 52,192,136
W_ 5
Ta 184,%0,53248
Ta 186,%0,55296
Mvaa 48,192,132
L_ v7,24,s55,0
Mvaa 48,196,172
S_ s55,s71,143360
Ta 188,%0,57344
Ta 190,%0,59392
Mvaa 48,220,212
S_ m0,s51,2048
ds_read2st64_b32 v[114:115], v5 offset0:10 offset1:11
A_ v96,v16,v96
Mvaa 44,220,208
ds_read2st64_b32 v[120:121], v5 offset0:12 offset1:13
A_ v97,v17,v97
Mvaa 44,196,168
L_ v7,24,s55,0
Mvaa 44,192,128
S_ s55,s71,153600
ds_read2st64_b32 v[122:123], v5 offset0:14 offset1:15
A_ v98,v18,v98
Mvaa 40,192,124
S_ m0,s51,3072
ds_read2st64_b32 v[128:129], v5 offset0:16 offset1:17
A_ v99,v19,v99
Mvaa 40,196,164
W_ 11
ds_read2st64_b32 v[130:131], v5 offset0:18 offset1:19
A_ v104,v20,v104
Mvaa 40,220,204
L_ v7,24,s55,0
Mvaa 36,220,200
S_ s55,s71,163840
ds_read2st64_b32 v[132:133], v5 offset0:36 offset1:37
A_ v105,v21,v105
Mvaa 36,196,160
S_ m0,s51,4096
ds_read2st64_b32 v[134:135], v5 offset0:38 offset1:39
A_ v106,v22,v106
Mvaa 36,192,120
ds_read2st64_b32 v[124:125], v5 offset0:32 offset1:33
A_ v107,v23,v107
s_waitcnt vmcnt(26)
Maaa 100,70,20
L_ v7,24,s55,0
Maaa 140,68,20
W_ 7
ds_read2st64_b32 v[126:127], v5 offset0:34 offset1:35
A_ v112,v24,v112
Maaa 104,70,24
S_ s55,s71,174080
ds_read2st64_b32 v[116:117], v5 offset0:28 offset1:29
A_ v113,v25,v113
Maaa 144,68,24
S_ m0,s51,5120
ds_read2st64_b32 v[118:119], v5 offset0:30 offset1:31
A_ v114,v26,v114
Maaa 108,70,28
L_ v7,24,s55,0
Maaa 148,68,28
S_ s79,s6,7
ds_read2st64_b32 v[108:109], v5 offset0:24 offset1:25
A_ v115,v27,v115
Maaa 112,70,32
W_ 9
ds_read2st64_b32 v[110:111], v5 offset0:26 offset1:27
A_ v120,v28,v120
Maaa 152,68,32
s_and_b32 s79, s79, 7
ds_read2st64_b32 v[100:101], v5 offset0:20 offset1:21
A_ v121,v29,v121
Maaa 116,70,36
s_lshl_b32 s79, s79, 7
ds_read2st64_b32 v[102:103], v5 offset0:22 offset1:23
A_ v122,v30,v122
Maaa 156,68,36
S_ s71,s72,s79
A_ v123,v31,v123
Maaa 224,70,60
W_ 10
A_ v128,v32,v128
A_ v129,v33,v129
Maaa 228,68,60
S_ s55,s71,491520
A_ v130,v34,v130
A_ v131,v35,v131
Maaa 232,70,64
W_ 8
A_ v132,v52,v132
A_ v133,v53,v133
Maaa 236,68,64
A_ v134,v54,v134
A_ v135,v55,v135
Zvav 56,180,136
W_ 6
Ta 192,v1,49152
Ta 194,v1,51200
Mvav 56,184,156
S_ m0,s51,12288
A_ v124,v48,v124
A_ v125,v49,v125
Mvav 56,188,176
s_nop 0
Ta 196,v1,53248
Ta 198,v1,55296
Zvav 60,188,180
L_ v7,24,s55,0
Mvav 60,184,160
S_ s55,s71,501760
Ta 220,v1,57344
Ta 222,v1,59392
Mvav 60,180,140
S_ m0,s51,13312
A_ v126,v50,v126
A_ v127,v51,v127
Zvav 64,180,144
W_ 10
A_ v116,v44,v116
A_ v117,v45,v117
Mvav 64,184,164
L_ v7,24,s55,0
Mvav 64,188,184
S_ s55,s71,512000
A_ v118,v46,v118
A_ v119,v47,v119
Zvav 68,188,188
W_ 8
A_ v108,v40,v108
A_ v109,v41,v109
Mvav 68,184,168
S_ m0,s51,14336
A_ v110,v42,v110
A_ v111,v43,v111
Mvav 68,180,148
L_ v7,24,s55,0
Zvav 72,180,152
s_waitcnt vmcnt(10)
W_ 7
A_ v100,v36,v100
W_ 6
Mvav 72,184,172
S_ s55,s71,522240
v_cmp_eq_u32 vcc, s62, v11
A_ v101,v37,v101
Mvav 72,188,192
W_ 0
A_ v102,v38,v102
s_mul_i32 s76, s75, 204800
Zvav 92,220,192
S_ m0,s51,15360
Ta 180,v2,49152
Ta 182,v2,51200
Mvav 92,196,172
L_ v7,24,s55,0
Mvav 92,192,152
S_ s55,s71,532480
Ta 184,v2,53248
Ta 186,v2,55296
Zvav 88,192,148
s_bitcmp1_b32 vcc_lo, s75
s_cbranch_scc0 .Lspin2
.Lspun2:
A_ v103,v39,v103
Mvav 88,196,168
S_ s56,s76,s84
Ta 188,v2,57344
Ta 190,v2,59392
Mvav 88,220,188
buffer_load_dwordx4 a[20:23], v6, s[28:31], s56 offen offset:0 sc1
Zvav 84,220,184
S_ m0,s51,16384
C_ 131,130,131
C_ 130,128,129
Mvav 84,196,164
C_ 132,132,133
C_ 133,134,135
Mvav 84,192,144
Zvav 80,192,140
buffer_load_dwordx4 a[24:27], v6, s[28:31], s56 offen offset:1024 sc1
Mvav 80,196,160
Mvav 80,220,180
Zvav 76,220,176
Mvav 76,196,156
buffer_load_dwordx4 a[28:31], v6, s[28:31], s56 offen offset:2048 sc1
Mvav 76,192,136
W_ 0
Zvav 16,180,136
s_nop 0
Ta 192,v3,49152
Ta 194,v3,51200
Mvav 16,184,156
Mvav 16,188,176
s_nop 0
Ta 196,v3,53248
Ta 198,v3,55296
Zvav 20,188,180
buffer_load_dwordx4 a[32:35], v6, s[28:31], s56 offen offset:3072 sc1
Mvav 20,184,160
S_ s56,s56,4096
Ta 220,v3,57344
Ta 222,v3,59392
Mvav 20,180,140
Zvav 24,180,144
Mvav 24,184,164
buffer_load_dwordx4 a[36:39], v6, s[28:31], s56 offen offset:0 sc1
Mvav 24,188,184
S_ s56,s76,s74
Zvav 28,188,188
Mvav 28,184,168
Mvav 28,180,148
buffer_load_dwordx4 a[60:63], v6, s[28:31], s56 offen offset:0 sc1
Zvav 32,180,152
S_ s56,s76,s82
Mvav 32,184,172
Mvav 32,188,192
s_waitcnt vmcnt(25) lgkmcnt(0)
s_barrier
Zvav 52,220,192
s_nop 0
Ta 180,%0,36864
Ta 182,%0,38912
Mvav 52,196,172
buffer_load_dwordx4 a[64:67], v6, s[28:31], s56 offen offset:0 sc1
Mvav 52,192,152
s_nop 0
Ta 184,%0,40960
Ta 186,%0,43008
Zvav 48,192,148
Mvav 48,196,168
s_nop 0
Ta 188,%0,45056
Ta 190,%0,47104
Mvav 48,220,188
buffer_store_dwordx4 v[130:133], v6, s[28:31], s73 offen offset:0 sc1
Zvav 44,220,184
s_sub_u32 s73, s73, 4096
Mvav 44,196,164
C_ 123,122,123
C_ 122,120,121
Mvav 44,192,144
C_ 124,124,125
C_ 125,126,127
Zvav 40,192,140
Mvav 40,196,160
Mvav 40,220,180
Zvav 36,220,176
Mvav 36,196,156
buffer_store_dwordx4 v[122:125], v6, s[28:31], s73 offen offset:3072 sc1
Mvav 36,192,136
W_ 0
C_ 115,114,115
C_ 114,112,113
Mvav 56,180,196
s_nop 0
Ta 192,v1,36864
Ta 194,v1,38912
Mvav 56,184,216
C_ 116,116,117
C_ 117,118,119
Mvav 56,188,236
s_nop 0
Ta 196,v1,40960
Ta 198,v1,43008
Mvav 60,188,240
Mvav 60,184,220
s_nop 0
Ta 220,v1,45056
Ta 222,v1,47104
Mvav 60,180,200
Mvav 64,180,204
buffer_store_dwordx4 v[114:117], v6, s[28:31], s73 offen offset:2048 sc1
Mvav 64,184,224
C_ 107,106,107
C_ 106,104,105
Mvav 64,188,244
C_ 108,108,109
C_ 109,110,111
Mvav 68,188,248
Mvav 68,184,228
Mvav 68,180,208
Mvav 72,180,212
Mvav 72,184,232
Mvav 72,188,252
W_ 0
Mvav 92,220,252
s_nop 0
Ta 180,v2,36864
Ta 182,v2,38912
Mvav 92,196,232
buffer_store_dwordx4 v[106:109], v6, s[28:31], s73 offen offset:1024 sc1
Mvav 92,192,212
s_nop 0
Ta 184,v2,40960
Ta 186,v2,43008
Mvav 88,192,208
C_ 99,98,99
C_ 98,96,97
Mvav 88,196,228
s_nop 0
Ta 188,v2,45056
Ta 190,v2,47104
Mvav 88,220,248
C_ 100,100,101
C_ 101,102,103
Mvav 84,220,244
Mvav 84,196,224
Mvav 84,192,204
Mvav 80,192,200
buffer_store_dwordx4 v[98:101], v6, s[28:31], s73 offen offset:0 sc1
Mvav 80,196,220
Mvav 80,220,240
Mvav 76,220,236
Mvav 76,196,216
Mvav 76,192,196
W_ 0
Mvav 16,180,196
s_nop 0
Ta 192,v3,36864
Ta 194,v3,38912
Mvav 16,184,216
Mvav 16,188,236
s_nop 0
Ta 196,v3,40960
Ta 198,v3,43008
Mvav 20,188,240
L_ v7,24,s55,0
Mvav 20,184,220
S_ s55,s71,542720
Ta 220,v3,45056
Ta 222,v3,47104
Mvav 20,180,200
S_ m0,s51,17408
Mvav 24,180,204
Mvav 24,184,224
L_ v7,24,s55,0
Mvav 24,188,244
S_ s55,s71,614400
Mvav 28,188,248
S_ m0,s51,49152
Mvav 28,184,228
Mvav 28,180,208
L_ v7,24,s55,0
Mvav 32,180,212
S_ s55,s71,624640
Mvav 32,184,232
S_ m0,s51,50176
Mvav 32,188,252
s_waitcnt vmcnt(28) lgkmcnt(0)
s_barrier
Mvav 52,220,252
s_nop 0
Ta 180,%0,24576
Ta 182,%0,26624
Mvav 52,196,232
L_ v7,24,s55,0
Mvav 52,192,212
S_ s55,s71,634880
Ta 184,%0,28672
Ta 186,%0,30720
Mvav 48,192,208
S_ m0,s51,51200
Mvav 48,196,228
s_nop 0
Ta 188,%0,32768
Ta 190,%0,34816
Mvav 48,220,248
L_ v7,24,s55,0
Mvav 44,220,244
S_ s55,s71,645120
Mvav 44,196,224
S_ m0,s51,52224
Mvav 44,192,204
Mvav 40,192,200
L_ v7,24,s55,0
Mvav 40,196,220
S_ s55,s71,655360
Mvav 40,220,240
S_ m0,s51,53248
Mvav 36,220,236
Mvav 36,196,216
L_ v7,24,s55,0
Mvav 36,192,196
W_ 0
S_ s55,s71,665600
Mvaa 56,180,0
S_ m0,s51,54272
Ta 192,v1,24576
Ta 194,v1,26624
Mvaa 56,184,40
Mvaa 56,188,80
s_nop 0
Ta 196,v1,28672
Ta 198,v1,30720
Mvaa 60,188,84
L_ v7,24,s55,0
Mvaa 60,184,44
S_ s55,s71,0
Ta 220,v1,32768
Ta 222,v1,34816
Mvaa 60,180,4
S_ m0,s51,36864
Mvaa 64,180,8
Mvaa 64,184,48
L_ v7,24,s55,0
Mvaa 64,188,88
S_ s55,s71,10240
Mvaa 68,188,240
S_ m0,s51,37888
Mvaa 68,184,52
Mvaa 68,180,12
L_ v7,24,s55,0
Mvaa 72,180,16
S_ s55,s71,20480
Mvaa 72,184,56
S_ m0,s51,38912
Mvaa 72,188,244
W_ 0
Mvaa 92,220,244
s_nop 0
Ta 180,v2,24576
Ta 182,v2,26624
Mvaa 92,196,56
L_ v7,24,s55,0
Mvaa 92,192,16
S_ s55,s71,30720
Ta 184,v2,28672
Ta 186,v2,30720
Mvaa 88,192,12
S_ m0,s51,39936
Mvaa 88,196,52
s_nop 0
Ta 188,v2,32768
Ta 190,v2,34816
Mvaa 88,220,240
L_ v7,24,s55,0
Mvaa 84,220,88
S_ s55,s71,40960
Mvaa 84,196,48
S_ m0,s51,40960
Mvaa 84,192,8
Mvaa 80,192,4
L_ v7,24,s55,0
Mvaa 80,196,44
S_ s55,s71,51200
Mvaa 80,220,84
S_ m0,s51,41984
Mvaa 76,220,80
Mvaa 76,196,40
L_ v7,24,s55,0
Mvaa 76,192,0
W_ 0
Mvaa 16,180,0
s_nop 0
Ta 192,v3,24576
Ta 194,v3,26624
Mvaa 16,184,40
Mvaa 16,188,80
s_nop 0
Ta 196,v3,28672
Ta 198,v3,30720
Mvaa 20,188,84
Mvaa 20,184,44
s_nop 0
Ta 220,v3,32768
Ta 222,v3,34816
Mvaa 20,180,4
Mvaa 24,180,8
Mvaa 24,184,48
Mvaa 24,188,88
Mvaa 28,188,240
Mvaa 28,184,52
Mvaa 28,180,12
Mvaa 32,180,16
Mvaa 32,184,56
Mvaa 32,188,244
s_waitcnt vmcnt(30) lgkmcnt(0)
s_barrier
Mvaa 52,220,244
s_nop 0
Ta 180,%0,0
Ta 182,%0,2048
Mvaa 52,196,56
Mvaa 52,192,16
s_nop 0
Ta 184,%0,4096
Ta 186,%0,6144
Mvaa 48,192,12
Mvaa 48,196,52
s_nop 0
Ta 188,%0,8192
Ta 190,%0,10240
Mvaa 48,220,240
Mvaa 44,220,88
Mvaa 44,196,48
Mvaa 44,192,8
Mvaa 40,192,4
Mvaa 40,196,44
Mvaa 40,220,84
Mvaa 36,220,80
Mvaa 36,196,40
Mvaa 36,192,0
W_ 0
Mvaa 56,180,120
S_ s81,s6,6
Ta 192,v1,0
Ta 194,v1,2048
Mvaa 56,184,160
s_and_b32 s81, s81, 7
Mvaa 56,188,200
s_mul_i32 s81, s81, 20480
Ta 196,v1,4096
Ta 198,v1,6144
Mvaa 60,188,204
s_mov_b32 m0, s52
S_ s73,s81,s78
Mvaa 60,184,164
S_ s73,s73,4096
Ta 220,v1,8192
Ta 222,v1,10240
Mvaa 60,180,124
s_nop 0
Y_ 56,0
Y_ 57,256
Mvaa 64,180,128
s_nop 0
Y_ 58,512
Y_ 59,768
Mvaa 64,184,168
.Lpad_o_9:
s_cbranch_execz .Lhop_o_r
Mvaa 64,188,208
s_nop 0
Y_ 60,1024
Y_ 61,1280
Mvaa 68,188,212
s_nop 0
Y_ 62,1536
Y_ 63,1792
Mvaa 68,184,172
Mvaa 68,180,132
s_nop 0
Y_ 64,2048
Y_ 65,2304
Mvaa 72,180,136
s_nop 0
Y_ 66,2560
Y_ 67,2816
Mvaa 72,184,176
Mvaa 72,188,216
W_ 12
Y_ 68,3072
Y_ 69,3328
Mvaa 92,220,216
s_nop 0
Ta 180,v2,0
Ta 182,v2,2048
Mvaa 92,196,176
s_nop 0
Y_ 70,3584
Y_ 71,3840
Mvaa 92,192,136
s_nop 0
Ta 184,v2,4096
Ta 186,v2,6144
Mvaa 88,192,132
s_nop 0
Y_ 72,4096
Y_ 73,4352
Mvaa 88,196,172
S_ s83,s6,5
Ta 188,v2,8192
Ta 190,v2,10240
Mvaa 88,220,212
s_and_b32 s83, s83, 7
Y_ 74,4608
Y_ 75,4864
Mvaa 84,220,208
s_lshl_b32 s83, s83, 2
Y_ 92,9216
Y_ 93,9472
Mvaa 84,196,168
s_nop 0
Y_ 94,9728
Y_ 95,9984
Mvaa 84,192,128
s_nop 0
Y_ 88,8192
Y_ 89,8448
Mvaa 80,192,124
s_nop 0
Y_ 90,8704
Y_ 91,8960
Mvaa 80,196,164
v_cmp_eq_u32_e64 s[36:37], s83, v8
Mvaa 80,220,204
s_sub_u32 s75, s6, 4
Y_ 84,7168
Y_ 85,7424
Mvaa 76,220,200
s_and_b32 s75, s75, 7
Y_ 86,7680
Y_ 87,7936
Mvaa 76,196,160
v_mov_b32 v10, 0x7fff0000
Mvaa 76,192,120
W_ 14
)A"
R"A(Y_ 80,6144
Y_ 81,6400
Mvaa 16,180,120
s_nop 0
Ta 192,v3,0
Ta 194,v3,2048
Mvaa 16,184,160
s_nop 0
Y_ 82,6656
Y_ 83,6912
Mvaa 16,188,200
s_nop 0
Ta 196,v3,4096
Ta 198,v3,6144
Mvaa 20,188,204
s_nop 0
Y_ 76,5120
Y_ 77,5376
Mvaa 20,184,164
s_nop 0
Ta 220,v3,8192
Ta 222,v3,10240
Mvaa 20,180,124
s_nop 0
Y_ 78,5632
Y_ 79,5888
Mvaa 24,180,128
buffer_load_dword v11, v8, s[92:95], s96 offen offset:384 sc1
Mvaa 24,184,168
v_cndmask_b32_e64 v10, v10, v9, s[36:37]
Mvaa 24,188,208
s_waitcnt vmcnt(13) lgkmcnt(0)
s_barrier
Mvaa 28,188,212
S_ s55,s71,122880
ds_read2st64_b32 v[96:97], v5 offset0:0 offset1:1
Mvaa 28,184,172
buffer_store_dword a255, v10, s[92:95], 0 offen offset:384 sc1
Mvaa 28,180,132
S_ m0,s51,24576
ds_read2st64_b32 v[98:99], v5 offset0:2 offset1:3
Mvaa 32,180,136
ds_read2st64_b32 v[104:105], v5 offset0:4 offset1:5
Mvaa 32,184,176
ds_read2st64_b32 v[106:107], v5 offset0:6 offset1:7
Mvaa 32,188,216
L_ v7,24,s55,0
Mvaa 52,220,216
S_ s55,s71,133120
Ta 180,%0,12288
Ta 182,%0,14336
Mvaa 52,196,176
S_ m0,s51,25600
ds_read2st64_b32 v[112:113], v5 offset0:8 offset1:9
Mvaa 52,192,136
W_ 5
Ta 184,%0,16384
Ta 186,%0,18432
Mvaa 48,192,132
L_ v7,24,s55,0
Mvaa 48,196,172
S_ s55,s71,143360
Ta 188,%0,20480
Ta 190,%0,22528
Mvaa 48,220,212
S_ m0,s51,26624
ds_read2st64_b32 v[114:115], v5 offset0:10 offset1:11
A_ v96,v16,v96
Mvaa 44,220,208
ds_read2st64_b32 v[120:121], v5 offset0:12 offset1:13
A_ v97,v17,v97
Mvaa 44,196,168
L_ v7,24,s55,0
Mvaa 44,192,128
S_ s55,s71,153600
ds_read2st64_b32 v[122:123], v5 offset0:14 offset1:15
A_ v98,v18,v98
Mvaa 40,192,124
S_ m0,s51,27648
ds_read2st64_b32 v[128:129], v5 offset0:16 offset1:17
A_ v99,v19,v99
Mvaa 40,196,164
W_ 11
ds_read2st64_b32 v[130:131], v5 offset0:18 offset1:19
A_ v104,v20,v104
Mvaa 40,220,204
L_ v7,24,s55,0
Mvaa 36,220,200
S_ s55,s71,163840
ds_read2st64_b32 v[132:133], v5 offset0:36 offset1:37
A_ v105,v21,v105
Mvaa 36,196,160
S_ m0,s51,28672
ds_read2st64_b32 v[134:135], v5 offset0:38 offset1:39
A_ v106,v22,v106
Mvaa 36,192,120
ds_read2st64_b32 v[124:125], v5 offset0:32 offset1:33
A_ v107,v23,v107
s_waitcnt vmcnt(25)
Maaa 100,70,20
L_ v7,24,s55,0
Maaa 140,68,20
W_ 7
ds_read2st64_b32 v[126:127], v5 offset0:34 offset1:35
A_ v112,v24,v112
Maaa 104,70,24
S_ s55,s71,174080
ds_read2st64_b32 v[116:117], v5 offset0:28 offset1:29
A_ v113,v25,v113
Maaa 144,68,24
S_ m0,s51,29696
ds_read2st64_b32 v[118:119], v5 offset0:30 offset1:31
A_ v114,v26,v114
Maaa 108,70,28
L_ v7,24,s55,0
Maaa 148,68,28
S_ s79,s6,8
ds_read2st64_b32 v[108:109], v5 offset0:24 offset1:25
A_ v115,v27,v115
Maaa 112,70,32
W_ 9
ds_read2st64_b32 v[110:111], v5 offset0:26 offset1:27
A_ v120,v28,v120
Maaa 152,68,32
s_and_b32 s79, s79, 7
ds_read2st64_b32 v[100:101], v5 offset0:20 offset1:21
A_ v121,v29,v121
Maaa 116,70,36
s_lshl_b32 s79, s79, 7
ds_read2st64_b32 v[102:103], v5 offset0:22 offset1:23
A_ v122,v30,v122
Maaa 156,68,36
S_ s71,s72,s79
A_ v123,v31,v123
Maaa 224,70,60
W_ 10
A_ v128,v32,v128
A_ v129,v33,v129
Maaa 228,68,60
S_ s55,s71,491520
A_ v130,v34,v130
A_ v131,v35,v131
Maaa 232,70,64
W_ 8
A_ v132,v52,v132
A_ v133,v53,v133
Maaa 236,68,64
A_ v134,v54,v134
A_ v135,v55,v135
Zvav 56,180,136
W_ 6
Ta 192,v1,12288
Ta 194,v1,14336
Mvav 56,184,156
S_ m0,s51,0
A_ v124,v48,v124
A_ v125,v49,v125
Mvav 56,188,176
s_nop 0
Ta 196,v1,16384
Ta 198,v1,18432
Zvav 60,188,180
L_ v7,24,s55,0
Mvav 60,184,160
S_ s55,s71,501760
Ta 220,v1,20480
Ta 222,v1,22528
Mvav 60,180,140
S_ m0,s51,1024
A_ v126,v50,v126
A_ v127,v51,v127
Zvav 64,180,144
W_ 10
A_ v116,v44,v116
A_ v117,v45,v117
Mvav 64,184,164
L_ v7,24,s55,0
Mvav 64,188,184
S_ s55,s71,512000
A_ v118,v46,v118
A_ v119,v47,v119
Zvav 68,188,188
W_ 8
A_ v108,v40,v108
A_ v109,v41,v109
Mvav 68,184,168
S_ m0,s51,2048
A_ v110,v42,v110
A_ v111,v43,v111
Mvav 68,180,148
L_ v7,24,s55,0
Zvav 72,180,152
s_waitcnt vmcnt(10)
W_ 7
A_ v100,v36,v100
W_ 6
Mvav 72,184,172
S_ s55,s71,522240
v_cmp_eq_u32 vcc, s62, v11
A_ v101,v37,v101
Mvav 72,188,192
W_ 0
A_ v102,v38,v102
s_mul_i32 s76, s75, 204800
Zvav 92,220,192
S_ m0,s51,3072
Ta 180,v2,12288
Ta 182,v2,14336
Mvav 92,196,172
L_ v7,24,s55,0
Mvav 92,192,152
S_ s55,s71,532480
Ta 184,v2,16384
Ta 186,v2,18432
Zvav 88,192,148
s_bitcmp1_b32 vcc_lo, s75
s_cbranch_scc0 .Lspin3
.Lspun3:
A_ v103,v39,v103
Mvav 88,196,168
S_ s56,s76,s84
Ta 188,v2,20480
Ta 190,v2,22528
Mvav 88,220,188
buffer_load_dwordx4 a[20:23], v6, s[28:31], s56 offen offset:0 sc1
Zvav 84,220,184
S_ m0,s51,4096
C_ 131,130,131
C_ 130,128,129
Mvav 84,196,164
C_ 132,132,133
C_ 133,134,135
Mvav 84,192,144
Zvav 80,192,140
buffer_load_dwordx4 a[24:27], v6, s[28:31], s56 offen offset:1024 sc1
Mvav 80,196,160
Mvav 80,220,180
Zvav 76,220,176
Mvav 76,196,156
buffer_load_dwordx4 a[28:31], v6, s[28:31], s56 offen offset:2048 sc1
Mvav 76,192,136
W_ 0
Zvav 16,180,136
s_nop 0
Ta 192,v3,12288
Ta 194,v3,14336
Mvav 16,184,156
Mvav 16,188,176
s_nop 0
Ta 196,v3,16384
Ta 198,v3,18432
Zvav 20,188,180
buffer_load_dwordx4 a[32:35], v6, s[28:31], s56 offen offset:3072 sc1
Mvav 20,184,160
S_ s56,s56,4096
Ta 220,v3,20480
Ta 222,v3,22528
Mvav 20,180,140
Zvav 24,180,144
Mvav 24,184,164
buffer_load_dwordx4 a[36:39], v6, s[28:31], s56 offen offset:0 sc1
Mvav 24,188,184
S_ s56,s76,s74
Zvav 28,188,188
Mvav 28,184,168
Mvav 28,180,148
buffer_load_dwordx4 a[60:63], v6, s[28:31], s56 offen offset:0 sc1
Zvav 32,180,152
S_ s56,s76,s82
Mvav 32,184,172
Mvav 32,188,192
s_waitcnt vmcnt(24) lgkmcnt(0)
s_barrier
Zvav 52,220,192
s_nop 0
Ta 180,%0,49152
Ta 182,%0,51200
Mvav 52,196,172
buffer_load_dwordx4 a[64:67], v6, s[28:31], s56 offen offset:0 sc1
Mvav 52,192,152
s_nop 0
Ta 184,%0,53248
Ta 186,%0,55296
Zvav 48,192,148
Mvav 48,196,168
s_nop 0
Ta 188,%0,57344
Ta 190,%0,59392
Mvav 48,220,188
buffer_store_dwordx4 v[130:133], v6, s[28:31], s73 offen offset:0 sc1
Zvav 44,220,184
s_sub_u32 s73, s73, 4096
Mvav 44,196,164
C_ 123,122,123
C_ 122,120,121
Mvav 44,192,144
C_ 124,124,125
C_ 125,126,127
Zvav 40,192,140
Mvav 40,196,160
Mvav 40,220,180
Zvav 36,220,176
Mvav 36,196,156
buffer_store_dwordx4 v[122:125], v6, s[28:31], s73 offen offset:3072 sc1
Mvav 36,192,136
W_ 0
C_ 115,114,115
C_ 114,112,113
Mvav 56,180,196
s_nop 0
Ta 192,v1,49152
Ta 194,v1,51200
Mvav 56,184,216
C_ 116,116,117
C_ 117,118,119
Mvav 56,188,236
s_nop 0
Ta 196,v1,53248
Ta 198,v1,55296
Mvav 60,188,240
Mvav 60,184,220
s_nop 0
Ta 220,v1,57344
Ta 222,v1,59392
Mvav 60,180,200
Mvav 64,180,204
buffer_store_dwordx4 v[114:117], v6, s[28:31], s73 offen offset:2048 sc1
Mvav 64,184,224
C_ 107,106,107
C_ 106,104,105
Mvav 64,188,244
C_ 108,108,109
C_ 109,110,111
Mvav 68,188,248
Mvav 68,184,228
Mvav 68,180,208
Mvav 72,180,212
Mvav 72,184,232
Mvav 72,188,252
W_ 0
Mvav 92,220,252
s_nop 0
Ta 180,v2,49152
Ta 182,v2,51200
Mvav 92,196,232
buffer_store_dwordx4 v[106:109], v6, s[28:31], s73 offen offset:1024 sc1
Mvav 92,192,212
s_nop 0
Ta 184,v2,53248
Ta 186,v2,55296
Mvav 88,192,208
C_ 99,98,99
C_ 98,96,97
Mvav 88,196,228
s_nop 0
Ta 188,v2,57344
Ta 190,v2,59392
Mvav 88,220,248
C_ 100,100,101
C_ 101,102,103
Mvav 84,220,244
Mvav 84,196,224
Mvav 84,192,204
Mvav 80,192,200
buffer_store_dwordx4 v[98:101], v6, s[28:31], s73 offen offset:0 sc1
Mvav 80,196,220
Mvav 80,220,240
Mvav 76,220,236
Mvav 76,196,216
Mvav 76,192,196
W_ 0
Mvav 16,180,196
s_nop 0
Ta 192,v3,49152
Ta 194,v3,51200
Mvav 16,184,216
Mvav 16,188,236
s_nop 0
Ta 196,v3,53248
Ta 198,v3,55296
Mvav 20,188,240
L_ v7,24,s55,0
Mvav 20,184,220
S_ s55,s71,542720
Ta 220,v3,57344
Ta 222,v3,59392
Mvav 20,180,200
S_ m0,s51,5120
Mvav 24,180,204
Mvav 24,184,224
L_ v7,24,s55,0
Mvav 24,188,244
S_ s55,s71,614400
Mvav 28,188,248
S_ m0,s51,12288
Mvav 28,184,228
Mvav 28,180,208
L_ v7,24,s55,0
Mvav 32,180,212
S_ s55,s71,624640
Mvav 32,184,232
S_ m0,s51,13312
Mvav 32,188,252
s_waitcnt vmcnt(27) lgkmcnt(0)
s_barrier
Mvav 52,220,252
s_nop 0
Ta 180,%0,36864
Ta 182,%0,38912
Mvav 52,196,232
L_ v7,24,s55,0
Mvav 52,192,212
S_ s55,s71,634880
Ta 184,%0,40960
Ta 186,%0,43008
Mvav 48,192,208
S_ m0,s51,14336
Mvav 48,196,228
s_nop 0
Ta 188,%0,45056
Ta 190,%0,47104
Mvav 48,220,248
L_ v7,24,s55,0
Mvav 44,220,244
s_sub_u32 s75, s6, 5
S_ s55,s71,645120
Mvav 44,196,224
s_and_b32 s75, s75, 7
S_ m0,s51,15360
Mvav 44,192,204
Mvav 40,192,200
buffer_load_dword v11, v8, s[92:95], s96 offen offset:384 sc1
Mvav 40,196,220
Mvav 40,220,240
Mvav 36,220,236
Mvav 36,196,216
L_ v7,24,s55,0
Mvav 36,192,196
W_ 0
S_ s55,s71,655360
Mvaa 56,180,0
S_ m0,s51,16384
Ta 192,v1,36864
Ta 194,v1,38912
Mvaa 56,184,40
Mvaa 56,188,80
s_nop 0
Ta 196,v1,40960
Ta 198,v1,43008
Mvaa 60,188,84
L_ v7,24,s55,0
Mvaa 60,184,44
S_ s55,s71,665600
Ta 220,v1,45056
Ta 222,v1,47104
Mvaa 60,180,4
S_ m0,s51,17408
Mvaa 64,180,8
Mvaa 64,184,48
L_ v7,24,s55,0
Mvaa 64,188,88
S_ s55,s71,0
Mvaa 68,188,240
S_ m0,s51,49152
Mvaa 68,184,52
Mvaa 68,180,12
L_ v7,24,s55,0
Mvaa 72,180,16
S_ s55,s71,10240
Mvaa 72,184,56
S_ m0,s51,50176
Mvaa 72,188,244
W_ 0
Mvaa 92,220,244
s_nop 0
Ta 180,v2,36864
Ta 182,v2,38912
Mvaa 92,196,56
L_ v7,24,s55,0
Mvaa 92,192,16
S_ s55,s71,20480
Ta 184,v2,40960
Ta 186,v2,43008
Mvaa 88,192,12
S_ m0,s51,51200
Mvaa 88,196,52
s_nop 0
Ta 188,v2,45056
Ta 190,v2,47104
Mvaa 88,220,240
L_ v7,24,s55,0
Mvaa 84,220,88
S_ s55,s71,30720
Mvaa 84,196,48
S_ m0,s51,52224
Mvaa 84,192,8
.Lpad_o_10:
s_cbranch_execz .Lhop_o_r
Mvaa 80,192,4
L_ v7,24,s55,0
Mvaa 80,196,44
S_ s83,s6,6
S_ s55,s71,40960
Mvaa 80,220,84
s_and_b32 s83, s83, 7
S_ m0,s51,53248
Mvaa 76,220,80
s_lshl_b32 s83, s83, 2
Mvaa 76,196,40
L_ v7,24,s55,0
Mvaa 76,192,0
W_ 0
v_cmp_eq_u32_e64 s[36:37], s83, v8
S_ s55,s71,51200
Mvaa 16,180,0
S_ m0,s51,54272
Ta 192,v3,36864
Ta 194,v3,38912
Mvaa 16,184,40
v_mov_b32 v10, 0x7fff0000
Mvaa 16,188,80
s_nop 0
Ta 196,v3,40960
Ta 198,v3,43008
Mvaa 20,188,84
L_ v7,24,s55,0
Mvaa 20,184,44
s_nop 0
Ta 220,v3,45056
Ta 222,v3,47104
Mvaa 20,180,4
v_cndmask_b32_e64 v10, v10, v9, s[36:37]
Mvaa 24,180,8
Mvaa 24,184,48
Mvaa 24,188,88
Mvaa 28,188,240
Mvaa 28,184,52
Mvaa 28,180,12
Mvaa 32,180,16
Mvaa 32,184,56
Mvaa 32,188,244
s_waitcnt vmcnt(15) lgkmcnt(0)
s_barrier
Mvaa 52,220,244
s_nop 0
Ta 180,%0,24576
Ta 182,%0,26624
Mvaa 52,196,56
buffer_store_dword a255, v10, s[92:95], 0 offen offset:384 sc1
Mvaa 52,192,16
s_nop 0
Ta 184,%0,28672
Ta 186,%0,30720
Mvaa 48,192,12
Mvaa 48,196,52
s_nop 0
Ta 188,%0,32768
Ta 190,%0,34816
Mvaa 48,220,240
Mvaa 44,220,88
Mvaa 44,196,48
Mvaa 44,192,8
Mvaa 40,192,4
Mvaa 40,196,44
Mvaa 40,220,84
Mvaa 36,220,80
Mvaa 36,196,40
Mvaa 36,192,0
W_ 0
Mvaa 56,180,120
S_ s81,s6,7
Ta 192,v1,24576
Ta 194,v1,26624
Mvaa 56,184,160
s_and_b32 s81, s81, 7
Mvaa 56,188,200
s_mul_i32 s81, s81, 20480
Ta 196,v1,28672
Ta 198,v1,30720
Mvaa 60,188,204
s_mov_b32 m0, s52
S_ s73,s81,s78
Mvaa 60,184,164
S_ s73,s73,4096
Ta 220,v1,32768
Ta 222,v1,34816
Mvaa 60,180,124
s_nop 0
Y_ 56,0
Y_ 57,256
Mvaa 64,180,128
s_nop 0
Y_ 58,512
Y_ 59,768
Mvaa 64,184,168
Mvaa 64,188,208
s_nop 0
Y_ 60,1024
Y_ 61,1280
Mvaa 68,188,212
s_nop 0
Y_ 62,1536
Y_ 63,1792
Mvaa 68,184,172
Mvaa 68,180,132
s_nop 0
Y_ 64,2048
Y_ 65,2304
Mvaa 72,180,136
s_nop 0
Y_ 66,2560
Y_ 67,2816
Mvaa 72,184,176
Mvaa 72,188,216
W_ 12
Y_ 68,3072
Y_ 69,3328
Mvaa 92,220,216
s_nop 0
Ta 180,v2,24576
Ta 182,v2,26624
Mvaa 92,196,176
s_nop 0
Y_ 70,3584
Y_ 71,3840
Mvaa 92,192,136
s_nop 0
Ta 184,v2,28672
Ta 186,v2,30720
Mvaa 88,192,132
s_nop 0
Y_ 72,4096
Y_ 73,4352
Mvaa 88,196,172
s_nop 0
Ta 188,v2,32768
Ta 190,v2,34816
Mvaa 88,220,212
s_nop 0
Y_ 74,4608
Y_ 75,4864
Mvaa 84,220,208
s_nop 0
Y_ 92,9216
Y_ 93,9472
Mvaa 84,196,168
s_nop 0
Y_ 94,9728
Y_ 95,9984
Mvaa 84,192,128
s_nop 0
Y_ 88,8192
Y_ 89,8448
Mvaa 80,192,124
s_nop 0
Y_ 90,8704
Y_ 91,8960
Mvaa 80,196,164
Mvaa 80,220,204
s_nop 0
Y_ 84,7168
Y_ 85,7424
Mvaa 76,220,200
s_nop 0
Y_ 86,7680
Y_ 87,7936
Mvaa 76,196,160
Mvaa 76,192,120
W_ 14
Y_ 80,6144
Y_ 81,6400
Mvaa 16,180,120
s_nop 0
Ta 192,v3,24576
Ta 194,v3,26624
Mvaa 16,184,160
s_nop 0
Y_ 82,6656
Y_ 83,6912
Mvaa 16,188,200
s_nop 0
Ta 196,v3,28672
Ta 198,v3,30720
Mvaa 20,188,204
s_nop 0
Y_ 76,5120
Y_ 77,5376
Mvaa 20,184,164
s_nop 0
Ta 220,v3,32768
Ta 222,v3,34816
Mvaa 20,180,124
s_nop 0
Y_ 78,5632
Y_ 79,5888
Mvaa 24,180,128
Mvaa 24,184,168
Mvaa 24,188,208
s_waitcnt vmcnt(14) lgkmcnt(0)
s_barrier
Mvaa 28,188,212
S_ s55,s71,122880
ds_read2st64_b32 v[96:97], v5 offset0:0 offset1:1
Mvaa 28,184,172
S_ m0,s51,36864
ds_read2st64_b32 v[98:99], v5 offset0:2 offset1:3
Mvaa 28,180,132
L_ v7,24,s55,0
Mvaa 32,180,136
S_ s55,s71,133120
ds_read2st64_b32 v[104:105], v5 offset0:4 offset1:5
Mvaa 32,184,176
s_waitcnt vmcnt(11)
ds_read2st64_b32 v[106:107], v5 offset0:6 offset1:7
S_ m0,s51,37888
Mvaa 32,188,216
ds_read2st64_b32 v[112:113], v5 offset0:8 offset1:9
v_cmp_eq_u32 vcc, s62, v11
Mvaa 52,220,216
s_mul_i32 s76, s75, 204800
Ta 180,%0,0
Ta 182,%0,2048
Mvaa 52,196,176
L_ v7,24,s55,0
Mvaa 52,192,136
W_ 5
Ta 184,%0,4096
Ta 186,%0,6144
Mvaa 48,192,132
s_bitcmp1_b32 vcc_lo, s75
s_cbranch_scc0 .Lspin4
.Lspun4:
ds_read2st64_b32 v[114:115], v5 offset0:10 offset1:11
Mvaa 48,196,172
S_ s55,s71,143360
Ta 188,%0,8192
Ta 190,%0,10240
Mvaa 48,220,212
S_ m0,s51,38912
ds_read2st64_b32 v[120:121], v5 offset0:12 offset1:13
A_ v96,v16,v96
Mvaa 44,220,208
L_ v7,24,s55,0
Mvaa 44,196,168
S_ s55,s71,153600
ds_read2st64_b32 v[122:123], v5 offset0:14 offset1:15
A_ v97,v17,v97
Mvaa 44,192,128
S_ m0,s51,39936
ds_read2st64_b32 v[128:129], v5 offset0:16 offset1:17
A_ v98,v18,v98
Mvaa 40,192,124
ds_read2st64_b32 v[130:131], v5 offset0:18 offset1:19
A_ v99,v19,v99
Mvaa 40,196,164
L_ v7,24,s55,0
Mvaa 40,220,204
W_ 12
ds_read2st64_b32 v[132:133], v5 offset0:36 offset1:37
A_ v104,v20,v104
Mvaa 36,220,200
S_ s55,s71,163840
ds_read2st64_b32 v[134:135], v5 offset0:38 offset1:39
A_ v105,v21,v105
Mvaa 36,196,160
S_ m0,s51,40960
ds_read2st64_b32 v[124:125], v5 offset0:32 offset1:33
A_ v106,v22,v106
Mvaa 36,192,120
L_ v7,24,s55,0
s_waitcnt vmcnt(26)
Maaa 100,70,20
S_ s55,s71,174080
ds_read2st64_b32 v[126:127], v5 offset0:34 offset1:35
A_ v107,v23,v107
Maaa 140,68,20
W_ 10
ds_read2st64_b32 v[116:117], v5 offset0:28 offset1:29
A_ v112,v24,v112
Maaa 104,70,24
S_ m0,s51,41984
ds_read2st64_b32 v[118:119], v5 offset0:30 offset1:31
A_ v113,v25,v113
Maaa 144,68,24
L_ v7,24,s55,0
Maaa 108,70,28
ds_read2st64_b32 v[108:109], v5 offset0:24 offset1:25
A_ v114,v26,v114
Maaa 148,68,28
ds_read2st64_b32 v[110:111], v5 offset0:26 offset1:27
A_ v115,v27,v115
Maaa 112,70,32
W_ 10
ds_read2st64_b32 v[100:101], v5 offset0:20 offset1:21
A_ v120,v28,v120
Maaa 152,68,32
ds_read2st64_b32 v[102:103], v5 offset0:22 offset1:23
A_ v121,v29,v121
Maaa 116,70,36
A_ v122,v30,v122
A_ v123,v31,v123
Maaa 156,68,36
W_ 10
A_ v128,v32,v128
A_ v129,v33,v129
Maaa 224,70,60
A_ v130,v34,v130
A_ v131,v35,v131
Maaa 228,68,60
W_ 8
A_ v132,v52,v132
A_ v133,v53,v133
Maaa 232,70,64
A_ v134,v54,v134
A_ v135,v55,v135
Maaa 236,68,64
W_ 7
A_ v124,v48,v124
W_ 6
Zvav 56,180,136
s_nop 0
Ta 192,v1,0
Ta 194,v1,2048
Mvav 56,184,156
A_ v125,v49,v125
A_ v126,v50,v126
Mvav 56,188,176
S_ s56,s76,s84
Ta 196,v1,4096
Ta 198,v1,6144
Zvav 60,188,180
buffer_load_dwordx4 a[20:23], v6, s[28:31], s56 offen offset:0 sc1
Mvav 60,184,160
s_nop 0
Ta 220,v1,8192
Ta 222,v1,10240
Mvav 60,180,140
A_ v127,v51,v127
C_ 131,130,131
Zvav 64,180,144
W_ 10
A_ v116,v44,v116
A_ v117,v45,v117
Mvav 64,184,164
buffer_load_dwordx4 a[24:27], v6, s[28:31], s56 offen offset:1024 sc1
Mvav 64,188,184
A_ v118,v46,v118
A_ v119,v47,v119
Zvav 68,188,188
W_ 8
A_ v108,v40,v108
A_ v109,v41,v109
Mvav 68,184,168
A_ v110,v42,v110
A_ v111,v43,v111
Mvav 68,180,148
buffer_load_dwordx4 a[28:31], v6, s[28:31], s56 offen offset:2048 sc1
Zvav 72,180,152
W_ 6
A_ v100,v36,v100
A_ v101,v37,v101
Mvav 72,184,172
A_ v102,v38,v102
A_ v103,v39,v103
Mvav 72,188,192
W_ 0
C_ 130,128,129
C_ 132,132,133
Zvav 92,220,192
s_nop 0
Ta 180,v2,0
Ta 182,v2,2048
Mvav 92,196,172
buffer_load_dwordx4 a[32:35], v6, s[28:31], s56 offen offset:3072 sc1
Mvav 92,192,152
S_ s56,s56,4096
Ta 184,v2,4096
Ta 186,v2,6144
Zvav 88,192,148
C_ 133,134,135
Mvav 88,196,168
s_nop 0
Ta 188,v2,8192
Ta 190,v2,10240
Mvav 88,220,188
buffer_load_dwordx4 a[36:39], v6, s[28:31], s56 offen offset:0 sc1
Zvav 84,220,184
S_ s56,s76,s74
Mvav 84,196,164
Mvav 84,192,144
Zvav 80,192,140
buffer_load_dwordx4 a[60:63], v6, s[28:31], s56 offen offset:0 sc1
Mvav 80,196,160
S_ s56,s76,s82
Mvav 80,220,180
Zvav 76,220,176
Mvav 76,196,156
buffer_load_dwordx4 a[64:67], v6, s[28:31], s56 offen offset:0 sc1
Mvav 76,192,136
W_ 0
s_sub_u32 s75, s6, 6
Maav 100,180,136
s_and_b32 s75, s75, 7
Ta 192,v3,0
Ta 194,v3,2048
Maav 100,184,156
Maav 100,188,176
s_nop 0
Ta 196,v3,4096
Ta 198,v3,6144
Maav 104,188,180
buffer_load_dword v11, v8, s[92:95], s96 offen offset:384 sc1
Maav 104,184,160
s_nop 0
Ta 220,v3,8192
Ta 222,v3,10240
Maav 104,180,140
Maav 108,180,144
Maav 108,184,164
buffer_store_dwordx4 v[130:133], v6, s[28:31], s73 offen offset:0 sc1
Maav 108,188,184
s_sub_u32 s73, s73, 4096
Maav 112,188,188
C_ 123,122,123
C_ 122,120,121
Maav 112,184,168
C_ 124,124,125
C_ 125,126,127
Maav 112,180,148
buffer_store_dwordx4 v[122:125], v6, s[28:31], s73 offen offset:3072 sc1
Maav 116,180,152
C_ 115,114,115
C_ 114,112,113
Maav 116,184,172
C_ 116,116,117
C_ 117,118,119
Maav 116,188,192
s_waitcnt vmcnt(23) lgkmcnt(0)
s_barrier
Maav 156,220,192
s_nop 0
Ta 180,%0,12288
Ta 182,%0,14336
Maav 156,196,172
buffer_store_dwordx4 v[114:117], v6, s[28:31], s73 offen offset:2048 sc1
Maav 156,192,152
s_nop 0
Ta 184,%0,16384
Ta 186,%0,18432
Maav 152,192,148
C_ 107,106,107
C_ 106,104,105
Maav 152,196,168
s_nop 0
Ta 188,%0,20480
Ta 190,%0,22528
Maav 152,220,188
C_ 108,108,109
C_ 109,110,111
Maav 148,220,184
buffer_store_dwordx4 v[106:109], v6, s[28:31], s73 offen offset:1024 sc1
Maav 148,196,164
C_ 99,98,99
C_ 98,96,97
Maav 148,192,144
C_ 100,100,101
C_ 101,102,103
Maav 144,192,140
Maav 144,196,160
buffer_store_dwordx4 v[98:101], v6, s[28:31], s73 offen offset:0 sc1
Maav 144,220,180
Maav 140,220,176
Maav 140,196,156
Maav 140,192,136
W_ 0
Mvav 56,180,196
s_nop 0
Ta 192,v1,12288
Ta 194,v1,14336
Mvav 56,184,216
Mvav 56,188,236
s_nop 0
Ta 196,v1,16384
Ta 198,v1,18432
Mvav 60,188,240
Mvav 60,184,220
s_nop 0
Ta 220,v1,20480
Ta 222,v1,22528
Mvav 60,180,200
v_bfe_u32 v4, v6, 4, 4
Mvav 64,180,204
v_mul_u32_u24 v4, 2560, v4
Mvav 64,184,224
v_bfe_u32 v10, v6, 8, 1
Mvav 64,188,244
v_lshl_add_u32 v4, v10, 5, v4
Mvav 68,188,248
v_lshrrev_b32 v10, 9, v6
Mvav 68,184,228
v_lshl_add_u32 v4, v10, 4, v4
Mvav 68,180,208
Mvav 72,180,212
Mvav 72,184,232
Mvav 72,188,252
W_ 0
Mvav 92,220,252
s_nop 0
Ta 180,v2,12288
Ta 182,v2,14336
Mvav 92,196,232
Mvav 92,192,212
s_nop 0
Ta 184,v2,16384
Ta 186,v2,18432
Mvav 88,192,208
Mvav 88,196,228
s_nop 0
Ta 188,v2,20480
Ta 190,v2,22528
Mvav 88,220,248
Mvav 84,220,244
Mvav 84,196,224
Mvav 84,192,204
Mvav 80,192,200
Mvav 80,196,220
Mvav 80,220,240
Mvav 76,220,236
Mvav 76,196,216
Mvav 76,192,196
W_ 0
Maav 100,180,196
s_nop 0
Ta 192,v3,12288
Ta 194,v3,14336
Maav 100,184,216
Maav 100,188,236
s_nop 0
Ta 196,v3,16384
Ta 198,v3,18432
Maav 104,188,240
Maav 104,184,220
s_nop 0
Ta 220,v3,20480
Ta 222,v3,22528
Maav 104,180,200
Maav 108,180,204
S_ s86,s6,1
Maav 108,184,224
s_and_b32 s86, s86, 7
Maav 108,188,244
s_waitcnt vmcnt(5)
Maav 112,188,248
v_cmp_eq_u32 vcc, s62, v11
Maav 112,184,228
s_mul_i32 s76, s75, 204800
Maav 112,180,208
s_bitcmp1_b32 vcc_lo, s75
s_cbranch_scc0 .Lspin5
.Lspun5:
Maav 116,180,212
s_bitcmp1_b32 vcc_lo, s86
s_cbranch_scc0 .Lspin6
.Lspun6:
Maav 116,184,232
S_ s56,s76,s84
Maav 116,188,252
s_waitcnt vmcnt(20) lgkmcnt(0)
s_barrier
Maav 156,220,252
s_nop 0
Ta 180,%0,49152
Ta 182,%0,51200
Maav 156,196,232
buffer_load_dwordx4 v[16:19], v6, s[28:31], s56 offen offset:0 sc1
Maav 156,192,212
s_nop 0
Ta 184,%0,53248
Ta 186,%0,55296
Maav 152,192,208
Maav 152,196,228
s_nop 0
Ta 188,%0,57344
Ta 190,%0,59392
Maav 152,220,248
buffer_load_dwordx4 v[20:23], v6, s[28:31], s56 offen offset:1024 sc1
Maav 148,220,244
.Lpad_o_11:
s_cbranch_execz .Lhop_o_r
Maav 148,196,224
Maav 148,192,204
Maav 144,192,200
buffer_load_dwordx4 v[24:27], v6, s[28:31], s56 offen offset:2048 sc1
Maav 144,196,220
Maav 144,220,240
Maav 140,220,236
Maav 140,196,216
buffer_load_dwordx4 v[28:31], v6, s[28:31], s56 offen offset:3072 sc1
Maav 140,192,196
W_ 0
S_ s56,s56,4096
Mvaa 56,180,0
s_nop 0
Ta 192,v1,49152
Ta 194,v1,51200
Mvaa 56,184,40
R_ v164,a224
R_ v165,a225
Mvaa 56,188,80
s_nop 0
Ta 196,v1,53248
Ta 198,v1,55296
Mvaa 60,188,84
buffer_load_dwordx4 v[32:35], v6, s[28:31], s56 offen offset:0 sc1
Mvaa 60,184,44
S_ s56,s76,s74
Ta 220,v1,57344
Ta 222,v1,59392
Mvaa 60,180,4
R_ v166,a226
R_ v167,a227
Mvaa 64,180,8
R_ v168,a228
R_ v169,a229
Mvaa 64,184,48
buffer_load_dwordx4 v[36:39], v6, s[28:31], s56 offen offset:0 sc1
Mvaa 64,188,88
S_ s56,s76,s82
R_ v170,a230
R_ v171,a231
Mvaa 68,188,240
R_ v172,a232
R_ v173,a233
Mvaa 68,184,52
R_ v174,a234
R_ v175,a235
Mvaa 68,180,12
buffer_load_dwordx4 v[40:43], v6, s[28:31], s56 offen offset:0 sc1
Mvaa 72,180,16
s_mul_i32 s76, s86, 204800
R_ v176,a236
R_ v177,a237
Mvaa 72,184,56
S_ s80,s76,s84
R_ v178,a238
R_ v179,a239
Mvaa 72,188,244
W_ 0
S_ s56,s76,s74
Mvaa 92,220,244
s_nop 0
Ta 180,v2,49152
Ta 182,v2,51200
Mvaa 92,196,56
buffer_load_dwordx4 v[156:159], v6, s[28:31], s56 offen offset:0 sc1
Mvaa 92,192,16
S_ s56,s76,s82
Ta 184,v2,53248
Ta 186,v2,55296
Mvaa 88,192,12
Mvaa 88,196,52
s_nop 0
Ta 188,v2,57344
Ta 190,v2,59392
Mvaa 88,220,240
buffer_load_dwordx4 v[160:163], v6, s[28:31], s56 offen offset:0 sc1
Mvaa 84,220,88
Mvaa 84,196,48
Mvaa 84,192,8
Mvaa 80,192,4
Mvaa 80,196,44
Mvaa 80,220,84
Mvaa 76,220,80
Mvaa 76,196,40
Mvaa 76,192,0
W_ 0
Maaa 100,180,0
s_nop 0
Ta 192,v3,49152
Ta 194,v3,51200
Maaa 100,184,40
Maaa 100,188,80
s_nop 0
Ta 196,v3,53248
Ta 198,v3,55296
Maaa 104,188,84
Maaa 104,184,44
s_nop 0
Ta 220,v3,57344
Ta 222,v3,59392
Maaa 104,180,4
Maaa 108,180,8
R_ v180,a100
R_ v181,a101
Maaa 108,184,48
R_ v182,a102
R_ v183,a103
Maaa 108,188,88
Maaa 112,188,240
R_ v184,a104
R_ v185,a105
Maaa 112,184,52
R_ v186,a106
R_ v187,a107
Maaa 112,180,12
Maaa 116,180,16
R_ v188,a108
R_ v189,a109
Maaa 116,184,56
R_ v190,a110
R_ v191,a111
Maaa 116,188,244
s_waitcnt vmcnt(22) lgkmcnt(0)
s_barrier
Maaa 156,220,244
s_nop 0
Ta 180,%0,36864
Ta 182,%0,38912
Maaa 156,196,56
R_ v192,a112
R_ v193,a113
Maaa 156,192,16
s_nop 0
Ta 184,%0,40960
Ta 186,%0,43008
Maaa 152,192,12
R_ v194,a114
R_ v195,a115
Maaa 152,196,52
s_nop 0
Ta 188,%0,45056
Ta 190,%0,47104
Maaa 152,220,240
R_ v196,a116
R_ v197,a117
Maaa 148,220,88
R_ v198,a118
R_ v199,a119
Maaa 148,196,48
R_ v216,a156
R_ v217,a157
Maaa 148,192,8
R_ v218,a158
R_ v219,a159
Maaa 144,192,4
R_ v212,a152
R_ v213,a153
Maaa 144,196,44
R_ v214,a154
R_ v215,a155
Maaa 144,220,84
Maaa 140,220,80
R_ v208,a148
R_ v209,a149
Maaa 140,196,40
R_ v210,a150
R_ v211,a151
Maaa 140,192,0
s_waitcnt vmcnt(15)
Mvaa 164,70,60
R_ v204,a144
R_ v205,a145
Mvaa 168,68,60
R_ v206,a146
R_ v207,a147
Mvaa 172,70,64
Mvaa 176,68,64
R_ v200,a140
R_ v201,a141
Mvaa 180,70,20
R_ v202,a142
R_ v203,a143
Mvaa 184,70,24
Mvaa 188,70,28
Mvaa 192,70,32
Mvaa 196,70,36
Mvaa 216,68,36
Mvaa 212,68,32
Mvaa 208,68,28
Mvaa 204,68,24
Mvaa 200,68,20
W_ 0
Mvaa 56,180,120
s_nop 0
Ta 192,v1,36864
Ta 194,v1,38912
Mvaa 56,184,160
Mvaa 56,188,200
s_nop 0
Ta 196,v1,40960
Ta 198,v1,43008
Mvaa 60,188,204
s_mov_b32 m0, s52
Mvaa 60,184,164
s_nop 0
Ta 220,v1,45056
Ta 222,v1,47104
Mvaa 60,180,124
s_nop 0
Y_ 56,0
Y_ 57,256
Mvaa 64,180,128
s_nop 0
Y_ 58,512
Y_ 59,768
Mvaa 64,184,168
Mvaa 64,188,208
s_nop 0
Y_ 60,1024
Y_ 61,1280
Mvaa 68,188,212
s_nop 0
Y_ 62,1536
Y_ 63,1792
Mvaa 68,184,172
Mvaa 68,180,132
s_nop 0
Y_ 64,2048
Y_ 65,2304
Mvaa 72,180,136
s_nop 0
Y_ 66,2560
Y_ 67,2816
Mvaa 72,184,176
Mvaa 72,188,216
W_ 12
Y_ 68,3072
Y_ 69,3328
Mvaa 92,220,216
s_nop 0
Ta 180,v2,36864
Ta 182,v2,38912
Mvaa 92,196,176
s_nop 0
Y_ 70,3584
Y_ 71,3840
Mvaa 92,192,136
s_nop 0
Ta 184,v2,40960
Ta 186,v2,43008
Mvaa 88,192,132
s_nop 0
Y_ 72,4096
Y_ 73,4352
Mvaa 88,196,172
s_nop 0
Ta 188,v2,45056
Ta 190,v2,47104
Mvaa 88,220,212
s_nop 0
Y_ 74,4608
Y_ 75,4864
Mvaa 84,220,208
s_nop 0
Y_ 92,9216
Y_ 93,9472
Mvaa 84,196,168
s_nop 0
Y_ 94,9728
Y_ 95,9984
Mvaa 84,192,128
s_nop 0
Y_ 88,8192
Y_ 89,8448
Mvaa 80,192,124
s_nop 0
Y_ 90,8704
Y_ 91,8960
Mvaa 80,196,164
Mvaa 80,220,204
s_nop 0
Y_ 84,7168
Y_ 85,7424
Mvaa 76,220,200
s_nop 0
Y_ 86,7680
Y_ 87,7936
Mvaa 76,196,160
Mvaa 76,192,120
W_ 14
Y_ 80,6144
Y_ 81,6400
Mvaa 180,180,120
S_ s56,s76,s84
Ta 192,v3,36864
Ta 194,v3,38912
Mvaa 180,184,160
s_nop 0
Y_ 82,6656
Y_ 83,6912
Mvaa 180,188,200
s_nop 0
Ta 196,v3,40960
Ta 198,v3,43008
Mvaa 184,188,204
s_nop 0
Y_ 76,5120
Y_ 77,5376
Mvaa 184,184,164
s_nop 0
Ta 220,v3,45056
Ta 222,v3,47104
Mvaa 184,180,124
s_nop 0
Y_ 78,5632
Y_ 79,5888
Mvaa 188,180,128
buffer_load_dwordx4 v[136:139], v6, s[28:31], s56 offen offset:0 sc1
Mvaa 188,184,168
Mvaa 188,188,208
s_waitcnt vmcnt(63) lgkmcnt(0)
s_barrier
Mvaa 192,188,212
ds_read2st64_b32 v[96:97], v5 offset0:0 offset1:1
Mvaa 192,184,172
buffer_load_dwordx4 v[140:143], v6, s[28:31], s56 offen offset:1024 sc1
Mvaa 192,180,132
ds_read2st64_b32 v[98:99], v5 offset0:2 offset1:3
Mvaa 196,180,136
ds_read2st64_b32 v[104:105], v5 offset0:4 offset1:5
Mvaa 196,184,176
ds_read2st64_b32 v[106:107], v5 offset0:6 offset1:7
Mvaa 196,188,216
buffer_load_dwordx4 v[144:147], v6, s[28:31], s56 offen offset:2048 sc1
Mvaa 216,220,216
ds_read2st64_b32 v[112:113], v5 offset0:8 offset1:9
Mvaa 216,196,176
ds_read2st64_b32 v[114:115], v5 offset0:10 offset1:11
W_ 5
A_ v96,v96,v180
Mvaa 216,192,136
ds_read2st64_b32 v[120:121], v5 offset0:12 offset1:13
A_ v97,v97,v181
Mvaa 212,192,132
buffer_load_dwordx4 v[148:151], v6, s[28:31], s56 offen offset:3072 sc1
Mvaa 212,196,172
S_ s56,s56,4096
ds_read2st64_b32 v[122:123], v5 offset0:14 offset1:15
W_ 6
A_ v98,v98,v182
Mvaa 212,220,212
ds_read2st64_b32 v[128:129], v5 offset0:16 offset1:17
A_ v99,v99,v183
Mvaa 208,220,208
ds_read2st64_b32 v[130:131], v5 offset0:18 offset1:19
W_ 7
A_ v104,v104,v184
Mvaa 208,196,168
buffer_load_dwordx4 v[152:155], v6, s[28:31], s56 offen offset:0 sc1
Mvaa 208,192,128
ds_read2st64_b32 v[132:133], v5 offset0:36 offset1:37
A_ v105,v105,v185
Mvaa 204,192,124
ds_read2st64_b32 v[134:135], v5 offset0:38 offset1:39
W_ 8
A_ v106,v106,v186
Mvaa 204,196,164
ds_read2st64_b32 v[124:125], v5 offset0:32 offset1:33
A_ v107,v107,v187
Mvaa 204,220,204
ds_read2st64_b32 v[126:127], v5 offset0:34 offset1:35
W_ 9
A_ v112,v112,v188
Mvaa 200,220,200
ds_read2st64_b32 v[116:117], v5 offset0:28 offset1:29
A_ v113,v113,v189
Mvaa 200,196,160
ds_read2st64_b32 v[118:119], v5 offset0:30 offset1:31
W_ 10
A_ v114,v114,v190
Mvaa 200,192,120
ds_read2st64_b32 v[108:109], v5 offset0:24 offset1:25
A_ v115,v115,v191
ds_read2st64_b32 v[110:111], v5 offset0:26 offset1:27
ds_read2st64_b32 v[100:101], v5 offset0:20 offset1:21
ds_read2st64_b32 v[102:103], v5 offset0:22 offset1:23
W_ 13
A_ v120,v120,v192
A_ v121,v121,v193
W_ 12
A_ v122,v122,v194
A_ v123,v123,v195
W_ 11
A_ v128,v128,v196
A_ v129,v129,v197
W_ 10
A_ v130,v130,v198
A_ v131,v131,v199
s_waitcnt vmcnt(5)
Mvav 164,70,36
W_ 9
A_ v132,v132,v216
A_ v133,v133,v217
Mvav 168,68,36
W_ 8
A_ v134,v134,v218
A_ v135,v135,v219
Mvav 172,70,40
W_ 7
A_ v124,v124,v212
A_ v125,v125,v213
Mvav 176,68,40
W_ 6
A_ v126,v126,v214
A_ v127,v127,v215
Mvav 164,70,156
W_ 5
A_ v116,v116,v208
A_ v117,v117,v209
Mvav 168,68,156
W_ 4
A_ v118,v118,v210
A_ v119,v119,v211
Mvav 172,70,160
W_ 3
A_ v108,v108,v204
A_ v109,v109,v205
Mvav 176,68,160
W_ 2
A_ v110,v110,v206
A_ v111,v111,v207
Mvav 96,70,16
W_ 1
A_ v100,v100,v200
A_ v101,v101,v201
Mvav 104,70,20
W_ 0
A_ v102,v102,v202
A_ v103,v103,v203
Mvav 112,70,24
C_ 220,164,165
C_ 221,166,167
Mvav 120,70,28
C_ 222,168,169
C_ 223,170,171
Mvav 128,70,32
v_permlane16_swap_b32 v220, v222
v_permlane16_swap_b32 v221, v223
Mvav 132,68,32
buffer_store_dwordx4 v[220:223], v4, s[32:35], s9 offen sc1
Mvav 124,68,28
C_ 228,172,173
C_ 229,174,175
Mvav 116,68,24
C_ 230,176,177
C_ 231,178,179
Mvav 108,68,20
v_permlane16_swap_b32 v228, v230
v_permlane16_swap_b32 v229, v231
Mvav 100,68,16
buffer_store_dwordx4 v[228:231], v4, s[32:35], s10 offen sc1
s_waitcnt vmcnt(2)
.Lsel6:
Nvav 180,70,136,96
Nvav 188,70,140,104
Nvav 196,70,144,112
Nvav 204,70,148,120
Nvav 212,70,152,128
Nvav 184,68,136,100
s_nop 3
A_ v10,v180,v188
Nvav 192,68,140,108
A_ v11,v196,v204
Nvav 200,68,144,116
A_ v10,v10,v212
v_cmp_u_f32 vcc, v10, v11
Nvav 208,68,148,124
s_cbranch_vccnz .Lretry
.Lgood:
Nvav 216,68,152,132
C_ 180,180,181
C_ 181,182,183
C_ 182,184,185
C_ 183,186,187
s_nop 0
v_permlane16_swap_b32 v180, v182
v_permlane16_swap_b32 v181, v183
buffer_store_dwordx4 v[180:183], v4, s[32:35], s8 offen sc1
C_ 188,188,189
C_ 189,190,191
C_ 190,192,193
C_ 191,194,195
S_ s8,s8,40960
v_permlane16_swap_b32 v188, v190
v_permlane16_swap_b32 v189, v191
buffer_store_dwordx4 v[188:191], v4, s[32:35], s8 offen sc1
C_ 196,196,197
C_ 197,198,199
C_ 198,200,201
C_ 199,202,203
S_ s8,s8,40960
v_permlane16_swap_b32 v196, v198
v_permlane16_swap_b32 v197, v199
buffer_store_dwordx4 v[196:199], v4, s[32:35], s8 offen sc1
C_ 204,204,205
C_ 205,206,207
C_ 206,208,209
C_ 207,210,211
S_ s8,s8,40960
v_permlane16_swap_b32 v204, v206
v_permlane16_swap_b32 v205, v207
buffer_store_dwordx4 v[204:207], v4, s[32:35], s8 offen sc1
C_ 212,212,213
C_ 213,214,215
C_ 214,216,217
C_ 215,218,219
S_ s8,s8,40960
v_permlane16_swap_b32 v212, v214
v_permlane16_swap_b32 v213, v215
buffer_store_dwordx4 v[212:215], v4, s[32:35], s8 offen sc1
s_endpgm
.Lspin0:
s_cmp_eq_u32 s57, 0
s_cbranch_scc1 .Lspun0
s_sub_u32 s57, s57, 1
s_sleep 4
buffer_load_dword v11, v8, s[92:95], s96 offen offset:384 sc1
s_waitcnt vmcnt(0)
v_cmp_eq_u32 vcc, s62, v11
s_nop 1
s_bitcmp1_b32 vcc_lo, s75
s_cbranch_scc0 .Lspin0
s_branch .Lspun0
.Lspin1:
s_cmp_eq_u32 s57, 0
s_cbranch_scc1 .Lspun1
s_sub_u32 s57, s57, 1
s_sleep 4
buffer_load_dword v11, v8, s[92:95], s96 offen offset:384 sc1
s_waitcnt vmcnt(0)
v_cmp_eq_u32 vcc, s62, v11
s_nop 1
s_bitcmp1_b32 vcc_lo, s75
s_cbranch_scc0 .Lspin1
s_branch .Lspun1
.Lspin2:
s_cmp_eq_u32 s57, 0
s_cbranch_scc1 .Lspun2
s_sub_u32 s57, s57, 1
s_sleep 4
buffer_load_dword v11, v8, s[92:95], s96 offen offset:384 sc1
s_waitcnt vmcnt(0)
v_cmp_eq_u32 vcc, s62, v11
s_nop 1
s_bitcmp1_b32 vcc_lo, s75
s_cbranch_scc0 .Lspin2
s_branch .Lspun2
.Lspin3:
s_cmp_eq_u32 s57, 0
s_cbranch_scc1 .Lspun3
s_sub_u32 s57, s57, 1
s_sleep 4
buffer_load_dword v11, v8, s[92:95], s96 offen offset:384 sc1
s_waitcnt vmcnt(0)
v_cmp_eq_u32 vcc, s62, v11
s_nop 1
s_bitcmp1_b32 vcc_lo, s75
s_cbranch_scc0 .Lspin3
s_branch .Lspun3
.Lspin4:
s_cmp_eq_u32 s57, 0
s_cbranch_scc1 .Lspun4
s_sub_u32 s57, s57, 1
s_sleep 4
buffer_load_dword v11, v8, s[92:95], s96 offen offset:384 sc1
s_waitcnt vmcnt(0)
v_cmp_eq_u32 vcc, s62, v11
s_nop 1
s_bitcmp1_b32 vcc_lo, s75
s_cbranch_scc0 .Lspin4
s_branch .Lspun4
.Lspin5:
s_cmp_eq_u32 s57, 0
s_cbranch_scc1 .Lspun5
s_sub_u32 s57, s57, 1
s_sleep 4
buffer_load_dword v11, v8, s[92:95], s96 offen offset:384 sc1
s_waitcnt vmcnt(0)
v_cmp_eq_u32 vcc, s62, v11
s_nop 1
s_bitcmp1_b32 vcc_lo, s75
s_cbranch_scc0 .Lspin5
s_branch .Lspun5
.Lspin6:
s_cmp_eq_u32 s57, 0
s_cbranch_scc1 .Lspun6
s_sub_u32 s57, s57, 1
s_sleep 4
buffer_load_dword v11, v8, s[92:95], s96 offen offset:384 sc1
s_waitcnt vmcnt(0)
v_cmp_eq_u32 vcc, s62, v11
s_nop 1
s_bitcmp1_b32 vcc_lo, s86
s_cbranch_scc0 .Lspin6
s_branch .Lspun6
.Lretry:
s_cmp_eq_u32 s57, 0
s_cbranch_scc1 .Lgood
s_sub_u32 s57, s57, 1
v_max_u32 v10, v136, v137
v_max3_u32 v10, v10, v138, v139
v_max3_u32 v10, v10, v140, v141
v_max3_u32 v10, v10, v142, v143
v_max3_u32 v10, v10, v144, v145
v_max3_u32 v10, v10, v146, v147
v_max3_u32 v10, v10, v148, v149
v_max3_u32 v10, v10, v150, v151
v_max3_u32 v10, v10, v152, v153
v_max3_u32 v10, v10, v154, v155
v_cmp_eq_u32 vcc, -1, v10
s_nop 4
s_cbranch_vccz .Lgood
s_nop 7
s_nop 7
S_ s53,s80,4096
buffer_load_dwordx4 v[136:139], v6, s[28:31], s80 offen offset:0 sc1
buffer_load_dwordx4 v[140:143], v6, s[28:31], s80 offen offset:1024 sc1
buffer_load_dwordx4 v[144:147], v6, s[28:31], s80 offen offset:2048 sc1
buffer_load_dwordx4 v[148:151], v6, s[28:31], s80 offen offset:3072 sc1
buffer_load_dwordx4 v[152:155], v6, s[28:31], s53 offen offset:0 sc1
s_waitcnt vmcnt(0)
s_branch .Lsel6
.Ldone:
s_waitcnt vmcnt(0)
.purgem S_
.purgem W_
.purgem L_
.purgem X_
.purgem R_
.purgem Ta
.purgem Da
.purgem Zvaa
.purgem Mvaa
.purgem U_
.purgem C_
.purgem Ln
.purgem Dv
.purgem Zvvv
.purgem Mvvv
.purgem B_
.purgem E_
.purgem Nvvv
.purgem Tv
.purgem Zaav
.purgem Zavv
.purgem A_
.purgem Mavv
.purgem Zvav
.purgem Mvav
.purgem Y_
.purgem Zaaa
.purgem Maaa
.purgem Maav
.purgem Nvav
)A"
    : "+v"(tid)
    : "s"(__builtin_amdgcn_kernarg_segment_ptr()), "s"(blockIdx.x)
    : "v1", "v2", "v3", "v4", "v5", "v6", "v7", "v8", "v9", "v10", "v11", "v12", "v13", "v14", "v15", "v16", "v17", "v18", "v19", "v20", "v21", "v22", "v23", "v24", "v25", "v26", "v27", "v28", "v29", "v30", "v31", "v32", "v33", "v34", "v35", "v36", "v37", "v38", "v39", "v40", "v41", "v42", "v43", "v44", "v45", "v46", "v47", "v48", "v49", "v50", "v51", "v52", "v53", "v54", "v55", "v56", "v57", "v58", "v59", "v60", "v61", "v62", "v63", "v64", "v65", "v66", "v67", "v68", "v69", "v70", "v71", "v72", "v73", "v74", "v75", "v76", "v77", "v78", "v79", "v80", "v81", "v82", "v83", "v84", "v85", "v86", "v87", "v88", "v89", "v90", "v91", "v92", "v93", "v94", "v95", "v96", "v97", "v98", "v99", "v100", "v101", "v102", "v103", "v104", "v105", "v106", "v107", "v108", "v109", "v110", "v111", "v112", "v113", "v114", "v115", "v116", "v117", "v118", "v119", "v120", "v121", "v122", "v123", "v124", "v125", "v126", "v127", "v128", "v129", "v130", "v131", "v132", "v133", "v134", "v135", "v136", "v137", "v138", "v139", "v140", "v141", "v142", "v143", "v144", "v145", "v146", "v147", "v148", "v149", "v150", "v151", "v152", "v153", "v154", "v155", "v156", "v157", "v158", "v159", "v160", "v161", "v162", "v163", "v164", "v165", "v166", "v167", "v168", "v169", "v170", "v171", "v172", "v173", "v174", "v175", "v176", "v177", "v178", "v179", "v180", "v181", "v182", "v183", "v184", "v185", "v186", "v187", "v188", "v189", "v190", "v191", "v192", "v193", "v194", "v195", "v196", "v197", "v198", "v199", "v200", "v201", "v202", "v203", "v204", "v205", "v206", "v207", "v208", "v209", "v210", "v211", "v212", "v213", "v214", "v215", "v216", "v217", "v218", "v219", "v220", "v221", "v222", "v223", "v224", "v225", "v226", "v227", "v228", "v229", "v230", "v231", "v232", "v233", "v234", "v235", "v236", "v237", "v238", "v239", "v240", "v241", "v242", "v243", "v244", "v245", "v246", "v247", "v248", "v249", "v250", "v251", "v252", "v253", "v254", "v255", "a0", "a1", "a2", "a3", "a4", "a5", "a6", "a7", "a8", "a9", "a10", "a11", "a12", "a13", "a14", "a15", "a16", "a17", "a18", "a19", "a20", "a21", "a22", "a23", "a24", "a25", "a26", "a27", "a28", "a29", "a30", "a31", "a32", "a33", "a34", "a35", "a36", "a37", "a38", "a39", "a40", "a41", "a42", "a43", "a44", "a45", "a46", "a47", "a48", "a49", "a50", "a51", "a52", "a53", "a54", "a55", "a56", "a57", "a58", "a59", "a60", "a61", "a62", "a63", "a64", "a65", "a66", "a67", "a68", "a69", "a70", "a71", "a72", "a73", "a74", "a75", "a76", "a77", "a78", "a79", "a80", "a81", "a82", "a83", "a84", "a85", "a86", "a87", "a88", "a89", "a90", "a91", "a92", "a93", "a94", "a95", "a96", "a97", "a98", "a99", "a100", "a101", "a102", "a103", "a104", "a105", "a106", "a107", "a108", "a109", "a110", "a111", "a112", "a113", "a114", "a115", "a116", "a117", "a118", "a119", "a120", "a121", "a122", "a123", "a124", "a125", "a126", "a127", "a128", "a129", "a130", "a131", "a132", "a133", "a134", "a135", "a136", "a137", "a138", "a139", "a140", "a141", "a142", "a143", "a144", "a145", "a146", "a147", "a148", "a149", "a150", "a151", "a152", "a153", "a154", "a155", "a156", "a157", "a158", "a159", "a160", "a161", "a162", "a163", "a164", "a165", "a166", "a167", "a168", "a169", "a170", "a171", "a172", "a173", "a174", "a175", "a176", "a177", "a178", "a179", "a180", "a181", "a182", "a183", "a184", "a185", "a186", "a187", "a188", "a189", "a190", "a191", "a192", "a193", "a194", "a195", "a196", "a197", "a198", "a199", "a200", "a201", "a202", "a203", "a204", "a205", "a206", "a207", "a208", "a209", "a210", "a211", "a212", "a213", "a214", "a215", "a216", "a217", "a218", "a219", "a220", "a221", "a222", "a223", "a224", "a225", "a226", "a227", "a228", "a229", "a230", "a231", "a232", "a233", "a234", "a235", "a236", "a237", "a238", "a239", "a240", "a241", "a242", "a243", "a244", "a245", "a246", "a247", "a248", "a249", "a250", "a251", "a252", "a253", "a254", "a255", "s3", "s4", "s5", "s6", "s7", "s8", "s9", "s10", "s11", "s12", "s13", "s14", "s15", "s16", "s17", "s18", "s19", "s20", "s21", "s22", "s23", "s24", "s25", "s26", "s27", "s28", "s29", "s30", "s31", "s32", "s33", "s34", "s35", "s36", "s37", "s38", "s39", "s40", "s41", "s42", "s43", "s44", "s45", "s46", "s47", "s48", "s49", "s50", "s51", "s52", "s53", "s54", "s55", "s56", "s57", "s58", "s59", "s60", "s61", "s62", "s63", "s64", "s65", "s66", "s67", "s68", "s69", "s70", "s71", "s72", "s73", "s74", "s75", "s76", "s77", "s78", "s79", "s80", "s81", "s82", "s83", "s84", "s85", "s86", "s87", "s88", "s89", "s90", "s91", "s92", "s93", "s94", "s95", "s96", "s97", "s98", "s99", "s100", "s101", "vcc", "scc", "memory");
}

extern "C" void kernel_launch(void* const* d_in, const int* in_sizes, int n_in, void* d_out, int out_size, void* d_ws, size_t ws_size, hipStream_t stream) {
  Args P;
  for (int br = 0; br < 2; ++br) {
    P.p[br][0] = d_in[5 * br]; P.p[br][1] = d_in[5 * br + 3]; P.p[br][2] = d_in[5 * br + 1]; P.p[br][3] = d_in[5 * br + 2]; P.p[br][4] = d_in[5 * br + 4];
    P.p[br][5] = d_ws; P.p[br][6] = (char*)d_out + br * 1280; P.p[br][7] = d_ws;
  }
  (void)hipFuncSetAttribute((const void*)fused_kernel, hipFuncAttributeMaxDynamicSharedMemorySize, LDS1);
  fused_kernel<<<256, 256, LDS1, stream>>>(P);
}
```
